```python
import torch

def _softmax(x, axis=-1):
    return torch.softmax(x, dim=axis)
def _gelu(x):
    return torch.nn.functional.gelu(x, approximate="tanh")
def _silu(x):
    return torch.nn.functional.silu(x)
def _sigmoid(x):
    return torch.sigmoid(x)
def _layernorm(x, axis=-1, eps=1e-5):
    m = x.mean(dim=axis, keepdim=True)
    v = x.var(dim=axis, keepdim=True, unbiased=False)
    return (x - m) / torch.sqrt(v + eps)
def _scatter_add(tgt, idx, src, axis=0):
    return tgt.clone().index_add_(axis, idx, src)
def _segment_sum(src, seg, n):
    out = torch.zeros((n,) + src.shape[1:], dtype=src.dtype)
    return out.index_add_(0, seg, src)
def _pad(x, pw):
    flat = tuple(v for pair in reversed(pw) for v in pair)
    return torch.nn.functional.pad(x, flat)

kernel_name = 'standalone__deep_cross_pnn__recsys'

def setup_inputs():

    _w = (lambda a: (lambda t: t.to(torch.bfloat16) if t.is_floating_point() else t)(torch.as_tensor(a)))
    B, F, D, V = 16384, 100, 128, min(1000, 100000)
    H = 4 * D
    return {
        "idx": _w((lambda lo, hi, *s: torch.randint(lo, hi, s))(0, V, B, F).to(torch.int64)),
        "table": _w((lambda *s: torch.randn(*s, dtype=torch.bfloat16) * 1.6)(V, D) * (D ** -0.5)),
        "w_p": _w((lambda *s: torch.randn(*s, dtype=torch.bfloat16) * 0.05)(F * F) * ((F * F) ** -0.5)),
        "w1": _w((lambda *s: torch.randn(*s, dtype=torch.bfloat16) * 1.6)(F * D, H) * ((F * D) ** -0.5)),
        "b1": _w((lambda *s: torch.randn(*s, dtype=torch.bfloat16) * 12.8)(H) * 0.01),
        "w2": _w((lambda *s: torch.randn(*s, dtype=torch.bfloat16) * 0.05)(H) * (H ** -0.5)),
        "b2": _w((lambda *s: torch.randn(*s, dtype=torch.bfloat16) * 1.6)(1) * 0.01),
    }

def reference(idx, table, w_p, w1, b1, w2, b2):
    emb = table[idx]
    B = emb.shape[0]; F = emb.shape[1]; D = emb.shape[2]
    ip = torch.einsum('bid,bjd->bij', emb, emb)
    ip = ip.reshape(B, F * F)
    logit_p = ip @ w_p
    z = emb.reshape(B, F * D)
    h = _gelu(z @ w1 + b1)
    logit_d = h @ w2 + b2
    return logit_p + logit_d
```

```cpp
#include <hip/hip_runtime.h>
#include <hip/hip_bf16.h>
#include <stdint.h>

#define K_ASM \
  "v_readfirstlane_b32 s27, v0\n" \
  "v_and_b32 v255, 63, v0\n" \
  "s_and_b32 s29, s26, 7\n" \
  "s_lshr_b32 s30, s26, 3\n" \
  "s_lshr_b32 s27, s27, 6\n" \
  "s_and_b32 s31, s30, 1\n" \
  "s_and_b32 s28, s27, 3\n" \
  "s_lshr_b32 s35, s30, 1\n" \
  "s_lshl3_add_u32 s7, s35, s29\n" \
  "v_lshrrev_b32 v1, 3, v255\n" \
  "v_and_b32 v2, 7, v255\n" \
  "v_lshlrev_b32 v1, 8, v1\n" \
  "v_lshl_or_b32 v254, v2, 4, v1\n" \
  "s_lshl_b32 s35, s31, 7\n" \
  "s_add_u32 s40, s10, s35\n" \
  "s_addc_u32 s41, s11, 0\n" \
  "s_mul_i32 s36, s7, 37\n" \
  "s_and_b32 s36, s36, 63\n" \
  "s_add_u32 s36, s36, s28\n" \
  "s_cmp_ge_u32 s27, 4\n" \
  "s_cbranch_scc1 LB_start\n" \
  "v_lshlrev_b32 v1, 4, v255\n" \
  "s_lshl_b32 s35, s28, 10\n" \
  "v_add_u32 v1, s35, v1\n" \
  "s_add_u32 m0, s35, 138240\n" \
  "s_nop 0\n" \
  "global_load_lds_dwordx4 v1, s[12:13]\n" \
  "v_add_u32 v1, 0x1000, v1\n" \
  "s_add_u32 m0, m0, 0x1000\n" \
  "s_nop 0\n" \
  "global_load_lds_dwordx4 v1, s[12:13]\n" \
  "v_add_u32 v1, 0x1000, v1\n" \
  "s_add_u32 m0, m0, 0x1000\n" \
  "s_nop 0\n" \
  "global_load_lds_dwordx4 v1, s[12:13]\n" \
  "v_add_u32 v1, 0x1000, v1\n" \
  "s_add_u32 m0, m0, 0x1000\n" \
  "s_nop 0\n" \
  "global_load_lds_dwordx4 v1, s[12:13]\n" \
  "v_add_u32 v1, 0x1000, v1\n" \
  "s_add_u32 m0, m0, 0x1000\n" \
  "v_min_u32 v1, 19984, v1\n" \
  "global_load_lds_dwordx4 v1, s[12:13]\n" \
  "v_and_b32 v252, 15, v255\n" \
  "v_lshrrev_b32 v253, 4, v255\n" \
  "v_mul_u32_u24 v2, 400, v253\n" \
  "v_min_u32 v3, 8, v252\n" \
  "v_lshl_add_u32 v250, v252, 4, v2\n" \
  "v_lshl_add_u32 v251, v3, 4, v2\n" \
  "s_lshl2_add_u32 s35, s7, s28\n" \
  "s_mul_i32 s35, s35, 12800\n" \
  "s_add_u32 s44, s8, s35\n" \
  "s_addc_u32 s45, s9, 0\n" \
  "global_load_dwordx4 v[232:235], v250, s[44:45] offset:0\n" \
  "global_load_dwordx4 v[236:239], v251, s[44:45] offset:256\n" \
  "global_load_dwordx4 v[240:243], v250, s[44:45] offset:1600\n" \
  "global_load_dwordx4 v[244:247], v251, s[44:45] offset:1856\n" \
  "v_lshlrev_b32 v249, 7, v255\n" \
  "s_setprio 2\n" \
  "v_lshrrev_b32 v1, 2, v252\n" \
  "v_and_b32 v2, 3, v252\n" \
  "v_cmp_eq_u32 vcc, v1, v253\n" \
  "v_mov_b32 v4, 0x3f80\n" \
  "v_and_b32 v13, 1, v253\n" \
  "v_lshlrev_b32 v14, 1, v13\n" \
  "v_xor_b32 v2, v2, v14\n" \
  "v_cndmask_b32 v2, 4, v2, vcc\n" \
  "v_cmp_eq_u32 vcc, 0, v2\n" \
  "s_nop 1\n" \
  "v_cndmask_b32 v216, 0, v4, vcc\n" \
  "v_lshlrev_b32 v220, 16, v216\n" \
  "v_cmp_eq_u32 vcc, 1, v2\n" \
  "s_nop 1\n" \
  "v_cndmask_b32 v217, 0, v4, vcc\n" \
  "v_lshlrev_b32 v221, 16, v217\n" \
  "v_cmp_eq_u32 vcc, 2, v2\n" \
  "s_nop 1\n" \
  "v_cndmask_b32 v218, 0, v4, vcc\n" \
  "v_lshlrev_b32 v222, 16, v218\n" \
  "v_cmp_eq_u32 vcc, 3, v2\n" \
  "s_nop 1\n" \
  "v_cndmask_b32 v219, 0, v4, vcc\n" \
  "v_lshlrev_b32 v223, 16, v219\n" \
  "v_and_b32 v1, 2, v252\n" \
  "v_lshlrev_b32 v1, 3, v1\n" \
  "v_lshlrev_b32 v1, v1, v4\n" \
  "v_and_b32 v2, 0x31, v255\n" \
  "v_mov_b64 v[224:225], 0\n" \
  "v_cmp_eq_u32 vcc, 0, v2\n" \
  "s_nop 1\n" \
  "v_cndmask_b32 v226, 0, v1, vcc\n" \
  "v_cmp_eq_u32 vcc, 1, v2\n" \
  "s_nop 1\n" \
  "v_cndmask_b32 v227, 0, v1, vcc\n" \
  "v_mul_u32_u24 v5, 400, v252\n" \
  "v_add_u32 v5, 138240, v5\n" \
  "v_lshl_add_u32 v6, v253, 4, v5\n" \
  "v_add_u32 v7, 6400, v5\n" \
  "v_add_u32 v8, 6400, v6\n" \
  "v_add_u32 v9, 12800, v5\n" \
  "v_add_u32 v10, 12800, v6\n" \
  "v_bfe_u32 v1, v252, 1, 1\n" \
  "v_and_b32 v2, 1, v252\n" \
  "v_lshl_add_u32 v1, v2, 1, v1\n" \
  "v_mul_u32_u24 v1, 200, v1\n" \
  "v_add_u32 v11, 157440, v1\n" \
  "v_lshl_add_u32 v12, v253, 4, v11\n" \
  "v_lshlrev_b32 v13, 3, v13\n" \
  "v_sub_u32 v16, v5, v13\n" \
  "v_add_u32 v5, v5, v13\n" \
  "v_sub_u32 v17, v6, v13\n" \
  "v_add_u32 v6, v6, v13\n" \
  "v_sub_u32 v18, v7, v13\n" \
  "v_add_u32 v7, v7, v13\n" \
  "v_sub_u32 v19, v8, v13\n" \
  "v_add_u32 v8, v8, v13\n" \
  "v_sub_u32 v20, v9, v13\n" \
  "v_add_u32 v9, v9, v13\n" \
  "v_sub_u32 v21, v10, v13\n" \
  "v_add_u32 v10, v10, v13\n" \
  "v_sub_u32 v22, v11, v13\n" \
  "v_add_u32 v11, v11, v13\n" \
  "v_sub_u32 v23, v12, v13\n" \
  "v_add_u32 v12, v12, v13\n" \
  "s_waitcnt vmcnt(4)\n" \
  "s_barrier\n" \
  "ds_read_b64 v[200:201], v12 offset:128\n" \
  "ds_read_b64 v[202:203], v23 offset:136\n" \
  "ds_read_b64 v[184:185], v9 offset:184\n" \
  "ds_read_b64 v[186:187], v20 offset:192\n" \
  "ds_read_b64 v[188:189], v9 offset:384\n" \
  "ds_read_b64 v[190:191], v20 offset:392\n" \
  "ds_read_b64 v[196:197], v12 offset:64\n" \
  "ds_read_b64 v[198:199], v23 offset:72\n" \
  "ds_read_b64 v[176:177], v7 offset:184\n" \
  "ds_read_b64 v[178:179], v18 offset:192\n" \
  "ds_read_b64 v[180:181], v7 offset:384\n" \
  "ds_read_b64 v[182:183], v18 offset:392\n" \
  "ds_read_b64 v[192:193], v12 offset:0\n" \
  "ds_read_b64 v[194:195], v23 offset:8\n" \
  "s_waitcnt lgkmcnt(12)\n" \
  "v_mfma_f32_16x16x32_bf16 v[44:47], v[200:203], v[216:219], 0\n" \
  "ds_read_b64 v[168:169], v5 offset:184\n" \
  "ds_read_b64 v[170:171], v16 offset:192\n" \
  "s_waitcnt lgkmcnt(12)\n" \
  "v_mfma_f32_16x16x32_bf16 v[44:47], v[224:227], v[184:187], v[44:47]\n" \
  "ds_read_b64 v[172:173], v5 offset:384\n" \
  "ds_read_b64 v[174:175], v16 offset:392\n" \
  "v_mfma_f32_16x16x32_bf16 v[228:231], v[200:203], v[220:223], 0\n" \
  "ds_read_b64 v[96:97], v6 offset:0\n" \
  "ds_read_b64 v[98:99], v17 offset:8\n" \
  "s_waitcnt lgkmcnt(14)\n" \
  "v_mfma_f32_16x16x32_bf16 v[228:231], v[224:227], v[188:191], v[228:231]\n" \
  "ds_read_b64 v[124:125], v8 offset:64\n" \
  "ds_read_b64 v[126:127], v19 offset:72\n" \
  "s_waitcnt lgkmcnt(14)\n" \
  "v_mfma_f32_16x16x32_bf16 v[60:63], v[196:199], v[216:219], 0\n" \
  "ds_read_b64 v[136:137], v8 offset:264\n" \
  "ds_read_b64 v[138:139], v19 offset:272\n" \
  "s_waitcnt lgkmcnt(14)\n" \
  "v_mfma_f32_16x16x32_bf16 v[60:63], v[224:227], v[176:179], v[60:63]\n" \
  "ds_read_b64 v[140:141], v8 offset:328\n" \
  "ds_read_b64 v[142:143], v19 offset:336\n" \
  "v_mfma_f32_16x16x32_bf16 v[212:215], v[196:199], v[220:223], 0\n" \
  "ds_read_b64 v[160:161], v10 offset:264\n" \
  "ds_read_b64 v[162:163], v21 offset:272\n" \
  "s_waitcnt lgkmcnt(15)\n" \
  "v_mfma_f32_16x16x32_bf16 v[212:215], v[224:227], v[180:183], v[212:215]\n" \
  "ds_read_b64 v[116:117], v6 offset:328\n" \
  "ds_read_b64 v[118:119], v17 offset:336\n" \
  "s_waitcnt lgkmcnt(15)\n" \
  "v_mfma_f32_16x16x32_bf16 v[68:71], v[192:195], v[216:219], 0\n" \
  "ds_read_b64 v[144:145], v10 offset:0\n" \
  "ds_read_b64 v[146:147], v21 offset:8\n" \
  "s_waitcnt lgkmcnt(15)\n" \
  "v_mfma_f32_16x16x32_bf16 v[68:71], v[224:227], v[168:171], v[68:71]\n" \
  "ds_read_b64 v[148:149], v10 offset:64\n" \
  "ds_read_b64 v[150:151], v21 offset:72\n" \
  "v_mfma_f32_16x16x32_bf16 v[208:211], v[192:195], v[220:223], 0\n" \
  "ds_read_b64 v[104:105], v6 offset:128\n" \
  "ds_read_b64 v[106:107], v17 offset:136\n" \
  "s_waitcnt lgkmcnt(15)\n" \
  "v_mfma_f32_16x16x32_bf16 v[208:211], v[224:227], v[172:175], v[208:211]\n" \
  "ds_read_b64 v[100:101], v6 offset:64\n" \
  "ds_read_b64 v[102:103], v17 offset:72\n" \
  "v_mfma_f32_16x16x32_bf16 v[92:95], v[96:99], v[216:219], 0\n" \
  "ds_read_b64 v[120:121], v8 offset:0\n" \
  "ds_read_b64 v[122:123], v19 offset:8\n" \
  "s_waitcnt lgkmcnt(15)\n" \
  "v_mfma_f32_16x16x32_bf16 v[24:27], v[124:127], v[220:223], 0\n" \
  "ds_read_b64 v[112:113], v6 offset:264\n" \
  "ds_read_b64 v[114:115], v17 offset:272\n" \
  "v_mfma_f32_16x16x32_bf16 v[24:27], v[216:219], v[136:139], v[24:27]\n" \
  "ds_read_b64 v[156:157], v10 offset:200\n" \
  "ds_read_b64 v[158:159], v21 offset:208\n" \
  "s_waitcnt lgkmcnt(15)\n" \
  "v_mfma_f32_16x16x32_bf16 v[28:31], v[140:143], v[220:223], 0\n" \
  "ds_read_b64 v[132:133], v8 offset:200\n" \
  "ds_read_b64 v[134:135], v19 offset:208\n" \
  "v_mfma_f32_16x16x32_bf16 v[28:31], v[220:223], v[160:163], v[28:31]\n" \
  "ds_read_b64 v[108:109], v6 offset:200\n" \
  "ds_read_b64 v[110:111], v17 offset:208\n" \
  "s_waitcnt lgkmcnt(15)\n" \
  "v_mfma_f32_16x16x32_bf16 v[32:35], v[116:119], v[216:219], 0\n" \
  "ds_read_b64 v[152:153], v10 offset:128\n" \
  "ds_read_b64 v[154:155], v21 offset:136\n" \
  "v_mfma_f32_16x16x32_bf16 v[32:35], v[220:223], v[144:147], v[32:35]\n" \
  "ds_read_b64 v[164:165], v10 offset:328\n" \
  "ds_read_b64 v[166:167], v21 offset:336\n" \
  "v_mfma_f32_16x16x32_bf16 v[36:39], v[140:143], v[216:219], 0\n" \
  "ds_read_b64 v[128:129], v8 offset:128\n" \
  "ds_read_b64 v[130:131], v19 offset:136\n" \
  "s_waitcnt lgkmcnt(15)\n" \
  "v_mfma_f32_16x16x32_bf16 v[36:39], v[220:223], v[148:151], v[36:39]\n" \
  "ds_read_b64 v[204:205], v11 offset:184\n" \
  "ds_read_b64 v[206:207], v22 offset:192\n" \
  "v_mfma_f32_16x16x32_bf16 v[40:43], v[104:107], v[216:219], 0\n" \
  "v_mfma_f32_16x16x32_bf16 v[40:43], v[216:219], v[144:147], v[40:43]\n" \
  "v_mfma_f32_16x16x32_bf16 v[48:51], v[100:103], v[216:219], 0\n" \
  "s_waitcnt lgkmcnt(15)\n" \
  "v_mfma_f32_16x16x32_bf16 v[48:51], v[216:219], v[120:123], v[48:51]\n" \
  "s_waitcnt lgkmcnt(14)\n" \
  "v_mfma_f32_16x16x32_bf16 v[52:55], v[112:115], v[216:219], 0\n" \
  "v_mfma_f32_16x16x32_bf16 v[52:55], v[220:223], v[120:123], v[52:55]\n" \
  "v_mfma_f32_16x16x32_bf16 v[56:59], v[116:119], v[220:223], 0\n" \
  "s_waitcnt lgkmcnt(12)\n" \
  "v_mfma_f32_16x16x32_bf16 v[56:59], v[220:223], v[156:159], v[56:59]\n" \
  "v_mfma_f32_16x16x32_bf16 v[64:67], v[104:107], v[220:223], 0\n" \
  "v_mfma_f32_16x16x32_bf16 v[64:67], v[216:219], v[156:159], v[64:67]\n" \
  "v_mfma_f32_16x16x32_bf16 v[72:75], v[136:139], v[220:223], 0\n" \
  "v_mfma_f32_16x16x32_bf16 v[76:79], v[112:115], v[220:223], 0\n" \
  "s_waitcnt lgkmcnt(10)\n" \
  "v_mfma_f32_16x16x32_bf16 v[76:79], v[220:223], v[132:135], v[76:79]\n" \
  "v_mfma_f32_16x16x32_bf16 v[80:83], v[100:103], v[220:223], 0\n" \
  "v_mfma_f32_16x16x32_bf16 v[80:83], v[216:219], v[132:135], v[80:83]\n" \
  "v_mfma_f32_16x16x32_bf16 v[84:87], v[96:99], v[220:223], 0\n" \
  "s_waitcnt lgkmcnt(8)\n" \
  "v_mfma_f32_16x16x32_bf16 v[84:87], v[216:219], v[108:111], v[84:87]\n" \
  "v_mfma_f32_16x16x32_bf16 v[88:91], v[108:111], v[220:223], 0\n" \
  "v_mfma_f32_16x16x32_bf16 v[0:3], v[124:127], v[216:219], 0\n" \
  "s_waitcnt lgkmcnt(6)\n" \
  "v_mfma_f32_16x16x32_bf16 v[4:7], v[152:155], v[216:219], 0\n" \
  "v_mfma_f32_16x16x32_bf16 v[8:11], v[152:155], v[220:223], 0\n" \
  "s_waitcnt lgkmcnt(4)\n" \
  "v_mfma_f32_16x16x32_bf16 v[8:11], v[216:219], v[164:167], v[8:11]\n" \
  "v_mfma_f32_16x16x32_bf16 v[12:15], v[164:167], v[220:223], 0\n" \
  "s_waitcnt lgkmcnt(2)\n" \
  "v_mfma_f32_16x16x32_bf16 v[16:19], v[128:131], v[220:223], 0\n" \
  "v_mfma_f32_16x16x32_bf16 v[16:19], v[216:219], v[160:163], v[16:19]\n" \
  "v_mfma_f32_16x16x32_bf16 v[20:23], v[128:131], v[216:219], 0\n" \
  "v_mfma_f32_16x16x32_bf16 v[20:23], v[216:219], v[148:151], v[20:23]\n" \
  "s_waitcnt lgkmcnt(0)\n" \
  "v_mov_b32 v254, 163328\n" \
  "v_mov_b32 v255, 1\n" \
  "s_mov_b64 exec, 1\n" \
  "ds_add_u32 v254, v255 offset:16\n" \
  "s_mov_b64 exec, -1\n" \
  "v_mfma_f32_16x16x32_bf16 v[168:171], v[216:219], v[96:99], 0\n" \
  "v_mfma_f32_16x16x32_bf16 v[172:175], v[204:207], v[224:227], 0\n" \
  "v_mfma_f32_16x16x32_bf16 v[176:179], v[224:227], v[204:207], 0\n" \
  "s_mov_b32 exec_lo, 0x0\n" \
  "s_mov_b32 exec_hi, 0xffffffff\n" \
  "v_mov_b64 v[44:45], v[228:229]\n" \
  "v_mov_b64 v[46:47], v[230:231]\n" \
  "v_mov_b64 v[60:61], v[212:213]\n" \
  "v_mov_b64 v[62:63], v[214:215]\n" \
  "v_mov_b64 v[68:69], v[208:209]\n" \
  "v_mov_b64 v[70:71], v[210:211]\n" \
  "s_mov_b32 exec_lo, 0xff00ff00\n" \
  "s_mov_b32 exec_hi, 0x0\n" \
  "v_add_f32 v92, v92, v168\n" \
  "v_add_f32 v93, v93, v169\n" \
  "v_add_f32 v94, v94, v170\n" \
  "v_add_f32 v95, v95, v171\n" \
  "s_mov_b32 exec_lo, 0xf0000f00\n" \
  "s_mov_b32 exec_hi, 0x0\n" \
  "v_sub_f32 v92, v92, v176\n" \
  "v_sub_f32 v93, v93, v177\n" \
  "v_sub_f32 v94, v94, v178\n" \
  "v_sub_f32 v95, v95, v179\n" \
  "s_mov_b32 exec_lo, 0x0\n" \
  "s_mov_b32 exec_hi, 0xff00ff\n" \
  "v_mov_b64 v[92:93], 0\n" \
  "v_mov_b64 v[94:95], 0\n" \
  "s_mov_b32 exec_lo, 0x0\n" \
  "s_mov_b32 exec_hi, 0xf0000f\n" \
  "v_mov_b64 v[92:93], v[172:173]\n" \
  "v_mov_b64 v[94:95], v[174:175]\n" \
  "s_mov_b64 exec, -1\n" \
  "v_lshlrev_b32 v188, 4, v253\n" \
  "v_and_b32 v96, 4, v252\n" \
  "v_lshl_add_u32 v96, v253, 5, v96\n" \
  "v_mov_b32 v97, 130000\n" \
  "v_cmp_gt_u32 vcc, 8, v252\n" \
  "v_mov_b32 v193, 0x4444\n" \
  "v_mov_b32 v98, 0xeeee\n" \
  "v_cndmask_b32 v189, v97, v96, vcc\n" \
  "v_cndmask_b32 v190, v96, v97, vcc\n" \
  "v_cndmask_b32_e64 v191, 0, 16, vcc\n" \
  "v_cndmask_b32_e64 v192, 16, 0, vcc\n" \
  "v_and_b32 v99, 4, v252\n" \
  "v_cmp_ne_u32 vcc, 0, v99\n" \
  "s_lshl_b32 s35, s28, 11\n" \
  "s_add_u32 s35, s35, 130048\n" \
  "v_cndmask_b32 v193, v193, v98, vcc\n" \
  "v_lshl_add_u32 v194, v252, 4, s35\n" \
  "v_and_b32 v96, 7, v252\n" \
  "v_lshlrev_b32 v96, 5, v96\n" \
  "v_lshl_add_u32 v96, v253, 8, v96\n" \
  "v_lshrrev_b32 v97, 3, v252\n" \
  "v_lshl_add_u32 v96, v97, 2, v96\n" \
  "v_add_u32 v195, s35, v96\n" \
  "v_and_b32 v97, v97, v252\n" \
  "v_lshlrev_b32 v179, 4, v97\n" \
  "s_mov_b32 s50, 65471463\n" \
  "v_mov_b64 v[96:97], 0\n" \
  "v_mov_b32 v98, 130000\n" \
  "ds_write_b64 v98, v[96:97]\n" \
  "ds_write_b64 v98, v[96:97] offset:8\n" \
  "ds_write_b64 v98, v[96:97] offset:16\n" \
  "ds_write_b64 v98, v[96:97] offset:24\n" \
  "s_waitcnt vmcnt(0)\n" \
  "v_cvt_pk_u16_u32 v232, v232, v233\n" \
  "v_cvt_pk_u16_u32 v234, v234, v235\n" \
  "v_cvt_pk_u16_u32 v236, v236, v237\n" \
  "v_cvt_pk_u16_u32 v238, v238, v239\n" \
  "v_pk_min_u16 v232, v232, s50\n" \
  "v_pk_min_u16 v234, v234, s50\n" \
  "v_pk_min_u16 v236, v236, s50\n" \
  "v_pk_min_u16 v238, v238, s50\n" \
  "v_pk_lshrrev_b16 v233, 3, v232 op_sel_hi:[0,1]\n" \
  "v_pk_lshrrev_b16 v235, 3, v234 op_sel_hi:[0,1]\n" \
  "v_pk_lshrrev_b16 v237, 3, v236 op_sel_hi:[0,1]\n" \
  "v_pk_lshrrev_b16 v239, 3, v238 op_sel_hi:[0,1]\n" \
  "v_pk_mad_u16 v232, v232, 8, v233 op_sel_hi:[1,0,1]\n" \
  "v_pk_mad_u16 v234, v234, 8, v235 op_sel_hi:[1,0,1]\n" \
  "v_pk_mad_u16 v236, v236, 8, v237 op_sel_hi:[1,0,1]\n" \
  "v_pk_mad_u16 v238, v238, 8, v239 op_sel_hi:[1,0,1]\n" \
  "v_alignbit_b32 v236, v236, v236, v179\n" \
  "v_alignbit_b32 v238, v238, v238, v179\n" \
  "ds_write2_b32 v195, v232, v234 offset0:0 offset1:4\n" \
  "ds_write2_b32 v195, v236, v238 offset0:2 offset1:6\n" \
  "v_cvt_pk_u16_u32 v240, v240, v241\n" \
  "v_cvt_pk_u16_u32 v242, v242, v243\n" \
  "v_cvt_pk_u16_u32 v244, v244, v245\n" \
  "v_cvt_pk_u16_u32 v246, v246, v247\n" \
  "v_pk_min_u16 v240, v240, s50\n" \
  "v_pk_min_u16 v242, v242, s50\n" \
  "v_pk_min_u16 v244, v244, s50\n" \
  "v_pk_min_u16 v246, v246, s50\n" \
  "v_pk_lshrrev_b16 v241, 3, v240 op_sel_hi:[0,1]\n" \
  "v_pk_lshrrev_b16 v243, 3, v242 op_sel_hi:[0,1]\n" \
  "v_pk_lshrrev_b16 v245, 3, v244 op_sel_hi:[0,1]\n" \
  "v_pk_lshrrev_b16 v247, 3, v246 op_sel_hi:[0,1]\n" \
  "v_pk_mad_u16 v240, v240, 8, v241 op_sel_hi:[1,0,1]\n" \
  "v_pk_mad_u16 v242, v242, 8, v243 op_sel_hi:[1,0,1]\n" \
  "v_pk_mad_u16 v244, v244, 8, v245 op_sel_hi:[1,0,1]\n" \
  "v_pk_mad_u16 v246, v246, 8, v247 op_sel_hi:[1,0,1]\n" \
  "v_alignbit_b32 v244, v244, v244, v179\n" \
  "v_alignbit_b32 v246, v246, v246, v179\n" \
  "v_add_u32 v241, 0x400, v195\n" \
  "ds_write2_b32 v241, v240, v242 offset0:0 offset1:4\n" \
  "ds_write2_b32 v241, v244, v246 offset0:2 offset1:6\n" \
  "v_add_u32 v96, 6400, v250\n" \
  "v_add_u32 v97, 6400, v251\n" \
  "v_mov_b32 v204, v249\n" \
  "v_mov_b32 v98, v254\n" \
  "L1_:\n" \
  "ds_read_b32 v99, v98 offset:12\n" \
  "s_waitcnt lgkmcnt(0)\n" \
  "v_readfirstlane_b32 s35, v99\n" \
  "s_cmp_lt_u32 s35, 4\n" \
  "s_cbranch_scc1 L1_\n" \
  "s_add_u32 s46, s44, 4800\n" \
  "s_addc_u32 s47, s45, 0\n" \
  "global_load_dwordx4 v[208:211], v96, s[44:45] offset:-3200\n" \
  "global_load_dwordx4 v[212:215], v97, s[44:45] offset:-2944\n" \
  "global_load_dwordx4 v[216:219], v96, s[44:45] offset:-1600\n" \
  "global_load_dwordx4 v[220:223], v97, s[44:45] offset:-1344\n" \
  "global_load_dwordx4 v[224:227], v96, s[44:45] offset:0\n" \
  "global_load_dwordx4 v[228:231], v97, s[44:45] offset:256\n" \
  "global_load_dwordx4 v[232:235], v96, s[44:45] offset:1600\n" \
  "global_load_dwordx4 v[236:239], v97, s[44:45] offset:1856\n" \
  "global_load_dwordx4 v[240:243], v96, s[44:45] offset:3200\n" \
  "global_load_dwordx4 v[244:247], v97, s[44:45] offset:3456\n" \
  "global_load_dwordx4 v[248:251], v96, s[46:47] offset:0\n" \
  "global_load_dwordx4 v[252:255], v97, s[46:47] offset:256\n" \
  "s_setprio 1\n" \
  "ds_read_b128 v[180:183], v194\n" \
  "v_add_u32 v194, 0x100, v194\n" \
  "v_mov_b64 v[164:165], 0\n" \
  "v_mov_b64 v[166:167], 0\n" \
  "v_mov_b64 v[168:169], 0\n" \
  "v_mov_b64 v[170:171], 0\n" \
  "v_mov_b64 v[172:173], 0\n" \
  "v_mov_b64 v[174:175], 0\n" \
  "s_waitcnt lgkmcnt(0)\n" \
  "v_mad_u32_u16 v184, v182, 16, v188\n" \
  "ds_read_b128 v[128:131], v184 offset:0\n" \
  "ds_read_b128 v[132:135], v184 offset:64\n" \
  "v_mad_u32_u16 v185, v181, 16, v188\n" \
  "v_mad_u32_u16 v186, v182, 16, v188 op_sel:[1,0,0,0]\n" \
  "ds_read_b128 v[112:115], v185 offset:0\n" \
  "ds_read_b128 v[116:119], v185 offset:64\n" \
  "ds_read_b128 v[136:139], v186 offset:0\n" \
  "ds_read_b128 v[140:143], v186 offset:64\n" \
  "v_mad_u32_u16 v187, v181, 16, v188 op_sel:[1,0,0,0]\n" \
  "ds_read_b128 v[120:123], v187 offset:0\n" \
  "ds_read_b128 v[124:127], v187 offset:64\n" \
  "v_mad_u32_u16 v184, v180, 16, v188 op_sel:[1,0,0,0]\n" \
  "ds_read_b128 v[104:107], v184 offset:0\n" \
  "ds_read_b128 v[108:111], v184 offset:64\n" \
  "v_mad_u32_u16 v185, v180, 16, v188\n" \
  "ds_read_b128 v[96:99], v185 offset:0\n" \
  "s_waitcnt lgkmcnt(8)\n" \
  "s_mov_b32 s51, 3\n" \
  "s_call_b64 s[52:53], LA_loop\n" \
  "LA_ev0:\n" \
  "v_mov_b32 v196, v178\n" \
  "s_waitcnt vmcnt(10)\n" \
  "v_cvt_pk_u16_u32 v208, v208, v209\n" \
  "v_cvt_pk_u16_u32 v210, v210, v211\n" \
  "v_cvt_pk_u16_u32 v212, v212, v213\n" \
  "v_cvt_pk_u16_u32 v214, v214, v215\n" \
  "v_pk_min_u16 v208, v208, s50\n" \
  "v_pk_min_u16 v210, v210, s50\n" \
  "v_pk_min_u16 v212, v212, s50\n" \
  "v_pk_min_u16 v214, v214, s50\n" \
  "v_pk_lshrrev_b16 v209, 3, v208 op_sel_hi:[0,1]\n" \
  "v_pk_lshrrev_b16 v211, 3, v210 op_sel_hi:[0,1]\n" \
  "v_pk_lshrrev_b16 v213, 3, v212 op_sel_hi:[0,1]\n" \
  "v_pk_lshrrev_b16 v215, 3, v214 op_sel_hi:[0,1]\n" \
  "v_pk_mad_u16 v208, v208, 8, v209 op_sel_hi:[1,0,1]\n" \
  "v_pk_mad_u16 v210, v210, 8, v211 op_sel_hi:[1,0,1]\n" \
  "v_pk_mad_u16 v212, v212, 8, v213 op_sel_hi:[1,0,1]\n" \
  "v_pk_mad_u16 v214, v214, 8, v215 op_sel_hi:[1,0,1]\n" \
  "v_alignbit_b32 v212, v212, v212, v179\n" \
  "v_alignbit_b32 v214, v214, v214, v179\n" \
  "ds_write2_b32 v195, v208, v210 offset0:0 offset1:4\n" \
  "ds_write2_b32 v195, v212, v214 offset0:2 offset1:6\n" \
  "s_mov_b32 s51, 4\n" \
  "s_call_b64 s[52:53], LA_loop\n" \
  "LA_ev1:\n" \
  "v_mov_b32 v197, v178\n" \
  "s_waitcnt vmcnt(8)\n" \
  "v_cvt_pk_u16_u32 v216, v216, v217\n" \
  "v_cvt_pk_u16_u32 v218, v218, v219\n" \
  "v_cvt_pk_u16_u32 v220, v220, v221\n" \
  "v_cvt_pk_u16_u32 v222, v222, v223\n" \
  "v_pk_min_u16 v216, v216, s50\n" \
  "v_pk_min_u16 v218, v218, s50\n" \
  "v_pk_min_u16 v220, v220, s50\n" \
  "v_pk_min_u16 v222, v222, s50\n" \
  "v_pk_lshrrev_b16 v217, 3, v216 op_sel_hi:[0,1]\n" \
  "v_pk_lshrrev_b16 v219, 3, v218 op_sel_hi:[0,1]\n" \
  "v_pk_lshrrev_b16 v221, 3, v220 op_sel_hi:[0,1]\n" \
  "v_pk_lshrrev_b16 v223, 3, v222 op_sel_hi:[0,1]\n" \
  "v_pk_mad_u16 v216, v216, 8, v217 op_sel_hi:[1,0,1]\n" \
  "v_pk_mad_u16 v218, v218, 8, v219 op_sel_hi:[1,0,1]\n" \
  "v_pk_mad_u16 v220, v220, 8, v221 op_sel_hi:[1,0,1]\n" \
  "v_pk_mad_u16 v222, v222, 8, v223 op_sel_hi:[1,0,1]\n" \
  "v_alignbit_b32 v220, v220, v220, v179\n" \
  "v_alignbit_b32 v222, v222, v222, v179\n" \
  "v_add_u32 v217, 0x400, v195\n" \
  "ds_write2_b32 v217, v216, v218 offset0:0 offset1:4\n" \
  "ds_write2_b32 v217, v220, v222 offset0:2 offset1:6\n" \
  "v_subrev_u32 v194, 0x800, v194\n" \
  "s_mov_b32 s51, 4\n" \
  "s_call_b64 s[52:53], LA_loop\n" \
  "LA_ev2:\n" \
  "v_mov_b32 v198, v178\n" \
  "s_waitcnt vmcnt(6)\n" \
  "v_cvt_pk_u16_u32 v224, v224, v225\n" \
  "v_cvt_pk_u16_u32 v226, v226, v227\n" \
  "v_cvt_pk_u16_u32 v228, v228, v229\n" \
  "v_cvt_pk_u16_u32 v230, v230, v231\n" \
  "v_pk_min_u16 v224, v224, s50\n" \
  "v_pk_min_u16 v226, v226, s50\n" \
  "v_pk_min_u16 v228, v228, s50\n" \
  "v_pk_min_u16 v230, v230, s50\n" \
  "v_pk_lshrrev_b16 v225, 3, v224 op_sel_hi:[0,1]\n" \
  "v_pk_lshrrev_b16 v227, 3, v226 op_sel_hi:[0,1]\n" \
  "v_pk_lshrrev_b16 v229, 3, v228 op_sel_hi:[0,1]\n" \
  "v_pk_lshrrev_b16 v231, 3, v230 op_sel_hi:[0,1]\n" \
  "v_pk_mad_u16 v224, v224, 8, v225 op_sel_hi:[1,0,1]\n" \
  "v_pk_mad_u16 v226, v226, 8, v227 op_sel_hi:[1,0,1]\n" \
  "v_pk_mad_u16 v228, v228, 8, v229 op_sel_hi:[1,0,1]\n" \
  "v_pk_mad_u16 v230, v230, 8, v231 op_sel_hi:[1,0,1]\n" \
  "v_alignbit_b32 v228, v228, v228, v179\n" \
  "v_alignbit_b32 v230, v230, v230, v179\n" \
  "ds_write2_b32 v195, v224, v226 offset0:0 offset1:4\n" \
  "ds_write2_b32 v195, v228, v230 offset0:2 offset1:6\n" \
  "s_mov_b32 s51, 4\n" \
  "s_call_b64 s[52:53], LA_loop\n" \
  "LA_ev3:\n" \
  "v_mov_b32 v199, v178\n" \
  "s_waitcnt vmcnt(4)\n" \
  "v_cvt_pk_u16_u32 v232, v232, v233\n" \
  "v_cvt_pk_u16_u32 v234, v234, v235\n" \
  "v_cvt_pk_u16_u32 v236, v236, v237\n" \
  "v_cvt_pk_u16_u32 v238, v238, v239\n" \
  "v_pk_min_u16 v232, v232, s50\n" \
  "v_pk_min_u16 v234, v234, s50\n" \
  "v_pk_min_u16 v236, v236, s50\n" \
  "v_pk_min_u16 v238, v238, s50\n" \
  "v_pk_lshrrev_b16 v233, 3, v232 op_sel_hi:[0,1]\n" \
  "v_pk_lshrrev_b16 v235, 3, v234 op_sel_hi:[0,1]\n" \
  "v_pk_lshrrev_b16 v237, 3, v236 op_sel_hi:[0,1]\n" \
  "v_pk_lshrrev_b16 v239, 3, v238 op_sel_hi:[0,1]\n" \
  "v_pk_mad_u16 v232, v232, 8, v233 op_sel_hi:[1,0,1]\n" \
  "v_pk_mad_u16 v234, v234, 8, v235 op_sel_hi:[1,0,1]\n" \
  "v_pk_mad_u16 v236, v236, 8, v237 op_sel_hi:[1,0,1]\n" \
  "v_pk_mad_u16 v238, v238, 8, v239 op_sel_hi:[1,0,1]\n" \
  "v_alignbit_b32 v236, v236, v236, v179\n" \
  "v_alignbit_b32 v238, v238, v238, v179\n" \
  "v_add_u32 v233, 0x400, v195\n" \
  "ds_write2_b32 v233, v232, v234 offset0:0 offset1:4\n" \
  "ds_write2_b32 v233, v236, v238 offset0:2 offset1:6\n" \
  "v_subrev_u32 v194, 0x800, v194\n" \
  "s_mov_b32 s51, 4\n" \
  "s_call_b64 s[52:53], LA_loop\n" \
  "LA_ev4:\n" \
  "v_mov_b32 v200, v178\n" \
  "s_waitcnt vmcnt(2)\n" \
  "v_cvt_pk_u16_u32 v240, v240, v241\n" \
  "v_cvt_pk_u16_u32 v242, v242, v243\n" \
  "v_cvt_pk_u16_u32 v244, v244, v245\n" \
  "v_cvt_pk_u16_u32 v246, v246, v247\n" \
  "v_pk_min_u16 v240, v240, s50\n" \
  "v_pk_min_u16 v242, v242, s50\n" \
  "v_pk_min_u16 v244, v244, s50\n" \
  "v_pk_min_u16 v246, v246, s50\n" \
  "v_pk_lshrrev_b16 v241, 3, v240 op_sel_hi:[0,1]\n" \
  "v_pk_lshrrev_b16 v243, 3, v242 op_sel_hi:[0,1]\n" \
  "v_pk_lshrrev_b16 v245, 3, v244 op_sel_hi:[0,1]\n" \
  "v_pk_lshrrev_b16 v247, 3, v246 op_sel_hi:[0,1]\n" \
  "v_pk_mad_u16 v240, v240, 8, v241 op_sel_hi:[1,0,1]\n" \
  "v_pk_mad_u16 v242, v242, 8, v243 op_sel_hi:[1,0,1]\n" \
  "v_pk_mad_u16 v244, v244, 8, v245 op_sel_hi:[1,0,1]\n" \
  "v_pk_mad_u16 v246, v246, 8, v247 op_sel_hi:[1,0,1]\n" \
  "v_alignbit_b32 v244, v244, v244, v179\n" \
  "v_alignbit_b32 v246, v246, v246, v179\n" \
  "ds_write2_b32 v195, v240, v242 offset0:0 offset1:4\n" \
  "ds_write2_b32 v195, v244, v246 offset0:2 offset1:6\n" \
  "s_mov_b32 s51, 4\n" \
  "s_call_b64 s[52:53], LA_loop\n" \
  "LA_ev5:\n" \
  "v_mov_b32 v201, v178\n" \
  "s_waitcnt vmcnt(0)\n" \
  "v_cvt_pk_u16_u32 v248, v248, v249\n" \
  "v_cvt_pk_u16_u32 v250, v250, v251\n" \
  "v_cvt_pk_u16_u32 v252, v252, v253\n" \
  "v_cvt_pk_u16_u32 v254, v254, v255\n" \
  "v_pk_min_u16 v248, v248, s50\n" \
  "v_pk_min_u16 v250, v250, s50\n" \
  "v_pk_min_u16 v252, v252, s50\n" \
  "v_pk_min_u16 v254, v254, s50\n" \
  "v_pk_lshrrev_b16 v249, 3, v248 op_sel_hi:[0,1]\n" \
  "v_pk_lshrrev_b16 v251, 3, v250 op_sel_hi:[0,1]\n" \
  "v_pk_lshrrev_b16 v253, 3, v252 op_sel_hi:[0,1]\n" \
  "v_pk_lshrrev_b16 v255, 3, v254 op_sel_hi:[0,1]\n" \
  "v_pk_mad_u16 v248, v248, 8, v249 op_sel_hi:[1,0,1]\n" \
  "v_pk_mad_u16 v250, v250, 8, v251 op_sel_hi:[1,0,1]\n" \
  "v_pk_mad_u16 v252, v252, 8, v253 op_sel_hi:[1,0,1]\n" \
  "v_pk_mad_u16 v254, v254, 8, v255 op_sel_hi:[1,0,1]\n" \
  "v_alignbit_b32 v252, v252, v252, v179\n" \
  "v_alignbit_b32 v254, v254, v254, v179\n" \
  "v_add_u32 v249, 0x400, v195\n" \
  "ds_write2_b32 v249, v248, v250 offset0:0 offset1:4\n" \
  "ds_write2_b32 v249, v252, v254 offset0:2 offset1:6\n" \
  "v_subrev_u32 v194, 0x800, v194\n" \
  "s_mov_b32 s51, 4\n" \
  "s_call_b64 s[52:53], LA_loop\n" \
  "LA_ev6:\n" \
  "v_mov_b32 v202, v178\n" \
  "s_mov_b32 s51, 4\n" \
  "s_call_b64 s[52:53], LA_loop\n" \
  "LA_ev7:\n" \
  "v_mov_b32 v203, v178\n" \
  "ds_read_b128 v[100:103], v185 offset:64\n" \
  "v_mad_u32_u16 v186, v183, v191, v189\n" \
  "ds_read2_b32 v[144:145], v186 offset0:0 offset1:2\n" \
  "ds_read2_b32 v[146:147], v186 offset0:4 offset1:6\n" \
  "v_mad_u32_u16 v187, v183, v192, v190\n" \
  "ds_read2_b32 v[148:149], v187 offset0:0 offset1:2\n" \
  "ds_read2_b32 v[150:151], v187 offset0:4 offset1:6\n" \
  "s_lshr_b32 s35, s30, 2\n" \
  "s_lshl3_add_u32 s33, s29, s35\n" \
  "s_and_b32 s34, s30, 3\n" \
  "s_lshl_b32 s59, s27, 11\n" \
  "s_mul_i32 s35, s33, 1600000\n" \
  "s_add_u32 s54, s24, s35\n" \
  "s_addc_u32 s55, s25, 0\n" \
  "s_mul_i32 s35, s34, 819200\n" \
  "s_lshl1_add_u32 s35, s59, s35\n" \
  "s_add_u32 s35, s35, 102400000\n" \
  "s_add_u32 s56, s24, s35\n" \
  "s_addc_u32 s57, s25, 0\n" \
  "v_mbcnt_lo_u32_b32 v189, -1, 0\n" \
  "v_mbcnt_hi_u32_b32 v189, -1, v189\n" \
  "v_lshlrev_b32 v188, 4, v189\n" \
  "s_waitcnt lgkmcnt(5)\n" \
  "v_mfma_f32_16x16x32_bf16 v[152:155], v[112:115], v[112:115], 0\n" \
  "v_fmac_f32 v176, v156, v88\n" \
  "v_fmac_f32 v176, v157, v89\n" \
  "v_mfma_f32_16x16x32_bf16 v[152:155], v[116:119], v[116:119], v[152:155]\n" \
  "v_fmac_f32 v176, v158, v90\n" \
  "v_fmac_f32 v176, v159, v91\n" \
  "v_mfma_f32_16x16x32_bf16 v[156:159], v[128:131], v[128:131], 0\n" \
  "v_fmac_f32 v176, v160, v92\n" \
  "v_fmac_f32 v176, v161, v93\n" \
  "v_mfma_f32_16x16x32_bf16 v[156:159], v[132:135], v[132:135], v[156:159]\n" \
  "v_fmac_f32 v176, v162, v94\n" \
  "v_fma_f32 v177, v163, v95, v176\n" \
  "v_mfma_f32_16x16x32_bf16 v[160:163], v[128:131], v[136:139], 0\n" \
  "v_mul_f32 v176, v152, v0\n" \
  "v_fmac_f32 v176, v153, v1\n" \
  "v_add_f32_dpp v177, v177, v177 row_ror:8 row_mask:0xf bank_mask:0xf\n" \
  "v_mfma_f32_16x16x32_bf16 v[160:163], v[132:135], v[140:143], v[160:163]\n" \
  "v_fmac_f32 v176, v154, v2\n" \
  "v_fmac_f32 v176, v155, v3\n" \
  "v_mfma_f32_16x16x32_bf16 v[152:155], v[136:139], v[136:139], 0\n" \
  "v_fmac_f32 v176, v156, v4\n" \
  "v_fmac_f32 v176, v157, v5\n" \
  "v_mfma_f32_16x16x32_bf16 v[152:155], v[140:143], v[140:143], v[152:155]\n" \
  "v_fmac_f32 v176, v158, v6\n" \
  "v_fmac_f32 v176, v159, v7\n" \
  "s_waitcnt lgkmcnt(0)\n" \
  "s_setprio 3\n" \
  "s_barrier\n" \
  "global_load_dwordx4 v[208:211], v188, s[56:57] offset:0 sc1\n" \
  "global_load_dwordx4 v[212:215], v188, s[56:57] offset:1024 sc1\n" \
  "global_load_dwordx4 v[216:219], v188, s[56:57] offset:2048 sc1\n" \
  "global_load_dwordx4 v[220:223], v188, s[56:57] offset:3072 sc1\n" \
  "s_lshr_b32 s35, s33, 2\n" \
  "s_lshl_b32 s35, s35, 6\n" \
  "s_and_b32 s36, s33, 3\n" \
  "s_lshl3_add_u32 s35, s36, s35\n" \
  "v_and_b32 v190, 16, v189\n" \
  "v_lshl_add_u32 v190, v190, 1, s35\n" \
  "global_load_dwordx2 v[204:205], v190, s[16:17]\n" \
  "global_load_dwordx2 v[206:207], v190, s[18:19]\n" \
  "v_mfma_f32_16x16x32_bf16 v[156:159], v[112:115], v[136:139], 0\n" \
  "v_fmac_f32 v176, v160, v8\n" \
  "v_fmac_f32 v176, v161, v9\n" \
  "v_mov_b32_dpp v178, v178 row_ror:4 row_mask:0xf bank_mask:0xf\n" \
  "v_mfma_f32_16x16x32_bf16 v[156:159], v[116:119], v[140:143], v[156:159]\n" \
  "v_fmac_f32 v176, v162, v10\n" \
  "v_fmac_f32 v176, v163, v11\n" \
  "v_mfma_f32_16x16x32_bf16 v[160:163], v[112:115], v[128:131], 0\n" \
  "v_fmac_f32 v176, v152, v12\n" \
  "v_fmac_f32 v176, v153, v13\n" \
  "v_mfma_f32_16x16x32_bf16 v[160:163], v[116:119], v[132:135], v[160:163]\n" \
  "v_fmac_f32 v176, v154, v14\n" \
  "v_fmac_f32 v176, v155, v15\n" \
  "v_mov_b64 v[174:175], 0\n" \
  "v_lshrrev_b32 v180, 1, v189\n" \
  "v_mfma_f32_16x16x32_bf16 v[152:155], v[112:115], v[120:123], 0\n" \
  "v_fmac_f32 v176, v156, v16\n" \
  "v_fmac_f32 v176, v157, v17\n" \
  "v_mov_b64 v[170:171], 0\n" \
  "v_xor_b32 v180, v180, v189\n" \
  "v_mfma_f32_16x16x32_bf16 v[152:155], v[116:119], v[124:127], v[152:155]\n" \
  "v_fmac_f32 v176, v158, v18\n" \
  "v_fmac_f32 v176, v159, v19\n" \
  "v_and_b32 v180, 8, v180\n" \
  "v_mfma_f32_16x16x32_bf16 v[156:159], v[120:123], v[136:139], 0\n" \
  "v_fmac_f32 v176, v160, v20\n" \
  "v_fmac_f32 v176, v161, v21\n" \
  "v_and_or_b32 v182, v189, 4, v180\n" \
  "v_mfma_f32_16x16x32_bf16 v[156:159], v[124:127], v[140:143], v[156:159]\n" \
  "v_fmac_f32 v176, v162, v22\n" \
  "v_fmac_f32 v176, v163, v23\n" \
  "v_mov_b64 v[166:167], 0\n" \
  "v_mov_b32 v183, 0x3f80\n" \
  "v_mfma_f32_16x16x32_bf16 v[160:163], v[104:107], v[128:131], 0\n" \
  "v_fmac_f32 v176, v152, v24\n" \
  "v_fmac_f32 v176, v153, v25\n" \
  "v_cmp_eq_u32 vcc, 0, v182\n" \
  "v_mfma_f32_16x16x32_bf16 v[160:163], v[108:111], v[132:135], v[160:163]\n" \
  "v_fmac_f32 v176, v154, v26\n" \
  "v_fmac_f32 v176, v155, v27\n" \
  "v_and_b32 v192, 3, v189\n" \
  "v_mfma_f32_16x16x32_bf16 v[152:155], v[120:123], v[128:131], 0\n" \
  "v_fmac_f32 v176, v156, v28\n" \
  "v_fmac_f32 v176, v157, v29\n" \
  "v_mul_u32_u24 v192, 0x5555, v192\n" \
  "v_mfma_f32_16x16x32_bf16 v[152:155], v[124:127], v[132:135], v[152:155]\n" \
  "v_fmac_f32 v176, v158, v30\n" \
  "v_fmac_f32 v176, v159, v31\n" \
  "v_add_f32_dpp v178, v177, v177 row_ror:4 row_mask:0xf bank_mask:0x1\n" \
  "v_cndmask_b32 v180, 0, v183, vcc\n" \
  "v_mfma_f32_16x16x32_bf16 v[156:159], v[96:99], v[128:131], 0\n" \
  "v_fmac_f32 v176, v160, v32\n" \
  "v_fmac_f32 v176, v161, v33\n" \
  "v_cmp_eq_u32 vcc, 4, v182\n" \
  "v_mfma_f32_16x16x32_bf16 v[156:159], v[100:103], v[132:135], v[156:159]\n" \
  "v_fmac_f32 v176, v162, v34\n" \
  "v_fmac_f32 v176, v163, v35\n" \
  "v_add_u32 v189, 16000, v188\n" \
  "v_smfmac_f32_16x16x64_bf16 v[172:175], v[144:147], v[128:135], v193\n" \
  "v_fmac_f32 v176, v152, v36\n" \
  "v_fmac_f32 v176, v153, v37\n" \
  "v_add_u32 v190, 32000, v188\n" \
  "v_smfmac_f32_16x16x64_bf16 v[172:175], v[148:151], v[136:143], v193\n" \
  "v_fmac_f32 v176, v154, v38\n" \
  "v_fmac_f32 v176, v155, v39\n" \
  "v_cndmask_b32 v181, 0, v183, vcc\n" \
  "v_mfma_f32_16x16x32_bf16 v[160:163], v[96:99], v[112:115], 0\n" \
  "v_fmac_f32 v176, v156, v40\n" \
  "v_fmac_f32 v176, v157, v41\n" \
  "v_add_u32 v191, 48000, v188\n" \
  "v_mfma_f32_16x16x32_bf16 v[160:163], v[100:103], v[116:119], v[160:163]\n" \
  "v_fmac_f32 v176, v158, v42\n" \
  "v_fmac_f32 v176, v159, v43\n" \
  "v_mov_b64 v[182:183], v[180:181]\n" \
  "v_mfma_f32_16x16x32_bf16 v[152:155], v[104:107], v[112:115], 0\n" \
  "v_fmac_f32 v176, v172, v44\n" \
  "v_fmac_f32 v176, v173, v45\n" \
  "v_mfma_f32_16x16x32_bf16 v[152:155], v[108:111], v[116:119], v[152:155]\n" \
  "v_fmac_f32 v176, v174, v46\n" \
  "v_fmac_f32 v176, v175, v47\n" \
  "v_mfma_f32_16x16x32_bf16 v[156:159], v[104:107], v[136:139], 0\n" \
  "v_fmac_f32 v176, v160, v48\n" \
  "v_fmac_f32 v176, v161, v49\n" \
  "v_mfma_f32_16x16x32_bf16 v[156:159], v[108:111], v[140:143], v[156:159]\n" \
  "v_fmac_f32 v176, v162, v50\n" \
  "v_fmac_f32 v176, v163, v51\n" \
  "v_smfmac_f32_16x16x64_bf16 v[168:171], v[144:147], v[112:119], v193\n" \
  "v_fmac_f32 v176, v152, v52\n" \
  "v_fmac_f32 v176, v153, v53\n" \
  "v_smfmac_f32_16x16x64_bf16 v[168:171], v[148:151], v[120:127], v193\n" \
  "v_fmac_f32 v176, v154, v54\n" \
  "v_fmac_f32 v176, v155, v55\n" \
  "v_mfma_f32_16x16x32_bf16 v[160:163], v[96:99], v[136:139], 0\n" \
  "v_fmac_f32 v176, v156, v56\n" \
  "v_fmac_f32 v176, v157, v57\n" \
  "v_mfma_f32_16x16x32_bf16 v[160:163], v[100:103], v[140:143], v[160:163]\n" \
  "v_fmac_f32 v176, v158, v58\n" \
  "v_fmac_f32 v176, v159, v59\n" \
  "v_smfmac_f32_16x16x64_bf16 v[164:167], v[148:151], v[104:111], v193\n" \
  "v_fmac_f32 v176, v168, v60\n" \
  "v_fmac_f32 v176, v169, v61\n" \
  "v_smfmac_f32_16x16x64_bf16 v[164:167], v[144:147], v[96:103], v193\n" \
  "v_fmac_f32 v176, v170, v62\n" \
  "v_fmac_f32 v176, v171, v63\n" \
  "v_mfma_f32_16x16x32_bf16 v[152:155], v[120:123], v[120:123], 0\n" \
  "v_fmac_f32 v176, v160, v64\n" \
  "v_fmac_f32 v176, v161, v65\n" \
  "v_mfma_f32_16x16x32_bf16 v[152:155], v[124:127], v[124:127], v[152:155]\n" \
  "v_fmac_f32 v176, v162, v66\n" \
  "v_fmac_f32 v176, v163, v67\n" \
  "v_mfma_f32_16x16x32_bf16 v[156:159], v[104:107], v[120:123], 0\n" \
  "v_fmac_f32 v176, v164, v68\n" \
  "v_fmac_f32 v176, v165, v69\n" \
  "v_mfma_f32_16x16x32_bf16 v[156:159], v[108:111], v[124:127], v[156:159]\n" \
  "v_fmac_f32 v176, v166, v70\n" \
  "v_fmac_f32 v176, v167, v71\n" \
  "v_mfma_f32_16x16x32_bf16 v[160:163], v[96:99], v[120:123], 0\n" \
  "v_fmac_f32 v176, v152, v72\n" \
  "v_fmac_f32 v176, v153, v73\n" \
  "v_mfma_f32_16x16x32_bf16 v[160:163], v[100:103], v[124:127], v[160:163]\n" \
  "v_fmac_f32 v176, v154, v74\n" \
  "v_fmac_f32 v176, v155, v75\n" \
  "v_mfma_f32_16x16x32_bf16 v[152:155], v[96:99], v[104:107], 0\n" \
  "v_fmac_f32 v176, v156, v76\n" \
  "v_fmac_f32 v176, v157, v77\n" \
  "v_mfma_f32_16x16x32_bf16 v[152:155], v[100:103], v[108:111], v[152:155]\n" \
  "v_fmac_f32 v176, v158, v78\n" \
  "v_fmac_f32 v176, v159, v79\n" \
  "v_mfma_f32_16x16x32_bf16 v[156:159], v[104:107], v[104:107], 0\n" \
  "v_fmac_f32 v176, v160, v80\n" \
  "v_fmac_f32 v176, v161, v81\n" \
  "v_mfma_f32_16x16x32_bf16 v[156:159], v[108:111], v[108:111], v[156:159]\n" \
  "v_fmac_f32 v176, v162, v82\n" \
  "v_fmac_f32 v176, v163, v83\n" \
  "v_mfma_f32_16x16x32_bf16 v[160:163], v[148:151], v[144:147], 0\n" \
  "v_fmac_f32 v176, v152, v84\n" \
  "v_fmac_f32 v176, v153, v85\n" \
  "v_mfma_f32_16x16x32_bf16 v[160:163], v[96:99], v[96:99], v[160:163]\n" \
  "v_fmac_f32 v176, v154, v86\n" \
  "v_fmac_f32 v176, v155, v87\n" \
  "v_mfma_f32_16x16x32_bf16 v[160:163], v[100:103], v[100:103], v[160:163]\n" \
  "v_fmac_f32 v176, v156, v88\n" \
  "v_fmac_f32 v176, v157, v89\n" \
  "v_fmac_f32 v176, v158, v90\n" \
  "v_fmac_f32 v176, v159, v91\n" \
  "s_nop 3\n" \
  "v_fmac_f32 v176, v160, v92\n" \
  "v_fmac_f32 v176, v161, v93\n" \
  "v_fmac_f32 v176, v162, v94\n" \
  "v_fma_f32 v177, v163, v95, v176\n" \
  "s_nop 1\n" \
  "v_add_f32_dpp v177, v177, v177 row_ror:8 row_mask:0xf bank_mask:0xf\n" \
  "v_mov_b32_dpp v178, v178 row_ror:4 row_mask:0xf bank_mask:0xf\n" \
  "s_nop 1\n" \
  "v_add_f32_dpp v178, v177, v177 row_ror:4 row_mask:0xf bank_mask:0x1\n" \
  "s_nop 1\n" \
  "v_mov_b32_dpp v196, v178 row_ror:8 row_mask:0xf bank_mask:0xc\n" \
  "s_nop 1\n" \
  "v_permlane16_swap_b32 v196, v197\n" \
  "v_permlane16_swap_b32 v198, v199\n" \
  "v_permlane16_swap_b32 v200, v201\n" \
  "v_permlane16_swap_b32 v202, v203\n" \
  "v_add_f32 v196, v196, v197\n" \
  "v_add_f32 v198, v198, v199\n" \
  "v_add_f32 v200, v200, v201\n" \
  "v_add_f32 v202, v202, v203\n" \
  "v_lshrrev_b32 v96, 4, v188\n" \
  "v_and_b32 v97, 1, v96\n" \
  "v_lshrrev_b32 v98, 4, v96\n" \
  "v_permlane32_swap_b32 v196, v198\n" \
  "v_permlane32_swap_b32 v200, v202\n" \
  "v_add_f32 v196, v196, v198\n" \
  "v_add_f32 v200, v200, v202\n" \
  "v_cmp_eq_u32 vcc, 1, v97\n" \
  "v_lshlrev_b32 v97, 2, v97\n" \
  "v_add_lshl_u32 v97, v97, v98, 2\n" \
  "v_bfe_u32 v98, v96, 2, 2\n" \
  "v_add_f32_dpp v196, v196, v196 quad_perm:[1,0,3,2] row_mask:0xf bank_mask:0xf\n" \
  "v_add_f32_dpp v200, v200, v200 quad_perm:[1,0,3,2] row_mask:0xf bank_mask:0xf\n" \
  "v_sub_u32 v97, v97, v98\n" \
  "v_cndmask_b32 v196, v196, v200, vcc\n" \
  "v_add_u32 v97, 1, v97\n" \
  "v_and_b32 v97, 31, v97\n" \
  "v_add_f32_dpp v196, v196, v196 quad_perm:[2,3,0,1] row_mask:0xf bank_mask:0xf\n" \
  "v_lshlrev_b32 v97, 2, v97\n" \
  "s_lshl2_add_u32 s35, s7, s28\n" \
  "s_lshl_b32 s35, s35, 7\n" \
  "s_lshl_b32 s36, s31, 16\n" \
  "s_add_u32 s35, s35, s36\n" \
  "s_add_u32 s35, s35, 109871104\n" \
  "s_add_u32 s40, s24, s35\n" \
  "s_addc_u32 s41, s25, 0\n" \
  "v_min_u32 v196, -2, v196\n" \
  "s_waitcnt vmcnt(0)\n" \
  "s_mov_b32 exec_lo, 0x33333333\n" \
  "s_mov_b32 exec_hi, 0x33333333\n" \
  "global_store_dword v97, v196, s[40:41] sc1\n" \
  "s_mov_b64 exec, -1\n" \
  "v_lshlrev_b32 v184, 16, v204\n" \
  "v_and_b32 v185, 0xffff0000, v204\n" \
  "v_lshlrev_b32 v186, 16, v205\n" \
  "v_and_b32 v187, 0xffff0000, v205\n" \
  "s_branch LG_start\n" \
  "LA_loop:\n" \
  "v_mfma_f32_16x16x32_bf16 v[152:155], v[112:115], v[112:115], 0\n" \
  "s_waitcnt lgkmcnt(7)\n" \
  "ds_read_b128 v[100:103], v185 offset:64\n" \
  "v_fmac_f32 v176, v156, v88\n" \
  "v_fmac_f32 v176, v157, v89\n" \
  "v_mad_u32_u16 v186, v183, v191, v189\n" \
  "v_mfma_f32_16x16x32_bf16 v[152:155], v[116:119], v[116:119], v[152:155]\n" \
  "ds_read2_b32 v[144:145], v186 offset0:0 offset1:2\n" \
  "v_fmac_f32 v176, v158, v90\n" \
  "v_fmac_f32 v176, v159, v91\n" \
  "v_mfma_f32_16x16x32_bf16 v[156:159], v[128:131], v[128:131], 0\n" \
  "ds_read2_b32 v[146:147], v186 offset0:4 offset1:6\n" \
  "v_fmac_f32 v176, v160, v92\n" \
  "v_fmac_f32 v176, v161, v93\n" \
  "v_mad_u32_u16 v187, v183, v192, v190\n" \
  "v_mfma_f32_16x16x32_bf16 v[156:159], v[132:135], v[132:135], v[156:159]\n" \
  "s_waitcnt lgkmcnt(9)\n" \
  "ds_read2_b32 v[148:149], v187 offset0:0 offset1:2\n" \
  "v_fmac_f32 v176, v162, v94\n" \
  "v_fma_f32 v177, v163, v95, v176\n" \
  "v_mfma_f32_16x16x32_bf16 v[160:163], v[128:131], v[136:139], 0\n" \
  "s_waitcnt lgkmcnt(9)\n" \
  "ds_read2_b32 v[150:151], v187 offset0:4 offset1:6\n" \
  "v_mul_f32 v176, v152, v0\n" \
  "v_fmac_f32 v176, v153, v1\n" \
  "v_add_f32_dpp v177, v177, v177 row_ror:8 row_mask:0xf bank_mask:0xf\n" \
  "v_mfma_f32_16x16x32_bf16 v[160:163], v[132:135], v[140:143], v[160:163]\n" \
  "ds_read_b128 v[180:183], v194\n" \
  "v_fmac_f32 v176, v154, v2\n" \
  "v_fmac_f32 v176, v155, v3\n" \
  "v_mfma_f32_16x16x32_bf16 v[152:155], v[136:139], v[136:139], 0\n" \
  "v_fmac_f32 v176, v156, v4\n" \
  "v_fmac_f32 v176, v157, v5\n" \
  "v_mfma_f32_16x16x32_bf16 v[152:155], v[140:143], v[140:143], v[152:155]\n" \
  "v_fmac_f32 v176, v158, v6\n" \
  "v_fmac_f32 v176, v159, v7\n" \
  "v_add_u32 v194, 0x100, v194\n" \
  "v_mfma_f32_16x16x32_bf16 v[156:159], v[112:115], v[136:139], 0\n" \
  "v_fmac_f32 v176, v160, v8\n" \
  "v_fmac_f32 v176, v161, v9\n" \
  "v_mov_b32_dpp v178, v178 row_ror:4 row_mask:0xf bank_mask:0xf\n" \
  "v_mfma_f32_16x16x32_bf16 v[156:159], v[116:119], v[140:143], v[156:159]\n" \
  "v_fmac_f32 v176, v162, v10\n" \
  "v_fmac_f32 v176, v163, v11\n" \
  "v_mfma_f32_16x16x32_bf16 v[160:163], v[112:115], v[128:131], 0\n" \
  "v_fmac_f32 v176, v152, v12\n" \
  "v_fmac_f32 v176, v153, v13\n" \
  "v_mfma_f32_16x16x32_bf16 v[160:163], v[116:119], v[132:135], v[160:163]\n" \
  "s_waitcnt lgkmcnt(10)\n" \
  "v_fmac_f32 v176, v154, v14\n" \
  "v_fmac_f32 v176, v155, v15\n" \
  "v_mov_b64 v[174:175], 0\n" \
  "v_mfma_f32_16x16x32_bf16 v[152:155], v[112:115], v[120:123], 0\n" \
  "s_waitcnt lgkmcnt(9)\n" \
  "v_fmac_f32 v176, v156, v16\n" \
  "v_fmac_f32 v176, v157, v17\n" \
  "v_mov_b64 v[170:171], 0\n" \
  "v_mfma_f32_16x16x32_bf16 v[152:155], v[116:119], v[124:127], v[152:155]\n" \
  "v_fmac_f32 v176, v158, v18\n" \
  "v_fmac_f32 v176, v159, v19\n" \
  "v_mfma_f32_16x16x32_bf16 v[156:159], v[120:123], v[136:139], 0\n" \
  "v_fmac_f32 v176, v160, v20\n" \
  "v_fmac_f32 v176, v161, v21\n" \
  "v_mfma_f32_16x16x32_bf16 v[156:159], v[124:127], v[140:143], v[156:159]\n" \
  "s_waitcnt lgkmcnt(8)\n" \
  "v_fmac_f32 v176, v162, v22\n" \
  "v_fmac_f32 v176, v163, v23\n" \
  "v_mov_b64 v[166:167], 0\n" \
  "v_mfma_f32_16x16x32_bf16 v[160:163], v[104:107], v[128:131], 0\n" \
  "s_waitcnt lgkmcnt(0)\n" \
  "v_fmac_f32 v176, v152, v24\n" \
  "v_fmac_f32 v176, v153, v25\n" \
  "v_mad_u32_u16 v184, v182, 16, v188\n" \
  "v_mfma_f32_16x16x32_bf16 v[160:163], v[108:111], v[132:135], v[160:163]\n" \
  "v_fmac_f32 v176, v154, v26\n" \
  "v_fmac_f32 v176, v155, v27\n" \
  "v_mfma_f32_16x16x32_bf16 v[152:155], v[120:123], v[128:131], 0\n" \
  "v_fmac_f32 v176, v156, v28\n" \
  "v_fmac_f32 v176, v157, v29\n" \
  "v_mfma_f32_16x16x32_bf16 v[152:155], v[124:127], v[132:135], v[152:155]\n" \
  "v_fmac_f32 v176, v158, v30\n" \
  "v_fmac_f32 v176, v159, v31\n" \
  "v_add_f32_dpp v178, v177, v177 row_ror:4 row_mask:0xf bank_mask:0x1\n" \
  "v_mfma_f32_16x16x32_bf16 v[156:159], v[96:99], v[128:131], 0\n" \
  "v_fmac_f32 v176, v160, v32\n" \
  "v_fmac_f32 v176, v161, v33\n" \
  "v_mfma_f32_16x16x32_bf16 v[156:159], v[100:103], v[132:135], v[156:159]\n" \
  "v_fmac_f32 v176, v162, v34\n" \
  "v_fmac_f32 v176, v163, v35\n" \
  "v_smfmac_f32_16x16x64_bf16 v[172:175], v[144:147], v[128:135], v193\n" \
  "ds_read_b128 v[128:131], v184 offset:0\n" \
  "v_fmac_f32 v176, v152, v36\n" \
  "v_fmac_f32 v176, v153, v37\n" \
  "v_smfmac_f32_16x16x64_bf16 v[172:175], v[148:151], v[136:143], v193\n" \
  "ds_read_b128 v[132:135], v184 offset:64\n" \
  "v_fmac_f32 v176, v154, v38\n" \
  "v_fmac_f32 v176, v155, v39\n" \
  "v_mfma_f32_16x16x32_bf16 v[160:163], v[96:99], v[112:115], 0\n" \
  "v_fmac_f32 v176, v156, v40\n" \
  "v_fmac_f32 v176, v157, v41\n" \
  "v_mfma_f32_16x16x32_bf16 v[160:163], v[100:103], v[116:119], v[160:163]\n" \
  "v_fmac_f32 v176, v158, v42\n" \
  "v_fmac_f32 v176, v159, v43\n" \
  "v_mfma_f32_16x16x32_bf16 v[152:155], v[104:107], v[112:115], 0\n" \
  "v_fmac_f32 v176, v172, v44\n" \
  "v_fmac_f32 v176, v173, v45\n" \
  "v_mad_u32_u16 v185, v181, 16, v188\n" \
  "v_mfma_f32_16x16x32_bf16 v[152:155], v[108:111], v[116:119], v[152:155]\n" \
  "v_fmac_f32 v176, v174, v46\n" \
  "v_fmac_f32 v176, v175, v47\n" \
  "v_mov_b64 v[172:173], 0\n" \
  "v_mfma_f32_16x16x32_bf16 v[156:159], v[104:107], v[136:139], 0\n" \
  "v_fmac_f32 v176, v160, v48\n" \
  "v_fmac_f32 v176, v161, v49\n" \
  "v_mad_u32_u16 v186, v182, 16, v188 op_sel:[1,0,0,0]\n" \
  "v_mfma_f32_16x16x32_bf16 v[156:159], v[108:111], v[140:143], v[156:159]\n" \
  "v_fmac_f32 v176, v162, v50\n" \
  "v_fmac_f32 v176, v163, v51\n" \
  "v_smfmac_f32_16x16x64_bf16 v[168:171], v[144:147], v[112:119], v193\n" \
  "ds_read_b128 v[112:115], v185 offset:0\n" \
  "v_fmac_f32 v176, v152, v52\n" \
  "v_fmac_f32 v176, v153, v53\n" \
  "v_smfmac_f32_16x16x64_bf16 v[168:171], v[148:151], v[120:127], v193\n" \
  "ds_read_b128 v[116:119], v185 offset:64\n" \
  "v_fmac_f32 v176, v154, v54\n" \
  "v_fmac_f32 v176, v155, v55\n" \
  "v_mfma_f32_16x16x32_bf16 v[160:163], v[96:99], v[136:139], 0\n" \
  "v_fmac_f32 v176, v156, v56\n" \
  "v_fmac_f32 v176, v157, v57\n" \
  "v_mfma_f32_16x16x32_bf16 v[160:163], v[100:103], v[140:143], v[160:163]\n" \
  "v_fmac_f32 v176, v158, v58\n" \
  "v_fmac_f32 v176, v159, v59\n" \
  "v_smfmac_f32_16x16x64_bf16 v[164:167], v[148:151], v[104:111], v193\n" \
  "ds_read_b128 v[136:139], v186 offset:0\n" \
  "v_fmac_f32 v176, v168, v60\n" \
  "v_fmac_f32 v176, v169, v61\n" \
  "v_smfmac_f32_16x16x64_bf16 v[164:167], v[144:147], v[96:103], v193\n" \
  "ds_read_b128 v[140:143], v186 offset:64\n" \
  "v_fmac_f32 v176, v170, v62\n" \
  "v_fmac_f32 v176, v171, v63\n" \
  "v_mfma_f32_16x16x32_bf16 v[152:155], v[120:123], v[120:123], 0\n" \
  "v_fmac_f32 v176, v160, v64\n" \
  "v_fmac_f32 v176, v161, v65\n" \
  "v_mfma_f32_16x16x32_bf16 v[152:155], v[124:127], v[124:127], v[152:155]\n" \
  "v_fmac_f32 v176, v162, v66\n" \
  "v_fmac_f32 v176, v163, v67\n" \
  "v_mad_u32_u16 v187, v181, 16, v188 op_sel:[1,0,0,0]\n" \
  "v_mfma_f32_16x16x32_bf16 v[156:159], v[104:107], v[120:123], 0\n" \
  "v_fmac_f32 v176, v164, v68\n" \
  "v_fmac_f32 v176, v165, v69\n" \
  "v_mov_b64 v[168:169], 0\n" \
  "v_mfma_f32_16x16x32_bf16 v[156:159], v[108:111], v[124:127], v[156:159]\n" \
  "v_fmac_f32 v176, v166, v70\n" \
  "v_fmac_f32 v176, v167, v71\n" \
  "v_mfma_f32_16x16x32_bf16 v[160:163], v[96:99], v[120:123], 0\n" \
  "ds_read_b128 v[120:123], v187 offset:0\n" \
  "v_fmac_f32 v176, v152, v72\n" \
  "v_fmac_f32 v176, v153, v73\n" \
  "v_mfma_f32_16x16x32_bf16 v[160:163], v[100:103], v[124:127], v[160:163]\n" \
  "v_fmac_f32 v176, v154, v74\n" \
  "v_fmac_f32 v176, v155, v75\n" \
  "v_mov_b64 v[164:165], 0\n" \
  "v_mfma_f32_16x16x32_bf16 v[152:155], v[96:99], v[104:107], 0\n" \
  "ds_read_b128 v[124:127], v187 offset:64\n" \
  "v_fmac_f32 v176, v156, v76\n" \
  "v_fmac_f32 v176, v157, v77\n" \
  "v_mad_u32_u16 v184, v180, 16, v188 op_sel:[1,0,0,0]\n" \
  "v_mfma_f32_16x16x32_bf16 v[152:155], v[100:103], v[108:111], v[152:155]\n" \
  "v_fmac_f32 v176, v158, v78\n" \
  "v_fmac_f32 v176, v159, v79\n" \
  "v_mfma_f32_16x16x32_bf16 v[156:159], v[104:107], v[104:107], 0\n" \
  "ds_read_b128 v[104:107], v184 offset:0\n" \
  "v_fmac_f32 v176, v160, v80\n" \
  "v_fmac_f32 v176, v161, v81\n" \
  "v_mfma_f32_16x16x32_bf16 v[156:159], v[108:111], v[108:111], v[156:159]\n" \
  "s_sub_u32 s51, s51, 1\n" \
  "v_fmac_f32 v176, v162, v82\n" \
  "v_fmac_f32 v176, v163, v83\n" \
  "v_mfma_f32_16x16x32_bf16 v[160:163], v[148:151], v[144:147], 0\n" \
  "s_cmp_lg_u32 s51, 0\n" \
  "ds_read_b128 v[108:111], v184 offset:64\n" \
  "v_fmac_f32 v176, v152, v84\n" \
  "v_fmac_f32 v176, v153, v85\n" \
  "v_mad_u32_u16 v185, v180, 16, v188\n" \
  "v_mfma_f32_16x16x32_bf16 v[160:163], v[96:99], v[96:99], v[160:163]\n" \
  "s_waitcnt lgkmcnt(7)\n" \
  "ds_read_b128 v[96:99], v185 offset:0\n" \
  "v_fmac_f32 v176, v154, v86\n" \
  "v_fmac_f32 v176, v155, v87\n" \
  "v_mfma_f32_16x16x32_bf16 v[160:163], v[100:103], v[100:103], v[160:163]\n" \
  "s_cbranch_scc1 LA_loop\n" \
  "s_setpc_b64 s[52:53]\n" \
  "LB_start:\n" \
  "s_setprio 3\n" \
  "s_mov_b32 s37, 12\n" \
  "L2_:\n" \
  "s_sub_u32 s35, s36, 125\n" \
  "s_min_u32 s35, s35, s36\n" \
  "s_mul_i32 m0, s35, 1040\n" \
  "s_lshl_b32 s35, s35, 11\n" \
  "s_add_u32 s42, s40, s35\n" \
  "s_addc_u32 s43, s41, 0\n" \
  "global_load_lds_dwordx4 v254, s[42:43]\n" \
  "s_add_u32 s36, s36, 4\n" \
  "s_sub_u32 s37, s37, 1\n" \
  "s_cmp_lg_u32 s37, 0\n" \
  "s_cbranch_scc1 L2_\n" \
  "v_mov_b32 v190, 163328\n" \
  "v_mov_b64 v[2:3], 0\n" \
  "ds_write_b64 v190, v[2:3]\n" \
  "ds_write_b64 v190, v[2:3] offset:8\n" \
  "ds_write_b64 v190, v[2:3] offset:16\n" \
  "s_waitcnt lgkmcnt(0)\n" \
  "s_barrier\n" \
  "s_mov_b32 s37, 20\n" \
  "L3_:\n" \
  "s_sub_u32 s35, s36, 125\n" \
  "s_min_u32 s35, s35, s36\n" \
  "s_mul_i32 m0, s35, 1040\n" \
  "s_lshl_b32 s35, s35, 11\n" \
  "s_add_u32 s42, s40, s35\n" \
  "s_addc_u32 s43, s41, 0\n" \
  "global_load_lds_dwordx4 v254, s[42:43]\n" \
  "s_add_u32 s36, s36, 4\n" \
  "s_sub_u32 s37, s37, 1\n" \
  "s_cmp_lg_u32 s37, 0\n" \
  "s_cbranch_scc1 L3_\n" \
  "s_add_u32 s48, s24, 110002176\n" \
  "s_addc_u32 s49, s25, 0\n" \
  "s_lshl_b32 s35, s26, 2\n" \
  "v_mov_b32 v193, s35\n" \
  "global_load_dword v193, v193, s[48:49] offset:1024 sc1\n" \
  "v_mov_b32 v191, 1\n" \
  "v_and_b32 v252, 15, v255\n" \
  "v_lshrrev_b32 v253, 4, v255\n" \
  "s_lshl2_add_u32 s35, s31, s28\n" \
  "s_lshl_b32 s35, s35, 1\n" \
  "v_bfe_u32 v1, v255, 2, 1\n" \
  "v_add_u32 v1, s35, v1\n" \
  "v_and_b32 v2, 3, v255\n" \
  "v_lshlrev_b32 v3, 5, v1\n" \
  "v_lshl_add_u32 v3, v2, 3, v3\n" \
  "v_lshlrev_b32 v4, 2, v2\n" \
  "v_add_u32 v4, 380, v4\n" \
  "v_cmp_gt_u32 vcc, 12, v1\n" \
  "v_lshrrev_b32 v5, 3, v255\n" \
  "v_mul_u32_u24 v5, 6400, v5\n" \
  "v_cndmask_b32 v3, v4, v3, vcc\n" \
  "v_add_u32 v5, 3200, v5\n" \
  "v_add_u32 v227, v5, v3\n" \
  "v_lshl_add_u32 v3, v1, 3, v2\n" \
  "v_lshl_add_u32 v4, v1, 3, 4\n" \
  "v_min_u32 v4, 96, v4\n" \
  "v_add_u32 v4, v4, v2\n" \
  "v_mul_u32_u24 v5, 205, v3\n" \
  "v_lshrrev_b32 v5, 11, v5\n" \
  "v_mul_u32_u24 v5, 10, v5\n" \
  "v_sub_u32 v5, v3, v5\n" \
  "v_lshlrev_b32 v236, 26, v5\n" \
  "v_mul_u32_u24 v5, 205, v4\n" \
  "v_lshrrev_b32 v5, 11, v5\n" \
  "v_mul_u32_u24 v5, 10, v5\n" \
  "v_sub_u32 v5, v4, v5\n" \
  "v_lshlrev_b32 v237, 26, v5\n" \
  "s_lshl2_add_u32 s4, s31, s28\n" \
  "s_lshl_b32 s4, s4, 7\n" \
  "s_add_u32 s36, s4, 96\n" \
  "s_min_u32 s36, s36, 984\n" \
  "s_add_u32 s37, s4, 112\n" \
  "s_min_u32 s37, s37, 984\n" \
  "v_add_u32 v186, s4, v252\n" \
  "v_add_u32 v187, s36, v252\n" \
  "v_add_u32 v188, s37, v252\n" \
  "s_mul_i32 s38, s7, 51200\n" \
  "s_add_u32 s42, s8, s38\n" \
  "s_addc_u32 s43, s9, 0\n" \
  "s_xor_b32 s38, s31, 1\n" \
  "s_lshl_b32 s38, s38, 7\n" \
  "s_add_u32 s94, s10, s38\n" \
  "s_addc_u32 s95, s11, 0\n" \
  "s_mul_i32 s5, s28, 4224\n" \
  "s_add_u32 s5, s5, 146432\n" \
  "v_lshrrev_b32 v2, 3, v186\n" \
  "v_lshl_add_u32 v2, v186, 3, v2\n" \
  "v_add_lshl_u32 v249, v2, v253, 4\n" \
  "v_lshrrev_b32 v2, 3, v187\n" \
  "v_lshl_add_u32 v2, v187, 3, v2\n" \
  "v_add_lshl_u32 v250, v2, v253, 4\n" \
  "v_lshrrev_b32 v2, 3, v188\n" \
  "v_lshl_add_u32 v2, v188, 3, v2\n" \
  "v_add_lshl_u32 v251, v2, v253, 4\n" \
  "v_mul_u32_u24 v1, 1600000, v253\n" \
  "v_lshl_add_u32 v186, v186, 4, v1\n" \
  "v_lshl_add_u32 v187, v187, 4, v1\n" \
  "v_lshl_add_u32 v188, v188, 4, v1\n" \
  "v_lshlrev_b32 v1, 4, v253\n" \
  "v_xor_b32 v244, v1, v254\n" \
  "v_lshl_add_u32 v245, v255, 4, s5\n" \
  "v_bfe_u32 v1, v252, 1, 2\n" \
  "v_xor_b32 v1, v1, v253\n" \
  "v_lshlrev_b32 v1, 4, v1\n" \
  "v_lshl_add_u32 v1, v252, 7, v1\n" \
  "v_add_u32 v246, s5, v1\n" \
  "v_lshrrev_b32 v1, 2, v255\n" \
  "s_lshl_b32 s35, s28, 5\n" \
  "s_lshl_b32 s36, s31, 6\n" \
  "s_add_u32 s35, s35, s36\n" \
  "s_and_b32 s35, s35, 127\n" \
  "v_add_u32 v1, s35, v1\n" \
  "v_lshrrev_b32 v2, 4, v255\n" \
  "v_and_b32 v2, 2, v2\n" \
  "v_xor_b32 v2, v2, v255\n" \
  "v_and_b32 v2, 3, v2\n" \
  "v_lshlrev_b32 v2, 4, v2\n" \
  "v_lshl_add_u32 v189, v1, 10, v2\n" \
  "v_add_u32 v194, 15360, v189\n" \
  "v_lshrrev_b32 v1, 2, v252\n" \
  "v_lshlrev_b32 v1, 6, v1\n" \
  "v_lshl_add_u32 v1, v253, 9, v1\n" \
  "v_and_b32 v2, 3, v252\n" \
  "v_lshl_add_u32 v1, v2, 3, v1\n" \
  "v_and_b32 v2, 1, v253\n" \
  "v_lshl_add_u32 v1, v2, 5, v1\n" \
  "v_add_u32 v184, 138240, v1\n" \
  "v_xor_b32 v1, 32, v1\n" \
  "v_add_u32 v185, 138240, v1\n" \
  "s_bfe_u32 s35, s30, 0x10001\n" \
  "s_lshl1_add_u32 s35, s29, s35\n" \
  "s_lshr_b32 s6, s30, 2\n" \
  "s_lshl_b32 s6, s6, 17\n" \
  "s_lshl_b32 s38, s35, 6\n" \
  "s_add_u32 s92, s14, s38\n" \
  "s_addc_u32 s93, s15, 0\n" \
  "s_lshr_b32 s37, s30, 3\n" \
  "s_add_u32 s37, s37, 96\n" \
  "s_lshl_b32 s34, s37, 17\n" \
  "s_mul_i32 s35, s35, 400\n" \
  "s_add_u32 s37, s37, s35\n" \
  "s_mul_i32 s37, s37, 16000\n" \
  "s_add_u32 s46, s24, s37\n" \
  "s_addc_u32 s47, s25, 0\n" \
  "s_and_b32 s36, s30, 4\n" \
  "s_lshr_b32 s37, s30, 3\n" \
  "s_add_u32 s36, s36, s37\n" \
  "s_add_u32 s36, s36, s35\n" \
  "s_mul_i32 s36, s36, 16000\n" \
  "s_add_u32 s44, s24, s36\n" \
  "s_addc_u32 s45, s25, 0\n" \
  "s_lshl_b32 s96, s28, 11\n" \
  "s_add_u32 s96, s96, 138240\n" \
  "s_add_u32 s35, s4, 0\n" \
  "s_min_u32 s35, s35, 984\n" \
  "s_lshl_b32 s35, s35, 8\n" \
  "s_add_u32 s58, s94, s35\n" \
  "s_addc_u32 s59, s95, 0\n" \
  "global_load_dwordx4 v[8:11], v244, s[58:59] offset:0\n" \
  "global_load_dwordx4 v[12:15], v244, s[58:59] offset:2048\n" \
  "s_add_u32 s35, s4, 16\n" \
  "s_min_u32 s35, s35, 984\n" \
  "s_lshl_b32 s35, s35, 8\n" \
  "s_add_u32 s58, s94, s35\n" \
  "s_addc_u32 s59, s95, 0\n" \
  "global_load_dwordx4 v[24:27], v244, s[58:59] offset:0\n" \
  "global_load_dwordx4 v[28:31], v244, s[58:59] offset:2048\n" \
  "s_add_u32 s35, s4, 32\n" \
  "s_min_u32 s35, s35, 984\n" \
  "s_lshl_b32 s35, s35, 8\n" \
  "s_add_u32 s58, s94, s35\n" \
  "s_addc_u32 s59, s95, 0\n" \
  "global_load_dwordx4 v[40:43], v244, s[58:59] offset:0\n" \
  "global_load_dwordx4 v[44:47], v244, s[58:59] offset:2048\n" \
  "s_add_u32 s35, s4, 48\n" \
  "s_min_u32 s35, s35, 984\n" \
  "s_lshl_b32 s35, s35, 8\n" \
  "s_add_u32 s58, s94, s35\n" \
  "s_addc_u32 s59, s95, 0\n" \
  "global_load_dwordx4 v[56:59], v244, s[58:59] offset:0\n" \
  "global_load_dwordx4 v[60:63], v244, s[58:59] offset:2048\n" \
  "s_waitcnt vmcnt(8)\n" \
  "s_mov_b64 exec, 1\n" \
  "ds_add_u32 v190, v191 offset:12\n" \
  "s_mov_b64 exec, -1\n" \
  "s_setprio 2\n" \
  "s_add_u32 s35, s4, 64\n" \
  "s_min_u32 s35, s35, 984\n" \
  "s_lshl_b32 s35, s35, 8\n" \
  "s_add_u32 s58, s94, s35\n" \
  "s_addc_u32 s59, s95, 0\n" \
  "global_load_dwordx4 v[72:75], v244, s[58:59] offset:0\n" \
  "global_load_dwordx4 v[76:79], v244, s[58:59] offset:2048\n" \
  "s_add_u32 s35, s4, 80\n" \
  "s_min_u32 s35, s35, 984\n" \
  "s_lshl_b32 s35, s35, 8\n" \
  "s_add_u32 s58, s94, s35\n" \
  "s_addc_u32 s59, s95, 0\n" \
  "global_load_dwordx4 v[88:91], v244, s[58:59] offset:0\n" \
  "global_load_dwordx4 v[92:95], v244, s[58:59] offset:2048\n" \
  "s_add_u32 s35, s4, 96\n" \
  "s_min_u32 s35, s35, 984\n" \
  "s_lshl_b32 s35, s35, 8\n" \
  "s_add_u32 s58, s94, s35\n" \
  "s_addc_u32 s59, s95, 0\n" \
  "global_load_dwordx4 v[104:107], v244, s[58:59] offset:0\n" \
  "global_load_dwordx4 v[108:111], v244, s[58:59] offset:2048\n" \
  "s_add_u32 s35, s4, 112\n" \
  "s_min_u32 s35, s35, 984\n" \
  "s_lshl_b32 s35, s35, 8\n" \
  "s_add_u32 s58, s94, s35\n" \
  "s_addc_u32 s59, s95, 0\n" \
  "global_load_dwordx4 v[120:123], v244, s[58:59] offset:0\n" \
  "global_load_dwordx4 v[124:127], v244, s[58:59] offset:2048\n" \
  "v_lshlrev_b32 v247, 7, v255\n" \
  "s_lshl2_add_u32 s35, s26, s28\n" \
  "s_mul_i32 s35, s35, 5888\n" \
  "s_add_u32 s35, s35, 1048576\n" \
  "s_add_u32 s58, s14, s35\n" \
  "s_addc_u32 s59, s15, 0\n" \
  "s_mov_b32 exec_lo, 0xffffffff\n" \
  "s_mov_b32 exec_hi, 0x3fff\n" \
  "global_load_dword v248, v247, s[58:59]\n" \
  "s_mov_b64 exec, -1\n" \
  "s_lshl2_add_u32 s35, s26, s28\n" \
  "s_mul_i32 s35, s35, 5888\n" \
  "s_add_u32 s35, s35, 7077888\n" \
  "s_add_u32 s58, s14, s35\n" \
  "s_addc_u32 s59, s15, 0\n" \
  "s_mov_b32 exec_lo, 0xffffffff\n" \
  "s_mov_b32 exec_hi, 0x3fff\n" \
  "global_load_dword v248, v247, s[58:59]\n" \
  "s_mov_b64 exec, -1\n" \
  "L4_:\n" \
  "ds_read_b32 v192, v190 offset:16\n" \
  "s_waitcnt lgkmcnt(0)\n" \
  "v_readfirstlane_b32 s35, v192\n" \
  "s_cmp_lt_u32 s35, 4\n" \
  "s_cbranch_scc1 L4_\n" \
  "s_add_u32 s40, s92, s6\n" \
  "s_addc_u32 s41, s93, 0\n" \
  "s_add_u32 s6, s6, 1048576\n" \
  "s_mov_b32 m0, s96\n" \
  "s_nop 0\n" \
  "global_load_lds_dwordx4 v189, s[40:41]\n" \
  "global_load_lds_dwordx4 v194, s[40:41] offset:1024\n" \
  "L5_:\n" \
  "ds_read_b32 v192, v190 offset:12\n" \
  "s_waitcnt lgkmcnt(0)\n" \
  "v_readfirstlane_b32 s35, v192\n" \
  "s_cmp_lt_u32 s35, 4\n" \
  "s_cbranch_scc1 L5_\n" \
  "ds_read_b128 v[0:3], v249 offset:0\n" \
  "ds_read_b128 v[4:7], v249 offset:64\n" \
  "global_load_dwordx2 v[196:197], v227, s[42:43] offset:-3200\n" \
  "global_load_dwordx2 v[198:199], v227, s[42:43] offset:-2800\n" \
  "s_waitcnt vmcnt(20)\n" \
  "ds_write_b128 v245, v[8:11] offset:0\n" \
  "ds_write_b128 v245, v[12:15] offset:1024\n" \
  "ds_read_b128 v[8:11], v246 offset:0\n" \
  "ds_read_b128 v[12:15], v246 offset:64\n" \
  "ds_read_b128 v[16:19], v249 offset:2080\n" \
  "ds_read_b128 v[20:23], v249 offset:2144\n" \
  "global_load_dwordx2 v[200:201], v227, s[42:43] offset:-2400\n" \
  "global_load_dwordx2 v[202:203], v227, s[42:43] offset:-2000\n" \
  "s_waitcnt vmcnt(20)\n" \
  "ds_write_b128 v245, v[24:27] offset:2048\n" \
  "ds_write_b128 v245, v[28:31] offset:3072\n" \
  "ds_read_b128 v[24:27], v246 offset:2048\n" \
  "ds_read_b128 v[28:31], v246 offset:2112\n" \
  "ds_read_b128 v[32:35], v249 offset:4160\n" \
  "ds_read_b128 v[36:39], v249 offset:4224\n" \
  "global_load_dwordx2 v[204:205], v227, s[42:43] offset:-1600\n" \
  "global_load_dwordx2 v[206:207], v227, s[42:43] offset:-1200\n" \
  "s_waitcnt vmcnt(20)\n" \
  "ds_write_b128 v245, v[40:43] offset:0\n" \
  "ds_write_b128 v245, v[44:47] offset:1024\n" \
  "ds_read_b128 v[40:43], v246 offset:0\n" \
  "ds_read_b128 v[44:47], v246 offset:64\n" \
  "ds_read_b128 v[48:51], v249 offset:6240\n" \
  "ds_read_b128 v[52:55], v249 offset:6304\n" \
  "global_load_dwordx2 v[208:209], v227, s[42:43] offset:-800\n" \
  "global_load_dwordx2 v[210:211], v227, s[42:43] offset:-400\n" \
  "s_waitcnt vmcnt(20)\n" \
  "ds_write_b128 v245, v[56:59] offset:2048\n" \
  "ds_write_b128 v245, v[60:63] offset:3072\n" \
  "ds_read_b128 v[56:59], v246 offset:2048\n" \
  "ds_read_b128 v[60:63], v246 offset:2112\n" \
  "ds_read_b128 v[64:67], v249 offset:8320\n" \
  "ds_read_b128 v[68:71], v249 offset:8384\n" \
  "global_load_dwordx2 v[212:213], v227, s[42:43] offset:0\n" \
  "global_load_dwordx2 v[214:215], v227, s[42:43] offset:400\n" \
  "s_waitcnt vmcnt(20)\n" \
  "ds_write_b128 v245, v[72:75] offset:0\n" \
  "ds_write_b128 v245, v[76:79] offset:1024\n" \
  "ds_read_b128 v[72:75], v246 offset:0\n" \
  "ds_read_b128 v[76:79], v246 offset:64\n" \
  "ds_read_b128 v[80:83], v249 offset:10400\n" \
  "ds_read_b128 v[84:87], v249 offset:10464\n" \
  "global_load_dwordx2 v[216:217], v227, s[42:43] offset:800\n" \
  "global_load_dwordx2 v[218:219], v227, s[42:43] offset:1200\n" \
  "s_waitcnt vmcnt(20)\n" \
  "ds_write_b128 v245, v[88:91] offset:2048\n" \
  "ds_write_b128 v245, v[92:95] offset:3072\n" \
  "ds_read_b128 v[88:91], v246 offset:2048\n" \
  "ds_read_b128 v[92:95], v246 offset:2112\n" \
  "ds_read_b128 v[96:99], v250 offset:0\n" \
  "ds_read_b128 v[100:103], v250 offset:64\n" \
  "global_load_dwordx2 v[220:221], v227, s[42:43] offset:1600\n" \
  "global_load_dwordx2 v[222:223], v227, s[42:43] offset:2000\n" \
  "s_waitcnt vmcnt(20)\n" \
  "ds_write_b128 v245, v[104:107] offset:0\n" \
  "ds_write_b128 v245, v[108:111] offset:1024\n" \
  "ds_read_b128 v[104:107], v246 offset:0\n" \
  "ds_read_b128 v[108:111], v246 offset:64\n" \
  "ds_read_b128 v[112:115], v251 offset:0\n" \
  "ds_read_b128 v[116:119], v251 offset:64\n" \
  "global_load_dwordx2 v[224:225], v227, s[42:43] offset:2400\n" \
  "global_load_dwordx2 v[226:227], v227, s[42:43] offset:2800\n" \
  "s_waitcnt vmcnt(20)\n" \
  "ds_write_b128 v245, v[120:123] offset:2048\n" \
  "ds_write_b128 v245, v[124:127] offset:3072\n" \
  "ds_read_b128 v[120:123], v246 offset:2048\n" \
  "ds_read_b128 v[124:127], v246 offset:2112\n" \
  "s_setprio 2\n" \
  "v_readfirstlane_b32 s39, v193\n" \
  "s_lshl_b32 s35, s26, 2\n" \
  "s_add_u32 s39, s39, 1\n" \
  "v_mov_b32 v193, s35\n" \
  "v_lshlrev_b32 v238, 4, v255\n" \
  "s_mov_b32 s51, 0x3e7\n" \
  "s_mov_b32 s52, 0x10002\n" \
  "s_mov_b32 s53, 0xc0c0400\n" \
  "s_mov_b32 s54, 0x4000c0c\n" \
  "s_mov_b32 s55, 0x3030303\n" \
  "s_mov_b32 s56, 0x55555555\n" \
  "s_mov_b32 s57, 0x7060302\n" \
  "s_mov_b32 s50, 0x0\n" \
  "s_mov_b32 s98, 0x0\n" \
  "s_mov_b32 s99, 0x8\n" \
  "s_mov_b32 s97, 0x2000\n" \
  "v_mov_b64 v[234:235], 0\n" \
  "s_lshl2_add_u32 s33, s26, s28\n" \
  "v_mov_b64 v[240:241], -1\n" \
  "v_mov_b64 v[242:243], -1\n" \
  "s_lshr_b32 s35, s30, 2\n" \
  "s_lshl3_add_u32 s35, s29, s35\n" \
  "s_and_b32 s36, s30, 3\n" \
  "s_lshl2_add_u32 s35, s35, s36\n" \
  "s_lshl2_add_u32 s35, s35, s28\n" \
  "s_lshl_b32 s35, s35, 12\n" \
  "s_add_u32 s35, s35, 105676800\n" \
  "s_add_u32 s58, s24, s35\n" \
  "s_addc_u32 s59, s25, 0\n" \
  "s_waitcnt vmcnt(16) lgkmcnt(0)\n" \
  "s_mov_b64 exec, 1\n" \
  "ds_add_u32 v190, v191 offset:0\n" \
  "s_mov_b64 exec, -1\n" \
  "L6_:\n" \
  "ds_read_b32 v192, v190 offset:0\n" \
  "s_waitcnt lgkmcnt(0)\n" \
  "v_readfirstlane_b32 s35, v192\n" \
  "s_cmp_lt_u32 s35, 4\n" \
  "s_cbranch_scc1 L6_\n" \
  "s_add_u32 s96, s96, 8192\n" \
  "s_add_u32 s40, s92, s6\n" \
  "s_addc_u32 s41, s93, 0\n" \
  "s_add_u32 s6, s6, 1048576\n" \
  "s_mov_b32 m0, s96\n" \
  "s_nop 0\n" \
  "global_load_lds_dwordx4 v189, s[40:41]\n" \
  "global_load_lds_dwordx4 v194, s[40:41] offset:1024\n" \
  "s_sub_u32 s96, s96, 8192\n" \
  "ds_read_b64_tr_b16 v[128:129], v184 offset:0\n" \
  "ds_read_b64_tr_b16 v[130:131], v184 offset:256\n" \
  "ds_read_b64_tr_b16 v[144:145], v185 offset:0\n" \
  "ds_read_b64_tr_b16 v[146:147], v185 offset:256\n" \
  "ds_read_b64_tr_b16 v[132:133], v184 offset:2048\n" \
  "ds_read_b64_tr_b16 v[134:135], v184 offset:2304\n" \
  "ds_read_b64_tr_b16 v[148:149], v185 offset:2048\n" \
  "ds_read_b64_tr_b16 v[150:151], v185 offset:2304\n" \
  "ds_read_b64_tr_b16 v[136:137], v184 offset:4096\n" \
  "ds_read_b64_tr_b16 v[138:139], v184 offset:4352\n" \
  "ds_read_b64_tr_b16 v[152:153], v185 offset:4096\n" \
  "ds_read_b64_tr_b16 v[154:155], v185 offset:4352\n" \
  "ds_read_b64_tr_b16 v[140:141], v184 offset:6144\n" \
  "ds_read_b64_tr_b16 v[142:143], v184 offset:6400\n" \
  "ds_read_b64_tr_b16 v[156:157], v185 offset:6144\n" \
  "ds_read_b64_tr_b16 v[158:159], v185 offset:6400\n" \
  "global_store_dwordx4 v238, v[240:243], s[58:59] offset:0 sc1\n" \
  "global_store_dwordx4 v238, v[240:243], s[58:59] offset:1024 sc1\n" \
  "global_store_dwordx4 v238, v[240:243], s[58:59] offset:2048 sc1\n" \
  "global_store_dwordx4 v238, v[240:243], s[58:59] offset:3072 sc1\n" \
  "s_lshl2_add_u32 s35, s7, s28\n" \
  "s_lshl_b32 s35, s35, 7\n" \
  "s_lshl_b32 s36, s31, 16\n" \
  "s_add_u32 s35, s35, s36\n" \
  "s_add_u32 s35, s35, 109871104\n" \
  "s_add_u32 s58, s24, s35\n" \
  "s_addc_u32 s59, s25, 0\n" \
  "s_mov_b64 exec, 0xff\n" \
  "global_store_dwordx4 v238, v[240:243], s[58:59] sc1\n" \
  "s_mov_b64 exec, -1\n" \
  "s_waitcnt vmcnt(7)\n" \
  "v_mov_b32 v192, s39\n" \
  "s_cmp_lg_u32 s28, 0\n" \
  "s_cbranch_scc1 LB_nc\n" \
  "s_mov_b64 exec, 1\n" \
  "global_store_dword v193, v192, s[48:49] offset:0 sc1\n" \
  "s_mov_b64 exec, -1\n" \
  "LB_nc:\n" \
  "s_cmp_lg_u32 s33, 1021\n" \
  "s_cbranch_scc1 LB_nm\n" \
  "v_lshlrev_b32 v242, 8, v255\n" \
  "v_mov_b64 v[240:241], 0\n" \
  "global_store_dwordx2 v242, v[240:241], s[48:49] offset:2048 sc1\n" \
  "v_add_u32 v242, 0x4000, v242\n" \
  "global_store_dwordx2 v242, v[240:241], s[48:49] offset:2048 sc1\n" \
  "v_add_u32 v242, 0x4000, v242\n" \
  "global_store_dwordx2 v242, v[240:241], s[48:49] offset:2048 sc1\n" \
  "v_add_u32 v242, 0x4000, v242\n" \
  "global_store_dwordx2 v242, v[240:241], s[48:49] offset:2048 sc1\n" \
  "v_add_u32 v242, 0x4000, v242\n" \
  "LB_nm:\n" \
  "LT1:\n" \
  "s_waitcnt lgkmcnt(14)\n" \
  "v_mfma_f32_16x16x32_bf16 v[160:163], v[128:131], v[0:3], 0\n" \
  "s_set_gpr_idx_on s50, gpr_idx(SRC0)\n" \
  "v_min_u32_e64 v232, v196, s51\n" \
  "v_min_u32_e64 v233, v197, s51\n" \
  "s_set_gpr_idx_off\n" \
  "s_waitcnt lgkmcnt(12)\n" \
  "v_mfma_f32_16x16x32_bf16 v[164:167], v[144:147], v[0:3], 0\n" \
  "v_mad_u32_u24 v232, v232, s52, v236\n" \
  "v_lshrrev_b32 v228, v232, v234\n" \
  "s_nop 0\n" \
  "s_waitcnt lgkmcnt(10)\n" \
  "v_mfma_f32_16x16x32_bf16 v[160:163], v[132:135], v[4:7], v[160:163]\n" \
  "v_lshrrev_b32_dpp v229, v232, v234 quad_perm:[2,3,0,1] row_mask:0xf bank_mask:0xf\n" \
  "v_lshrrev_b32_dpp v230, v232, v234 quad_perm:[1,2,3,0] row_mask:0xf bank_mask:0xf\n" \
  "s_nop 0\n" \
  "s_waitcnt lgkmcnt(8)\n" \
  "v_mfma_f32_16x16x32_bf16 v[164:167], v[148:151], v[4:7], v[164:167]\n" \
  "v_lshrrev_b32_dpp v231, v232, v234 quad_perm:[3,0,1,2] row_mask:0xf bank_mask:0xf\n" \
  "v_perm_b32 v228, v229, v228, s53\n" \
  "s_nop 0\n" \
  "s_waitcnt lgkmcnt(6)\n" \
  "v_mfma_f32_16x16x32_bf16 v[160:163], v[136:139], v[8:11], v[160:163]\n" \
  "v_perm_b32 v230, v231, v230, s54\n" \
  "v_bitop3_b32 v228, v228, v230, s55 bitop3:0xa8\n" \
  "s_nop 0\n" \
  "s_waitcnt lgkmcnt(4)\n" \
  "v_mfma_f32_16x16x32_bf16 v[164:167], v[152:155], v[8:11], v[164:167]\n" \
  "v_lshl_or_b32 v229, v234, 1, s56\n" \
  "v_add_u32_dpp v228, v228, v228 quad_perm:[2,3,0,1] row_mask:0xf bank_mask:0xf\n" \
  "s_nop 0\n" \
  "s_waitcnt lgkmcnt(2)\n" \
  "v_mfma_f32_16x16x32_bf16 v[160:163], v[140:143], v[12:15], v[160:163]\n" \
  "v_min_u32_sdwa v230, v228, v228 dst_sel:DWORD dst_unused:UNUSED_PAD src0_sel:BYTE_0 src1_sel:BYTE_1\n" \
  "s_nop 1\n" \
  "s_waitcnt lgkmcnt(0)\n" \
  "v_mfma_f32_16x16x32_bf16 v[164:167], v[156:159], v[12:15], v[164:167]\n" \
  "v_min_u32_sdwa v231, v228, v228 dst_sel:DWORD dst_unused:UNUSED_PAD src0_sel:BYTE_2 src1_sel:BYTE_3\n" \
  "v_sub_u32 v230, v230, v231\n" \
  "s_nop 1\n" \
  "v_mfma_f32_16x16x32_bf16 v[168:171], v[128:131], v[16:19], 0\n" \
  "v_mad_u32_u24 v233, v233, s52, v237\n" \
  "v_add_u32_dpp v230, v230, v230 quad_perm:[1,0,3,2] row_mask:0xf bank_mask:0xf\n" \
  "s_nop 1\n" \
  "v_mfma_f32_16x16x32_bf16 v[172:175], v[144:147], v[16:19], 0\n" \
  "v_cmp_ge_i32 vcc, 0, v230\n" \
  "s_nop 1\n" \
  "v_cndmask_b32_sdwa v228, v228, v228, vcc dst_sel:DWORD dst_unused:UNUSED_PAD src0_sel:WORD_1 src1_sel:WORD_0\n" \
  "s_mov_b64 s[58:59], vcc\n" \
  "s_nop 0\n" \
  "v_mfma_f32_16x16x32_bf16 v[168:171], v[132:135], v[20:23], v[168:171]\n" \
  "v_cmp_le_u32_sdwa vcc, v228, v228 src0_sel:BYTE_0 src1_sel:BYTE_1\n" \
  "s_nop 1\n" \
  "v_cndmask_b32_dpp v232, v232, v232, vcc quad_perm:[2,3,0,1] row_mask:0xf bank_mask:0xf\n" \
  "s_mov_b64 vcc, s[58:59]\n" \
  "s_nop 0\n" \
  "v_mfma_f32_16x16x32_bf16 v[172:175], v[148:151], v[20:23], v[172:175]\n" \
  "v_cvt_pk_bf16_f32 v176, v160, v161\n" \
  "v_cndmask_b32_dpp v232, v232, v232, vcc quad_perm:[3,0,1,2] row_mask:0xf bank_mask:0xf\n" \
  "s_nop 1\n" \
  "v_mfma_f32_16x16x32_bf16 v[168:171], v[136:139], v[24:27], v[168:171]\n" \
  "v_cvt_pk_bf16_f32 v177, v162, v163\n" \
  "s_nop 2\n" \
  "v_mfma_f32_16x16x32_bf16 v[172:175], v[152:155], v[24:27], v[172:175]\n" \
  "v_cvt_pk_bf16_f32 v178, v164, v165\n" \
  "v_lshlrev_b32_e64 v230, v232, 3\n" \
  "s_nop 1\n" \
  "v_mfma_f32_16x16x32_bf16 v[168:171], v[140:143], v[28:31], v[168:171]\n" \
  "v_cvt_pk_bf16_f32 v179, v166, v167\n" \
  "v_and_or_b32 v234, v229, v230, v234\n" \
  "s_nop 1\n" \
  "v_mfma_f32_16x16x32_bf16 v[172:175], v[156:159], v[28:31], v[172:175]\n" \
  "s_waitcnt vmcnt(5)\n" \
  "global_store_dwordx4 v186, v[176:179], s[44:45] offset:0 sc1\n" \
  "v_lshrrev_b32 v228, v233, v235\n" \
  "v_lshrrev_b32_dpp v229, v233, v235 quad_perm:[2,3,0,1] row_mask:0xf bank_mask:0xf\n" \
  "v_mfma_f32_16x16x32_bf16 v[160:163], v[128:131], v[32:35], 0\n" \
  "s_cmp_lg_u32 s33, 1021\n" \
  "s_cbranch_scc0 LT1_e\n" \
  "LT1_r:\n" \
  "v_lshrrev_b32_dpp v230, v233, v235 quad_perm:[1,2,3,0] row_mask:0xf bank_mask:0xf\n" \
  "v_lshrrev_b32_dpp v231, v233, v235 quad_perm:[3,0,1,2] row_mask:0xf bank_mask:0xf\n" \
  "v_mfma_f32_16x16x32_bf16 v[164:167], v[144:147], v[32:35], 0\n" \
  "v_perm_b32 v228, v229, v228, s53\n" \
  "v_perm_b32 v230, v231, v230, s54\n" \
  "s_nop 1\n" \
  "v_mfma_f32_16x16x32_bf16 v[160:163], v[132:135], v[36:39], v[160:163]\n" \
  "v_bitop3_b32 v228, v228, v230, s55 bitop3:0xa8\n" \
  "v_lshl_or_b32 v229, v235, 1, s56\n" \
  "s_nop 1\n" \
  "v_mfma_f32_16x16x32_bf16 v[164:167], v[148:151], v[36:39], v[164:167]\n" \
  "v_cvt_pk_bf16_f32 v180, v168, v169\n" \
  "s_nop 2\n" \
  "v_mfma_f32_16x16x32_bf16 v[160:163], v[136:139], v[40:43], v[160:163]\n" \
  "v_cvt_pk_bf16_f32 v181, v170, v171\n" \
  "v_add_u32_dpp v228, v228, v228 quad_perm:[2,3,0,1] row_mask:0xf bank_mask:0xf\n" \
  "s_nop 1\n" \
  "v_mfma_f32_16x16x32_bf16 v[164:167], v[152:155], v[40:43], v[164:167]\n" \
  "v_cvt_pk_bf16_f32 v182, v172, v173\n" \
  "v_min_u32_sdwa v230, v228, v228 dst_sel:DWORD dst_unused:UNUSED_PAD src0_sel:BYTE_0 src1_sel:BYTE_1\n" \
  "s_nop 1\n" \
  "v_mfma_f32_16x16x32_bf16 v[160:163], v[140:143], v[44:47], v[160:163]\n" \
  "v_cvt_pk_bf16_f32 v183, v174, v175\n" \
  "v_min_u32_sdwa v231, v228, v228 dst_sel:DWORD dst_unused:UNUSED_PAD src0_sel:BYTE_2 src1_sel:BYTE_3\n" \
  "s_nop 1\n" \
  "v_mfma_f32_16x16x32_bf16 v[164:167], v[156:159], v[44:47], v[164:167]\n" \
  "s_waitcnt vmcnt(5)\n" \
  "global_store_dwordx4 v186, v[180:183], s[44:45] offset:256 sc1\n" \
  "v_sub_u32 v230, v230, v231\n" \
  "s_nop 1\n" \
  "v_add_u32_dpp v230, v230, v230 quad_perm:[1,0,3,2] row_mask:0xf bank_mask:0xf\n" \
  "v_mfma_f32_16x16x32_bf16 v[168:171], v[128:131], v[48:51], 0\n" \
  "v_cmp_ge_i32 vcc, 0, v230\n" \
  "s_nop 1\n" \
  "v_cndmask_b32_sdwa v228, v228, v228, vcc dst_sel:DWORD dst_unused:UNUSED_PAD src0_sel:WORD_1 src1_sel:WORD_0\n" \
  "s_mov_b64 s[58:59], vcc\n" \
  "s_nop 0\n" \
  "v_mfma_f32_16x16x32_bf16 v[172:175], v[144:147], v[48:51], 0\n" \
  "v_cmp_le_u32_sdwa vcc, v228, v228 src0_sel:BYTE_0 src1_sel:BYTE_1\n" \
  "s_nop 2\n" \
  "v_mfma_f32_16x16x32_bf16 v[168:171], v[132:135], v[52:55], v[168:171]\n" \
  "v_cndmask_b32_dpp v233, v233, v233, vcc quad_perm:[2,3,0,1] row_mask:0xf bank_mask:0xf\n" \
  "s_mov_b64 vcc, s[58:59]\n" \
  "s_nop 0\n" \
  "v_cndmask_b32_dpp v233, v233, v233, vcc quad_perm:[3,0,1,2] row_mask:0xf bank_mask:0xf\n" \
  "s_nop 0\n" \
  "v_mfma_f32_16x16x32_bf16 v[172:175], v[148:151], v[52:55], v[172:175]\n" \
  "v_cvt_pk_bf16_f32 v176, v160, v161\n" \
  "v_lshlrev_b32_e64 v230, v233, 3\n" \
  "s_nop 1\n" \
  "v_mfma_f32_16x16x32_bf16 v[168:171], v[136:139], v[56:59], v[168:171]\n" \
  "v_cvt_pk_bf16_f32 v177, v162, v163\n" \
  "v_and_or_b32 v235, v229, v230, v235\n" \
  "s_nop 1\n" \
  "v_mfma_f32_16x16x32_bf16 v[172:175], v[152:155], v[56:59], v[172:175]\n" \
  "v_cvt_pk_bf16_f32 v178, v164, v165\n" \
  "s_set_gpr_idx_on s50, gpr_idx(DST)\n" \
  "v_perm_b32 v196, v233, v232, s57\n" \
  "s_set_gpr_idx_off\n" \
  "s_add_u32 s50, s50, 2\n" \
  "v_mfma_f32_16x16x32_bf16 v[168:171], v[140:143], v[60:63], v[168:171]\n" \
  "v_cvt_pk_bf16_f32 v179, v166, v167\n" \
  "s_set_gpr_idx_on s50, gpr_idx(SRC0)\n" \
  "v_min_u32_e64 v232, v196, s51\n" \
  "v_min_u32_e64 v233, v197, s51\n" \
  "s_set_gpr_idx_off\n" \
  "v_mfma_f32_16x16x32_bf16 v[172:175], v[156:159], v[60:63], v[172:175]\n" \
  "s_waitcnt vmcnt(5)\n" \
  "global_store_dwordx4 v186, v[176:179], s[44:45] offset:512 sc1\n" \
  "s_nop 1\n" \
  "v_mfma_f32_16x16x32_bf16 v[160:163], v[128:131], v[64:67], 0\n" \
  "v_mad_u32_u24 v232, v232, s52, v236\n" \
  "v_lshrrev_b32 v228, v232, v234\n" \
  "s_nop 1\n" \
  "v_mfma_f32_16x16x32_bf16 v[164:167], v[144:147], v[64:67], 0\n" \
  "v_lshrrev_b32_dpp v229, v232, v234 quad_perm:[2,3,0,1] row_mask:0xf bank_mask:0xf\n" \
  "v_lshrrev_b32_dpp v230, v232, v234 quad_perm:[1,2,3,0] row_mask:0xf bank_mask:0xf\n" \
  "s_nop 1\n" \
  "v_mfma_f32_16x16x32_bf16 v[160:163], v[132:135], v[68:71], v[160:163]\n" \
  "v_lshrrev_b32_dpp v231, v232, v234 quad_perm:[3,0,1,2] row_mask:0xf bank_mask:0xf\n" \
  "v_perm_b32 v228, v229, v228, s53\n" \
  "s_nop 1\n" \
  "v_mfma_f32_16x16x32_bf16 v[164:167], v[148:151], v[68:71], v[164:167]\n" \
  "v_cvt_pk_bf16_f32 v180, v168, v169\n" \
  "v_perm_b32 v230, v231, v230, s54\n" \
  "s_nop 1\n" \
  "v_mfma_f32_16x16x32_bf16 v[160:163], v[136:139], v[72:75], v[160:163]\n" \
  "v_cvt_pk_bf16_f32 v181, v170, v171\n" \
  "v_bitop3_b32 v228, v228, v230, s55 bitop3:0xa8\n" \
  "s_nop 1\n" \
  "v_mfma_f32_16x16x32_bf16 v[164:167], v[152:155], v[72:75], v[164:167]\n" \
  "v_cvt_pk_bf16_f32 v182, v172, v173\n" \
  "v_lshl_or_b32 v229, v234, 1, s56\n" \
  "s_nop 1\n" \
  "v_mfma_f32_16x16x32_bf16 v[160:163], v[140:143], v[76:79], v[160:163]\n" \
  "v_cvt_pk_bf16_f32 v183, v174, v175\n" \
  "s_nop 2\n" \
  "v_mfma_f32_16x16x32_bf16 v[164:167], v[156:159], v[76:79], v[164:167]\n" \
  "s_waitcnt vmcnt(5)\n" \
  "global_store_dwordx4 v186, v[180:183], s[44:45] offset:768 sc1\n" \
  "v_add_u32_dpp v228, v228, v228 quad_perm:[2,3,0,1] row_mask:0xf bank_mask:0xf\n" \
  "v_min_u32_sdwa v230, v228, v228 dst_sel:DWORD dst_unused:UNUSED_PAD src0_sel:BYTE_0 src1_sel:BYTE_1\n" \
  "v_mfma_f32_16x16x32_bf16 v[168:171], v[128:131], v[80:83], 0\n" \
  "s_mov_b64 exec, 1\n" \
  "ds_add_u32 v190, v191 offset:0\n" \
  "s_mov_b64 exec, -1\n" \
  "v_min_u32_sdwa v231, v228, v228 dst_sel:DWORD dst_unused:UNUSED_PAD src0_sel:BYTE_2 src1_sel:BYTE_3\n" \
  "v_sub_u32 v230, v230, v231\n" \
  "v_mfma_f32_16x16x32_bf16 v[172:175], v[144:147], v[80:83], 0\n" \
  "v_mad_u32_u24 v233, v233, s52, v237\n" \
  "v_add_u32_dpp v230, v230, v230 quad_perm:[1,0,3,2] row_mask:0xf bank_mask:0xf\n" \
  "s_nop 1\n" \
  "v_mfma_f32_16x16x32_bf16 v[168:171], v[132:135], v[84:87], v[168:171]\n" \
  "v_cmp_ge_i32 vcc, 0, v230\n" \
  "s_nop 1\n" \
  "v_cndmask_b32_sdwa v228, v228, v228, vcc dst_sel:DWORD dst_unused:UNUSED_PAD src0_sel:WORD_1 src1_sel:WORD_0\n" \
  "s_mov_b64 s[58:59], vcc\n" \
  "s_nop 0\n" \
  "v_mfma_f32_16x16x32_bf16 v[172:175], v[148:151], v[84:87], v[172:175]\n" \
  "v_cvt_pk_bf16_f32 v176, v160, v161\n" \
  "v_cmp_le_u32_sdwa vcc, v228, v228 src0_sel:BYTE_0 src1_sel:BYTE_1\n" \
  "s_nop 1\n" \
  "v_mfma_f32_16x16x32_bf16 v[168:171], v[136:139], v[88:91], v[168:171]\n" \
  "v_cvt_pk_bf16_f32 v177, v162, v163\n" \
  "s_nop 2\n" \
  "v_mfma_f32_16x16x32_bf16 v[172:175], v[152:155], v[88:91], v[172:175]\n" \
  "v_cvt_pk_bf16_f32 v178, v164, v165\n" \
  "v_cndmask_b32_dpp v232, v232, v232, vcc quad_perm:[2,3,0,1] row_mask:0xf bank_mask:0xf\n" \
  "s_mov_b64 vcc, s[58:59]\n" \
  "s_nop 0\n" \
  "v_mfma_f32_16x16x32_bf16 v[168:171], v[140:143], v[92:95], v[168:171]\n" \
  "v_cvt_pk_bf16_f32 v179, v166, v167\n" \
  "v_cndmask_b32_dpp v232, v232, v232, vcc quad_perm:[3,0,1,2] row_mask:0xf bank_mask:0xf\n" \
  "s_nop 1\n" \
  "v_mfma_f32_16x16x32_bf16 v[172:175], v[156:159], v[92:95], v[172:175]\n" \
  "s_waitcnt vmcnt(5)\n" \
  "global_store_dwordx4 v186, v[176:179], s[44:45] offset:1024 sc1\n" \
  "v_lshlrev_b32_e64 v230, v232, 3\n" \
  "v_and_or_b32 v234, v229, v230, v234\n" \
  "v_mfma_f32_16x16x32_bf16 v[160:163], v[128:131], v[96:99], 0\n" \
  "ds_read_b32 v192, v190\n" \
  "v_add_u32 v184, s97, v184\n" \
  "v_lshrrev_b32 v228, v233, v235\n" \
  "v_lshrrev_b32_dpp v229, v233, v235 quad_perm:[2,3,0,1] row_mask:0xf bank_mask:0xf\n" \
  "v_mfma_f32_16x16x32_bf16 v[164:167], v[144:147], v[96:99], 0\n" \
  "v_add_u32 v185, s97, v185\n" \
  "v_lshrrev_b32_dpp v230, v233, v235 quad_perm:[1,2,3,0] row_mask:0xf bank_mask:0xf\n" \
  "v_lshrrev_b32_dpp v231, v233, v235 quad_perm:[3,0,1,2] row_mask:0xf bank_mask:0xf\n" \
  "s_nop 0\n" \
  "v_mfma_f32_16x16x32_bf16 v[160:163], v[132:135], v[100:103], v[160:163]\n" \
  "s_cmp_lt_u32 s98, 10\n" \
  "s_cselect_b32 s6, s6, s34\n" \
  "s_add_u32 s40, s92, s6\n" \
  "s_addc_u32 s41, s93, 0\n" \
  "s_add_u32 s6, s6, 1048576\n" \
  "v_perm_b32 v228, v229, v228, s53\n" \
  "v_perm_b32 v230, v231, v230, s54\n" \
  "v_mfma_f32_16x16x32_bf16 v[164:167], v[148:151], v[100:103], v[164:167]\n" \
  "v_cvt_pk_bf16_f32 v180, v168, v169\n" \
  "s_nop 2\n" \
  "v_mfma_f32_16x16x32_bf16 v[160:163], v[136:139], v[104:107], v[160:163]\n" \
  "v_cvt_pk_bf16_f32 v181, v170, v171\n" \
  "v_bitop3_b32 v228, v228, v230, s55 bitop3:0xa8\n" \
  "s_nop 1\n" \
  "v_mfma_f32_16x16x32_bf16 v[164:167], v[152:155], v[104:107], v[164:167]\n" \
  "s_waitcnt lgkmcnt(0)\n" \
  "v_readfirstlane_b32 s35, v192\n" \
  "v_cvt_pk_bf16_f32 v182, v172, v173\n" \
  "v_lshl_or_b32 v229, v235, 1, s56\n" \
  "v_mfma_f32_16x16x32_bf16 v[160:163], v[140:143], v[108:111], v[160:163]\n" \
  "s_cmp_lt_u32 s35, s99\n" \
  "s_cbranch_scc1 LT1_w\n" \
  "LT1_c:\n" \
  "s_mov_b32 m0, s96\n" \
  "s_nop 0\n" \
  "global_load_lds_dwordx4 v189, s[40:41]\n" \
  "global_load_lds_dwordx4 v194, s[40:41] offset:1024\n" \
  "s_add_u32 s96, s96, s97\n" \
  "s_sub_u32 s97, 0, s97\n" \
  "s_add_u32 s99, s99, 4\n" \
  "v_cvt_pk_bf16_f32 v183, v174, v175\n" \
  "v_add_u32_dpp v228, v228, v228 quad_perm:[2,3,0,1] row_mask:0xf bank_mask:0xf\n" \
  "v_mfma_f32_16x16x32_bf16 v[164:167], v[156:159], v[108:111], v[164:167]\n" \
  "s_waitcnt vmcnt(5)\n" \
  "global_store_dwordx4 v186, v[180:183], s[44:45] offset:1280 sc1\n" \
  "v_min_u32_sdwa v230, v228, v228 dst_sel:DWORD dst_unused:UNUSED_PAD src0_sel:BYTE_0 src1_sel:BYTE_1\n" \
  "v_min_u32_sdwa v231, v228, v228 dst_sel:DWORD dst_unused:UNUSED_PAD src0_sel:BYTE_2 src1_sel:BYTE_3\n" \
  "v_mfma_f32_16x16x32_bf16 v[168:171], v[128:131], v[112:115], 0\n" \
  "ds_read_b64_tr_b16 v[128:129], v184 offset:0\n" \
  "ds_read_b64_tr_b16 v[130:131], v184 offset:256\n" \
  "v_sub_u32 v230, v230, v231\n" \
  "s_nop 1\n" \
  "v_add_u32_dpp v230, v230, v230 quad_perm:[1,0,3,2] row_mask:0xf bank_mask:0xf\n" \
  "v_mfma_f32_16x16x32_bf16 v[172:175], v[144:147], v[112:115], 0\n" \
  "ds_read_b64_tr_b16 v[144:145], v185 offset:0\n" \
  "ds_read_b64_tr_b16 v[146:147], v185 offset:256\n" \
  "v_cmp_ge_i32 vcc, 0, v230\n" \
  "s_nop 0\n" \
  "v_mfma_f32_16x16x32_bf16 v[168:171], v[132:135], v[116:119], v[168:171]\n" \
  "ds_read_b64_tr_b16 v[132:133], v184 offset:2048\n" \
  "ds_read_b64_tr_b16 v[134:135], v184 offset:2304\n" \
  "v_cndmask_b32_sdwa v228, v228, v228, vcc dst_sel:DWORD dst_unused:UNUSED_PAD src0_sel:WORD_1 src1_sel:WORD_0\n" \
  "s_mov_b64 s[58:59], vcc\n" \
  "v_cmp_le_u32_sdwa vcc, v228, v228 src0_sel:BYTE_0 src1_sel:BYTE_1\n" \
  "v_mfma_f32_16x16x32_bf16 v[172:175], v[148:151], v[116:119], v[172:175]\n" \
  "ds_read_b64_tr_b16 v[148:149], v185 offset:2048\n" \
  "ds_read_b64_tr_b16 v[150:151], v185 offset:2304\n" \
  "v_cvt_pk_bf16_f32 v176, v160, v161\n" \
  "v_cndmask_b32_dpp v233, v233, v233, vcc quad_perm:[2,3,0,1] row_mask:0xf bank_mask:0xf\n" \
  "s_mov_b64 vcc, s[58:59]\n" \
  "v_mfma_f32_16x16x32_bf16 v[168:171], v[136:139], v[120:123], v[168:171]\n" \
  "ds_read_b64_tr_b16 v[136:137], v184 offset:4096\n" \
  "ds_read_b64_tr_b16 v[138:139], v184 offset:4352\n" \
  "v_cvt_pk_bf16_f32 v177, v162, v163\n" \
  "v_cndmask_b32_dpp v233, v233, v233, vcc quad_perm:[3,0,1,2] row_mask:0xf bank_mask:0xf\n" \
  "v_mfma_f32_16x16x32_bf16 v[172:175], v[152:155], v[120:123], v[172:175]\n" \
  "ds_read_b64_tr_b16 v[152:153], v185 offset:4096\n" \
  "ds_read_b64_tr_b16 v[154:155], v185 offset:4352\n" \
  "v_cvt_pk_bf16_f32 v178, v164, v165\n" \
  "v_lshlrev_b32_e64 v230, v233, 3\n" \
  "v_mfma_f32_16x16x32_bf16 v[168:171], v[140:143], v[124:127], v[168:171]\n" \
  "ds_read_b64_tr_b16 v[140:141], v184 offset:6144\n" \
  "ds_read_b64_tr_b16 v[142:143], v184 offset:6400\n" \
  "v_cvt_pk_bf16_f32 v179, v166, v167\n" \
  "v_and_or_b32 v235, v229, v230, v235\n" \
  "v_mfma_f32_16x16x32_bf16 v[172:175], v[156:159], v[124:127], v[172:175]\n" \
  "ds_read_b64_tr_b16 v[156:157], v185 offset:6144\n" \
  "ds_read_b64_tr_b16 v[158:159], v185 offset:6400\n" \
  "s_waitcnt vmcnt(5)\n" \
  "global_store_dwordx4 v187, v[176:179], s[44:45] offset:0 sc1\n" \
  "s_set_gpr_idx_on s50, gpr_idx(DST)\n" \
  "v_perm_b32 v196, v233, v232, s57\n" \
  "s_set_gpr_idx_off\n" \
  "s_add_u32 s50, s50, 2\n" \
  "s_add_u32 s98, s98, 1\n" \
  "v_cvt_pk_bf16_f32 v180, v168, v169\n" \
  "v_cvt_pk_bf16_f32 v181, v170, v171\n" \
  "v_cvt_pk_bf16_f32 v182, v172, v173\n" \
  "v_cvt_pk_bf16_f32 v183, v174, v175\n" \
  "s_waitcnt vmcnt(5)\n" \
  "global_store_dwordx4 v188, v[180:183], s[44:45] offset:0 sc1\n" \
  "s_add_u32 s44, s44, 128000\n" \
  "s_addc_u32 s45, s45, 0\n" \
  "s_cmp_lt_u32 s98, 8\n" \
  "s_cbranch_scc1 LT1\n" \
  "s_mul_i32 s5, s28, 2176\n" \
  "s_add_u32 s5, s5, 154624\n" \
  "v_and_b32 v228, 4, v255\n" \
  "v_bfe_u32 v229, v255, 3, 1\n" \
  "v_lshl_add_u32 v228, v229, 1, v228\n" \
  "v_mul_u32_u24 v228, 272, v228\n" \
  "v_lshl_add_u32 v228, v253, 2, v228\n" \
  "v_and_b32 v229, 1, v255\n" \
  "v_lshl_add_u32 v228, v229, 1, v228\n" \
  "v_add_u32 v228, s5, v228\n" \
  "v_and_b32 v229, 2, v255\n" \
  "v_mul_u32_u24 v229, 136, v229\n" \
  "v_sub_u32 v230, v228, v229\n" \
  "v_add_u32 v228, v228, v229\n" \
  "v_add_u32 v229, 272, v230\n" \
  "v_lshl_add_u32 v230, v253, 4, v238\n" \
  "v_add_u32 v230, s5, v230\n" \
  "ds_write_b16 v228, v196 offset:0\n" \
  "ds_write_b16 v228, v198 offset:16\n" \
  "ds_write_b16 v228, v200 offset:32\n" \
  "ds_write_b16 v228, v202 offset:48\n" \
  "ds_write_b16 v229, v204 offset:64\n" \
  "ds_write_b16 v229, v206 offset:80\n" \
  "ds_write_b16 v229, v208 offset:96\n" \
  "ds_write_b16 v229, v210 offset:112\n" \
  "ds_write_b16 v229, v212 offset:128\n" \
  "ds_write_b16 v229, v214 offset:144\n" \
  "ds_write_b16 v229, v216 offset:160\n" \
  "ds_write_b16 v229, v218 offset:176\n" \
  "ds_write_b16 v228, v220 offset:192\n" \
  "ds_write_b16 v228, v222 offset:208\n" \
  "ds_write_b16 v228, v224 offset:224\n" \
  "ds_write_b16 v228, v226 offset:240\n" \
  "ds_read_b128 v[244:247], v230 offset:0\n" \
  "ds_read_b128 v[248:251], v230 offset:1088\n" \
  "ds_write_b16_d16_hi v228, v196 offset:0\n" \
  "ds_write_b16_d16_hi v228, v198 offset:16\n" \
  "ds_write_b16_d16_hi v228, v200 offset:32\n" \
  "ds_write_b16_d16_hi v228, v202 offset:48\n" \
  "ds_write_b16_d16_hi v229, v204 offset:64\n" \
  "ds_write_b16_d16_hi v229, v206 offset:80\n" \
  "ds_write_b16_d16_hi v229, v208 offset:96\n" \
  "ds_write_b16_d16_hi v229, v210 offset:112\n" \
  "ds_write_b16_d16_hi v229, v212 offset:128\n" \
  "ds_write_b16_d16_hi v229, v214 offset:144\n" \
  "ds_write_b16_d16_hi v229, v216 offset:160\n" \
  "ds_write_b16_d16_hi v229, v218 offset:176\n" \
  "ds_write_b16_d16_hi v228, v220 offset:192\n" \
  "ds_write_b16_d16_hi v228, v222 offset:208\n" \
  "ds_write_b16_d16_hi v228, v224 offset:224\n" \
  "ds_write_b16_d16_hi v228, v226 offset:240\n" \
  "ds_read_b128 v[160:163], v230 offset:0\n" \
  "ds_read_b128 v[164:167], v230 offset:1088\n" \
  "s_lshr_b32 s35, s7, 5\n" \
  "s_mul_i32 s35, s35, 819200\n" \
  "s_and_b32 s36, s7, 31\n" \
  "s_lshl_b32 s36, s36, 10\n" \
  "s_add_u32 s35, s35, s36\n" \
  "s_add_u32 s35, s35, 102400000\n" \
  "s_lshr_b32 s36, s4, 5\n" \
  "s_waitcnt lgkmcnt(0)\n" \
  "s_add_u32 s37, s36, 0\n" \
  "s_cmp_ge_u32 s37, 24\n" \
  "s_cbranch_scc1 L9_\n" \
  "s_min_u32 s37, s37, 24\n" \
  "s_lshl_b32 s37, s37, 15\n" \
  "s_add_u32 s37, s37, s35\n" \
  "s_add_u32 s58, s24, s37\n" \
  "s_addc_u32 s59, s25, 0\n" \
  "global_store_dwordx4 v238, v[244:247], s[58:59] sc1\n" \
  "L9_:\n" \
  "s_add_u32 s37, s36, 1\n" \
  "s_cmp_ge_u32 s37, 26\n" \
  "s_cbranch_scc1 L10_\n" \
  "s_min_u32 s37, s37, 24\n" \
  "s_lshl_b32 s37, s37, 15\n" \
  "s_add_u32 s37, s37, s35\n" \
  "s_add_u32 s58, s24, s37\n" \
  "s_addc_u32 s59, s25, 0\n" \
  "global_store_dwordx4 v238, v[160:163], s[58:59] sc1\n" \
  "L10_:\n" \
  "s_add_u32 s37, s36, 2\n" \
  "s_cmp_ge_u32 s37, 24\n" \
  "s_cbranch_scc1 L11_\n" \
  "s_min_u32 s37, s37, 24\n" \
  "s_lshl_b32 s37, s37, 15\n" \
  "s_add_u32 s37, s37, s35\n" \
  "s_add_u32 s58, s24, s37\n" \
  "s_addc_u32 s59, s25, 0\n" \
  "global_store_dwordx4 v238, v[248:251], s[58:59] sc1\n" \
  "L11_:\n" \
  "s_add_u32 s37, s36, 3\n" \
  "s_cmp_ge_u32 s37, 24\n" \
  "s_cbranch_scc1 L12_\n" \
  "s_min_u32 s37, s37, 24\n" \
  "s_lshl_b32 s37, s37, 15\n" \
  "s_add_u32 s37, s37, s35\n" \
  "s_add_u32 s58, s24, s37\n" \
  "s_addc_u32 s59, s25, 0\n" \
  "global_store_dwordx4 v238, v[164:167], s[58:59] sc1\n" \
  "L12_:\n" \
  "global_load_dword v193, v193, s[48:49] offset:1024 sc1\n" \
  "s_lshl_b32 s35, s29, 8\n" \
  "v_lshl_add_u32 v196, v255, 11, s35\n" \
  "s_lshl_b32 s35, s30, 11\n" \
  "v_lshl_add_u32 v197, v255, 8, s35\n" \
  "LT2:\n" \
  "s_waitcnt lgkmcnt(14)\n" \
  "v_mfma_f32_16x16x32_bf16 v[160:163], v[128:131], v[0:3], 0\n" \
  "s_nop 2\n" \
  "s_waitcnt lgkmcnt(12)\n" \
  "v_mfma_f32_16x16x32_bf16 v[164:167], v[144:147], v[0:3], 0\n" \
  "s_nop 2\n" \
  "s_waitcnt lgkmcnt(10)\n" \
  "v_mfma_f32_16x16x32_bf16 v[160:163], v[132:135], v[4:7], v[160:163]\n" \
  "s_nop 2\n" \
  "s_waitcnt lgkmcnt(8)\n" \
  "v_mfma_f32_16x16x32_bf16 v[164:167], v[148:151], v[4:7], v[164:167]\n" \
  "s_nop 2\n" \
  "s_waitcnt lgkmcnt(6)\n" \
  "v_mfma_f32_16x16x32_bf16 v[160:163], v[136:139], v[8:11], v[160:163]\n" \
  "s_nop 2\n" \
  "s_waitcnt lgkmcnt(4)\n" \
  "v_mfma_f32_16x16x32_bf16 v[164:167], v[152:155], v[8:11], v[164:167]\n" \
  "s_nop 2\n" \
  "s_waitcnt lgkmcnt(2)\n" \
  "v_mfma_f32_16x16x32_bf16 v[160:163], v[140:143], v[12:15], v[160:163]\n" \
  "s_nop 2\n" \
  "s_waitcnt lgkmcnt(0)\n" \
  "v_mfma_f32_16x16x32_bf16 v[164:167], v[156:159], v[12:15], v[164:167]\n" \
  "s_nop 3\n" \
  "v_mfma_f32_16x16x32_bf16 v[168:171], v[128:131], v[16:19], 0\n" \
  "s_nop 3\n" \
  "v_mfma_f32_16x16x32_bf16 v[172:175], v[144:147], v[16:19], 0\n" \
  "s_nop 3\n" \
  "v_mfma_f32_16x16x32_bf16 v[168:171], v[132:135], v[20:23], v[168:171]\n" \
  "s_nop 3\n" \
  "v_mfma_f32_16x16x32_bf16 v[172:175], v[148:151], v[20:23], v[172:175]\n" \
  "v_cvt_pk_bf16_f32 v176, v160, v161\n" \
  "s_nop 2\n" \
  "v_mfma_f32_16x16x32_bf16 v[168:171], v[136:139], v[24:27], v[168:171]\n" \
  "v_cvt_pk_bf16_f32 v177, v162, v163\n" \
  "s_nop 2\n" \
  "v_mfma_f32_16x16x32_bf16 v[172:175], v[152:155], v[24:27], v[172:175]\n" \
  "v_cvt_pk_bf16_f32 v178, v164, v165\n" \
  "s_nop 2\n" \
  "v_mfma_f32_16x16x32_bf16 v[168:171], v[140:143], v[28:31], v[168:171]\n" \
  "v_cvt_pk_bf16_f32 v179, v166, v167\n" \
  "s_nop 2\n" \
  "v_mfma_f32_16x16x32_bf16 v[172:175], v[156:159], v[28:31], v[172:175]\n" \
  "s_waitcnt vmcnt(5)\n" \
  "global_store_dwordx4 v186, v[176:179], s[44:45] offset:0 sc1\n" \
  "s_nop 1\n" \
  "v_mfma_f32_16x16x32_bf16 v[160:163], v[128:131], v[32:35], 0\n" \
  "s_nop 3\n" \
  "v_mfma_f32_16x16x32_bf16 v[164:167], v[144:147], v[32:35], 0\n" \
  "s_nop 3\n" \
  "v_mfma_f32_16x16x32_bf16 v[160:163], v[132:135], v[36:39], v[160:163]\n" \
  "s_nop 3\n" \
  "v_mfma_f32_16x16x32_bf16 v[164:167], v[148:151], v[36:39], v[164:167]\n" \
  "v_cvt_pk_bf16_f32 v180, v168, v169\n" \
  "s_nop 2\n" \
  "v_mfma_f32_16x16x32_bf16 v[160:163], v[136:139], v[40:43], v[160:163]\n" \
  "v_cvt_pk_bf16_f32 v181, v170, v171\n" \
  "s_nop 2\n" \
  "v_mfma_f32_16x16x32_bf16 v[164:167], v[152:155], v[40:43], v[164:167]\n" \
  "v_cvt_pk_bf16_f32 v182, v172, v173\n" \
  "s_nop 2\n" \
  "v_mfma_f32_16x16x32_bf16 v[160:163], v[140:143], v[44:47], v[160:163]\n" \
  "v_cvt_pk_bf16_f32 v183, v174, v175\n" \
  "s_nop 2\n" \
  "v_mfma_f32_16x16x32_bf16 v[164:167], v[156:159], v[44:47], v[164:167]\n" \
  "s_waitcnt vmcnt(5)\n" \
  "global_store_dwordx4 v186, v[180:183], s[44:45] offset:256 sc1\n" \
  "s_nop 1\n" \
  "v_mfma_f32_16x16x32_bf16 v[168:171], v[128:131], v[48:51], 0\n" \
  "s_nop 3\n" \
  "v_mfma_f32_16x16x32_bf16 v[172:175], v[144:147], v[48:51], 0\n" \
  "s_nop 3\n" \
  "v_mfma_f32_16x16x32_bf16 v[168:171], v[132:135], v[52:55], v[168:171]\n" \
  "s_nop 3\n" \
  "v_mfma_f32_16x16x32_bf16 v[172:175], v[148:151], v[52:55], v[172:175]\n" \
  "v_cvt_pk_bf16_f32 v176, v160, v161\n" \
  "s_nop 2\n" \
  "v_mfma_f32_16x16x32_bf16 v[168:171], v[136:139], v[56:59], v[168:171]\n" \
  "v_cvt_pk_bf16_f32 v177, v162, v163\n" \
  "s_nop 2\n" \
  "v_mfma_f32_16x16x32_bf16 v[172:175], v[152:155], v[56:59], v[172:175]\n" \
  "v_cvt_pk_bf16_f32 v178, v164, v165\n" \
  "s_nop 2\n" \
  "v_mfma_f32_16x16x32_bf16 v[168:171], v[140:143], v[60:63], v[168:171]\n" \
  "v_cvt_pk_bf16_f32 v179, v166, v167\n" \
  "s_nop 2\n" \
  "v_mfma_f32_16x16x32_bf16 v[172:175], v[156:159], v[60:63], v[172:175]\n" \
  "s_waitcnt vmcnt(5)\n" \
  "global_store_dwordx4 v186, v[176:179], s[44:45] offset:512 sc1\n" \
  "s_nop 1\n" \
  "v_mfma_f32_16x16x32_bf16 v[160:163], v[128:131], v[64:67], 0\n" \
  "s_nop 3\n" \
  "v_mfma_f32_16x16x32_bf16 v[164:167], v[144:147], v[64:67], 0\n" \
  "s_nop 3\n" \
  "v_mfma_f32_16x16x32_bf16 v[160:163], v[132:135], v[68:71], v[160:163]\n" \
  "s_nop 3\n" \
  "v_mfma_f32_16x16x32_bf16 v[164:167], v[148:151], v[68:71], v[164:167]\n" \
  "v_cvt_pk_bf16_f32 v180, v168, v169\n" \
  "s_nop 2\n" \
  "v_mfma_f32_16x16x32_bf16 v[160:163], v[136:139], v[72:75], v[160:163]\n" \
  "v_cvt_pk_bf16_f32 v181, v170, v171\n" \
  "s_nop 2\n" \
  "v_mfma_f32_16x16x32_bf16 v[164:167], v[152:155], v[72:75], v[164:167]\n" \
  "v_cvt_pk_bf16_f32 v182, v172, v173\n" \
  "s_nop 2\n" \
  "v_mfma_f32_16x16x32_bf16 v[160:163], v[140:143], v[76:79], v[160:163]\n" \
  "v_cvt_pk_bf16_f32 v183, v174, v175\n" \
  "s_nop 2\n" \
  "v_mfma_f32_16x16x32_bf16 v[164:167], v[156:159], v[76:79], v[164:167]\n" \
  "s_waitcnt vmcnt(5)\n" \
  "global_store_dwordx4 v186, v[180:183], s[44:45] offset:768 sc1\n" \
  "s_nop 1\n" \
  "v_mfma_f32_16x16x32_bf16 v[168:171], v[128:131], v[80:83], 0\n" \
  "s_mov_b64 exec, 1\n" \
  "ds_add_u32 v190, v191 offset:0\n" \
  "s_mov_b64 exec, -1\n" \
  "s_nop 0\n" \
  "v_mfma_f32_16x16x32_bf16 v[172:175], v[144:147], v[80:83], 0\n" \
  "s_nop 3\n" \
  "v_mfma_f32_16x16x32_bf16 v[168:171], v[132:135], v[84:87], v[168:171]\n" \
  "s_nop 3\n" \
  "v_mfma_f32_16x16x32_bf16 v[172:175], v[148:151], v[84:87], v[172:175]\n" \
  "v_cvt_pk_bf16_f32 v176, v160, v161\n" \
  "s_nop 2\n" \
  "v_mfma_f32_16x16x32_bf16 v[168:171], v[136:139], v[88:91], v[168:171]\n" \
  "v_cvt_pk_bf16_f32 v177, v162, v163\n" \
  "s_nop 2\n" \
  "v_mfma_f32_16x16x32_bf16 v[172:175], v[152:155], v[88:91], v[172:175]\n" \
  "v_cvt_pk_bf16_f32 v178, v164, v165\n" \
  "s_nop 2\n" \
  "v_mfma_f32_16x16x32_bf16 v[168:171], v[140:143], v[92:95], v[168:171]\n" \
  "v_cvt_pk_bf16_f32 v179, v166, v167\n" \
  "s_nop 2\n" \
  "v_mfma_f32_16x16x32_bf16 v[172:175], v[156:159], v[92:95], v[172:175]\n" \
  "s_waitcnt vmcnt(5)\n" \
  "global_store_dwordx4 v186, v[176:179], s[44:45] offset:1024 sc1\n" \
  "s_nop 1\n" \
  "v_mfma_f32_16x16x32_bf16 v[160:163], v[128:131], v[96:99], 0\n" \
  "ds_read_b32 v192, v190\n" \
  "v_add_u32 v184, s97, v184\n" \
  "s_nop 1\n" \
  "v_mfma_f32_16x16x32_bf16 v[164:167], v[144:147], v[96:99], 0\n" \
  "v_add_u32 v185, s97, v185\n" \
  "s_nop 2\n" \
  "v_mfma_f32_16x16x32_bf16 v[160:163], v[132:135], v[100:103], v[160:163]\n" \
  "s_cmp_lt_u32 s98, 10\n" \
  "s_cselect_b32 s6, s6, s34\n" \
  "s_add_u32 s40, s92, s6\n" \
  "s_addc_u32 s41, s93, 0\n" \
  "s_add_u32 s6, s6, 1048576\n" \
  "v_mfma_f32_16x16x32_bf16 v[164:167], v[148:151], v[100:103], v[164:167]\n" \
  "v_cvt_pk_bf16_f32 v180, v168, v169\n" \
  "s_nop 2\n" \
  "v_mfma_f32_16x16x32_bf16 v[160:163], v[136:139], v[104:107], v[160:163]\n" \
  "v_cvt_pk_bf16_f32 v181, v170, v171\n" \
  "s_nop 2\n" \
  "v_mfma_f32_16x16x32_bf16 v[164:167], v[152:155], v[104:107], v[164:167]\n" \
  "s_waitcnt lgkmcnt(0)\n" \
  "v_readfirstlane_b32 s35, v192\n" \
  "v_cvt_pk_bf16_f32 v182, v172, v173\n" \
  "s_nop 0\n" \
  "v_mfma_f32_16x16x32_bf16 v[160:163], v[140:143], v[108:111], v[160:163]\n" \
  "s_cmp_lt_u32 s35, s99\n" \
  "s_cbranch_scc1 LT2_w\n" \
  "LT2_c:\n" \
  "s_mov_b32 m0, s96\n" \
  "s_nop 0\n" \
  "global_load_lds_dwordx4 v189, s[40:41]\n" \
  "global_load_lds_dwordx4 v194, s[40:41] offset:1024\n" \
  "s_add_u32 s96, s96, s97\n" \
  "s_sub_u32 s97, 0, s97\n" \
  "s_add_u32 s99, s99, 4\n" \
  "v_cvt_pk_bf16_f32 v183, v174, v175\n" \
  "v_mfma_f32_16x16x32_bf16 v[164:167], v[156:159], v[108:111], v[164:167]\n" \
  "s_lshl4_add_u32 s35, s28, s98\n" \
  "s_cmp_eq_u32 s35, 9\n" \
  "s_cbranch_scc1 LT2_a\n" \
  "LT2_r:\n" \
  "s_waitcnt vmcnt(5)\n" \
  "global_store_dwordx4 v186, v[180:183], s[44:45] offset:1280 sc1\n" \
  "v_mfma_f32_16x16x32_bf16 v[168:171], v[128:131], v[112:115], 0\n" \
  "ds_read_b64_tr_b16 v[128:129], v184 offset:0\n" \
  "ds_read_b64_tr_b16 v[130:131], v184 offset:256\n" \
  "s_nop 1\n" \
  "v_mfma_f32_16x16x32_bf16 v[172:175], v[144:147], v[112:115], 0\n" \
  "ds_read_b64_tr_b16 v[144:145], v185 offset:0\n" \
  "ds_read_b64_tr_b16 v[146:147], v185 offset:256\n" \
  "s_nop 1\n" \
  "v_mfma_f32_16x16x32_bf16 v[168:171], v[132:135], v[116:119], v[168:171]\n" \
  "ds_read_b64_tr_b16 v[132:133], v184 offset:2048\n" \
  "ds_read_b64_tr_b16 v[134:135], v184 offset:2304\n" \
  "s_nop 1\n" \
  "v_mfma_f32_16x16x32_bf16 v[172:175], v[148:151], v[116:119], v[172:175]\n" \
  "ds_read_b64_tr_b16 v[148:149], v185 offset:2048\n" \
  "ds_read_b64_tr_b16 v[150:151], v185 offset:2304\n" \
  "v_cvt_pk_bf16_f32 v176, v160, v161\n" \
  "s_nop 0\n" \
  "v_mfma_f32_16x16x32_bf16 v[168:171], v[136:139], v[120:123], v[168:171]\n" \
  "ds_read_b64_tr_b16 v[136:137], v184 offset:4096\n" \
  "ds_read_b64_tr_b16 v[138:139], v184 offset:4352\n" \
  "v_cvt_pk_bf16_f32 v177, v162, v163\n" \
  "s_nop 0\n" \
  "v_mfma_f32_16x16x32_bf16 v[172:175], v[152:155], v[120:123], v[172:175]\n" \
  "ds_read_b64_tr_b16 v[152:153], v185 offset:4096\n" \
  "ds_read_b64_tr_b16 v[154:155], v185 offset:4352\n" \
  "v_cvt_pk_bf16_f32 v178, v164, v165\n" \
  "s_nop 0\n" \
  "v_mfma_f32_16x16x32_bf16 v[168:171], v[140:143], v[124:127], v[168:171]\n" \
  "ds_read_b64_tr_b16 v[140:141], v184 offset:6144\n" \
  "ds_read_b64_tr_b16 v[142:143], v184 offset:6400\n" \
  "v_cvt_pk_bf16_f32 v179, v166, v167\n" \
  "s_nop 0\n" \
  "v_mfma_f32_16x16x32_bf16 v[172:175], v[156:159], v[124:127], v[172:175]\n" \
  "ds_read_b64_tr_b16 v[156:157], v185 offset:6144\n" \
  "ds_read_b64_tr_b16 v[158:159], v185 offset:6400\n" \
  "s_waitcnt vmcnt(5)\n" \
  "global_store_dwordx4 v187, v[176:179], s[44:45] offset:0 sc1\n" \
  "s_add_u32 s98, s98, 1\n" \
  "v_cvt_pk_bf16_f32 v180, v168, v169\n" \
  "v_cvt_pk_bf16_f32 v181, v170, v171\n" \
  "s_nop 0\n" \
  "v_cvt_pk_bf16_f32 v182, v172, v173\n" \
  "v_cvt_pk_bf16_f32 v183, v174, v175\n" \
  "s_waitcnt vmcnt(5)\n" \
  "global_store_dwordx4 v188, v[180:183], s[44:45] offset:0 sc1\n" \
  "s_add_u32 s44, s44, 128000\n" \
  "s_addc_u32 s45, s45, 0\n" \
  "s_cmp_lt_u32 s98, 12\n" \
  "s_cbranch_scc1 LT2\n" \
  "s_mov_b64 exec, 0xff\n" \
  "global_load_dword v198, v197, s[48:49] offset:2048 sc1\n" \
  "s_mov_b64 exec, -1\n" \
  "s_bitcmp1_b32 s30, 2\n" \
  "s_cbranch_scc1 LB_hi\n" \
  "s_waitcnt lgkmcnt(14)\n" \
  "v_mfma_f32_16x16x32_bf16 v[160:163], v[128:131], v[0:3], 0\n" \
  "s_nop 2\n" \
  "s_waitcnt lgkmcnt(12)\n" \
  "v_mfma_f32_16x16x32_bf16 v[164:167], v[144:147], v[0:3], 0\n" \
  "s_nop 2\n" \
  "s_waitcnt lgkmcnt(10)\n" \
  "v_mfma_f32_16x16x32_bf16 v[160:163], v[132:135], v[4:7], v[160:163]\n" \
  "s_nop 2\n" \
  "s_waitcnt lgkmcnt(8)\n" \
  "v_mfma_f32_16x16x32_bf16 v[164:167], v[148:151], v[4:7], v[164:167]\n" \
  "s_nop 2\n" \
  "s_waitcnt lgkmcnt(6)\n" \
  "v_mfma_f32_16x16x32_bf16 v[160:163], v[136:139], v[8:11], v[160:163]\n" \
  "s_nop 2\n" \
  "s_waitcnt lgkmcnt(4)\n" \
  "v_mfma_f32_16x16x32_bf16 v[164:167], v[152:155], v[8:11], v[164:167]\n" \
  "s_nop 2\n" \
  "s_waitcnt lgkmcnt(2)\n" \
  "v_mfma_f32_16x16x32_bf16 v[160:163], v[140:143], v[12:15], v[160:163]\n" \
  "s_nop 2\n" \
  "s_waitcnt lgkmcnt(0)\n" \
  "v_mfma_f32_16x16x32_bf16 v[164:167], v[156:159], v[12:15], v[164:167]\n" \
  "s_nop 3\n" \
  "v_mfma_f32_16x16x32_bf16 v[168:171], v[128:131], v[16:19], 0\n" \
  "s_nop 3\n" \
  "v_mfma_f32_16x16x32_bf16 v[172:175], v[144:147], v[16:19], 0\n" \
  "s_nop 3\n" \
  "v_mfma_f32_16x16x32_bf16 v[168:171], v[132:135], v[20:23], v[168:171]\n" \
  "s_nop 3\n" \
  "v_mfma_f32_16x16x32_bf16 v[172:175], v[148:151], v[20:23], v[172:175]\n" \
  "v_cvt_pk_bf16_f32 v176, v160, v161\n" \
  "s_nop 2\n" \
  "v_mfma_f32_16x16x32_bf16 v[168:171], v[136:139], v[24:27], v[168:171]\n" \
  "v_cvt_pk_bf16_f32 v177, v162, v163\n" \
  "s_nop 2\n" \
  "v_mfma_f32_16x16x32_bf16 v[172:175], v[152:155], v[24:27], v[172:175]\n" \
  "v_cvt_pk_bf16_f32 v178, v164, v165\n" \
  "s_nop 2\n" \
  "v_mfma_f32_16x16x32_bf16 v[168:171], v[140:143], v[28:31], v[168:171]\n" \
  "v_cvt_pk_bf16_f32 v179, v166, v167\n" \
  "s_nop 2\n" \
  "v_mfma_f32_16x16x32_bf16 v[172:175], v[156:159], v[28:31], v[172:175]\n" \
  "s_waitcnt vmcnt(5)\n" \
  "global_store_dwordx4 v186, v[176:179], s[46:47] offset:0 sc1\n" \
  "s_nop 1\n" \
  "v_mfma_f32_16x16x32_bf16 v[160:163], v[128:131], v[32:35], 0\n" \
  "s_nop 3\n" \
  "v_mfma_f32_16x16x32_bf16 v[164:167], v[144:147], v[32:35], 0\n" \
  "s_nop 3\n" \
  "v_mfma_f32_16x16x32_bf16 v[160:163], v[132:135], v[36:39], v[160:163]\n" \
  "s_nop 3\n" \
  "v_mfma_f32_16x16x32_bf16 v[164:167], v[148:151], v[36:39], v[164:167]\n" \
  "v_cvt_pk_bf16_f32 v180, v168, v169\n" \
  "s_nop 2\n" \
  "v_mfma_f32_16x16x32_bf16 v[160:163], v[136:139], v[40:43], v[160:163]\n" \
  "v_cvt_pk_bf16_f32 v181, v170, v171\n" \
  "s_nop 2\n" \
  "v_mfma_f32_16x16x32_bf16 v[164:167], v[152:155], v[40:43], v[164:167]\n" \
  "v_cvt_pk_bf16_f32 v182, v172, v173\n" \
  "s_nop 2\n" \
  "v_mfma_f32_16x16x32_bf16 v[160:163], v[140:143], v[44:47], v[160:163]\n" \
  "v_cvt_pk_bf16_f32 v183, v174, v175\n" \
  "s_nop 2\n" \
  "v_mfma_f32_16x16x32_bf16 v[164:167], v[156:159], v[44:47], v[164:167]\n" \
  "s_waitcnt vmcnt(5)\n" \
  "global_store_dwordx4 v186, v[180:183], s[46:47] offset:256 sc1\n" \
  "s_nop 1\n" \
  "v_mfma_f32_16x16x32_bf16 v[168:171], v[128:131], v[48:51], 0\n" \
  "s_nop 3\n" \
  "v_mfma_f32_16x16x32_bf16 v[172:175], v[144:147], v[48:51], 0\n" \
  "s_nop 3\n" \
  "v_mfma_f32_16x16x32_bf16 v[168:171], v[132:135], v[52:55], v[168:171]\n" \
  "s_nop 3\n" \
  "v_mfma_f32_16x16x32_bf16 v[172:175], v[148:151], v[52:55], v[172:175]\n" \
  "v_cvt_pk_bf16_f32 v176, v160, v161\n" \
  "s_nop 2\n" \
  "v_mfma_f32_16x16x32_bf16 v[168:171], v[136:139], v[56:59], v[168:171]\n" \
  "v_cvt_pk_bf16_f32 v177, v162, v163\n" \
  "s_nop 2\n" \
  "v_mfma_f32_16x16x32_bf16 v[172:175], v[152:155], v[56:59], v[172:175]\n" \
  "v_cvt_pk_bf16_f32 v178, v164, v165\n" \
  "s_nop 2\n" \
  "v_mfma_f32_16x16x32_bf16 v[168:171], v[140:143], v[60:63], v[168:171]\n" \
  "v_cvt_pk_bf16_f32 v179, v166, v167\n" \
  "s_nop 2\n" \
  "v_mfma_f32_16x16x32_bf16 v[172:175], v[156:159], v[60:63], v[172:175]\n" \
  "s_waitcnt vmcnt(5)\n" \
  "global_store_dwordx4 v186, v[176:179], s[46:47] offset:512 sc1\n" \
  "s_nop 0\n" \
  "v_cvt_pk_bf16_f32 v180, v168, v169\n" \
  "v_cvt_pk_bf16_f32 v181, v170, v171\n" \
  "s_nop 2\n" \
  "v_cvt_pk_bf16_f32 v182, v172, v173\n" \
  "v_cvt_pk_bf16_f32 v183, v174, v175\n" \
  "s_waitcnt vmcnt(5)\n" \
  "global_store_dwordx4 v186, v[180:183], s[46:47] offset:768 sc1\n" \
  "s_branch LB_end\n" \
  "LT1_w:\n" \
  "L7_:\n" \
  "s_sleep 1\n" \
  "ds_read_b32 v192, v190 offset:0\n" \
  "s_waitcnt lgkmcnt(0)\n" \
  "v_readfirstlane_b32 s35, v192\n" \
  "s_cmp_lt_u32 s35, s99\n" \
  "s_cbranch_scc1 L7_\n" \
  "s_branch LT1_c\n" \
  "LT1_e:\n" \
  "s_bitcmp1_b32 72, s98\n" \
  "s_cbranch_scc0 LT1_r\n" \
  "global_load_dwordx4 v[240:243], v238, s[48:49] offset:0 sc1\n" \
  "s_waitcnt vmcnt(0)\n" \
  "global_store_dwordx4 v238, v[240:243], s[48:49] offset:1024 sc1\n" \
  "s_branch LT1_r\n" \
  "LT2_w:\n" \
  "L8_:\n" \
  "s_sleep 1\n" \
  "ds_read_b32 v192, v190 offset:0\n" \
  "s_waitcnt lgkmcnt(0)\n" \
  "v_readfirstlane_b32 s35, v192\n" \
  "s_cmp_lt_u32 s35, s99\n" \
  "s_cbranch_scc1 L8_\n" \
  "s_branch LT2_c\n" \
  "LT2_a:\n" \
  "s_mov_b32 s50, 0x10000\n" \
  "LB_ec:\n" \
  "v_readfirstlane_b32 s35, v193\n" \
  "s_cmp_eq_u32 s35, s39\n" \
  "s_cbranch_scc1 LB_ok\n" \
  "s_lshl_b32 s35, s26, 2\n" \
  "v_mov_b32 v193, s35\n" \
  "s_sleep 8\n" \
  "global_load_dword v193, v193, s[48:49] offset:1024 sc1\n" \
  "s_waitcnt vmcnt(0)\n" \
  "s_sub_u32 s50, s50, 1\n" \
  "s_cbranch_scc0 LB_ec\n" \
  "LB_ok:\n" \
  "s_mov_b32 exec_hi, 0\n" \
  "global_atomic_add v196, v191, s[48:49] offset:2048 sc1\n" \
  "s_mov_b64 exec, -1\n" \
  "s_branch LT2_r\n" \
  "LB_hi:\n" \
  "s_waitcnt lgkmcnt(14)\n" \
  "v_mfma_f32_16x16x32_bf16 v[160:163], v[128:131], v[64:67], 0\n" \
  "s_nop 2\n" \
  "s_waitcnt lgkmcnt(12)\n" \
  "v_mfma_f32_16x16x32_bf16 v[164:167], v[144:147], v[64:67], 0\n" \
  "s_nop 2\n" \
  "s_waitcnt lgkmcnt(10)\n" \
  "v_mfma_f32_16x16x32_bf16 v[160:163], v[132:135], v[68:71], v[160:163]\n" \
  "s_nop 2\n" \
  "s_waitcnt lgkmcnt(8)\n" \
  "v_mfma_f32_16x16x32_bf16 v[164:167], v[148:151], v[68:71], v[164:167]\n" \
  "s_nop 2\n" \
  "s_waitcnt lgkmcnt(6)\n" \
  "v_mfma_f32_16x16x32_bf16 v[160:163], v[136:139], v[72:75], v[160:163]\n" \
  "s_nop 2\n" \
  "s_waitcnt lgkmcnt(4)\n" \
  "v_mfma_f32_16x16x32_bf16 v[164:167], v[152:155], v[72:75], v[164:167]\n" \
  "s_nop 2\n" \
  "s_waitcnt lgkmcnt(2)\n" \
  "v_mfma_f32_16x16x32_bf16 v[160:163], v[140:143], v[76:79], v[160:163]\n" \
  "s_nop 2\n" \
  "s_waitcnt lgkmcnt(0)\n" \
  "v_mfma_f32_16x16x32_bf16 v[164:167], v[156:159], v[76:79], v[164:167]\n" \
  "s_nop 3\n" \
  "v_mfma_f32_16x16x32_bf16 v[168:171], v[128:131], v[80:83], 0\n" \
  "s_nop 3\n" \
  "v_mfma_f32_16x16x32_bf16 v[172:175], v[144:147], v[80:83], 0\n" \
  "s_nop 3\n" \
  "v_mfma_f32_16x16x32_bf16 v[168:171], v[132:135], v[84:87], v[168:171]\n" \
  "s_nop 3\n" \
  "v_mfma_f32_16x16x32_bf16 v[172:175], v[148:151], v[84:87], v[172:175]\n" \
  "v_cvt_pk_bf16_f32 v176, v160, v161\n" \
  "s_nop 2\n" \
  "v_mfma_f32_16x16x32_bf16 v[168:171], v[136:139], v[88:91], v[168:171]\n" \
  "v_cvt_pk_bf16_f32 v177, v162, v163\n" \
  "s_nop 2\n" \
  "v_mfma_f32_16x16x32_bf16 v[172:175], v[152:155], v[88:91], v[172:175]\n" \
  "v_cvt_pk_bf16_f32 v178, v164, v165\n" \
  "s_nop 2\n" \
  "v_mfma_f32_16x16x32_bf16 v[168:171], v[140:143], v[92:95], v[168:171]\n" \
  "v_cvt_pk_bf16_f32 v179, v166, v167\n" \
  "s_nop 2\n" \
  "v_mfma_f32_16x16x32_bf16 v[172:175], v[156:159], v[92:95], v[172:175]\n" \
  "s_waitcnt vmcnt(5)\n" \
  "global_store_dwordx4 v186, v[176:179], s[46:47] offset:1024 sc1\n" \
  "s_nop 1\n" \
  "v_mfma_f32_16x16x32_bf16 v[160:163], v[128:131], v[96:99], 0\n" \
  "s_nop 3\n" \
  "v_mfma_f32_16x16x32_bf16 v[164:167], v[144:147], v[96:99], 0\n" \
  "s_nop 3\n" \
  "v_mfma_f32_16x16x32_bf16 v[160:163], v[132:135], v[100:103], v[160:163]\n" \
  "s_nop 3\n" \
  "v_mfma_f32_16x16x32_bf16 v[164:167], v[148:151], v[100:103], v[164:167]\n" \
  "v_cvt_pk_bf16_f32 v180, v168, v169\n" \
  "s_nop 2\n" \
  "v_mfma_f32_16x16x32_bf16 v[160:163], v[136:139], v[104:107], v[160:163]\n" \
  "v_cvt_pk_bf16_f32 v181, v170, v171\n" \
  "s_nop 2\n" \
  "v_mfma_f32_16x16x32_bf16 v[164:167], v[152:155], v[104:107], v[164:167]\n" \
  "v_cvt_pk_bf16_f32 v182, v172, v173\n" \
  "s_nop 2\n" \
  "v_mfma_f32_16x16x32_bf16 v[160:163], v[140:143], v[108:111], v[160:163]\n" \
  "v_cvt_pk_bf16_f32 v183, v174, v175\n" \
  "s_nop 2\n" \
  "v_mfma_f32_16x16x32_bf16 v[164:167], v[156:159], v[108:111], v[164:167]\n" \
  "s_waitcnt vmcnt(5)\n" \
  "global_store_dwordx4 v186, v[180:183], s[46:47] offset:1280 sc1\n" \
  "s_nop 1\n" \
  "v_mfma_f32_16x16x32_bf16 v[168:171], v[128:131], v[112:115], 0\n" \
  "s_nop 3\n" \
  "v_mfma_f32_16x16x32_bf16 v[172:175], v[144:147], v[112:115], 0\n" \
  "s_nop 3\n" \
  "v_mfma_f32_16x16x32_bf16 v[168:171], v[132:135], v[116:119], v[168:171]\n" \
  "s_nop 3\n" \
  "v_mfma_f32_16x16x32_bf16 v[172:175], v[148:151], v[116:119], v[172:175]\n" \
  "v_cvt_pk_bf16_f32 v176, v160, v161\n" \
  "s_nop 2\n" \
  "v_mfma_f32_16x16x32_bf16 v[168:171], v[136:139], v[120:123], v[168:171]\n" \
  "v_cvt_pk_bf16_f32 v177, v162, v163\n" \
  "s_nop 2\n" \
  "v_mfma_f32_16x16x32_bf16 v[172:175], v[152:155], v[120:123], v[172:175]\n" \
  "v_cvt_pk_bf16_f32 v178, v164, v165\n" \
  "s_nop 2\n" \
  "v_mfma_f32_16x16x32_bf16 v[168:171], v[140:143], v[124:127], v[168:171]\n" \
  "v_cvt_pk_bf16_f32 v179, v166, v167\n" \
  "s_nop 2\n" \
  "v_mfma_f32_16x16x32_bf16 v[172:175], v[156:159], v[124:127], v[172:175]\n" \
  "s_waitcnt vmcnt(5)\n" \
  "global_store_dwordx4 v187, v[176:179], s[46:47] offset:0 sc1\n" \
  "s_nop 0\n" \
  "v_cvt_pk_bf16_f32 v180, v168, v169\n" \
  "v_cvt_pk_bf16_f32 v181, v170, v171\n" \
  "s_nop 2\n" \
  "v_cvt_pk_bf16_f32 v182, v172, v173\n" \
  "v_cvt_pk_bf16_f32 v183, v174, v175\n" \
  "s_waitcnt vmcnt(5)\n" \
  "global_store_dwordx4 v188, v[180:183], s[46:47] offset:0 sc1\n" \
  "LB_end:\n" \
  "s_setprio 2\n" \
  "s_waitcnt vmcnt(4)\n" \
  "s_mov_b32 s50, 0x10000\n" \
  "LB_p:\n" \
  "s_mov_b64 exec, 0xff\n" \
  "v_cmp_le_u32 vcc, 32, v198\n" \
  "s_mov_b64 exec, -1\n" \
  "s_sub_u32 s50, s50, 1\n" \
  "s_cselect_b32 s35, 0xff, vcc_lo\n" \
  "s_cmp_eq_u32 s35, 0xff\n" \
  "s_cbranch_scc1 LB_p_x\n" \
  "s_cmp_lg_u32 s33, 1021\n" \
  "s_cbranch_scc1 LB_ne\n" \
  "global_load_dwordx4 v[240:243], v238, s[48:49] offset:0 sc1\n" \
  "s_waitcnt vmcnt(0)\n" \
  "global_store_dwordx4 v238, v[240:243], s[48:49] offset:1024 sc1\n" \
  "LB_ne:\n" \
  "s_sleep 2\n" \
  "s_mov_b64 exec, 0xff\n" \
  "global_load_dword v198, v197, s[48:49] offset:2048 sc1\n" \
  "s_mov_b64 exec, -1\n" \
  "s_waitcnt vmcnt(0)\n" \
  "s_branch LB_p\n" \
  "LB_p_x:\n" \
  "s_lshr_b32 s35, s30, 2\n" \
  "s_lshl3_add_u32 s33, s29, s35\n" \
  "s_and_b32 s34, s30, 3\n" \
  "s_lshl_b32 s59, s27, 11\n" \
  "s_mul_i32 s35, s33, 1600000\n" \
  "s_add_u32 s54, s24, s35\n" \
  "s_addc_u32 s55, s25, 0\n" \
  "s_mul_i32 s35, s34, 819200\n" \
  "s_lshl1_add_u32 s35, s59, s35\n" \
  "s_add_u32 s35, s35, 102400000\n" \
  "s_add_u32 s56, s24, s35\n" \
  "s_addc_u32 s57, s25, 0\n" \
  "v_mbcnt_lo_u32_b32 v189, -1, 0\n" \
  "v_mbcnt_hi_u32_b32 v189, -1, v189\n" \
  "v_lshlrev_b32 v188, 4, v189\n" \
  "s_lshl_b32 s35, s28, 12\n" \
  "s_add_u32 s40, s54, s35\n" \
  "s_addc_u32 s41, s55, 0\n" \
  "v_add_u32 v1, s35, v188\n" \
  "v_mov_b32 v2, 1\n" \
  "global_load_dwordx4 v[116:119], v188, s[40:41] offset:0 sc1\n" \
  "global_load_dwordx4 v[120:123], v188, s[40:41] offset:1024 sc1\n" \
  "global_load_dwordx4 v[124:127], v188, s[40:41] offset:2048 sc1\n" \
  "global_load_dwordx4 v[128:131], v188, s[40:41] offset:3072 sc1\n" \
  "s_add_u32 s40, s40, 16000\n" \
  "s_addc_u32 s41, s41, 0\n" \
  "global_load_dwordx4 v[132:135], v188, s[40:41] offset:0 sc1\n" \
  "global_load_dwordx4 v[136:139], v188, s[40:41] offset:1024 sc1\n" \
  "global_load_dwordx4 v[140:143], v188, s[40:41] offset:2048 sc1\n" \
  "global_load_dwordx4 v[144:147], v188, s[40:41] offset:3072 sc1\n" \
  "s_add_u32 s40, s40, 16000\n" \
  "s_addc_u32 s41, s41, 0\n" \
  "global_load_dwordx4 v[148:151], v188, s[40:41] offset:0 sc1\n" \
  "global_load_dwordx4 v[152:155], v188, s[40:41] offset:1024 sc1\n" \
  "global_load_dwordx4 v[156:159], v188, s[40:41] offset:2048 sc1\n" \
  "global_load_dwordx4 v[160:163], v188, s[40:41] offset:3072 sc1\n" \
  "s_add_u32 s40, s40, 16000\n" \
  "s_addc_u32 s41, s41, 0\n" \
  "global_load_dwordx4 v[164:167], v188, s[40:41] offset:0 sc1\n" \
  "global_load_dwordx4 v[168:171], v188, s[40:41] offset:1024 sc1\n" \
  "global_load_dwordx4 v[172:175], v188, s[40:41] offset:2048 sc1\n" \
  "global_load_dwordx4 v[176:179], v188, s[40:41] offset:3072 sc1\n" \
  "s_add_u32 s40, s40, 16000\n" \
  "s_addc_u32 s41, s41, 0\n" \
  "global_load_dwordx4 v[208:211], v188, s[56:57] offset:0 sc1\n" \
  "global_load_dwordx4 v[212:215], v188, s[56:57] offset:1024 sc1\n" \
  "global_load_dwordx4 v[216:219], v188, s[56:57] offset:2048 sc1\n" \
  "global_load_dwordx4 v[220:223], v188, s[56:57] offset:3072 sc1\n" \
  "s_lshr_b32 s35, s33, 2\n" \
  "s_lshl_b32 s35, s35, 6\n" \
  "s_and_b32 s36, s33, 3\n" \
  "s_lshl3_add_u32 s35, s36, s35\n" \
  "v_and_b32 v190, 16, v189\n" \
  "v_lshl_add_u32 v190, v190, 1, s35\n" \
  "global_load_dwordx2 v[204:205], v190, s[16:17]\n" \
  "global_load_dwordx2 v[206:207], v190, s[18:19]\n" \
  "v_lshrrev_b32 v180, 1, v189\n" \
  "v_xor_b32 v180, v180, v189\n" \
  "v_and_b32 v180, 8, v180\n" \
  "v_and_or_b32 v182, v189, 4, v180\n" \
  "v_mov_b32 v183, 0x3f80\n" \
  "v_cmp_eq_u32 vcc, 0, v182\n" \
  "v_and_b32 v192, 3, v189\n" \
  "v_mul_u32_u24 v192, 0x5555, v192\n" \
  "v_cndmask_b32 v180, 0, v183, vcc\n" \
  "v_cmp_eq_u32 vcc, 4, v182\n" \
  "v_add_u32 v189, 16000, v188\n" \
  "v_add_u32 v190, 32000, v188\n" \
  "v_cndmask_b32 v181, 0, v183, vcc\n" \
  "v_add_u32 v191, 48000, v188\n" \
  "v_mov_b64 v[182:183], v[180:181]\n" \
  "s_waitcnt vmcnt(0)\n" \
  "v_lshlrev_b32 v184, 16, v204\n" \
  "v_and_b32 v185, 0xffff0000, v204\n" \
  "v_lshlrev_b32 v186, 16, v205\n" \
  "v_and_b32 v187, 0xffff0000, v205\n" \
  "s_setprio 0\n" \
  "s_barrier\n" \
  "s_cmp_lg_u32 s28, 0\n" \
  "s_cbranch_scc1 LB_nb\n" \
  "s_mov_b32 exec_hi, 0\n" \
  "global_atomic_add v196, v2, s[48:49] offset:2052 sc1\n" \
  "s_mov_b64 exec, -1\n" \
  "LB_nb:\n" \
  "ds_write_b128 v1, v[116:119] offset:0\n" \
  "ds_write_b128 v1, v[120:123] offset:1024\n" \
  "ds_write_b128 v1, v[124:127] offset:2048\n" \
  "ds_write_b128 v1, v[128:131] offset:3072\n" \
  "ds_write_b128 v1, v[132:135] offset:16384\n" \
  "ds_write_b128 v1, v[136:139] offset:17408\n" \
  "ds_write_b128 v1, v[140:143] offset:18432\n" \
  "ds_write_b128 v1, v[144:147] offset:19456\n" \
  "ds_write_b128 v1, v[148:151] offset:32768\n" \
  "ds_write_b128 v1, v[152:155] offset:33792\n" \
  "ds_write_b128 v1, v[156:159] offset:34816\n" \
  "ds_write_b128 v1, v[160:163] offset:35840\n" \
  "ds_write_b128 v1, v[164:167] offset:49152\n" \
  "ds_write_b128 v1, v[168:171] offset:50176\n" \
  "ds_write_b128 v1, v[172:175] offset:51200\n" \
  "ds_write_b128 v1, v[176:179] offset:52224\n" \
  "s_waitcnt lgkmcnt(0)\n" \
  "LG_start:\n" \
  "s_add_u32 s35, s59, 64000\n" \
  "s_add_u32 s54, s54, s35\n" \
  "s_addc_u32 s55, s55, 0\n" \
  "s_add_u32 s56, s56, 32768\n" \
  "s_addc_u32 s57, s57, 0\n" \
  "s_add_u32 s58, s59, 0x10000\n" \
  "s_mov_b32 s51, 7\n" \
  "s_setprio 0\n" \
  "s_barrier\n" \
  "s_mov_b32 m0, s58\n" \
  "s_add_u32 s35, s58, 0x8000\n" \
  "global_load_lds_dwordx4 v188, s[54:55] sc1\n" \
  "global_load_lds_dwordx4 v188, s[54:55] offset:1024 sc1\n" \
  "s_add_u32 m0, s58, 0x4000\n" \
  "s_cmp_lt_u32 s35, 0x28000\n" \
  "global_load_lds_dwordx4 v189, s[54:55] sc1\n" \
  "global_load_lds_dwordx4 v189, s[54:55] offset:1024 sc1\n" \
  "s_cselect_b32 s58, s35, s59\n" \
  "s_add_u32 s54, s54, 32000\n" \
  "s_addc_u32 s55, s55, 0\n" \
  "global_load_dwordx4 v[224:227], v188, s[56:57] sc1\n" \
  "global_load_dwordx4 v[228:231], v188, s[56:57] offset:1024 sc1\n" \
  "global_load_dwordx4 v[232:235], v188, s[56:57] offset:2048 sc1\n" \
  "global_load_dwordx4 v[236:239], v188, s[56:57] offset:3072 sc1\n" \
  "s_add_u32 s56, s56, 32768\n" \
  "s_addc_u32 s57, s57, 0\n" \
  "v_lshlrev_b32_sdwa v64, 4, v208 dst_sel:DWORD dst_unused:UNUSED_PAD src0_sel:DWORD src1_sel:WORD_0\n" \
  "ds_read_b128 v[68:71], v64\n" \
  "v_mov_b64 v[0:1], v[184:185]\n" \
  "v_lshlrev_b32_sdwa v65, 4, v208 dst_sel:DWORD dst_unused:UNUSED_PAD src0_sel:DWORD src1_sel:WORD_1\n" \
  "ds_read_b128 v[72:75], v65\n" \
  "v_mov_b64 v[2:3], v[186:187]\n" \
  "s_barrier\n" \
  "s_mov_b32 m0, s58\n" \
  "s_add_u32 s35, s58, 0x8000\n" \
  "global_load_lds_dwordx4 v188, s[54:55] sc1\n" \
  "global_load_lds_dwordx4 v188, s[54:55] offset:1024 sc1\n" \
  "v_lshlrev_b32_sdwa v66, 4, v209 dst_sel:DWORD dst_unused:UNUSED_PAD src0_sel:DWORD src1_sel:WORD_0\n" \
  "ds_read_b128 v[76:79], v66\n" \
  "v_mov_b64 v[4:5], v[184:185]\n" \
  "v_lshlrev_b32_sdwa v67, 4, v209 dst_sel:DWORD dst_unused:UNUSED_PAD src0_sel:DWORD src1_sel:WORD_1\n" \
  "ds_read_b128 v[80:83], v67\n" \
  "v_mov_b64 v[6:7], v[186:187]\n" \
  "v_lshlrev_b32_sdwa v64, 4, v210 dst_sel:DWORD dst_unused:UNUSED_PAD src0_sel:DWORD src1_sel:WORD_0\n" \
  "ds_read_b128 v[84:87], v64\n" \
  "v_mov_b64 v[8:9], v[184:185]\n" \
  "v_lshlrev_b32_sdwa v65, 4, v210 dst_sel:DWORD dst_unused:UNUSED_PAD src0_sel:DWORD src1_sel:WORD_1\n" \
  "ds_read_b128 v[88:91], v65\n" \
  "v_mov_b64 v[10:11], v[186:187]\n" \
  "v_lshlrev_b32_sdwa v66, 4, v211 dst_sel:DWORD dst_unused:UNUSED_PAD src0_sel:DWORD src1_sel:WORD_0\n" \
  "ds_read_b128 v[92:95], v66\n" \
  "v_mov_b64 v[12:13], v[184:185]\n" \
  "v_lshlrev_b32_sdwa v67, 4, v211 dst_sel:DWORD dst_unused:UNUSED_PAD src0_sel:DWORD src1_sel:WORD_1\n" \
  "ds_read_b128 v[96:99], v67\n" \
  "v_mov_b64 v[14:15], v[186:187]\n" \
  "s_add_u32 m0, s58, 0x4000\n" \
  "s_cmp_lt_u32 s35, 0x28000\n" \
  "global_load_lds_dwordx4 v189, s[54:55] sc1\n" \
  "global_load_lds_dwordx4 v189, s[54:55] offset:1024 sc1\n" \
  "s_cselect_b32 s58, s35, s59\n" \
  "v_lshlrev_b32_sdwa v64, 4, v212 dst_sel:DWORD dst_unused:UNUSED_PAD src0_sel:DWORD src1_sel:WORD_0\n" \
  "ds_read_b128 v[100:103], v64\n" \
  "v_mov_b64 v[16:17], v[184:185]\n" \
  "v_lshlrev_b32_sdwa v65, 4, v212 dst_sel:DWORD dst_unused:UNUSED_PAD src0_sel:DWORD src1_sel:WORD_1\n" \
  "ds_read_b128 v[104:107], v65\n" \
  "v_mov_b64 v[18:19], v[186:187]\n" \
  "v_lshlrev_b32_sdwa v66, 4, v213 dst_sel:DWORD dst_unused:UNUSED_PAD src0_sel:DWORD src1_sel:WORD_0\n" \
  "ds_read_b128 v[108:111], v66\n" \
  "v_mov_b64 v[20:21], v[184:185]\n" \
  "v_lshlrev_b32_sdwa v67, 4, v213 dst_sel:DWORD dst_unused:UNUSED_PAD src0_sel:DWORD src1_sel:WORD_1\n" \
  "ds_read_b128 v[112:115], v67\n" \
  "v_mov_b64 v[22:23], v[186:187]\n" \
  "s_waitcnt lgkmcnt(10)\n" \
  "v_smfmac_f32_16x16x64_bf16 v[0:3], v[180:183], v[68:75], v192\n" \
  "v_lshlrev_b32_sdwa v64, 4, v214 dst_sel:DWORD dst_unused:UNUSED_PAD src0_sel:DWORD src1_sel:WORD_0\n" \
  "ds_read_b128 v[68:71], v64\n" \
  "v_mov_b64 v[24:25], v[184:185]\n" \
  "v_lshlrev_b32_sdwa v65, 4, v214 dst_sel:DWORD dst_unused:UNUSED_PAD src0_sel:DWORD src1_sel:WORD_1\n" \
  "ds_read_b128 v[72:75], v65\n" \
  "v_mov_b64 v[26:27], v[186:187]\n" \
  "s_mov_b32 m0, s58\n" \
  "s_add_u32 s35, s58, 0x8000\n" \
  "global_load_lds_dwordx4 v190, s[54:55] sc1\n" \
  "global_load_lds_dwordx4 v190, s[54:55] offset:1024 sc1\n" \
  "s_waitcnt lgkmcnt(10)\n" \
  "v_smfmac_f32_16x16x64_bf16 v[4:7], v[180:183], v[76:83], v192\n" \
  "v_lshlrev_b32_sdwa v66, 4, v215 dst_sel:DWORD dst_unused:UNUSED_PAD src0_sel:DWORD src1_sel:WORD_0\n" \
  "ds_read_b128 v[76:79], v66\n" \
  "v_mov_b64 v[28:29], v[184:185]\n" \
  "v_lshlrev_b32_sdwa v67, 4, v215 dst_sel:DWORD dst_unused:UNUSED_PAD src0_sel:DWORD src1_sel:WORD_1\n" \
  "ds_read_b128 v[80:83], v67\n" \
  "v_mov_b64 v[30:31], v[186:187]\n" \
  "s_waitcnt lgkmcnt(10)\n" \
  "v_smfmac_f32_16x16x64_bf16 v[8:11], v[180:183], v[84:91], v192\n" \
  "v_lshlrev_b32_sdwa v64, 4, v216 dst_sel:DWORD dst_unused:UNUSED_PAD src0_sel:DWORD src1_sel:WORD_0\n" \
  "ds_read_b128 v[84:87], v64\n" \
  "v_mov_b64 v[32:33], v[184:185]\n" \
  "v_lshlrev_b32_sdwa v65, 4, v216 dst_sel:DWORD dst_unused:UNUSED_PAD src0_sel:DWORD src1_sel:WORD_1\n" \
  "ds_read_b128 v[88:91], v65\n" \
  "v_mov_b64 v[34:35], v[186:187]\n" \
  "s_add_u32 m0, s58, 0x4000\n" \
  "s_cmp_lt_u32 s35, 0x28000\n" \
  "global_load_lds_dwordx4 v191, s[54:55] sc1\n" \
  "global_load_lds_dwordx4 v191, s[54:55] offset:1024 sc1\n" \
  "s_cselect_b32 s58, s35, s59\n" \
  "s_waitcnt lgkmcnt(10)\n" \
  "v_smfmac_f32_16x16x64_bf16 v[12:15], v[180:183], v[92:99], v192\n" \
  "v_lshlrev_b32_sdwa v66, 4, v217 dst_sel:DWORD dst_unused:UNUSED_PAD src0_sel:DWORD src1_sel:WORD_0\n" \
  "ds_read_b128 v[92:95], v66\n" \
  "v_mov_b64 v[36:37], v[184:185]\n" \
  "v_lshlrev_b32_sdwa v67, 4, v217 dst_sel:DWORD dst_unused:UNUSED_PAD src0_sel:DWORD src1_sel:WORD_1\n" \
  "ds_read_b128 v[96:99], v67\n" \
  "v_mov_b64 v[38:39], v[186:187]\n" \
  "s_waitcnt lgkmcnt(10)\n" \
  "v_smfmac_f32_16x16x64_bf16 v[16:19], v[180:183], v[100:107], v192\n" \
  "v_lshlrev_b32_sdwa v64, 4, v218 dst_sel:DWORD dst_unused:UNUSED_PAD src0_sel:DWORD src1_sel:WORD_0\n" \
  "ds_read_b128 v[100:103], v64\n" \
  "v_mov_b64 v[40:41], v[184:185]\n" \
  "v_lshlrev_b32_sdwa v65, 4, v218 dst_sel:DWORD dst_unused:UNUSED_PAD src0_sel:DWORD src1_sel:WORD_1\n" \
  "ds_read_b128 v[104:107], v65\n" \
  "v_mov_b64 v[42:43], v[186:187]\n" \
  "global_load_dwordx4 v[240:243], v188, s[56:57] sc1\n" \
  "global_load_dwordx4 v[244:247], v188, s[56:57] offset:1024 sc1\n" \
  "s_waitcnt lgkmcnt(10)\n" \
  "v_smfmac_f32_16x16x64_bf16 v[20:23], v[180:183], v[108:115], v192\n" \
  "v_lshlrev_b32_sdwa v66, 4, v219 dst_sel:DWORD dst_unused:UNUSED_PAD src0_sel:DWORD src1_sel:WORD_0\n" \
  "ds_read_b128 v[108:111], v66\n" \
  "v_mov_b64 v[44:45], v[184:185]\n" \
  "v_lshlrev_b32_sdwa v67, 4, v219 dst_sel:DWORD dst_unused:UNUSED_PAD src0_sel:DWORD src1_sel:WORD_1\n" \
  "ds_read_b128 v[112:115], v67\n" \
  "v_mov_b64 v[46:47], v[186:187]\n" \
  "s_waitcnt lgkmcnt(10)\n" \
  "v_smfmac_f32_16x16x64_bf16 v[24:27], v[180:183], v[68:75], v192\n" \
  "v_lshlrev_b32_sdwa v64, 4, v220 dst_sel:DWORD dst_unused:UNUSED_PAD src0_sel:DWORD src1_sel:WORD_0\n" \
  "ds_read_b128 v[68:71], v64\n" \
  "v_mov_b64 v[48:49], v[184:185]\n" \
  "v_lshlrev_b32_sdwa v65, 4, v220 dst_sel:DWORD dst_unused:UNUSED_PAD src0_sel:DWORD src1_sel:WORD_1\n" \
  "ds_read_b128 v[72:75], v65\n" \
  "v_mov_b64 v[50:51], v[186:187]\n" \
  "global_load_dwordx4 v[248:251], v188, s[56:57] offset:2048 sc1\n" \
  "global_load_dwordx4 v[252:255], v188, s[56:57] offset:3072 sc1\n" \
  "s_waitcnt lgkmcnt(10)\n" \
  "v_smfmac_f32_16x16x64_bf16 v[28:31], v[180:183], v[76:83], v192\n" \
  "v_lshlrev_b32_sdwa v66, 4, v221 dst_sel:DWORD dst_unused:UNUSED_PAD src0_sel:DWORD src1_sel:WORD_0\n" \
  "ds_read_b128 v[76:79], v66\n" \
  "v_mov_b64 v[52:53], v[184:185]\n" \
  "v_lshlrev_b32_sdwa v67, 4, v221 dst_sel:DWORD dst_unused:UNUSED_PAD src0_sel:DWORD src1_sel:WORD_1\n" \
  "ds_read_b128 v[80:83], v67\n" \
  "v_mov_b64 v[54:55], v[186:187]\n" \
  "s_waitcnt lgkmcnt(10)\n" \
  "v_smfmac_f32_16x16x64_bf16 v[32:35], v[180:183], v[84:91], v192\n" \
  "v_lshlrev_b32_sdwa v64, 4, v222 dst_sel:DWORD dst_unused:UNUSED_PAD src0_sel:DWORD src1_sel:WORD_0\n" \
  "ds_read_b128 v[84:87], v64\n" \
  "v_mov_b64 v[56:57], v[184:185]\n" \
  "v_lshlrev_b32_sdwa v65, 4, v222 dst_sel:DWORD dst_unused:UNUSED_PAD src0_sel:DWORD src1_sel:WORD_1\n" \
  "ds_read_b128 v[88:91], v65\n" \
  "v_mov_b64 v[58:59], v[186:187]\n" \
  "s_waitcnt lgkmcnt(10)\n" \
  "v_smfmac_f32_16x16x64_bf16 v[36:39], v[180:183], v[92:99], v192\n" \
  "v_lshlrev_b32_sdwa v66, 4, v223 dst_sel:DWORD dst_unused:UNUSED_PAD src0_sel:DWORD src1_sel:WORD_0\n" \
  "ds_read_b128 v[92:95], v66\n" \
  "v_mov_b64 v[60:61], v[184:185]\n" \
  "v_lshlrev_b32_sdwa v67, 4, v223 dst_sel:DWORD dst_unused:UNUSED_PAD src0_sel:DWORD src1_sel:WORD_1\n" \
  "ds_read_b128 v[96:99], v67\n" \
  "v_mov_b64 v[62:63], v[186:187]\n" \
  "s_add_u32 s56, s56, 32768\n" \
  "s_addc_u32 s57, s57, 0\n" \
  "s_add_u32 s54, s54, 64000\n" \
  "s_addc_u32 s55, s55, 0\n" \
  "s_waitcnt lgkmcnt(10)\n" \
  "v_smfmac_f32_16x16x64_bf16 v[40:43], v[180:183], v[100:107], v192\n" \
  "s_waitcnt lgkmcnt(8)\n" \
  "v_smfmac_f32_16x16x64_bf16 v[44:47], v[180:183], v[108:115], v192\n" \
  "s_waitcnt lgkmcnt(6)\n" \
  "v_smfmac_f32_16x16x64_bf16 v[48:51], v[180:183], v[68:75], v192\n" \
  "s_waitcnt lgkmcnt(4)\n" \
  "v_smfmac_f32_16x16x64_bf16 v[52:55], v[180:183], v[76:83], v192\n" \
  "s_waitcnt vmcnt(8)\n" \
  "s_barrier\n" \
  "LG_loop:\n" \
  "v_lshlrev_b32_sdwa v64, 4, v224 dst_sel:DWORD dst_unused:UNUSED_PAD src0_sel:DWORD src1_sel:WORD_0\n" \
  "ds_read_b128 v[100:103], v64\n" \
  "v_lshlrev_b32_sdwa v65, 4, v224 dst_sel:DWORD dst_unused:UNUSED_PAD src0_sel:DWORD src1_sel:WORD_1\n" \
  "ds_read_b128 v[104:107], v65\n" \
  "s_waitcnt lgkmcnt(4)\n" \
  "v_smfmac_f32_16x16x64_bf16 v[56:59], v[180:183], v[84:91], v192\n" \
  "s_waitcnt lgkmcnt(2)\n" \
  "v_smfmac_f32_16x16x64_bf16 v[60:63], v[180:183], v[92:99], v192\n" \
  "s_barrier\n" \
  "s_mov_b32 m0, s58\n" \
  "s_add_u32 s35, s58, 0x8000\n" \
  "global_load_lds_dwordx4 v188, s[54:55] sc1\n" \
  "global_load_lds_dwordx4 v188, s[54:55] offset:1024 sc1\n" \
  "v_lshlrev_b32_sdwa v66, 4, v225 dst_sel:DWORD dst_unused:UNUSED_PAD src0_sel:DWORD src1_sel:WORD_0\n" \
  "ds_read_b128 v[108:111], v66\n" \
  "v_lshlrev_b32_sdwa v67, 4, v225 dst_sel:DWORD dst_unused:UNUSED_PAD src0_sel:DWORD src1_sel:WORD_1\n" \
  "ds_read_b128 v[112:115], v67\n" \
  "v_lshlrev_b32_sdwa v64, 4, v226 dst_sel:DWORD dst_unused:UNUSED_PAD src0_sel:DWORD src1_sel:WORD_0\n" \
  "ds_read_b128 v[68:71], v64\n" \
  "v_lshlrev_b32_sdwa v65, 4, v226 dst_sel:DWORD dst_unused:UNUSED_PAD src0_sel:DWORD src1_sel:WORD_1\n" \
  "ds_read_b128 v[72:75], v65\n" \
  "v_lshlrev_b32_sdwa v66, 4, v227 dst_sel:DWORD dst_unused:UNUSED_PAD src0_sel:DWORD src1_sel:WORD_0\n" \
  "ds_read_b128 v[76:79], v66\n" \
  "v_lshlrev_b32_sdwa v67, 4, v227 dst_sel:DWORD dst_unused:UNUSED_PAD src0_sel:DWORD src1_sel:WORD_1\n" \
  "ds_read_b128 v[80:83], v67\n" \
  "s_add_u32 m0, s58, 0x4000\n" \
  "s_cmp_lt_u32 s35, 0x28000\n" \
  "global_load_lds_dwordx4 v189, s[54:55] sc1\n" \
  "global_load_lds_dwordx4 v189, s[54:55] offset:1024 sc1\n" \
  "s_cselect_b32 s58, s35, s59\n" \
  "v_lshlrev_b32_sdwa v64, 4, v228 dst_sel:DWORD dst_unused:UNUSED_PAD src0_sel:DWORD src1_sel:WORD_0\n" \
  "ds_read_b128 v[84:87], v64\n" \
  "v_lshlrev_b32_sdwa v65, 4, v228 dst_sel:DWORD dst_unused:UNUSED_PAD src0_sel:DWORD src1_sel:WORD_1\n" \
  "ds_read_b128 v[88:91], v65\n" \
  "v_lshlrev_b32_sdwa v66, 4, v229 dst_sel:DWORD dst_unused:UNUSED_PAD src0_sel:DWORD src1_sel:WORD_0\n" \
  "ds_read_b128 v[92:95], v66\n" \
  "v_lshlrev_b32_sdwa v67, 4, v229 dst_sel:DWORD dst_unused:UNUSED_PAD src0_sel:DWORD src1_sel:WORD_1\n" \
  "ds_read_b128 v[96:99], v67\n" \
  "s_waitcnt lgkmcnt(10)\n" \
  "v_smfmac_f32_16x16x64_bf16 v[0:3], v[180:183], v[100:107], v192\n" \
  "v_lshlrev_b32_sdwa v64, 4, v230 dst_sel:DWORD dst_unused:UNUSED_PAD src0_sel:DWORD src1_sel:WORD_0\n" \
  "ds_read_b128 v[100:103], v64\n" \
  "v_lshlrev_b32_sdwa v65, 4, v230 dst_sel:DWORD dst_unused:UNUSED_PAD src0_sel:DWORD src1_sel:WORD_1\n" \
  "ds_read_b128 v[104:107], v65\n" \
  "s_mov_b32 m0, s58\n" \
  "s_add_u32 s35, s58, 0x8000\n" \
  "global_load_lds_dwordx4 v190, s[54:55] sc1\n" \
  "global_load_lds_dwordx4 v190, s[54:55] offset:1024 sc1\n" \
  "s_waitcnt lgkmcnt(10)\n" \
  "v_smfmac_f32_16x16x64_bf16 v[4:7], v[180:183], v[108:115], v192\n" \
  "v_lshlrev_b32_sdwa v66, 4, v231 dst_sel:DWORD dst_unused:UNUSED_PAD src0_sel:DWORD src1_sel:WORD_0\n" \
  "ds_read_b128 v[108:111], v66\n" \
  "v_lshlrev_b32_sdwa v67, 4, v231 dst_sel:DWORD dst_unused:UNUSED_PAD src0_sel:DWORD src1_sel:WORD_1\n" \
  "ds_read_b128 v[112:115], v67\n" \
  "s_waitcnt lgkmcnt(10)\n" \
  "v_smfmac_f32_16x16x64_bf16 v[8:11], v[180:183], v[68:75], v192\n" \
  "v_lshlrev_b32_sdwa v64, 4, v232 dst_sel:DWORD dst_unused:UNUSED_PAD src0_sel:DWORD src1_sel:WORD_0\n" \
  "ds_read_b128 v[68:71], v64\n" \
  "v_lshlrev_b32_sdwa v65, 4, v232 dst_sel:DWORD dst_unused:UNUSED_PAD src0_sel:DWORD src1_sel:WORD_1\n" \
  "ds_read_b128 v[72:75], v65\n" \
  "s_add_u32 m0, s58, 0x4000\n" \
  "s_cmp_lt_u32 s35, 0x28000\n" \
  "global_load_lds_dwordx4 v191, s[54:55] sc1\n" \
  "global_load_lds_dwordx4 v191, s[54:55] offset:1024 sc1\n" \
  "s_cselect_b32 s58, s35, s59\n" \
  "s_waitcnt lgkmcnt(10)\n" \
  "v_smfmac_f32_16x16x64_bf16 v[12:15], v[180:183], v[76:83], v192\n" \
  "v_lshlrev_b32_sdwa v66, 4, v233 dst_sel:DWORD dst_unused:UNUSED_PAD src0_sel:DWORD src1_sel:WORD_0\n" \
  "ds_read_b128 v[76:79], v66\n" \
  "v_lshlrev_b32_sdwa v67, 4, v233 dst_sel:DWORD dst_unused:UNUSED_PAD src0_sel:DWORD src1_sel:WORD_1\n" \
  "ds_read_b128 v[80:83], v67\n" \
  "s_waitcnt lgkmcnt(10)\n" \
  "v_smfmac_f32_16x16x64_bf16 v[16:19], v[180:183], v[84:91], v192\n" \
  "v_lshlrev_b32_sdwa v64, 4, v234 dst_sel:DWORD dst_unused:UNUSED_PAD src0_sel:DWORD src1_sel:WORD_0\n" \
  "ds_read_b128 v[84:87], v64\n" \
  "v_lshlrev_b32_sdwa v65, 4, v234 dst_sel:DWORD dst_unused:UNUSED_PAD src0_sel:DWORD src1_sel:WORD_1\n" \
  "ds_read_b128 v[88:91], v65\n" \
  "global_load_dwordx4 v[208:211], v188, s[56:57] sc1\n" \
  "global_load_dwordx4 v[212:215], v188, s[56:57] offset:1024 sc1\n" \
  "s_waitcnt lgkmcnt(10)\n" \
  "v_smfmac_f32_16x16x64_bf16 v[20:23], v[180:183], v[92:99], v192\n" \
  "v_lshlrev_b32_sdwa v66, 4, v235 dst_sel:DWORD dst_unused:UNUSED_PAD src0_sel:DWORD src1_sel:WORD_0\n" \
  "ds_read_b128 v[92:95], v66\n" \
  "v_lshlrev_b32_sdwa v67, 4, v235 dst_sel:DWORD dst_unused:UNUSED_PAD src0_sel:DWORD src1_sel:WORD_1\n" \
  "ds_read_b128 v[96:99], v67\n" \
  "s_waitcnt lgkmcnt(10)\n" \
  "v_smfmac_f32_16x16x64_bf16 v[24:27], v[180:183], v[100:107], v192\n" \
  "v_lshlrev_b32_sdwa v64, 4, v236 dst_sel:DWORD dst_unused:UNUSED_PAD src0_sel:DWORD src1_sel:WORD_0\n" \
  "ds_read_b128 v[100:103], v64\n" \
  "v_lshlrev_b32_sdwa v65, 4, v236 dst_sel:DWORD dst_unused:UNUSED_PAD src0_sel:DWORD src1_sel:WORD_1\n" \
  "ds_read_b128 v[104:107], v65\n" \
  "global_load_dwordx4 v[216:219], v188, s[56:57] offset:2048 sc1\n" \
  "global_load_dwordx4 v[220:223], v188, s[56:57] offset:3072 sc1\n" \
  "s_waitcnt lgkmcnt(10)\n" \
  "v_smfmac_f32_16x16x64_bf16 v[28:31], v[180:183], v[108:115], v192\n" \
  "v_lshlrev_b32_sdwa v66, 4, v237 dst_sel:DWORD dst_unused:UNUSED_PAD src0_sel:DWORD src1_sel:WORD_0\n" \
  "ds_read_b128 v[108:111], v66\n" \
  "v_lshlrev_b32_sdwa v67, 4, v237 dst_sel:DWORD dst_unused:UNUSED_PAD src0_sel:DWORD src1_sel:WORD_1\n" \
  "ds_read_b128 v[112:115], v67\n" \
  "s_waitcnt lgkmcnt(10)\n" \
  "v_smfmac_f32_16x16x64_bf16 v[32:35], v[180:183], v[68:75], v192\n" \
  "v_lshlrev_b32_sdwa v64, 4, v238 dst_sel:DWORD dst_unused:UNUSED_PAD src0_sel:DWORD src1_sel:WORD_0\n" \
  "ds_read_b128 v[68:71], v64\n" \
  "v_lshlrev_b32_sdwa v65, 4, v238 dst_sel:DWORD dst_unused:UNUSED_PAD src0_sel:DWORD src1_sel:WORD_1\n" \
  "ds_read_b128 v[72:75], v65\n" \
  "s_waitcnt lgkmcnt(10)\n" \
  "v_smfmac_f32_16x16x64_bf16 v[36:39], v[180:183], v[76:83], v192\n" \
  "v_lshlrev_b32_sdwa v66, 4, v239 dst_sel:DWORD dst_unused:UNUSED_PAD src0_sel:DWORD src1_sel:WORD_0\n" \
  "ds_read_b128 v[76:79], v66\n" \
  "v_lshlrev_b32_sdwa v67, 4, v239 dst_sel:DWORD dst_unused:UNUSED_PAD src0_sel:DWORD src1_sel:WORD_1\n" \
  "ds_read_b128 v[80:83], v67\n" \
  "s_add_u32 s56, s56, 32768\n" \
  "s_addc_u32 s57, s57, 0\n" \
  "s_add_u32 s54, s54, 64000\n" \
  "s_addc_u32 s55, s55, 0\n" \
  "s_waitcnt lgkmcnt(10)\n" \
  "v_smfmac_f32_16x16x64_bf16 v[40:43], v[180:183], v[84:91], v192\n" \
  "s_waitcnt lgkmcnt(8)\n" \
  "v_smfmac_f32_16x16x64_bf16 v[44:47], v[180:183], v[92:99], v192\n" \
  "s_waitcnt lgkmcnt(6)\n" \
  "v_smfmac_f32_16x16x64_bf16 v[48:51], v[180:183], v[100:107], v192\n" \
  "s_waitcnt lgkmcnt(4)\n" \
  "v_smfmac_f32_16x16x64_bf16 v[52:55], v[180:183], v[108:115], v192\n" \
  "s_waitcnt vmcnt(8)\n" \
  "s_barrier\n" \
  "v_lshlrev_b32_sdwa v64, 4, v240 dst_sel:DWORD dst_unused:UNUSED_PAD src0_sel:DWORD src1_sel:WORD_0\n" \
  "ds_read_b128 v[84:87], v64\n" \
  "v_lshlrev_b32_sdwa v65, 4, v240 dst_sel:DWORD dst_unused:UNUSED_PAD src0_sel:DWORD src1_sel:WORD_1\n" \
  "ds_read_b128 v[88:91], v65\n" \
  "s_waitcnt lgkmcnt(4)\n" \
  "v_smfmac_f32_16x16x64_bf16 v[56:59], v[180:183], v[68:75], v192\n" \
  "s_waitcnt lgkmcnt(2)\n" \
  "v_smfmac_f32_16x16x64_bf16 v[60:63], v[180:183], v[76:83], v192\n" \
  "s_barrier\n" \
  "s_mov_b32 m0, s58\n" \
  "s_add_u32 s35, s58, 0x8000\n" \
  "global_load_lds_dwordx4 v188, s[54:55] sc1\n" \
  "global_load_lds_dwordx4 v188, s[54:55] offset:1024 sc1\n" \
  "v_lshlrev_b32_sdwa v66, 4, v241 dst_sel:DWORD dst_unused:UNUSED_PAD src0_sel:DWORD src1_sel:WORD_0\n" \
  "ds_read_b128 v[92:95], v66\n" \
  "v_lshlrev_b32_sdwa v67, 4, v241 dst_sel:DWORD dst_unused:UNUSED_PAD src0_sel:DWORD src1_sel:WORD_1\n" \
  "ds_read_b128 v[96:99], v67\n" \
  "v_lshlrev_b32_sdwa v64, 4, v242 dst_sel:DWORD dst_unused:UNUSED_PAD src0_sel:DWORD src1_sel:WORD_0\n" \
  "ds_read_b128 v[100:103], v64\n" \
  "v_lshlrev_b32_sdwa v65, 4, v242 dst_sel:DWORD dst_unused:UNUSED_PAD src0_sel:DWORD src1_sel:WORD_1\n" \
  "ds_read_b128 v[104:107], v65\n" \
  "v_lshlrev_b32_sdwa v66, 4, v243 dst_sel:DWORD dst_unused:UNUSED_PAD src0_sel:DWORD src1_sel:WORD_0\n" \
  "ds_read_b128 v[108:111], v66\n" \
  "v_lshlrev_b32_sdwa v67, 4, v243 dst_sel:DWORD dst_unused:UNUSED_PAD src0_sel:DWORD src1_sel:WORD_1\n" \
  "ds_read_b128 v[112:115], v67\n" \
  "s_add_u32 m0, s58, 0x4000\n" \
  "s_cmp_lt_u32 s35, 0x28000\n" \
  "global_load_lds_dwordx4 v189, s[54:55] sc1\n" \
  "global_load_lds_dwordx4 v189, s[54:55] offset:1024 sc1\n" \
  "s_cselect_b32 s58, s35, s59\n" \
  "v_lshlrev_b32_sdwa v64, 4, v244 dst_sel:DWORD dst_unused:UNUSED_PAD src0_sel:DWORD src1_sel:WORD_0\n" \
  "ds_read_b128 v[68:71], v64\n" \
  "v_lshlrev_b32_sdwa v65, 4, v244 dst_sel:DWORD dst_unused:UNUSED_PAD src0_sel:DWORD src1_sel:WORD_1\n" \
  "ds_read_b128 v[72:75], v65\n" \
  "v_lshlrev_b32_sdwa v66, 4, v245 dst_sel:DWORD dst_unused:UNUSED_PAD src0_sel:DWORD src1_sel:WORD_0\n" \
  "ds_read_b128 v[76:79], v66\n" \
  "v_lshlrev_b32_sdwa v67, 4, v245 dst_sel:DWORD dst_unused:UNUSED_PAD src0_sel:DWORD src1_sel:WORD_1\n" \
  "ds_read_b128 v[80:83], v67\n" \
  "s_waitcnt lgkmcnt(10)\n" \
  "v_smfmac_f32_16x16x64_bf16 v[0:3], v[180:183], v[84:91], v192\n" \
  "v_lshlrev_b32_sdwa v64, 4, v246 dst_sel:DWORD dst_unused:UNUSED_PAD src0_sel:DWORD src1_sel:WORD_0\n" \
  "ds_read_b128 v[84:87], v64\n" \
  "v_lshlrev_b32_sdwa v65, 4, v246 dst_sel:DWORD dst_unused:UNUSED_PAD src0_sel:DWORD src1_sel:WORD_1\n" \
  "ds_read_b128 v[88:91], v65\n" \
  "s_mov_b32 m0, s58\n" \
  "s_add_u32 s35, s58, 0x8000\n" \
  "global_load_lds_dwordx4 v190, s[54:55] sc1\n" \
  "global_load_lds_dwordx4 v190, s[54:55] offset:1024 sc1\n" \
  "s_waitcnt lgkmcnt(10)\n" \
  "v_smfmac_f32_16x16x64_bf16 v[4:7], v[180:183], v[92:99], v192\n" \
  "v_lshlrev_b32_sdwa v66, 4, v247 dst_sel:DWORD dst_unused:UNUSED_PAD src0_sel:DWORD src1_sel:WORD_0\n" \
  "ds_read_b128 v[92:95], v66\n" \
  "v_lshlrev_b32_sdwa v67, 4, v247 dst_sel:DWORD dst_unused:UNUSED_PAD src0_sel:DWORD src1_sel:WORD_1\n" \
  "ds_read_b128 v[96:99], v67\n" \
  "s_waitcnt lgkmcnt(10)\n" \
  "v_smfmac_f32_16x16x64_bf16 v[8:11], v[180:183], v[100:107], v192\n" \
  "v_lshlrev_b32_sdwa v64, 4, v248 dst_sel:DWORD dst_unused:UNUSED_PAD src0_sel:DWORD src1_sel:WORD_0\n" \
  "ds_read_b128 v[100:103], v64\n" \
  "v_lshlrev_b32_sdwa v65, 4, v248 dst_sel:DWORD dst_unused:UNUSED_PAD src0_sel:DWORD src1_sel:WORD_1\n" \
  "ds_read_b128 v[104:107], v65\n" \
  "s_add_u32 m0, s58, 0x4000\n" \
  "s_cmp_lt_u32 s35, 0x28000\n" \
  "global_load_lds_dwordx4 v191, s[54:55] sc1\n" \
  "global_load_lds_dwordx4 v191, s[54:55] offset:1024 sc1\n" \
  "s_cselect_b32 s58, s35, s59\n" \
  "s_waitcnt lgkmcnt(10)\n" \
  "v_smfmac_f32_16x16x64_bf16 v[12:15], v[180:183], v[108:115], v192\n" \
  "v_lshlrev_b32_sdwa v66, 4, v249 dst_sel:DWORD dst_unused:UNUSED_PAD src0_sel:DWORD src1_sel:WORD_0\n" \
  "ds_read_b128 v[108:111], v66\n" \
  "v_lshlrev_b32_sdwa v67, 4, v249 dst_sel:DWORD dst_unused:UNUSED_PAD src0_sel:DWORD src1_sel:WORD_1\n" \
  "ds_read_b128 v[112:115], v67\n" \
  "s_waitcnt lgkmcnt(10)\n" \
  "v_smfmac_f32_16x16x64_bf16 v[16:19], v[180:183], v[68:75], v192\n" \
  "v_lshlrev_b32_sdwa v64, 4, v250 dst_sel:DWORD dst_unused:UNUSED_PAD src0_sel:DWORD src1_sel:WORD_0\n" \
  "ds_read_b128 v[68:71], v64\n" \
  "v_lshlrev_b32_sdwa v65, 4, v250 dst_sel:DWORD dst_unused:UNUSED_PAD src0_sel:DWORD src1_sel:WORD_1\n" \
  "ds_read_b128 v[72:75], v65\n" \
  "global_load_dwordx4 v[224:227], v188, s[56:57] sc1\n" \
  "global_load_dwordx4 v[228:231], v188, s[56:57] offset:1024 sc1\n" \
  "s_waitcnt lgkmcnt(10)\n" \
  "v_smfmac_f32_16x16x64_bf16 v[20:23], v[180:183], v[76:83], v192\n" \
  "v_lshlrev_b32_sdwa v66, 4, v251 dst_sel:DWORD dst_unused:UNUSED_PAD src0_sel:DWORD src1_sel:WORD_0\n" \
  "ds_read_b128 v[76:79], v66\n" \
  "v_lshlrev_b32_sdwa v67, 4, v251 dst_sel:DWORD dst_unused:UNUSED_PAD src0_sel:DWORD src1_sel:WORD_1\n" \
  "ds_read_b128 v[80:83], v67\n" \
  "s_waitcnt lgkmcnt(10)\n" \
  "v_smfmac_f32_16x16x64_bf16 v[24:27], v[180:183], v[84:91], v192\n" \
  "v_lshlrev_b32_sdwa v64, 4, v252 dst_sel:DWORD dst_unused:UNUSED_PAD src0_sel:DWORD src1_sel:WORD_0\n" \
  "ds_read_b128 v[84:87], v64\n" \
  "v_lshlrev_b32_sdwa v65, 4, v252 dst_sel:DWORD dst_unused:UNUSED_PAD src0_sel:DWORD src1_sel:WORD_1\n" \
  "ds_read_b128 v[88:91], v65\n" \
  "global_load_dwordx4 v[232:235], v188, s[56:57] offset:2048 sc1\n" \
  "global_load_dwordx4 v[236:239], v188, s[56:57] offset:3072 sc1\n" \
  "s_waitcnt lgkmcnt(10)\n" \
  "v_smfmac_f32_16x16x64_bf16 v[28:31], v[180:183], v[92:99], v192\n" \
  "v_lshlrev_b32_sdwa v66, 4, v253 dst_sel:DWORD dst_unused:UNUSED_PAD src0_sel:DWORD src1_sel:WORD_0\n" \
  "ds_read_b128 v[92:95], v66\n" \
  "v_lshlrev_b32_sdwa v67, 4, v253 dst_sel:DWORD dst_unused:UNUSED_PAD src0_sel:DWORD src1_sel:WORD_1\n" \
  "ds_read_b128 v[96:99], v67\n" \
  "s_waitcnt lgkmcnt(10)\n" \
  "v_smfmac_f32_16x16x64_bf16 v[32:35], v[180:183], v[100:107], v192\n" \
  "v_lshlrev_b32_sdwa v64, 4, v254 dst_sel:DWORD dst_unused:UNUSED_PAD src0_sel:DWORD src1_sel:WORD_0\n" \
  "ds_read_b128 v[100:103], v64\n" \
  "v_lshlrev_b32_sdwa v65, 4, v254 dst_sel:DWORD dst_unused:UNUSED_PAD src0_sel:DWORD src1_sel:WORD_1\n" \
  "ds_read_b128 v[104:107], v65\n" \
  "s_waitcnt lgkmcnt(10)\n" \
  "v_smfmac_f32_16x16x64_bf16 v[36:39], v[180:183], v[108:115], v192\n" \
  "v_lshlrev_b32_sdwa v66, 4, v255 dst_sel:DWORD dst_unused:UNUSED_PAD src0_sel:DWORD src1_sel:WORD_0\n" \
  "ds_read_b128 v[108:111], v66\n" \
  "v_lshlrev_b32_sdwa v67, 4, v255 dst_sel:DWORD dst_unused:UNUSED_PAD src0_sel:DWORD src1_sel:WORD_1\n" \
  "ds_read_b128 v[112:115], v67\n" \
  "s_add_u32 s56, s56, 32768\n" \
  "s_addc_u32 s57, s57, 0\n" \
  "s_add_u32 s54, s54, 64000\n" \
  "s_addc_u32 s55, s55, 0\n" \
  "s_waitcnt lgkmcnt(10)\n" \
  "v_smfmac_f32_16x16x64_bf16 v[40:43], v[180:183], v[68:75], v192\n" \
  "s_waitcnt lgkmcnt(8)\n" \
  "v_smfmac_f32_16x16x64_bf16 v[44:47], v[180:183], v[76:83], v192\n" \
  "s_waitcnt lgkmcnt(6)\n" \
  "v_smfmac_f32_16x16x64_bf16 v[48:51], v[180:183], v[84:91], v192\n" \
  "s_waitcnt lgkmcnt(4)\n" \
  "v_smfmac_f32_16x16x64_bf16 v[52:55], v[180:183], v[92:99], v192\n" \
  "s_waitcnt vmcnt(8)\n" \
  "s_barrier\n" \
  "v_lshlrev_b32_sdwa v64, 4, v208 dst_sel:DWORD dst_unused:UNUSED_PAD src0_sel:DWORD src1_sel:WORD_0\n" \
  "ds_read_b128 v[68:71], v64\n" \
  "v_lshlrev_b32_sdwa v65, 4, v208 dst_sel:DWORD dst_unused:UNUSED_PAD src0_sel:DWORD src1_sel:WORD_1\n" \
  "ds_read_b128 v[72:75], v65\n" \
  "s_waitcnt lgkmcnt(4)\n" \
  "v_smfmac_f32_16x16x64_bf16 v[56:59], v[180:183], v[100:107], v192\n" \
  "s_waitcnt lgkmcnt(2)\n" \
  "v_smfmac_f32_16x16x64_bf16 v[60:63], v[180:183], v[108:115], v192\n" \
  "s_barrier\n" \
  "s_mov_b32 m0, s58\n" \
  "s_add_u32 s35, s58, 0x8000\n" \
  "global_load_lds_dwordx4 v188, s[54:55] sc1\n" \
  "global_load_lds_dwordx4 v188, s[54:55] offset:1024 sc1\n" \
  "v_lshlrev_b32_sdwa v66, 4, v209 dst_sel:DWORD dst_unused:UNUSED_PAD src0_sel:DWORD src1_sel:WORD_0\n" \
  "ds_read_b128 v[76:79], v66\n" \
  "v_lshlrev_b32_sdwa v67, 4, v209 dst_sel:DWORD dst_unused:UNUSED_PAD src0_sel:DWORD src1_sel:WORD_1\n" \
  "ds_read_b128 v[80:83], v67\n" \
  "v_lshlrev_b32_sdwa v64, 4, v210 dst_sel:DWORD dst_unused:UNUSED_PAD src0_sel:DWORD src1_sel:WORD_0\n" \
  "ds_read_b128 v[84:87], v64\n" \
  "v_lshlrev_b32_sdwa v65, 4, v210 dst_sel:DWORD dst_unused:UNUSED_PAD src0_sel:DWORD src1_sel:WORD_1\n" \
  "ds_read_b128 v[88:91], v65\n" \
  "v_lshlrev_b32_sdwa v66, 4, v211 dst_sel:DWORD dst_unused:UNUSED_PAD src0_sel:DWORD src1_sel:WORD_0\n" \
  "ds_read_b128 v[92:95], v66\n" \
  "v_lshlrev_b32_sdwa v67, 4, v211 dst_sel:DWORD dst_unused:UNUSED_PAD src0_sel:DWORD src1_sel:WORD_1\n" \
  "ds_read_b128 v[96:99], v67\n" \
  "s_add_u32 m0, s58, 0x4000\n" \
  "s_cmp_lt_u32 s35, 0x28000\n" \
  "global_load_lds_dwordx4 v189, s[54:55] sc1\n" \
  "global_load_lds_dwordx4 v189, s[54:55] offset:1024 sc1\n" \
  "s_cselect_b32 s58, s35, s59\n" \
  "v_lshlrev_b32_sdwa v64, 4, v212 dst_sel:DWORD dst_unused:UNUSED_PAD src0_sel:DWORD src1_sel:WORD_0\n" \
  "ds_read_b128 v[100:103], v64\n" \
  "v_lshlrev_b32_sdwa v65, 4, v212 dst_sel:DWORD dst_unused:UNUSED_PAD src0_sel:DWORD src1_sel:WORD_1\n" \
  "ds_read_b128 v[104:107], v65\n" \
  "v_lshlrev_b32_sdwa v66, 4, v213 dst_sel:DWORD dst_unused:UNUSED_PAD src0_sel:DWORD src1_sel:WORD_0\n" \
  "ds_read_b128 v[108:111], v66\n" \
  "v_lshlrev_b32_sdwa v67, 4, v213 dst_sel:DWORD dst_unused:UNUSED_PAD src0_sel:DWORD src1_sel:WORD_1\n" \
  "ds_read_b128 v[112:115], v67\n" \
  "s_waitcnt lgkmcnt(10)\n" \
  "v_smfmac_f32_16x16x64_bf16 v[0:3], v[180:183], v[68:75], v192\n" \
  "v_lshlrev_b32_sdwa v64, 4, v214 dst_sel:DWORD dst_unused:UNUSED_PAD src0_sel:DWORD src1_sel:WORD_0\n" \
  "ds_read_b128 v[68:71], v64\n" \
  "v_lshlrev_b32_sdwa v65, 4, v214 dst_sel:DWORD dst_unused:UNUSED_PAD src0_sel:DWORD src1_sel:WORD_1\n" \
  "ds_read_b128 v[72:75], v65\n" \
  "s_mov_b32 m0, s58\n" \
  "s_add_u32 s35, s58, 0x8000\n" \
  "global_load_lds_dwordx4 v190, s[54:55] sc1\n" \
  "global_load_lds_dwordx4 v190, s[54:55] offset:1024 sc1\n" \
  "s_waitcnt lgkmcnt(10)\n" \
  "v_smfmac_f32_16x16x64_bf16 v[4:7], v[180:183], v[76:83], v192\n" \
  "v_lshlrev_b32_sdwa v66, 4, v215 dst_sel:DWORD dst_unused:UNUSED_PAD src0_sel:DWORD src1_sel:WORD_0\n" \
  "ds_read_b128 v[76:79], v66\n" \
  "v_lshlrev_b32_sdwa v67, 4, v215 dst_sel:DWORD dst_unused:UNUSED_PAD src0_sel:DWORD src1_sel:WORD_1\n" \
  "ds_read_b128 v[80:83], v67\n" \
  "s_waitcnt lgkmcnt(10)\n" \
  "v_smfmac_f32_16x16x64_bf16 v[8:11], v[180:183], v[84:91], v192\n" \
  "v_lshlrev_b32_sdwa v64, 4, v216 dst_sel:DWORD dst_unused:UNUSED_PAD src0_sel:DWORD src1_sel:WORD_0\n" \
  "ds_read_b128 v[84:87], v64\n" \
  "v_lshlrev_b32_sdwa v65, 4, v216 dst_sel:DWORD dst_unused:UNUSED_PAD src0_sel:DWORD src1_sel:WORD_1\n" \
  "ds_read_b128 v[88:91], v65\n" \
  "s_add_u32 m0, s58, 0x4000\n" \
  "s_cmp_lt_u32 s35, 0x28000\n" \
  "global_load_lds_dwordx4 v191, s[54:55] sc1\n" \
  "global_load_lds_dwordx4 v191, s[54:55] offset:1024 sc1\n" \
  "s_cselect_b32 s58, s35, s59\n" \
  "s_waitcnt lgkmcnt(10)\n" \
  "v_smfmac_f32_16x16x64_bf16 v[12:15], v[180:183], v[92:99], v192\n" \
  "v_lshlrev_b32_sdwa v66, 4, v217 dst_sel:DWORD dst_unused:UNUSED_PAD src0_sel:DWORD src1_sel:WORD_0\n" \
  "ds_read_b128 v[92:95], v66\n" \
  "v_lshlrev_b32_sdwa v67, 4, v217 dst_sel:DWORD dst_unused:UNUSED_PAD src0_sel:DWORD src1_sel:WORD_1\n" \
  "ds_read_b128 v[96:99], v67\n" \
  "s_waitcnt lgkmcnt(10)\n" \
  "v_smfmac_f32_16x16x64_bf16 v[16:19], v[180:183], v[100:107], v192\n" \
  "v_lshlrev_b32_sdwa v64, 4, v218 dst_sel:DWORD dst_unused:UNUSED_PAD src0_sel:DWORD src1_sel:WORD_0\n" \
  "ds_read_b128 v[100:103], v64\n" \
  "v_lshlrev_b32_sdwa v65, 4, v218 dst_sel:DWORD dst_unused:UNUSED_PAD src0_sel:DWORD src1_sel:WORD_1\n" \
  "ds_read_b128 v[104:107], v65\n" \
  "global_load_dwordx4 v[240:243], v188, s[56:57] sc1\n" \
  "global_load_dwordx4 v[244:247], v188, s[56:57] offset:1024 sc1\n" \
  "s_waitcnt lgkmcnt(10)\n" \
  "v_smfmac_f32_16x16x64_bf16 v[20:23], v[180:183], v[108:115], v192\n" \
  "v_lshlrev_b32_sdwa v66, 4, v219 dst_sel:DWORD dst_unused:UNUSED_PAD src0_sel:DWORD src1_sel:WORD_0\n" \
  "ds_read_b128 v[108:111], v66\n" \
  "v_lshlrev_b32_sdwa v67, 4, v219 dst_sel:DWORD dst_unused:UNUSED_PAD src0_sel:DWORD src1_sel:WORD_1\n" \
  "ds_read_b128 v[112:115], v67\n" \
  "s_waitcnt lgkmcnt(10)\n" \
  "v_smfmac_f32_16x16x64_bf16 v[24:27], v[180:183], v[68:75], v192\n" \
  "v_lshlrev_b32_sdwa v64, 4, v220 dst_sel:DWORD dst_unused:UNUSED_PAD src0_sel:DWORD src1_sel:WORD_0\n" \
  "ds_read_b128 v[68:71], v64\n" \
  "v_lshlrev_b32_sdwa v65, 4, v220 dst_sel:DWORD dst_unused:UNUSED_PAD src0_sel:DWORD src1_sel:WORD_1\n" \
  "ds_read_b128 v[72:75], v65\n" \
  "global_load_dwordx4 v[248:251], v188, s[56:57] offset:2048 sc1\n" \
  "global_load_dwordx4 v[252:255], v188, s[56:57] offset:3072 sc1\n" \
  "s_waitcnt lgkmcnt(10)\n" \
  "v_smfmac_f32_16x16x64_bf16 v[28:31], v[180:183], v[76:83], v192\n" \
  "v_lshlrev_b32_sdwa v66, 4, v221 dst_sel:DWORD dst_unused:UNUSED_PAD src0_sel:DWORD src1_sel:WORD_0\n" \
  "ds_read_b128 v[76:79], v66\n" \
  "v_lshlrev_b32_sdwa v67, 4, v221 dst_sel:DWORD dst_unused:UNUSED_PAD src0_sel:DWORD src1_sel:WORD_1\n" \
  "ds_read_b128 v[80:83], v67\n" \
  "s_waitcnt lgkmcnt(10)\n" \
  "v_smfmac_f32_16x16x64_bf16 v[32:35], v[180:183], v[84:91], v192\n" \
  "v_lshlrev_b32_sdwa v64, 4, v222 dst_sel:DWORD dst_unused:UNUSED_PAD src0_sel:DWORD src1_sel:WORD_0\n" \
  "ds_read_b128 v[84:87], v64\n" \
  "v_lshlrev_b32_sdwa v65, 4, v222 dst_sel:DWORD dst_unused:UNUSED_PAD src0_sel:DWORD src1_sel:WORD_1\n" \
  "ds_read_b128 v[88:91], v65\n" \
  "s_waitcnt lgkmcnt(10)\n" \
  "v_smfmac_f32_16x16x64_bf16 v[36:39], v[180:183], v[92:99], v192\n" \
  "v_lshlrev_b32_sdwa v66, 4, v223 dst_sel:DWORD dst_unused:UNUSED_PAD src0_sel:DWORD src1_sel:WORD_0\n" \
  "ds_read_b128 v[92:95], v66\n" \
  "v_lshlrev_b32_sdwa v67, 4, v223 dst_sel:DWORD dst_unused:UNUSED_PAD src0_sel:DWORD src1_sel:WORD_1\n" \
  "ds_read_b128 v[96:99], v67\n" \
  "s_add_u32 s56, s56, 32768\n" \
  "s_addc_u32 s57, s57, 0\n" \
  "s_add_u32 s54, s54, 64000\n" \
  "s_addc_u32 s55, s55, 0\n" \
  "s_waitcnt lgkmcnt(10)\n" \
  "v_smfmac_f32_16x16x64_bf16 v[40:43], v[180:183], v[100:107], v192\n" \
  "s_waitcnt lgkmcnt(8)\n" \
  "v_smfmac_f32_16x16x64_bf16 v[44:47], v[180:183], v[108:115], v192\n" \
  "s_waitcnt lgkmcnt(6)\n" \
  "v_smfmac_f32_16x16x64_bf16 v[48:51], v[180:183], v[68:75], v192\n" \
  "s_waitcnt lgkmcnt(4)\n" \
  "v_smfmac_f32_16x16x64_bf16 v[52:55], v[180:183], v[76:83], v192\n" \
  "s_lshl3_add_u32 s35, s27, s51\n" \
  "s_cmp_eq_u32 s35, 37\n" \
  "s_cbranch_scc1 LG_chk\n" \
  "LG_ret:\n" \
  "s_sub_u32 s51, s51, 1\n" \
  "s_cmp_lg_u32 s51, 0\n" \
  "s_waitcnt vmcnt(8)\n" \
  "s_barrier\n" \
  "s_cbranch_scc1 LG_loop\n" \
  "v_lshlrev_b32_sdwa v64, 4, v224 dst_sel:DWORD dst_unused:UNUSED_PAD src0_sel:DWORD src1_sel:WORD_0\n" \
  "ds_read_b128 v[100:103], v64\n" \
  "v_lshlrev_b32_sdwa v65, 4, v224 dst_sel:DWORD dst_unused:UNUSED_PAD src0_sel:DWORD src1_sel:WORD_1\n" \
  "ds_read_b128 v[104:107], v65\n" \
  "s_waitcnt lgkmcnt(4)\n" \
  "v_smfmac_f32_16x16x64_bf16 v[56:59], v[180:183], v[84:91], v192\n" \
  "s_waitcnt lgkmcnt(2)\n" \
  "v_smfmac_f32_16x16x64_bf16 v[60:63], v[180:183], v[92:99], v192\n" \
  "s_barrier\n" \
  "s_mov_b32 m0, s58\n" \
  "s_add_u32 s35, s58, 0x8000\n" \
  "global_load_lds_dwordx4 v188, s[54:55] sc1\n" \
  "global_load_lds_dwordx4 v188, s[54:55] offset:1024 sc1\n" \
  "v_lshlrev_b32_sdwa v66, 4, v225 dst_sel:DWORD dst_unused:UNUSED_PAD src0_sel:DWORD src1_sel:WORD_0\n" \
  "ds_read_b128 v[108:111], v66\n" \
  "v_lshlrev_b32_sdwa v67, 4, v225 dst_sel:DWORD dst_unused:UNUSED_PAD src0_sel:DWORD src1_sel:WORD_1\n" \
  "ds_read_b128 v[112:115], v67\n" \
  "v_lshlrev_b32_sdwa v64, 4, v226 dst_sel:DWORD dst_unused:UNUSED_PAD src0_sel:DWORD src1_sel:WORD_0\n" \
  "ds_read_b128 v[68:71], v64\n" \
  "v_lshlrev_b32_sdwa v65, 4, v226 dst_sel:DWORD dst_unused:UNUSED_PAD src0_sel:DWORD src1_sel:WORD_1\n" \
  "ds_read_b128 v[72:75], v65\n" \
  "v_lshlrev_b32_sdwa v66, 4, v227 dst_sel:DWORD dst_unused:UNUSED_PAD src0_sel:DWORD src1_sel:WORD_0\n" \
  "ds_read_b128 v[76:79], v66\n" \
  "v_lshlrev_b32_sdwa v67, 4, v227 dst_sel:DWORD dst_unused:UNUSED_PAD src0_sel:DWORD src1_sel:WORD_1\n" \
  "ds_read_b128 v[80:83], v67\n" \
  "s_add_u32 m0, s58, 0x4000\n" \
  "s_cmp_lt_u32 s35, 0x28000\n" \
  "global_load_lds_dwordx4 v189, s[54:55] sc1\n" \
  "global_load_lds_dwordx4 v189, s[54:55] offset:1024 sc1\n" \
  "s_cselect_b32 s58, s35, s59\n" \
  "v_lshlrev_b32_sdwa v64, 4, v228 dst_sel:DWORD dst_unused:UNUSED_PAD src0_sel:DWORD src1_sel:WORD_0\n" \
  "ds_read_b128 v[84:87], v64\n" \
  "v_lshlrev_b32_sdwa v65, 4, v228 dst_sel:DWORD dst_unused:UNUSED_PAD src0_sel:DWORD src1_sel:WORD_1\n" \
  "ds_read_b128 v[88:91], v65\n" \
  "v_lshlrev_b32_sdwa v66, 4, v229 dst_sel:DWORD dst_unused:UNUSED_PAD src0_sel:DWORD src1_sel:WORD_0\n" \
  "ds_read_b128 v[92:95], v66\n" \
  "v_lshlrev_b32_sdwa v67, 4, v229 dst_sel:DWORD dst_unused:UNUSED_PAD src0_sel:DWORD src1_sel:WORD_1\n" \
  "ds_read_b128 v[96:99], v67\n" \
  "s_waitcnt lgkmcnt(10)\n" \
  "v_smfmac_f32_16x16x64_bf16 v[0:3], v[180:183], v[100:107], v192\n" \
  "v_lshlrev_b32_sdwa v64, 4, v230 dst_sel:DWORD dst_unused:UNUSED_PAD src0_sel:DWORD src1_sel:WORD_0\n" \
  "ds_read_b128 v[100:103], v64\n" \
  "v_lshlrev_b32_sdwa v65, 4, v230 dst_sel:DWORD dst_unused:UNUSED_PAD src0_sel:DWORD src1_sel:WORD_1\n" \
  "ds_read_b128 v[104:107], v65\n" \
  "s_mov_b32 m0, s58\n" \
  "s_add_u32 s35, s58, 0x8000\n" \
  "global_load_lds_dwordx4 v190, s[54:55] sc1\n" \
  "global_load_lds_dwordx4 v190, s[54:55] offset:1024 sc1\n" \
  "s_waitcnt lgkmcnt(10)\n" \
  "v_smfmac_f32_16x16x64_bf16 v[4:7], v[180:183], v[108:115], v192\n" \
  "v_lshlrev_b32_sdwa v66, 4, v231 dst_sel:DWORD dst_unused:UNUSED_PAD src0_sel:DWORD src1_sel:WORD_0\n" \
  "ds_read_b128 v[108:111], v66\n" \
  "v_lshlrev_b32_sdwa v67, 4, v231 dst_sel:DWORD dst_unused:UNUSED_PAD src0_sel:DWORD src1_sel:WORD_1\n" \
  "ds_read_b128 v[112:115], v67\n" \
  "s_waitcnt lgkmcnt(10)\n" \
  "v_smfmac_f32_16x16x64_bf16 v[8:11], v[180:183], v[68:75], v192\n" \
  "v_lshlrev_b32_sdwa v64, 4, v232 dst_sel:DWORD dst_unused:UNUSED_PAD src0_sel:DWORD src1_sel:WORD_0\n" \
  "ds_read_b128 v[68:71], v64\n" \
  "v_lshlrev_b32_sdwa v65, 4, v232 dst_sel:DWORD dst_unused:UNUSED_PAD src0_sel:DWORD src1_sel:WORD_1\n" \
  "ds_read_b128 v[72:75], v65\n" \
  "s_add_u32 m0, s58, 0x4000\n" \
  "s_cmp_lt_u32 s35, 0x28000\n" \
  "global_load_lds_dwordx4 v191, s[54:55] sc1\n" \
  "global_load_lds_dwordx4 v191, s[54:55] offset:1024 sc1\n" \
  "s_cselect_b32 s58, s35, s59\n" \
  "s_waitcnt lgkmcnt(10)\n" \
  "v_smfmac_f32_16x16x64_bf16 v[12:15], v[180:183], v[76:83], v192\n" \
  "v_lshlrev_b32_sdwa v66, 4, v233 dst_sel:DWORD dst_unused:UNUSED_PAD src0_sel:DWORD src1_sel:WORD_0\n" \
  "ds_read_b128 v[76:79], v66\n" \
  "v_lshlrev_b32_sdwa v67, 4, v233 dst_sel:DWORD dst_unused:UNUSED_PAD src0_sel:DWORD src1_sel:WORD_1\n" \
  "ds_read_b128 v[80:83], v67\n" \
  "s_waitcnt lgkmcnt(10)\n" \
  "v_smfmac_f32_16x16x64_bf16 v[16:19], v[180:183], v[84:91], v192\n" \
  "v_lshlrev_b32_sdwa v64, 4, v234 dst_sel:DWORD dst_unused:UNUSED_PAD src0_sel:DWORD src1_sel:WORD_0\n" \
  "ds_read_b128 v[84:87], v64\n" \
  "v_lshlrev_b32_sdwa v65, 4, v234 dst_sel:DWORD dst_unused:UNUSED_PAD src0_sel:DWORD src1_sel:WORD_1\n" \
  "ds_read_b128 v[88:91], v65\n" \
  "global_load_dwordx4 v[208:211], v188, s[56:57] sc1\n" \
  "global_load_dwordx4 v[212:215], v188, s[56:57] offset:1024 sc1\n" \
  "s_waitcnt lgkmcnt(10)\n" \
  "v_smfmac_f32_16x16x64_bf16 v[20:23], v[180:183], v[92:99], v192\n" \
  "v_lshlrev_b32_sdwa v66, 4, v235 dst_sel:DWORD dst_unused:UNUSED_PAD src0_sel:DWORD src1_sel:WORD_0\n" \
  "ds_read_b128 v[92:95], v66\n" \
  "v_lshlrev_b32_sdwa v67, 4, v235 dst_sel:DWORD dst_unused:UNUSED_PAD src0_sel:DWORD src1_sel:WORD_1\n" \
  "ds_read_b128 v[96:99], v67\n" \
  "s_waitcnt lgkmcnt(10)\n" \
  "v_smfmac_f32_16x16x64_bf16 v[24:27], v[180:183], v[100:107], v192\n" \
  "v_lshlrev_b32_sdwa v64, 4, v236 dst_sel:DWORD dst_unused:UNUSED_PAD src0_sel:DWORD src1_sel:WORD_0\n" \
  "ds_read_b128 v[100:103], v64\n" \
  "v_lshlrev_b32_sdwa v65, 4, v236 dst_sel:DWORD dst_unused:UNUSED_PAD src0_sel:DWORD src1_sel:WORD_1\n" \
  "ds_read_b128 v[104:107], v65\n" \
  "global_load_dwordx4 v[216:219], v188, s[56:57] offset:2048 sc1\n" \
  "global_load_dwordx4 v[220:223], v188, s[56:57] offset:3072 sc1\n" \
  "s_waitcnt lgkmcnt(10)\n" \
  "v_smfmac_f32_16x16x64_bf16 v[28:31], v[180:183], v[108:115], v192\n" \
  "v_lshlrev_b32_sdwa v66, 4, v237 dst_sel:DWORD dst_unused:UNUSED_PAD src0_sel:DWORD src1_sel:WORD_0\n" \
  "ds_read_b128 v[108:111], v66\n" \
  "v_lshlrev_b32_sdwa v67, 4, v237 dst_sel:DWORD dst_unused:UNUSED_PAD src0_sel:DWORD src1_sel:WORD_1\n" \
  "ds_read_b128 v[112:115], v67\n" \
  "s_waitcnt lgkmcnt(10)\n" \
  "v_smfmac_f32_16x16x64_bf16 v[32:35], v[180:183], v[68:75], v192\n" \
  "v_lshlrev_b32_sdwa v64, 4, v238 dst_sel:DWORD dst_unused:UNUSED_PAD src0_sel:DWORD src1_sel:WORD_0\n" \
  "ds_read_b128 v[68:71], v64\n" \
  "v_lshlrev_b32_sdwa v65, 4, v238 dst_sel:DWORD dst_unused:UNUSED_PAD src0_sel:DWORD src1_sel:WORD_1\n" \
  "ds_read_b128 v[72:75], v65\n" \
  "s_waitcnt lgkmcnt(10)\n" \
  "v_smfmac_f32_16x16x64_bf16 v[36:39], v[180:183], v[76:83], v192\n" \
  "v_lshlrev_b32_sdwa v66, 4, v239 dst_sel:DWORD dst_unused:UNUSED_PAD src0_sel:DWORD src1_sel:WORD_0\n" \
  "ds_read_b128 v[76:79], v66\n" \
  "v_lshlrev_b32_sdwa v67, 4, v239 dst_sel:DWORD dst_unused:UNUSED_PAD src0_sel:DWORD src1_sel:WORD_1\n" \
  "ds_read_b128 v[80:83], v67\n" \
  "s_add_u32 s56, s56, 32768\n" \
  "s_addc_u32 s57, s57, 0\n" \
  "s_add_u32 s54, s54, 64000\n" \
  "s_addc_u32 s55, s55, 0\n" \
  "s_waitcnt lgkmcnt(10)\n" \
  "v_smfmac_f32_16x16x64_bf16 v[40:43], v[180:183], v[84:91], v192\n" \
  "s_waitcnt lgkmcnt(8)\n" \
  "v_smfmac_f32_16x16x64_bf16 v[44:47], v[180:183], v[92:99], v192\n" \
  "s_waitcnt lgkmcnt(6)\n" \
  "v_smfmac_f32_16x16x64_bf16 v[48:51], v[180:183], v[100:107], v192\n" \
  "s_waitcnt lgkmcnt(4)\n" \
  "v_smfmac_f32_16x16x64_bf16 v[52:55], v[180:183], v[108:115], v192\n" \
  "s_waitcnt vmcnt(8)\n" \
  "s_barrier\n" \
  "v_lshlrev_b32_sdwa v64, 4, v240 dst_sel:DWORD dst_unused:UNUSED_PAD src0_sel:DWORD src1_sel:WORD_0\n" \
  "ds_read_b128 v[84:87], v64\n" \
  "v_lshlrev_b32_sdwa v65, 4, v240 dst_sel:DWORD dst_unused:UNUSED_PAD src0_sel:DWORD src1_sel:WORD_1\n" \
  "ds_read_b128 v[88:91], v65\n" \
  "s_waitcnt lgkmcnt(4)\n" \
  "v_smfmac_f32_16x16x64_bf16 v[56:59], v[180:183], v[68:75], v192\n" \
  "s_waitcnt lgkmcnt(2)\n" \
  "v_smfmac_f32_16x16x64_bf16 v[60:63], v[180:183], v[76:83], v192\n" \
  "s_barrier\n" \
  "s_mov_b32 m0, s58\n" \
  "s_add_u32 s35, s58, 0x8000\n" \
  "global_load_lds_dwordx4 v188, s[54:55] sc1\n" \
  "global_load_lds_dwordx4 v188, s[54:55] offset:1024 sc1\n" \
  "v_lshlrev_b32_sdwa v66, 4, v241 dst_sel:DWORD dst_unused:UNUSED_PAD src0_sel:DWORD src1_sel:WORD_0\n" \
  "ds_read_b128 v[92:95], v66\n" \
  "v_lshlrev_b32_sdwa v67, 4, v241 dst_sel:DWORD dst_unused:UNUSED_PAD src0_sel:DWORD src1_sel:WORD_1\n" \
  "ds_read_b128 v[96:99], v67\n" \
  "v_lshlrev_b32_sdwa v64, 4, v242 dst_sel:DWORD dst_unused:UNUSED_PAD src0_sel:DWORD src1_sel:WORD_0\n" \
  "ds_read_b128 v[100:103], v64\n" \
  "v_lshlrev_b32_sdwa v65, 4, v242 dst_sel:DWORD dst_unused:UNUSED_PAD src0_sel:DWORD src1_sel:WORD_1\n" \
  "ds_read_b128 v[104:107], v65\n" \
  "v_lshlrev_b32_sdwa v66, 4, v243 dst_sel:DWORD dst_unused:UNUSED_PAD src0_sel:DWORD src1_sel:WORD_0\n" \
  "ds_read_b128 v[108:111], v66\n" \
  "v_lshlrev_b32_sdwa v67, 4, v243 dst_sel:DWORD dst_unused:UNUSED_PAD src0_sel:DWORD src1_sel:WORD_1\n" \
  "ds_read_b128 v[112:115], v67\n" \
  "s_add_u32 m0, s58, 0x4000\n" \
  "s_cmp_lt_u32 s35, 0x28000\n" \
  "global_load_lds_dwordx4 v189, s[54:55] sc1\n" \
  "global_load_lds_dwordx4 v189, s[54:55] offset:1024 sc1\n" \
  "s_cselect_b32 s58, s35, s59\n" \
  "v_lshlrev_b32_sdwa v64, 4, v244 dst_sel:DWORD dst_unused:UNUSED_PAD src0_sel:DWORD src1_sel:WORD_0\n" \
  "ds_read_b128 v[68:71], v64\n" \
  "v_lshlrev_b32_sdwa v65, 4, v244 dst_sel:DWORD dst_unused:UNUSED_PAD src0_sel:DWORD src1_sel:WORD_1\n" \
  "ds_read_b128 v[72:75], v65\n" \
  "v_lshlrev_b32_sdwa v66, 4, v245 dst_sel:DWORD dst_unused:UNUSED_PAD src0_sel:DWORD src1_sel:WORD_0\n" \
  "ds_read_b128 v[76:79], v66\n" \
  "v_lshlrev_b32_sdwa v67, 4, v245 dst_sel:DWORD dst_unused:UNUSED_PAD src0_sel:DWORD src1_sel:WORD_1\n" \
  "ds_read_b128 v[80:83], v67\n" \
  "s_waitcnt lgkmcnt(10)\n" \
  "v_smfmac_f32_16x16x64_bf16 v[0:3], v[180:183], v[84:91], v192\n" \
  "v_lshlrev_b32_sdwa v64, 4, v246 dst_sel:DWORD dst_unused:UNUSED_PAD src0_sel:DWORD src1_sel:WORD_0\n" \
  "ds_read_b128 v[84:87], v64\n" \
  "v_lshlrev_b32_sdwa v65, 4, v246 dst_sel:DWORD dst_unused:UNUSED_PAD src0_sel:DWORD src1_sel:WORD_1\n" \
  "ds_read_b128 v[88:91], v65\n" \
  "s_waitcnt lgkmcnt(10)\n" \
  "v_smfmac_f32_16x16x64_bf16 v[4:7], v[180:183], v[92:99], v192\n" \
  "v_lshlrev_b32_sdwa v66, 4, v247 dst_sel:DWORD dst_unused:UNUSED_PAD src0_sel:DWORD src1_sel:WORD_0\n" \
  "ds_read_b128 v[92:95], v66\n" \
  "v_lshlrev_b32_sdwa v67, 4, v247 dst_sel:DWORD dst_unused:UNUSED_PAD src0_sel:DWORD src1_sel:WORD_1\n" \
  "ds_read_b128 v[96:99], v67\n" \
  "s_waitcnt lgkmcnt(10)\n" \
  "v_smfmac_f32_16x16x64_bf16 v[8:11], v[180:183], v[100:107], v192\n" \
  "v_lshlrev_b32_sdwa v64, 4, v248 dst_sel:DWORD dst_unused:UNUSED_PAD src0_sel:DWORD src1_sel:WORD_0\n" \
  "ds_read_b128 v[100:103], v64\n" \
  "v_lshlrev_b32_sdwa v65, 4, v248 dst_sel:DWORD dst_unused:UNUSED_PAD src0_sel:DWORD src1_sel:WORD_1\n" \
  "ds_read_b128 v[104:107], v65\n" \
  "s_waitcnt lgkmcnt(10)\n" \
  "v_smfmac_f32_16x16x64_bf16 v[12:15], v[180:183], v[108:115], v192\n" \
  "v_lshlrev_b32_sdwa v66, 4, v249 dst_sel:DWORD dst_unused:UNUSED_PAD src0_sel:DWORD src1_sel:WORD_0\n" \
  "ds_read_b128 v[108:111], v66\n" \
  "v_lshlrev_b32_sdwa v67, 4, v249 dst_sel:DWORD dst_unused:UNUSED_PAD src0_sel:DWORD src1_sel:WORD_1\n" \
  "ds_read_b128 v[112:115], v67\n" \
  "s_waitcnt lgkmcnt(10)\n" \
  "v_smfmac_f32_16x16x64_bf16 v[16:19], v[180:183], v[68:75], v192\n" \
  "v_lshlrev_b32_sdwa v64, 4, v250 dst_sel:DWORD dst_unused:UNUSED_PAD src0_sel:DWORD src1_sel:WORD_0\n" \
  "ds_read_b128 v[68:71], v64\n" \
  "v_lshlrev_b32_sdwa v65, 4, v250 dst_sel:DWORD dst_unused:UNUSED_PAD src0_sel:DWORD src1_sel:WORD_1\n" \
  "ds_read_b128 v[72:75], v65\n" \
  "s_waitcnt lgkmcnt(10)\n" \
  "v_smfmac_f32_16x16x64_bf16 v[20:23], v[180:183], v[76:83], v192\n" \
  "v_lshlrev_b32_sdwa v66, 4, v251 dst_sel:DWORD dst_unused:UNUSED_PAD src0_sel:DWORD src1_sel:WORD_0\n" \
  "ds_read_b128 v[76:79], v66\n" \
  "v_lshlrev_b32_sdwa v67, 4, v251 dst_sel:DWORD dst_unused:UNUSED_PAD src0_sel:DWORD src1_sel:WORD_1\n" \
  "ds_read_b128 v[80:83], v67\n" \
  "s_waitcnt lgkmcnt(10)\n" \
  "v_smfmac_f32_16x16x64_bf16 v[24:27], v[180:183], v[84:91], v192\n" \
  "v_lshlrev_b32_sdwa v64, 4, v252 dst_sel:DWORD dst_unused:UNUSED_PAD src0_sel:DWORD src1_sel:WORD_0\n" \
  "ds_read_b128 v[84:87], v64\n" \
  "v_lshlrev_b32_sdwa v65, 4, v252 dst_sel:DWORD dst_unused:UNUSED_PAD src0_sel:DWORD src1_sel:WORD_1\n" \
  "ds_read_b128 v[88:91], v65\n" \
  "s_waitcnt lgkmcnt(10)\n" \
  "v_smfmac_f32_16x16x64_bf16 v[28:31], v[180:183], v[92:99], v192\n" \
  "v_lshlrev_b32_sdwa v66, 4, v253 dst_sel:DWORD dst_unused:UNUSED_PAD src0_sel:DWORD src1_sel:WORD_0\n" \
  "ds_read_b128 v[92:95], v66\n" \
  "v_lshlrev_b32_sdwa v67, 4, v253 dst_sel:DWORD dst_unused:UNUSED_PAD src0_sel:DWORD src1_sel:WORD_1\n" \
  "ds_read_b128 v[96:99], v67\n" \
  "s_waitcnt lgkmcnt(10)\n" \
  "v_smfmac_f32_16x16x64_bf16 v[32:35], v[180:183], v[100:107], v192\n" \
  "v_lshlrev_b32_sdwa v64, 4, v254 dst_sel:DWORD dst_unused:UNUSED_PAD src0_sel:DWORD src1_sel:WORD_0\n" \
  "ds_read_b128 v[100:103], v64\n" \
  "v_lshlrev_b32_sdwa v65, 4, v254 dst_sel:DWORD dst_unused:UNUSED_PAD src0_sel:DWORD src1_sel:WORD_1\n" \
  "ds_read_b128 v[104:107], v65\n" \
  "s_waitcnt lgkmcnt(10)\n" \
  "v_smfmac_f32_16x16x64_bf16 v[36:39], v[180:183], v[108:115], v192\n" \
  "v_lshlrev_b32_sdwa v66, 4, v255 dst_sel:DWORD dst_unused:UNUSED_PAD src0_sel:DWORD src1_sel:WORD_0\n" \
  "ds_read_b128 v[108:111], v66\n" \
  "v_lshlrev_b32_sdwa v67, 4, v255 dst_sel:DWORD dst_unused:UNUSED_PAD src0_sel:DWORD src1_sel:WORD_1\n" \
  "ds_read_b128 v[112:115], v67\n" \
  "s_add_u32 s56, s56, 32768\n" \
  "s_addc_u32 s57, s57, 0\n" \
  "s_add_u32 s54, s54, 64000\n" \
  "s_addc_u32 s55, s55, 0\n" \
  "s_waitcnt lgkmcnt(10)\n" \
  "v_smfmac_f32_16x16x64_bf16 v[40:43], v[180:183], v[68:75], v192\n" \
  "s_waitcnt lgkmcnt(8)\n" \
  "v_smfmac_f32_16x16x64_bf16 v[44:47], v[180:183], v[76:83], v192\n" \
  "s_waitcnt lgkmcnt(6)\n" \
  "v_smfmac_f32_16x16x64_bf16 v[48:51], v[180:183], v[84:91], v192\n" \
  "s_waitcnt lgkmcnt(4)\n" \
  "v_smfmac_f32_16x16x64_bf16 v[52:55], v[180:183], v[92:99], v192\n" \
  "s_waitcnt vmcnt(0)\n" \
  "s_barrier\n" \
  "v_lshlrev_b32_sdwa v64, 4, v208 dst_sel:DWORD dst_unused:UNUSED_PAD src0_sel:DWORD src1_sel:WORD_0\n" \
  "ds_read_b128 v[68:71], v64\n" \
  "v_lshlrev_b32_sdwa v65, 4, v208 dst_sel:DWORD dst_unused:UNUSED_PAD src0_sel:DWORD src1_sel:WORD_1\n" \
  "ds_read_b128 v[72:75], v65\n" \
  "v_lshlrev_b32_sdwa v66, 4, v209 dst_sel:DWORD dst_unused:UNUSED_PAD src0_sel:DWORD src1_sel:WORD_0\n" \
  "ds_read_b128 v[76:79], v66\n" \
  "v_lshlrev_b32_sdwa v67, 4, v209 dst_sel:DWORD dst_unused:UNUSED_PAD src0_sel:DWORD src1_sel:WORD_1\n" \
  "ds_read_b128 v[80:83], v67\n" \
  "v_lshlrev_b32_sdwa v64, 4, v210 dst_sel:DWORD dst_unused:UNUSED_PAD src0_sel:DWORD src1_sel:WORD_0\n" \
  "ds_read_b128 v[84:87], v64\n" \
  "v_lshlrev_b32_sdwa v65, 4, v210 dst_sel:DWORD dst_unused:UNUSED_PAD src0_sel:DWORD src1_sel:WORD_1\n" \
  "ds_read_b128 v[88:91], v65\n" \
  "v_lshlrev_b32_sdwa v66, 4, v211 dst_sel:DWORD dst_unused:UNUSED_PAD src0_sel:DWORD src1_sel:WORD_0\n" \
  "ds_read_b128 v[92:95], v66\n" \
  "v_lshlrev_b32_sdwa v67, 4, v211 dst_sel:DWORD dst_unused:UNUSED_PAD src0_sel:DWORD src1_sel:WORD_1\n" \
  "ds_read_b128 v[96:99], v67\n" \
  "s_waitcnt lgkmcnt(10)\n" \
  "v_smfmac_f32_16x16x64_bf16 v[56:59], v[180:183], v[100:107], v192\n" \
  "v_lshlrev_b32_sdwa v64, 4, v212 dst_sel:DWORD dst_unused:UNUSED_PAD src0_sel:DWORD src1_sel:WORD_0\n" \
  "ds_read_b128 v[100:103], v64\n" \
  "v_lshlrev_b32_sdwa v65, 4, v212 dst_sel:DWORD dst_unused:UNUSED_PAD src0_sel:DWORD src1_sel:WORD_1\n" \
  "ds_read_b128 v[104:107], v65\n" \
  "s_waitcnt lgkmcnt(10)\n" \
  "v_smfmac_f32_16x16x64_bf16 v[60:63], v[180:183], v[108:115], v192\n" \
  "v_lshlrev_b32_sdwa v66, 4, v213 dst_sel:DWORD dst_unused:UNUSED_PAD src0_sel:DWORD src1_sel:WORD_0\n" \
  "ds_read_b128 v[108:111], v66\n" \
  "v_lshlrev_b32_sdwa v67, 4, v213 dst_sel:DWORD dst_unused:UNUSED_PAD src0_sel:DWORD src1_sel:WORD_1\n" \
  "ds_read_b128 v[112:115], v67\n" \
  "s_waitcnt lgkmcnt(10)\n" \
  "v_smfmac_f32_16x16x64_bf16 v[0:3], v[180:183], v[68:75], v192\n" \
  "v_lshlrev_b32_sdwa v64, 4, v214 dst_sel:DWORD dst_unused:UNUSED_PAD src0_sel:DWORD src1_sel:WORD_0\n" \
  "ds_read_b128 v[68:71], v64\n" \
  "v_lshlrev_b32_sdwa v65, 4, v214 dst_sel:DWORD dst_unused:UNUSED_PAD src0_sel:DWORD src1_sel:WORD_1\n" \
  "ds_read_b128 v[72:75], v65\n" \
  "s_waitcnt lgkmcnt(10)\n" \
  "v_smfmac_f32_16x16x64_bf16 v[4:7], v[180:183], v[76:83], v192\n" \
  "v_lshlrev_b32_sdwa v66, 4, v215 dst_sel:DWORD dst_unused:UNUSED_PAD src0_sel:DWORD src1_sel:WORD_0\n" \
  "ds_read_b128 v[76:79], v66\n" \
  "v_lshlrev_b32_sdwa v67, 4, v215 dst_sel:DWORD dst_unused:UNUSED_PAD src0_sel:DWORD src1_sel:WORD_1\n" \
  "ds_read_b128 v[80:83], v67\n" \
  "s_waitcnt lgkmcnt(10)\n" \
  "v_smfmac_f32_16x16x64_bf16 v[8:11], v[180:183], v[84:91], v192\n" \
  "v_lshlrev_b32_sdwa v64, 4, v216 dst_sel:DWORD dst_unused:UNUSED_PAD src0_sel:DWORD src1_sel:WORD_0\n" \
  "ds_read_b128 v[84:87], v64\n" \
  "v_lshlrev_b32_sdwa v65, 4, v216 dst_sel:DWORD dst_unused:UNUSED_PAD src0_sel:DWORD src1_sel:WORD_1\n" \
  "ds_read_b128 v[88:91], v65\n" \
  "s_waitcnt lgkmcnt(10)\n" \
  "v_smfmac_f32_16x16x64_bf16 v[12:15], v[180:183], v[92:99], v192\n" \
  "v_lshlrev_b32_sdwa v66, 4, v217 dst_sel:DWORD dst_unused:UNUSED_PAD src0_sel:DWORD src1_sel:WORD_0\n" \
  "ds_read_b128 v[92:95], v66\n" \
  "v_lshlrev_b32_sdwa v67, 4, v217 dst_sel:DWORD dst_unused:UNUSED_PAD src0_sel:DWORD src1_sel:WORD_1\n" \
  "ds_read_b128 v[96:99], v67\n" \
  "s_waitcnt lgkmcnt(10)\n" \
  "v_smfmac_f32_16x16x64_bf16 v[16:19], v[180:183], v[100:107], v192\n" \
  "v_lshlrev_b32_sdwa v64, 4, v218 dst_sel:DWORD dst_unused:UNUSED_PAD src0_sel:DWORD src1_sel:WORD_0\n" \
  "ds_read_b128 v[100:103], v64\n" \
  "v_lshlrev_b32_sdwa v65, 4, v218 dst_sel:DWORD dst_unused:UNUSED_PAD src0_sel:DWORD src1_sel:WORD_1\n" \
  "ds_read_b128 v[104:107], v65\n" \
  "s_waitcnt lgkmcnt(10)\n" \
  "v_smfmac_f32_16x16x64_bf16 v[20:23], v[180:183], v[108:115], v192\n" \
  "v_lshlrev_b32_sdwa v66, 4, v219 dst_sel:DWORD dst_unused:UNUSED_PAD src0_sel:DWORD src1_sel:WORD_0\n" \
  "ds_read_b128 v[108:111], v66\n" \
  "v_lshlrev_b32_sdwa v67, 4, v219 dst_sel:DWORD dst_unused:UNUSED_PAD src0_sel:DWORD src1_sel:WORD_1\n" \
  "ds_read_b128 v[112:115], v67\n" \
  "s_waitcnt lgkmcnt(10)\n" \
  "v_smfmac_f32_16x16x64_bf16 v[24:27], v[180:183], v[68:75], v192\n" \
  "v_lshlrev_b32_sdwa v64, 4, v220 dst_sel:DWORD dst_unused:UNUSED_PAD src0_sel:DWORD src1_sel:WORD_0\n" \
  "ds_read_b128 v[68:71], v64\n" \
  "v_lshlrev_b32_sdwa v65, 4, v220 dst_sel:DWORD dst_unused:UNUSED_PAD src0_sel:DWORD src1_sel:WORD_1\n" \
  "ds_read_b128 v[72:75], v65\n" \
  "s_waitcnt lgkmcnt(10)\n" \
  "v_smfmac_f32_16x16x64_bf16 v[28:31], v[180:183], v[76:83], v192\n" \
  "v_lshlrev_b32_sdwa v66, 4, v221 dst_sel:DWORD dst_unused:UNUSED_PAD src0_sel:DWORD src1_sel:WORD_0\n" \
  "ds_read_b128 v[76:79], v66\n" \
  "v_lshlrev_b32_sdwa v67, 4, v221 dst_sel:DWORD dst_unused:UNUSED_PAD src0_sel:DWORD src1_sel:WORD_1\n" \
  "ds_read_b128 v[80:83], v67\n" \
  "s_waitcnt lgkmcnt(10)\n" \
  "v_smfmac_f32_16x16x64_bf16 v[32:35], v[180:183], v[84:91], v192\n" \
  "v_lshlrev_b32_sdwa v64, 4, v222 dst_sel:DWORD dst_unused:UNUSED_PAD src0_sel:DWORD src1_sel:WORD_0\n" \
  "ds_read_b128 v[84:87], v64\n" \
  "v_lshlrev_b32_sdwa v65, 4, v222 dst_sel:DWORD dst_unused:UNUSED_PAD src0_sel:DWORD src1_sel:WORD_1\n" \
  "ds_read_b128 v[88:91], v65\n" \
  "s_waitcnt lgkmcnt(10)\n" \
  "v_smfmac_f32_16x16x64_bf16 v[36:39], v[180:183], v[92:99], v192\n" \
  "v_lshlrev_b32_sdwa v66, 4, v223 dst_sel:DWORD dst_unused:UNUSED_PAD src0_sel:DWORD src1_sel:WORD_0\n" \
  "ds_read_b128 v[92:95], v66\n" \
  "v_lshlrev_b32_sdwa v67, 4, v223 dst_sel:DWORD dst_unused:UNUSED_PAD src0_sel:DWORD src1_sel:WORD_1\n" \
  "ds_read_b128 v[96:99], v67\n" \
  "s_waitcnt lgkmcnt(10)\n" \
  "v_smfmac_f32_16x16x64_bf16 v[40:43], v[180:183], v[100:107], v192\n" \
  "s_waitcnt lgkmcnt(8)\n" \
  "v_smfmac_f32_16x16x64_bf16 v[44:47], v[180:183], v[108:115], v192\n" \
  "s_waitcnt lgkmcnt(6)\n" \
  "v_smfmac_f32_16x16x64_bf16 v[48:51], v[180:183], v[68:75], v192\n" \
  "s_waitcnt lgkmcnt(4)\n" \
  "v_smfmac_f32_16x16x64_bf16 v[52:55], v[180:183], v[76:83], v192\n" \
  "s_waitcnt lgkmcnt(2)\n" \
  "v_smfmac_f32_16x16x64_bf16 v[56:59], v[180:183], v[84:91], v192\n" \
  "s_waitcnt lgkmcnt(0)\n" \
  "v_smfmac_f32_16x16x64_bf16 v[60:63], v[180:183], v[92:99], v192\n" \
  "v_lshlrev_b32 v120, 16, v206\n" \
  "v_and_b32 v121, 0xffff0000, v206\n" \
  "v_lshlrev_b32 v122, 16, v207\n" \
  "v_and_b32 v123, 0xffff0000, v207\n" \
  "v_mov_b32 v124, 0xc0135761\n" \
  "v_mov_b32 v125, 0xc0135761\n" \
  "s_mov_b32 s52, 0xbdd2d3e8\n" \
  "s_mov_b32 s53, s52\n" \
  "s_lshl2_add_u32 s35, s33, s34\n" \
  "s_lshl_b32 s35, s35, 14\n" \
  "s_add_u32 s35, s35, s59\n" \
  "s_add_u32 s35, s35, 105676800\n" \
  "s_add_u32 s40, s24, s35\n" \
  "s_addc_u32 s41, s25, 0\n" \
  "v_pk_mul_f32 v[68:69], v[0:1], v[0:1]\n" \
  "v_pk_mul_f32 v[70:71], v[2:3], v[2:3]\n" \
  "v_pk_mul_f32 v[76:77], v[4:5], v[4:5]\n" \
  "v_pk_mul_f32 v[78:79], v[6:7], v[6:7]\n" \
  "v_pk_fma_f32 v[68:69], v[68:69], s[52:53], v[124:125]\n" \
  "v_pk_fma_f32 v[70:71], v[70:71], s[52:53], v[124:125]\n" \
  "v_pk_fma_f32 v[76:77], v[76:77], s[52:53], v[124:125]\n" \
  "v_pk_fma_f32 v[78:79], v[78:79], s[52:53], v[124:125]\n" \
  "v_pk_mul_f32 v[68:69], v[68:69], v[0:1]\n" \
  "v_pk_mul_f32 v[70:71], v[70:71], v[2:3]\n" \
  "v_pk_mul_f32 v[76:77], v[76:77], v[4:5]\n" \
  "v_pk_mul_f32 v[78:79], v[78:79], v[6:7]\n" \
  "v_exp_f32 v68, v68\n" \
  "v_exp_f32 v69, v69\n" \
  "v_pk_mul_f32 v[0:1], v[0:1], v[120:121]\n" \
  "v_exp_f32 v70, v70\n" \
  "v_exp_f32 v71, v71\n" \
  "v_pk_mul_f32 v[2:3], v[2:3], v[122:123]\n" \
  "v_exp_f32 v76, v76\n" \
  "v_exp_f32 v77, v77\n" \
  "v_pk_mul_f32 v[4:5], v[4:5], v[120:121]\n" \
  "v_exp_f32 v78, v78\n" \
  "v_exp_f32 v79, v79\n" \
  "v_pk_mul_f32 v[6:7], v[6:7], v[122:123]\n" \
  "v_pk_add_f32 v[68:69], v[68:69], 1.0 op_sel_hi:[1,0]\n" \
  "v_pk_add_f32 v[70:71], v[70:71], 1.0 op_sel_hi:[1,0]\n" \
  "v_pk_add_f32 v[76:77], v[76:77], 1.0 op_sel_hi:[1,0]\n" \
  "v_pk_add_f32 v[78:79], v[78:79], 1.0 op_sel_hi:[1,0]\n" \
  "v_rcp_f32 v68, v68\n" \
  "v_rcp_f32 v69, v69\n" \
  "v_rcp_f32 v70, v70\n" \
  "v_rcp_f32 v71, v71\n" \
  "v_rcp_f32 v76, v76\n" \
  "v_rcp_f32 v77, v77\n" \
  "v_rcp_f32 v78, v78\n" \
  "v_rcp_f32 v79, v79\n" \
  "v_pk_mul_f32 v[0:1], v[0:1], v[68:69]\n" \
  "v_pk_mul_f32 v[4:5], v[4:5], v[76:77]\n" \
  "v_pk_fma_f32 v[0:1], v[2:3], v[70:71], v[0:1]\n" \
  "v_pk_fma_f32 v[4:5], v[6:7], v[78:79], v[4:5]\n" \
  "v_add_f32 v0, v0, v1\n" \
  "v_add_f32 v4, v4, v5\n" \
  "v_pk_mul_f32 v[68:69], v[8:9], v[8:9]\n" \
  "v_pk_mul_f32 v[70:71], v[10:11], v[10:11]\n" \
  "v_pk_mul_f32 v[76:77], v[12:13], v[12:13]\n" \
  "v_pk_mul_f32 v[78:79], v[14:15], v[14:15]\n" \
  "v_pk_fma_f32 v[68:69], v[68:69], s[52:53], v[124:125]\n" \
  "v_pk_fma_f32 v[70:71], v[70:71], s[52:53], v[124:125]\n" \
  "v_pk_fma_f32 v[76:77], v[76:77], s[52:53], v[124:125]\n" \
  "v_pk_fma_f32 v[78:79], v[78:79], s[52:53], v[124:125]\n" \
  "v_pk_mul_f32 v[68:69], v[68:69], v[8:9]\n" \
  "v_pk_mul_f32 v[70:71], v[70:71], v[10:11]\n" \
  "v_pk_mul_f32 v[76:77], v[76:77], v[12:13]\n" \
  "v_pk_mul_f32 v[78:79], v[78:79], v[14:15]\n" \
  "v_exp_f32 v68, v68\n" \
  "v_exp_f32 v69, v69\n" \
  "v_pk_mul_f32 v[8:9], v[8:9], v[120:121]\n" \
  "v_exp_f32 v70, v70\n" \
  "v_exp_f32 v71, v71\n" \
  "v_pk_mul_f32 v[10:11], v[10:11], v[122:123]\n" \
  "v_exp_f32 v76, v76\n" \
  "v_exp_f32 v77, v77\n" \
  "v_pk_mul_f32 v[12:13], v[12:13], v[120:121]\n" \
  "v_exp_f32 v78, v78\n" \
  "v_exp_f32 v79, v79\n" \
  "v_pk_mul_f32 v[14:15], v[14:15], v[122:123]\n" \
  "v_pk_add_f32 v[68:69], v[68:69], 1.0 op_sel_hi:[1,0]\n" \
  "v_pk_add_f32 v[70:71], v[70:71], 1.0 op_sel_hi:[1,0]\n" \
  "v_pk_add_f32 v[76:77], v[76:77], 1.0 op_sel_hi:[1,0]\n" \
  "v_pk_add_f32 v[78:79], v[78:79], 1.0 op_sel_hi:[1,0]\n" \
  "v_rcp_f32 v68, v68\n" \
  "v_rcp_f32 v69, v69\n" \
  "v_rcp_f32 v70, v70\n" \
  "v_rcp_f32 v71, v71\n" \
  "v_rcp_f32 v76, v76\n" \
  "v_rcp_f32 v77, v77\n" \
  "v_rcp_f32 v78, v78\n" \
  "v_rcp_f32 v79, v79\n" \
  "v_pk_mul_f32 v[8:9], v[8:9], v[68:69]\n" \
  "v_pk_mul_f32 v[12:13], v[12:13], v[76:77]\n" \
  "v_pk_fma_f32 v[8:9], v[10:11], v[70:71], v[8:9]\n" \
  "v_pk_fma_f32 v[12:13], v[14:15], v[78:79], v[12:13]\n" \
  "v_add_f32 v8, v8, v9\n" \
  "v_add_f32 v12, v12, v13\n" \
  "v_pk_mul_f32 v[68:69], v[16:17], v[16:17]\n" \
  "v_pk_mul_f32 v[70:71], v[18:19], v[18:19]\n" \
  "v_pk_mul_f32 v[76:77], v[20:21], v[20:21]\n" \
  "v_pk_mul_f32 v[78:79], v[22:23], v[22:23]\n" \
  "v_pk_fma_f32 v[68:69], v[68:69], s[52:53], v[124:125]\n" \
  "v_pk_fma_f32 v[70:71], v[70:71], s[52:53], v[124:125]\n" \
  "v_pk_fma_f32 v[76:77], v[76:77], s[52:53], v[124:125]\n" \
  "v_pk_fma_f32 v[78:79], v[78:79], s[52:53], v[124:125]\n" \
  "v_pk_mul_f32 v[68:69], v[68:69], v[16:17]\n" \
  "v_pk_mul_f32 v[70:71], v[70:71], v[18:19]\n" \
  "v_pk_mul_f32 v[76:77], v[76:77], v[20:21]\n" \
  "v_pk_mul_f32 v[78:79], v[78:79], v[22:23]\n" \
  "v_exp_f32 v68, v68\n" \
  "v_exp_f32 v69, v69\n" \
  "v_pk_mul_f32 v[16:17], v[16:17], v[120:121]\n" \
  "v_exp_f32 v70, v70\n" \
  "v_exp_f32 v71, v71\n" \
  "v_pk_mul_f32 v[18:19], v[18:19], v[122:123]\n" \
  "v_exp_f32 v76, v76\n" \
  "v_exp_f32 v77, v77\n" \
  "v_pk_mul_f32 v[20:21], v[20:21], v[120:121]\n" \
  "v_exp_f32 v78, v78\n" \
  "v_exp_f32 v79, v79\n" \
  "v_pk_mul_f32 v[22:23], v[22:23], v[122:123]\n" \
  "v_pk_add_f32 v[68:69], v[68:69], 1.0 op_sel_hi:[1,0]\n" \
  "v_pk_add_f32 v[70:71], v[70:71], 1.0 op_sel_hi:[1,0]\n" \
  "v_pk_add_f32 v[76:77], v[76:77], 1.0 op_sel_hi:[1,0]\n" \
  "v_pk_add_f32 v[78:79], v[78:79], 1.0 op_sel_hi:[1,0]\n" \
  "v_rcp_f32 v68, v68\n" \
  "v_rcp_f32 v69, v69\n" \
  "v_rcp_f32 v70, v70\n" \
  "v_rcp_f32 v71, v71\n" \
  "v_rcp_f32 v76, v76\n" \
  "v_rcp_f32 v77, v77\n" \
  "v_rcp_f32 v78, v78\n" \
  "v_rcp_f32 v79, v79\n" \
  "v_pk_mul_f32 v[16:17], v[16:17], v[68:69]\n" \
  "v_pk_mul_f32 v[20:21], v[20:21], v[76:77]\n" \
  "v_pk_fma_f32 v[16:17], v[18:19], v[70:71], v[16:17]\n" \
  "v_pk_fma_f32 v[20:21], v[22:23], v[78:79], v[20:21]\n" \
  "v_add_f32 v16, v16, v17\n" \
  "v_add_f32 v20, v20, v21\n" \
  "v_pk_mul_f32 v[68:69], v[24:25], v[24:25]\n" \
  "v_pk_mul_f32 v[70:71], v[26:27], v[26:27]\n" \
  "v_pk_mul_f32 v[76:77], v[28:29], v[28:29]\n" \
  "v_pk_mul_f32 v[78:79], v[30:31], v[30:31]\n" \
  "v_pk_fma_f32 v[68:69], v[68:69], s[52:53], v[124:125]\n" \
  "v_pk_fma_f32 v[70:71], v[70:71], s[52:53], v[124:125]\n" \
  "v_pk_fma_f32 v[76:77], v[76:77], s[52:53], v[124:125]\n" \
  "v_pk_fma_f32 v[78:79], v[78:79], s[52:53], v[124:125]\n" \
  "v_pk_mul_f32 v[68:69], v[68:69], v[24:25]\n" \
  "v_pk_mul_f32 v[70:71], v[70:71], v[26:27]\n" \
  "v_pk_mul_f32 v[76:77], v[76:77], v[28:29]\n" \
  "v_pk_mul_f32 v[78:79], v[78:79], v[30:31]\n" \
  "v_exp_f32 v68, v68\n" \
  "v_exp_f32 v69, v69\n" \
  "v_pk_mul_f32 v[24:25], v[24:25], v[120:121]\n" \
  "v_exp_f32 v70, v70\n" \
  "v_exp_f32 v71, v71\n" \
  "v_pk_mul_f32 v[26:27], v[26:27], v[122:123]\n" \
  "v_exp_f32 v76, v76\n" \
  "v_exp_f32 v77, v77\n" \
  "v_pk_mul_f32 v[28:29], v[28:29], v[120:121]\n" \
  "v_exp_f32 v78, v78\n" \
  "v_exp_f32 v79, v79\n" \
  "v_pk_mul_f32 v[30:31], v[30:31], v[122:123]\n" \
  "v_pk_add_f32 v[68:69], v[68:69], 1.0 op_sel_hi:[1,0]\n" \
  "v_pk_add_f32 v[70:71], v[70:71], 1.0 op_sel_hi:[1,0]\n" \
  "v_pk_add_f32 v[76:77], v[76:77], 1.0 op_sel_hi:[1,0]\n" \
  "v_pk_add_f32 v[78:79], v[78:79], 1.0 op_sel_hi:[1,0]\n" \
  "v_rcp_f32 v68, v68\n" \
  "v_rcp_f32 v69, v69\n" \
  "v_rcp_f32 v70, v70\n" \
  "v_rcp_f32 v71, v71\n" \
  "v_rcp_f32 v76, v76\n" \
  "v_rcp_f32 v77, v77\n" \
  "v_rcp_f32 v78, v78\n" \
  "v_rcp_f32 v79, v79\n" \
  "v_pk_mul_f32 v[24:25], v[24:25], v[68:69]\n" \
  "v_pk_mul_f32 v[28:29], v[28:29], v[76:77]\n" \
  "v_pk_fma_f32 v[24:25], v[26:27], v[70:71], v[24:25]\n" \
  "v_pk_fma_f32 v[28:29], v[30:31], v[78:79], v[28:29]\n" \
  "v_add_f32 v24, v24, v25\n" \
  "v_add_f32 v28, v28, v29\n" \
  "v_permlane16_swap_b32 v0, v16\n" \
  "v_permlane16_swap_b32 v4, v20\n" \
  "v_permlane16_swap_b32 v8, v24\n" \
  "v_permlane16_swap_b32 v12, v28\n" \
  "v_add_f32 v100, v0, v16\n" \
  "v_add_f32 v101, v4, v20\n" \
  "v_add_f32 v102, v8, v24\n" \
  "v_add_f32 v103, v12, v28\n" \
  "v_min_u32 v100, -2, v100\n" \
  "v_min_u32 v101, -2, v101\n" \
  "v_min_u32 v102, -2, v102\n" \
  "v_min_u32 v103, -2, v103\n" \
  "global_store_dwordx4 v188, v[100:103], s[40:41] offset:0 sc1\n" \
  "v_pk_mul_f32 v[68:69], v[32:33], v[32:33]\n" \
  "v_pk_mul_f32 v[70:71], v[34:35], v[34:35]\n" \
  "v_pk_mul_f32 v[76:77], v[36:37], v[36:37]\n" \
  "v_pk_mul_f32 v[78:79], v[38:39], v[38:39]\n" \
  "v_pk_fma_f32 v[68:69], v[68:69], s[52:53], v[124:125]\n" \
  "v_pk_fma_f32 v[70:71], v[70:71], s[52:53], v[124:125]\n" \
  "v_pk_fma_f32 v[76:77], v[76:77], s[52:53], v[124:125]\n" \
  "v_pk_fma_f32 v[78:79], v[78:79], s[52:53], v[124:125]\n" \
  "v_pk_mul_f32 v[68:69], v[68:69], v[32:33]\n" \
  "v_pk_mul_f32 v[70:71], v[70:71], v[34:35]\n" \
  "v_pk_mul_f32 v[76:77], v[76:77], v[36:37]\n" \
  "v_pk_mul_f32 v[78:79], v[78:79], v[38:39]\n" \
  "v_exp_f32 v68, v68\n" \
  "v_exp_f32 v69, v69\n" \
  "v_pk_mul_f32 v[32:33], v[32:33], v[120:121]\n" \
  "v_exp_f32 v70, v70\n" \
  "v_exp_f32 v71, v71\n" \
  "v_pk_mul_f32 v[34:35], v[34:35], v[122:123]\n" \
  "v_exp_f32 v76, v76\n" \
  "v_exp_f32 v77, v77\n" \
  "v_pk_mul_f32 v[36:37], v[36:37], v[120:121]\n" \
  "v_exp_f32 v78, v78\n" \
  "v_exp_f32 v79, v79\n" \
  "v_pk_mul_f32 v[38:39], v[38:39], v[122:123]\n" \
  "v_pk_add_f32 v[68:69], v[68:69], 1.0 op_sel_hi:[1,0]\n" \
  "v_pk_add_f32 v[70:71], v[70:71], 1.0 op_sel_hi:[1,0]\n" \
  "v_pk_add_f32 v[76:77], v[76:77], 1.0 op_sel_hi:[1,0]\n" \
  "v_pk_add_f32 v[78:79], v[78:79], 1.0 op_sel_hi:[1,0]\n" \
  "v_rcp_f32 v68, v68\n" \
  "v_rcp_f32 v69, v69\n" \
  "v_rcp_f32 v70, v70\n" \
  "v_rcp_f32 v71, v71\n" \
  "v_rcp_f32 v76, v76\n" \
  "v_rcp_f32 v77, v77\n" \
  "v_rcp_f32 v78, v78\n" \
  "v_rcp_f32 v79, v79\n" \
  "v_pk_mul_f32 v[32:33], v[32:33], v[68:69]\n" \
  "v_pk_mul_f32 v[36:37], v[36:37], v[76:77]\n" \
  "v_pk_fma_f32 v[32:33], v[34:35], v[70:71], v[32:33]\n" \
  "v_pk_fma_f32 v[36:37], v[38:39], v[78:79], v[36:37]\n" \
  "v_add_f32 v32, v32, v33\n" \
  "v_add_f32 v36, v36, v37\n" \
  "v_pk_mul_f32 v[68:69], v[40:41], v[40:41]\n" \
  "v_pk_mul_f32 v[70:71], v[42:43], v[42:43]\n" \
  "v_pk_mul_f32 v[76:77], v[44:45], v[44:45]\n" \
  "v_pk_mul_f32 v[78:79], v[46:47], v[46:47]\n" \
  "v_pk_fma_f32 v[68:69], v[68:69], s[52:53], v[124:125]\n" \
  "v_pk_fma_f32 v[70:71], v[70:71], s[52:53], v[124:125]\n" \
  "v_pk_fma_f32 v[76:77], v[76:77], s[52:53], v[124:125]\n" \
  "v_pk_fma_f32 v[78:79], v[78:79], s[52:53], v[124:125]\n" \
  "v_pk_mul_f32 v[68:69], v[68:69], v[40:41]\n" \
  "v_pk_mul_f32 v[70:71], v[70:71], v[42:43]\n" \
  "v_pk_mul_f32 v[76:77], v[76:77], v[44:45]\n" \
  "v_pk_mul_f32 v[78:79], v[78:79], v[46:47]\n" \
  "v_exp_f32 v68, v68\n" \
  "v_exp_f32 v69, v69\n" \
  "v_pk_mul_f32 v[40:41], v[40:41], v[120:121]\n" \
  "v_exp_f32 v70, v70\n" \
  "v_exp_f32 v71, v71\n" \
  "v_pk_mul_f32 v[42:43], v[42:43], v[122:123]\n" \
  "v_exp_f32 v76, v76\n" \
  "v_exp_f32 v77, v77\n" \
  "v_pk_mul_f32 v[44:45], v[44:45], v[120:121]\n" \
  "v_exp_f32 v78, v78\n" \
  "v_exp_f32 v79, v79\n" \
  "v_pk_mul_f32 v[46:47], v[46:47], v[122:123]\n" \
  "v_pk_add_f32 v[68:69], v[68:69], 1.0 op_sel_hi:[1,0]\n" \
  "v_pk_add_f32 v[70:71], v[70:71], 1.0 op_sel_hi:[1,0]\n" \
  "v_pk_add_f32 v[76:77], v[76:77], 1.0 op_sel_hi:[1,0]\n" \
  "v_pk_add_f32 v[78:79], v[78:79], 1.0 op_sel_hi:[1,0]\n" \
  "v_rcp_f32 v68, v68\n" \
  "v_rcp_f32 v69, v69\n" \
  "v_rcp_f32 v70, v70\n" \
  "v_rcp_f32 v71, v71\n" \
  "v_rcp_f32 v76, v76\n" \
  "v_rcp_f32 v77, v77\n" \
  "v_rcp_f32 v78, v78\n" \
  "v_rcp_f32 v79, v79\n" \
  "v_pk_mul_f32 v[40:41], v[40:41], v[68:69]\n" \
  "v_pk_mul_f32 v[44:45], v[44:45], v[76:77]\n" \
  "v_pk_fma_f32 v[40:41], v[42:43], v[70:71], v[40:41]\n" \
  "v_pk_fma_f32 v[44:45], v[46:47], v[78:79], v[44:45]\n" \
  "v_add_f32 v40, v40, v41\n" \
  "v_add_f32 v44, v44, v45\n" \
  "v_pk_mul_f32 v[68:69], v[48:49], v[48:49]\n" \
  "v_pk_mul_f32 v[70:71], v[50:51], v[50:51]\n" \
  "v_pk_mul_f32 v[76:77], v[52:53], v[52:53]\n" \
  "v_pk_mul_f32 v[78:79], v[54:55], v[54:55]\n" \
  "v_pk_fma_f32 v[68:69], v[68:69], s[52:53], v[124:125]\n" \
  "v_pk_fma_f32 v[70:71], v[70:71], s[52:53], v[124:125]\n" \
  "v_pk_fma_f32 v[76:77], v[76:77], s[52:53], v[124:125]\n" \
  "v_pk_fma_f32 v[78:79], v[78:79], s[52:53], v[124:125]\n" \
  "v_pk_mul_f32 v[68:69], v[68:69], v[48:49]\n" \
  "v_pk_mul_f32 v[70:71], v[70:71], v[50:51]\n" \
  "v_pk_mul_f32 v[76:77], v[76:77], v[52:53]\n" \
  "v_pk_mul_f32 v[78:79], v[78:79], v[54:55]\n" \
  "v_exp_f32 v68, v68\n" \
  "v_exp_f32 v69, v69\n" \
  "v_pk_mul_f32 v[48:49], v[48:49], v[120:121]\n" \
  "v_exp_f32 v70, v70\n" \
  "v_exp_f32 v71, v71\n" \
  "v_pk_mul_f32 v[50:51], v[50:51], v[122:123]\n" \
  "v_exp_f32 v76, v76\n" \
  "v_exp_f32 v77, v77\n" \
  "v_pk_mul_f32 v[52:53], v[52:53], v[120:121]\n" \
  "v_exp_f32 v78, v78\n" \
  "v_exp_f32 v79, v79\n" \
  "v_pk_mul_f32 v[54:55], v[54:55], v[122:123]\n" \
  "v_pk_add_f32 v[68:69], v[68:69], 1.0 op_sel_hi:[1,0]\n" \
  "v_pk_add_f32 v[70:71], v[70:71], 1.0 op_sel_hi:[1,0]\n" \
  "v_pk_add_f32 v[76:77], v[76:77], 1.0 op_sel_hi:[1,0]\n" \
  "v_pk_add_f32 v[78:79], v[78:79], 1.0 op_sel_hi:[1,0]\n" \
  "v_rcp_f32 v68, v68\n" \
  "v_rcp_f32 v69, v69\n" \
  "v_rcp_f32 v70, v70\n" \
  "v_rcp_f32 v71, v71\n" \
  "v_rcp_f32 v76, v76\n" \
  "v_rcp_f32 v77, v77\n" \
  "v_rcp_f32 v78, v78\n" \
  "v_rcp_f32 v79, v79\n" \
  "v_pk_mul_f32 v[48:49], v[48:49], v[68:69]\n" \
  "v_pk_mul_f32 v[52:53], v[52:53], v[76:77]\n" \
  "v_pk_fma_f32 v[48:49], v[50:51], v[70:71], v[48:49]\n" \
  "v_pk_fma_f32 v[52:53], v[54:55], v[78:79], v[52:53]\n" \
  "v_add_f32 v48, v48, v49\n" \
  "v_add_f32 v52, v52, v53\n" \
  "v_pk_mul_f32 v[68:69], v[56:57], v[56:57]\n" \
  "v_pk_mul_f32 v[70:71], v[58:59], v[58:59]\n" \
  "v_pk_mul_f32 v[76:77], v[60:61], v[60:61]\n" \
  "v_pk_mul_f32 v[78:79], v[62:63], v[62:63]\n" \
  "v_pk_fma_f32 v[68:69], v[68:69], s[52:53], v[124:125]\n" \
  "v_pk_fma_f32 v[70:71], v[70:71], s[52:53], v[124:125]\n" \
  "v_pk_fma_f32 v[76:77], v[76:77], s[52:53], v[124:125]\n" \
  "v_pk_fma_f32 v[78:79], v[78:79], s[52:53], v[124:125]\n" \
  "v_pk_mul_f32 v[68:69], v[68:69], v[56:57]\n" \
  "v_pk_mul_f32 v[70:71], v[70:71], v[58:59]\n" \
  "v_pk_mul_f32 v[76:77], v[76:77], v[60:61]\n" \
  "v_pk_mul_f32 v[78:79], v[78:79], v[62:63]\n" \
  "v_exp_f32 v68, v68\n" \
  "v_exp_f32 v69, v69\n" \
  "v_pk_mul_f32 v[56:57], v[56:57], v[120:121]\n" \
  "v_exp_f32 v70, v70\n" \
  "v_exp_f32 v71, v71\n" \
  "v_pk_mul_f32 v[58:59], v[58:59], v[122:123]\n" \
  "v_exp_f32 v76, v76\n" \
  "v_exp_f32 v77, v77\n" \
  "v_pk_mul_f32 v[60:61], v[60:61], v[120:121]\n" \
  "v_exp_f32 v78, v78\n" \
  "v_exp_f32 v79, v79\n" \
  "v_pk_mul_f32 v[62:63], v[62:63], v[122:123]\n" \
  "v_pk_add_f32 v[68:69], v[68:69], 1.0 op_sel_hi:[1,0]\n" \
  "v_pk_add_f32 v[70:71], v[70:71], 1.0 op_sel_hi:[1,0]\n" \
  "v_pk_add_f32 v[76:77], v[76:77], 1.0 op_sel_hi:[1,0]\n" \
  "v_pk_add_f32 v[78:79], v[78:79], 1.0 op_sel_hi:[1,0]\n" \
  "v_rcp_f32 v68, v68\n" \
  "v_rcp_f32 v69, v69\n" \
  "v_rcp_f32 v70, v70\n" \
  "v_rcp_f32 v71, v71\n" \
  "v_rcp_f32 v76, v76\n" \
  "v_rcp_f32 v77, v77\n" \
  "v_rcp_f32 v78, v78\n" \
  "v_rcp_f32 v79, v79\n" \
  "v_pk_mul_f32 v[56:57], v[56:57], v[68:69]\n" \
  "v_pk_mul_f32 v[60:61], v[60:61], v[76:77]\n" \
  "v_pk_fma_f32 v[56:57], v[58:59], v[70:71], v[56:57]\n" \
  "v_pk_fma_f32 v[60:61], v[62:63], v[78:79], v[60:61]\n" \
  "v_add_f32 v56, v56, v57\n" \
  "v_add_f32 v60, v60, v61\n" \
  "v_permlane16_swap_b32 v32, v48\n" \
  "v_permlane16_swap_b32 v36, v52\n" \
  "v_permlane16_swap_b32 v40, v56\n" \
  "v_permlane16_swap_b32 v44, v60\n" \
  "v_add_f32 v104, v32, v48\n" \
  "v_add_f32 v105, v36, v52\n" \
  "v_add_f32 v106, v40, v56\n" \
  "v_add_f32 v107, v44, v60\n" \
  "v_min_u32 v104, -2, v104\n" \
  "v_min_u32 v105, -2, v105\n" \
  "v_min_u32 v106, -2, v106\n" \
  "v_min_u32 v107, -2, v107\n" \
  "global_store_dwordx4 v188, v[104:107], s[40:41] offset:1024 sc1\n" \
  "v_lshrrev_b32 v0, 4, v188\n" \
  "v_lshrrev_b32 v1, 4, v0\n" \
  "v_and_b32 v2, 15, v0\n" \
  "v_lshlrev_b32 v2, 4, v2\n" \
  "v_lshl_add_u32 v1, v1, 17, v2\n" \
  "s_lshl_b32 s35, s27, 19\n" \
  "s_lshl_b32 s36, s26, 8\n" \
  "s_add_u32 s35, s35, s36\n" \
  "s_add_u32 s35, s35, 105676800\n" \
  "s_add_u32 s40, s24, s35\n" \
  "s_addc_u32 s41, s25, 0\n" \
  "s_add_u32 s42, s40, 65536\n" \
  "s_addc_u32 s43, s41, 0\n" \
  "s_add_u32 s44, s24, 109871104\n" \
  "s_addc_u32 s45, s25, 0\n" \
  "s_add_u32 s46, s44, 65536\n" \
  "s_addc_u32 s47, s45, 0\n" \
  "s_lshr_b32 s35, s26, 2\n" \
  "s_lshl_b32 s35, s35, 8\n" \
  "s_and_b32 s36, s26, 1\n" \
  "s_lshl_b32 s36, s36, 7\n" \
  "s_add_u32 s35, s35, s36\n" \
  "s_and_b32 s36, s26, 2\n" \
  "s_lshl3_add_u32 s35, s36, s35\n" \
  "v_and_b32 v2, 3, v0\n" \
  "v_lshrrev_b32 v3, 2, v0\n" \
  "v_lshl_add_u32 v2, v2, 5, v3\n" \
  "v_add_u32 v2, s35, v2\n" \
  "v_lshlrev_b32 v3, 2, v2\n" \
  "s_cmp_eq_u32 s27, 0\n" \
  "s_cselect_b32 s35, -1, 0\n" \
  "v_and_b32 v3, s35, v3\n" \
  "v_mov_b32 v4, 0\n" \
  "global_load_ushort v4, v4, s[20:21]\n" \
  "s_mov_b32 s50, 0x10000\n" \
  "LF_poll:\n" \
  "global_load_dwordx4 v[8:11], v1, s[40:41] sc1\n" \
  "global_load_dwordx4 v[12:15], v1, s[42:43] sc1\n" \
  "global_load_dword v16, v3, s[44:45] sc1\n" \
  "global_load_dword v17, v3, s[46:47] sc1\n" \
  "s_waitcnt vmcnt(0)\n" \
  "v_max3_u32 v5, v8, v9, v10\n" \
  "v_max3_u32 v6, v11, v12, v13\n" \
  "v_max3_u32 v5, v5, v14, v15\n" \
  "v_max3_u32 v6, v6, v16, v17\n" \
  "v_max_u32 v5, v5, v6\n" \
  "v_cmp_eq_u32 vcc, -1, v5\n" \
  "s_sub_u32 s50, s50, 1\n" \
  "s_cbranch_scc1 LF_go\n" \
  "s_nop 2\n" \
  "s_cbranch_vccz LF_go\n" \
  "s_sleep 2\n" \
  "s_branch LF_poll\n" \
  "LF_go:\n" \
  "v_pk_add_f32 v[8:9], v[8:9], v[12:13]\n" \
  "v_pk_add_f32 v[10:11], v[10:11], v[14:15]\n" \
  "s_nop 0\n" \
  "v_permlane16_swap_b32 v8, v9\n" \
  "v_permlane16_swap_b32 v10, v11\n" \
  "v_add_f32 v8, v8, v9\n" \
  "v_add_f32 v10, v10, v11\n" \
  "s_nop 1\n" \
  "v_permlane32_swap_b32 v8, v10\n" \
  "v_add_f32 v8, v8, v10\n" \
  "v_and_b32 v5, 15, v0\n" \
  "v_lshrrev_b32 v6, 4, v0\n" \
  "v_lshl_add_u32 v5, v5, 2, v6\n" \
  "s_lshl_b32 s35, s27, 8\n" \
  "v_lshl_add_u32 v5, v5, 2, s35\n" \
  "ds_write_b32 v5, v8\n" \
  "s_waitcnt lgkmcnt(0)\n" \
  "s_barrier\n" \
  "s_cmp_lg_u32 s27, 0\n" \
  "s_cbranch_scc1 LF_end\n" \
  "v_lshlrev_b32 v5, 2, v0\n" \
  "ds_read2st64_b32 v[8:9], v5 offset0:0 offset1:1\n" \
  "ds_read2st64_b32 v[10:11], v5 offset0:2 offset1:3\n" \
  "ds_read2st64_b32 v[12:13], v5 offset0:4 offset1:5\n" \
  "ds_read2st64_b32 v[14:15], v5 offset0:6 offset1:7\n" \
  "v_add_f32 v16, v16, v17\n" \
  "v_lshlrev_b32 v4, 16, v4\n" \
  "v_cvt_pk_bf16_f32 v16, v16, v16\n" \
  "v_lshlrev_b32 v2, 1, v2\n" \
  "s_waitcnt lgkmcnt(0)\n" \
  "v_pk_add_f32 v[8:9], v[8:9], v[10:11]\n" \
  "v_pk_add_f32 v[12:13], v[12:13], v[14:15]\n" \
  "v_pk_add_f32 v[8:9], v[8:9], v[12:13]\n" \
  "v_add_f32 v8, v8, v9\n" \
  "v_cvt_pk_bf16_f32 v8, v8, v8\n" \
  "v_lshlrev_b32 v8, 16, v8\n" \
  "v_add_f32 v8, v8, v4\n" \
  "v_cvt_pk_bf16_f32 v8, v8, v8\n" \
  "v_lshlrev_b32 v8, 16, v8\n" \
  "v_lshlrev_b32 v16, 16, v16\n" \
  "v_add_f32 v8, v8, v16\n" \
  "v_cvt_pk_bf16_f32 v8, v8, v8\n" \
  "global_store_short v2, v8, s[22:23]\n" \
  "LF_end:\n" \
  "s_branch LZ_end\n" \
  "LG_chk:\n" \
  "s_mov_b64 exec, 0xff\n" \
  "global_load_dword v198, v197, s[48:49] offset:2052 sc1\n" \
  "s_mov_b64 exec, -1\n" \
  "s_waitcnt vmcnt(0)\n" \
  "s_mov_b32 s50, 0x10000\n" \
  "LG_p:\n" \
  "s_mov_b64 exec, 0xff\n" \
  "v_cmp_le_u32 vcc, 32, v198\n" \
  "s_mov_b64 exec, -1\n" \
  "s_sub_u32 s50, s50, 1\n" \
  "s_cselect_b32 s35, 0xff, vcc_lo\n" \
  "s_cmp_eq_u32 s35, 0xff\n" \
  "s_cbranch_scc1 LG_p_x\n" \
  "s_sleep 2\n" \
  "s_mov_b64 exec, 0xff\n" \
  "global_load_dword v198, v197, s[48:49] offset:2052 sc1\n" \
  "s_mov_b64 exec, -1\n" \
  "s_waitcnt vmcnt(0)\n" \
  "s_branch LG_p\n" \
  "LG_p_x:\n" \
  "s_branch LG_ret\n" \
  "LZ_end:\n" \
  "s_waitcnt vmcnt(0) lgkmcnt(0)\n"

#define R10(p, b) p #b "0", p #b "1", p #b "2", p #b "3", p #b "4", p #b "5", p #b "6", p #b "7", p #b "8", p #b "9"
__global__ __launch_bounds__(512, 2) void k1(const int* __restrict__ idx, const unsigned short* __restrict__ table,
                                             const unsigned short* __restrict__ w_p, const unsigned short* __restrict__ w1,
                                             const unsigned short* __restrict__ b1, const unsigned short* __restrict__ w2,
                                             const unsigned short* __restrict__ b2, __hip_bfloat16* __restrict__ out, char* __restrict__ ws) {
  __shared__ __attribute__((aligned(1024))) char smem[163840];
  unsigned tid = threadIdx.x;
  asm volatile(K_ASM
               : "+{v0}"(tid)
               : "{s[8:9]}"(idx), "{s[10:11]}"(table), "{s[12:13]}"(w_p), "{s[14:15]}"(w1), "{s[16:17]}"(b1), "{s[18:19]}"(w2), "{s[20:21]}"(b2),
                 "{s[22:23]}"(out), "{s[24:25]}"(ws), "{s26}"(blockIdx.x), "s"((unsigned)(uintptr_t)smem)
               : "memory", "scc", "vcc", "s4", "s5", "s6", "s7", "s27", "s28", "s29", "s30", "s31", "s33", "s34", "s35", "s36", "s37", "s38", "s39", R10("s", 4), R10("s", 5), R10("s", 6), R10("s", 7), R10("s", 8), R10("s", 9),
                 "v1", "v2", "v3", "v4", "v5", "v6", "v7", "v8", "v9", R10("v", 1), R10("v", 2), R10("v", 3), R10("v", 4), R10("v", 5), R10("v", 6), R10("v", 7), R10("v", 8), R10("v", 9),
                 R10("v", 10), R10("v", 11), R10("v", 12), R10("v", 13), R10("v", 14), R10("v", 15), R10("v", 16), R10("v", 17), R10("v", 18), R10("v", 19),
                 R10("v", 20), R10("v", 21), R10("v", 22), R10("v", 23), R10("v", 24), "v250", "v251", "v252", "v253", "v254", "v255");
}

extern "C" void kernel_launch(void* const* d_in, const int* in_sizes, int n_in, void* d_out, int out_size, void* d_ws,
                              size_t ws_size, hipStream_t stream) {
  k1<<<dim3(256), dim3(512), 0, stream>>>((const int*)d_in[0], (const unsigned short*)d_in[1], (const unsigned short*)d_in[2], (const unsigned short*)d_in[3],
                                          (const unsigned short*)d_in[4], (const unsigned short*)d_in[5], (const unsigned short*)d_in[6], (__hip_bfloat16*)d_out, (char*)d_ws);
}
```

```cpp
#include <hip/hip_runtime.h>
#include <hip/hip_bf16.h>
#include <stdint.h>

#define K_ASM \
  "LZ_first:\n" \
  "v_readfirstlane_b32 s27, v0\n" \
  "v_and_b32 v255, 63, v0\n" \
  "s_and_b32 s29, s26, 7\n" \
  "s_lshr_b32 s30, s26, 3\n" \
  "s_lshr_b32 s27, s27, 6\n" \
  "s_and_b32 s31, s30, 1\n" \
  "s_and_b32 s28, s27, 3\n" \
  "s_lshr_b32 s35, s30, 1\n" \
  "s_lshl3_add_u32 s7, s35, s29\n" \
  "v_lshrrev_b32 v1, 3, v255\n" \
  "v_and_b32 v2, 7, v255\n" \
  "v_lshlrev_b32 v1, 8, v1\n" \
  "v_lshl_or_b32 v254, v2, 4, v1\n" \
  "s_lshl_b32 s35, s31, 7\n" \
  "s_add_u32 s40, s10, s35\n" \
  "s_addc_u32 s41, s11, 0\n" \
  "s_mul_i32 s36, s7, 37\n" \
  "s_and_b32 s36, s36, 63\n" \
  "s_add_u32 s36, s36, s28\n" \
  "s_cmp_ge_u32 s27, 4\n" \
  "s_cbranch_scc1 LB_start\n" \
  "v_lshlrev_b32 v1, 4, v255\n" \
  "s_lshl_b32 s35, s28, 10\n" \
  "v_add_u32 v1, s35, v1\n" \
  "s_add_u32 m0, s35, 138240\n" \
  "s_nop 0\n" \
  "global_load_lds_dwordx4 v1, s[12:13]\n" \
  "v_add_u32 v1, 0x1000, v1\n" \
  "s_add_u32 m0, m0, 0x1000\n" \
  "s_nop 0\n" \
  "global_load_lds_dwordx4 v1, s[12:13]\n" \
  "v_add_u32 v1, 0x1000, v1\n" \
  "s_add_u32 m0, m0, 0x1000\n" \
  "s_nop 0\n" \
  "global_load_lds_dwordx4 v1, s[12:13]\n" \
  "v_add_u32 v1, 0x1000, v1\n" \
  "s_add_u32 m0, m0, 0x1000\n" \
  "s_nop 0\n" \
  "global_load_lds_dwordx4 v1, s[12:13]\n" \
  "v_add_u32 v1, 0x1000, v1\n" \
  "s_add_u32 m0, m0, 0x1000\n" \
  "v_min_u32 v1, 19984, v1\n" \
  "global_load_lds_dwordx4 v1, s[12:13]\n" \
  "v_and_b32 v252, 15, v255\n" \
  "v_lshrrev_b32 v253, 4, v255\n" \
  "v_mul_u32_u24 v2, 400, v253\n" \
  "v_min_u32 v3, 8, v252\n" \
  "v_lshl_add_u32 v250, v252, 4, v2\n" \
  "v_lshl_add_u32 v251, v3, 4, v2\n" \
  "s_lshl2_add_u32 s35, s7, s28\n" \
  "s_mul_i32 s35, s35, 12800\n" \
  "s_add_u32 s44, s8, s35\n" \
  "s_addc_u32 s45, s9, 0\n" \
  "global_load_dwordx4 v[232:235], v250, s[44:45] offset:0\n" \
  "global_load_dwordx4 v[236:239], v251, s[44:45] offset:256\n" \
  "global_load_dwordx4 v[240:243], v250, s[44:45] offset:1600\n" \
  "global_load_dwordx4 v[244:247], v251, s[44:45] offset:1856\n" \
  "s_setprio 2\n" \
  "v_lshrrev_b32 v1, 2, v252\n" \
  "v_and_b32 v2, 3, v252\n" \
  "v_cmp_eq_u32 vcc, v1, v253\n" \
  "v_mov_b32 v4, 0x3f80\n" \
  "v_and_b32 v13, 1, v253\n" \
  "v_lshlrev_b32 v14, 1, v13\n" \
  "v_xor_b32 v2, v2, v14\n" \
  "v_cndmask_b32 v2, 4, v2, vcc\n" \
  "v_cmp_eq_u32 vcc, 0, v2\n" \
  "s_nop 1\n" \
  "v_cndmask_b32 v216, 0, v4, vcc\n" \
  "v_lshlrev_b32 v220, 16, v216\n" \
  "v_cmp_eq_u32 vcc, 1, v2\n" \
  "s_nop 1\n" \
  "v_cndmask_b32 v217, 0, v4, vcc\n" \
  "v_lshlrev_b32 v221, 16, v217\n" \
  "v_cmp_eq_u32 vcc, 2, v2\n" \
  "s_nop 1\n" \
  "v_cndmask_b32 v218, 0, v4, vcc\n" \
  "v_lshlrev_b32 v222, 16, v218\n" \
  "v_cmp_eq_u32 vcc, 3, v2\n" \
  "s_nop 1\n" \
  "v_cndmask_b32 v219, 0, v4, vcc\n" \
  "v_lshlrev_b32 v223, 16, v219\n" \
  "v_and_b32 v1, 2, v252\n" \
  "v_lshlrev_b32 v1, 3, v1\n" \
  "v_lshlrev_b32 v1, v1, v4\n" \
  "v_and_b32 v2, 0x31, v255\n" \
  "v_mov_b64 v[224:225], 0\n" \
  "v_cmp_eq_u32 vcc, 0, v2\n" \
  "s_nop 1\n" \
  "v_cndmask_b32 v226, 0, v1, vcc\n" \
  "v_cmp_eq_u32 vcc, 1, v2\n" \
  "s_nop 1\n" \
  "v_cndmask_b32 v227, 0, v1, vcc\n" \
  "v_mul_u32_u24 v5, 400, v252\n" \
  "v_add_u32 v5, 138240, v5\n" \
  "v_lshl_add_u32 v6, v253, 4, v5\n" \
  "v_add_u32 v7, 6400, v5\n" \
  "v_add_u32 v8, 6400, v6\n" \
  "v_add_u32 v9, 12800, v5\n" \
  "v_add_u32 v10, 12800, v6\n" \
  "v_bfe_u32 v1, v252, 1, 1\n" \
  "v_and_b32 v2, 1, v252\n" \
  "v_lshl_add_u32 v1, v2, 1, v1\n" \
  "v_mul_u32_u24 v1, 200, v1\n" \
  "v_add_u32 v11, 157440, v1\n" \
  "v_lshl_add_u32 v12, v253, 4, v11\n" \
  "v_lshlrev_b32 v13, 3, v13\n" \
  "v_sub_u32 v16, v5, v13\n" \
  "v_add_u32 v5, v5, v13\n" \
  "v_sub_u32 v17, v6, v13\n" \
  "v_add_u32 v6, v6, v13\n" \
  "v_sub_u32 v18, v7, v13\n" \
  "v_add_u32 v7, v7, v13\n" \
  "v_sub_u32 v19, v8, v13\n" \
  "v_add_u32 v8, v8, v13\n" \
  "v_sub_u32 v20, v9, v13\n" \
  "v_add_u32 v9, v9, v13\n" \
  "v_sub_u32 v21, v10, v13\n" \
  "v_add_u32 v10, v10, v13\n" \
  "v_sub_u32 v22, v11, v13\n" \
  "v_add_u32 v11, v11, v13\n" \
  "v_sub_u32 v23, v12, v13\n" \
  "v_add_u32 v12, v12, v13\n" \
  "s_waitcnt vmcnt(4)\n" \
  "s_barrier\n" \
  "ds_read_b64 v[200:201], v12 offset:128\n" \
  "ds_read_b64 v[202:203], v23 offset:136\n" \
  "ds_read_b64 v[184:185], v9 offset:184\n" \
  "ds_read_b64 v[186:187], v20 offset:192\n" \
  "ds_read_b64 v[188:189], v9 offset:384\n" \
  "ds_read_b64 v[190:191], v20 offset:392\n" \
  "ds_read_b64 v[196:197], v12 offset:64\n" \
  "ds_read_b64 v[198:199], v23 offset:72\n" \
  "ds_read_b64 v[176:177], v7 offset:184\n" \
  "ds_read_b64 v[178:179], v18 offset:192\n" \
  "ds_read_b64 v[180:181], v7 offset:384\n" \
  "ds_read_b64 v[182:183], v18 offset:392\n" \
  "ds_read_b64 v[192:193], v12 offset:0\n" \
  "ds_read_b64 v[194:195], v23 offset:8\n" \
  "s_waitcnt lgkmcnt(12)\n" \
  "v_mfma_f32_16x16x32_bf16 v[44:47], v[200:203], v[216:219], 0\n" \
  "ds_read_b64 v[168:169], v5 offset:184\n" \
  "ds_read_b64 v[170:171], v16 offset:192\n" \
  "s_waitcnt lgkmcnt(12)\n" \
  "v_mfma_f32_16x16x32_bf16 v[44:47], v[224:227], v[184:187], v[44:47]\n" \
  "ds_read_b64 v[172:173], v5 offset:384\n" \
  "ds_read_b64 v[174:175], v16 offset:392\n" \
  "v_mfma_f32_16x16x32_bf16 v[228:231], v[200:203], v[220:223], 0\n" \
  "ds_read_b64 v[96:97], v6 offset:0\n" \
  "ds_read_b64 v[98:99], v17 offset:8\n" \
  "s_waitcnt lgkmcnt(14)\n" \
  "v_mfma_f32_16x16x32_bf16 v[228:231], v[224:227], v[188:191], v[228:231]\n" \
  "ds_read_b64 v[124:125], v8 offset:64\n" \
  "ds_read_b64 v[126:127], v19 offset:72\n" \
  "s_waitcnt lgkmcnt(14)\n" \
  "v_mfma_f32_16x16x32_bf16 v[60:63], v[196:199], v[216:219], 0\n" \
  "ds_read_b64 v[136:137], v8 offset:264\n" \
  "ds_read_b64 v[138:139], v19 offset:272\n" \
  "s_waitcnt lgkmcnt(14)\n" \
  "v_mfma_f32_16x16x32_bf16 v[60:63], v[224:227], v[176:179], v[60:63]\n" \
  "ds_read_b64 v[140:141], v8 offset:328\n" \
  "ds_read_b64 v[142:143], v19 offset:336\n" \
  "v_mfma_f32_16x16x32_bf16 v[212:215], v[196:199], v[220:223], 0\n" \
  "ds_read_b64 v[160:161], v10 offset:264\n" \
  "ds_read_b64 v[162:163], v21 offset:272\n" \
  "s_waitcnt lgkmcnt(15)\n" \
  "v_mfma_f32_16x16x32_bf16 v[212:215], v[224:227], v[180:183], v[212:215]\n" \
  "ds_read_b64 v[116:117], v6 offset:328\n" \
  "ds_read_b64 v[118:119], v17 offset:336\n" \
  "s_waitcnt lgkmcnt(15)\n" \
  "v_mfma_f32_16x16x32_bf16 v[68:71], v[192:195], v[216:219], 0\n" \
  "ds_read_b64 v[144:145], v10 offset:0\n" \
  "ds_read_b64 v[146:147], v21 offset:8\n" \
  "s_waitcnt lgkmcnt(15)\n" \
  "v_mfma_f32_16x16x32_bf16 v[68:71], v[224:227], v[168:171], v[68:71]\n" \
  "ds_read_b64 v[148:149], v10 offset:64\n" \
  "ds_read_b64 v[150:151], v21 offset:72\n" \
  "v_mfma_f32_16x16x32_bf16 v[208:211], v[192:195], v[220:223], 0\n" \
  "ds_read_b64 v[104:105], v6 offset:128\n" \
  "ds_read_b64 v[106:107], v17 offset:136\n" \
  "s_waitcnt lgkmcnt(15)\n" \
  "v_mfma_f32_16x16x32_bf16 v[208:211], v[224:227], v[172:175], v[208:211]\n" \
  "ds_read_b64 v[100:101], v6 offset:64\n" \
  "ds_read_b64 v[102:103], v17 offset:72\n" \
  "v_mfma_f32_16x16x32_bf16 v[92:95], v[96:99], v[216:219], 0\n" \
  "ds_read_b64 v[120:121], v8 offset:0\n" \
  "ds_read_b64 v[122:123], v19 offset:8\n" \
  "s_waitcnt lgkmcnt(15)\n" \
  "v_mfma_f32_16x16x32_bf16 v[24:27], v[124:127], v[220:223], 0\n" \
  "ds_read_b64 v[112:113], v6 offset:264\n" \
  "ds_read_b64 v[114:115], v17 offset:272\n" \
  "v_mfma_f32_16x16x32_bf16 v[24:27], v[216:219], v[136:139], v[24:27]\n" \
  "ds_read_b64 v[156:157], v10 offset:200\n" \
  "ds_read_b64 v[158:159], v21 offset:208\n" \
  "s_waitcnt lgkmcnt(15)\n" \
  "v_mfma_f32_16x16x32_bf16 v[28:31], v[140:143], v[220:223], 0\n" \
  "ds_read_b64 v[132:133], v8 offset:200\n" \
  "ds_read_b64 v[134:135], v19 offset:208\n" \
  "v_mfma_f32_16x16x32_bf16 v[28:31], v[220:223], v[160:163], v[28:31]\n" \
  "ds_read_b64 v[108:109], v6 offset:200\n" \
  "ds_read_b64 v[110:111], v17 offset:208\n" \
  "s_waitcnt lgkmcnt(15)\n" \
  "v_mfma_f32_16x16x32_bf16 v[32:35], v[116:119], v[216:219], 0\n" \
  "ds_read_b64 v[152:153], v10 offset:128\n" \
  "ds_read_b64 v[154:155], v21 offset:136\n" \
  "v_mfma_f32_16x16x32_bf16 v[32:35], v[220:223], v[144:147], v[32:35]\n" \
  "ds_read_b64 v[164:165], v10 offset:328\n" \
  "ds_read_b64 v[166:167], v21 offset:336\n" \
  "v_mfma_f32_16x16x32_bf16 v[36:39], v[140:143], v[216:219], 0\n" \
  "ds_read_b64 v[128:129], v8 offset:128\n" \
  "ds_read_b64 v[130:131], v19 offset:136\n" \
  "s_waitcnt lgkmcnt(15)\n" \
  "v_mfma_f32_16x16x32_bf16 v[36:39], v[220:223], v[148:151], v[36:39]\n" \
  "ds_read_b64 v[204:205], v11 offset:184\n" \
  "ds_read_b64 v[206:207], v22 offset:192\n" \
  "v_mfma_f32_16x16x32_bf16 v[40:43], v[104:107], v[216:219], 0\n" \
  "v_mfma_f32_16x16x32_bf16 v[40:43], v[216:219], v[144:147], v[40:43]\n" \
  "v_mfma_f32_16x16x32_bf16 v[48:51], v[100:103], v[216:219], 0\n" \
  "s_waitcnt lgkmcnt(15)\n" \
  "v_mfma_f32_16x16x32_bf16 v[48:51], v[216:219], v[120:123], v[48:51]\n" \
  "s_waitcnt lgkmcnt(14)\n" \
  "v_mfma_f32_16x16x32_bf16 v[52:55], v[112:115], v[216:219], 0\n" \
  "v_mfma_f32_16x16x32_bf16 v[52:55], v[220:223], v[120:123], v[52:55]\n" \
  "v_mfma_f32_16x16x32_bf16 v[56:59], v[116:119], v[220:223], 0\n" \
  "s_waitcnt lgkmcnt(12)\n" \
  "v_mfma_f32_16x16x32_bf16 v[56:59], v[220:223], v[156:159], v[56:59]\n" \
  "v_mfma_f32_16x16x32_bf16 v[64:67], v[104:107], v[220:223], 0\n" \
  "v_mfma_f32_16x16x32_bf16 v[64:67], v[216:219], v[156:159], v[64:67]\n" \
  "v_mfma_f32_16x16x32_bf16 v[72:75], v[136:139], v[220:223], 0\n" \
  "v_mfma_f32_16x16x32_bf16 v[76:79], v[112:115], v[220:223], 0\n" \
  "s_waitcnt lgkmcnt(10)\n" \
  "v_mfma_f32_16x16x32_bf16 v[76:79], v[220:223], v[132:135], v[76:79]\n" \
  "v_mfma_f32_16x16x32_bf16 v[80:83], v[100:103], v[220:223], 0\n" \
  "v_mfma_f32_16x16x32_bf16 v[80:83], v[216:219], v[132:135], v[80:83]\n" \
  "v_mfma_f32_16x16x32_bf16 v[84:87], v[96:99], v[220:223], 0\n" \
  "s_waitcnt lgkmcnt(8)\n" \
  "v_mfma_f32_16x16x32_bf16 v[84:87], v[216:219], v[108:111], v[84:87]\n" \
  "v_mfma_f32_16x16x32_bf16 v[88:91], v[108:111], v[220:223], 0\n" \
  "v_mfma_f32_16x16x32_bf16 v[0:3], v[124:127], v[216:219], 0\n" \
  "s_waitcnt lgkmcnt(6)\n" \
  "v_mfma_f32_16x16x32_bf16 v[4:7], v[152:155], v[216:219], 0\n" \
  "v_mfma_f32_16x16x32_bf16 v[8:11], v[152:155], v[220:223], 0\n" \
  "s_waitcnt lgkmcnt(4)\n" \
  "v_mfma_f32_16x16x32_bf16 v[8:11], v[216:219], v[164:167], v[8:11]\n" \
  "v_mfma_f32_16x16x32_bf16 v[12:15], v[164:167], v[220:223], 0\n" \
  "s_waitcnt lgkmcnt(2)\n" \
  "v_mfma_f32_16x16x32_bf16 v[16:19], v[128:131], v[220:223], 0\n" \
  "v_mfma_f32_16x16x32_bf16 v[16:19], v[216:219], v[160:163], v[16:19]\n" \
  "v_mfma_f32_16x16x32_bf16 v[20:23], v[128:131], v[216:219], 0\n" \
  "v_mfma_f32_16x16x32_bf16 v[20:23], v[216:219], v[148:151], v[20:23]\n" \
  "s_waitcnt lgkmcnt(0)\n" \
  "v_mov_b32 v254, 163328\n" \
  "v_mov_b32 v255, 1\n" \
  "s_mov_b64 exec, 1\n" \
  "ds_add_u32 v254, v255 offset:16\n" \
  "s_mov_b64 exec, -1\n" \
  "v_mfma_f32_16x16x32_bf16 v[168:171], v[216:219], v[96:99], 0\n" \
  "v_mfma_f32_16x16x32_bf16 v[172:175], v[204:207], v[224:227], 0\n" \
  "v_mfma_f32_16x16x32_bf16 v[176:179], v[224:227], v[204:207], 0\n" \
  "s_mov_b32 exec_lo, 0x0\n" \
  "s_mov_b32 exec_hi, 0xffffffff\n" \
  "v_mov_b64 v[44:45], v[228:229]\n" \
  "v_mov_b64 v[46:47], v[230:231]\n" \
  "v_mov_b64 v[60:61], v[212:213]\n" \
  "v_mov_b64 v[62:63], v[214:215]\n" \
  "v_mov_b64 v[68:69], v[208:209]\n" \
  "v_mov_b64 v[70:71], v[210:211]\n" \
  "s_mov_b32 exec_lo, 0xff00ff00\n" \
  "s_mov_b32 exec_hi, 0x0\n" \
  "v_add_f32 v92, v92, v168\n" \
  "v_add_f32 v93, v93, v169\n" \
  "v_add_f32 v94, v94, v170\n" \
  "v_add_f32 v95, v95, v171\n" \
  "s_mov_b32 exec_lo, 0xf0000f00\n" \
  "s_mov_b32 exec_hi, 0x0\n" \
  "v_sub_f32 v92, v92, v176\n" \
  "v_sub_f32 v93, v93, v177\n" \
  "v_sub_f32 v94, v94, v178\n" \
  "v_sub_f32 v95, v95, v179\n" \
  "s_mov_b32 exec_lo, 0x0\n" \
  "s_mov_b32 exec_hi, 0xff00ff\n" \
  "v_mov_b64 v[92:93], 0\n" \
  "v_mov_b64 v[94:95], 0\n" \
  "s_mov_b32 exec_lo, 0x0\n" \
  "s_mov_b32 exec_hi, 0xf0000f\n" \
  "v_mov_b64 v[92:93], v[172:173]\n" \
  "v_mov_b64 v[94:95], v[174:175]\n" \
  "s_mov_b64 exec, -1\n" \
  "v_lshlrev_b32 v188, 4, v253\n" \
  "v_and_b32 v96, 4, v252\n" \
  "v_lshl_add_u32 v96, v253, 5, v96\n" \
  "v_mov_b32 v97, 130000\n" \
  "v_cmp_gt_u32 vcc, 8, v252\n" \
  "v_mov_b32 v193, 0x4444\n" \
  "v_mov_b32 v98, 0xeeee\n" \
  "v_cndmask_b32 v189, v97, v96, vcc\n" \
  "v_cndmask_b32 v190, v96, v97, vcc\n" \
  "v_cndmask_b32_e64 v191, 0, 16, vcc\n" \
  "v_cndmask_b32_e64 v192, 16, 0, vcc\n" \
  "v_and_b32 v99, 4, v252\n" \
  "v_cmp_ne_u32 vcc, 0, v99\n" \
  "s_lshl_b32 s35, s28, 11\n" \
  "s_add_u32 s35, s35, 130048\n" \
  "v_cndmask_b32 v193, v193, v98, vcc\n" \
  "v_lshl_add_u32 v194, v252, 4, s35\n" \
  "v_and_b32 v96, 7, v252\n" \
  "v_lshlrev_b32 v96, 5, v96\n" \
  "v_lshl_add_u32 v96, v253, 8, v96\n" \
  "v_lshrrev_b32 v97, 3, v252\n" \
  "v_lshl_add_u32 v96, v97, 2, v96\n" \
  "v_add_u32 v195, s35, v96\n" \
  "v_and_b32 v97, v97, v252\n" \
  "v_lshlrev_b32 v179, 4, v97\n" \
  "s_mov_b32 s50, 65471463\n" \
  "v_mov_b64 v[96:97], 0\n" \
  "v_mov_b32 v98, 130000\n" \
  "ds_write_b64 v98, v[96:97]\n" \
  "ds_write_b64 v98, v[96:97] offset:8\n" \
  "ds_write_b64 v98, v[96:97] offset:16\n" \
  "ds_write_b64 v98, v[96:97] offset:24\n" \
  "s_waitcnt vmcnt(0)\n" \
  "v_cvt_pk_u16_u32 v232, v232, v233\n" \
  "v_cvt_pk_u16_u32 v234, v234, v235\n" \
  "v_cvt_pk_u16_u32 v236, v236, v237\n" \
  "v_cvt_pk_u16_u32 v238, v238, v239\n" \
  "v_pk_min_u16 v232, v232, s50\n" \
  "v_pk_min_u16 v234, v234, s50\n" \
  "v_pk_min_u16 v236, v236, s50\n" \
  "v_pk_min_u16 v238, v238, s50\n" \
  "v_pk_lshrrev_b16 v233, 3, v232 op_sel_hi:[0,1]\n" \
  "v_pk_lshrrev_b16 v235, 3, v234 op_sel_hi:[0,1]\n" \
  "v_pk_lshrrev_b16 v237, 3, v236 op_sel_hi:[0,1]\n" \
  "v_pk_lshrrev_b16 v239, 3, v238 op_sel_hi:[0,1]\n" \
  "v_pk_mad_u16 v232, v232, 8, v233 op_sel_hi:[1,0,1]\n" \
  "v_pk_mad_u16 v234, v234, 8, v235 op_sel_hi:[1,0,1]\n" \
  "v_pk_mad_u16 v236, v236, 8, v237 op_sel_hi:[1,0,1]\n" \
  "v_pk_mad_u16 v238, v238, 8, v239 op_sel_hi:[1,0,1]\n" \
  "v_alignbit_b32 v236, v236, v236, v179\n" \
  "v_alignbit_b32 v238, v238, v238, v179\n" \
  "ds_write2_b32 v195, v232, v234 offset0:0 offset1:4\n" \
  "ds_write2_b32 v195, v236, v238 offset0:2 offset1:6\n" \
  "v_cvt_pk_u16_u32 v240, v240, v241\n" \
  "v_cvt_pk_u16_u32 v242, v242, v243\n" \
  "v_cvt_pk_u16_u32 v244, v244, v245\n" \
  "v_cvt_pk_u16_u32 v246, v246, v247\n" \
  "v_pk_min_u16 v240, v240, s50\n" \
  "v_pk_min_u16 v242, v242, s50\n" \
  "v_pk_min_u16 v244, v244, s50\n" \
  "v_pk_min_u16 v246, v246, s50\n" \
  "v_pk_lshrrev_b16 v241, 3, v240 op_sel_hi:[0,1]\n" \
  "v_pk_lshrrev_b16 v243, 3, v242 op_sel_hi:[0,1]\n" \
  "v_pk_lshrrev_b16 v245, 3, v244 op_sel_hi:[0,1]\n" \
  "v_pk_lshrrev_b16 v247, 3, v246 op_sel_hi:[0,1]\n" \
  "v_pk_mad_u16 v240, v240, 8, v241 op_sel_hi:[1,0,1]\n" \
  "v_pk_mad_u16 v242, v242, 8, v243 op_sel_hi:[1,0,1]\n" \
  "v_pk_mad_u16 v244, v244, 8, v245 op_sel_hi:[1,0,1]\n" \
  "v_pk_mad_u16 v246, v246, 8, v247 op_sel_hi:[1,0,1]\n" \
  "v_alignbit_b32 v244, v244, v244, v179\n" \
  "v_alignbit_b32 v246, v246, v246, v179\n" \
  "v_add_u32 v241, 0x400, v195\n" \
  "ds_write2_b32 v241, v240, v242 offset0:0 offset1:4\n" \
  "ds_write2_b32 v241, v244, v246 offset0:2 offset1:6\n" \
  "v_add_u32 v96, 6400, v250\n" \
  "v_add_u32 v97, 6400, v251\n" \
  "v_mov_b32 v98, v254\n" \
  "L1_:\n" \
  "ds_read_b32 v99, v98 offset:12\n" \
  "s_waitcnt lgkmcnt(0)\n" \
  "v_readfirstlane_b32 s35, v99\n" \
  "s_cmp_lt_u32 s35, 4\n" \
  "s_cbranch_scc1 L1_\n" \
  "v_lshlrev_b32 v203, 4, v253\n" \
  "s_lshr_b32 s35, s30, 4\n" \
  "s_lshl1_add_u32 s35, s29, s35\n" \
  "s_lshl_b32 s35, s35, 6\n" \
  "s_bfe_u32 s37, s30, 0x20002\n" \
  "s_lshl3_add_u32 s35, s37, s35\n" \
  "v_and_b32 v203, 16, v203\n" \
  "v_lshl_add_u32 v203, v203, 1, s35\n" \
  "global_load_dwordx2 v[204:205], v203, s[16:17]\n" \
  "global_load_dwordx2 v[206:207], v203, s[18:19]\n" \
  "v_mov_b32 v203, 0\n" \
  "global_load_ushort v203, v203, s[20:21]\n" \
  "s_add_u32 s46, s44, 4800\n" \
  "s_addc_u32 s47, s45, 0\n" \
  "global_load_dwordx4 v[208:211], v96, s[44:45] offset:-3200\n" \
  "global_load_dwordx4 v[212:215], v97, s[44:45] offset:-2944\n" \
  "global_load_dwordx4 v[216:219], v96, s[44:45] offset:-1600\n" \
  "global_load_dwordx4 v[220:223], v97, s[44:45] offset:-1344\n" \
  "global_load_dwordx4 v[224:227], v96, s[44:45] offset:0\n" \
  "global_load_dwordx4 v[228:231], v97, s[44:45] offset:256\n" \
  "global_load_dwordx4 v[232:235], v96, s[44:45] offset:1600\n" \
  "global_load_dwordx4 v[236:239], v97, s[44:45] offset:1856\n" \
  "global_load_dwordx4 v[240:243], v96, s[44:45] offset:3200\n" \
  "global_load_dwordx4 v[244:247], v97, s[44:45] offset:3456\n" \
  "global_load_dwordx4 v[248:251], v96, s[46:47] offset:0\n" \
  "global_load_dwordx4 v[252:255], v97, s[46:47] offset:256\n" \
  "s_setprio 1\n" \
  "ds_read_b128 v[180:183], v194\n" \
  "v_add_u32 v194, 0x100, v194\n" \
  "v_mov_b64 v[164:165], 0\n" \
  "v_mov_b64 v[166:167], 0\n" \
  "v_mov_b64 v[168:169], 0\n" \
  "v_mov_b64 v[170:171], 0\n" \
  "v_mov_b64 v[172:173], 0\n" \
  "v_mov_b64 v[174:175], 0\n" \
  "s_waitcnt lgkmcnt(0)\n" \
  "v_mad_u32_u16 v184, v182, 16, v188\n" \
  "ds_read_b128 v[128:131], v184 offset:0\n" \
  "ds_read_b128 v[132:135], v184 offset:64\n" \
  "v_mad_u32_u16 v185, v181, 16, v188\n" \
  "v_mad_u32_u16 v186, v182, 16, v188 op_sel:[1,0,0,0]\n" \
  "ds_read_b128 v[112:115], v185 offset:0\n" \
  "ds_read_b128 v[116:119], v185 offset:64\n" \
  "ds_read_b128 v[136:139], v186 offset:0\n" \
  "ds_read_b128 v[140:143], v186 offset:64\n" \
  "v_mad_u32_u16 v187, v181, 16, v188 op_sel:[1,0,0,0]\n" \
  "ds_read_b128 v[120:123], v187 offset:0\n" \
  "ds_read_b128 v[124:127], v187 offset:64\n" \
  "v_mad_u32_u16 v184, v180, 16, v188 op_sel:[1,0,0,0]\n" \
  "ds_read_b128 v[104:107], v184 offset:0\n" \
  "ds_read_b128 v[108:111], v184 offset:64\n" \
  "v_mad_u32_u16 v185, v180, 16, v188\n" \
  "ds_read_b128 v[96:99], v185 offset:0\n" \
  "s_waitcnt lgkmcnt(8)\n" \
  "s_mov_b32 s51, 3\n" \
  "s_call_b64 s[52:53], LA_loop\n" \
  "LA_ev0:\n" \
  "v_mov_b32 v196, v178\n" \
  "s_waitcnt vmcnt(10)\n" \
  "v_cvt_pk_u16_u32 v208, v208, v209\n" \
  "v_cvt_pk_u16_u32 v210, v210, v211\n" \
  "v_cvt_pk_u16_u32 v212, v212, v213\n" \
  "v_cvt_pk_u16_u32 v214, v214, v215\n" \
  "v_pk_min_u16 v208, v208, s50\n" \
  "v_pk_min_u16 v210, v210, s50\n" \
  "v_pk_min_u16 v212, v212, s50\n" \
  "v_pk_min_u16 v214, v214, s50\n" \
  "v_pk_lshrrev_b16 v209, 3, v208 op_sel_hi:[0,1]\n" \
  "v_pk_lshrrev_b16 v211, 3, v210 op_sel_hi:[0,1]\n" \
  "v_pk_lshrrev_b16 v213, 3, v212 op_sel_hi:[0,1]\n" \
  "v_pk_lshrrev_b16 v215, 3, v214 op_sel_hi:[0,1]\n" \
  "v_pk_mad_u16 v208, v208, 8, v209 op_sel_hi:[1,0,1]\n" \
  "v_pk_mad_u16 v210, v210, 8, v211 op_sel_hi:[1,0,1]\n" \
  "v_pk_mad_u16 v212, v212, 8, v213 op_sel_hi:[1,0,1]\n" \
  "v_pk_mad_u16 v214, v214, 8, v215 op_sel_hi:[1,0,1]\n" \
  "v_alignbit_b32 v212, v212, v212, v179\n" \
  "v_alignbit_b32 v214, v214, v214, v179\n" \
  "ds_write2_b32 v195, v208, v210 offset0:0 offset1:4\n" \
  "ds_write2_b32 v195, v212, v214 offset0:2 offset1:6\n" \
  "v_readfirstlane_b32 s6, v203\n" \
  "s_mov_b32 s51, 4\n" \
  "s_call_b64 s[52:53], LA_loop\n" \
  "LA_ev1:\n" \
  "v_mov_b32 v197, v178\n" \
  "s_waitcnt vmcnt(8)\n" \
  "v_cvt_pk_u16_u32 v216, v216, v217\n" \
  "v_cvt_pk_u16_u32 v218, v218, v219\n" \
  "v_cvt_pk_u16_u32 v220, v220, v221\n" \
  "v_cvt_pk_u16_u32 v222, v222, v223\n" \
  "v_pk_min_u16 v216, v216, s50\n" \
  "v_pk_min_u16 v218, v218, s50\n" \
  "v_pk_min_u16 v220, v220, s50\n" \
  "v_pk_min_u16 v222, v222, s50\n" \
  "v_pk_lshrrev_b16 v217, 3, v216 op_sel_hi:[0,1]\n" \
  "v_pk_lshrrev_b16 v219, 3, v218 op_sel_hi:[0,1]\n" \
  "v_pk_lshrrev_b16 v221, 3, v220 op_sel_hi:[0,1]\n" \
  "v_pk_lshrrev_b16 v223, 3, v222 op_sel_hi:[0,1]\n" \
  "v_pk_mad_u16 v216, v216, 8, v217 op_sel_hi:[1,0,1]\n" \
  "v_pk_mad_u16 v218, v218, 8, v219 op_sel_hi:[1,0,1]\n" \
  "v_pk_mad_u16 v220, v220, 8, v221 op_sel_hi:[1,0,1]\n" \
  "v_pk_mad_u16 v222, v222, 8, v223 op_sel_hi:[1,0,1]\n" \
  "v_alignbit_b32 v220, v220, v220, v179\n" \
  "v_alignbit_b32 v222, v222, v222, v179\n" \
  "v_add_u32 v217, 0x400, v195\n" \
  "ds_write2_b32 v217, v216, v218 offset0:0 offset1:4\n" \
  "ds_write2_b32 v217, v220, v222 offset0:2 offset1:6\n" \
  "v_subrev_u32 v194, 0x800, v194\n" \
  "s_mov_b32 s51, 4\n" \
  "s_call_b64 s[52:53], LA_loop\n" \
  "LA_ev2:\n" \
  "v_mov_b32 v198, v178\n" \
  "s_waitcnt vmcnt(6)\n" \
  "v_cvt_pk_u16_u32 v224, v224, v225\n" \
  "v_cvt_pk_u16_u32 v226, v226, v227\n" \
  "v_cvt_pk_u16_u32 v228, v228, v229\n" \
  "v_cvt_pk_u16_u32 v230, v230, v231\n" \
  "v_pk_min_u16 v224, v224, s50\n" \
  "v_pk_min_u16 v226, v226, s50\n" \
  "v_pk_min_u16 v228, v228, s50\n" \
  "v_pk_min_u16 v230, v230, s50\n" \
  "v_pk_lshrrev_b16 v225, 3, v224 op_sel_hi:[0,1]\n" \
  "v_pk_lshrrev_b16 v227, 3, v226 op_sel_hi:[0,1]\n" \
  "v_pk_lshrrev_b16 v229, 3, v228 op_sel_hi:[0,1]\n" \
  "v_pk_lshrrev_b16 v231, 3, v230 op_sel_hi:[0,1]\n" \
  "v_pk_mad_u16 v224, v224, 8, v225 op_sel_hi:[1,0,1]\n" \
  "v_pk_mad_u16 v226, v226, 8, v227 op_sel_hi:[1,0,1]\n" \
  "v_pk_mad_u16 v228, v228, 8, v229 op_sel_hi:[1,0,1]\n" \
  "v_pk_mad_u16 v230, v230, 8, v231 op_sel_hi:[1,0,1]\n" \
  "v_alignbit_b32 v228, v228, v228, v179\n" \
  "v_alignbit_b32 v230, v230, v230, v179\n" \
  "ds_write2_b32 v195, v224, v226 offset0:0 offset1:4\n" \
  "ds_write2_b32 v195, v228, v230 offset0:2 offset1:6\n" \
  "s_mov_b32 s51, 4\n" \
  "s_call_b64 s[52:53], LA_loop\n" \
  "LA_ev3:\n" \
  "v_mov_b32 v199, v178\n" \
  "s_waitcnt vmcnt(4)\n" \
  "v_cvt_pk_u16_u32 v232, v232, v233\n" \
  "v_cvt_pk_u16_u32 v234, v234, v235\n" \
  "v_cvt_pk_u16_u32 v236, v236, v237\n" \
  "v_cvt_pk_u16_u32 v238, v238, v239\n" \
  "v_pk_min_u16 v232, v232, s50\n" \
  "v_pk_min_u16 v234, v234, s50\n" \
  "v_pk_min_u16 v236, v236, s50\n" \
  "v_pk_min_u16 v238, v238, s50\n" \
  "v_pk_lshrrev_b16 v233, 3, v232 op_sel_hi:[0,1]\n" \
  "v_pk_lshrrev_b16 v235, 3, v234 op_sel_hi:[0,1]\n" \
  "v_pk_lshrrev_b16 v237, 3, v236 op_sel_hi:[0,1]\n" \
  "v_pk_lshrrev_b16 v239, 3, v238 op_sel_hi:[0,1]\n" \
  "v_pk_mad_u16 v232, v232, 8, v233 op_sel_hi:[1,0,1]\n" \
  "v_pk_mad_u16 v234, v234, 8, v235 op_sel_hi:[1,0,1]\n" \
  "v_pk_mad_u16 v236, v236, 8, v237 op_sel_hi:[1,0,1]\n" \
  "v_pk_mad_u16 v238, v238, 8, v239 op_sel_hi:[1,0,1]\n" \
  "v_alignbit_b32 v236, v236, v236, v179\n" \
  "v_alignbit_b32 v238, v238, v238, v179\n" \
  "v_add_u32 v233, 0x400, v195\n" \
  "ds_write2_b32 v233, v232, v234 offset0:0 offset1:4\n" \
  "ds_write2_b32 v233, v236, v238 offset0:2 offset1:6\n" \
  "v_subrev_u32 v194, 0x800, v194\n" \
  "s_mov_b32 s51, 4\n" \
  "s_call_b64 s[52:53], LA_loop\n" \
  "LA_ev4:\n" \
  "v_mov_b32 v200, v178\n" \
  "s_waitcnt vmcnt(2)\n" \
  "v_cvt_pk_u16_u32 v240, v240, v241\n" \
  "v_cvt_pk_u16_u32 v242, v242, v243\n" \
  "v_cvt_pk_u16_u32 v244, v244, v245\n" \
  "v_cvt_pk_u16_u32 v246, v246, v247\n" \
  "v_pk_min_u16 v240, v240, s50\n" \
  "v_pk_min_u16 v242, v242, s50\n" \
  "v_pk_min_u16 v244, v244, s50\n" \
  "v_pk_min_u16 v246, v246, s50\n" \
  "v_pk_lshrrev_b16 v241, 3, v240 op_sel_hi:[0,1]\n" \
  "v_pk_lshrrev_b16 v243, 3, v242 op_sel_hi:[0,1]\n" \
  "v_pk_lshrrev_b16 v245, 3, v244 op_sel_hi:[0,1]\n" \
  "v_pk_lshrrev_b16 v247, 3, v246 op_sel_hi:[0,1]\n" \
  "v_pk_mad_u16 v240, v240, 8, v241 op_sel_hi:[1,0,1]\n" \
  "v_pk_mad_u16 v242, v242, 8, v243 op_sel_hi:[1,0,1]\n" \
  "v_pk_mad_u16 v244, v244, 8, v245 op_sel_hi:[1,0,1]\n" \
  "v_pk_mad_u16 v246, v246, 8, v247 op_sel_hi:[1,0,1]\n" \
  "v_alignbit_b32 v244, v244, v244, v179\n" \
  "v_alignbit_b32 v246, v246, v246, v179\n" \
  "ds_write2_b32 v195, v240, v242 offset0:0 offset1:4\n" \
  "ds_write2_b32 v195, v244, v246 offset0:2 offset1:6\n" \
  "s_mov_b32 s51, 4\n" \
  "s_call_b64 s[52:53], LA_loop\n" \
  "LA_ev5:\n" \
  "v_mov_b32 v201, v178\n" \
  "s_waitcnt vmcnt(0)\n" \
  "v_cvt_pk_u16_u32 v248, v248, v249\n" \
  "v_cvt_pk_u16_u32 v250, v250, v251\n" \
  "v_cvt_pk_u16_u32 v252, v252, v253\n" \
  "v_cvt_pk_u16_u32 v254, v254, v255\n" \
  "v_pk_min_u16 v248, v248, s50\n" \
  "v_pk_min_u16 v250, v250, s50\n" \
  "v_pk_min_u16 v252, v252, s50\n" \
  "v_pk_min_u16 v254, v254, s50\n" \
  "v_pk_lshrrev_b16 v249, 3, v248 op_sel_hi:[0,1]\n" \
  "v_pk_lshrrev_b16 v251, 3, v250 op_sel_hi:[0,1]\n" \
  "v_pk_lshrrev_b16 v253, 3, v252 op_sel_hi:[0,1]\n" \
  "v_pk_lshrrev_b16 v255, 3, v254 op_sel_hi:[0,1]\n" \
  "v_pk_mad_u16 v248, v248, 8, v249 op_sel_hi:[1,0,1]\n" \
  "v_pk_mad_u16 v250, v250, 8, v251 op_sel_hi:[1,0,1]\n" \
  "v_pk_mad_u16 v252, v252, 8, v253 op_sel_hi:[1,0,1]\n" \
  "v_pk_mad_u16 v254, v254, 8, v255 op_sel_hi:[1,0,1]\n" \
  "v_alignbit_b32 v252, v252, v252, v179\n" \
  "v_alignbit_b32 v254, v254, v254, v179\n" \
  "v_add_u32 v249, 0x400, v195\n" \
  "ds_write2_b32 v249, v248, v250 offset0:0 offset1:4\n" \
  "ds_write2_b32 v249, v252, v254 offset0:2 offset1:6\n" \
  "v_subrev_u32 v194, 0x800, v194\n" \
  "s_mov_b32 s51, 4\n" \
  "s_call_b64 s[52:53], LA_loop\n" \
  "LA_ev6:\n" \
  "v_mov_b32 v202, v178\n" \
  "s_mov_b32 s51, 4\n" \
  "s_call_b64 s[52:53], LA_loop\n" \
  "LA_ev7:\n" \
  "v_mov_b32 v203, v178\n" \
  "ds_read_b128 v[100:103], v185 offset:64\n" \
  "v_mad_u32_u16 v186, v183, v191, v189\n" \
  "ds_read2_b32 v[144:145], v186 offset0:0 offset1:2\n" \
  "ds_read2_b32 v[146:147], v186 offset0:4 offset1:6\n" \
  "v_mad_u32_u16 v187, v183, v192, v190\n" \
  "ds_read2_b32 v[148:149], v187 offset0:0 offset1:2\n" \
  "ds_read2_b32 v[150:151], v187 offset0:4 offset1:6\n" \
  "s_lshr_b32 s35, s30, 2\n" \
  "s_lshl3_add_u32 s33, s29, s35\n" \
  "s_and_b32 s34, s30, 3\n" \
  "s_lshl_b32 s59, s27, 11\n" \
  "s_mul_i32 s35, s33, 1600000\n" \
  "s_add_u32 s54, s24, s35\n" \
  "s_addc_u32 s55, s25, 0\n" \
  "s_mul_i32 s35, s34, 819200\n" \
  "s_lshl1_add_u32 s35, s59, s35\n" \
  "s_add_u32 s35, s35, 102400000\n" \
  "s_add_u32 s56, s24, s35\n" \
  "s_addc_u32 s57, s25, 0\n" \
  "v_mbcnt_lo_u32_b32 v189, -1, 0\n" \
  "v_mbcnt_hi_u32_b32 v189, -1, v189\n" \
  "v_lshlrev_b32 v188, 4, v189\n" \
  "s_waitcnt lgkmcnt(5)\n" \
  "v_mfma_f32_16x16x32_bf16 v[152:155], v[112:115], v[112:115], 0\n" \
  "v_fmac_f32 v176, v156, v88\n" \
  "v_fmac_f32 v176, v157, v89\n" \
  "v_mfma_f32_16x16x32_bf16 v[152:155], v[116:119], v[116:119], v[152:155]\n" \
  "v_fmac_f32 v176, v158, v90\n" \
  "v_fmac_f32 v176, v159, v91\n" \
  "v_mfma_f32_16x16x32_bf16 v[156:159], v[128:131], v[128:131], 0\n" \
  "v_fmac_f32 v176, v160, v92\n" \
  "v_fmac_f32 v176, v161, v93\n" \
  "v_mfma_f32_16x16x32_bf16 v[156:159], v[132:135], v[132:135], v[156:159]\n" \
  "v_fmac_f32 v176, v162, v94\n" \
  "v_fma_f32 v177, v163, v95, v176\n" \
  "v_mfma_f32_16x16x32_bf16 v[160:163], v[128:131], v[136:139], 0\n" \
  "v_mul_f32 v176, v152, v0\n" \
  "v_fmac_f32 v176, v153, v1\n" \
  "v_add_f32_dpp v177, v177, v177 row_ror:8 row_mask:0xf bank_mask:0xf\n" \
  "v_mfma_f32_16x16x32_bf16 v[160:163], v[132:135], v[140:143], v[160:163]\n" \
  "v_fmac_f32 v176, v154, v2\n" \
  "v_fmac_f32 v176, v155, v3\n" \
  "v_mfma_f32_16x16x32_bf16 v[152:155], v[136:139], v[136:139], 0\n" \
  "v_fmac_f32 v176, v156, v4\n" \
  "v_fmac_f32 v176, v157, v5\n" \
  "v_mfma_f32_16x16x32_bf16 v[152:155], v[140:143], v[140:143], v[152:155]\n" \
  "v_fmac_f32 v176, v158, v6\n" \
  "v_fmac_f32 v176, v159, v7\n" \
  "s_waitcnt lgkmcnt(0)\n" \
  "s_setprio 3\n" \
  "s_barrier\n" \
  "global_load_dwordx4 v[208:211], v188, s[56:57] offset:0 sc1\n" \
  "global_load_dwordx4 v[212:215], v188, s[56:57] offset:1024 sc1\n" \
  "global_load_dwordx4 v[216:219], v188, s[56:57] offset:2048 sc1\n" \
  "global_load_dwordx4 v[220:223], v188, s[56:57] offset:3072 sc1\n" \
  "v_mfma_f32_16x16x32_bf16 v[156:159], v[112:115], v[136:139], 0\n" \
  "v_fmac_f32 v176, v160, v8\n" \
  "v_fmac_f32 v176, v161, v9\n" \
  "v_mov_b32_dpp v178, v178 row_ror:4 row_mask:0xf bank_mask:0xf\n" \
  "v_lshrrev_b32 v180, 1, v189\n" \
  "v_mfma_f32_16x16x32_bf16 v[156:159], v[116:119], v[140:143], v[156:159]\n" \
  "v_fmac_f32 v176, v162, v10\n" \
  "v_fmac_f32 v176, v163, v11\n" \
  "v_xor_b32 v180, v180, v189\n" \
  "v_mfma_f32_16x16x32_bf16 v[160:163], v[112:115], v[128:131], 0\n" \
  "v_fmac_f32 v176, v152, v12\n" \
  "v_fmac_f32 v176, v153, v13\n" \
  "v_and_b32 v180, 8, v180\n" \
  "v_mfma_f32_16x16x32_bf16 v[160:163], v[116:119], v[132:135], v[160:163]\n" \
  "v_fmac_f32 v176, v154, v14\n" \
  "v_fmac_f32 v176, v155, v15\n" \
  "v_mov_b64 v[174:175], 0\n" \
  "v_bfe_u32 v181, v189, 2, 1\n" \
  "v_mfma_f32_16x16x32_bf16 v[152:155], v[112:115], v[120:123], 0\n" \
  "v_fmac_f32 v176, v156, v16\n" \
  "v_fmac_f32 v176, v157, v17\n" \
  "v_mov_b64 v[170:171], 0\n" \
  "v_lshlrev_b32_e64 v181, v181, 5\n" \
  "v_mfma_f32_16x16x32_bf16 v[152:155], v[116:119], v[124:127], v[152:155]\n" \
  "v_fmac_f32 v176, v158, v18\n" \
  "v_fmac_f32 v176, v159, v19\n" \
  "v_lshlrev_b32 v180, v180, v181\n" \
  "v_mfma_f32_16x16x32_bf16 v[156:159], v[120:123], v[136:139], 0\n" \
  "v_fmac_f32 v176, v160, v20\n" \
  "v_fmac_f32 v176, v161, v21\n" \
  "v_bfe_u32 v181, v189, 2, 2\n" \
  "v_mfma_f32_16x16x32_bf16 v[156:159], v[124:127], v[140:143], v[156:159]\n" \
  "v_fmac_f32 v176, v162, v22\n" \
  "v_fmac_f32 v176, v163, v23\n" \
  "v_mov_b64 v[166:167], 0\n" \
  "v_lshlrev_b32_e64 v181, v181, 1\n" \
  "v_mfma_f32_16x16x32_bf16 v[160:163], v[104:107], v[128:131], 0\n" \
  "v_fmac_f32 v176, v152, v24\n" \
  "v_fmac_f32 v176, v153, v25\n" \
  "v_cmp_gt_u32 vcc, 32, v189\n" \
  "v_mfma_f32_16x16x32_bf16 v[160:163], v[108:111], v[132:135], v[160:163]\n" \
  "v_fmac_f32 v176, v154, v26\n" \
  "v_fmac_f32 v176, v155, v27\n" \
  "v_mov_b32 v191, 0x3f80\n" \
  "v_mfma_f32_16x16x32_bf16 v[152:155], v[120:123], v[128:131], 0\n" \
  "v_fmac_f32 v176, v156, v28\n" \
  "v_fmac_f32 v176, v157, v29\n" \
  "v_add_u32 v190, 32000, v188\n" \
  "v_mfma_f32_16x16x32_bf16 v[152:155], v[124:127], v[132:135], v[152:155]\n" \
  "v_fmac_f32 v176, v158, v30\n" \
  "v_fmac_f32 v176, v159, v31\n" \
  "v_add_f32_dpp v178, v177, v177 row_ror:4 row_mask:0xf bank_mask:0x1\n" \
  "v_cndmask_b32 v192, v181, v180, vcc\n" \
  "v_mfma_f32_16x16x32_bf16 v[156:159], v[96:99], v[128:131], 0\n" \
  "v_fmac_f32 v176, v160, v32\n" \
  "v_fmac_f32 v176, v161, v33\n" \
  "v_bfe_i32 v180, v192, 0, 1\n" \
  "v_mfma_f32_16x16x32_bf16 v[156:159], v[100:103], v[132:135], v[156:159]\n" \
  "v_fmac_f32 v176, v162, v34\n" \
  "v_fmac_f32 v176, v163, v35\n" \
  "v_and_b32 v180, v180, v191\n" \
  "v_smfmac_f32_16x16x64_bf16 v[172:175], v[144:147], v[128:135], v193\n" \
  "v_fmac_f32 v176, v152, v36\n" \
  "v_fmac_f32 v176, v153, v37\n" \
  "v_bfe_i32 v181, v192, 1, 1\n" \
  "v_smfmac_f32_16x16x64_bf16 v[172:175], v[148:151], v[136:143], v193\n" \
  "v_fmac_f32 v176, v154, v38\n" \
  "v_fmac_f32 v176, v155, v39\n" \
  "v_and_b32 v181, v181, v191\n" \
  "v_mfma_f32_16x16x32_bf16 v[160:163], v[96:99], v[112:115], 0\n" \
  "v_fmac_f32 v176, v156, v40\n" \
  "v_fmac_f32 v176, v157, v41\n" \
  "v_bfe_i32 v182, v192, 2, 1\n" \
  "v_mfma_f32_16x16x32_bf16 v[160:163], v[100:103], v[116:119], v[160:163]\n" \
  "v_fmac_f32 v176, v158, v42\n" \
  "v_fmac_f32 v176, v159, v43\n" \
  "v_and_b32 v182, v182, v191\n" \
  "v_mfma_f32_16x16x32_bf16 v[152:155], v[104:107], v[112:115], 0\n" \
  "v_fmac_f32 v176, v172, v44\n" \
  "v_fmac_f32 v176, v173, v45\n" \
  "v_bfe_i32 v183, v192, 3, 1\n" \
  "v_mfma_f32_16x16x32_bf16 v[152:155], v[108:111], v[116:119], v[152:155]\n" \
  "v_fmac_f32 v176, v174, v46\n" \
  "v_fmac_f32 v176, v175, v47\n" \
  "v_and_b32 v183, v183, v191\n" \
  "v_mfma_f32_16x16x32_bf16 v[156:159], v[104:107], v[136:139], 0\n" \
  "v_fmac_f32 v176, v160, v48\n" \
  "v_fmac_f32 v176, v161, v49\n" \
  "v_and_b32 v192, 3, v189\n" \
  "v_mfma_f32_16x16x32_bf16 v[156:159], v[108:111], v[140:143], v[156:159]\n" \
  "v_fmac_f32 v176, v162, v50\n" \
  "v_fmac_f32 v176, v163, v51\n" \
  "v_mul_u32_u24 v192, 0x5555, v192\n" \
  "v_smfmac_f32_16x16x64_bf16 v[168:171], v[144:147], v[112:119], v193\n" \
  "v_fmac_f32 v176, v152, v52\n" \
  "v_fmac_f32 v176, v153, v53\n" \
  "v_add_u32 v189, 16000, v188\n" \
  "v_smfmac_f32_16x16x64_bf16 v[168:171], v[148:151], v[120:127], v193\n" \
  "v_fmac_f32 v176, v154, v54\n" \
  "v_fmac_f32 v176, v155, v55\n" \
  "v_add_u32 v191, 48000, v188\n" \
  "v_mfma_f32_16x16x32_bf16 v[160:163], v[96:99], v[136:139], 0\n" \
  "v_fmac_f32 v176, v156, v56\n" \
  "v_fmac_f32 v176, v157, v57\n" \
  "v_mfma_f32_16x16x32_bf16 v[160:163], v[100:103], v[140:143], v[160:163]\n" \
  "v_fmac_f32 v176, v158, v58\n" \
  "v_fmac_f32 v176, v159, v59\n" \
  "s_add_u32 s35, s59, 64000\n" \
  "s_add_u32 s54, s54, s35\n" \
  "s_addc_u32 s55, s55, 0\n" \
  "s_add_u32 s56, s56, 32768\n" \
  "s_addc_u32 s57, s57, 0\n" \
  "s_add_u32 s58, s59, 0x10000\n" \
  "s_mov_b32 m0, s58\n" \
  "s_add_u32 s35, s58, 0x8000\n" \
  "global_load_lds_dwordx4 v188, s[54:55] sc1\n" \
  "global_load_lds_dwordx4 v188, s[54:55] offset:1024 sc1\n" \
  "v_smfmac_f32_16x16x64_bf16 v[164:167], v[148:151], v[104:111], v193\n" \
  "v_fmac_f32 v176, v168, v60\n" \
  "v_fmac_f32 v176, v169, v61\n" \
  "v_smfmac_f32_16x16x64_bf16 v[164:167], v[144:147], v[96:103], v193\n" \
  "v_fmac_f32 v176, v170, v62\n" \
  "v_fmac_f32 v176, v171, v63\n" \
  "v_mfma_f32_16x16x32_bf16 v[152:155], v[120:123], v[120:123], 0\n" \
  "v_fmac_f32 v176, v160, v64\n" \
  "v_fmac_f32 v176, v161, v65\n" \
  "s_add_u32 m0, s58, 0x4000\n" \
  "s_cmp_lt_u32 s35, 0x28000\n" \
  "global_load_lds_dwordx4 v189, s[54:55] sc1\n" \
  "global_load_lds_dwordx4 v189, s[54:55] offset:1024 sc1\n" \
  "s_cselect_b32 s58, s35, s59\n" \
  "s_add_u32 s54, s54, 32000\n" \
  "s_addc_u32 s55, s55, 0\n" \
  "v_mfma_f32_16x16x32_bf16 v[152:155], v[124:127], v[124:127], v[152:155]\n" \
  "v_fmac_f32 v176, v162, v66\n" \
  "v_fmac_f32 v176, v163, v67\n" \
  "v_mfma_f32_16x16x32_bf16 v[156:159], v[104:107], v[120:123], 0\n" \
  "v_fmac_f32 v176, v164, v68\n" \
  "v_fmac_f32 v176, v165, v69\n" \
  "v_mfma_f32_16x16x32_bf16 v[156:159], v[108:111], v[124:127], v[156:159]\n" \
  "v_fmac_f32 v176, v166, v70\n" \
  "v_fmac_f32 v176, v167, v71\n" \
  "global_load_dwordx4 v[224:227], v188, s[56:57] sc1\n" \
  "global_load_dwordx4 v[228:231], v188, s[56:57] offset:1024 sc1\n" \
  "v_mfma_f32_16x16x32_bf16 v[160:163], v[96:99], v[120:123], 0\n" \
  "v_fmac_f32 v176, v152, v72\n" \
  "v_fmac_f32 v176, v153, v73\n" \
  "v_mfma_f32_16x16x32_bf16 v[160:163], v[100:103], v[124:127], v[160:163]\n" \
  "v_fmac_f32 v176, v154, v74\n" \
  "v_fmac_f32 v176, v155, v75\n" \
  "v_mfma_f32_16x16x32_bf16 v[152:155], v[96:99], v[104:107], 0\n" \
  "v_fmac_f32 v176, v156, v76\n" \
  "v_fmac_f32 v176, v157, v77\n" \
  "global_load_dwordx4 v[232:235], v188, s[56:57] offset:2048 sc1\n" \
  "global_load_dwordx4 v[236:239], v188, s[56:57] offset:3072 sc1\n" \
  "s_add_u32 s56, s56, 32768\n" \
  "s_addc_u32 s57, s57, 0\n" \
  "v_mfma_f32_16x16x32_bf16 v[152:155], v[100:103], v[108:111], v[152:155]\n" \
  "v_fmac_f32 v176, v158, v78\n" \
  "v_fmac_f32 v176, v159, v79\n" \
  "v_mfma_f32_16x16x32_bf16 v[156:159], v[104:107], v[104:107], 0\n" \
  "v_fmac_f32 v176, v160, v80\n" \
  "v_fmac_f32 v176, v161, v81\n" \
  "v_mfma_f32_16x16x32_bf16 v[156:159], v[108:111], v[108:111], v[156:159]\n" \
  "v_fmac_f32 v176, v162, v82\n" \
  "v_fmac_f32 v176, v163, v83\n" \
  "v_mfma_f32_16x16x32_bf16 v[160:163], v[148:151], v[144:147], 0\n" \
  "v_fmac_f32 v176, v152, v84\n" \
  "v_fmac_f32 v176, v153, v85\n" \
  "v_mfma_f32_16x16x32_bf16 v[160:163], v[96:99], v[96:99], v[160:163]\n" \
  "v_fmac_f32 v176, v154, v86\n" \
  "v_fmac_f32 v176, v155, v87\n" \
  "v_mfma_f32_16x16x32_bf16 v[160:163], v[100:103], v[100:103], v[160:163]\n" \
  "v_fmac_f32 v176, v156, v88\n" \
  "v_fmac_f32 v176, v157, v89\n" \
  "v_fmac_f32 v176, v158, v90\n" \
  "v_fmac_f32 v176, v159, v91\n" \
  "s_nop 3\n" \
  "v_fmac_f32 v176, v160, v92\n" \
  "v_fmac_f32 v176, v161, v93\n" \
  "v_fmac_f32 v176, v162, v94\n" \
  "v_fma_f32 v177, v163, v95, v176\n" \
  "s_nop 1\n" \
  "v_add_f32_dpp v177, v177, v177 row_ror:8 row_mask:0xf bank_mask:0xf\n" \
  "v_mov_b32_dpp v178, v178 row_ror:4 row_mask:0xf bank_mask:0xf\n" \
  "s_nop 1\n" \
  "v_add_f32_dpp v178, v177, v177 row_ror:4 row_mask:0xf bank_mask:0x1\n" \
  "s_nop 1\n" \
  "v_mov_b32_dpp v196, v178 row_ror:8 row_mask:0xf bank_mask:0xc\n" \
  "s_nop 1\n" \
  "v_permlane16_swap_b32 v196, v197\n" \
  "v_permlane16_swap_b32 v198, v199\n" \
  "v_permlane16_swap_b32 v200, v201\n" \
  "v_permlane16_swap_b32 v202, v203\n" \
  "v_add_f32 v196, v196, v197\n" \
  "v_add_f32 v198, v198, v199\n" \
  "v_add_f32 v200, v200, v201\n" \
  "v_add_f32 v202, v202, v203\n" \
  "v_lshrrev_b32 v96, 4, v188\n" \
  "v_and_b32 v97, 1, v96\n" \
  "v_lshrrev_b32 v98, 4, v96\n" \
  "v_permlane32_swap_b32 v196, v198\n" \
  "v_permlane32_swap_b32 v200, v202\n" \
  "v_add_f32 v196, v196, v198\n" \
  "v_add_f32 v200, v200, v202\n" \
  "v_cmp_eq_u32 vcc, 1, v97\n" \
  "v_lshlrev_b32 v97, 2, v97\n" \
  "v_add_lshl_u32 v97, v97, v98, 2\n" \
  "v_bfe_u32 v98, v96, 2, 2\n" \
  "v_add_f32_dpp v196, v196, v196 quad_perm:[1,0,3,2] row_mask:0xf bank_mask:0xf\n" \
  "v_add_f32_dpp v200, v200, v200 quad_perm:[1,0,3,2] row_mask:0xf bank_mask:0xf\n" \
  "v_sub_u32 v97, v97, v98\n" \
  "v_cndmask_b32 v196, v196, v200, vcc\n" \
  "v_add_u32 v97, 1, v97\n" \
  "v_and_b32 v97, 31, v97\n" \
  "v_add_f32_dpp v196, v196, v196 quad_perm:[2,3,0,1] row_mask:0xf bank_mask:0xf\n" \
  "v_lshlrev_b32 v97, 2, v97\n" \
  "s_lshl2_add_u32 s35, s7, s28\n" \
  "s_lshl_b32 s35, s35, 7\n" \
  "s_lshl_b32 s36, s31, 16\n" \
  "s_add_u32 s35, s35, s36\n" \
  "s_add_u32 s35, s35, 109871104\n" \
  "s_add_u32 s40, s24, s35\n" \
  "s_addc_u32 s41, s25, 0\n" \
  "v_min_u32 v196, -2, v196\n" \
  "s_waitcnt vmcnt(8)\n" \
  "s_mov_b32 exec_lo, 0x33333333\n" \
  "s_mov_b32 exec_hi, 0x33333333\n" \
  "global_store_dword v97, v196, s[40:41] sc1\n" \
  "s_mov_b64 exec, -1\n" \
  "v_lshlrev_b32 v184, 16, v204\n" \
  "v_and_b32 v185, 0xffff0000, v204\n" \
  "v_lshlrev_b32 v186, 16, v205\n" \
  "v_and_b32 v187, 0xffff0000, v205\n" \
  "s_branch LG_start\n" \
  "LA_loop:\n" \
  "v_mfma_f32_16x16x32_bf16 v[152:155], v[112:115], v[112:115], 0\n" \
  "s_waitcnt lgkmcnt(7)\n" \
  "ds_read_b128 v[100:103], v185 offset:64\n" \
  "v_fmac_f32 v176, v156, v88\n" \
  "v_fmac_f32 v176, v157, v89\n" \
  "v_mad_u32_u16 v186, v183, v191, v189\n" \
  "v_mfma_f32_16x16x32_bf16 v[152:155], v[116:119], v[116:119], v[152:155]\n" \
  "ds_read2_b32 v[144:145], v186 offset0:0 offset1:2\n" \
  "v_fmac_f32 v176, v158, v90\n" \
  "v_fmac_f32 v176, v159, v91\n" \
  "v_mfma_f32_16x16x32_bf16 v[156:159], v[128:131], v[128:131], 0\n" \
  "ds_read2_b32 v[146:147], v186 offset0:4 offset1:6\n" \
  "v_fmac_f32 v176, v160, v92\n" \
  "v_fmac_f32 v176, v161, v93\n" \
  "v_mad_u32_u16 v187, v183, v192, v190\n" \
  "v_mfma_f32_16x16x32_bf16 v[156:159], v[132:135], v[132:135], v[156:159]\n" \
  "s_waitcnt lgkmcnt(9)\n" \
  "ds_read2_b32 v[148:149], v187 offset0:0 offset1:2\n" \
  "v_fmac_f32 v176, v162, v94\n" \
  "v_fma_f32 v177, v163, v95, v176\n" \
  "v_mfma_f32_16x16x32_bf16 v[160:163], v[128:131], v[136:139], 0\n" \
  "s_waitcnt lgkmcnt(9)\n" \
  "ds_read2_b32 v[150:151], v187 offset0:4 offset1:6\n" \
  "v_mul_f32 v176, v152, v0\n" \
  "v_fmac_f32 v176, v153, v1\n" \
  "v_add_f32_dpp v177, v177, v177 row_ror:8 row_mask:0xf bank_mask:0xf\n" \
  "v_mfma_f32_16x16x32_bf16 v[160:163], v[132:135], v[140:143], v[160:163]\n" \
  "ds_read_b128 v[180:183], v194\n" \
  "v_fmac_f32 v176, v154, v2\n" \
  "v_fmac_f32 v176, v155, v3\n" \
  "v_mfma_f32_16x16x32_bf16 v[152:155], v[136:139], v[136:139], 0\n" \
  "v_fmac_f32 v176, v156, v4\n" \
  "v_fmac_f32 v176, v157, v5\n" \
  "v_mfma_f32_16x16x32_bf16 v[152:155], v[140:143], v[140:143], v[152:155]\n" \
  "v_fmac_f32 v176, v158, v6\n" \
  "v_fmac_f32 v176, v159, v7\n" \
  "v_add_u32 v194, 0x100, v194\n" \
  "v_mfma_f32_16x16x32_bf16 v[156:159], v[112:115], v[136:139], 0\n" \
  "v_fmac_f32 v176, v160, v8\n" \
  "v_fmac_f32 v176, v161, v9\n" \
  "v_mov_b32_dpp v178, v178 row_ror:4 row_mask:0xf bank_mask:0xf\n" \
  "v_mfma_f32_16x16x32_bf16 v[156:159], v[116:119], v[140:143], v[156:159]\n" \
  "v_fmac_f32 v176, v162, v10\n" \
  "v_fmac_f32 v176, v163, v11\n" \
  "v_mfma_f32_16x16x32_bf16 v[160:163], v[112:115], v[128:131], 0\n" \
  "v_fmac_f32 v176, v152, v12\n" \
  "v_fmac_f32 v176, v153, v13\n" \
  "v_mfma_f32_16x16x32_bf16 v[160:163], v[116:119], v[132:135], v[160:163]\n" \
  "s_waitcnt lgkmcnt(10)\n" \
  "v_fmac_f32 v176, v154, v14\n" \
  "v_fmac_f32 v176, v155, v15\n" \
  "v_mov_b64 v[174:175], 0\n" \
  "v_mfma_f32_16x16x32_bf16 v[152:155], v[112:115], v[120:123], 0\n" \
  "s_waitcnt lgkmcnt(9)\n" \
  "v_fmac_f32 v176, v156, v16\n" \
  "v_fmac_f32 v176, v157, v17\n" \
  "v_mov_b64 v[170:171], 0\n" \
  "v_mfma_f32_16x16x32_bf16 v[152:155], v[116:119], v[124:127], v[152:155]\n" \
  "v_fmac_f32 v176, v158, v18\n" \
  "v_fmac_f32 v176, v159, v19\n" \
  "v_mfma_f32_16x16x32_bf16 v[156:159], v[120:123], v[136:139], 0\n" \
  "v_fmac_f32 v176, v160, v20\n" \
  "v_fmac_f32 v176, v161, v21\n" \
  "v_mfma_f32_16x16x32_bf16 v[156:159], v[124:127], v[140:143], v[156:159]\n" \
  "s_waitcnt lgkmcnt(8)\n" \
  "v_fmac_f32 v176, v162, v22\n" \
  "v_fmac_f32 v176, v163, v23\n" \
  "v_mov_b64 v[166:167], 0\n" \
  "v_mfma_f32_16x16x32_bf16 v[160:163], v[104:107], v[128:131], 0\n" \
  "s_waitcnt lgkmcnt(0)\n" \
  "v_fmac_f32 v176, v152, v24\n" \
  "v_fmac_f32 v176, v153, v25\n" \
  "v_mad_u32_u16 v184, v182, 16, v188\n" \
  "v_mfma_f32_16x16x32_bf16 v[160:163], v[108:111], v[132:135], v[160:163]\n" \
  "v_fmac_f32 v176, v154, v26\n" \
  "v_fmac_f32 v176, v155, v27\n" \
  "v_mfma_f32_16x16x32_bf16 v[152:155], v[120:123], v[128:131], 0\n" \
  "v_fmac_f32 v176, v156, v28\n" \
  "v_fmac_f32 v176, v157, v29\n" \
  "v_mfma_f32_16x16x32_bf16 v[152:155], v[124:127], v[132:135], v[152:155]\n" \
  "v_fmac_f32 v176, v158, v30\n" \
  "v_fmac_f32 v176, v159, v31\n" \
  "v_add_f32_dpp v178, v177, v177 row_ror:4 row_mask:0xf bank_mask:0x1\n" \
  "v_mfma_f32_16x16x32_bf16 v[156:159], v[96:99], v[128:131], 0\n" \
  "v_fmac_f32 v176, v160, v32\n" \
  "v_fmac_f32 v176, v161, v33\n" \
  "v_mfma_f32_16x16x32_bf16 v[156:159], v[100:103], v[132:135], v[156:159]\n" \
  "v_fmac_f32 v176, v162, v34\n" \
  "v_fmac_f32 v176, v163, v35\n" \
  "v_smfmac_f32_16x16x64_bf16 v[172:175], v[144:147], v[128:135], v193\n" \
  "ds_read_b128 v[128:131], v184 offset:0\n" \
  "v_fmac_f32 v176, v152, v36\n" \
  "v_fmac_f32 v176, v153, v37\n" \
  "v_smfmac_f32_16x16x64_bf16 v[172:175], v[148:151], v[136:143], v193\n" \
  "ds_read_b128 v[132:135], v184 offset:64\n" \
  "v_fmac_f32 v176, v154, v38\n" \
  "v_fmac_f32 v176, v155, v39\n" \
  "v_mfma_f32_16x16x32_bf16 v[160:163], v[96:99], v[112:115], 0\n" \
  "v_fmac_f32 v176, v156, v40\n" \
  "v_fmac_f32 v176, v157, v41\n" \
  "v_mfma_f32_16x16x32_bf16 v[160:163], v[100:103], v[116:119], v[160:163]\n" \
  "v_fmac_f32 v176, v158, v42\n" \
  "v_fmac_f32 v176, v159, v43\n" \
  "v_mfma_f32_16x16x32_bf16 v[152:155], v[104:107], v[112:115], 0\n" \
  "v_fmac_f32 v176, v172, v44\n" \
  "v_fmac_f32 v176, v173, v45\n" \
  "v_mad_u32_u16 v185, v181, 16, v188\n" \
  "v_mfma_f32_16x16x32_bf16 v[152:155], v[108:111], v[116:119], v[152:155]\n" \
  "v_fmac_f32 v176, v174, v46\n" \
  "v_fmac_f32 v176, v175, v47\n" \
  "v_mov_b64 v[172:173], 0\n" \
  "v_mfma_f32_16x16x32_bf16 v[156:159], v[104:107], v[136:139], 0\n" \
  "v_fmac_f32 v176, v160, v48\n" \
  "v_fmac_f32 v176, v161, v49\n" \
  "v_mad_u32_u16 v186, v182, 16, v188 op_sel:[1,0,0,0]\n" \
  "v_mfma_f32_16x16x32_bf16 v[156:159], v[108:111], v[140:143], v[156:159]\n" \
  "v_fmac_f32 v176, v162, v50\n" \
  "v_fmac_f32 v176, v163, v51\n" \
  "v_smfmac_f32_16x16x64_bf16 v[168:171], v[144:147], v[112:119], v193\n" \
  "ds_read_b128 v[112:115], v185 offset:0\n" \
  "v_fmac_f32 v176, v152, v52\n" \
  "v_fmac_f32 v176, v153, v53\n" \
  "v_smfmac_f32_16x16x64_bf16 v[168:171], v[148:151], v[120:127], v193\n" \
  "ds_read_b128 v[116:119], v185 offset:64\n" \
  "v_fmac_f32 v176, v154, v54\n" \
  "v_fmac_f32 v176, v155, v55\n" \
  "v_mfma_f32_16x16x32_bf16 v[160:163], v[96:99], v[136:139], 0\n" \
  "v_fmac_f32 v176, v156, v56\n" \
  "v_fmac_f32 v176, v157, v57\n" \
  "v_mfma_f32_16x16x32_bf16 v[160:163], v[100:103], v[140:143], v[160:163]\n" \
  "v_fmac_f32 v176, v158, v58\n" \
  "v_fmac_f32 v176, v159, v59\n" \
  "v_smfmac_f32_16x16x64_bf16 v[164:167], v[148:151], v[104:111], v193\n" \
  "ds_read_b128 v[136:139], v186 offset:0\n" \
  "v_fmac_f32 v176, v168, v60\n" \
  "v_fmac_f32 v176, v169, v61\n" \
  "v_smfmac_f32_16x16x64_bf16 v[164:167], v[144:147], v[96:103], v193\n" \
  "ds_read_b128 v[140:143], v186 offset:64\n" \
  "v_fmac_f32 v176, v170, v62\n" \
  "v_fmac_f32 v176, v171, v63\n" \
  "v_mfma_f32_16x16x32_bf16 v[152:155], v[120:123], v[120:123], 0\n" \
  "v_fmac_f32 v176, v160, v64\n" \
  "v_fmac_f32 v176, v161, v65\n" \
  "v_mfma_f32_16x16x32_bf16 v[152:155], v[124:127], v[124:127], v[152:155]\n" \
  "v_fmac_f32 v176, v162, v66\n" \
  "v_fmac_f32 v176, v163, v67\n" \
  "v_mad_u32_u16 v187, v181, 16, v188 op_sel:[1,0,0,0]\n" \
  "v_mfma_f32_16x16x32_bf16 v[156:159], v[104:107], v[120:123], 0\n" \
  "v_fmac_f32 v176, v164, v68\n" \
  "v_fmac_f32 v176, v165, v69\n" \
  "v_mov_b64 v[168:169], 0\n" \
  "v_mfma_f32_16x16x32_bf16 v[156:159], v[108:111], v[124:127], v[156:159]\n" \
  "v_fmac_f32 v176, v166, v70\n" \
  "v_fmac_f32 v176, v167, v71\n" \
  "v_mfma_f32_16x16x32_bf16 v[160:163], v[96:99], v[120:123], 0\n" \
  "ds_read_b128 v[120:123], v187 offset:0\n" \
  "v_fmac_f32 v176, v152, v72\n" \
  "v_fmac_f32 v176, v153, v73\n" \
  "v_mfma_f32_16x16x32_bf16 v[160:163], v[100:103], v[124:127], v[160:163]\n" \
  "v_fmac_f32 v176, v154, v74\n" \
  "v_fmac_f32 v176, v155, v75\n" \
  "v_mov_b64 v[164:165], 0\n" \
  "v_mfma_f32_16x16x32_bf16 v[152:155], v[96:99], v[104:107], 0\n" \
  "ds_read_b128 v[124:127], v187 offset:64\n" \
  "v_fmac_f32 v176, v156, v76\n" \
  "v_fmac_f32 v176, v157, v77\n" \
  "v_mad_u32_u16 v184, v180, 16, v188 op_sel:[1,0,0,0]\n" \
  "v_mfma_f32_16x16x32_bf16 v[152:155], v[100:103], v[108:111], v[152:155]\n" \
  "v_fmac_f32 v176, v158, v78\n" \
  "v_fmac_f32 v176, v159, v79\n" \
  "v_mfma_f32_16x16x32_bf16 v[156:159], v[104:107], v[104:107], 0\n" \
  "ds_read_b128 v[104:107], v184 offset:0\n" \
  "v_fmac_f32 v176, v160, v80\n" \
  "v_fmac_f32 v176, v161, v81\n" \
  "v_mfma_f32_16x16x32_bf16 v[156:159], v[108:111], v[108:111], v[156:159]\n" \
  "s_sub_u32 s51, s51, 1\n" \
  "v_fmac_f32 v176, v162, v82\n" \
  "v_fmac_f32 v176, v163, v83\n" \
  "v_mfma_f32_16x16x32_bf16 v[160:163], v[148:151], v[144:147], 0\n" \
  "s_cmp_lg_u32 s51, 0\n" \
  "ds_read_b128 v[108:111], v184 offset:64\n" \
  "v_fmac_f32 v176, v152, v84\n" \
  "v_fmac_f32 v176, v153, v85\n" \
  "v_mad_u32_u16 v185, v180, 16, v188\n" \
  "v_mfma_f32_16x16x32_bf16 v[160:163], v[96:99], v[96:99], v[160:163]\n" \
  "s_waitcnt lgkmcnt(7)\n" \
  "ds_read_b128 v[96:99], v185 offset:0\n" \
  "v_fmac_f32 v176, v154, v86\n" \
  "v_fmac_f32 v176, v155, v87\n" \
  "v_mfma_f32_16x16x32_bf16 v[160:163], v[100:103], v[100:103], v[160:163]\n" \
  "s_cbranch_scc1 LA_loop\n" \
  "s_setpc_b64 s[52:53]\n" \
  "LB_start:\n" \
  "s_setprio 3\n" \
  "s_mov_b32 s37, 12\n" \
  "L2_:\n" \
  "s_sub_u32 s35, s36, 125\n" \
  "s_min_u32 s35, s35, s36\n" \
  "s_mul_i32 m0, s35, 1040\n" \
  "s_lshl_b32 s35, s35, 11\n" \
  "s_add_u32 s42, s40, s35\n" \
  "s_addc_u32 s43, s41, 0\n" \
  "global_load_lds_dwordx4 v254, s[42:43]\n" \
  "s_add_u32 s36, s36, 4\n" \
  "s_sub_u32 s37, s37, 1\n" \
  "s_cmp_lg_u32 s37, 0\n" \
  "s_cbranch_scc1 L2_\n" \
  "v_mov_b32 v190, 163328\n" \
  "v_mov_b64 v[2:3], 0\n" \
  "ds_write_b64 v190, v[2:3]\n" \
  "ds_write_b64 v190, v[2:3] offset:8\n" \
  "ds_write_b64 v190, v[2:3] offset:16\n" \
  "s_waitcnt lgkmcnt(0)\n" \
  "s_barrier\n" \
  "v_lshlrev_b32 v247, 7, v255\n" \
  "s_mov_b32 s37, 20\n" \
  "L3_:\n" \
  "s_sub_u32 s35, s36, 125\n" \
  "s_min_u32 s35, s35, s36\n" \
  "s_mul_i32 m0, s35, 1040\n" \
  "s_lshl_b32 s35, s35, 11\n" \
  "s_add_u32 s42, s40, s35\n" \
  "s_addc_u32 s43, s41, 0\n" \
  "global_load_lds_dwordx4 v254, s[42:43]\n" \
  "s_add_u32 s36, s36, 4\n" \
  "s_sub_u32 s37, s37, 1\n" \
  "s_cmp_lg_u32 s37, 0\n" \
  "s_cbranch_scc1 L3_\n" \
  "s_add_u32 s48, s24, 110002176\n" \
  "s_addc_u32 s49, s25, 0\n" \
  "s_lshl_b32 s35, s26, 2\n" \
  "v_mov_b32 v193, s35\n" \
  "global_load_dword v193, v193, s[48:49] offset:1024 sc1\n" \
  "v_mov_b32 v191, 1\n" \
  "v_and_b32 v252, 15, v255\n" \
  "v_lshrrev_b32 v253, 4, v255\n" \
  "s_lshl2_add_u32 s35, s31, s28\n" \
  "s_lshl_b32 s35, s35, 1\n" \
  "v_bfe_u32 v1, v255, 2, 1\n" \
  "v_add_u32 v1, s35, v1\n" \
  "v_and_b32 v2, 3, v255\n" \
  "v_lshlrev_b32 v3, 5, v1\n" \
  "v_lshl_add_u32 v3, v2, 3, v3\n" \
  "v_lshlrev_b32 v4, 2, v2\n" \
  "v_add_u32 v4, 380, v4\n" \
  "v_cmp_gt_u32 vcc, 12, v1\n" \
  "v_lshrrev_b32 v5, 3, v255\n" \
  "v_mul_u32_u24 v5, 6400, v5\n" \
  "v_cndmask_b32 v3, v4, v3, vcc\n" \
  "v_add_u32 v5, 3200, v5\n" \
  "v_add_u32 v227, v5, v3\n" \
  "v_lshl_add_u32 v3, v1, 3, v2\n" \
  "v_lshl_add_u32 v4, v1, 3, 4\n" \
  "v_min_u32 v4, 96, v4\n" \
  "v_add_u32 v4, v4, v2\n" \
  "v_mul_u32_u24 v5, 205, v3\n" \
  "v_lshrrev_b32 v5, 11, v5\n" \
  "v_mul_u32_u24 v5, 10, v5\n" \
  "v_sub_u32 v5, v3, v5\n" \
  "v_lshlrev_b32 v236, 26, v5\n" \
  "v_mul_u32_u24 v5, 205, v4\n" \
  "v_lshrrev_b32 v5, 11, v5\n" \
  "v_mul_u32_u24 v5, 10, v5\n" \
  "v_sub_u32 v5, v4, v5\n" \
  "v_lshlrev_b32 v237, 26, v5\n" \
  "s_lshl2_add_u32 s4, s31, s28\n" \
  "s_lshl_b32 s4, s4, 7\n" \
  "s_add_u32 s36, s4, 96\n" \
  "s_min_u32 s36, s36, 984\n" \
  "s_add_u32 s37, s4, 112\n" \
  "s_min_u32 s37, s37, 984\n" \
  "v_add_u32 v186, s4, v252\n" \
  "v_add_u32 v187, s36, v252\n" \
  "v_add_u32 v188, s37, v252\n" \
  "s_mul_i32 s38, s7, 51200\n" \
  "s_add_u32 s42, s8, s38\n" \
  "s_addc_u32 s43, s9, 0\n" \
  "s_xor_b32 s38, s31, 1\n" \
  "s_lshl_b32 s38, s38, 7\n" \
  "s_add_u32 s94, s10, s38\n" \
  "s_addc_u32 s95, s11, 0\n" \
  "s_mul_i32 s5, s28, 4224\n" \
  "s_add_u32 s5, s5, 146432\n" \
  "v_lshrrev_b32 v2, 3, v186\n" \
  "v_lshl_add_u32 v2, v186, 3, v2\n" \
  "v_add_lshl_u32 v249, v2, v253, 4\n" \
  "v_lshrrev_b32 v2, 3, v187\n" \
  "v_lshl_add_u32 v2, v187, 3, v2\n" \
  "v_add_lshl_u32 v250, v2, v253, 4\n" \
  "v_lshrrev_b32 v2, 3, v188\n" \
  "v_lshl_add_u32 v2, v188, 3, v2\n" \
  "v_add_lshl_u32 v251, v2, v253, 4\n" \
  "v_mul_u32_u24 v1, 1600000, v253\n" \
  "v_lshl_add_u32 v186, v186, 4, v1\n" \
  "v_lshl_add_u32 v187, v187, 4, v1\n" \
  "v_lshl_add_u32 v188, v188, 4, v1\n" \
  "v_lshlrev_b32 v1, 4, v253\n" \
  "v_xor_b32 v244, v1, v254\n" \
  "v_lshl_add_u32 v245, v255, 4, s5\n" \
  "v_bfe_u32 v1, v252, 1, 2\n" \
  "v_xor_b32 v1, v1, v253\n" \
  "v_lshlrev_b32 v1, 4, v1\n" \
  "v_lshl_add_u32 v1, v252, 7, v1\n" \
  "v_add_u32 v246, s5, v1\n" \
  "v_lshrrev_b32 v1, 2, v255\n" \
  "s_lshl_b32 s35, s28, 5\n" \
  "s_lshl_b32 s36, s31, 6\n" \
  "s_add_u32 s35, s35, s36\n" \
  "s_and_b32 s35, s35, 127\n" \
  "v_add_u32 v1, s35, v1\n" \
  "v_lshrrev_b32 v2, 4, v255\n" \
  "v_and_b32 v2, 2, v2\n" \
  "v_xor_b32 v2, v2, v255\n" \
  "v_and_b32 v2, 3, v2\n" \
  "v_lshlrev_b32 v2, 4, v2\n" \
  "v_lshl_add_u32 v189, v1, 10, v2\n" \
  "v_add_u32 v194, 15360, v189\n" \
  "v_lshrrev_b32 v1, 2, v252\n" \
  "v_lshlrev_b32 v1, 6, v1\n" \
  "v_lshl_add_u32 v1, v253, 9, v1\n" \
  "v_and_b32 v2, 3, v252\n" \
  "v_lshl_add_u32 v1, v2, 3, v1\n" \
  "v_and_b32 v2, 1, v253\n" \
  "v_lshl_add_u32 v1, v2, 5, v1\n" \
  "v_add_u32 v184, 138240, v1\n" \
  "v_xor_b32 v1, 32, v1\n" \
  "v_add_u32 v185, 138240, v1\n" \
  "s_bfe_u32 s35, s30, 0x10001\n" \
  "s_lshl1_add_u32 s35, s29, s35\n" \
  "s_lshr_b32 s6, s30, 2\n" \
  "s_lshl_b32 s6, s6, 17\n" \
  "s_lshl_b32 s38, s35, 6\n" \
  "s_add_u32 s92, s14, s38\n" \
  "s_addc_u32 s93, s15, 0\n" \
  "s_lshr_b32 s37, s30, 3\n" \
  "s_add_u32 s37, s37, 96\n" \
  "s_lshl_b32 s34, s37, 17\n" \
  "s_mul_i32 s35, s35, 400\n" \
  "s_add_u32 s37, s37, s35\n" \
  "s_mul_i32 s37, s37, 16000\n" \
  "s_add_u32 s46, s24, s37\n" \
  "s_addc_u32 s47, s25, 0\n" \
  "s_and_b32 s36, s30, 4\n" \
  "s_lshr_b32 s37, s30, 3\n" \
  "s_add_u32 s36, s36, s37\n" \
  "s_add_u32 s36, s36, s35\n" \
  "s_mul_i32 s36, s36, 16000\n" \
  "s_add_u32 s44, s24, s36\n" \
  "s_addc_u32 s45, s25, 0\n" \
  "s_lshl_b32 s96, s28, 11\n" \
  "s_add_u32 s96, s96, 138240\n" \
  "s_add_u32 s35, s4, 0\n" \
  "s_min_u32 s35, s35, 984\n" \
  "s_lshl_b32 s35, s35, 8\n" \
  "s_add_u32 s58, s94, s35\n" \
  "s_addc_u32 s59, s95, 0\n" \
  "global_load_dwordx4 v[8:11], v244, s[58:59] offset:0\n" \
  "global_load_dwordx4 v[12:15], v244, s[58:59] offset:2048\n" \
  "s_add_u32 s35, s4, 16\n" \
  "s_min_u32 s35, s35, 984\n" \
  "s_lshl_b32 s35, s35, 8\n" \
  "s_add_u32 s58, s94, s35\n" \
  "s_addc_u32 s59, s95, 0\n" \
  "global_load_dwordx4 v[24:27], v244, s[58:59] offset:0\n" \
  "global_load_dwordx4 v[28:31], v244, s[58:59] offset:2048\n" \
  "s_add_u32 s35, s4, 32\n" \
  "s_min_u32 s35, s35, 984\n" \
  "s_lshl_b32 s35, s35, 8\n" \
  "s_add_u32 s58, s94, s35\n" \
  "s_addc_u32 s59, s95, 0\n" \
  "global_load_dwordx4 v[40:43], v244, s[58:59] offset:0\n" \
  "global_load_dwordx4 v[44:47], v244, s[58:59] offset:2048\n" \
  "s_add_u32 s35, s4, 48\n" \
  "s_min_u32 s35, s35, 984\n" \
  "s_lshl_b32 s35, s35, 8\n" \
  "s_add_u32 s58, s94, s35\n" \
  "s_addc_u32 s59, s95, 0\n" \
  "global_load_dwordx4 v[56:59], v244, s[58:59] offset:0\n" \
  "global_load_dwordx4 v[60:63], v244, s[58:59] offset:2048\n" \
  "s_waitcnt vmcnt(8)\n" \
  "s_mov_b64 exec, 1\n" \
  "ds_add_u32 v190, v191 offset:12\n" \
  "s_mov_b64 exec, -1\n" \
  "s_setprio 2\n" \
  "s_add_u32 s35, s4, 64\n" \
  "s_min_u32 s35, s35, 984\n" \
  "s_lshl_b32 s35, s35, 8\n" \
  "s_add_u32 s58, s94, s35\n" \
  "s_addc_u32 s59, s95, 0\n" \
  "global_load_dwordx4 v[72:75], v244, s[58:59] offset:0\n" \
  "global_load_dwordx4 v[76:79], v244, s[58:59] offset:2048\n" \
  "s_add_u32 s35, s4, 80\n" \
  "s_min_u32 s35, s35, 984\n" \
  "s_lshl_b32 s35, s35, 8\n" \
  "s_add_u32 s58, s94, s35\n" \
  "s_addc_u32 s59, s95, 0\n" \
  "global_load_dwordx4 v[88:91], v244, s[58:59] offset:0\n" \
  "global_load_dwordx4 v[92:95], v244, s[58:59] offset:2048\n" \
  "s_add_u32 s35, s4, 96\n" \
  "s_min_u32 s35, s35, 984\n" \
  "s_lshl_b32 s35, s35, 8\n" \
  "s_add_u32 s58, s94, s35\n" \
  "s_addc_u32 s59, s95, 0\n" \
  "global_load_dwordx4 v[104:107], v244, s[58:59] offset:0\n" \
  "global_load_dwordx4 v[108:111], v244, s[58:59] offset:2048\n" \
  "s_add_u32 s35, s4, 112\n" \
  "s_min_u32 s35, s35, 984\n" \
  "s_lshl_b32 s35, s35, 8\n" \
  "s_add_u32 s58, s94, s35\n" \
  "s_addc_u32 s59, s95, 0\n" \
  "global_load_dwordx4 v[120:123], v244, s[58:59] offset:0\n" \
  "global_load_dwordx4 v[124:127], v244, s[58:59] offset:2048\n" \
  "L4_:\n" \
  "ds_read_b32 v192, v190 offset:16\n" \
  "s_waitcnt lgkmcnt(0)\n" \
  "v_readfirstlane_b32 s35, v192\n" \
  "s_cmp_lt_u32 s35, 4\n" \
  "s_cbranch_scc1 L4_\n" \
  "s_add_u32 s40, s92, s6\n" \
  "s_addc_u32 s41, s93, 0\n" \
  "s_add_u32 s6, s6, 1048576\n" \
  "s_mov_b32 m0, s96\n" \
  "s_nop 0\n" \
  "global_load_lds_dwordx4 v189, s[40:41]\n" \
  "global_load_lds_dwordx4 v194, s[40:41] offset:1024\n" \
  "s_lshl2_add_u32 s35, s26, s28\n" \
  "s_mul_i32 s35, s35, 5888\n" \
  "s_add_u32 s35, s35, 1048576\n" \
  "s_add_u32 s58, s14, s35\n" \
  "s_addc_u32 s59, s15, 0\n" \
  "s_mov_b32 exec_lo, 0xffffffff\n" \
  "s_mov_b32 exec_hi, 0x3fff\n" \
  "global_load_dword v248, v247, s[58:59]\n" \
  "s_mov_b64 exec, -1\n" \
  "L5_:\n" \
  "ds_read_b32 v192, v190 offset:12\n" \
  "s_waitcnt lgkmcnt(0)\n" \
  "v_readfirstlane_b32 s35, v192\n" \
  "s_cmp_lt_u32 s35, 4\n" \
  "s_cbranch_scc1 L5_\n" \
  "ds_read_b128 v[0:3], v249 offset:0\n" \
  "ds_read_b128 v[4:7], v249 offset:64\n" \
  "global_load_dwordx2 v[196:197], v227, s[42:43] offset:-3200\n" \
  "global_load_dwordx2 v[198:199], v227, s[42:43] offset:-2800\n" \
  "s_waitcnt vmcnt(19)\n" \
  "ds_write_b128 v245, v[8:11] offset:0\n" \
  "ds_write_b128 v245, v[12:15] offset:1024\n" \
  "ds_read_b128 v[8:11], v246 offset:0\n" \
  "ds_read_b128 v[12:15], v246 offset:64\n" \
  "ds_read_b128 v[16:19], v249 offset:2080\n" \
  "ds_read_b128 v[20:23], v249 offset:2144\n" \
  "global_load_dwordx2 v[200:201], v227, s[42:43] offset:-2400\n" \
  "global_load_dwordx2 v[202:203], v227, s[42:43] offset:-2000\n" \
  "s_waitcnt vmcnt(19)\n" \
  "ds_write_b128 v245, v[24:27] offset:2048\n" \
  "ds_write_b128 v245, v[28:31] offset:3072\n" \
  "ds_read_b128 v[24:27], v246 offset:2048\n" \
  "ds_read_b128 v[28:31], v246 offset:2112\n" \
  "ds_read_b128 v[32:35], v249 offset:4160\n" \
  "ds_read_b128 v[36:39], v249 offset:4224\n" \
  "global_load_dwordx2 v[204:205], v227, s[42:43] offset:-1600\n" \
  "global_load_dwordx2 v[206:207], v227, s[42:43] offset:-1200\n" \
  "s_waitcnt vmcnt(19)\n" \
  "ds_write_b128 v245, v[40:43] offset:0\n" \
  "ds_write_b128 v245, v[44:47] offset:1024\n" \
  "ds_read_b128 v[40:43], v246 offset:0\n" \
  "ds_read_b128 v[44:47], v246 offset:64\n" \
  "ds_read_b128 v[48:51], v249 offset:6240\n" \
  "ds_read_b128 v[52:55], v249 offset:6304\n" \
  "global_load_dwordx2 v[208:209], v227, s[42:43] offset:-800\n" \
  "global_load_dwordx2 v[210:211], v227, s[42:43] offset:-400\n" \
  "s_waitcnt vmcnt(19)\n" \
  "ds_write_b128 v245, v[56:59] offset:2048\n" \
  "ds_write_b128 v245, v[60:63] offset:3072\n" \
  "ds_read_b128 v[56:59], v246 offset:2048\n" \
  "ds_read_b128 v[60:63], v246 offset:2112\n" \
  "ds_read_b128 v[64:67], v249 offset:8320\n" \
  "ds_read_b128 v[68:71], v249 offset:8384\n" \
  "global_load_dwordx2 v[212:213], v227, s[42:43] offset:0\n" \
  "global_load_dwordx2 v[214:215], v227, s[42:43] offset:400\n" \
  "s_waitcnt vmcnt(19)\n" \
  "ds_write_b128 v245, v[72:75] offset:0\n" \
  "ds_write_b128 v245, v[76:79] offset:1024\n" \
  "ds_read_b128 v[72:75], v246 offset:0\n" \
  "ds_read_b128 v[76:79], v246 offset:64\n" \
  "ds_read_b128 v[80:83], v249 offset:10400\n" \
  "ds_read_b128 v[84:87], v249 offset:10464\n" \
  "global_load_dwordx2 v[216:217], v227, s[42:43] offset:800\n" \
  "global_load_dwordx2 v[218:219], v227, s[42:43] offset:1200\n" \
  "s_waitcnt vmcnt(19)\n" \
  "ds_write_b128 v245, v[88:91] offset:2048\n" \
  "ds_write_b128 v245, v[92:95] offset:3072\n" \
  "ds_read_b128 v[88:91], v246 offset:2048\n" \
  "ds_read_b128 v[92:95], v246 offset:2112\n" \
  "ds_read_b128 v[96:99], v250 offset:0\n" \
  "ds_read_b128 v[100:103], v250 offset:64\n" \
  "global_load_dwordx2 v[220:221], v227, s[42:43] offset:1600\n" \
  "global_load_dwordx2 v[222:223], v227, s[42:43] offset:2000\n" \
  "s_waitcnt vmcnt(19)\n" \
  "ds_write_b128 v245, v[104:107] offset:0\n" \
  "ds_write_b128 v245, v[108:111] offset:1024\n" \
  "ds_read_b128 v[104:107], v246 offset:0\n" \
  "ds_read_b128 v[108:111], v246 offset:64\n" \
  "ds_read_b128 v[112:115], v251 offset:0\n" \
  "ds_read_b128 v[116:119], v251 offset:64\n" \
  "global_load_dwordx2 v[224:225], v227, s[42:43] offset:2400\n" \
  "global_load_dwordx2 v[226:227], v227, s[42:43] offset:2800\n" \
  "s_waitcnt vmcnt(19)\n" \
  "ds_write_b128 v245, v[120:123] offset:2048\n" \
  "ds_write_b128 v245, v[124:127] offset:3072\n" \
  "ds_read_b128 v[120:123], v246 offset:2048\n" \
  "ds_read_b128 v[124:127], v246 offset:2112\n" \
  "s_getpc_b64 s[58:59]\n" \
  "LB_pc:\n" \
  "s_sub_u32 s58, s58, LB_pc-LZ_first\n" \
  "s_subb_u32 s59, s59, 0\n" \
  "s_mul_i32 s35, s28, 6656\n" \
  "v_lshl_add_u32 v195, v255, 7, s35\n" \
  "v_min_u32 v195, LZ_end-LZ_first, v195\n" \
  "s_mov_b32 exec_hi, 0xfffff\n" \
  "global_load_dword v248, v195, s[58:59]\n" \
  "s_mov_b64 exec, -1\n" \
  "s_lshl2_add_u32 s35, s26, s28\n" \
  "s_mul_i32 s35, s35, 5888\n" \
  "s_add_u32 s35, s35, 7077888\n" \
  "s_add_u32 s58, s14, s35\n" \
  "s_addc_u32 s59, s15, 0\n" \
  "s_mov_b32 exec_lo, 0xffffffff\n" \
  "s_mov_b32 exec_hi, 0x3fff\n" \
  "global_load_dword v248, v247, s[58:59]\n" \
  "s_mov_b64 exec, -1\n" \
  "s_setprio 2\n" \
  "v_readfirstlane_b32 s39, v193\n" \
  "s_lshl_b32 s35, s26, 2\n" \
  "s_add_u32 s39, s39, 1\n" \
  "v_mov_b32 v193, s35\n" \
  "v_lshlrev_b32 v238, 4, v255\n" \
  "s_mov_b32 s51, 0x3e7\n" \
  "s_mov_b32 s52, 0x10002\n" \
  "s_mov_b32 s53, 0xc0c0400\n" \
  "s_mov_b32 s54, 0x4000c0c\n" \
  "s_mov_b32 s55, 0x3030303\n" \
  "s_mov_b32 s56, 0x55555555\n" \
  "s_mov_b32 s57, 0x7060302\n" \
  "s_mov_b32 s50, 0x0\n" \
  "s_mov_b32 s98, 0x0\n" \
  "s_mov_b32 s99, 0x8\n" \
  "s_mov_b32 s97, 0x2000\n" \
  "v_mov_b64 v[234:235], 0\n" \
  "s_lshl2_add_u32 s33, s26, s28\n" \
  "v_mov_b64 v[240:241], -1\n" \
  "v_mov_b64 v[242:243], -1\n" \
  "s_lshr_b32 s35, s30, 2\n" \
  "s_lshl3_add_u32 s35, s29, s35\n" \
  "s_and_b32 s36, s30, 3\n" \
  "s_lshl2_add_u32 s35, s35, s36\n" \
  "s_lshl2_add_u32 s35, s35, s28\n" \
  "s_lshl_b32 s35, s35, 12\n" \
  "s_add_u32 s35, s35, 105676800\n" \
  "s_add_u32 s58, s24, s35\n" \
  "s_addc_u32 s59, s25, 0\n" \
  "s_waitcnt vmcnt(19)\n" \
  "s_waitcnt lgkmcnt(0)\n" \
  "s_mov_b64 exec, 1\n" \
  "ds_add_u32 v190, v191 offset:0\n" \
  "s_mov_b64 exec, -1\n" \
  "L6_:\n" \
  "ds_read_b32 v192, v190 offset:0\n" \
  "s_waitcnt lgkmcnt(0)\n" \
  "v_readfirstlane_b32 s35, v192\n" \
  "s_cmp_lt_u32 s35, 4\n" \
  "s_cbranch_scc1 L6_\n" \
  "s_add_u32 s96, s96, 8192\n" \
  "s_add_u32 s40, s92, s6\n" \
  "s_addc_u32 s41, s93, 0\n" \
  "s_add_u32 s6, s6, 1048576\n" \
  "s_mov_b32 m0, s96\n" \
  "s_nop 0\n" \
  "global_load_lds_dwordx4 v189, s[40:41]\n" \
  "global_load_lds_dwordx4 v194, s[40:41] offset:1024\n" \
  "s_sub_u32 s96, s96, 8192\n" \
  "ds_read_b64_tr_b16 v[128:129], v184 offset:0\n" \
  "ds_read_b64_tr_b16 v[130:131], v184 offset:256\n" \
  "ds_read_b64_tr_b16 v[144:145], v185 offset:0\n" \
  "ds_read_b64_tr_b16 v[146:147], v185 offset:256\n" \
  "ds_read_b64_tr_b16 v[132:133], v184 offset:2048\n" \
  "ds_read_b64_tr_b16 v[134:135], v184 offset:2304\n" \
  "ds_read_b64_tr_b16 v[148:149], v185 offset:2048\n" \
  "ds_read_b64_tr_b16 v[150:151], v185 offset:2304\n" \
  "ds_read_b64_tr_b16 v[136:137], v184 offset:4096\n" \
  "ds_read_b64_tr_b16 v[138:139], v184 offset:4352\n" \
  "ds_read_b64_tr_b16 v[152:153], v185 offset:4096\n" \
  "ds_read_b64_tr_b16 v[154:155], v185 offset:4352\n" \
  "ds_read_b64_tr_b16 v[140:141], v184 offset:6144\n" \
  "ds_read_b64_tr_b16 v[142:143], v184 offset:6400\n" \
  "ds_read_b64_tr_b16 v[156:157], v185 offset:6144\n" \
  "ds_read_b64_tr_b16 v[158:159], v185 offset:6400\n" \
  "global_store_dwordx4 v238, v[240:243], s[58:59] offset:0 sc1\n" \
  "global_store_dwordx4 v238, v[240:243], s[58:59] offset:1024 sc1\n" \
  "global_store_dwordx4 v238, v[240:243], s[58:59] offset:2048 sc1\n" \
  "global_store_dwordx4 v238, v[240:243], s[58:59] offset:3072 sc1\n" \
  "s_lshl2_add_u32 s35, s7, s28\n" \
  "s_lshl_b32 s35, s35, 7\n" \
  "s_lshl_b32 s36, s31, 16\n" \
  "s_add_u32 s35, s35, s36\n" \
  "s_add_u32 s35, s35, 109871104\n" \
  "s_add_u32 s58, s24, s35\n" \
  "s_addc_u32 s59, s25, 0\n" \
  "s_mov_b64 exec, 0xff\n" \
  "global_store_dwordx4 v238, v[240:243], s[58:59] sc1\n" \
  "s_mov_b64 exec, -1\n" \
  "s_waitcnt vmcnt(9)\n" \
  "v_mov_b32 v192, s39\n" \
  "s_cmp_lg_u32 s28, 0\n" \
  "s_cbranch_scc1 LB_nc\n" \
  "s_mov_b64 exec, 1\n" \
  "global_store_dword v193, v192, s[48:49] offset:0 sc1\n" \
  "s_mov_b64 exec, -1\n" \
  "LB_nc:\n" \
  "s_cmp_lg_u32 s33, 1021\n" \
  "s_cbranch_scc1 LB_nm\n" \
  "v_lshlrev_b32 v242, 8, v255\n" \
  "v_mov_b64 v[240:241], 0\n" \
  "global_store_dwordx2 v242, v[240:241], s[48:49] offset:2048 sc1\n" \
  "v_add_u32 v242, 0x4000, v242\n" \
  "global_store_dwordx2 v242, v[240:241], s[48:49] offset:2048 sc1\n" \
  "v_add_u32 v242, 0x4000, v242\n" \
  "global_store_dwordx2 v242, v[240:241], s[48:49] offset:2048 sc1\n" \
  "v_add_u32 v242, 0x4000, v242\n" \
  "global_store_dwordx2 v242, v[240:241], s[48:49] offset:2048 sc1\n" \
  "v_add_u32 v242, 0x4000, v242\n" \
  "LB_nm:\n" \
  "LT1:\n" \
  "s_waitcnt lgkmcnt(14)\n" \
  "v_mfma_f32_16x16x32_bf16 v[160:163], v[128:131], v[0:3], 0\n" \
  "s_and_b32 s35, s98, 3\n" \
  "s_cmp_eq_u32 s35, 2\n" \
  "s_cselect_b32 vcc_lo, 0x33333333, -1\n" \
  "s_mov_b32 vcc_hi, vcc_lo\n" \
  "v_cndmask_b32_dpp v234, v234, v234, vcc row_ror:8 row_mask:0xf bank_mask:0xf\n" \
  "v_cndmask_b32_dpp v235, v235, v235, vcc row_ror:8 row_mask:0xf bank_mask:0xf\n" \
  "s_waitcnt lgkmcnt(12)\n" \
  "v_mfma_f32_16x16x32_bf16 v[164:167], v[144:147], v[0:3], 0\n" \
  "s_set_gpr_idx_on s50, gpr_idx(SRC0)\n" \
  "v_min_u32_e64 v232, v196, s51\n" \
  "v_min_u32_e64 v233, v197, s51\n" \
  "s_set_gpr_idx_off\n" \
  "s_waitcnt lgkmcnt(10)\n" \
  "v_mfma_f32_16x16x32_bf16 v[160:163], v[132:135], v[4:7], v[160:163]\n" \
  "v_mad_u32_u24 v232, v232, s52, v236\n" \
  "v_lshrrev_b32 v228, v232, v234\n" \
  "s_nop 0\n" \
  "s_waitcnt lgkmcnt(8)\n" \
  "v_mfma_f32_16x16x32_bf16 v[164:167], v[148:151], v[4:7], v[164:167]\n" \
  "v_lshrrev_b32_dpp v229, v232, v234 quad_perm:[2,3,0,1] row_mask:0xf bank_mask:0xf\n" \
  "v_lshrrev_b32_dpp v230, v232, v234 quad_perm:[1,2,3,0] row_mask:0xf bank_mask:0xf\n" \
  "s_nop 0\n" \
  "s_waitcnt lgkmcnt(6)\n" \
  "v_mfma_f32_16x16x32_bf16 v[160:163], v[136:139], v[8:11], v[160:163]\n" \
  "v_lshrrev_b32_dpp v231, v232, v234 quad_perm:[3,0,1,2] row_mask:0xf bank_mask:0xf\n" \
  "v_perm_b32 v228, v229, v228, s53\n" \
  "s_nop 0\n" \
  "s_waitcnt lgkmcnt(4)\n" \
  "v_mfma_f32_16x16x32_bf16 v[164:167], v[152:155], v[8:11], v[164:167]\n" \
  "v_perm_b32 v230, v231, v230, s54\n" \
  "v_bitop3_b32 v228, v228, v230, s55 bitop3:0xa8\n" \
  "s_nop 0\n" \
  "s_waitcnt lgkmcnt(2)\n" \
  "v_mfma_f32_16x16x32_bf16 v[160:163], v[140:143], v[12:15], v[160:163]\n" \
  "v_lshl_or_b32 v229, v234, 1, s56\n" \
  "v_add_u32_dpp v228, v228, v228 quad_perm:[2,3,0,1] row_mask:0xf bank_mask:0xf\n" \
  "s_nop 0\n" \
  "s_waitcnt lgkmcnt(0)\n" \
  "v_mfma_f32_16x16x32_bf16 v[164:167], v[156:159], v[12:15], v[164:167]\n" \
  "v_min_u32_sdwa v230, v228, v228 dst_sel:DWORD dst_unused:UNUSED_PAD src0_sel:BYTE_0 src1_sel:BYTE_1\n" \
  "s_nop 2\n" \
  "v_mfma_f32_16x16x32_bf16 v[168:171], v[128:131], v[16:19], 0\n" \
  "v_min_u32_sdwa v231, v228, v228 dst_sel:DWORD dst_unused:UNUSED_PAD src0_sel:BYTE_2 src1_sel:BYTE_3\n" \
  "v_sub_u32 v230, v230, v231\n" \
  "s_nop 1\n" \
  "v_mfma_f32_16x16x32_bf16 v[172:175], v[144:147], v[16:19], 0\n" \
  "v_mad_u32_u24 v233, v233, s52, v237\n" \
  "v_add_u32_dpp v230, v230, v230 quad_perm:[1,0,3,2] row_mask:0xf bank_mask:0xf\n" \
  "s_nop 1\n" \
  "v_mfma_f32_16x16x32_bf16 v[168:171], v[132:135], v[20:23], v[168:171]\n" \
  "v_cmp_ge_i32 vcc, 0, v230\n" \
  "s_nop 1\n" \
  "v_cndmask_b32_sdwa v228, v228, v228, vcc dst_sel:DWORD dst_unused:UNUSED_PAD src0_sel:WORD_1 src1_sel:WORD_0\n" \
  "s_mov_b64 s[58:59], vcc\n" \
  "s_nop 0\n" \
  "v_mfma_f32_16x16x32_bf16 v[172:175], v[148:151], v[20:23], v[172:175]\n" \
  "v_cvt_pk_bf16_f32 v176, v160, v161\n" \
  "v_cmp_le_u32_sdwa vcc, v228, v228 src0_sel:BYTE_0 src1_sel:BYTE_1\n" \
  "s_nop 1\n" \
  "v_mfma_f32_16x16x32_bf16 v[168:171], v[136:139], v[24:27], v[168:171]\n" \
  "v_cvt_pk_bf16_f32 v177, v162, v163\n" \
  "v_cndmask_b32_dpp v232, v232, v232, vcc quad_perm:[2,3,0,1] row_mask:0xf bank_mask:0xf\n" \
  "s_mov_b64 vcc, s[58:59]\n" \
  "s_nop 0\n" \
  "v_mfma_f32_16x16x32_bf16 v[172:175], v[152:155], v[24:27], v[172:175]\n" \
  "v_cvt_pk_bf16_f32 v178, v164, v165\n" \
  "v_cndmask_b32_dpp v232, v232, v232, vcc quad_perm:[3,0,1,2] row_mask:0xf bank_mask:0xf\n" \
  "s_nop 1\n" \
  "v_mfma_f32_16x16x32_bf16 v[168:171], v[140:143], v[28:31], v[168:171]\n" \
  "v_cvt_pk_bf16_f32 v179, v166, v167\n" \
  "v_lshlrev_b32_e64 v230, v232, 3\n" \
  "s_nop 1\n" \
  "v_mfma_f32_16x16x32_bf16 v[172:175], v[156:159], v[28:31], v[172:175]\n" \
  "s_waitcnt vmcnt(5)\n" \
  "global_store_dwordx4 v186, v[176:179], s[44:45] offset:0 sc1\n" \
  "v_and_or_b32 v234, v229, v230, v234\n" \
  "s_nop 0\n" \
  "v_mfma_f32_16x16x32_bf16 v[160:163], v[128:131], v[32:35], 0\n" \
  "s_cmp_lg_u32 s33, 1021\n" \
  "s_cbranch_scc0 LT1_e\n" \
  "LT1_r:\n" \
  "v_lshrrev_b32 v228, v233, v235\n" \
  "v_lshrrev_b32_dpp v229, v233, v235 quad_perm:[2,3,0,1] row_mask:0xf bank_mask:0xf\n" \
  "v_mfma_f32_16x16x32_bf16 v[164:167], v[144:147], v[32:35], 0\n" \
  "v_lshrrev_b32_dpp v230, v233, v235 quad_perm:[1,2,3,0] row_mask:0xf bank_mask:0xf\n" \
  "v_lshrrev_b32_dpp v231, v233, v235 quad_perm:[3,0,1,2] row_mask:0xf bank_mask:0xf\n" \
  "s_nop 1\n" \
  "v_mfma_f32_16x16x32_bf16 v[160:163], v[132:135], v[36:39], v[160:163]\n" \
  "v_perm_b32 v228, v229, v228, s53\n" \
  "v_perm_b32 v230, v231, v230, s54\n" \
  "s_nop 1\n" \
  "v_mfma_f32_16x16x32_bf16 v[164:167], v[148:151], v[36:39], v[164:167]\n" \
  "v_cvt_pk_bf16_f32 v180, v168, v169\n" \
  "v_bitop3_b32 v228, v228, v230, s55 bitop3:0xa8\n" \
  "s_nop 1\n" \
  "v_mfma_f32_16x16x32_bf16 v[160:163], v[136:139], v[40:43], v[160:163]\n" \
  "v_cvt_pk_bf16_f32 v181, v170, v171\n" \
  "v_lshl_or_b32 v229, v235, 1, s56\n" \
  "s_nop 1\n" \
  "v_mfma_f32_16x16x32_bf16 v[164:167], v[152:155], v[40:43], v[164:167]\n" \
  "v_cvt_pk_bf16_f32 v182, v172, v173\n" \
  "v_add_u32_dpp v228, v228, v228 quad_perm:[2,3,0,1] row_mask:0xf bank_mask:0xf\n" \
  "s_nop 1\n" \
  "v_mfma_f32_16x16x32_bf16 v[160:163], v[140:143], v[44:47], v[160:163]\n" \
  "v_cvt_pk_bf16_f32 v183, v174, v175\n" \
  "v_min_u32_sdwa v230, v228, v228 dst_sel:DWORD dst_unused:UNUSED_PAD src0_sel:BYTE_0 src1_sel:BYTE_1\n" \
  "s_nop 1\n" \
  "v_mfma_f32_16x16x32_bf16 v[164:167], v[156:159], v[44:47], v[164:167]\n" \
  "s_waitcnt vmcnt(5)\n" \
  "global_store_dwordx4 v186, v[180:183], s[44:45] offset:256 sc1\n" \
  "v_min_u32_sdwa v231, v228, v228 dst_sel:DWORD dst_unused:UNUSED_PAD src0_sel:BYTE_2 src1_sel:BYTE_3\n" \
  "s_nop 0\n" \
  "v_mfma_f32_16x16x32_bf16 v[168:171], v[128:131], v[48:51], 0\n" \
  "v_sub_u32 v230, v230, v231\n" \
  "s_nop 1\n" \
  "v_add_u32_dpp v230, v230, v230 quad_perm:[1,0,3,2] row_mask:0xf bank_mask:0xf\n" \
  "s_nop 1\n" \
  "v_mfma_f32_16x16x32_bf16 v[172:175], v[144:147], v[48:51], 0\n" \
  "v_cmp_ge_i32 vcc, 0, v230\n" \
  "s_nop 1\n" \
  "v_cndmask_b32_sdwa v228, v228, v228, vcc dst_sel:DWORD dst_unused:UNUSED_PAD src0_sel:WORD_1 src1_sel:WORD_0\n" \
  "s_mov_b64 s[58:59], vcc\n" \
  "s_nop 0\n" \
  "v_mfma_f32_16x16x32_bf16 v[168:171], v[132:135], v[52:55], v[168:171]\n" \
  "v_cmp_le_u32_sdwa vcc, v228, v228 src0_sel:BYTE_0 src1_sel:BYTE_1\n" \
  "s_nop 1\n" \
  "v_cndmask_b32_dpp v233, v233, v233, vcc quad_perm:[2,3,0,1] row_mask:0xf bank_mask:0xf\n" \
  "s_mov_b64 vcc, s[58:59]\n" \
  "s_nop 0\n" \
  "v_mfma_f32_16x16x32_bf16 v[172:175], v[148:151], v[52:55], v[172:175]\n" \
  "v_cvt_pk_bf16_f32 v176, v160, v161\n" \
  "v_cndmask_b32_dpp v233, v233, v233, vcc quad_perm:[3,0,1,2] row_mask:0xf bank_mask:0xf\n" \
  "s_nop 1\n" \
  "v_mfma_f32_16x16x32_bf16 v[168:171], v[136:139], v[56:59], v[168:171]\n" \
  "v_cvt_pk_bf16_f32 v177, v162, v163\n" \
  "v_lshlrev_b32_e64 v230, v233, 3\n" \
  "s_nop 1\n" \
  "v_mfma_f32_16x16x32_bf16 v[172:175], v[152:155], v[56:59], v[172:175]\n" \
  "v_cvt_pk_bf16_f32 v178, v164, v165\n" \
  "v_and_or_b32 v235, v229, v230, v235\n" \
  "s_nop 1\n" \
  "v_mfma_f32_16x16x32_bf16 v[168:171], v[140:143], v[60:63], v[168:171]\n" \
  "v_cvt_pk_bf16_f32 v179, v166, v167\n" \
  "s_set_gpr_idx_on s50, gpr_idx(DST)\n" \
  "v_perm_b32 v196, v233, v232, s57\n" \
  "s_set_gpr_idx_off\n" \
  "s_add_u32 s50, s50, 2\n" \
  "v_mfma_f32_16x16x32_bf16 v[172:175], v[156:159], v[60:63], v[172:175]\n" \
  "s_waitcnt vmcnt(5)\n" \
  "global_store_dwordx4 v186, v[176:179], s[44:45] offset:512 sc1\n" \
  "s_set_gpr_idx_on s50, gpr_idx(SRC0)\n" \
  "v_min_u32_e64 v232, v196, s51\n" \
  "v_min_u32_e64 v233, v197, s51\n" \
  "s_set_gpr_idx_off\n" \
  "v_mfma_f32_16x16x32_bf16 v[160:163], v[128:131], v[64:67], 0\n" \
  "v_mad_u32_u24 v232, v232, s52, v236\n" \
  "s_nop 2\n" \
  "v_mfma_f32_16x16x32_bf16 v[164:167], v[144:147], v[64:67], 0\n" \
  "v_lshrrev_b32 v228, v232, v234\n" \
  "v_lshrrev_b32_dpp v229, v232, v234 quad_perm:[2,3,0,1] row_mask:0xf bank_mask:0xf\n" \
  "s_nop 1\n" \
  "v_mfma_f32_16x16x32_bf16 v[160:163], v[132:135], v[68:71], v[160:163]\n" \
  "v_lshrrev_b32_dpp v230, v232, v234 quad_perm:[1,2,3,0] row_mask:0xf bank_mask:0xf\n" \
  "v_lshrrev_b32_dpp v231, v232, v234 quad_perm:[3,0,1,2] row_mask:0xf bank_mask:0xf\n" \
  "s_nop 1\n" \
  "v_mfma_f32_16x16x32_bf16 v[164:167], v[148:151], v[68:71], v[164:167]\n" \
  "v_cvt_pk_bf16_f32 v180, v168, v169\n" \
  "v_perm_b32 v228, v229, v228, s53\n" \
  "s_nop 1\n" \
  "v_mfma_f32_16x16x32_bf16 v[160:163], v[136:139], v[72:75], v[160:163]\n" \
  "v_cvt_pk_bf16_f32 v181, v170, v171\n" \
  "v_perm_b32 v230, v231, v230, s54\n" \
  "s_nop 1\n" \
  "v_mfma_f32_16x16x32_bf16 v[164:167], v[152:155], v[72:75], v[164:167]\n" \
  "v_cvt_pk_bf16_f32 v182, v172, v173\n" \
  "v_bitop3_b32 v228, v228, v230, s55 bitop3:0xa8\n" \
  "s_nop 1\n" \
  "v_mfma_f32_16x16x32_bf16 v[160:163], v[140:143], v[76:79], v[160:163]\n" \
  "v_cvt_pk_bf16_f32 v183, v174, v175\n" \
  "v_lshl_or_b32 v229, v234, 1, s56\n" \
  "s_nop 1\n" \
  "v_mfma_f32_16x16x32_bf16 v[164:167], v[156:159], v[76:79], v[164:167]\n" \
  "s_waitcnt vmcnt(5)\n" \
  "global_store_dwordx4 v186, v[180:183], s[44:45] offset:768 sc1\n" \
  "v_add_u32_dpp v228, v228, v228 quad_perm:[2,3,0,1] row_mask:0xf bank_mask:0xf\n" \
  "s_nop 0\n" \
  "v_mfma_f32_16x16x32_bf16 v[168:171], v[128:131], v[80:83], 0\n" \
  "s_mov_b64 exec, 1\n" \
  "ds_add_u32 v190, v191 offset:0\n" \
  "s_mov_b64 exec, -1\n" \
  "v_min_u32_sdwa v230, v228, v228 dst_sel:DWORD dst_unused:UNUSED_PAD src0_sel:BYTE_0 src1_sel:BYTE_1\n" \
  "v_min_u32_sdwa v231, v228, v228 dst_sel:DWORD dst_unused:UNUSED_PAD src0_sel:BYTE_2 src1_sel:BYTE_3\n" \
  "v_mfma_f32_16x16x32_bf16 v[172:175], v[144:147], v[80:83], 0\n" \
  "v_sub_u32 v230, v230, v231\n" \
  "v_mad_u32_u24 v233, v233, s52, v237\n" \
  "s_nop 1\n" \
  "v_mfma_f32_16x16x32_bf16 v[168:171], v[132:135], v[84:87], v[168:171]\n" \
  "v_add_u32_dpp v230, v230, v230 quad_perm:[1,0,3,2] row_mask:0xf bank_mask:0xf\n" \
  "v_cmp_ge_i32 vcc, 0, v230\n" \
  "s_nop 1\n" \
  "v_mfma_f32_16x16x32_bf16 v[172:175], v[148:151], v[84:87], v[172:175]\n" \
  "v_cvt_pk_bf16_f32 v176, v160, v161\n" \
  "v_cndmask_b32_sdwa v228, v228, v228, vcc dst_sel:DWORD dst_unused:UNUSED_PAD src0_sel:WORD_1 src1_sel:WORD_0\n" \
  "s_mov_b64 s[58:59], vcc\n" \
  "s_nop 0\n" \
  "v_mfma_f32_16x16x32_bf16 v[168:171], v[136:139], v[88:91], v[168:171]\n" \
  "v_cvt_pk_bf16_f32 v177, v162, v163\n" \
  "v_cmp_le_u32_sdwa vcc, v228, v228 src0_sel:BYTE_0 src1_sel:BYTE_1\n" \
  "s_nop 1\n" \
  "v_mfma_f32_16x16x32_bf16 v[172:175], v[152:155], v[88:91], v[172:175]\n" \
  "v_cvt_pk_bf16_f32 v178, v164, v165\n" \
  "v_cndmask_b32_dpp v232, v232, v232, vcc quad_perm:[2,3,0,1] row_mask:0xf bank_mask:0xf\n" \
  "s_mov_b64 vcc, s[58:59]\n" \
  "s_nop 0\n" \
  "v_mfma_f32_16x16x32_bf16 v[168:171], v[140:143], v[92:95], v[168:171]\n" \
  "v_cvt_pk_bf16_f32 v179, v166, v167\n" \
  "v_cndmask_b32_dpp v232, v232, v232, vcc quad_perm:[3,0,1,2] row_mask:0xf bank_mask:0xf\n" \
  "s_nop 1\n" \
  "v_mfma_f32_16x16x32_bf16 v[172:175], v[156:159], v[92:95], v[172:175]\n" \
  "s_waitcnt vmcnt(5)\n" \
  "global_store_dwordx4 v186, v[176:179], s[44:45] offset:1024 sc1\n" \
  "v_lshlrev_b32_e64 v230, v232, 3\n" \
  "s_nop 0\n" \
  "v_mfma_f32_16x16x32_bf16 v[160:163], v[128:131], v[96:99], 0\n" \
  "ds_read_b32 v192, v190\n" \
  "v_add_u32 v184, s97, v184\n" \
  "v_and_or_b32 v234, v229, v230, v234\n" \
  "v_lshrrev_b32 v228, v233, v235\n" \
  "v_mfma_f32_16x16x32_bf16 v[164:167], v[144:147], v[96:99], 0\n" \
  "v_add_u32 v185, s97, v185\n" \
  "v_lshrrev_b32_dpp v229, v233, v235 quad_perm:[2,3,0,1] row_mask:0xf bank_mask:0xf\n" \
  "v_lshrrev_b32_dpp v230, v233, v235 quad_perm:[1,2,3,0] row_mask:0xf bank_mask:0xf\n" \
  "s_nop 0\n" \
  "v_mfma_f32_16x16x32_bf16 v[160:163], v[132:135], v[100:103], v[160:163]\n" \
  "s_cmp_lt_u32 s98, 10\n" \
  "s_cselect_b32 s6, s6, s34\n" \
  "s_add_u32 s40, s92, s6\n" \
  "s_addc_u32 s41, s93, 0\n" \
  "s_add_u32 s6, s6, 1048576\n" \
  "v_lshrrev_b32_dpp v231, v233, v235 quad_perm:[3,0,1,2] row_mask:0xf bank_mask:0xf\n" \
  "v_perm_b32 v228, v229, v228, s53\n" \
  "v_mfma_f32_16x16x32_bf16 v[164:167], v[148:151], v[100:103], v[164:167]\n" \
  "v_cvt_pk_bf16_f32 v180, v168, v169\n" \
  "v_perm_b32 v230, v231, v230, s54\n" \
  "s_nop 1\n" \
  "v_mfma_f32_16x16x32_bf16 v[160:163], v[136:139], v[104:107], v[160:163]\n" \
  "v_cvt_pk_bf16_f32 v181, v170, v171\n" \
  "v_bitop3_b32 v228, v228, v230, s55 bitop3:0xa8\n" \
  "s_nop 1\n" \
  "v_mfma_f32_16x16x32_bf16 v[164:167], v[152:155], v[104:107], v[164:167]\n" \
  "s_waitcnt lgkmcnt(0)\n" \
  "v_readfirstlane_b32 s35, v192\n" \
  "v_cvt_pk_bf16_f32 v182, v172, v173\n" \
  "v_lshl_or_b32 v229, v235, 1, s56\n" \
  "v_mfma_f32_16x16x32_bf16 v[160:163], v[140:143], v[108:111], v[160:163]\n" \
  "s_cmp_lt_u32 s35, s99\n" \
  "s_cbranch_scc1 LT1_w\n" \
  "LT1_c:\n" \
  "s_mov_b32 m0, s96\n" \
  "s_nop 0\n" \
  "global_load_lds_dwordx4 v189, s[40:41]\n" \
  "global_load_lds_dwordx4 v194, s[40:41] offset:1024\n" \
  "s_add_u32 s96, s96, s97\n" \
  "s_sub_u32 s97, 0, s97\n" \
  "s_add_u32 s99, s99, 4\n" \
  "v_cvt_pk_bf16_f32 v183, v174, v175\n" \
  "v_add_u32_dpp v228, v228, v228 quad_perm:[2,3,0,1] row_mask:0xf bank_mask:0xf\n" \
  "v_mfma_f32_16x16x32_bf16 v[164:167], v[156:159], v[108:111], v[164:167]\n" \
  "s_waitcnt vmcnt(5)\n" \
  "global_store_dwordx4 v186, v[180:183], s[44:45] offset:1280 sc1\n" \
  "v_min_u32_sdwa v230, v228, v228 dst_sel:DWORD dst_unused:UNUSED_PAD src0_sel:BYTE_0 src1_sel:BYTE_1\n" \
  "s_nop 0\n" \
  "v_mfma_f32_16x16x32_bf16 v[168:171], v[128:131], v[112:115], 0\n" \
  "ds_read_b64_tr_b16 v[128:129], v184 offset:0\n" \
  "ds_read_b64_tr_b16 v[130:131], v184 offset:256\n" \
  "v_min_u32_sdwa v231, v228, v228 dst_sel:DWORD dst_unused:UNUSED_PAD src0_sel:BYTE_2 src1_sel:BYTE_3\n" \
  "v_sub_u32 v230, v230, v231\n" \
  "v_mfma_f32_16x16x32_bf16 v[172:175], v[144:147], v[112:115], 0\n" \
  "ds_read_b64_tr_b16 v[144:145], v185 offset:0\n" \
  "ds_read_b64_tr_b16 v[146:147], v185 offset:256\n" \
  "v_add_u32_dpp v230, v230, v230 quad_perm:[1,0,3,2] row_mask:0xf bank_mask:0xf\n" \
  "v_cmp_ge_i32 vcc, 0, v230\n" \
  "v_mfma_f32_16x16x32_bf16 v[168:171], v[132:135], v[116:119], v[168:171]\n" \
  "ds_read_b64_tr_b16 v[132:133], v184 offset:2048\n" \
  "ds_read_b64_tr_b16 v[134:135], v184 offset:2304\n" \
  "v_cndmask_b32_sdwa v228, v228, v228, vcc dst_sel:DWORD dst_unused:UNUSED_PAD src0_sel:WORD_1 src1_sel:WORD_0\n" \
  "s_mov_b64 s[58:59], vcc\n" \
  "v_cmp_le_u32_sdwa vcc, v228, v228 src0_sel:BYTE_0 src1_sel:BYTE_1\n" \
  "v_mfma_f32_16x16x32_bf16 v[172:175], v[148:151], v[116:119], v[172:175]\n" \
  "ds_read_b64_tr_b16 v[148:149], v185 offset:2048\n" \
  "ds_read_b64_tr_b16 v[150:151], v185 offset:2304\n" \
  "v_cvt_pk_bf16_f32 v176, v160, v161\n" \
  "v_cndmask_b32_dpp v233, v233, v233, vcc quad_perm:[2,3,0,1] row_mask:0xf bank_mask:0xf\n" \
  "s_mov_b64 vcc, s[58:59]\n" \
  "v_mfma_f32_16x16x32_bf16 v[168:171], v[136:139], v[120:123], v[168:171]\n" \
  "ds_read_b64_tr_b16 v[136:137], v184 offset:4096\n" \
  "ds_read_b64_tr_b16 v[138:139], v184 offset:4352\n" \
  "v_cvt_pk_bf16_f32 v177, v162, v163\n" \
  "v_cndmask_b32_dpp v233, v233, v233, vcc quad_perm:[3,0,1,2] row_mask:0xf bank_mask:0xf\n" \
  "v_mfma_f32_16x16x32_bf16 v[172:175], v[152:155], v[120:123], v[172:175]\n" \
  "ds_read_b64_tr_b16 v[152:153], v185 offset:4096\n" \
  "ds_read_b64_tr_b16 v[154:155], v185 offset:4352\n" \
  "v_cvt_pk_bf16_f32 v178, v164, v165\n" \
  "v_lshlrev_b32_e64 v230, v233, 3\n" \
  "v_mfma_f32_16x16x32_bf16 v[168:171], v[140:143], v[124:127], v[168:171]\n" \
  "ds_read_b64_tr_b16 v[140:141], v184 offset:6144\n" \
  "ds_read_b64_tr_b16 v[142:143], v184 offset:6400\n" \
  "v_cvt_pk_bf16_f32 v179, v166, v167\n" \
  "v_and_or_b32 v235, v229, v230, v235\n" \
  "v_mfma_f32_16x16x32_bf16 v[172:175], v[156:159], v[124:127], v[172:175]\n" \
  "ds_read_b64_tr_b16 v[156:157], v185 offset:6144\n" \
  "ds_read_b64_tr_b16 v[158:159], v185 offset:6400\n" \
  "s_waitcnt vmcnt(5)\n" \
  "global_store_dwordx4 v187, v[176:179], s[44:45] offset:0 sc1\n" \
  "s_set_gpr_idx_on s50, gpr_idx(DST)\n" \
  "v_perm_b32 v196, v233, v232, s57\n" \
  "s_set_gpr_idx_off\n" \
  "s_add_u32 s50, s50, 2\n" \
  "s_add_u32 s98, s98, 1\n" \
  "v_cvt_pk_bf16_f32 v180, v168, v169\n" \
  "v_cvt_pk_bf16_f32 v181, v170, v171\n" \
  "v_cvt_pk_bf16_f32 v182, v172, v173\n" \
  "v_cvt_pk_bf16_f32 v183, v174, v175\n" \
  "s_waitcnt vmcnt(5)\n" \
  "global_store_dwordx4 v188, v[180:183], s[44:45] offset:0 sc1\n" \
  "s_add_u32 s44, s44, 128000\n" \
  "s_addc_u32 s45, s45, 0\n" \
  "s_cmp_lt_u32 s98, 8\n" \
  "s_cbranch_scc1 LT1\n" \
  "s_mul_i32 s5, s28, 2176\n" \
  "s_add_u32 s5, s5, 154624\n" \
  "v_bfe_u32 v228, v255, 3, 1\n" \
  "v_and_b32 v229, 1, v255\n" \
  "v_lshl_add_u32 v230, v228, 1, v229\n" \
  "v_lshl_add_u32 v231, v229, 1, v228\n" \
  "v_xor_b32 v229, 1, v230\n" \
  "s_mov_b32 vcc_lo, 0xcccccccc\n" \
  "s_mov_b32 vcc_hi, vcc_lo\n" \
  "v_cndmask_b32 v228, v230, v231, vcc\n" \
  "v_cndmask_b32 v229, v229, v231, vcc\n" \
  "v_and_b32 v230, 4, v255\n" \
  "v_mul_u32_u24 v230, 272, v230\n" \
  "v_lshl_add_u32 v230, v253, 2, v230\n" \
  "v_and_or_b32 v230, v255, 2, v230\n" \
  "v_add_u32 v230, s5, v230\n" \
  "s_movk_i32 s35, 272\n" \
  "v_mad_u32_u24 v228, v228, s35, v230\n" \
  "v_mad_u32_u24 v229, v229, s35, v230\n" \
  "v_lshl_add_u32 v230, v253, 4, v238\n" \
  "v_add_u32 v230, s5, v230\n" \
  "ds_write_b16 v228, v196 offset:0\n" \
  "ds_write_b16 v228, v198 offset:16\n" \
  "ds_write_b16 v228, v200 offset:32\n" \
  "ds_write_b16 v228, v202 offset:48\n" \
  "ds_write_b16 v229, v204 offset:64\n" \
  "ds_write_b16 v229, v206 offset:80\n" \
  "ds_write_b16 v229, v208 offset:96\n" \
  "ds_write_b16 v229, v210 offset:112\n" \
  "ds_write_b16 v229, v212 offset:128\n" \
  "ds_write_b16 v229, v214 offset:144\n" \
  "ds_write_b16 v229, v216 offset:160\n" \
  "ds_write_b16 v229, v218 offset:176\n" \
  "ds_write_b16 v228, v220 offset:192\n" \
  "ds_write_b16 v228, v222 offset:208\n" \
  "ds_write_b16 v228, v224 offset:224\n" \
  "ds_write_b16 v228, v226 offset:240\n" \
  "ds_read_b128 v[244:247], v230 offset:0\n" \
  "ds_read_b128 v[248:251], v230 offset:1088\n" \
  "ds_write_b16_d16_hi v228, v196 offset:0\n" \
  "ds_write_b16_d16_hi v228, v198 offset:16\n" \
  "ds_write_b16_d16_hi v228, v200 offset:32\n" \
  "ds_write_b16_d16_hi v228, v202 offset:48\n" \
  "ds_write_b16_d16_hi v229, v204 offset:64\n" \
  "ds_write_b16_d16_hi v229, v206 offset:80\n" \
  "ds_write_b16_d16_hi v229, v208 offset:96\n" \
  "ds_write_b16_d16_hi v229, v210 offset:112\n" \
  "ds_write_b16_d16_hi v229, v212 offset:128\n" \
  "ds_write_b16_d16_hi v229, v214 offset:144\n" \
  "ds_write_b16_d16_hi v229, v216 offset:160\n" \
  "ds_write_b16_d16_hi v229, v218 offset:176\n" \
  "ds_write_b16_d16_hi v228, v220 offset:192\n" \
  "ds_write_b16_d16_hi v228, v222 offset:208\n" \
  "ds_write_b16_d16_hi v228, v224 offset:224\n" \
  "ds_write_b16_d16_hi v228, v226 offset:240\n" \
  "ds_read_b128 v[160:163], v230 offset:0\n" \
  "ds_read_b128 v[164:167], v230 offset:1088\n" \
  "s_lshr_b32 s35, s7, 5\n" \
  "s_mul_i32 s35, s35, 819200\n" \
  "s_and_b32 s36, s7, 31\n" \
  "s_lshl_b32 s36, s36, 10\n" \
  "s_add_u32 s35, s35, s36\n" \
  "s_add_u32 s35, s35, 102400000\n" \
  "s_lshr_b32 s36, s4, 5\n" \
  "s_waitcnt lgkmcnt(0)\n" \
  "s_add_u32 s37, s36, 0\n" \
  "s_cmp_ge_u32 s37, 24\n" \
  "s_cbranch_scc1 L9_\n" \
  "s_min_u32 s37, s37, 24\n" \
  "s_lshl_b32 s37, s37, 15\n" \
  "s_add_u32 s37, s37, s35\n" \
  "s_add_u32 s58, s24, s37\n" \
  "s_addc_u32 s59, s25, 0\n" \
  "global_store_dwordx4 v238, v[244:247], s[58:59] sc1\n" \
  "L9_:\n" \
  "s_add_u32 s37, s36, 1\n" \
  "s_cmp_ge_u32 s37, 26\n" \
  "s_cbranch_scc1 L10_\n" \
  "s_min_u32 s37, s37, 24\n" \
  "s_lshl_b32 s37, s37, 15\n" \
  "s_add_u32 s37, s37, s35\n" \
  "s_add_u32 s58, s24, s37\n" \
  "s_addc_u32 s59, s25, 0\n" \
  "global_store_dwordx4 v238, v[160:163], s[58:59] sc1\n" \
  "L10_:\n" \
  "s_add_u32 s37, s36, 2\n" \
  "s_cmp_ge_u32 s37, 24\n" \
  "s_cbranch_scc1 L11_\n" \
  "s_min_u32 s37, s37, 24\n" \
  "s_lshl_b32 s37, s37, 15\n" \
  "s_add_u32 s37, s37, s35\n" \
  "s_add_u32 s58, s24, s37\n" \
  "s_addc_u32 s59, s25, 0\n" \
  "global_store_dwordx4 v238, v[248:251], s[58:59] sc1\n" \
  "L11_:\n" \
  "s_add_u32 s37, s36, 3\n" \
  "s_cmp_ge_u32 s37, 24\n" \
  "s_cbranch_scc1 L12_\n" \
  "s_min_u32 s37, s37, 24\n" \
  "s_lshl_b32 s37, s37, 15\n" \
  "s_add_u32 s37, s37, s35\n" \
  "s_add_u32 s58, s24, s37\n" \
  "s_addc_u32 s59, s25, 0\n" \
  "global_store_dwordx4 v238, v[164:167], s[58:59] sc1\n" \
  "L12_:\n" \
  "global_load_dword v193, v193, s[48:49] offset:1024 sc1\n" \
  "s_lshl_b32 s35, s29, 8\n" \
  "v_lshl_add_u32 v196, v255, 11, s35\n" \
  "s_lshl_b32 s35, s30, 11\n" \
  "v_lshl_add_u32 v197, v255, 8, s35\n" \
  "LT2:\n" \
  "s_waitcnt lgkmcnt(14)\n" \
  "v_mfma_f32_16x16x32_bf16 v[160:163], v[128:131], v[0:3], 0\n" \
  "s_nop 2\n" \
  "s_waitcnt lgkmcnt(12)\n" \
  "v_mfma_f32_16x16x32_bf16 v[164:167], v[144:147], v[0:3], 0\n" \
  "s_nop 2\n" \
  "s_waitcnt lgkmcnt(10)\n" \
  "v_mfma_f32_16x16x32_bf16 v[160:163], v[132:135], v[4:7], v[160:163]\n" \
  "s_nop 2\n" \
  "s_waitcnt lgkmcnt(8)\n" \
  "v_mfma_f32_16x16x32_bf16 v[164:167], v[148:151], v[4:7], v[164:167]\n" \
  "s_nop 2\n" \
  "s_waitcnt lgkmcnt(6)\n" \
  "v_mfma_f32_16x16x32_bf16 v[160:163], v[136:139], v[8:11], v[160:163]\n" \
  "s_nop 2\n" \
  "s_waitcnt lgkmcnt(4)\n" \
  "v_mfma_f32_16x16x32_bf16 v[164:167], v[152:155], v[8:11], v[164:167]\n" \
  "s_nop 2\n" \
  "s_waitcnt lgkmcnt(2)\n" \
  "v_mfma_f32_16x16x32_bf16 v[160:163], v[140:143], v[12:15], v[160:163]\n" \
  "s_nop 2\n" \
  "s_waitcnt lgkmcnt(0)\n" \
  "v_mfma_f32_16x16x32_bf16 v[164:167], v[156:159], v[12:15], v[164:167]\n" \
  "s_nop 3\n" \
  "v_mfma_f32_16x16x32_bf16 v[168:171], v[128:131], v[16:19], 0\n" \
  "s_nop 3\n" \
  "v_mfma_f32_16x16x32_bf16 v[172:175], v[144:147], v[16:19], 0\n" \
  "s_nop 3\n" \
  "v_mfma_f32_16x16x32_bf16 v[168:171], v[132:135], v[20:23], v[168:171]\n" \
  "s_nop 3\n" \
  "v_mfma_f32_16x16x32_bf16 v[172:175], v[148:151], v[20:23], v[172:175]\n" \
  "v_cvt_pk_bf16_f32 v176, v160, v161\n" \
  "s_nop 2\n" \
  "v_mfma_f32_16x16x32_bf16 v[168:171], v[136:139], v[24:27], v[168:171]\n" \
  "v_cvt_pk_bf16_f32 v177, v162, v163\n" \
  "s_nop 2\n" \
  "v_mfma_f32_16x16x32_bf16 v[172:175], v[152:155], v[24:27], v[172:175]\n" \
  "v_cvt_pk_bf16_f32 v178, v164, v165\n" \
  "s_nop 2\n" \
  "v_mfma_f32_16x16x32_bf16 v[168:171], v[140:143], v[28:31], v[168:171]\n" \
  "v_cvt_pk_bf16_f32 v179, v166, v167\n" \
  "s_nop 2\n" \
  "v_mfma_f32_16x16x32_bf16 v[172:175], v[156:159], v[28:31], v[172:175]\n" \
  "s_waitcnt vmcnt(5)\n" \
  "global_store_dwordx4 v186, v[176:179], s[44:45] offset:0 sc1\n" \
  "s_nop 1\n" \
  "v_mfma_f32_16x16x32_bf16 v[160:163], v[128:131], v[32:35], 0\n" \
  "s_nop 3\n" \
  "v_mfma_f32_16x16x32_bf16 v[164:167], v[144:147], v[32:35], 0\n" \
  "s_nop 3\n" \
  "v_mfma_f32_16x16x32_bf16 v[160:163], v[132:135], v[36:39], v[160:163]\n" \
  "s_nop 3\n" \
  "v_mfma_f32_16x16x32_bf16 v[164:167], v[148:151], v[36:39], v[164:167]\n" \
  "v_cvt_pk_bf16_f32 v180, v168, v169\n" \
  "s_nop 2\n" \
  "v_mfma_f32_16x16x32_bf16 v[160:163], v[136:139], v[40:43], v[160:163]\n" \
  "v_cvt_pk_bf16_f32 v181, v170, v171\n" \
  "s_nop 2\n" \
  "v_mfma_f32_16x16x32_bf16 v[164:167], v[152:155], v[40:43], v[164:167]\n" \
  "v_cvt_pk_bf16_f32 v182, v172, v173\n" \
  "s_nop 2\n" \
  "v_mfma_f32_16x16x32_bf16 v[160:163], v[140:143], v[44:47], v[160:163]\n" \
  "v_cvt_pk_bf16_f32 v183, v174, v175\n" \
  "s_nop 2\n" \
  "v_mfma_f32_16x16x32_bf16 v[164:167], v[156:159], v[44:47], v[164:167]\n" \
  "s_waitcnt vmcnt(5)\n" \
  "global_store_dwordx4 v186, v[180:183], s[44:45] offset:256 sc1\n" \
  "s_nop 1\n" \
  "v_mfma_f32_16x16x32_bf16 v[168:171], v[128:131], v[48:51], 0\n" \
  "s_nop 3\n" \
  "v_mfma_f32_16x16x32_bf16 v[172:175], v[144:147], v[48:51], 0\n" \
  "s_nop 3\n" \
  "v_mfma_f32_16x16x32_bf16 v[168:171], v[132:135], v[52:55], v[168:171]\n" \
  "s_nop 3\n" \
  "v_mfma_f32_16x16x32_bf16 v[172:175], v[148:151], v[52:55], v[172:175]\n" \
  "v_cvt_pk_bf16_f32 v176, v160, v161\n" \
  "s_nop 2\n" \
  "v_mfma_f32_16x16x32_bf16 v[168:171], v[136:139], v[56:59], v[168:171]\n" \
  "v_cvt_pk_bf16_f32 v177, v162, v163\n" \
  "s_nop 2\n" \
  "v_mfma_f32_16x16x32_bf16 v[172:175], v[152:155], v[56:59], v[172:175]\n" \
  "v_cvt_pk_bf16_f32 v178, v164, v165\n" \
  "s_nop 2\n" \
  "v_mfma_f32_16x16x32_bf16 v[168:171], v[140:143], v[60:63], v[168:171]\n" \
  "v_cvt_pk_bf16_f32 v179, v166, v167\n" \
  "s_nop 2\n" \
  "v_mfma_f32_16x16x32_bf16 v[172:175], v[156:159], v[60:63], v[172:175]\n" \
  "s_waitcnt vmcnt(5)\n" \
  "global_store_dwordx4 v186, v[176:179], s[44:45] offset:512 sc1\n" \
  "s_nop 1\n" \
  "v_mfma_f32_16x16x32_bf16 v[160:163], v[128:131], v[64:67], 0\n" \
  "s_nop 3\n" \
  "v_mfma_f32_16x16x32_bf16 v[164:167], v[144:147], v[64:67], 0\n" \
  "s_nop 3\n" \
  "v_mfma_f32_16x16x32_bf16 v[160:163], v[132:135], v[68:71], v[160:163]\n" \
  "s_nop 3\n" \
  "v_mfma_f32_16x16x32_bf16 v[164:167], v[148:151], v[68:71], v[164:167]\n" \
  "v_cvt_pk_bf16_f32 v180, v168, v169\n" \
  "s_nop 2\n" \
  "v_mfma_f32_16x16x32_bf16 v[160:163], v[136:139], v[72:75], v[160:163]\n" \
  "v_cvt_pk_bf16_f32 v181, v170, v171\n" \
  "s_nop 2\n" \
  "v_mfma_f32_16x16x32_bf16 v[164:167], v[152:155], v[72:75], v[164:167]\n" \
  "v_cvt_pk_bf16_f32 v182, v172, v173\n" \
  "s_nop 2\n" \
  "v_mfma_f32_16x16x32_bf16 v[160:163], v[140:143], v[76:79], v[160:163]\n" \
  "v_cvt_pk_bf16_f32 v183, v174, v175\n" \
  "s_nop 2\n" \
  "v_mfma_f32_16x16x32_bf16 v[164:167], v[156:159], v[76:79], v[164:167]\n" \
  "s_waitcnt vmcnt(5)\n" \
  "global_store_dwordx4 v186, v[180:183], s[44:45] offset:768 sc1\n" \
  "s_nop 1\n" \
  "v_mfma_f32_16x16x32_bf16 v[168:171], v[128:131], v[80:83], 0\n" \
  "s_mov_b64 exec, 1\n" \
  "ds_add_u32 v190, v191 offset:0\n" \
  "s_mov_b64 exec, -1\n" \
  "s_nop 0\n" \
  "v_mfma_f32_16x16x32_bf16 v[172:175], v[144:147], v[80:83], 0\n" \
  "s_nop 3\n" \
  "v_mfma_f32_16x16x32_bf16 v[168:171], v[132:135], v[84:87], v[168:171]\n" \
  "s_nop 3\n" \
  "v_mfma_f32_16x16x32_bf16 v[172:175], v[148:151], v[84:87], v[172:175]\n" \
  "v_cvt_pk_bf16_f32 v176, v160, v161\n" \
  "s_nop 2\n" \
  "v_mfma_f32_16x16x32_bf16 v[168:171], v[136:139], v[88:91], v[168:171]\n" \
  "v_cvt_pk_bf16_f32 v177, v162, v163\n" \
  "s_nop 2\n" \
  "v_mfma_f32_16x16x32_bf16 v[172:175], v[152:155], v[88:91], v[172:175]\n" \
  "v_cvt_pk_bf16_f32 v178, v164, v165\n" \
  "s_nop 2\n" \
  "v_mfma_f32_16x16x32_bf16 v[168:171], v[140:143], v[92:95], v[168:171]\n" \
  "v_cvt_pk_bf16_f32 v179, v166, v167\n" \
  "s_nop 2\n" \
  "v_mfma_f32_16x16x32_bf16 v[172:175], v[156:159], v[92:95], v[172:175]\n" \
  "s_waitcnt vmcnt(5)\n" \
  "global_store_dwordx4 v186, v[176:179], s[44:45] offset:1024 sc1\n" \
  "s_nop 1\n" \
  "v_mfma_f32_16x16x32_bf16 v[160:163], v[128:131], v[96:99], 0\n" \
  "ds_read_b32 v192, v190\n" \
  "v_add_u32 v184, s97, v184\n" \
  "s_nop 1\n" \
  "v_mfma_f32_16x16x32_bf16 v[164:167], v[144:147], v[96:99], 0\n" \
  "v_add_u32 v185, s97, v185\n" \
  "s_nop 2\n" \
  "v_mfma_f32_16x16x32_bf16 v[160:163], v[132:135], v[100:103], v[160:163]\n" \
  "s_cmp_lt_u32 s98, 10\n" \
  "s_cselect_b32 s6, s6, s34\n" \
  "s_add_u32 s40, s92, s6\n" \
  "s_addc_u32 s41, s93, 0\n" \
  "s_add_u32 s6, s6, 1048576\n" \
  "v_mfma_f32_16x16x32_bf16 v[164:167], v[148:151], v[100:103], v[164:167]\n" \
  "v_cvt_pk_bf16_f32 v180, v168, v169\n" \
  "s_nop 2\n" \
  "v_mfma_f32_16x16x32_bf16 v[160:163], v[136:139], v[104:107], v[160:163]\n" \
  "v_cvt_pk_bf16_f32 v181, v170, v171\n" \
  "s_nop 2\n" \
  "v_mfma_f32_16x16x32_bf16 v[164:167], v[152:155], v[104:107], v[164:167]\n" \
  "s_waitcnt lgkmcnt(0)\n" \
  "v_readfirstlane_b32 s35, v192\n" \
  "v_cvt_pk_bf16_f32 v182, v172, v173\n" \
  "s_nop 0\n" \
  "v_mfma_f32_16x16x32_bf16 v[160:163], v[140:143], v[108:111], v[160:163]\n" \
  "s_cmp_lt_u32 s35, s99\n" \
  "s_cbranch_scc1 LT2_w\n" \
  "LT2_c:\n" \
  "s_mov_b32 m0, s96\n" \
  "s_nop 0\n" \
  "global_load_lds_dwordx4 v189, s[40:41]\n" \
  "global_load_lds_dwordx4 v194, s[40:41] offset:1024\n" \
  "s_add_u32 s96, s96, s97\n" \
  "s_sub_u32 s97, 0, s97\n" \
  "s_add_u32 s99, s99, 4\n" \
  "v_cvt_pk_bf16_f32 v183, v174, v175\n" \
  "v_mfma_f32_16x16x32_bf16 v[164:167], v[156:159], v[108:111], v[164:167]\n" \
  "s_lshl4_add_u32 s35, s28, s98\n" \
  "s_cmp_eq_u32 s35, 9\n" \
  "s_cbranch_scc1 LT2_a\n" \
  "LT2_r:\n" \
  "s_waitcnt vmcnt(5)\n" \
  "global_store_dwordx4 v186, v[180:183], s[44:45] offset:1280 sc1\n" \
  "v_mfma_f32_16x16x32_bf16 v[168:171], v[128:131], v[112:115], 0\n" \
  "ds_read_b64_tr_b16 v[128:129], v184 offset:0\n" \
  "ds_read_b64_tr_b16 v[130:131], v184 offset:256\n" \
  "s_nop 1\n" \
  "v_mfma_f32_16x16x32_bf16 v[172:175], v[144:147], v[112:115], 0\n" \
  "ds_read_b64_tr_b16 v[144:145], v185 offset:0\n" \
  "ds_read_b64_tr_b16 v[146:147], v185 offset:256\n" \
  "s_nop 1\n" \
  "v_mfma_f32_16x16x32_bf16 v[168:171], v[132:135], v[116:119], v[168:171]\n" \
  "ds_read_b64_tr_b16 v[132:133], v184 offset:2048\n" \
  "ds_read_b64_tr_b16 v[134:135], v184 offset:2304\n" \
  "s_nop 1\n" \
  "v_mfma_f32_16x16x32_bf16 v[172:175], v[148:151], v[116:119], v[172:175]\n" \
  "ds_read_b64_tr_b16 v[148:149], v185 offset:2048\n" \
  "ds_read_b64_tr_b16 v[150:151], v185 offset:2304\n" \
  "v_cvt_pk_bf16_f32 v176, v160, v161\n" \
  "s_nop 0\n" \
  "v_mfma_f32_16x16x32_bf16 v[168:171], v[136:139], v[120:123], v[168:171]\n" \
  "ds_read_b64_tr_b16 v[136:137], v184 offset:4096\n" \
  "ds_read_b64_tr_b16 v[138:139], v184 offset:4352\n" \
  "v_cvt_pk_bf16_f32 v177, v162, v163\n" \
  "s_nop 0\n" \
  "v_mfma_f32_16x16x32_bf16 v[172:175], v[152:155], v[120:123], v[172:175]\n" \
  "ds_read_b64_tr_b16 v[152:153], v185 offset:4096\n" \
  "ds_read_b64_tr_b16 v[154:155], v185 offset:4352\n" \
  "v_cvt_pk_bf16_f32 v178, v164, v165\n" \
  "s_nop 0\n" \
  "v_mfma_f32_16x16x32_bf16 v[168:171], v[140:143], v[124:127], v[168:171]\n" \
  "ds_read_b64_tr_b16 v[140:141], v184 offset:6144\n" \
  "ds_read_b64_tr_b16 v[142:143], v184 offset:6400\n" \
  "v_cvt_pk_bf16_f32 v179, v166, v167\n" \
  "s_nop 0\n" \
  "v_mfma_f32_16x16x32_bf16 v[172:175], v[156:159], v[124:127], v[172:175]\n" \
  "ds_read_b64_tr_b16 v[156:157], v185 offset:6144\n" \
  "ds_read_b64_tr_b16 v[158:159], v185 offset:6400\n" \
  "s_waitcnt vmcnt(5)\n" \
  "global_store_dwordx4 v187, v[176:179], s[44:45] offset:0 sc1\n" \
  "s_add_u32 s98, s98, 1\n" \
  "v_cvt_pk_bf16_f32 v180, v168, v169\n" \
  "v_cvt_pk_bf16_f32 v181, v170, v171\n" \
  "s_nop 0\n" \
  "v_cvt_pk_bf16_f32 v182, v172, v173\n" \
  "v_cvt_pk_bf16_f32 v183, v174, v175\n" \
  "s_waitcnt vmcnt(5)\n" \
  "global_store_dwordx4 v188, v[180:183], s[44:45] offset:0 sc1\n" \
  "s_add_u32 s44, s44, 128000\n" \
  "s_addc_u32 s45, s45, 0\n" \
  "s_cmp_lt_u32 s98, 12\n" \
  "s_cbranch_scc1 LT2\n" \
  "s_mov_b64 exec, 0xff\n" \
  "global_load_dword v198, v197, s[48:49] offset:2048 sc1\n" \
  "s_mov_b64 exec, -1\n" \
  "s_bitcmp1_b32 s30, 2\n" \
  "s_cbranch_scc1 LB_hi\n" \
  "s_waitcnt lgkmcnt(14)\n" \
  "v_mfma_f32_16x16x32_bf16 v[160:163], v[128:131], v[0:3], 0\n" \
  "s_nop 2\n" \
  "s_waitcnt lgkmcnt(12)\n" \
  "v_mfma_f32_16x16x32_bf16 v[164:167], v[144:147], v[0:3], 0\n" \
  "s_nop 2\n" \
  "s_waitcnt lgkmcnt(10)\n" \
  "v_mfma_f32_16x16x32_bf16 v[160:163], v[132:135], v[4:7], v[160:163]\n" \
  "s_nop 2\n" \
  "s_waitcnt lgkmcnt(8)\n" \
  "v_mfma_f32_16x16x32_bf16 v[164:167], v[148:151], v[4:7], v[164:167]\n" \
  "s_nop 2\n" \
  "s_waitcnt lgkmcnt(6)\n" \
  "v_mfma_f32_16x16x32_bf16 v[160:163], v[136:139], v[8:11], v[160:163]\n" \
  "s_nop 2\n" \
  "s_waitcnt lgkmcnt(4)\n" \
  "v_mfma_f32_16x16x32_bf16 v[164:167], v[152:155], v[8:11], v[164:167]\n" \
  "s_nop 2\n" \
  "s_waitcnt lgkmcnt(2)\n" \
  "v_mfma_f32_16x16x32_bf16 v[160:163], v[140:143], v[12:15], v[160:163]\n" \
  "s_nop 2\n" \
  "s_waitcnt lgkmcnt(0)\n" \
  "v_mfma_f32_16x16x32_bf16 v[164:167], v[156:159], v[12:15], v[164:167]\n" \
  "s_nop 3\n" \
  "v_mfma_f32_16x16x32_bf16 v[168:171], v[128:131], v[16:19], 0\n" \
  "s_nop 3\n" \
  "v_mfma_f32_16x16x32_bf16 v[172:175], v[144:147], v[16:19], 0\n" \
  "s_nop 3\n" \
  "v_mfma_f32_16x16x32_bf16 v[168:171], v[132:135], v[20:23], v[168:171]\n" \
  "s_nop 3\n" \
  "v_mfma_f32_16x16x32_bf16 v[172:175], v[148:151], v[20:23], v[172:175]\n" \
  "v_cvt_pk_bf16_f32 v176, v160, v161\n" \
  "s_nop 2\n" \
  "v_mfma_f32_16x16x32_bf16 v[168:171], v[136:139], v[24:27], v[168:171]\n" \
  "v_cvt_pk_bf16_f32 v177, v162, v163\n" \
  "s_nop 2\n" \
  "v_mfma_f32_16x16x32_bf16 v[172:175], v[152:155], v[24:27], v[172:175]\n" \
  "v_cvt_pk_bf16_f32 v178, v164, v165\n" \
  "s_nop 2\n" \
  "v_mfma_f32_16x16x32_bf16 v[168:171], v[140:143], v[28:31], v[168:171]\n" \
  "v_cvt_pk_bf16_f32 v179, v166, v167\n" \
  "s_nop 2\n" \
  "v_mfma_f32_16x16x32_bf16 v[172:175], v[156:159], v[28:31], v[172:175]\n" \
  "s_waitcnt vmcnt(5)\n" \
  "global_store_dwordx4 v186, v[176:179], s[46:47] offset:0 sc1\n" \
  "s_nop 1\n" \
  "v_mfma_f32_16x16x32_bf16 v[160:163], v[128:131], v[32:35], 0\n" \
  "s_nop 3\n" \
  "v_mfma_f32_16x16x32_bf16 v[164:167], v[144:147], v[32:35], 0\n" \
  "s_nop 3\n" \
  "v_mfma_f32_16x16x32_bf16 v[160:163], v[132:135], v[36:39], v[160:163]\n" \
  "s_nop 3\n" \
  "v_mfma_f32_16x16x32_bf16 v[164:167], v[148:151], v[36:39], v[164:167]\n" \
  "v_cvt_pk_bf16_f32 v180, v168, v169\n" \
  "s_nop 2\n" \
  "v_mfma_f32_16x16x32_bf16 v[160:163], v[136:139], v[40:43], v[160:163]\n" \
  "v_cvt_pk_bf16_f32 v181, v170, v171\n" \
  "s_nop 2\n" \
  "v_mfma_f32_16x16x32_bf16 v[164:167], v[152:155], v[40:43], v[164:167]\n" \
  "v_cvt_pk_bf16_f32 v182, v172, v173\n" \
  "s_nop 2\n" \
  "v_mfma_f32_16x16x32_bf16 v[160:163], v[140:143], v[44:47], v[160:163]\n" \
  "v_cvt_pk_bf16_f32 v183, v174, v175\n" \
  "s_nop 2\n" \
  "v_mfma_f32_16x16x32_bf16 v[164:167], v[156:159], v[44:47], v[164:167]\n" \
  "s_waitcnt vmcnt(5)\n" \
  "global_store_dwordx4 v186, v[180:183], s[46:47] offset:256 sc1\n" \
  "s_nop 1\n" \
  "v_mfma_f32_16x16x32_bf16 v[168:171], v[128:131], v[48:51], 0\n" \
  "s_nop 3\n" \
  "v_mfma_f32_16x16x32_bf16 v[172:175], v[144:147], v[48:51], 0\n" \
  "s_nop 3\n" \
  "v_mfma_f32_16x16x32_bf16 v[168:171], v[132:135], v[52:55], v[168:171]\n" \
  "s_nop 3\n" \
  "v_mfma_f32_16x16x32_bf16 v[172:175], v[148:151], v[52:55], v[172:175]\n" \
  "v_cvt_pk_bf16_f32 v176, v160, v161\n" \
  "s_nop 2\n" \
  "v_mfma_f32_16x16x32_bf16 v[168:171], v[136:139], v[56:59], v[168:171]\n" \
  "v_cvt_pk_bf16_f32 v177, v162, v163\n" \
  "s_nop 2\n" \
  "v_mfma_f32_16x16x32_bf16 v[172:175], v[152:155], v[56:59], v[172:175]\n" \
  "v_cvt_pk_bf16_f32 v178, v164, v165\n" \
  "s_nop 2\n" \
  "v_mfma_f32_16x16x32_bf16 v[168:171], v[140:143], v[60:63], v[168:171]\n" \
  "v_cvt_pk_bf16_f32 v179, v166, v167\n" \
  "s_nop 2\n" \
  "v_mfma_f32_16x16x32_bf16 v[172:175], v[156:159], v[60:63], v[172:175]\n" \
  "s_waitcnt vmcnt(5)\n" \
  "global_store_dwordx4 v186, v[176:179], s[46:47] offset:512 sc1\n" \
  "s_nop 0\n" \
  "v_cvt_pk_bf16_f32 v180, v168, v169\n" \
  "v_cvt_pk_bf16_f32 v181, v170, v171\n" \
  "s_nop 2\n" \
  "v_cvt_pk_bf16_f32 v182, v172, v173\n" \
  "v_cvt_pk_bf16_f32 v183, v174, v175\n" \
  "s_waitcnt vmcnt(5)\n" \
  "global_store_dwordx4 v186, v[180:183], s[46:47] offset:768 sc1\n" \
  "s_branch LB_end\n" \
  "LT1_w:\n" \
  "L7_:\n" \
  "s_sleep 1\n" \
  "ds_read_b32 v192, v190 offset:0\n" \
  "s_waitcnt lgkmcnt(0)\n" \
  "v_readfirstlane_b32 s35, v192\n" \
  "s_cmp_lt_u32 s35, s99\n" \
  "s_cbranch_scc1 L7_\n" \
  "s_branch LT1_c\n" \
  "LT1_e:\n" \
  "s_bitcmp1_b32 72, s98\n" \
  "s_cbranch_scc0 LT1_r\n" \
  "global_load_dwordx4 v[240:243], v238, s[48:49] offset:0 sc1\n" \
  "s_waitcnt vmcnt(0)\n" \
  "global_store_dwordx4 v238, v[240:243], s[48:49] offset:1024 sc1\n" \
  "s_branch LT1_r\n" \
  "LT2_w:\n" \
  "L8_:\n" \
  "s_sleep 1\n" \
  "ds_read_b32 v192, v190 offset:0\n" \
  "s_waitcnt lgkmcnt(0)\n" \
  "v_readfirstlane_b32 s35, v192\n" \
  "s_cmp_lt_u32 s35, s99\n" \
  "s_cbranch_scc1 L8_\n" \
  "s_branch LT2_c\n" \
  "LT2_a:\n" \
  "s_mov_b32 s50, 0x10000\n" \
  "LB_ec:\n" \
  "v_readfirstlane_b32 s35, v193\n" \
  "s_cmp_eq_u32 s35, s39\n" \
  "s_cbranch_scc1 LB_ok\n" \
  "s_lshl_b32 s35, s26, 2\n" \
  "v_mov_b32 v193, s35\n" \
  "s_sleep 8\n" \
  "global_load_dword v193, v193, s[48:49] offset:1024 sc1\n" \
  "s_waitcnt vmcnt(0)\n" \
  "s_sub_u32 s50, s50, 1\n" \
  "s_cbranch_scc0 LB_ec\n" \
  "LB_ok:\n" \
  "s_mov_b32 exec_hi, 0\n" \
  "global_atomic_add v196, v191, s[48:49] offset:2048 sc1\n" \
  "s_mov_b64 exec, -1\n" \
  "s_branch LT2_r\n" \
  "LB_hi:\n" \
  "s_waitcnt lgkmcnt(14)\n" \
  "v_mfma_f32_16x16x32_bf16 v[160:163], v[128:131], v[64:67], 0\n" \
  "s_nop 2\n" \
  "s_waitcnt lgkmcnt(12)\n" \
  "v_mfma_f32_16x16x32_bf16 v[164:167], v[144:147], v[64:67], 0\n" \
  "s_nop 2\n" \
  "s_waitcnt lgkmcnt(10)\n" \
  "v_mfma_f32_16x16x32_bf16 v[160:163], v[132:135], v[68:71], v[160:163]\n" \
  "s_nop 2\n" \
  "s_waitcnt lgkmcnt(8)\n" \
  "v_mfma_f32_16x16x32_bf16 v[164:167], v[148:151], v[68:71], v[164:167]\n" \
  "s_nop 2\n" \
  "s_waitcnt lgkmcnt(6)\n" \
  "v_mfma_f32_16x16x32_bf16 v[160:163], v[136:139], v[72:75], v[160:163]\n" \
  "s_nop 2\n" \
  "s_waitcnt lgkmcnt(4)\n" \
  "v_mfma_f32_16x16x32_bf16 v[164:167], v[152:155], v[72:75], v[164:167]\n" \
  "s_nop 2\n" \
  "s_waitcnt lgkmcnt(2)\n" \
  "v_mfma_f32_16x16x32_bf16 v[160:163], v[140:143], v[76:79], v[160:163]\n" \
  "s_nop 2\n" \
  "s_waitcnt lgkmcnt(0)\n" \
  "v_mfma_f32_16x16x32_bf16 v[164:167], v[156:159], v[76:79], v[164:167]\n" \
  "s_nop 3\n" \
  "v_mfma_f32_16x16x32_bf16 v[168:171], v[128:131], v[80:83], 0\n" \
  "s_nop 3\n" \
  "v_mfma_f32_16x16x32_bf16 v[172:175], v[144:147], v[80:83], 0\n" \
  "s_nop 3\n" \
  "v_mfma_f32_16x16x32_bf16 v[168:171], v[132:135], v[84:87], v[168:171]\n" \
  "s_nop 3\n" \
  "v_mfma_f32_16x16x32_bf16 v[172:175], v[148:151], v[84:87], v[172:175]\n" \
  "v_cvt_pk_bf16_f32 v176, v160, v161\n" \
  "s_nop 2\n" \
  "v_mfma_f32_16x16x32_bf16 v[168:171], v[136:139], v[88:91], v[168:171]\n" \
  "v_cvt_pk_bf16_f32 v177, v162, v163\n" \
  "s_nop 2\n" \
  "v_mfma_f32_16x16x32_bf16 v[172:175], v[152:155], v[88:91], v[172:175]\n" \
  "v_cvt_pk_bf16_f32 v178, v164, v165\n" \
  "s_nop 2\n" \
  "v_mfma_f32_16x16x32_bf16 v[168:171], v[140:143], v[92:95], v[168:171]\n" \
  "v_cvt_pk_bf16_f32 v179, v166, v167\n" \
  "s_nop 2\n" \
  "v_mfma_f32_16x16x32_bf16 v[172:175], v[156:159], v[92:95], v[172:175]\n" \
  "s_waitcnt vmcnt(5)\n" \
  "global_store_dwordx4 v186, v[176:179], s[46:47] offset:1024 sc1\n" \
  "s_nop 1\n" \
  "v_mfma_f32_16x16x32_bf16 v[160:163], v[128:131], v[96:99], 0\n" \
  "s_nop 3\n" \
  "v_mfma_f32_16x16x32_bf16 v[164:167], v[144:147], v[96:99], 0\n" \
  "s_nop 3\n" \
  "v_mfma_f32_16x16x32_bf16 v[160:163], v[132:135], v[100:103], v[160:163]\n" \
  "s_nop 3\n" \
  "v_mfma_f32_16x16x32_bf16 v[164:167], v[148:151], v[100:103], v[164:167]\n" \
  "v_cvt_pk_bf16_f32 v180, v168, v169\n" \
  "s_nop 2\n" \
  "v_mfma_f32_16x16x32_bf16 v[160:163], v[136:139], v[104:107], v[160:163]\n" \
  "v_cvt_pk_bf16_f32 v181, v170, v171\n" \
  "s_nop 2\n" \
  "v_mfma_f32_16x16x32_bf16 v[164:167], v[152:155], v[104:107], v[164:167]\n" \
  "v_cvt_pk_bf16_f32 v182, v172, v173\n" \
  "s_nop 2\n" \
  "v_mfma_f32_16x16x32_bf16 v[160:163], v[140:143], v[108:111], v[160:163]\n" \
  "v_cvt_pk_bf16_f32 v183, v174, v175\n" \
  "s_nop 2\n" \
  "v_mfma_f32_16x16x32_bf16 v[164:167], v[156:159], v[108:111], v[164:167]\n" \
  "s_waitcnt vmcnt(5)\n" \
  "global_store_dwordx4 v186, v[180:183], s[46:47] offset:1280 sc1\n" \
  "s_nop 1\n" \
  "v_mfma_f32_16x16x32_bf16 v[168:171], v[128:131], v[112:115], 0\n" \
  "s_nop 3\n" \
  "v_mfma_f32_16x16x32_bf16 v[172:175], v[144:147], v[112:115], 0\n" \
  "s_nop 3\n" \
  "v_mfma_f32_16x16x32_bf16 v[168:171], v[132:135], v[116:119], v[168:171]\n" \
  "s_nop 3\n" \
  "v_mfma_f32_16x16x32_bf16 v[172:175], v[148:151], v[116:119], v[172:175]\n" \
  "v_cvt_pk_bf16_f32 v176, v160, v161\n" \
  "s_nop 2\n" \
  "v_mfma_f32_16x16x32_bf16 v[168:171], v[136:139], v[120:123], v[168:171]\n" \
  "v_cvt_pk_bf16_f32 v177, v162, v163\n" \
  "s_nop 2\n" \
  "v_mfma_f32_16x16x32_bf16 v[172:175], v[152:155], v[120:123], v[172:175]\n" \
  "v_cvt_pk_bf16_f32 v178, v164, v165\n" \
  "s_nop 2\n" \
  "v_mfma_f32_16x16x32_bf16 v[168:171], v[140:143], v[124:127], v[168:171]\n" \
  "v_cvt_pk_bf16_f32 v179, v166, v167\n" \
  "s_nop 2\n" \
  "v_mfma_f32_16x16x32_bf16 v[172:175], v[156:159], v[124:127], v[172:175]\n" \
  "s_waitcnt vmcnt(5)\n" \
  "global_store_dwordx4 v187, v[176:179], s[46:47] offset:0 sc1\n" \
  "s_nop 0\n" \
  "v_cvt_pk_bf16_f32 v180, v168, v169\n" \
  "v_cvt_pk_bf16_f32 v181, v170, v171\n" \
  "s_nop 2\n" \
  "v_cvt_pk_bf16_f32 v182, v172, v173\n" \
  "v_cvt_pk_bf16_f32 v183, v174, v175\n" \
  "s_waitcnt vmcnt(5)\n" \
  "global_store_dwordx4 v188, v[180:183], s[46:47] offset:0 sc1\n" \
  "LB_end:\n" \
  "s_setprio 2\n" \
  "s_waitcnt vmcnt(4)\n" \
  "s_mov_b32 s50, 0x10000\n" \
  "LB_p:\n" \
  "s_mov_b64 exec, 0xff\n" \
  "v_cmp_le_u32 vcc, 32, v198\n" \
  "s_mov_b64 exec, -1\n" \
  "s_sub_u32 s50, s50, 1\n" \
  "s_cselect_b32 s35, 0xff, vcc_lo\n" \
  "s_cmp_eq_u32 s35, 0xff\n" \
  "s_cbranch_scc1 LB_p_x\n" \
  "s_cmp_lg_u32 s33, 1021\n" \
  "s_cbranch_scc1 LB_ne\n" \
  "global_load_dwordx4 v[240:243], v238, s[48:49] offset:0 sc1\n" \
  "s_waitcnt vmcnt(0)\n" \
  "global_store_dwordx4 v238, v[240:243], s[48:49] offset:1024 sc1\n" \
  "LB_ne:\n" \
  "s_sleep 2\n" \
  "s_mov_b64 exec, 0xff\n" \
  "global_load_dword v198, v197, s[48:49] offset:2048 sc1\n" \
  "s_mov_b64 exec, -1\n" \
  "s_waitcnt vmcnt(0)\n" \
  "s_branch LB_p\n" \
  "LB_p_x:\n" \
  "s_lshr_b32 s35, s30, 2\n" \
  "s_lshl3_add_u32 s33, s29, s35\n" \
  "s_and_b32 s34, s30, 3\n" \
  "s_lshl_b32 s59, s27, 11\n" \
  "s_mul_i32 s35, s33, 1600000\n" \
  "s_add_u32 s54, s24, s35\n" \
  "s_addc_u32 s55, s25, 0\n" \
  "s_mul_i32 s35, s34, 819200\n" \
  "s_lshl1_add_u32 s35, s59, s35\n" \
  "s_add_u32 s35, s35, 102400000\n" \
  "s_add_u32 s56, s24, s35\n" \
  "s_addc_u32 s57, s25, 0\n" \
  "v_mbcnt_lo_u32_b32 v189, -1, 0\n" \
  "v_mbcnt_hi_u32_b32 v189, -1, v189\n" \
  "v_lshlrev_b32 v188, 4, v189\n" \
  "s_lshl_b32 s35, s28, 12\n" \
  "s_add_u32 s40, s54, s35\n" \
  "s_addc_u32 s41, s55, 0\n" \
  "v_add_u32 v1, s35, v188\n" \
  "v_mov_b32 v2, 1\n" \
  "global_load_dwordx4 v[116:119], v188, s[40:41] offset:0 sc1\n" \
  "global_load_dwordx4 v[120:123], v188, s[40:41] offset:1024 sc1\n" \
  "global_load_dwordx4 v[124:127], v188, s[40:41] offset:2048 sc1\n" \
  "global_load_dwordx4 v[128:131], v188, s[40:41] offset:3072 sc1\n" \
  "s_add_u32 s40, s40, 16000\n" \
  "s_addc_u32 s41, s41, 0\n" \
  "global_load_dwordx4 v[132:135], v188, s[40:41] offset:0 sc1\n" \
  "global_load_dwordx4 v[136:139], v188, s[40:41] offset:1024 sc1\n" \
  "global_load_dwordx4 v[140:143], v188, s[40:41] offset:2048 sc1\n" \
  "global_load_dwordx4 v[144:147], v188, s[40:41] offset:3072 sc1\n" \
  "s_add_u32 s40, s40, 16000\n" \
  "s_addc_u32 s41, s41, 0\n" \
  "global_load_dwordx4 v[148:151], v188, s[40:41] offset:0 sc1\n" \
  "global_load_dwordx4 v[152:155], v188, s[40:41] offset:1024 sc1\n" \
  "global_load_dwordx4 v[156:159], v188, s[40:41] offset:2048 sc1\n" \
  "global_load_dwordx4 v[160:163], v188, s[40:41] offset:3072 sc1\n" \
  "s_add_u32 s40, s40, 16000\n" \
  "s_addc_u32 s41, s41, 0\n" \
  "global_load_dwordx4 v[164:167], v188, s[40:41] offset:0 sc1\n" \
  "global_load_dwordx4 v[168:171], v188, s[40:41] offset:1024 sc1\n" \
  "global_load_dwordx4 v[172:175], v188, s[40:41] offset:2048 sc1\n" \
  "global_load_dwordx4 v[176:179], v188, s[40:41] offset:3072 sc1\n" \
  "s_add_u32 s40, s40, 16000\n" \
  "s_addc_u32 s41, s41, 0\n" \
  "global_load_dwordx4 v[208:211], v188, s[56:57] offset:0 sc1\n" \
  "global_load_dwordx4 v[212:215], v188, s[56:57] offset:1024 sc1\n" \
  "global_load_dwordx4 v[216:219], v188, s[56:57] offset:2048 sc1\n" \
  "global_load_dwordx4 v[220:223], v188, s[56:57] offset:3072 sc1\n" \
  "s_lshr_b32 s35, s30, 4\n" \
  "s_lshl1_add_u32 s35, s29, s35\n" \
  "s_lshl_b32 s35, s35, 6\n" \
  "s_bfe_u32 s37, s30, 0x20002\n" \
  "s_lshl3_add_u32 s35, s37, s35\n" \
  "v_and_b32 v190, 16, v189\n" \
  "v_lshl_add_u32 v190, v190, 1, s35\n" \
  "global_load_dwordx2 v[204:205], v190, s[16:17]\n" \
  "global_load_dwordx2 v[206:207], v190, s[18:19]\n" \
  "v_lshrrev_b32 v180, 1, v189\n" \
  "v_xor_b32 v180, v180, v189\n" \
  "v_and_b32 v180, 8, v180\n" \
  "v_bfe_u32 v181, v189, 2, 1\n" \
  "v_lshlrev_b32_e64 v181, v181, 5\n" \
  "v_lshlrev_b32 v180, v180, v181\n" \
  "v_bfe_u32 v181, v189, 2, 2\n" \
  "v_lshlrev_b32_e64 v181, v181, 1\n" \
  "v_cmp_gt_u32 vcc, 32, v189\n" \
  "v_mov_b32 v191, 0x3f80\n" \
  "v_add_u32 v190, 32000, v188\n" \
  "v_cndmask_b32 v192, v181, v180, vcc\n" \
  "v_bfe_i32 v180, v192, 0, 1\n" \
  "v_and_b32 v180, v180, v191\n" \
  "v_bfe_i32 v181, v192, 1, 1\n" \
  "v_and_b32 v181, v181, v191\n" \
  "v_bfe_i32 v182, v192, 2, 1\n" \
  "v_and_b32 v182, v182, v191\n" \
  "v_bfe_i32 v183, v192, 3, 1\n" \
  "v_and_b32 v183, v183, v191\n" \
  "v_and_b32 v192, 3, v189\n" \
  "v_mul_u32_u24 v192, 0x5555, v192\n" \
  "v_add_u32 v189, 16000, v188\n" \
  "v_add_u32 v191, 48000, v188\n" \
  "s_waitcnt vmcnt(0)\n" \
  "v_lshlrev_b32 v184, 16, v204\n" \
  "v_and_b32 v185, 0xffff0000, v204\n" \
  "v_lshlrev_b32 v186, 16, v205\n" \
  "v_and_b32 v187, 0xffff0000, v205\n" \
  "s_setprio 0\n" \
  "s_barrier\n" \
  "s_cmp_lg_u32 s28, 0\n" \
  "s_cbranch_scc1 LB_nb\n" \
  "s_mov_b32 exec_hi, 0\n" \
  "global_atomic_add v196, v2, s[48:49] offset:2052 sc1\n" \
  "s_mov_b64 exec, -1\n" \
  "LB_nb:\n" \
  "ds_write_b128 v1, v[116:119] offset:0\n" \
  "ds_write_b128 v1, v[120:123] offset:1024\n" \
  "ds_write_b128 v1, v[124:127] offset:2048\n" \
  "ds_write_b128 v1, v[128:131] offset:3072\n" \
  "s_add_u32 s35, s59, 64000\n" \
  "s_add_u32 s54, s54, s35\n" \
  "s_addc_u32 s55, s55, 0\n" \
  "s_add_u32 s56, s56, 32768\n" \
  "s_addc_u32 s57, s57, 0\n" \
  "s_add_u32 s58, s59, 0x10000\n" \
  "s_mov_b32 m0, s58\n" \
  "s_add_u32 s35, s58, 0x8000\n" \
  "global_load_lds_dwordx4 v188, s[54:55] sc1\n" \
  "global_load_lds_dwordx4 v188, s[54:55] offset:1024 sc1\n" \
  "ds_write_b128 v1, v[132:135] offset:16384\n" \
  "ds_write_b128 v1, v[136:139] offset:17408\n" \
  "ds_write_b128 v1, v[140:143] offset:18432\n" \
  "ds_write_b128 v1, v[144:147] offset:19456\n" \
  "s_add_u32 m0, s58, 0x4000\n" \
  "s_cmp_lt_u32 s35, 0x28000\n" \
  "global_load_lds_dwordx4 v189, s[54:55] sc1\n" \
  "global_load_lds_dwordx4 v189, s[54:55] offset:1024 sc1\n" \
  "s_cselect_b32 s58, s35, s59\n" \
  "s_add_u32 s54, s54, 32000\n" \
  "s_addc_u32 s55, s55, 0\n" \
  "ds_write_b128 v1, v[148:151] offset:32768\n" \
  "ds_write_b128 v1, v[152:155] offset:33792\n" \
  "ds_write_b128 v1, v[156:159] offset:34816\n" \
  "ds_write_b128 v1, v[160:163] offset:35840\n" \
  "global_load_dwordx4 v[224:227], v188, s[56:57] sc1\n" \
  "global_load_dwordx4 v[228:231], v188, s[56:57] offset:1024 sc1\n" \
  "ds_write_b128 v1, v[164:167] offset:49152\n" \
  "ds_write_b128 v1, v[168:171] offset:50176\n" \
  "ds_write_b128 v1, v[172:175] offset:51200\n" \
  "ds_write_b128 v1, v[176:179] offset:52224\n" \
  "global_load_dwordx4 v[232:235], v188, s[56:57] offset:2048 sc1\n" \
  "global_load_dwordx4 v[236:239], v188, s[56:57] offset:3072 sc1\n" \
  "s_add_u32 s56, s56, 32768\n" \
  "s_addc_u32 s57, s57, 0\n" \
  "s_waitcnt lgkmcnt(0)\n" \
  "LG_start:\n" \
  "s_mov_b32 s51, 7\n" \
  "s_setprio 0\n" \
  "s_barrier\n" \
  "v_lshlrev_b32_sdwa v64, 4, v208 dst_sel:DWORD dst_unused:UNUSED_PAD src0_sel:DWORD src1_sel:WORD_0\n" \
  "ds_read_b128 v[68:71], v64\n" \
  "v_mov_b64 v[0:1], v[184:185]\n" \
  "v_lshlrev_b32_sdwa v65, 4, v208 dst_sel:DWORD dst_unused:UNUSED_PAD src0_sel:DWORD src1_sel:WORD_1\n" \
  "ds_read_b128 v[72:75], v65\n" \
  "v_mov_b64 v[2:3], v[186:187]\n" \
  "v_lshlrev_b32_sdwa v66, 4, v209 dst_sel:DWORD dst_unused:UNUSED_PAD src0_sel:DWORD src1_sel:WORD_0\n" \
  "ds_read_b128 v[76:79], v66\n" \
  "v_mov_b64 v[4:5], v[184:185]\n" \
  "v_lshlrev_b32_sdwa v67, 4, v209 dst_sel:DWORD dst_unused:UNUSED_PAD src0_sel:DWORD src1_sel:WORD_1\n" \
  "ds_read_b128 v[80:83], v67\n" \
  "v_mov_b64 v[6:7], v[186:187]\n" \
  "s_mov_b32 m0, s58\n" \
  "s_add_u32 s35, s58, 0x8000\n" \
  "global_load_lds_dwordx4 v188, s[54:55] sc1\n" \
  "global_load_lds_dwordx4 v188, s[54:55] offset:1024 sc1\n" \
  "v_lshlrev_b32_sdwa v64, 4, v210 dst_sel:DWORD dst_unused:UNUSED_PAD src0_sel:DWORD src1_sel:WORD_0\n" \
  "ds_read_b128 v[84:87], v64\n" \
  "v_mov_b64 v[8:9], v[184:185]\n" \
  "v_lshlrev_b32_sdwa v65, 4, v210 dst_sel:DWORD dst_unused:UNUSED_PAD src0_sel:DWORD src1_sel:WORD_1\n" \
  "ds_read_b128 v[88:91], v65\n" \
  "v_mov_b64 v[10:11], v[186:187]\n" \
  "v_lshlrev_b32_sdwa v66, 4, v211 dst_sel:DWORD dst_unused:UNUSED_PAD src0_sel:DWORD src1_sel:WORD_0\n" \
  "ds_read_b128 v[92:95], v66\n" \
  "v_mov_b64 v[12:13], v[184:185]\n" \
  "v_lshlrev_b32_sdwa v67, 4, v211 dst_sel:DWORD dst_unused:UNUSED_PAD src0_sel:DWORD src1_sel:WORD_1\n" \
  "ds_read_b128 v[96:99], v67\n" \
  "v_mov_b64 v[14:15], v[186:187]\n" \
  "s_add_u32 m0, s58, 0x4000\n" \
  "s_cmp_lt_u32 s35, 0x28000\n" \
  "global_load_lds_dwordx4 v189, s[54:55] sc1\n" \
  "global_load_lds_dwordx4 v189, s[54:55] offset:1024 sc1\n" \
  "s_cselect_b32 s58, s35, s59\n" \
  "v_lshlrev_b32_sdwa v64, 4, v212 dst_sel:DWORD dst_unused:UNUSED_PAD src0_sel:DWORD src1_sel:WORD_0\n" \
  "ds_read_b128 v[100:103], v64\n" \
  "v_mov_b64 v[16:17], v[184:185]\n" \
  "v_lshlrev_b32_sdwa v65, 4, v212 dst_sel:DWORD dst_unused:UNUSED_PAD src0_sel:DWORD src1_sel:WORD_1\n" \
  "ds_read_b128 v[104:107], v65\n" \
  "v_mov_b64 v[18:19], v[186:187]\n" \
  "v_lshlrev_b32_sdwa v66, 4, v213 dst_sel:DWORD dst_unused:UNUSED_PAD src0_sel:DWORD src1_sel:WORD_0\n" \
  "ds_read_b128 v[108:111], v66\n" \
  "v_mov_b64 v[20:21], v[184:185]\n" \
  "v_lshlrev_b32_sdwa v67, 4, v213 dst_sel:DWORD dst_unused:UNUSED_PAD src0_sel:DWORD src1_sel:WORD_1\n" \
  "ds_read_b128 v[112:115], v67\n" \
  "v_mov_b64 v[22:23], v[186:187]\n" \
  "s_waitcnt lgkmcnt(10)\n" \
  "v_smfmac_f32_16x16x64_bf16 v[0:3], v[180:183], v[68:75], v192\n" \
  "v_lshlrev_b32_sdwa v64, 4, v214 dst_sel:DWORD dst_unused:UNUSED_PAD src0_sel:DWORD src1_sel:WORD_0\n" \
  "ds_read_b128 v[68:71], v64\n" \
  "v_mov_b64 v[24:25], v[184:185]\n" \
  "v_lshlrev_b32_sdwa v65, 4, v214 dst_sel:DWORD dst_unused:UNUSED_PAD src0_sel:DWORD src1_sel:WORD_1\n" \
  "ds_read_b128 v[72:75], v65\n" \
  "v_mov_b64 v[26:27], v[186:187]\n" \
  "s_mov_b32 m0, s58\n" \
  "s_add_u32 s35, s58, 0x8000\n" \
  "global_load_lds_dwordx4 v190, s[54:55] sc1\n" \
  "global_load_lds_dwordx4 v190, s[54:55] offset:1024 sc1\n" \
  "s_waitcnt lgkmcnt(10)\n" \
  "v_smfmac_f32_16x16x64_bf16 v[4:7], v[180:183], v[76:83], v192\n" \
  "v_lshlrev_b32_sdwa v66, 4, v215 dst_sel:DWORD dst_unused:UNUSED_PAD src0_sel:DWORD src1_sel:WORD_0\n" \
  "ds_read_b128 v[76:79], v66\n" \
  "v_mov_b64 v[28:29], v[184:185]\n" \
  "v_lshlrev_b32_sdwa v67, 4, v215 dst_sel:DWORD dst_unused:UNUSED_PAD src0_sel:DWORD src1_sel:WORD_1\n" \
  "ds_read_b128 v[80:83], v67\n" \
  "v_mov_b64 v[30:31], v[186:187]\n" \
  "s_waitcnt lgkmcnt(10)\n" \
  "v_smfmac_f32_16x16x64_bf16 v[8:11], v[180:183], v[84:91], v192\n" \
  "v_lshlrev_b32_sdwa v64, 4, v216 dst_sel:DWORD dst_unused:UNUSED_PAD src0_sel:DWORD src1_sel:WORD_0\n" \
  "ds_read_b128 v[84:87], v64\n" \
  "v_mov_b64 v[32:33], v[184:185]\n" \
  "v_lshlrev_b32_sdwa v65, 4, v216 dst_sel:DWORD dst_unused:UNUSED_PAD src0_sel:DWORD src1_sel:WORD_1\n" \
  "ds_read_b128 v[88:91], v65\n" \
  "v_mov_b64 v[34:35], v[186:187]\n" \
  "s_add_u32 m0, s58, 0x4000\n" \
  "s_cmp_lt_u32 s35, 0x28000\n" \
  "global_load_lds_dwordx4 v191, s[54:55] sc1\n" \
  "global_load_lds_dwordx4 v191, s[54:55] offset:1024 sc1\n" \
  "s_cselect_b32 s58, s35, s59\n" \
  "s_waitcnt lgkmcnt(10)\n" \
  "v_smfmac_f32_16x16x64_bf16 v[12:15], v[180:183], v[92:99], v192\n" \
  "v_lshlrev_b32_sdwa v66, 4, v217 dst_sel:DWORD dst_unused:UNUSED_PAD src0_sel:DWORD src1_sel:WORD_0\n" \
  "ds_read_b128 v[92:95], v66\n" \
  "v_mov_b64 v[36:37], v[184:185]\n" \
  "v_lshlrev_b32_sdwa v67, 4, v217 dst_sel:DWORD dst_unused:UNUSED_PAD src0_sel:DWORD src1_sel:WORD_1\n" \
  "ds_read_b128 v[96:99], v67\n" \
  "v_mov_b64 v[38:39], v[186:187]\n" \
  "s_waitcnt lgkmcnt(10)\n" \
  "v_smfmac_f32_16x16x64_bf16 v[16:19], v[180:183], v[100:107], v192\n" \
  "v_lshlrev_b32_sdwa v64, 4, v218 dst_sel:DWORD dst_unused:UNUSED_PAD src0_sel:DWORD src1_sel:WORD_0\n" \
  "ds_read_b128 v[100:103], v64\n" \
  "v_mov_b64 v[40:41], v[184:185]\n" \
  "v_lshlrev_b32_sdwa v65, 4, v218 dst_sel:DWORD dst_unused:UNUSED_PAD src0_sel:DWORD src1_sel:WORD_1\n" \
  "ds_read_b128 v[104:107], v65\n" \
  "v_mov_b64 v[42:43], v[186:187]\n" \
  "global_load_dwordx4 v[240:243], v188, s[56:57] sc1\n" \
  "global_load_dwordx4 v[244:247], v188, s[56:57] offset:1024 sc1\n" \
  "s_waitcnt lgkmcnt(10)\n" \
  "v_smfmac_f32_16x16x64_bf16 v[20:23], v[180:183], v[108:115], v192\n" \
  "v_lshlrev_b32_sdwa v66, 4, v219 dst_sel:DWORD dst_unused:UNUSED_PAD src0_sel:DWORD src1_sel:WORD_0\n" \
  "ds_read_b128 v[108:111], v66\n" \
  "v_mov_b64 v[44:45], v[184:185]\n" \
  "v_lshlrev_b32_sdwa v67, 4, v219 dst_sel:DWORD dst_unused:UNUSED_PAD src0_sel:DWORD src1_sel:WORD_1\n" \
  "ds_read_b128 v[112:115], v67\n" \
  "v_mov_b64 v[46:47], v[186:187]\n" \
  "s_waitcnt lgkmcnt(10)\n" \
  "v_smfmac_f32_16x16x64_bf16 v[24:27], v[180:183], v[68:75], v192\n" \
  "v_lshlrev_b32_sdwa v64, 4, v220 dst_sel:DWORD dst_unused:UNUSED_PAD src0_sel:DWORD src1_sel:WORD_0\n" \
  "ds_read_b128 v[68:71], v64\n" \
  "v_mov_b64 v[48:49], v[184:185]\n" \
  "v_lshlrev_b32_sdwa v65, 4, v220 dst_sel:DWORD dst_unused:UNUSED_PAD src0_sel:DWORD src1_sel:WORD_1\n" \
  "ds_read_b128 v[72:75], v65\n" \
  "v_mov_b64 v[50:51], v[186:187]\n" \
  "global_load_dwordx4 v[248:251], v188, s[56:57] offset:2048 sc1\n" \
  "global_load_dwordx4 v[252:255], v188, s[56:57] offset:3072 sc1\n" \
  "s_waitcnt lgkmcnt(10)\n" \
  "v_smfmac_f32_16x16x64_bf16 v[28:31], v[180:183], v[76:83], v192\n" \
  "v_lshlrev_b32_sdwa v66, 4, v221 dst_sel:DWORD dst_unused:UNUSED_PAD src0_sel:DWORD src1_sel:WORD_0\n" \
  "ds_read_b128 v[76:79], v66\n" \
  "v_mov_b64 v[52:53], v[184:185]\n" \
  "v_lshlrev_b32_sdwa v67, 4, v221 dst_sel:DWORD dst_unused:UNUSED_PAD src0_sel:DWORD src1_sel:WORD_1\n" \
  "ds_read_b128 v[80:83], v67\n" \
  "v_mov_b64 v[54:55], v[186:187]\n" \
  "s_waitcnt lgkmcnt(10)\n" \
  "v_smfmac_f32_16x16x64_bf16 v[32:35], v[180:183], v[84:91], v192\n" \
  "v_lshlrev_b32_sdwa v64, 4, v222 dst_sel:DWORD dst_unused:UNUSED_PAD src0_sel:DWORD src1_sel:WORD_0\n" \
  "ds_read_b128 v[84:87], v64\n" \
  "v_mov_b64 v[56:57], v[184:185]\n" \
  "v_lshlrev_b32_sdwa v65, 4, v222 dst_sel:DWORD dst_unused:UNUSED_PAD src0_sel:DWORD src1_sel:WORD_1\n" \
  "ds_read_b128 v[88:91], v65\n" \
  "v_mov_b64 v[58:59], v[186:187]\n" \
  "s_waitcnt lgkmcnt(10)\n" \
  "v_smfmac_f32_16x16x64_bf16 v[36:39], v[180:183], v[92:99], v192\n" \
  "v_lshlrev_b32_sdwa v66, 4, v223 dst_sel:DWORD dst_unused:UNUSED_PAD src0_sel:DWORD src1_sel:WORD_0\n" \
  "ds_read_b128 v[92:95], v66\n" \
  "v_mov_b64 v[60:61], v[184:185]\n" \
  "v_lshlrev_b32_sdwa v67, 4, v223 dst_sel:DWORD dst_unused:UNUSED_PAD src0_sel:DWORD src1_sel:WORD_1\n" \
  "ds_read_b128 v[96:99], v67\n" \
  "v_mov_b64 v[62:63], v[186:187]\n" \
  "s_add_u32 s56, s56, 32768\n" \
  "s_addc_u32 s57, s57, 0\n" \
  "s_add_u32 s54, s54, 64000\n" \
  "s_addc_u32 s55, s55, 0\n" \
  "s_waitcnt lgkmcnt(10)\n" \
  "v_smfmac_f32_16x16x64_bf16 v[40:43], v[180:183], v[100:107], v192\n" \
  "s_waitcnt lgkmcnt(8)\n" \
  "v_smfmac_f32_16x16x64_bf16 v[44:47], v[180:183], v[108:115], v192\n" \
  "s_waitcnt vmcnt(8)\n" \
  "s_barrier\n" \
  "LG_loop:\n" \
  "v_lshlrev_b32_sdwa v64, 4, v224 dst_sel:DWORD dst_unused:UNUSED_PAD src0_sel:DWORD src1_sel:WORD_0\n" \
  "ds_read_b128 v[100:103], v64\n" \
  "v_lshlrev_b32_sdwa v65, 4, v224 dst_sel:DWORD dst_unused:UNUSED_PAD src0_sel:DWORD src1_sel:WORD_1\n" \
  "ds_read_b128 v[104:107], v65\n" \
  "v_lshlrev_b32_sdwa v66, 4, v225 dst_sel:DWORD dst_unused:UNUSED_PAD src0_sel:DWORD src1_sel:WORD_0\n" \
  "ds_read_b128 v[108:111], v66\n" \
  "v_lshlrev_b32_sdwa v67, 4, v225 dst_sel:DWORD dst_unused:UNUSED_PAD src0_sel:DWORD src1_sel:WORD_1\n" \
  "ds_read_b128 v[112:115], v67\n" \
  "s_waitcnt lgkmcnt(10)\n" \
  "v_smfmac_f32_16x16x64_bf16 v[48:51], v[180:183], v[68:75], v192\n" \
  "s_waitcnt lgkmcnt(8)\n" \
  "v_smfmac_f32_16x16x64_bf16 v[52:55], v[180:183], v[76:83], v192\n" \
  "s_waitcnt lgkmcnt(6)\n" \
  "v_smfmac_f32_16x16x64_bf16 v[56:59], v[180:183], v[84:91], v192\n" \
  "s_waitcnt lgkmcnt(4)\n" \
  "v_smfmac_f32_16x16x64_bf16 v[60:63], v[180:183], v[92:99], v192\n" \
  "s_barrier\n" \
  "s_mov_b32 m0, s58\n" \
  "s_add_u32 s35, s58, 0x8000\n" \
  "global_load_lds_dwordx4 v188, s[54:55] sc1\n" \
  "global_load_lds_dwordx4 v188, s[54:55] offset:1024 sc1\n" \
  "v_lshlrev_b32_sdwa v64, 4, v226 dst_sel:DWORD dst_unused:UNUSED_PAD src0_sel:DWORD src1_sel:WORD_0\n" \
  "ds_read_b128 v[68:71], v64\n" \
  "v_lshlrev_b32_sdwa v65, 4, v226 dst_sel:DWORD dst_unused:UNUSED_PAD src0_sel:DWORD src1_sel:WORD_1\n" \
  "ds_read_b128 v[72:75], v65\n" \
  "v_lshlrev_b32_sdwa v66, 4, v227 dst_sel:DWORD dst_unused:UNUSED_PAD src0_sel:DWORD src1_sel:WORD_0\n" \
  "ds_read_b128 v[76:79], v66\n" \
  "v_lshlrev_b32_sdwa v67, 4, v227 dst_sel:DWORD dst_unused:UNUSED_PAD src0_sel:DWORD src1_sel:WORD_1\n" \
  "ds_read_b128 v[80:83], v67\n" \
  "s_add_u32 m0, s58, 0x4000\n" \
  "s_cmp_lt_u32 s35, 0x28000\n" \
  "global_load_lds_dwordx4 v189, s[54:55] sc1\n" \
  "global_load_lds_dwordx4 v189, s[54:55] offset:1024 sc1\n" \
  "s_cselect_b32 s58, s35, s59\n" \
  "v_lshlrev_b32_sdwa v64, 4, v228 dst_sel:DWORD dst_unused:UNUSED_PAD src0_sel:DWORD src1_sel:WORD_0\n" \
  "ds_read_b128 v[84:87], v64\n" \
  "v_lshlrev_b32_sdwa v65, 4, v228 dst_sel:DWORD dst_unused:UNUSED_PAD src0_sel:DWORD src1_sel:WORD_1\n" \
  "ds_read_b128 v[88:91], v65\n" \
  "v_lshlrev_b32_sdwa v66, 4, v229 dst_sel:DWORD dst_unused:UNUSED_PAD src0_sel:DWORD src1_sel:WORD_0\n" \
  "ds_read_b128 v[92:95], v66\n" \
  "v_lshlrev_b32_sdwa v67, 4, v229 dst_sel:DWORD dst_unused:UNUSED_PAD src0_sel:DWORD src1_sel:WORD_1\n" \
  "ds_read_b128 v[96:99], v67\n" \
  "s_waitcnt lgkmcnt(10)\n" \
  "v_smfmac_f32_16x16x64_bf16 v[0:3], v[180:183], v[100:107], v192\n" \
  "v_lshlrev_b32_sdwa v64, 4, v230 dst_sel:DWORD dst_unused:UNUSED_PAD src0_sel:DWORD src1_sel:WORD_0\n" \
  "ds_read_b128 v[100:103], v64\n" \
  "v_lshlrev_b32_sdwa v65, 4, v230 dst_sel:DWORD dst_unused:UNUSED_PAD src0_sel:DWORD src1_sel:WORD_1\n" \
  "ds_read_b128 v[104:107], v65\n" \
  "s_mov_b32 m0, s58\n" \
  "s_add_u32 s35, s58, 0x8000\n" \
  "global_load_lds_dwordx4 v190, s[54:55] sc1\n" \
  "global_load_lds_dwordx4 v190, s[54:55] offset:1024 sc1\n" \
  "s_waitcnt lgkmcnt(10)\n" \
  "v_smfmac_f32_16x16x64_bf16 v[4:7], v[180:183], v[108:115], v192\n" \
  "v_lshlrev_b32_sdwa v66, 4, v231 dst_sel:DWORD dst_unused:UNUSED_PAD src0_sel:DWORD src1_sel:WORD_0\n" \
  "ds_read_b128 v[108:111], v66\n" \
  "v_lshlrev_b32_sdwa v67, 4, v231 dst_sel:DWORD dst_unused:UNUSED_PAD src0_sel:DWORD src1_sel:WORD_1\n" \
  "ds_read_b128 v[112:115], v67\n" \
  "s_waitcnt lgkmcnt(10)\n" \
  "v_smfmac_f32_16x16x64_bf16 v[8:11], v[180:183], v[68:75], v192\n" \
  "v_lshlrev_b32_sdwa v64, 4, v232 dst_sel:DWORD dst_unused:UNUSED_PAD src0_sel:DWORD src1_sel:WORD_0\n" \
  "ds_read_b128 v[68:71], v64\n" \
  "v_lshlrev_b32_sdwa v65, 4, v232 dst_sel:DWORD dst_unused:UNUSED_PAD src0_sel:DWORD src1_sel:WORD_1\n" \
  "ds_read_b128 v[72:75], v65\n" \
  "s_add_u32 m0, s58, 0x4000\n" \
  "s_cmp_lt_u32 s35, 0x28000\n" \
  "global_load_lds_dwordx4 v191, s[54:55] sc1\n" \
  "global_load_lds_dwordx4 v191, s[54:55] offset:1024 sc1\n" \
  "s_cselect_b32 s58, s35, s59\n" \
  "s_waitcnt lgkmcnt(10)\n" \
  "v_smfmac_f32_16x16x64_bf16 v[12:15], v[180:183], v[76:83], v192\n" \
  "v_lshlrev_b32_sdwa v66, 4, v233 dst_sel:DWORD dst_unused:UNUSED_PAD src0_sel:DWORD src1_sel:WORD_0\n" \
  "ds_read_b128 v[76:79], v66\n" \
  "v_lshlrev_b32_sdwa v67, 4, v233 dst_sel:DWORD dst_unused:UNUSED_PAD src0_sel:DWORD src1_sel:WORD_1\n" \
  "ds_read_b128 v[80:83], v67\n" \
  "s_waitcnt lgkmcnt(10)\n" \
  "v_smfmac_f32_16x16x64_bf16 v[16:19], v[180:183], v[84:91], v192\n" \
  "v_lshlrev_b32_sdwa v64, 4, v234 dst_sel:DWORD dst_unused:UNUSED_PAD src0_sel:DWORD src1_sel:WORD_0\n" \
  "ds_read_b128 v[84:87], v64\n" \
  "v_lshlrev_b32_sdwa v65, 4, v234 dst_sel:DWORD dst_unused:UNUSED_PAD src0_sel:DWORD src1_sel:WORD_1\n" \
  "ds_read_b128 v[88:91], v65\n" \
  "global_load_dwordx4 v[208:211], v188, s[56:57] sc1\n" \
  "global_load_dwordx4 v[212:215], v188, s[56:57] offset:1024 sc1\n" \
  "s_waitcnt lgkmcnt(10)\n" \
  "v_smfmac_f32_16x16x64_bf16 v[20:23], v[180:183], v[92:99], v192\n" \
  "v_lshlrev_b32_sdwa v66, 4, v235 dst_sel:DWORD dst_unused:UNUSED_PAD src0_sel:DWORD src1_sel:WORD_0\n" \
  "ds_read_b128 v[92:95], v66\n" \
  "v_lshlrev_b32_sdwa v67, 4, v235 dst_sel:DWORD dst_unused:UNUSED_PAD src0_sel:DWORD src1_sel:WORD_1\n" \
  "ds_read_b128 v[96:99], v67\n" \
  "s_waitcnt lgkmcnt(10)\n" \
  "v_smfmac_f32_16x16x64_bf16 v[24:27], v[180:183], v[100:107], v192\n" \
  "v_lshlrev_b32_sdwa v64, 4, v236 dst_sel:DWORD dst_unused:UNUSED_PAD src0_sel:DWORD src1_sel:WORD_0\n" \
  "ds_read_b128 v[100:103], v64\n" \
  "v_lshlrev_b32_sdwa v65, 4, v236 dst_sel:DWORD dst_unused:UNUSED_PAD src0_sel:DWORD src1_sel:WORD_1\n" \
  "ds_read_b128 v[104:107], v65\n" \
  "global_load_dwordx4 v[216:219], v188, s[56:57] offset:2048 sc1\n" \
  "global_load_dwordx4 v[220:223], v188, s[56:57] offset:3072 sc1\n" \
  "s_waitcnt lgkmcnt(10)\n" \
  "v_smfmac_f32_16x16x64_bf16 v[28:31], v[180:183], v[108:115], v192\n" \
  "v_lshlrev_b32_sdwa v66, 4, v237 dst_sel:DWORD dst_unused:UNUSED_PAD src0_sel:DWORD src1_sel:WORD_0\n" \
  "ds_read_b128 v[108:111], v66\n" \
  "v_lshlrev_b32_sdwa v67, 4, v237 dst_sel:DWORD dst_unused:UNUSED_PAD src0_sel:DWORD src1_sel:WORD_1\n" \
  "ds_read_b128 v[112:115], v67\n" \
  "s_waitcnt lgkmcnt(10)\n" \
  "v_smfmac_f32_16x16x64_bf16 v[32:35], v[180:183], v[68:75], v192\n" \
  "v_lshlrev_b32_sdwa v64, 4, v238 dst_sel:DWORD dst_unused:UNUSED_PAD src0_sel:DWORD src1_sel:WORD_0\n" \
  "ds_read_b128 v[68:71], v64\n" \
  "v_lshlrev_b32_sdwa v65, 4, v238 dst_sel:DWORD dst_unused:UNUSED_PAD src0_sel:DWORD src1_sel:WORD_1\n" \
  "ds_read_b128 v[72:75], v65\n" \
  "s_waitcnt lgkmcnt(10)\n" \
  "v_smfmac_f32_16x16x64_bf16 v[36:39], v[180:183], v[76:83], v192\n" \
  "v_lshlrev_b32_sdwa v66, 4, v239 dst_sel:DWORD dst_unused:UNUSED_PAD src0_sel:DWORD src1_sel:WORD_0\n" \
  "ds_read_b128 v[76:79], v66\n" \
  "v_lshlrev_b32_sdwa v67, 4, v239 dst_sel:DWORD dst_unused:UNUSED_PAD src0_sel:DWORD src1_sel:WORD_1\n" \
  "ds_read_b128 v[80:83], v67\n" \
  "s_add_u32 s56, s56, 32768\n" \
  "s_addc_u32 s57, s57, 0\n" \
  "s_add_u32 s54, s54, 64000\n" \
  "s_addc_u32 s55, s55, 0\n" \
  "s_waitcnt lgkmcnt(10)\n" \
  "v_smfmac_f32_16x16x64_bf16 v[40:43], v[180:183], v[84:91], v192\n" \
  "s_waitcnt lgkmcnt(8)\n" \
  "v_smfmac_f32_16x16x64_bf16 v[44:47], v[180:183], v[92:99], v192\n" \
  "s_waitcnt vmcnt(8)\n" \
  "s_barrier\n" \
  "v_lshlrev_b32_sdwa v64, 4, v240 dst_sel:DWORD dst_unused:UNUSED_PAD src0_sel:DWORD src1_sel:WORD_0\n" \
  "ds_read_b128 v[84:87], v64\n" \
  "v_lshlrev_b32_sdwa v65, 4, v240 dst_sel:DWORD dst_unused:UNUSED_PAD src0_sel:DWORD src1_sel:WORD_1\n" \
  "ds_read_b128 v[88:91], v65\n" \
  "v_lshlrev_b32_sdwa v66, 4, v241 dst_sel:DWORD dst_unused:UNUSED_PAD src0_sel:DWORD src1_sel:WORD_0\n" \
  "ds_read_b128 v[92:95], v66\n" \
  "v_lshlrev_b32_sdwa v67, 4, v241 dst_sel:DWORD dst_unused:UNUSED_PAD src0_sel:DWORD src1_sel:WORD_1\n" \
  "ds_read_b128 v[96:99], v67\n" \
  "s_waitcnt lgkmcnt(10)\n" \
  "v_smfmac_f32_16x16x64_bf16 v[48:51], v[180:183], v[100:107], v192\n" \
  "s_waitcnt lgkmcnt(8)\n" \
  "v_smfmac_f32_16x16x64_bf16 v[52:55], v[180:183], v[108:115], v192\n" \
  "s_waitcnt lgkmcnt(6)\n" \
  "v_smfmac_f32_16x16x64_bf16 v[56:59], v[180:183], v[68:75], v192\n" \
  "s_waitcnt lgkmcnt(4)\n" \
  "v_smfmac_f32_16x16x64_bf16 v[60:63], v[180:183], v[76:83], v192\n" \
  "s_barrier\n" \
  "s_mov_b32 m0, s58\n" \
  "s_add_u32 s35, s58, 0x8000\n" \
  "global_load_lds_dwordx4 v188, s[54:55] sc1\n" \
  "global_load_lds_dwordx4 v188, s[54:55] offset:1024 sc1\n" \
  "v_lshlrev_b32_sdwa v64, 4, v242 dst_sel:DWORD dst_unused:UNUSED_PAD src0_sel:DWORD src1_sel:WORD_0\n" \
  "ds_read_b128 v[100:103], v64\n" \
  "v_lshlrev_b32_sdwa v65, 4, v242 dst_sel:DWORD dst_unused:UNUSED_PAD src0_sel:DWORD src1_sel:WORD_1\n" \
  "ds_read_b128 v[104:107], v65\n" \
  "v_lshlrev_b32_sdwa v66, 4, v243 dst_sel:DWORD dst_unused:UNUSED_PAD src0_sel:DWORD src1_sel:WORD_0\n" \
  "ds_read_b128 v[108:111], v66\n" \
  "v_lshlrev_b32_sdwa v67, 4, v243 dst_sel:DWORD dst_unused:UNUSED_PAD src0_sel:DWORD src1_sel:WORD_1\n" \
  "ds_read_b128 v[112:115], v67\n" \
  "s_add_u32 m0, s58, 0x4000\n" \
  "s_cmp_lt_u32 s35, 0x28000\n" \
  "global_load_lds_dwordx4 v189, s[54:55] sc1\n" \
  "global_load_lds_dwordx4 v189, s[54:55] offset:1024 sc1\n" \
  "s_cselect_b32 s58, s35, s59\n" \
  "v_lshlrev_b32_sdwa v64, 4, v244 dst_sel:DWORD dst_unused:UNUSED_PAD src0_sel:DWORD src1_sel:WORD_0\n" \
  "ds_read_b128 v[68:71], v64\n" \
  "v_lshlrev_b32_sdwa v65, 4, v244 dst_sel:DWORD dst_unused:UNUSED_PAD src0_sel:DWORD src1_sel:WORD_1\n" \
  "ds_read_b128 v[72:75], v65\n" \
  "v_lshlrev_b32_sdwa v66, 4, v245 dst_sel:DWORD dst_unused:UNUSED_PAD src0_sel:DWORD src1_sel:WORD_0\n" \
  "ds_read_b128 v[76:79], v66\n" \
  "v_lshlrev_b32_sdwa v67, 4, v245 dst_sel:DWORD dst_unused:UNUSED_PAD src0_sel:DWORD src1_sel:WORD_1\n" \
  "ds_read_b128 v[80:83], v67\n" \
  "s_waitcnt lgkmcnt(10)\n" \
  "v_smfmac_f32_16x16x64_bf16 v[0:3], v[180:183], v[84:91], v192\n" \
  "v_lshlrev_b32_sdwa v64, 4, v246 dst_sel:DWORD dst_unused:UNUSED_PAD src0_sel:DWORD src1_sel:WORD_0\n" \
  "ds_read_b128 v[84:87], v64\n" \
  "v_lshlrev_b32_sdwa v65, 4, v246 dst_sel:DWORD dst_unused:UNUSED_PAD src0_sel:DWORD src1_sel:WORD_1\n" \
  "ds_read_b128 v[88:91], v65\n" \
  "s_mov_b32 m0, s58\n" \
  "s_add_u32 s35, s58, 0x8000\n" \
  "global_load_lds_dwordx4 v190, s[54:55] sc1\n" \
  "global_load_lds_dwordx4 v190, s[54:55] offset:1024 sc1\n" \
  "s_waitcnt lgkmcnt(10)\n" \
  "v_smfmac_f32_16x16x64_bf16 v[4:7], v[180:183], v[92:99], v192\n" \
  "v_lshlrev_b32_sdwa v66, 4, v247 dst_sel:DWORD dst_unused:UNUSED_PAD src0_sel:DWORD src1_sel:WORD_0\n" \
  "ds_read_b128 v[92:95], v66\n" \
  "v_lshlrev_b32_sdwa v67, 4, v247 dst_sel:DWORD dst_unused:UNUSED_PAD src0_sel:DWORD src1_sel:WORD_1\n" \
  "ds_read_b128 v[96:99], v67\n" \
  "s_waitcnt lgkmcnt(10)\n" \
  "v_smfmac_f32_16x16x64_bf16 v[8:11], v[180:183], v[100:107], v192\n" \
  "v_lshlrev_b32_sdwa v64, 4, v248 dst_sel:DWORD dst_unused:UNUSED_PAD src0_sel:DWORD src1_sel:WORD_0\n" \
  "ds_read_b128 v[100:103], v64\n" \
  "v_lshlrev_b32_sdwa v65, 4, v248 dst_sel:DWORD dst_unused:UNUSED_PAD src0_sel:DWORD src1_sel:WORD_1\n" \
  "ds_read_b128 v[104:107], v65\n" \
  "s_add_u32 m0, s58, 0x4000\n" \
  "s_cmp_lt_u32 s35, 0x28000\n" \
  "global_load_lds_dwordx4 v191, s[54:55] sc1\n" \
  "global_load_lds_dwordx4 v191, s[54:55] offset:1024 sc1\n" \
  "s_cselect_b32 s58, s35, s59\n" \
  "s_waitcnt lgkmcnt(10)\n" \
  "v_smfmac_f32_16x16x64_bf16 v[12:15], v[180:183], v[108:115], v192\n" \
  "v_lshlrev_b32_sdwa v66, 4, v249 dst_sel:DWORD dst_unused:UNUSED_PAD src0_sel:DWORD src1_sel:WORD_0\n" \
  "ds_read_b128 v[108:111], v66\n" \
  "v_lshlrev_b32_sdwa v67, 4, v249 dst_sel:DWORD dst_unused:UNUSED_PAD src0_sel:DWORD src1_sel:WORD_1\n" \
  "ds_read_b128 v[112:115], v67\n" \
  "s_waitcnt lgkmcnt(10)\n" \
  "v_smfmac_f32_16x16x64_bf16 v[16:19], v[180:183], v[68:75], v192\n" \
  "v_lshlrev_b32_sdwa v64, 4, v250 dst_sel:DWORD dst_unused:UNUSED_PAD src0_sel:DWORD src1_sel:WORD_0\n" \
  "ds_read_b128 v[68:71], v64\n" \
  "v_lshlrev_b32_sdwa v65, 4, v250 dst_sel:DWORD dst_unused:UNUSED_PAD src0_sel:DWORD src1_sel:WORD_1\n" \
  "ds_read_b128 v[72:75], v65\n" \
  "global_load_dwordx4 v[224:227], v188, s[56:57] sc1\n" \
  "global_load_dwordx4 v[228:231], v188, s[56:57] offset:1024 sc1\n" \
  "s_waitcnt lgkmcnt(10)\n" \
  "v_smfmac_f32_16x16x64_bf16 v[20:23], v[180:183], v[76:83], v192\n" \
  "v_lshlrev_b32_sdwa v66, 4, v251 dst_sel:DWORD dst_unused:UNUSED_PAD src0_sel:DWORD src1_sel:WORD_0\n" \
  "ds_read_b128 v[76:79], v66\n" \
  "v_lshlrev_b32_sdwa v67, 4, v251 dst_sel:DWORD dst_unused:UNUSED_PAD src0_sel:DWORD src1_sel:WORD_1\n" \
  "ds_read_b128 v[80:83], v67\n" \
  "s_waitcnt lgkmcnt(10)\n" \
  "v_smfmac_f32_16x16x64_bf16 v[24:27], v[180:183], v[84:91], v192\n" \
  "v_lshlrev_b32_sdwa v64, 4, v252 dst_sel:DWORD dst_unused:UNUSED_PAD src0_sel:DWORD src1_sel:WORD_0\n" \
  "ds_read_b128 v[84:87], v64\n" \
  "v_lshlrev_b32_sdwa v65, 4, v252 dst_sel:DWORD dst_unused:UNUSED_PAD src0_sel:DWORD src1_sel:WORD_1\n" \
  "ds_read_b128 v[88:91], v65\n" \
  "global_load_dwordx4 v[232:235], v188, s[56:57] offset:2048 sc1\n" \
  "global_load_dwordx4 v[236:239], v188, s[56:57] offset:3072 sc1\n" \
  "s_waitcnt lgkmcnt(10)\n" \
  "v_smfmac_f32_16x16x64_bf16 v[28:31], v[180:183], v[92:99], v192\n" \
  "v_lshlrev_b32_sdwa v66, 4, v253 dst_sel:DWORD dst_unused:UNUSED_PAD src0_sel:DWORD src1_sel:WORD_0\n" \
  "ds_read_b128 v[92:95], v66\n" \
  "v_lshlrev_b32_sdwa v67, 4, v253 dst_sel:DWORD dst_unused:UNUSED_PAD src0_sel:DWORD src1_sel:WORD_1\n" \
  "ds_read_b128 v[96:99], v67\n" \
  "s_waitcnt lgkmcnt(10)\n" \
  "v_smfmac_f32_16x16x64_bf16 v[32:35], v[180:183], v[100:107], v192\n" \
  "v_lshlrev_b32_sdwa v64, 4, v254 dst_sel:DWORD dst_unused:UNUSED_PAD src0_sel:DWORD src1_sel:WORD_0\n" \
  "ds_read_b128 v[100:103], v64\n" \
  "v_lshlrev_b32_sdwa v65, 4, v254 dst_sel:DWORD dst_unused:UNUSED_PAD src0_sel:DWORD src1_sel:WORD_1\n" \
  "ds_read_b128 v[104:107], v65\n" \
  "s_waitcnt lgkmcnt(10)\n" \
  "v_smfmac_f32_16x16x64_bf16 v[36:39], v[180:183], v[108:115], v192\n" \
  "v_lshlrev_b32_sdwa v66, 4, v255 dst_sel:DWORD dst_unused:UNUSED_PAD src0_sel:DWORD src1_sel:WORD_0\n" \
  "ds_read_b128 v[108:111], v66\n" \
  "v_lshlrev_b32_sdwa v67, 4, v255 dst_sel:DWORD dst_unused:UNUSED_PAD src0_sel:DWORD src1_sel:WORD_1\n" \
  "ds_read_b128 v[112:115], v67\n" \
  "s_add_u32 s56, s56, 32768\n" \
  "s_addc_u32 s57, s57, 0\n" \
  "s_add_u32 s54, s54, 64000\n" \
  "s_addc_u32 s55, s55, 0\n" \
  "s_waitcnt lgkmcnt(10)\n" \
  "v_smfmac_f32_16x16x64_bf16 v[40:43], v[180:183], v[68:75], v192\n" \
  "s_waitcnt lgkmcnt(8)\n" \
  "v_smfmac_f32_16x16x64_bf16 v[44:47], v[180:183], v[76:83], v192\n" \
  "s_waitcnt vmcnt(8)\n" \
  "s_barrier\n" \
  "s_lshl3_add_u32 s36, s27, s51\n" \
  "s_cmp_eq_u32 s36, 37\n" \
  "s_cbranch_scc1 LG_pol\n" \
  "LG_rt0:\n" \
  "v_lshlrev_b32_sdwa v64, 4, v208 dst_sel:DWORD dst_unused:UNUSED_PAD src0_sel:DWORD src1_sel:WORD_0\n" \
  "ds_read_b128 v[68:71], v64\n" \
  "v_lshlrev_b32_sdwa v65, 4, v208 dst_sel:DWORD dst_unused:UNUSED_PAD src0_sel:DWORD src1_sel:WORD_1\n" \
  "ds_read_b128 v[72:75], v65\n" \
  "v_lshlrev_b32_sdwa v66, 4, v209 dst_sel:DWORD dst_unused:UNUSED_PAD src0_sel:DWORD src1_sel:WORD_0\n" \
  "ds_read_b128 v[76:79], v66\n" \
  "v_lshlrev_b32_sdwa v67, 4, v209 dst_sel:DWORD dst_unused:UNUSED_PAD src0_sel:DWORD src1_sel:WORD_1\n" \
  "ds_read_b128 v[80:83], v67\n" \
  "s_waitcnt lgkmcnt(10)\n" \
  "v_smfmac_f32_16x16x64_bf16 v[48:51], v[180:183], v[84:91], v192\n" \
  "s_waitcnt lgkmcnt(8)\n" \
  "v_smfmac_f32_16x16x64_bf16 v[52:55], v[180:183], v[92:99], v192\n" \
  "s_waitcnt lgkmcnt(6)\n" \
  "v_smfmac_f32_16x16x64_bf16 v[56:59], v[180:183], v[100:107], v192\n" \
  "s_waitcnt lgkmcnt(4)\n" \
  "v_smfmac_f32_16x16x64_bf16 v[60:63], v[180:183], v[108:115], v192\n" \
  "s_barrier\n" \
  "s_mov_b32 m0, s58\n" \
  "s_add_u32 s35, s58, 0x8000\n" \
  "global_load_lds_dwordx4 v188, s[54:55] sc1\n" \
  "global_load_lds_dwordx4 v188, s[54:55] offset:1024 sc1\n" \
  "v_lshlrev_b32_sdwa v64, 4, v210 dst_sel:DWORD dst_unused:UNUSED_PAD src0_sel:DWORD src1_sel:WORD_0\n" \
  "ds_read_b128 v[84:87], v64\n" \
  "v_lshlrev_b32_sdwa v65, 4, v210 dst_sel:DWORD dst_unused:UNUSED_PAD src0_sel:DWORD src1_sel:WORD_1\n" \
  "ds_read_b128 v[88:91], v65\n" \
  "v_lshlrev_b32_sdwa v66, 4, v211 dst_sel:DWORD dst_unused:UNUSED_PAD src0_sel:DWORD src1_sel:WORD_0\n" \
  "ds_read_b128 v[92:95], v66\n" \
  "v_lshlrev_b32_sdwa v67, 4, v211 dst_sel:DWORD dst_unused:UNUSED_PAD src0_sel:DWORD src1_sel:WORD_1\n" \
  "ds_read_b128 v[96:99], v67\n" \
  "s_add_u32 m0, s58, 0x4000\n" \
  "s_cmp_lt_u32 s35, 0x28000\n" \
  "global_load_lds_dwordx4 v189, s[54:55] sc1\n" \
  "global_load_lds_dwordx4 v189, s[54:55] offset:1024 sc1\n" \
  "s_cselect_b32 s58, s35, s59\n" \
  "v_lshlrev_b32_sdwa v64, 4, v212 dst_sel:DWORD dst_unused:UNUSED_PAD src0_sel:DWORD src1_sel:WORD_0\n" \
  "ds_read_b128 v[100:103], v64\n" \
  "v_lshlrev_b32_sdwa v65, 4, v212 dst_sel:DWORD dst_unused:UNUSED_PAD src0_sel:DWORD src1_sel:WORD_1\n" \
  "ds_read_b128 v[104:107], v65\n" \
  "v_lshlrev_b32_sdwa v66, 4, v213 dst_sel:DWORD dst_unused:UNUSED_PAD src0_sel:DWORD src1_sel:WORD_0\n" \
  "ds_read_b128 v[108:111], v66\n" \
  "v_lshlrev_b32_sdwa v67, 4, v213 dst_sel:DWORD dst_unused:UNUSED_PAD src0_sel:DWORD src1_sel:WORD_1\n" \
  "ds_read_b128 v[112:115], v67\n" \
  "s_waitcnt lgkmcnt(10)\n" \
  "v_smfmac_f32_16x16x64_bf16 v[0:3], v[180:183], v[68:75], v192\n" \
  "v_lshlrev_b32_sdwa v64, 4, v214 dst_sel:DWORD dst_unused:UNUSED_PAD src0_sel:DWORD src1_sel:WORD_0\n" \
  "ds_read_b128 v[68:71], v64\n" \
  "v_lshlrev_b32_sdwa v65, 4, v214 dst_sel:DWORD dst_unused:UNUSED_PAD src0_sel:DWORD src1_sel:WORD_1\n" \
  "ds_read_b128 v[72:75], v65\n" \
  "s_mov_b32 m0, s58\n" \
  "s_add_u32 s35, s58, 0x8000\n" \
  "global_load_lds_dwordx4 v190, s[54:55] sc1\n" \
  "global_load_lds_dwordx4 v190, s[54:55] offset:1024 sc1\n" \
  "s_waitcnt lgkmcnt(10)\n" \
  "v_smfmac_f32_16x16x64_bf16 v[4:7], v[180:183], v[76:83], v192\n" \
  "v_lshlrev_b32_sdwa v66, 4, v215 dst_sel:DWORD dst_unused:UNUSED_PAD src0_sel:DWORD src1_sel:WORD_0\n" \
  "ds_read_b128 v[76:79], v66\n" \
  "v_lshlrev_b32_sdwa v67, 4, v215 dst_sel:DWORD dst_unused:UNUSED_PAD src0_sel:DWORD src1_sel:WORD_1\n" \
  "ds_read_b128 v[80:83], v67\n" \
  "s_waitcnt lgkmcnt(10)\n" \
  "v_smfmac_f32_16x16x64_bf16 v[8:11], v[180:183], v[84:91], v192\n" \
  "v_lshlrev_b32_sdwa v64, 4, v216 dst_sel:DWORD dst_unused:UNUSED_PAD src0_sel:DWORD src1_sel:WORD_0\n" \
  "ds_read_b128 v[84:87], v64\n" \
  "v_lshlrev_b32_sdwa v65, 4, v216 dst_sel:DWORD dst_unused:UNUSED_PAD src0_sel:DWORD src1_sel:WORD_1\n" \
  "ds_read_b128 v[88:91], v65\n" \
  "s_add_u32 m0, s58, 0x4000\n" \
  "s_cmp_lt_u32 s35, 0x28000\n" \
  "global_load_lds_dwordx4 v191, s[54:55] sc1\n" \
  "global_load_lds_dwordx4 v191, s[54:55] offset:1024 sc1\n" \
  "s_cselect_b32 s58, s35, s59\n" \
  "s_waitcnt lgkmcnt(10)\n" \
  "v_smfmac_f32_16x16x64_bf16 v[12:15], v[180:183], v[92:99], v192\n" \
  "v_lshlrev_b32_sdwa v66, 4, v217 dst_sel:DWORD dst_unused:UNUSED_PAD src0_sel:DWORD src1_sel:WORD_0\n" \
  "ds_read_b128 v[92:95], v66\n" \
  "v_lshlrev_b32_sdwa v67, 4, v217 dst_sel:DWORD dst_unused:UNUSED_PAD src0_sel:DWORD src1_sel:WORD_1\n" \
  "ds_read_b128 v[96:99], v67\n" \
  "s_waitcnt lgkmcnt(10)\n" \
  "v_smfmac_f32_16x16x64_bf16 v[16:19], v[180:183], v[100:107], v192\n" \
  "v_lshlrev_b32_sdwa v64, 4, v218 dst_sel:DWORD dst_unused:UNUSED_PAD src0_sel:DWORD src1_sel:WORD_0\n" \
  "ds_read_b128 v[100:103], v64\n" \
  "v_lshlrev_b32_sdwa v65, 4, v218 dst_sel:DWORD dst_unused:UNUSED_PAD src0_sel:DWORD src1_sel:WORD_1\n" \
  "ds_read_b128 v[104:107], v65\n" \
  "global_load_dwordx4 v[240:243], v188, s[56:57] sc1\n" \
  "global_load_dwordx4 v[244:247], v188, s[56:57] offset:1024 sc1\n" \
  "s_waitcnt lgkmcnt(10)\n" \
  "v_smfmac_f32_16x16x64_bf16 v[20:23], v[180:183], v[108:115], v192\n" \
  "v_lshlrev_b32_sdwa v66, 4, v219 dst_sel:DWORD dst_unused:UNUSED_PAD src0_sel:DWORD src1_sel:WORD_0\n" \
  "ds_read_b128 v[108:111], v66\n" \
  "v_lshlrev_b32_sdwa v67, 4, v219 dst_sel:DWORD dst_unused:UNUSED_PAD src0_sel:DWORD src1_sel:WORD_1\n" \
  "ds_read_b128 v[112:115], v67\n" \
  "s_waitcnt lgkmcnt(10)\n" \
  "v_smfmac_f32_16x16x64_bf16 v[24:27], v[180:183], v[68:75], v192\n" \
  "v_lshlrev_b32_sdwa v64, 4, v220 dst_sel:DWORD dst_unused:UNUSED_PAD src0_sel:DWORD src1_sel:WORD_0\n" \
  "ds_read_b128 v[68:71], v64\n" \
  "v_lshlrev_b32_sdwa v65, 4, v220 dst_sel:DWORD dst_unused:UNUSED_PAD src0_sel:DWORD src1_sel:WORD_1\n" \
  "ds_read_b128 v[72:75], v65\n" \
  "global_load_dwordx4 v[248:251], v188, s[56:57] offset:2048 sc1\n" \
  "global_load_dwordx4 v[252:255], v188, s[56:57] offset:3072 sc1\n" \
  "s_waitcnt lgkmcnt(10)\n" \
  "v_smfmac_f32_16x16x64_bf16 v[28:31], v[180:183], v[76:83], v192\n" \
  "v_lshlrev_b32_sdwa v66, 4, v221 dst_sel:DWORD dst_unused:UNUSED_PAD src0_sel:DWORD src1_sel:WORD_0\n" \
  "ds_read_b128 v[76:79], v66\n" \
  "v_lshlrev_b32_sdwa v67, 4, v221 dst_sel:DWORD dst_unused:UNUSED_PAD src0_sel:DWORD src1_sel:WORD_1\n" \
  "ds_read_b128 v[80:83], v67\n" \
  "s_waitcnt lgkmcnt(10)\n" \
  "v_smfmac_f32_16x16x64_bf16 v[32:35], v[180:183], v[84:91], v192\n" \
  "v_lshlrev_b32_sdwa v64, 4, v222 dst_sel:DWORD dst_unused:UNUSED_PAD src0_sel:DWORD src1_sel:WORD_0\n" \
  "ds_read_b128 v[84:87], v64\n" \
  "v_lshlrev_b32_sdwa v65, 4, v222 dst_sel:DWORD dst_unused:UNUSED_PAD src0_sel:DWORD src1_sel:WORD_1\n" \
  "ds_read_b128 v[88:91], v65\n" \
  "s_waitcnt lgkmcnt(10)\n" \
  "v_smfmac_f32_16x16x64_bf16 v[36:39], v[180:183], v[92:99], v192\n" \
  "v_lshlrev_b32_sdwa v66, 4, v223 dst_sel:DWORD dst_unused:UNUSED_PAD src0_sel:DWORD src1_sel:WORD_0\n" \
  "ds_read_b128 v[92:95], v66\n" \
  "v_lshlrev_b32_sdwa v67, 4, v223 dst_sel:DWORD dst_unused:UNUSED_PAD src0_sel:DWORD src1_sel:WORD_1\n" \
  "ds_read_b128 v[96:99], v67\n" \
  "s_add_u32 s56, s56, 32768\n" \
  "s_addc_u32 s57, s57, 0\n" \
  "s_add_u32 s54, s54, 64000\n" \
  "s_addc_u32 s55, s55, 0\n" \
  "s_waitcnt lgkmcnt(10)\n" \
  "v_smfmac_f32_16x16x64_bf16 v[40:43], v[180:183], v[100:107], v192\n" \
  "s_waitcnt lgkmcnt(8)\n" \
  "v_smfmac_f32_16x16x64_bf16 v[44:47], v[180:183], v[108:115], v192\n" \
  "s_waitcnt vmcnt(8)\n" \
  "s_cmp_eq_u32 s36, 37\n" \
  "s_cbranch_scc1 LG_chk\n" \
  "LG_ret:\n" \
  "s_sub_u32 s51, s51, 1\n" \
  "s_cmp_lg_u32 s51, 0\n" \
  "s_barrier\n" \
  "s_cbranch_scc1 LG_loop\n" \
  "v_lshlrev_b32_sdwa v64, 4, v224 dst_sel:DWORD dst_unused:UNUSED_PAD src0_sel:DWORD src1_sel:WORD_0\n" \
  "ds_read_b128 v[100:103], v64\n" \
  "v_lshlrev_b32_sdwa v65, 4, v224 dst_sel:DWORD dst_unused:UNUSED_PAD src0_sel:DWORD src1_sel:WORD_1\n" \
  "ds_read_b128 v[104:107], v65\n" \
  "v_lshlrev_b32_sdwa v66, 4, v225 dst_sel:DWORD dst_unused:UNUSED_PAD src0_sel:DWORD src1_sel:WORD_0\n" \
  "ds_read_b128 v[108:111], v66\n" \
  "v_lshlrev_b32_sdwa v67, 4, v225 dst_sel:DWORD dst_unused:UNUSED_PAD src0_sel:DWORD src1_sel:WORD_1\n" \
  "ds_read_b128 v[112:115], v67\n" \
  "s_waitcnt lgkmcnt(10)\n" \
  "v_smfmac_f32_16x16x64_bf16 v[48:51], v[180:183], v[68:75], v192\n" \
  "s_waitcnt lgkmcnt(8)\n" \
  "v_smfmac_f32_16x16x64_bf16 v[52:55], v[180:183], v[76:83], v192\n" \
  "s_waitcnt lgkmcnt(6)\n" \
  "v_smfmac_f32_16x16x64_bf16 v[56:59], v[180:183], v[84:91], v192\n" \
  "s_waitcnt lgkmcnt(4)\n" \
  "v_smfmac_f32_16x16x64_bf16 v[60:63], v[180:183], v[92:99], v192\n" \
  "s_barrier\n" \
  "s_mov_b32 m0, s58\n" \
  "s_add_u32 s35, s58, 0x8000\n" \
  "global_load_lds_dwordx4 v188, s[54:55] sc1\n" \
  "global_load_lds_dwordx4 v188, s[54:55] offset:1024 sc1\n" \
  "v_lshlrev_b32_sdwa v64, 4, v226 dst_sel:DWORD dst_unused:UNUSED_PAD src0_sel:DWORD src1_sel:WORD_0\n" \
  "ds_read_b128 v[68:71], v64\n" \
  "v_lshlrev_b32_sdwa v65, 4, v226 dst_sel:DWORD dst_unused:UNUSED_PAD src0_sel:DWORD src1_sel:WORD_1\n" \
  "ds_read_b128 v[72:75], v65\n" \
  "v_lshlrev_b32_sdwa v66, 4, v227 dst_sel:DWORD dst_unused:UNUSED_PAD src0_sel:DWORD src1_sel:WORD_0\n" \
  "ds_read_b128 v[76:79], v66\n" \
  "v_lshlrev_b32_sdwa v67, 4, v227 dst_sel:DWORD dst_unused:UNUSED_PAD src0_sel:DWORD src1_sel:WORD_1\n" \
  "ds_read_b128 v[80:83], v67\n" \
  "s_add_u32 m0, s58, 0x4000\n" \
  "s_cmp_lt_u32 s35, 0x28000\n" \
  "global_load_lds_dwordx4 v189, s[54:55] sc1\n" \
  "global_load_lds_dwordx4 v189, s[54:55] offset:1024 sc1\n" \
  "s_cselect_b32 s58, s35, s59\n" \
  "v_lshlrev_b32_sdwa v64, 4, v228 dst_sel:DWORD dst_unused:UNUSED_PAD src0_sel:DWORD src1_sel:WORD_0\n" \
  "ds_read_b128 v[84:87], v64\n" \
  "v_lshlrev_b32_sdwa v65, 4, v228 dst_sel:DWORD dst_unused:UNUSED_PAD src0_sel:DWORD src1_sel:WORD_1\n" \
  "ds_read_b128 v[88:91], v65\n" \
  "v_lshlrev_b32_sdwa v66, 4, v229 dst_sel:DWORD dst_unused:UNUSED_PAD src0_sel:DWORD src1_sel:WORD_0\n" \
  "ds_read_b128 v[92:95], v66\n" \
  "v_lshlrev_b32_sdwa v67, 4, v229 dst_sel:DWORD dst_unused:UNUSED_PAD src0_sel:DWORD src1_sel:WORD_1\n" \
  "ds_read_b128 v[96:99], v67\n" \
  "s_waitcnt lgkmcnt(10)\n" \
  "v_smfmac_f32_16x16x64_bf16 v[0:3], v[180:183], v[100:107], v192\n" \
  "v_lshlrev_b32_sdwa v64, 4, v230 dst_sel:DWORD dst_unused:UNUSED_PAD src0_sel:DWORD src1_sel:WORD_0\n" \
  "ds_read_b128 v[100:103], v64\n" \
  "v_lshlrev_b32_sdwa v65, 4, v230 dst_sel:DWORD dst_unused:UNUSED_PAD src0_sel:DWORD src1_sel:WORD_1\n" \
  "ds_read_b128 v[104:107], v65\n" \
  "s_mov_b32 m0, s58\n" \
  "s_add_u32 s35, s58, 0x8000\n" \
  "global_load_lds_dwordx4 v190, s[54:55] sc1\n" \
  "global_load_lds_dwordx4 v190, s[54:55] offset:1024 sc1\n" \
  "s_waitcnt lgkmcnt(10)\n" \
  "v_smfmac_f32_16x16x64_bf16 v[4:7], v[180:183], v[108:115], v192\n" \
  "v_lshlrev_b32_sdwa v66, 4, v231 dst_sel:DWORD dst_unused:UNUSED_PAD src0_sel:DWORD src1_sel:WORD_0\n" \
  "ds_read_b128 v[108:111], v66\n" \
  "v_lshlrev_b32_sdwa v67, 4, v231 dst_sel:DWORD dst_unused:UNUSED_PAD src0_sel:DWORD src1_sel:WORD_1\n" \
  "ds_read_b128 v[112:115], v67\n" \
  "s_waitcnt lgkmcnt(10)\n" \
  "v_smfmac_f32_16x16x64_bf16 v[8:11], v[180:183], v[68:75], v192\n" \
  "v_lshlrev_b32_sdwa v64, 4, v232 dst_sel:DWORD dst_unused:UNUSED_PAD src0_sel:DWORD src1_sel:WORD_0\n" \
  "ds_read_b128 v[68:71], v64\n" \
  "v_lshlrev_b32_sdwa v65, 4, v232 dst_sel:DWORD dst_unused:UNUSED_PAD src0_sel:DWORD src1_sel:WORD_1\n" \
  "ds_read_b128 v[72:75], v65\n" \
  "s_add_u32 m0, s58, 0x4000\n" \
  "s_cmp_lt_u32 s35, 0x28000\n" \
  "global_load_lds_dwordx4 v191, s[54:55] sc1\n" \
  "global_load_lds_dwordx4 v191, s[54:55] offset:1024 sc1\n" \
  "s_cselect_b32 s58, s35, s59\n" \
  "s_waitcnt lgkmcnt(10)\n" \
  "v_smfmac_f32_16x16x64_bf16 v[12:15], v[180:183], v[76:83], v192\n" \
  "v_lshlrev_b32_sdwa v66, 4, v233 dst_sel:DWORD dst_unused:UNUSED_PAD src0_sel:DWORD src1_sel:WORD_0\n" \
  "ds_read_b128 v[76:79], v66\n" \
  "v_lshlrev_b32_sdwa v67, 4, v233 dst_sel:DWORD dst_unused:UNUSED_PAD src0_sel:DWORD src1_sel:WORD_1\n" \
  "ds_read_b128 v[80:83], v67\n" \
  "s_waitcnt lgkmcnt(10)\n" \
  "v_smfmac_f32_16x16x64_bf16 v[16:19], v[180:183], v[84:91], v192\n" \
  "v_lshlrev_b32_sdwa v64, 4, v234 dst_sel:DWORD dst_unused:UNUSED_PAD src0_sel:DWORD src1_sel:WORD_0\n" \
  "ds_read_b128 v[84:87], v64\n" \
  "v_lshlrev_b32_sdwa v65, 4, v234 dst_sel:DWORD dst_unused:UNUSED_PAD src0_sel:DWORD src1_sel:WORD_1\n" \
  "ds_read_b128 v[88:91], v65\n" \
  "global_load_dwordx4 v[208:211], v188, s[56:57] sc1\n" \
  "global_load_dwordx4 v[212:215], v188, s[56:57] offset:1024 sc1\n" \
  "s_waitcnt lgkmcnt(10)\n" \
  "v_smfmac_f32_16x16x64_bf16 v[20:23], v[180:183], v[92:99], v192\n" \
  "v_lshlrev_b32_sdwa v66, 4, v235 dst_sel:DWORD dst_unused:UNUSED_PAD src0_sel:DWORD src1_sel:WORD_0\n" \
  "ds_read_b128 v[92:95], v66\n" \
  "v_lshlrev_b32_sdwa v67, 4, v235 dst_sel:DWORD dst_unused:UNUSED_PAD src0_sel:DWORD src1_sel:WORD_1\n" \
  "ds_read_b128 v[96:99], v67\n" \
  "s_waitcnt lgkmcnt(10)\n" \
  "v_smfmac_f32_16x16x64_bf16 v[24:27], v[180:183], v[100:107], v192\n" \
  "v_lshlrev_b32_sdwa v64, 4, v236 dst_sel:DWORD dst_unused:UNUSED_PAD src0_sel:DWORD src1_sel:WORD_0\n" \
  "ds_read_b128 v[100:103], v64\n" \
  "v_lshlrev_b32_sdwa v65, 4, v236 dst_sel:DWORD dst_unused:UNUSED_PAD src0_sel:DWORD src1_sel:WORD_1\n" \
  "ds_read_b128 v[104:107], v65\n" \
  "global_load_dwordx4 v[216:219], v188, s[56:57] offset:2048 sc1\n" \
  "global_load_dwordx4 v[220:223], v188, s[56:57] offset:3072 sc1\n" \
  "s_waitcnt lgkmcnt(10)\n" \
  "v_smfmac_f32_16x16x64_bf16 v[28:31], v[180:183], v[108:115], v192\n" \
  "v_lshlrev_b32_sdwa v66, 4, v237 dst_sel:DWORD dst_unused:UNUSED_PAD src0_sel:DWORD src1_sel:WORD_0\n" \
  "ds_read_b128 v[108:111], v66\n" \
  "v_lshlrev_b32_sdwa v67, 4, v237 dst_sel:DWORD dst_unused:UNUSED_PAD src0_sel:DWORD src1_sel:WORD_1\n" \
  "ds_read_b128 v[112:115], v67\n" \
  "s_waitcnt lgkmcnt(10)\n" \
  "v_smfmac_f32_16x16x64_bf16 v[32:35], v[180:183], v[68:75], v192\n" \
  "v_lshlrev_b32_sdwa v64, 4, v238 dst_sel:DWORD dst_unused:UNUSED_PAD src0_sel:DWORD src1_sel:WORD_0\n" \
  "ds_read_b128 v[68:71], v64\n" \
  "v_lshlrev_b32_sdwa v65, 4, v238 dst_sel:DWORD dst_unused:UNUSED_PAD src0_sel:DWORD src1_sel:WORD_1\n" \
  "ds_read_b128 v[72:75], v65\n" \
  "s_waitcnt lgkmcnt(10)\n" \
  "v_smfmac_f32_16x16x64_bf16 v[36:39], v[180:183], v[76:83], v192\n" \
  "v_lshlrev_b32_sdwa v66, 4, v239 dst_sel:DWORD dst_unused:UNUSED_PAD src0_sel:DWORD src1_sel:WORD_0\n" \
  "ds_read_b128 v[76:79], v66\n" \
  "v_lshlrev_b32_sdwa v67, 4, v239 dst_sel:DWORD dst_unused:UNUSED_PAD src0_sel:DWORD src1_sel:WORD_1\n" \
  "ds_read_b128 v[80:83], v67\n" \
  "s_add_u32 s56, s56, 32768\n" \
  "s_addc_u32 s57, s57, 0\n" \
  "s_add_u32 s54, s54, 64000\n" \
  "s_addc_u32 s55, s55, 0\n" \
  "s_waitcnt lgkmcnt(10)\n" \
  "v_smfmac_f32_16x16x64_bf16 v[40:43], v[180:183], v[84:91], v192\n" \
  "s_waitcnt lgkmcnt(8)\n" \
  "v_smfmac_f32_16x16x64_bf16 v[44:47], v[180:183], v[92:99], v192\n" \
  "s_waitcnt vmcnt(8)\n" \
  "s_barrier\n" \
  "v_lshlrev_b32_sdwa v64, 4, v240 dst_sel:DWORD dst_unused:UNUSED_PAD src0_sel:DWORD src1_sel:WORD_0\n" \
  "ds_read_b128 v[84:87], v64\n" \
  "v_lshlrev_b32_sdwa v65, 4, v240 dst_sel:DWORD dst_unused:UNUSED_PAD src0_sel:DWORD src1_sel:WORD_1\n" \
  "ds_read_b128 v[88:91], v65\n" \
  "v_lshlrev_b32_sdwa v66, 4, v241 dst_sel:DWORD dst_unused:UNUSED_PAD src0_sel:DWORD src1_sel:WORD_0\n" \
  "ds_read_b128 v[92:95], v66\n" \
  "v_lshlrev_b32_sdwa v67, 4, v241 dst_sel:DWORD dst_unused:UNUSED_PAD src0_sel:DWORD src1_sel:WORD_1\n" \
  "ds_read_b128 v[96:99], v67\n" \
  "s_waitcnt lgkmcnt(10)\n" \
  "v_smfmac_f32_16x16x64_bf16 v[48:51], v[180:183], v[100:107], v192\n" \
  "s_waitcnt lgkmcnt(8)\n" \
  "v_smfmac_f32_16x16x64_bf16 v[52:55], v[180:183], v[108:115], v192\n" \
  "s_waitcnt lgkmcnt(6)\n" \
  "v_smfmac_f32_16x16x64_bf16 v[56:59], v[180:183], v[68:75], v192\n" \
  "s_waitcnt lgkmcnt(4)\n" \
  "v_smfmac_f32_16x16x64_bf16 v[60:63], v[180:183], v[76:83], v192\n" \
  "s_barrier\n" \
  "s_mov_b32 m0, s58\n" \
  "s_add_u32 s35, s58, 0x8000\n" \
  "global_load_lds_dwordx4 v188, s[54:55] sc1\n" \
  "global_load_lds_dwordx4 v188, s[54:55] offset:1024 sc1\n" \
  "s_add_u32 m0, s58, 0x4000\n" \
  "s_cmp_lt_u32 s35, 0x28000\n" \
  "global_load_lds_dwordx4 v189, s[54:55] sc1\n" \
  "global_load_lds_dwordx4 v189, s[54:55] offset:1024 sc1\n" \
  "s_cselect_b32 s58, s35, s59\n" \
  "v_lshlrev_b32 v120, 16, v206\n" \
  "v_and_b32 v121, 0xffff0000, v206\n" \
  "v_lshlrev_b32 v122, 16, v207\n" \
  "v_and_b32 v123, 0xffff0000, v207\n" \
  "v_mov_b32 v124, 0xc0135761\n" \
  "v_mov_b32 v125, 0xc0135761\n" \
  "s_mov_b32 s52, 0xbdd2d3e8\n" \
  "s_mov_b32 s53, s52\n" \
  "s_lshl2_add_u32 s35, s33, s34\n" \
  "s_lshl_b32 s35, s35, 14\n" \
  "s_add_u32 s35, s35, s59\n" \
  "s_add_u32 s35, s35, 105676800\n" \
  "s_add_u32 s40, s24, s35\n" \
  "s_addc_u32 s41, s25, 0\n" \
  "v_lshlrev_b32_sdwa v64, 4, v242 dst_sel:DWORD dst_unused:UNUSED_PAD src0_sel:DWORD src1_sel:WORD_0\n" \
  "ds_read_b128 v[100:103], v64\n" \
  "v_lshlrev_b32_sdwa v65, 4, v242 dst_sel:DWORD dst_unused:UNUSED_PAD src0_sel:DWORD src1_sel:WORD_1\n" \
  "ds_read_b128 v[104:107], v65\n" \
  "v_lshlrev_b32_sdwa v66, 4, v243 dst_sel:DWORD dst_unused:UNUSED_PAD src0_sel:DWORD src1_sel:WORD_0\n" \
  "ds_read_b128 v[108:111], v66\n" \
  "v_lshlrev_b32_sdwa v67, 4, v243 dst_sel:DWORD dst_unused:UNUSED_PAD src0_sel:DWORD src1_sel:WORD_1\n" \
  "ds_read_b128 v[112:115], v67\n" \
  "v_lshlrev_b32_sdwa v64, 4, v244 dst_sel:DWORD dst_unused:UNUSED_PAD src0_sel:DWORD src1_sel:WORD_0\n" \
  "ds_read_b128 v[68:71], v64\n" \
  "v_lshlrev_b32_sdwa v65, 4, v244 dst_sel:DWORD dst_unused:UNUSED_PAD src0_sel:DWORD src1_sel:WORD_1\n" \
  "ds_read_b128 v[72:75], v65\n" \
  "v_lshlrev_b32_sdwa v66, 4, v245 dst_sel:DWORD dst_unused:UNUSED_PAD src0_sel:DWORD src1_sel:WORD_0\n" \
  "ds_read_b128 v[76:79], v66\n" \
  "v_lshlrev_b32_sdwa v67, 4, v245 dst_sel:DWORD dst_unused:UNUSED_PAD src0_sel:DWORD src1_sel:WORD_1\n" \
  "ds_read_b128 v[80:83], v67\n" \
  "s_waitcnt lgkmcnt(10)\n" \
  "v_smfmac_f32_16x16x64_bf16 v[0:3], v[180:183], v[84:91], v192\n" \
  "v_lshlrev_b32_sdwa v64, 4, v246 dst_sel:DWORD dst_unused:UNUSED_PAD src0_sel:DWORD src1_sel:WORD_0\n" \
  "ds_read_b128 v[84:87], v64\n" \
  "v_lshlrev_b32_sdwa v65, 4, v246 dst_sel:DWORD dst_unused:UNUSED_PAD src0_sel:DWORD src1_sel:WORD_1\n" \
  "ds_read_b128 v[88:91], v65\n" \
  "s_waitcnt lgkmcnt(10)\n" \
  "v_smfmac_f32_16x16x64_bf16 v[4:7], v[180:183], v[92:99], v192\n" \
  "v_lshlrev_b32_sdwa v66, 4, v247 dst_sel:DWORD dst_unused:UNUSED_PAD src0_sel:DWORD src1_sel:WORD_0\n" \
  "ds_read_b128 v[92:95], v66\n" \
  "v_lshlrev_b32_sdwa v67, 4, v247 dst_sel:DWORD dst_unused:UNUSED_PAD src0_sel:DWORD src1_sel:WORD_1\n" \
  "ds_read_b128 v[96:99], v67\n" \
  "s_waitcnt lgkmcnt(10)\n" \
  "v_smfmac_f32_16x16x64_bf16 v[8:11], v[180:183], v[100:107], v192\n" \
  "v_lshlrev_b32_sdwa v64, 4, v248 dst_sel:DWORD dst_unused:UNUSED_PAD src0_sel:DWORD src1_sel:WORD_0\n" \
  "ds_read_b128 v[100:103], v64\n" \
  "v_lshlrev_b32_sdwa v65, 4, v248 dst_sel:DWORD dst_unused:UNUSED_PAD src0_sel:DWORD src1_sel:WORD_1\n" \
  "ds_read_b128 v[104:107], v65\n" \
  "s_waitcnt lgkmcnt(10)\n" \
  "v_smfmac_f32_16x16x64_bf16 v[12:15], v[180:183], v[108:115], v192\n" \
  "v_lshlrev_b32_sdwa v66, 4, v249 dst_sel:DWORD dst_unused:UNUSED_PAD src0_sel:DWORD src1_sel:WORD_0\n" \
  "ds_read_b128 v[108:111], v66\n" \
  "v_lshlrev_b32_sdwa v67, 4, v249 dst_sel:DWORD dst_unused:UNUSED_PAD src0_sel:DWORD src1_sel:WORD_1\n" \
  "ds_read_b128 v[112:115], v67\n" \
  "s_waitcnt vmcnt(0)\n" \
  "s_barrier\n" \
  "s_waitcnt lgkmcnt(10)\n" \
  "v_smfmac_f32_16x16x64_bf16 v[16:19], v[180:183], v[68:75], v192\n" \
  "v_lshlrev_b32_sdwa v64, 4, v208 dst_sel:DWORD dst_unused:UNUSED_PAD src0_sel:DWORD src1_sel:WORD_0\n" \
  "ds_read_b128 v[68:71], v64\n" \
  "v_lshlrev_b32_sdwa v65, 4, v208 dst_sel:DWORD dst_unused:UNUSED_PAD src0_sel:DWORD src1_sel:WORD_1\n" \
  "ds_read_b128 v[72:75], v65\n" \
  "s_waitcnt lgkmcnt(10)\n" \
  "v_smfmac_f32_16x16x64_bf16 v[20:23], v[180:183], v[76:83], v192\n" \
  "v_lshlrev_b32_sdwa v66, 4, v209 dst_sel:DWORD dst_unused:UNUSED_PAD src0_sel:DWORD src1_sel:WORD_0\n" \
  "ds_read_b128 v[76:79], v66\n" \
  "v_lshlrev_b32_sdwa v67, 4, v209 dst_sel:DWORD dst_unused:UNUSED_PAD src0_sel:DWORD src1_sel:WORD_1\n" \
  "ds_read_b128 v[80:83], v67\n" \
  "s_waitcnt lgkmcnt(10)\n" \
  "v_smfmac_f32_16x16x64_bf16 v[24:27], v[180:183], v[84:91], v192\n" \
  "v_lshlrev_b32_sdwa v64, 4, v210 dst_sel:DWORD dst_unused:UNUSED_PAD src0_sel:DWORD src1_sel:WORD_0\n" \
  "ds_read_b128 v[84:87], v64\n" \
  "v_lshlrev_b32_sdwa v65, 4, v210 dst_sel:DWORD dst_unused:UNUSED_PAD src0_sel:DWORD src1_sel:WORD_1\n" \
  "ds_read_b128 v[88:91], v65\n" \
  "s_waitcnt lgkmcnt(10)\n" \
  "v_smfmac_f32_16x16x64_bf16 v[28:31], v[180:183], v[92:99], v192\n" \
  "v_lshlrev_b32_sdwa v66, 4, v211 dst_sel:DWORD dst_unused:UNUSED_PAD src0_sel:DWORD src1_sel:WORD_0\n" \
  "ds_read_b128 v[92:95], v66\n" \
  "v_lshlrev_b32_sdwa v67, 4, v211 dst_sel:DWORD dst_unused:UNUSED_PAD src0_sel:DWORD src1_sel:WORD_1\n" \
  "ds_read_b128 v[96:99], v67\n" \
  "s_waitcnt lgkmcnt(10)\n" \
  "v_smfmac_f32_16x16x64_bf16 v[32:35], v[180:183], v[100:107], v192\n" \
  "v_lshlrev_b32_sdwa v64, 4, v212 dst_sel:DWORD dst_unused:UNUSED_PAD src0_sel:DWORD src1_sel:WORD_0\n" \
  "ds_read_b128 v[100:103], v64\n" \
  "v_lshlrev_b32_sdwa v65, 4, v212 dst_sel:DWORD dst_unused:UNUSED_PAD src0_sel:DWORD src1_sel:WORD_1\n" \
  "ds_read_b128 v[104:107], v65\n" \
  "s_waitcnt lgkmcnt(10)\n" \
  "v_smfmac_f32_16x16x64_bf16 v[36:39], v[180:183], v[108:115], v192\n" \
  "v_lshlrev_b32_sdwa v66, 4, v213 dst_sel:DWORD dst_unused:UNUSED_PAD src0_sel:DWORD src1_sel:WORD_0\n" \
  "ds_read_b128 v[108:111], v66\n" \
  "v_lshlrev_b32_sdwa v67, 4, v213 dst_sel:DWORD dst_unused:UNUSED_PAD src0_sel:DWORD src1_sel:WORD_1\n" \
  "ds_read_b128 v[112:115], v67\n" \
  "s_waitcnt lgkmcnt(10)\n" \
  "v_smfmac_f32_16x16x64_bf16 v[0:3], v[180:183], v[68:75], v192\n" \
  "v_lshlrev_b32_sdwa v64, 4, v214 dst_sel:DWORD dst_unused:UNUSED_PAD src0_sel:DWORD src1_sel:WORD_0\n" \
  "ds_read_b128 v[68:71], v64\n" \
  "v_lshlrev_b32_sdwa v65, 4, v214 dst_sel:DWORD dst_unused:UNUSED_PAD src0_sel:DWORD src1_sel:WORD_1\n" \
  "ds_read_b128 v[72:75], v65\n" \
  "s_waitcnt lgkmcnt(10)\n" \
  "v_smfmac_f32_16x16x64_bf16 v[4:7], v[180:183], v[76:83], v192\n" \
  "v_lshlrev_b32_sdwa v66, 4, v215 dst_sel:DWORD dst_unused:UNUSED_PAD src0_sel:DWORD src1_sel:WORD_0\n" \
  "ds_read_b128 v[76:79], v66\n" \
  "v_lshlrev_b32_sdwa v67, 4, v215 dst_sel:DWORD dst_unused:UNUSED_PAD src0_sel:DWORD src1_sel:WORD_1\n" \
  "ds_read_b128 v[80:83], v67\n" \
  "v_pk_mul_f32 v[128:129], v[0:1], v[0:1]\n" \
  "v_pk_mul_f32 v[130:131], v[2:3], v[2:3]\n" \
  "v_pk_fma_f32 v[128:129], v[128:129], s[52:53], v[124:125]\n" \
  "v_pk_fma_f32 v[130:131], v[130:131], s[52:53], v[124:125]\n" \
  "v_pk_mul_f32 v[128:129], v[128:129], v[0:1]\n" \
  "v_pk_mul_f32 v[130:131], v[130:131], v[2:3]\n" \
  "v_exp_f32 v128, v128\n" \
  "v_exp_f32 v129, v129\n" \
  "v_pk_mul_f32 v[0:1], v[0:1], v[120:121]\n" \
  "v_exp_f32 v130, v130\n" \
  "v_exp_f32 v131, v131\n" \
  "v_pk_mul_f32 v[2:3], v[2:3], v[122:123]\n" \
  "v_pk_add_f32 v[128:129], v[128:129], 1.0 op_sel_hi:[1,0]\n" \
  "v_pk_add_f32 v[130:131], v[130:131], 1.0 op_sel_hi:[1,0]\n" \
  "v_rcp_f32 v128, v128\n" \
  "v_rcp_f32 v129, v129\n" \
  "s_waitcnt lgkmcnt(10)\n" \
  "v_smfmac_f32_16x16x64_bf16 v[8:11], v[180:183], v[84:91], v192\n" \
  "v_lshlrev_b32_sdwa v64, 4, v216 dst_sel:DWORD dst_unused:UNUSED_PAD src0_sel:DWORD src1_sel:WORD_0\n" \
  "ds_read_b128 v[84:87], v64\n" \
  "v_lshlrev_b32_sdwa v65, 4, v216 dst_sel:DWORD dst_unused:UNUSED_PAD src0_sel:DWORD src1_sel:WORD_1\n" \
  "ds_read_b128 v[88:91], v65\n" \
  "v_rcp_f32 v130, v130\n" \
  "v_rcp_f32 v131, v131\n" \
  "v_pk_mul_f32 v[0:1], v[0:1], v[128:129]\n" \
  "v_pk_fma_f32 v[0:1], v[2:3], v[130:131], v[0:1]\n" \
  "v_add_f32 v0, v0, v1\n" \
  "v_pk_mul_f32 v[132:133], v[4:5], v[4:5]\n" \
  "v_pk_mul_f32 v[134:135], v[6:7], v[6:7]\n" \
  "v_pk_fma_f32 v[132:133], v[132:133], s[52:53], v[124:125]\n" \
  "v_pk_fma_f32 v[134:135], v[134:135], s[52:53], v[124:125]\n" \
  "v_pk_mul_f32 v[132:133], v[132:133], v[4:5]\n" \
  "v_pk_mul_f32 v[134:135], v[134:135], v[6:7]\n" \
  "v_exp_f32 v132, v132\n" \
  "v_exp_f32 v133, v133\n" \
  "v_pk_mul_f32 v[4:5], v[4:5], v[120:121]\n" \
  "v_exp_f32 v134, v134\n" \
  "v_exp_f32 v135, v135\n" \
  "s_waitcnt lgkmcnt(10)\n" \
  "v_smfmac_f32_16x16x64_bf16 v[12:15], v[180:183], v[92:99], v192\n" \
  "v_lshlrev_b32_sdwa v66, 4, v217 dst_sel:DWORD dst_unused:UNUSED_PAD src0_sel:DWORD src1_sel:WORD_0\n" \
  "ds_read_b128 v[92:95], v66\n" \
  "v_lshlrev_b32_sdwa v67, 4, v217 dst_sel:DWORD dst_unused:UNUSED_PAD src0_sel:DWORD src1_sel:WORD_1\n" \
  "ds_read_b128 v[96:99], v67\n" \
  "v_pk_mul_f32 v[6:7], v[6:7], v[122:123]\n" \
  "v_pk_add_f32 v[132:133], v[132:133], 1.0 op_sel_hi:[1,0]\n" \
  "v_pk_add_f32 v[134:135], v[134:135], 1.0 op_sel_hi:[1,0]\n" \
  "v_rcp_f32 v132, v132\n" \
  "v_rcp_f32 v133, v133\n" \
  "v_rcp_f32 v134, v134\n" \
  "v_rcp_f32 v135, v135\n" \
  "v_pk_mul_f32 v[4:5], v[4:5], v[132:133]\n" \
  "v_pk_fma_f32 v[4:5], v[6:7], v[134:135], v[4:5]\n" \
  "v_add_f32 v4, v4, v5\n" \
  "v_pk_mul_f32 v[136:137], v[8:9], v[8:9]\n" \
  "v_pk_mul_f32 v[138:139], v[10:11], v[10:11]\n" \
  "v_pk_fma_f32 v[136:137], v[136:137], s[52:53], v[124:125]\n" \
  "v_pk_fma_f32 v[138:139], v[138:139], s[52:53], v[124:125]\n" \
  "v_pk_mul_f32 v[136:137], v[136:137], v[8:9]\n" \
  "v_pk_mul_f32 v[138:139], v[138:139], v[10:11]\n" \
  "s_waitcnt lgkmcnt(10)\n" \
  "v_smfmac_f32_16x16x64_bf16 v[16:19], v[180:183], v[100:107], v192\n" \
  "v_lshlrev_b32_sdwa v64, 4, v250 dst_sel:DWORD dst_unused:UNUSED_PAD src0_sel:DWORD src1_sel:WORD_0\n" \
  "ds_read_b128 v[100:103], v64\n" \
  "v_lshlrev_b32_sdwa v65, 4, v250 dst_sel:DWORD dst_unused:UNUSED_PAD src0_sel:DWORD src1_sel:WORD_1\n" \
  "ds_read_b128 v[104:107], v65\n" \
  "v_exp_f32 v136, v136\n" \
  "v_exp_f32 v137, v137\n" \
  "v_pk_mul_f32 v[8:9], v[8:9], v[120:121]\n" \
  "v_exp_f32 v138, v138\n" \
  "v_exp_f32 v139, v139\n" \
  "v_pk_mul_f32 v[10:11], v[10:11], v[122:123]\n" \
  "v_pk_add_f32 v[136:137], v[136:137], 1.0 op_sel_hi:[1,0]\n" \
  "v_pk_add_f32 v[138:139], v[138:139], 1.0 op_sel_hi:[1,0]\n" \
  "v_rcp_f32 v136, v136\n" \
  "v_rcp_f32 v137, v137\n" \
  "v_rcp_f32 v138, v138\n" \
  "v_rcp_f32 v139, v139\n" \
  "v_pk_mul_f32 v[8:9], v[8:9], v[136:137]\n" \
  "v_pk_fma_f32 v[8:9], v[10:11], v[138:139], v[8:9]\n" \
  "v_add_f32 v8, v8, v9\n" \
  "v_pk_mul_f32 v[140:141], v[12:13], v[12:13]\n" \
  "s_waitcnt lgkmcnt(10)\n" \
  "v_smfmac_f32_16x16x64_bf16 v[20:23], v[180:183], v[108:115], v192\n" \
  "v_lshlrev_b32_sdwa v66, 4, v218 dst_sel:DWORD dst_unused:UNUSED_PAD src0_sel:DWORD src1_sel:WORD_0\n" \
  "ds_read_b128 v[108:111], v66\n" \
  "v_lshlrev_b32_sdwa v67, 4, v218 dst_sel:DWORD dst_unused:UNUSED_PAD src0_sel:DWORD src1_sel:WORD_1\n" \
  "ds_read_b128 v[112:115], v67\n" \
  "v_pk_mul_f32 v[142:143], v[14:15], v[14:15]\n" \
  "v_pk_fma_f32 v[140:141], v[140:141], s[52:53], v[124:125]\n" \
  "v_pk_fma_f32 v[142:143], v[142:143], s[52:53], v[124:125]\n" \
  "v_pk_mul_f32 v[140:141], v[140:141], v[12:13]\n" \
  "v_pk_mul_f32 v[142:143], v[142:143], v[14:15]\n" \
  "v_exp_f32 v140, v140\n" \
  "v_exp_f32 v141, v141\n" \
  "v_pk_mul_f32 v[12:13], v[12:13], v[120:121]\n" \
  "v_exp_f32 v142, v142\n" \
  "v_exp_f32 v143, v143\n" \
  "v_pk_mul_f32 v[14:15], v[14:15], v[122:123]\n" \
  "v_pk_add_f32 v[140:141], v[140:141], 1.0 op_sel_hi:[1,0]\n" \
  "v_pk_add_f32 v[142:143], v[142:143], 1.0 op_sel_hi:[1,0]\n" \
  "v_rcp_f32 v140, v140\n" \
  "v_rcp_f32 v141, v141\n" \
  "v_rcp_f32 v142, v142\n" \
  "s_waitcnt lgkmcnt(10)\n" \
  "v_smfmac_f32_16x16x64_bf16 v[24:27], v[180:183], v[68:75], v192\n" \
  "v_lshlrev_b32_sdwa v64, 4, v251 dst_sel:DWORD dst_unused:UNUSED_PAD src0_sel:DWORD src1_sel:WORD_0\n" \
  "ds_read_b128 v[68:71], v64\n" \
  "v_lshlrev_b32_sdwa v65, 4, v251 dst_sel:DWORD dst_unused:UNUSED_PAD src0_sel:DWORD src1_sel:WORD_1\n" \
  "ds_read_b128 v[72:75], v65\n" \
  "v_rcp_f32 v143, v143\n" \
  "v_pk_mul_f32 v[12:13], v[12:13], v[140:141]\n" \
  "v_pk_fma_f32 v[12:13], v[14:15], v[142:143], v[12:13]\n" \
  "v_add_f32 v12, v12, v13\n" \
  "v_pk_mul_f32 v[128:129], v[16:17], v[16:17]\n" \
  "v_pk_mul_f32 v[130:131], v[18:19], v[18:19]\n" \
  "v_pk_fma_f32 v[128:129], v[128:129], s[52:53], v[124:125]\n" \
  "v_pk_fma_f32 v[130:131], v[130:131], s[52:53], v[124:125]\n" \
  "v_pk_mul_f32 v[128:129], v[128:129], v[16:17]\n" \
  "v_pk_mul_f32 v[130:131], v[130:131], v[18:19]\n" \
  "v_exp_f32 v128, v128\n" \
  "v_exp_f32 v129, v129\n" \
  "v_pk_mul_f32 v[16:17], v[16:17], v[120:121]\n" \
  "v_exp_f32 v130, v130\n" \
  "v_exp_f32 v131, v131\n" \
  "v_pk_mul_f32 v[18:19], v[18:19], v[122:123]\n" \
  "s_waitcnt lgkmcnt(10)\n" \
  "v_smfmac_f32_16x16x64_bf16 v[28:31], v[180:183], v[76:83], v192\n" \
  "v_lshlrev_b32_sdwa v66, 4, v219 dst_sel:DWORD dst_unused:UNUSED_PAD src0_sel:DWORD src1_sel:WORD_0\n" \
  "ds_read_b128 v[76:79], v66\n" \
  "v_lshlrev_b32_sdwa v67, 4, v219 dst_sel:DWORD dst_unused:UNUSED_PAD src0_sel:DWORD src1_sel:WORD_1\n" \
  "ds_read_b128 v[80:83], v67\n" \
  "v_pk_add_f32 v[128:129], v[128:129], 1.0 op_sel_hi:[1,0]\n" \
  "v_pk_add_f32 v[130:131], v[130:131], 1.0 op_sel_hi:[1,0]\n" \
  "v_rcp_f32 v128, v128\n" \
  "v_rcp_f32 v129, v129\n" \
  "v_rcp_f32 v130, v130\n" \
  "v_rcp_f32 v131, v131\n" \
  "v_pk_mul_f32 v[16:17], v[16:17], v[128:129]\n" \
  "v_pk_fma_f32 v[16:17], v[18:19], v[130:131], v[16:17]\n" \
  "v_add_f32 v16, v16, v17\n" \
  "v_pk_mul_f32 v[132:133], v[20:21], v[20:21]\n" \
  "v_pk_mul_f32 v[134:135], v[22:23], v[22:23]\n" \
  "v_pk_fma_f32 v[132:133], v[132:133], s[52:53], v[124:125]\n" \
  "v_pk_fma_f32 v[134:135], v[134:135], s[52:53], v[124:125]\n" \
  "v_pk_mul_f32 v[132:133], v[132:133], v[20:21]\n" \
  "v_pk_mul_f32 v[134:135], v[134:135], v[22:23]\n" \
  "v_exp_f32 v132, v132\n" \
  "s_waitcnt lgkmcnt(10)\n" \
  "v_smfmac_f32_16x16x64_bf16 v[32:35], v[180:183], v[84:91], v192\n" \
  "v_lshlrev_b32_sdwa v64, 4, v252 dst_sel:DWORD dst_unused:UNUSED_PAD src0_sel:DWORD src1_sel:WORD_0\n" \
  "ds_read_b128 v[84:87], v64\n" \
  "v_lshlrev_b32_sdwa v65, 4, v252 dst_sel:DWORD dst_unused:UNUSED_PAD src0_sel:DWORD src1_sel:WORD_1\n" \
  "ds_read_b128 v[88:91], v65\n" \
  "v_exp_f32 v133, v133\n" \
  "v_pk_mul_f32 v[20:21], v[20:21], v[120:121]\n" \
  "v_exp_f32 v134, v134\n" \
  "v_exp_f32 v135, v135\n" \
  "v_pk_mul_f32 v[22:23], v[22:23], v[122:123]\n" \
  "v_pk_add_f32 v[132:133], v[132:133], 1.0 op_sel_hi:[1,0]\n" \
  "v_pk_add_f32 v[134:135], v[134:135], 1.0 op_sel_hi:[1,0]\n" \
  "v_rcp_f32 v132, v132\n" \
  "v_rcp_f32 v133, v133\n" \
  "v_rcp_f32 v134, v134\n" \
  "v_rcp_f32 v135, v135\n" \
  "v_pk_mul_f32 v[20:21], v[20:21], v[132:133]\n" \
  "v_pk_fma_f32 v[20:21], v[22:23], v[134:135], v[20:21]\n" \
  "v_add_f32 v20, v20, v21\n" \
  "v_pk_mul_f32 v[136:137], v[24:25], v[24:25]\n" \
  "v_pk_mul_f32 v[138:139], v[26:27], v[26:27]\n" \
  "s_waitcnt lgkmcnt(10)\n" \
  "v_smfmac_f32_16x16x64_bf16 v[36:39], v[180:183], v[92:99], v192\n" \
  "v_lshlrev_b32_sdwa v66, 4, v220 dst_sel:DWORD dst_unused:UNUSED_PAD src0_sel:DWORD src1_sel:WORD_0\n" \
  "ds_read_b128 v[92:95], v66\n" \
  "v_lshlrev_b32_sdwa v67, 4, v220 dst_sel:DWORD dst_unused:UNUSED_PAD src0_sel:DWORD src1_sel:WORD_1\n" \
  "ds_read_b128 v[96:99], v67\n" \
  "v_pk_fma_f32 v[136:137], v[136:137], s[52:53], v[124:125]\n" \
  "v_pk_fma_f32 v[138:139], v[138:139], s[52:53], v[124:125]\n" \
  "v_pk_mul_f32 v[136:137], v[136:137], v[24:25]\n" \
  "v_pk_mul_f32 v[138:139], v[138:139], v[26:27]\n" \
  "v_exp_f32 v136, v136\n" \
  "v_exp_f32 v137, v137\n" \
  "v_pk_mul_f32 v[24:25], v[24:25], v[120:121]\n" \
  "v_exp_f32 v138, v138\n" \
  "v_exp_f32 v139, v139\n" \
  "v_pk_mul_f32 v[26:27], v[26:27], v[122:123]\n" \
  "v_pk_add_f32 v[136:137], v[136:137], 1.0 op_sel_hi:[1,0]\n" \
  "v_pk_add_f32 v[138:139], v[138:139], 1.0 op_sel_hi:[1,0]\n" \
  "v_rcp_f32 v136, v136\n" \
  "v_rcp_f32 v137, v137\n" \
  "v_rcp_f32 v138, v138\n" \
  "v_rcp_f32 v139, v139\n" \
  "s_waitcnt lgkmcnt(10)\n" \
  "v_smfmac_f32_16x16x64_bf16 v[40:43], v[180:183], v[100:107], v192\n" \
  "v_lshlrev_b32_sdwa v64, 4, v253 dst_sel:DWORD dst_unused:UNUSED_PAD src0_sel:DWORD src1_sel:WORD_0\n" \
  "ds_read_b128 v[100:103], v64\n" \
  "v_lshlrev_b32_sdwa v65, 4, v253 dst_sel:DWORD dst_unused:UNUSED_PAD src0_sel:DWORD src1_sel:WORD_1\n" \
  "ds_read_b128 v[104:107], v65\n" \
  "v_pk_mul_f32 v[24:25], v[24:25], v[136:137]\n" \
  "v_pk_fma_f32 v[24:25], v[26:27], v[138:139], v[24:25]\n" \
  "v_add_f32 v24, v24, v25\n" \
  "v_pk_mul_f32 v[140:141], v[28:29], v[28:29]\n" \
  "v_pk_mul_f32 v[142:143], v[30:31], v[30:31]\n" \
  "v_pk_fma_f32 v[140:141], v[140:141], s[52:53], v[124:125]\n" \
  "v_pk_fma_f32 v[142:143], v[142:143], s[52:53], v[124:125]\n" \
  "v_pk_mul_f32 v[140:141], v[140:141], v[28:29]\n" \
  "v_pk_mul_f32 v[142:143], v[142:143], v[30:31]\n" \
  "v_exp_f32 v140, v140\n" \
  "v_exp_f32 v141, v141\n" \
  "v_pk_mul_f32 v[28:29], v[28:29], v[120:121]\n" \
  "v_exp_f32 v142, v142\n" \
  "v_exp_f32 v143, v143\n" \
  "v_pk_mul_f32 v[30:31], v[30:31], v[122:123]\n" \
  "v_pk_add_f32 v[140:141], v[140:141], 1.0 op_sel_hi:[1,0]\n" \
  "s_waitcnt lgkmcnt(10)\n" \
  "v_smfmac_f32_16x16x64_bf16 v[40:43], v[180:183], v[108:115], v192\n" \
  "v_lshlrev_b32_sdwa v66, 4, v221 dst_sel:DWORD dst_unused:UNUSED_PAD src0_sel:DWORD src1_sel:WORD_0\n" \
  "ds_read_b128 v[108:111], v66\n" \
  "v_lshlrev_b32_sdwa v67, 4, v221 dst_sel:DWORD dst_unused:UNUSED_PAD src0_sel:DWORD src1_sel:WORD_1\n" \
  "ds_read_b128 v[112:115], v67\n" \
  "v_pk_add_f32 v[142:143], v[142:143], 1.0 op_sel_hi:[1,0]\n" \
  "v_rcp_f32 v140, v140\n" \
  "v_rcp_f32 v141, v141\n" \
  "v_rcp_f32 v142, v142\n" \
  "v_rcp_f32 v143, v143\n" \
  "v_pk_mul_f32 v[28:29], v[28:29], v[140:141]\n" \
  "v_pk_fma_f32 v[28:29], v[30:31], v[142:143], v[28:29]\n" \
  "v_add_f32 v28, v28, v29\n" \
  "v_permlane16_swap_b32 v0, v16\n" \
  "v_permlane16_swap_b32 v4, v20\n" \
  "v_permlane16_swap_b32 v8, v24\n" \
  "v_permlane16_swap_b32 v12, v28\n" \
  "v_add_f32 v144, v0, v16\n" \
  "v_add_f32 v145, v4, v20\n" \
  "v_add_f32 v146, v8, v24\n" \
  "v_add_f32 v147, v12, v28\n" \
  "s_waitcnt lgkmcnt(10)\n" \
  "v_smfmac_f32_16x16x64_bf16 v[44:47], v[180:183], v[68:75], v192\n" \
  "v_lshlrev_b32_sdwa v64, 4, v254 dst_sel:DWORD dst_unused:UNUSED_PAD src0_sel:DWORD src1_sel:WORD_0\n" \
  "ds_read_b128 v[68:71], v64\n" \
  "v_lshlrev_b32_sdwa v65, 4, v254 dst_sel:DWORD dst_unused:UNUSED_PAD src0_sel:DWORD src1_sel:WORD_1\n" \
  "ds_read_b128 v[72:75], v65\n" \
  "v_min_u32 v144, -2, v144\n" \
  "v_min_u32 v145, -2, v145\n" \
  "v_min_u32 v146, -2, v146\n" \
  "v_min_u32 v147, -2, v147\n" \
  "global_store_dwordx4 v188, v[144:147], s[40:41] offset:0 sc1\n" \
  "v_pk_mul_f32 v[128:129], v[32:33], v[32:33]\n" \
  "v_pk_mul_f32 v[130:131], v[34:35], v[34:35]\n" \
  "v_pk_fma_f32 v[128:129], v[128:129], s[52:53], v[124:125]\n" \
  "v_pk_fma_f32 v[130:131], v[130:131], s[52:53], v[124:125]\n" \
  "v_pk_mul_f32 v[128:129], v[128:129], v[32:33]\n" \
  "v_pk_mul_f32 v[130:131], v[130:131], v[34:35]\n" \
  "v_exp_f32 v128, v128\n" \
  "v_exp_f32 v129, v129\n" \
  "v_pk_mul_f32 v[32:33], v[32:33], v[120:121]\n" \
  "v_exp_f32 v130, v130\n" \
  "v_exp_f32 v131, v131\n" \
  "s_waitcnt lgkmcnt(10)\n" \
  "v_smfmac_f32_16x16x64_bf16 v[44:47], v[180:183], v[76:83], v192\n" \
  "v_lshlrev_b32_sdwa v66, 4, v222 dst_sel:DWORD dst_unused:UNUSED_PAD src0_sel:DWORD src1_sel:WORD_0\n" \
  "ds_read_b128 v[76:79], v66\n" \
  "v_lshlrev_b32_sdwa v67, 4, v222 dst_sel:DWORD dst_unused:UNUSED_PAD src0_sel:DWORD src1_sel:WORD_1\n" \
  "ds_read_b128 v[80:83], v67\n" \
  "v_pk_mul_f32 v[34:35], v[34:35], v[122:123]\n" \
  "v_pk_add_f32 v[128:129], v[128:129], 1.0 op_sel_hi:[1,0]\n" \
  "v_pk_add_f32 v[130:131], v[130:131], 1.0 op_sel_hi:[1,0]\n" \
  "v_rcp_f32 v128, v128\n" \
  "v_rcp_f32 v129, v129\n" \
  "v_rcp_f32 v130, v130\n" \
  "v_rcp_f32 v131, v131\n" \
  "v_pk_mul_f32 v[32:33], v[32:33], v[128:129]\n" \
  "v_pk_fma_f32 v[32:33], v[34:35], v[130:131], v[32:33]\n" \
  "v_add_f32 v32, v32, v33\n" \
  "v_pk_mul_f32 v[132:133], v[36:37], v[36:37]\n" \
  "v_pk_mul_f32 v[134:135], v[38:39], v[38:39]\n" \
  "v_pk_fma_f32 v[132:133], v[132:133], s[52:53], v[124:125]\n" \
  "v_pk_fma_f32 v[134:135], v[134:135], s[52:53], v[124:125]\n" \
  "v_pk_mul_f32 v[132:133], v[132:133], v[36:37]\n" \
  "v_pk_mul_f32 v[134:135], v[134:135], v[38:39]\n" \
  "s_waitcnt lgkmcnt(10)\n" \
  "v_smfmac_f32_16x16x64_bf16 v[48:51], v[180:183], v[84:91], v192\n" \
  "v_lshlrev_b32_sdwa v64, 4, v255 dst_sel:DWORD dst_unused:UNUSED_PAD src0_sel:DWORD src1_sel:WORD_0\n" \
  "ds_read_b128 v[84:87], v64\n" \
  "v_lshlrev_b32_sdwa v65, 4, v255 dst_sel:DWORD dst_unused:UNUSED_PAD src0_sel:DWORD src1_sel:WORD_1\n" \
  "ds_read_b128 v[88:91], v65\n" \
  "v_exp_f32 v132, v132\n" \
  "v_exp_f32 v133, v133\n" \
  "v_pk_mul_f32 v[36:37], v[36:37], v[120:121]\n" \
  "v_exp_f32 v134, v134\n" \
  "v_exp_f32 v135, v135\n" \
  "v_pk_mul_f32 v[38:39], v[38:39], v[122:123]\n" \
  "v_pk_add_f32 v[132:133], v[132:133], 1.0 op_sel_hi:[1,0]\n" \
  "v_pk_add_f32 v[134:135], v[134:135], 1.0 op_sel_hi:[1,0]\n" \
  "v_rcp_f32 v132, v132\n" \
  "v_rcp_f32 v133, v133\n" \
  "v_rcp_f32 v134, v134\n" \
  "v_rcp_f32 v135, v135\n" \
  "v_pk_mul_f32 v[36:37], v[36:37], v[132:133]\n" \
  "v_pk_fma_f32 v[36:37], v[38:39], v[134:135], v[36:37]\n" \
  "v_add_f32 v36, v36, v37\n" \
  "v_pk_mul_f32 v[136:137], v[40:41], v[40:41]\n" \
  "s_waitcnt lgkmcnt(10)\n" \
  "v_smfmac_f32_16x16x64_bf16 v[48:51], v[180:183], v[92:99], v192\n" \
  "v_lshlrev_b32_sdwa v66, 4, v223 dst_sel:DWORD dst_unused:UNUSED_PAD src0_sel:DWORD src1_sel:WORD_0\n" \
  "ds_read_b128 v[92:95], v66\n" \
  "v_lshlrev_b32_sdwa v67, 4, v223 dst_sel:DWORD dst_unused:UNUSED_PAD src0_sel:DWORD src1_sel:WORD_1\n" \
  "ds_read_b128 v[96:99], v67\n" \
  "v_pk_mul_f32 v[138:139], v[42:43], v[42:43]\n" \
  "v_pk_fma_f32 v[136:137], v[136:137], s[52:53], v[124:125]\n" \
  "v_pk_fma_f32 v[138:139], v[138:139], s[52:53], v[124:125]\n" \
  "v_pk_mul_f32 v[136:137], v[136:137], v[40:41]\n" \
  "v_pk_mul_f32 v[138:139], v[138:139], v[42:43]\n" \
  "v_exp_f32 v136, v136\n" \
  "v_exp_f32 v137, v137\n" \
  "v_pk_mul_f32 v[40:41], v[40:41], v[120:121]\n" \
  "v_exp_f32 v138, v138\n" \
  "v_exp_f32 v139, v139\n" \
  "v_pk_mul_f32 v[42:43], v[42:43], v[122:123]\n" \
  "v_pk_add_f32 v[136:137], v[136:137], 1.0 op_sel_hi:[1,0]\n" \
  "v_pk_add_f32 v[138:139], v[138:139], 1.0 op_sel_hi:[1,0]\n" \
  "v_rcp_f32 v136, v136\n" \
  "v_rcp_f32 v137, v137\n" \
  "v_rcp_f32 v138, v138\n" \
  "s_waitcnt lgkmcnt(10)\n" \
  "v_smfmac_f32_16x16x64_bf16 v[52:55], v[180:183], v[100:107], v192\n" \
  "v_rcp_f32 v139, v139\n" \
  "v_pk_mul_f32 v[40:41], v[40:41], v[136:137]\n" \
  "v_pk_fma_f32 v[40:41], v[42:43], v[138:139], v[40:41]\n" \
  "v_add_f32 v40, v40, v41\n" \
  "v_pk_mul_f32 v[140:141], v[44:45], v[44:45]\n" \
  "v_pk_mul_f32 v[142:143], v[46:47], v[46:47]\n" \
  "v_pk_fma_f32 v[140:141], v[140:141], s[52:53], v[124:125]\n" \
  "v_pk_fma_f32 v[142:143], v[142:143], s[52:53], v[124:125]\n" \
  "v_pk_mul_f32 v[140:141], v[140:141], v[44:45]\n" \
  "v_pk_mul_f32 v[142:143], v[142:143], v[46:47]\n" \
  "v_exp_f32 v140, v140\n" \
  "v_exp_f32 v141, v141\n" \
  "v_pk_mul_f32 v[44:45], v[44:45], v[120:121]\n" \
  "v_exp_f32 v142, v142\n" \
  "v_exp_f32 v143, v143\n" \
  "v_pk_mul_f32 v[46:47], v[46:47], v[122:123]\n" \
  "v_pk_add_f32 v[140:141], v[140:141], 1.0 op_sel_hi:[1,0]\n" \
  "v_pk_add_f32 v[142:143], v[142:143], 1.0 op_sel_hi:[1,0]\n" \
  "v_rcp_f32 v140, v140\n" \
  "v_rcp_f32 v141, v141\n" \
  "v_rcp_f32 v142, v142\n" \
  "v_rcp_f32 v143, v143\n" \
  "v_pk_mul_f32 v[44:45], v[44:45], v[140:141]\n" \
  "v_pk_fma_f32 v[44:45], v[46:47], v[142:143], v[44:45]\n" \
  "v_add_f32 v44, v44, v45\n" \
  "v_pk_mul_f32 v[128:129], v[48:49], v[48:49]\n" \
  "v_pk_mul_f32 v[130:131], v[50:51], v[50:51]\n" \
  "v_pk_fma_f32 v[128:129], v[128:129], s[52:53], v[124:125]\n" \
  "v_pk_fma_f32 v[130:131], v[130:131], s[52:53], v[124:125]\n" \
  "v_pk_mul_f32 v[128:129], v[128:129], v[48:49]\n" \
  "v_pk_mul_f32 v[130:131], v[130:131], v[50:51]\n" \
  "v_exp_f32 v128, v128\n" \
  "s_waitcnt lgkmcnt(8)\n" \
  "v_smfmac_f32_16x16x64_bf16 v[52:55], v[180:183], v[108:115], v192\n" \
  "v_exp_f32 v129, v129\n" \
  "v_pk_mul_f32 v[48:49], v[48:49], v[120:121]\n" \
  "v_exp_f32 v130, v130\n" \
  "v_exp_f32 v131, v131\n" \
  "v_pk_mul_f32 v[50:51], v[50:51], v[122:123]\n" \
  "v_pk_add_f32 v[128:129], v[128:129], 1.0 op_sel_hi:[1,0]\n" \
  "v_pk_add_f32 v[130:131], v[130:131], 1.0 op_sel_hi:[1,0]\n" \
  "v_rcp_f32 v128, v128\n" \
  "v_rcp_f32 v129, v129\n" \
  "v_rcp_f32 v130, v130\n" \
  "v_rcp_f32 v131, v131\n" \
  "v_pk_mul_f32 v[48:49], v[48:49], v[128:129]\n" \
  "v_pk_fma_f32 v[48:49], v[50:51], v[130:131], v[48:49]\n" \
  "v_add_f32 v48, v48, v49\n" \
  "s_waitcnt lgkmcnt(6)\n" \
  "v_smfmac_f32_16x16x64_bf16 v[56:59], v[180:183], v[68:75], v192\n" \
  "v_pk_mul_f32 v[132:133], v[52:53], v[52:53]\n" \
  "v_pk_mul_f32 v[134:135], v[54:55], v[54:55]\n" \
  "v_pk_fma_f32 v[132:133], v[132:133], s[52:53], v[124:125]\n" \
  "v_pk_fma_f32 v[134:135], v[134:135], s[52:53], v[124:125]\n" \
  "v_pk_mul_f32 v[132:133], v[132:133], v[52:53]\n" \
  "v_pk_mul_f32 v[134:135], v[134:135], v[54:55]\n" \
  "v_exp_f32 v132, v132\n" \
  "v_exp_f32 v133, v133\n" \
  "v_pk_mul_f32 v[52:53], v[52:53], v[120:121]\n" \
  "v_exp_f32 v134, v134\n" \
  "v_exp_f32 v135, v135\n" \
  "v_pk_mul_f32 v[54:55], v[54:55], v[122:123]\n" \
  "v_pk_add_f32 v[132:133], v[132:133], 1.0 op_sel_hi:[1,0]\n" \
  "v_pk_add_f32 v[134:135], v[134:135], 1.0 op_sel_hi:[1,0]\n" \
  "v_rcp_f32 v132, v132\n" \
  "v_rcp_f32 v133, v133\n" \
  "v_rcp_f32 v134, v134\n" \
  "v_rcp_f32 v135, v135\n" \
  "v_pk_mul_f32 v[52:53], v[52:53], v[132:133]\n" \
  "v_pk_fma_f32 v[52:53], v[54:55], v[134:135], v[52:53]\n" \
  "v_add_f32 v52, v52, v53\n" \
  "s_waitcnt lgkmcnt(4)\n" \
  "v_smfmac_f32_16x16x64_bf16 v[56:59], v[180:183], v[76:83], v192\n" \
  "s_waitcnt lgkmcnt(2)\n" \
  "v_smfmac_f32_16x16x64_bf16 v[60:63], v[180:183], v[84:91], v192\n" \
  "s_nop 5\n" \
  "v_pk_mul_f32 v[136:137], v[56:57], v[56:57]\n" \
  "v_pk_mul_f32 v[138:139], v[58:59], v[58:59]\n" \
  "v_pk_fma_f32 v[136:137], v[136:137], s[52:53], v[124:125]\n" \
  "v_pk_fma_f32 v[138:139], v[138:139], s[52:53], v[124:125]\n" \
  "v_pk_mul_f32 v[136:137], v[136:137], v[56:57]\n" \
  "v_pk_mul_f32 v[138:139], v[138:139], v[58:59]\n" \
  "v_exp_f32 v136, v136\n" \
  "v_exp_f32 v137, v137\n" \
  "v_pk_mul_f32 v[56:57], v[56:57], v[120:121]\n" \
  "v_exp_f32 v138, v138\n" \
  "v_exp_f32 v139, v139\n" \
  "v_pk_mul_f32 v[58:59], v[58:59], v[122:123]\n" \
  "v_pk_add_f32 v[136:137], v[136:137], 1.0 op_sel_hi:[1,0]\n" \
  "v_pk_add_f32 v[138:139], v[138:139], 1.0 op_sel_hi:[1,0]\n" \
  "v_rcp_f32 v136, v136\n" \
  "v_rcp_f32 v137, v137\n" \
  "v_rcp_f32 v138, v138\n" \
  "v_rcp_f32 v139, v139\n" \
  "v_pk_mul_f32 v[56:57], v[56:57], v[136:137]\n" \
  "v_pk_fma_f32 v[56:57], v[58:59], v[138:139], v[56:57]\n" \
  "v_add_f32 v56, v56, v57\n" \
  "s_waitcnt lgkmcnt(0)\n" \
  "v_smfmac_f32_16x16x64_bf16 v[60:63], v[180:183], v[92:99], v192\n" \
  "s_nop 7\n" \
  "v_pk_mul_f32 v[140:141], v[60:61], v[60:61]\n" \
  "v_pk_mul_f32 v[142:143], v[62:63], v[62:63]\n" \
  "v_pk_fma_f32 v[140:141], v[140:141], s[52:53], v[124:125]\n" \
  "v_pk_fma_f32 v[142:143], v[142:143], s[52:53], v[124:125]\n" \
  "v_pk_mul_f32 v[140:141], v[140:141], v[60:61]\n" \
  "v_pk_mul_f32 v[142:143], v[142:143], v[62:63]\n" \
  "v_exp_f32 v140, v140\n" \
  "v_exp_f32 v141, v141\n" \
  "v_pk_mul_f32 v[60:61], v[60:61], v[120:121]\n" \
  "v_exp_f32 v142, v142\n" \
  "v_exp_f32 v143, v143\n" \
  "v_pk_mul_f32 v[62:63], v[62:63], v[122:123]\n" \
  "v_pk_add_f32 v[140:141], v[140:141], 1.0 op_sel_hi:[1,0]\n" \
  "v_pk_add_f32 v[142:143], v[142:143], 1.0 op_sel_hi:[1,0]\n" \
  "v_rcp_f32 v140, v140\n" \
  "v_rcp_f32 v141, v141\n" \
  "v_rcp_f32 v142, v142\n" \
  "v_rcp_f32 v143, v143\n" \
  "v_pk_mul_f32 v[60:61], v[60:61], v[140:141]\n" \
  "v_pk_fma_f32 v[60:61], v[62:63], v[142:143], v[60:61]\n" \
  "v_add_f32 v60, v60, v61\n" \
  "v_permlane16_swap_b32 v32, v48\n" \
  "v_permlane16_swap_b32 v36, v52\n" \
  "v_permlane16_swap_b32 v40, v56\n" \
  "v_permlane16_swap_b32 v44, v60\n" \
  "v_add_f32 v148, v32, v48\n" \
  "v_add_f32 v149, v36, v52\n" \
  "v_add_f32 v150, v40, v56\n" \
  "v_add_f32 v151, v44, v60\n" \
  "v_min_u32 v148, -2, v148\n" \
  "v_min_u32 v149, -2, v149\n" \
  "v_min_u32 v150, -2, v150\n" \
  "v_min_u32 v151, -2, v151\n" \
  "global_store_dwordx4 v188, v[148:151], s[40:41] offset:1024 sc1\n" \
  "v_lshrrev_b32 v0, 4, v188\n" \
  "v_lshrrev_b32 v1, 4, v0\n" \
  "v_and_b32 v2, 15, v0\n" \
  "v_lshlrev_b32 v2, 4, v2\n" \
  "v_lshl_add_u32 v1, v1, 17, v2\n" \
  "s_lshl_b32 s35, s27, 19\n" \
  "s_lshl_b32 s36, s26, 8\n" \
  "s_add_u32 s35, s35, s36\n" \
  "s_add_u32 s35, s35, 105676800\n" \
  "s_add_u32 s40, s24, s35\n" \
  "s_addc_u32 s41, s25, 0\n" \
  "s_add_u32 s42, s40, 65536\n" \
  "s_addc_u32 s43, s41, 0\n" \
  "s_add_u32 s44, s24, 109871104\n" \
  "s_addc_u32 s45, s25, 0\n" \
  "s_add_u32 s46, s44, 65536\n" \
  "s_addc_u32 s47, s45, 0\n" \
  "s_lshr_b32 s35, s26, 2\n" \
  "s_lshl_b32 s35, s35, 8\n" \
  "s_and_b32 s36, s26, 1\n" \
  "s_lshl_b32 s36, s36, 7\n" \
  "s_add_u32 s35, s35, s36\n" \
  "s_and_b32 s36, s26, 2\n" \
  "s_lshl3_add_u32 s35, s36, s35\n" \
  "v_and_b32 v2, 3, v0\n" \
  "v_lshrrev_b32 v3, 2, v0\n" \
  "v_lshl_add_u32 v2, v2, 5, v3\n" \
  "v_add_u32 v2, s35, v2\n" \
  "v_lshlrev_b32 v3, 2, v2\n" \
  "s_cmp_eq_u32 s27, 0\n" \
  "s_cselect_b32 s35, -1, 0\n" \
  "v_and_b32 v3, s35, v3\n" \
  "v_mov_b32 v4, s6\n" \
  "s_mov_b32 s50, 0x10000\n" \
  "LF_poll:\n" \
  "global_load_dwordx4 v[8:11], v1, s[40:41] sc1\n" \
  "global_load_dwordx4 v[12:15], v1, s[42:43] sc1\n" \
  "global_load_dword v16, v3, s[44:45] sc1\n" \
  "global_load_dword v17, v3, s[46:47] sc1\n" \
  "s_waitcnt vmcnt(0)\n" \
  "v_max3_u32 v5, v8, v9, v10\n" \
  "v_max3_u32 v6, v11, v12, v13\n" \
  "v_max3_u32 v5, v5, v14, v15\n" \
  "v_max3_u32 v6, v6, v16, v17\n" \
  "v_max_u32 v5, v5, v6\n" \
  "v_cmp_eq_u32 vcc, -1, v5\n" \
  "s_sub_u32 s50, s50, 1\n" \
  "s_cbranch_scc1 LF_go\n" \
  "s_nop 2\n" \
  "s_cbranch_vccz LF_go\n" \
  "s_sleep 2\n" \
  "s_branch LF_poll\n" \
  "LF_go:\n" \
  "v_pk_add_f32 v[8:9], v[8:9], v[12:13]\n" \
  "v_pk_add_f32 v[10:11], v[10:11], v[14:15]\n" \
  "s_nop 0\n" \
  "v_permlane16_swap_b32 v8, v9\n" \
  "v_permlane16_swap_b32 v10, v11\n" \
  "v_add_f32 v8, v8, v9\n" \
  "v_add_f32 v10, v10, v11\n" \
  "s_nop 1\n" \
  "v_permlane32_swap_b32 v8, v10\n" \
  "v_add_f32 v8, v8, v10\n" \
  "v_and_b32 v5, 15, v0\n" \
  "v_lshrrev_b32 v6, 4, v0\n" \
  "v_lshl_add_u32 v5, v5, 2, v6\n" \
  "s_lshl_b32 s35, s27, 8\n" \
  "v_lshl_add_u32 v5, v5, 2, s35\n" \
  "ds_write_b32 v5, v8\n" \
  "s_waitcnt lgkmcnt(0)\n" \
  "s_barrier\n" \
  "s_cmp_lg_u32 s27, 0\n" \
  "s_cbranch_scc1 LF_end\n" \
  "v_lshlrev_b32 v5, 2, v0\n" \
  "ds_read2st64_b32 v[8:9], v5 offset0:0 offset1:1\n" \
  "ds_read2st64_b32 v[10:11], v5 offset0:2 offset1:3\n" \
  "ds_read2st64_b32 v[12:13], v5 offset0:4 offset1:5\n" \
  "ds_read2st64_b32 v[14:15], v5 offset0:6 offset1:7\n" \
  "v_add_f32 v16, v16, v17\n" \
  "v_lshlrev_b32 v4, 16, v4\n" \
  "v_cvt_pk_bf16_f32 v16, v16, v16\n" \
  "v_lshlrev_b32 v2, 1, v2\n" \
  "s_waitcnt lgkmcnt(0)\n" \
  "v_pk_add_f32 v[8:9], v[8:9], v[10:11]\n" \
  "v_pk_add_f32 v[12:13], v[12:13], v[14:15]\n" \
  "v_pk_add_f32 v[8:9], v[8:9], v[12:13]\n" \
  "v_add_f32 v8, v8, v9\n" \
  "v_cvt_pk_bf16_f32 v8, v8, v8\n" \
  "v_lshlrev_b32 v8, 16, v8\n" \
  "v_add_f32 v8, v8, v4\n" \
  "v_cvt_pk_bf16_f32 v8, v8, v8\n" \
  "v_lshlrev_b32 v8, 16, v8\n" \
  "v_lshlrev_b32 v16, 16, v16\n" \
  "v_add_f32 v8, v8, v16\n" \
  "v_cvt_pk_bf16_f32 v8, v8, v8\n" \
  "global_store_short v2, v8, s[22:23]\n" \
  "LF_end:\n" \
  "s_branch LZ_end\n" \
  "LG_pol:\n" \
  "s_mov_b64 exec, 0xff\n" \
  "global_load_dword v198, v197, s[48:49] offset:2052 sc1\n" \
  "s_mov_b64 exec, -1\n" \
  "s_branch LG_rt0\n" \
  "LG_chk:\n" \
  "s_mov_b32 s50, 0x10000\n" \
  "LG_p:\n" \
  "s_mov_b64 exec, 0xff\n" \
  "v_cmp_le_u32 vcc, 32, v198\n" \
  "s_mov_b64 exec, -1\n" \
  "s_sub_u32 s50, s50, 1\n" \
  "s_cselect_b32 s35, 0xff, vcc_lo\n" \
  "s_cmp_eq_u32 s35, 0xff\n" \
  "s_cbranch_scc1 LG_p_x\n" \
  "s_sleep 2\n" \
  "s_mov_b64 exec, 0xff\n" \
  "global_load_dword v198, v197, s[48:49] offset:2052 sc1\n" \
  "s_mov_b64 exec, -1\n" \
  "s_waitcnt vmcnt(0)\n" \
  "s_branch LG_p\n" \
  "LG_p_x:\n" \
  "s_branch LG_ret\n" \
  "LZ_end:\n" \
  "s_waitcnt vmcnt(0) lgkmcnt(0)\n"

#define R10(p, b) p #b "0", p #b "1", p #b "2", p #b "3", p #b "4", p #b "5", p #b "6", p #b "7", p #b "8", p #b "9"
__global__ __launch_bounds__(512, 2) void k1(const int* __restrict__ idx, const unsigned short* __restrict__ table,
                                             const unsigned short* __restrict__ w_p, const unsigned short* __restrict__ w1,
                                             const unsigned short* __restrict__ b1, const unsigned short* __restrict__ w2,
                                             const unsigned short* __restrict__ b2, __hip_bfloat16* __restrict__ out, char* __restrict__ ws) {
  __shared__ __attribute__((aligned(1024))) char smem[163840];
  unsigned tid = threadIdx.x;
  asm volatile(K_ASM
               : "+{v0}"(tid)
               : "{s[8:9]}"(idx), "{s[10:11]}"(table), "{s[12:13]}"(w_p), "{s[14:15]}"(w1), "{s[16:17]}"(b1), "{s[18:19]}"(w2), "{s[20:21]}"(b2),
                 "{s[22:23]}"(out), "{s[24:25]}"(ws), "{s26}"(blockIdx.x), "s"((unsigned)(uintptr_t)smem)
               : "memory", "scc", "vcc", "s4", "s5", "s6", "s7", "s27", "s28", "s29", "s30", "s31", "s33", "s34", "s35", "s36", "s37", "s38", "s39", R10("s", 4), R10("s", 5), R10("s", 6), R10("s", 7), R10("s", 8), R10("s", 9),
                 "v1", "v2", "v3", "v4", "v5", "v6", "v7", "v8", "v9", R10("v", 1), R10("v", 2), R10("v", 3), R10("v", 4), R10("v", 5), R10("v", 6), R10("v", 7), R10("v", 8), R10("v", 9),
                 R10("v", 10), R10("v", 11), R10("v", 12), R10("v", 13), R10("v", 14), R10("v", 15), R10("v", 16), R10("v", 17), R10("v", 18), R10("v", 19),
                 R10("v", 20), R10("v", 21), R10("v", 22), R10("v", 23), R10("v", 24), "v250", "v251", "v252", "v253", "v254", "v255");
}

extern "C" void kernel_launch(void* const* d_in, const int* in_sizes, int n_in, void* d_out, int out_size, void* d_ws,
                              size_t ws_size, hipStream_t stream) {
  k1<<<dim3(256), dim3(512), 0, stream>>>((const int*)d_in[0], (const unsigned short*)d_in[1], (const unsigned short*)d_in[2], (const unsigned short*)d_in[3],
                                          (const unsigned short*)d_in[4], (const unsigned short*)d_in[5], (const unsigned short*)d_in[6], (__hip_bfloat16*)d_out, (char*)d_ws);
}
```

```cpp
#include <hip/hip_runtime.h>
#include <hip/hip_bf16.h>
#include <stdint.h>

#define K_ASM \
  "LZ_first:\n" \
  "v_readfirstlane_b32 s27, v0\n" \
  "v_and_b32 v255, 63, v0\n" \
  "s_and_b32 s29, s26, 7\n" \
  "s_lshr_b32 s30, s26, 3\n" \
  "s_lshr_b32 s27, s27, 6\n" \
  "s_and_b32 s31, s30, 1\n" \
  "s_and_b32 s28, s27, 3\n" \
  "s_lshr_b32 s35, s30, 1\n" \
  "s_lshl3_add_u32 s7, s35, s29\n" \
  "v_lshrrev_b32 v1, 3, v255\n" \
  "v_and_b32 v2, 7, v255\n" \
  "v_lshlrev_b32 v1, 8, v1\n" \
  "v_lshl_or_b32 v254, v2, 4, v1\n" \
  "s_lshl_b32 s35, s31, 7\n" \
  "s_add_u32 s40, s10, s35\n" \
  "s_addc_u32 s41, s11, 0\n" \
  "s_mul_i32 s36, s7, 37\n" \
  "s_and_b32 s36, s36, 63\n" \
  "s_add_u32 s36, s36, s28\n" \
  "s_cmp_ge_u32 s27, 4\n" \
  "s_cbranch_scc1 LB_start\n" \
  "v_lshlrev_b32 v1, 4, v255\n" \
  "s_lshl_b32 s35, s28, 10\n" \
  "v_add_u32 v1, s35, v1\n" \
  "s_add_u32 m0, s35, 138240\n" \
  "s_nop 0\n" \
  "global_load_lds_dwordx4 v1, s[12:13]\n" \
  "v_add_u32 v1, 0x1000, v1\n" \
  "s_add_u32 m0, m0, 0x1000\n" \
  "s_nop 0\n" \
  "global_load_lds_dwordx4 v1, s[12:13]\n" \
  "v_add_u32 v1, 0x1000, v1\n" \
  "s_add_u32 m0, m0, 0x1000\n" \
  "s_nop 0\n" \
  "global_load_lds_dwordx4 v1, s[12:13]\n" \
  "v_add_u32 v1, 0x1000, v1\n" \
  "s_add_u32 m0, m0, 0x1000\n" \
  "s_nop 0\n" \
  "global_load_lds_dwordx4 v1, s[12:13]\n" \
  "v_add_u32 v1, 0x1000, v1\n" \
  "s_add_u32 m0, m0, 0x1000\n" \
  "v_min_u32 v1, 19984, v1\n" \
  "global_load_lds_dwordx4 v1, s[12:13]\n" \
  "v_and_b32 v252, 15, v255\n" \
  "v_lshrrev_b32 v253, 4, v255\n" \
  "v_mul_u32_u24 v2, 400, v253\n" \
  "v_min_u32 v3, 8, v252\n" \
  "v_lshl_add_u32 v250, v252, 4, v2\n" \
  "v_lshl_add_u32 v251, v3, 4, v2\n" \
  "s_lshl2_add_u32 s35, s7, s28\n" \
  "s_mul_i32 s35, s35, 12800\n" \
  "s_add_u32 s44, s8, s35\n" \
  "s_addc_u32 s45, s9, 0\n" \
  "global_load_dwordx4 v[232:235], v250, s[44:45] offset:0\n" \
  "global_load_dwordx4 v[236:239], v251, s[44:45] offset:256\n" \
  "global_load_dwordx4 v[240:243], v250, s[44:45] offset:1600\n" \
  "global_load_dwordx4 v[244:247], v251, s[44:45] offset:1856\n" \
  "s_setprio 2\n" \
  "v_lshrrev_b32 v1, 2, v252\n" \
  "v_and_b32 v2, 3, v252\n" \
  "v_cmp_eq_u32 vcc, v1, v253\n" \
  "v_mov_b32 v4, 0x3f80\n" \
  "v_and_b32 v13, 1, v253\n" \
  "v_lshlrev_b32 v14, 1, v13\n" \
  "v_xor_b32 v2, v2, v14\n" \
  "v_cndmask_b32 v2, 4, v2, vcc\n" \
  "v_cmp_eq_u32 vcc, 0, v2\n" \
  "s_nop 1\n" \
  "v_cndmask_b32 v216, 0, v4, vcc\n" \
  "v_lshlrev_b32 v220, 16, v216\n" \
  "v_cmp_eq_u32 vcc, 1, v2\n" \
  "s_nop 1\n" \
  "v_cndmask_b32 v217, 0, v4, vcc\n" \
  "v_lshlrev_b32 v221, 16, v217\n" \
  "v_cmp_eq_u32 vcc, 2, v2\n" \
  "s_nop 1\n" \
  "v_cndmask_b32 v218, 0, v4, vcc\n" \
  "v_lshlrev_b32 v222, 16, v218\n" \
  "v_cmp_eq_u32 vcc, 3, v2\n" \
  "s_nop 1\n" \
  "v_cndmask_b32 v219, 0, v4, vcc\n" \
  "v_lshlrev_b32 v223, 16, v219\n" \
  "v_and_b32 v1, 2, v252\n" \
  "v_lshlrev_b32 v1, 3, v1\n" \
  "v_lshlrev_b32 v1, v1, v4\n" \
  "v_and_b32 v2, 0x31, v255\n" \
  "v_mov_b64 v[224:225], 0\n" \
  "v_cmp_eq_u32 vcc, 0, v2\n" \
  "s_nop 1\n" \
  "v_cndmask_b32 v226, 0, v1, vcc\n" \
  "v_cmp_eq_u32 vcc, 1, v2\n" \
  "s_nop 1\n" \
  "v_cndmask_b32 v227, 0, v1, vcc\n" \
  "v_mul_u32_u24 v5, 400, v252\n" \
  "v_add_u32 v5, 138240, v5\n" \
  "v_lshl_add_u32 v6, v253, 4, v5\n" \
  "v_add_u32 v7, 6400, v5\n" \
  "v_add_u32 v8, 6400, v6\n" \
  "v_add_u32 v9, 12800, v5\n" \
  "v_add_u32 v10, 12800, v6\n" \
  "v_bfe_u32 v1, v252, 1, 1\n" \
  "v_and_b32 v2, 1, v252\n" \
  "v_lshl_add_u32 v1, v2, 1, v1\n" \
  "v_mul_u32_u24 v1, 200, v1\n" \
  "v_add_u32 v11, 157440, v1\n" \
  "v_lshl_add_u32 v12, v253, 4, v11\n" \
  "v_lshlrev_b32 v13, 3, v13\n" \
  "v_sub_u32 v16, v5, v13\n" \
  "v_add_u32 v5, v5, v13\n" \
  "v_sub_u32 v17, v6, v13\n" \
  "v_add_u32 v6, v6, v13\n" \
  "v_sub_u32 v18, v7, v13\n" \
  "v_add_u32 v7, v7, v13\n" \
  "v_sub_u32 v19, v8, v13\n" \
  "v_add_u32 v8, v8, v13\n" \
  "v_sub_u32 v20, v9, v13\n" \
  "v_add_u32 v9, v9, v13\n" \
  "v_sub_u32 v21, v10, v13\n" \
  "v_add_u32 v10, v10, v13\n" \
  "v_sub_u32 v22, v11, v13\n" \
  "v_add_u32 v11, v11, v13\n" \
  "v_sub_u32 v23, v12, v13\n" \
  "v_add_u32 v12, v12, v13\n" \
  "s_waitcnt vmcnt(4)\n" \
  "s_barrier\n" \
  "ds_read_b64 v[200:201], v12 offset:128\n" \
  "ds_read_b64 v[202:203], v23 offset:136\n" \
  "ds_read_b64 v[184:185], v9 offset:184\n" \
  "ds_read_b64 v[186:187], v20 offset:192\n" \
  "ds_read_b64 v[188:189], v9 offset:384\n" \
  "ds_read_b64 v[190:191], v20 offset:392\n" \
  "ds_read_b64 v[196:197], v12 offset:64\n" \
  "ds_read_b64 v[198:199], v23 offset:72\n" \
  "ds_read_b64 v[176:177], v7 offset:184\n" \
  "ds_read_b64 v[178:179], v18 offset:192\n" \
  "ds_read_b64 v[180:181], v7 offset:384\n" \
  "ds_read_b64 v[182:183], v18 offset:392\n" \
  "ds_read_b64 v[192:193], v12 offset:0\n" \
  "ds_read_b64 v[194:195], v23 offset:8\n" \
  "s_waitcnt lgkmcnt(12)\n" \
  "v_mfma_f32_16x16x32_bf16 v[44:47], v[200:203], v[216:219], 0\n" \
  "ds_read_b64 v[168:169], v5 offset:184\n" \
  "ds_read_b64 v[170:171], v16 offset:192\n" \
  "s_waitcnt lgkmcnt(12)\n" \
  "v_mfma_f32_16x16x32_bf16 v[44:47], v[224:227], v[184:187], v[44:47]\n" \
  "ds_read_b64 v[172:173], v5 offset:384\n" \
  "ds_read_b64 v[174:175], v16 offset:392\n" \
  "v_mfma_f32_16x16x32_bf16 v[228:231], v[200:203], v[220:223], 0\n" \
  "ds_read_b64 v[96:97], v6 offset:0\n" \
  "ds_read_b64 v[98:99], v17 offset:8\n" \
  "s_waitcnt lgkmcnt(14)\n" \
  "v_mfma_f32_16x16x32_bf16 v[228:231], v[224:227], v[188:191], v[228:231]\n" \
  "ds_read_b64 v[124:125], v8 offset:64\n" \
  "ds_read_b64 v[126:127], v19 offset:72\n" \
  "s_waitcnt lgkmcnt(14)\n" \
  "v_mfma_f32_16x16x32_bf16 v[60:63], v[196:199], v[216:219], 0\n" \
  "ds_read_b64 v[136:137], v8 offset:264\n" \
  "ds_read_b64 v[138:139], v19 offset:272\n" \
  "s_waitcnt lgkmcnt(14)\n" \
  "v_mfma_f32_16x16x32_bf16 v[60:63], v[224:227], v[176:179], v[60:63]\n" \
  "ds_read_b64 v[140:141], v8 offset:328\n" \
  "ds_read_b64 v[142:143], v19 offset:336\n" \
  "v_mfma_f32_16x16x32_bf16 v[212:215], v[196:199], v[220:223], 0\n" \
  "ds_read_b64 v[160:161], v10 offset:264\n" \
  "ds_read_b64 v[162:163], v21 offset:272\n" \
  "s_waitcnt lgkmcnt(15)\n" \
  "v_mfma_f32_16x16x32_bf16 v[212:215], v[224:227], v[180:183], v[212:215]\n" \
  "ds_read_b64 v[116:117], v6 offset:328\n" \
  "ds_read_b64 v[118:119], v17 offset:336\n" \
  "s_waitcnt lgkmcnt(15)\n" \
  "v_mfma_f32_16x16x32_bf16 v[68:71], v[192:195], v[216:219], 0\n" \
  "ds_read_b64 v[144:145], v10 offset:0\n" \
  "ds_read_b64 v[146:147], v21 offset:8\n" \
  "s_waitcnt lgkmcnt(15)\n" \
  "v_mfma_f32_16x16x32_bf16 v[68:71], v[224:227], v[168:171], v[68:71]\n" \
  "ds_read_b64 v[148:149], v10 offset:64\n" \
  "ds_read_b64 v[150:151], v21 offset:72\n" \
  "v_mfma_f32_16x16x32_bf16 v[208:211], v[192:195], v[220:223], 0\n" \
  "ds_read_b64 v[104:105], v6 offset:128\n" \
  "ds_read_b64 v[106:107], v17 offset:136\n" \
  "s_waitcnt lgkmcnt(15)\n" \
  "v_mfma_f32_16x16x32_bf16 v[208:211], v[224:227], v[172:175], v[208:211]\n" \
  "ds_read_b64 v[100:101], v6 offset:64\n" \
  "ds_read_b64 v[102:103], v17 offset:72\n" \
  "v_mfma_f32_16x16x32_bf16 v[92:95], v[96:99], v[216:219], 0\n" \
  "ds_read_b64 v[120:121], v8 offset:0\n" \
  "ds_read_b64 v[122:123], v19 offset:8\n" \
  "s_waitcnt lgkmcnt(15)\n" \
  "v_mfma_f32_16x16x32_bf16 v[24:27], v[124:127], v[220:223], 0\n" \
  "ds_read_b64 v[112:113], v6 offset:264\n" \
  "ds_read_b64 v[114:115], v17 offset:272\n" \
  "v_mfma_f32_16x16x32_bf16 v[24:27], v[216:219], v[136:139], v[24:27]\n" \
  "ds_read_b64 v[156:157], v10 offset:200\n" \
  "ds_read_b64 v[158:159], v21 offset:208\n" \
  "s_waitcnt lgkmcnt(15)\n" \
  "v_mfma_f32_16x16x32_bf16 v[28:31], v[140:143], v[220:223], 0\n" \
  "ds_read_b64 v[132:133], v8 offset:200\n" \
  "ds_read_b64 v[134:135], v19 offset:208\n" \
  "v_mfma_f32_16x16x32_bf16 v[28:31], v[220:223], v[160:163], v[28:31]\n" \
  "ds_read_b64 v[108:109], v6 offset:200\n" \
  "ds_read_b64 v[110:111], v17 offset:208\n" \
  "s_waitcnt lgkmcnt(15)\n" \
  "v_mfma_f32_16x16x32_bf16 v[32:35], v[116:119], v[216:219], 0\n" \
  "ds_read_b64 v[152:153], v10 offset:128\n" \
  "ds_read_b64 v[154:155], v21 offset:136\n" \
  "v_mfma_f32_16x16x32_bf16 v[32:35], v[220:223], v[144:147], v[32:35]\n" \
  "ds_read_b64 v[164:165], v10 offset:328\n" \
  "ds_read_b64 v[166:167], v21 offset:336\n" \
  "v_mfma_f32_16x16x32_bf16 v[36:39], v[140:143], v[216:219], 0\n" \
  "ds_read_b64 v[128:129], v8 offset:128\n" \
  "ds_read_b64 v[130:131], v19 offset:136\n" \
  "s_waitcnt lgkmcnt(15)\n" \
  "v_mfma_f32_16x16x32_bf16 v[36:39], v[220:223], v[148:151], v[36:39]\n" \
  "ds_read_b64 v[204:205], v11 offset:184\n" \
  "ds_read_b64 v[206:207], v22 offset:192\n" \
  "v_mfma_f32_16x16x32_bf16 v[40:43], v[104:107], v[216:219], 0\n" \
  "v_mfma_f32_16x16x32_bf16 v[40:43], v[216:219], v[144:147], v[40:43]\n" \
  "v_mfma_f32_16x16x32_bf16 v[48:51], v[100:103], v[216:219], 0\n" \
  "s_waitcnt lgkmcnt(15)\n" \
  "v_mfma_f32_16x16x32_bf16 v[48:51], v[216:219], v[120:123], v[48:51]\n" \
  "s_waitcnt lgkmcnt(14)\n" \
  "v_mfma_f32_16x16x32_bf16 v[52:55], v[112:115], v[216:219], 0\n" \
  "v_mfma_f32_16x16x32_bf16 v[52:55], v[220:223], v[120:123], v[52:55]\n" \
  "v_mfma_f32_16x16x32_bf16 v[56:59], v[116:119], v[220:223], 0\n" \
  "s_waitcnt lgkmcnt(12)\n" \
  "v_mfma_f32_16x16x32_bf16 v[56:59], v[220:223], v[156:159], v[56:59]\n" \
  "v_mfma_f32_16x16x32_bf16 v[64:67], v[104:107], v[220:223], 0\n" \
  "v_mfma_f32_16x16x32_bf16 v[64:67], v[216:219], v[156:159], v[64:67]\n" \
  "v_mfma_f32_16x16x32_bf16 v[72:75], v[136:139], v[220:223], 0\n" \
  "v_mfma_f32_16x16x32_bf16 v[76:79], v[112:115], v[220:223], 0\n" \
  "s_waitcnt lgkmcnt(10)\n" \
  "v_mfma_f32_16x16x32_bf16 v[76:79], v[220:223], v[132:135], v[76:79]\n" \
  "v_mfma_f32_16x16x32_bf16 v[80:83], v[100:103], v[220:223], 0\n" \
  "v_mfma_f32_16x16x32_bf16 v[80:83], v[216:219], v[132:135], v[80:83]\n" \
  "v_mfma_f32_16x16x32_bf16 v[84:87], v[96:99], v[220:223], 0\n" \
  "s_waitcnt lgkmcnt(8)\n" \
  "v_mfma_f32_16x16x32_bf16 v[84:87], v[216:219], v[108:111], v[84:87]\n" \
  "v_mfma_f32_16x16x32_bf16 v[88:91], v[108:111], v[220:223], 0\n" \
  "v_mfma_f32_16x16x32_bf16 v[0:3], v[124:127], v[216:219], 0\n" \
  "s_waitcnt lgkmcnt(6)\n" \
  "v_mfma_f32_16x16x32_bf16 v[4:7], v[152:155], v[216:219], 0\n" \
  "v_mfma_f32_16x16x32_bf16 v[8:11], v[152:155], v[220:223], 0\n" \
  "s_waitcnt lgkmcnt(4)\n" \
  "v_mfma_f32_16x16x32_bf16 v[8:11], v[216:219], v[164:167], v[8:11]\n" \
  "v_mfma_f32_16x16x32_bf16 v[12:15], v[164:167], v[220:223], 0\n" \
  "s_waitcnt lgkmcnt(2)\n" \
  "v_mfma_f32_16x16x32_bf16 v[16:19], v[128:131], v[220:223], 0\n" \
  "v_mfma_f32_16x16x32_bf16 v[16:19], v[216:219], v[160:163], v[16:19]\n" \
  "v_mfma_f32_16x16x32_bf16 v[20:23], v[128:131], v[216:219], 0\n" \
  "v_mfma_f32_16x16x32_bf16 v[20:23], v[216:219], v[148:151], v[20:23]\n" \
  "s_waitcnt lgkmcnt(0)\n" \
  "v_mov_b32 v254, 163328\n" \
  "v_mov_b32 v255, 1\n" \
  "s_mov_b64 exec, 1\n" \
  "ds_add_u32 v254, v255 offset:16\n" \
  "s_mov_b64 exec, -1\n" \
  "v_mfma_f32_16x16x32_bf16 v[168:171], v[216:219], v[96:99], 0\n" \
  "v_mfma_f32_16x16x32_bf16 v[172:175], v[204:207], v[224:227], 0\n" \
  "v_mfma_f32_16x16x32_bf16 v[176:179], v[224:227], v[204:207], 0\n" \
  "s_mov_b32 exec_lo, 0x0\n" \
  "s_mov_b32 exec_hi, 0xffffffff\n" \
  "v_mov_b64 v[44:45], v[228:229]\n" \
  "v_mov_b64 v[46:47], v[230:231]\n" \
  "v_mov_b64 v[60:61], v[212:213]\n" \
  "v_mov_b64 v[62:63], v[214:215]\n" \
  "v_mov_b64 v[68:69], v[208:209]\n" \
  "v_mov_b64 v[70:71], v[210:211]\n" \
  "s_mov_b32 exec_lo, 0xff00ff00\n" \
  "s_mov_b32 exec_hi, 0x0\n" \
  "v_add_f32 v92, v92, v168\n" \
  "v_add_f32 v93, v93, v169\n" \
  "v_add_f32 v94, v94, v170\n" \
  "v_add_f32 v95, v95, v171\n" \
  "s_mov_b32 exec_lo, 0xf0000f00\n" \
  "s_mov_b32 exec_hi, 0x0\n" \
  "v_sub_f32 v92, v92, v176\n" \
  "v_sub_f32 v93, v93, v177\n" \
  "v_sub_f32 v94, v94, v178\n" \
  "v_sub_f32 v95, v95, v179\n" \
  "s_mov_b32 exec_lo, 0x0\n" \
  "s_mov_b32 exec_hi, 0xff00ff\n" \
  "v_mov_b64 v[92:93], 0\n" \
  "v_mov_b64 v[94:95], 0\n" \
  "s_mov_b32 exec_lo, 0x0\n" \
  "s_mov_b32 exec_hi, 0xf0000f\n" \
  "v_mov_b64 v[92:93], v[172:173]\n" \
  "v_mov_b64 v[94:95], v[174:175]\n" \
  "s_mov_b64 exec, -1\n" \
  "v_lshlrev_b32 v188, 4, v253\n" \
  "v_and_b32 v96, 4, v252\n" \
  "v_lshl_add_u32 v96, v253, 5, v96\n" \
  "v_mov_b32 v97, 130000\n" \
  "v_cmp_gt_u32 vcc, 8, v252\n" \
  "v_mov_b32 v193, 0x4444\n" \
  "v_mov_b32 v98, 0xeeee\n" \
  "v_cndmask_b32 v189, v97, v96, vcc\n" \
  "v_cndmask_b32 v190, v96, v97, vcc\n" \
  "v_cndmask_b32_e64 v191, 0, 16, vcc\n" \
  "v_cndmask_b32_e64 v192, 16, 0, vcc\n" \
  "v_and_b32 v99, 4, v252\n" \
  "v_cmp_ne_u32 vcc, 0, v99\n" \
  "s_lshl_b32 s35, s28, 11\n" \
  "s_add_u32 s35, s35, 130048\n" \
  "v_cndmask_b32 v193, v193, v98, vcc\n" \
  "v_lshl_add_u32 v194, v252, 4, s35\n" \
  "v_and_b32 v96, 7, v252\n" \
  "v_lshlrev_b32 v96, 5, v96\n" \
  "v_lshl_add_u32 v96, v253, 8, v96\n" \
  "v_lshrrev_b32 v97, 3, v252\n" \
  "v_lshl_add_u32 v96, v97, 2, v96\n" \
  "v_add_u32 v195, s35, v96\n" \
  "v_and_b32 v97, v97, v252\n" \
  "v_lshlrev_b32 v179, 4, v97\n" \
  "v_mov_b64 v[96:97], 0\n" \
  "v_mov_b32 v98, 130000\n" \
  "ds_write_b64 v98, v[96:97]\n" \
  "ds_write_b64 v98, v[96:97] offset:8\n" \
  "ds_write_b64 v98, v[96:97] offset:16\n" \
  "ds_write_b64 v98, v[96:97] offset:24\n" \
  "s_waitcnt vmcnt(0)\n" \
  "v_cvt_pk_u16_u32 v232, v232, v233\n" \
  "v_cvt_pk_u16_u32 v234, v234, v235\n" \
  "v_cvt_pk_u16_u32 v236, v236, v237\n" \
  "v_cvt_pk_u16_u32 v238, v238, v239\n" \
  "v_pk_lshrrev_b16 v233, 3, v232 op_sel_hi:[0,1]\n" \
  "v_pk_lshrrev_b16 v235, 3, v234 op_sel_hi:[0,1]\n" \
  "v_pk_lshrrev_b16 v237, 3, v236 op_sel_hi:[0,1]\n" \
  "v_pk_lshrrev_b16 v239, 3, v238 op_sel_hi:[0,1]\n" \
  "v_pk_mad_u16 v232, v232, 8, v233 op_sel_hi:[1,0,1]\n" \
  "v_pk_mad_u16 v234, v234, 8, v235 op_sel_hi:[1,0,1]\n" \
  "v_pk_mad_u16 v236, v236, 8, v237 op_sel_hi:[1,0,1]\n" \
  "v_pk_mad_u16 v238, v238, 8, v239 op_sel_hi:[1,0,1]\n" \
  "v_alignbit_b32 v236, v236, v236, v179\n" \
  "v_alignbit_b32 v238, v238, v238, v179\n" \
  "ds_write2_b32 v195, v232, v234 offset0:0 offset1:4\n" \
  "ds_write2_b32 v195, v236, v238 offset0:2 offset1:6\n" \
  "v_cvt_pk_u16_u32 v240, v240, v241\n" \
  "v_cvt_pk_u16_u32 v242, v242, v243\n" \
  "v_cvt_pk_u16_u32 v244, v244, v245\n" \
  "v_cvt_pk_u16_u32 v246, v246, v247\n" \
  "v_pk_lshrrev_b16 v241, 3, v240 op_sel_hi:[0,1]\n" \
  "v_pk_lshrrev_b16 v243, 3, v242 op_sel_hi:[0,1]\n" \
  "v_pk_lshrrev_b16 v245, 3, v244 op_sel_hi:[0,1]\n" \
  "v_pk_lshrrev_b16 v247, 3, v246 op_sel_hi:[0,1]\n" \
  "v_pk_mad_u16 v240, v240, 8, v241 op_sel_hi:[1,0,1]\n" \
  "v_pk_mad_u16 v242, v242, 8, v243 op_sel_hi:[1,0,1]\n" \
  "v_pk_mad_u16 v244, v244, 8, v245 op_sel_hi:[1,0,1]\n" \
  "v_pk_mad_u16 v246, v246, 8, v247 op_sel_hi:[1,0,1]\n" \
  "v_alignbit_b32 v244, v244, v244, v179\n" \
  "v_alignbit_b32 v246, v246, v246, v179\n" \
  "v_add_u32 v241, 0x400, v195\n" \
  "ds_write2_b32 v241, v240, v242 offset0:0 offset1:4\n" \
  "ds_write2_b32 v241, v244, v246 offset0:2 offset1:6\n" \
  "v_add_u32 v96, 6400, v250\n" \
  "v_add_u32 v97, 6400, v251\n" \
  "v_mov_b32 v98, v254\n" \
  "L1_:\n" \
  "ds_read_b32 v99, v98 offset:12\n" \
  "s_waitcnt lgkmcnt(0)\n" \
  "v_readfirstlane_b32 s35, v99\n" \
  "s_cmp_lt_u32 s35, 4\n" \
  "s_cbranch_scc1 L1_\n" \
  "v_lshlrev_b32 v203, 4, v253\n" \
  "s_lshr_b32 s35, s30, 4\n" \
  "s_lshl1_add_u32 s35, s29, s35\n" \
  "s_lshl_b32 s35, s35, 6\n" \
  "s_bfe_u32 s37, s30, 0x20002\n" \
  "s_lshl3_add_u32 s35, s37, s35\n" \
  "v_and_b32 v203, 16, v203\n" \
  "v_lshl_add_u32 v203, v203, 1, s35\n" \
  "global_load_dwordx2 v[204:205], v203, s[16:17]\n" \
  "global_load_dwordx2 v[206:207], v203, s[18:19]\n" \
  "v_mov_b32 v203, 0\n" \
  "global_load_ushort v203, v203, s[20:21]\n" \
  "s_add_u32 s46, s44, 4800\n" \
  "s_addc_u32 s47, s45, 0\n" \
  "global_load_dwordx4 v[208:211], v96, s[44:45] offset:-3200\n" \
  "global_load_dwordx4 v[212:215], v97, s[44:45] offset:-2944\n" \
  "global_load_dwordx4 v[216:219], v96, s[44:45] offset:-1600\n" \
  "global_load_dwordx4 v[220:223], v97, s[44:45] offset:-1344\n" \
  "global_load_dwordx4 v[224:227], v96, s[44:45] offset:0\n" \
  "global_load_dwordx4 v[228:231], v97, s[44:45] offset:256\n" \
  "global_load_dwordx4 v[232:235], v96, s[44:45] offset:1600\n" \
  "global_load_dwordx4 v[236:239], v97, s[44:45] offset:1856\n" \
  "global_load_dwordx4 v[240:243], v96, s[44:45] offset:3200\n" \
  "global_load_dwordx4 v[244:247], v97, s[44:45] offset:3456\n" \
  "global_load_dwordx4 v[248:251], v96, s[46:47] offset:0\n" \
  "global_load_dwordx4 v[252:255], v97, s[46:47] offset:256\n" \
  "s_setprio 1\n" \
  "ds_read_b128 v[180:183], v194\n" \
  "v_add_u32 v194, 0x100, v194\n" \
  "v_mov_b64 v[164:165], 0\n" \
  "v_mov_b64 v[166:167], 0\n" \
  "v_mov_b64 v[168:169], 0\n" \
  "v_mov_b64 v[170:171], 0\n" \
  "v_mov_b64 v[172:173], 0\n" \
  "v_mov_b64 v[174:175], 0\n" \
  "s_waitcnt lgkmcnt(0)\n" \
  "v_mad_u32_u16 v184, v182, 16, v188\n" \
  "ds_read_b128 v[128:131], v184 offset:0\n" \
  "ds_read_b128 v[132:135], v184 offset:64\n" \
  "v_mad_u32_u16 v185, v181, 16, v188\n" \
  "v_mad_u32_u16 v186, v182, 16, v188 op_sel:[1,0,0,0]\n" \
  "ds_read_b128 v[112:115], v185 offset:0\n" \
  "ds_read_b128 v[116:119], v185 offset:64\n" \
  "ds_read_b128 v[136:139], v186 offset:0\n" \
  "ds_read_b128 v[140:143], v186 offset:64\n" \
  "v_mad_u32_u16 v187, v181, 16, v188 op_sel:[1,0,0,0]\n" \
  "ds_read_b128 v[120:123], v187 offset:0\n" \
  "ds_read_b128 v[124:127], v187 offset:64\n" \
  "v_mad_u32_u16 v184, v180, 16, v188 op_sel:[1,0,0,0]\n" \
  "ds_read_b128 v[104:107], v184 offset:0\n" \
  "ds_read_b128 v[108:111], v184 offset:64\n" \
  "v_mad_u32_u16 v185, v180, 16, v188\n" \
  "ds_read_b128 v[96:99], v185 offset:0\n" \
  "s_waitcnt lgkmcnt(8)\n" \
  "s_mov_b32 s51, 3\n" \
  "s_call_b64 s[52:53], LA_loop\n" \
  "LA_ev0:\n" \
  "v_mov_b32 v196, v178\n" \
  "s_waitcnt vmcnt(10)\n" \
  "v_cvt_pk_u16_u32 v208, v208, v209\n" \
  "v_cvt_pk_u16_u32 v210, v210, v211\n" \
  "v_cvt_pk_u16_u32 v212, v212, v213\n" \
  "v_cvt_pk_u16_u32 v214, v214, v215\n" \
  "v_pk_lshrrev_b16 v209, 3, v208 op_sel_hi:[0,1]\n" \
  "v_pk_lshrrev_b16 v211, 3, v210 op_sel_hi:[0,1]\n" \
  "v_pk_lshrrev_b16 v213, 3, v212 op_sel_hi:[0,1]\n" \
  "v_pk_lshrrev_b16 v215, 3, v214 op_sel_hi:[0,1]\n" \
  "v_pk_mad_u16 v208, v208, 8, v209 op_sel_hi:[1,0,1]\n" \
  "v_pk_mad_u16 v210, v210, 8, v211 op_sel_hi:[1,0,1]\n" \
  "v_pk_mad_u16 v212, v212, 8, v213 op_sel_hi:[1,0,1]\n" \
  "v_pk_mad_u16 v214, v214, 8, v215 op_sel_hi:[1,0,1]\n" \
  "v_alignbit_b32 v212, v212, v212, v179\n" \
  "v_alignbit_b32 v214, v214, v214, v179\n" \
  "ds_write2_b32 v195, v208, v210 offset0:0 offset1:4\n" \
  "ds_write2_b32 v195, v212, v214 offset0:2 offset1:6\n" \
  "v_readfirstlane_b32 s6, v203\n" \
  "s_mov_b32 s51, 4\n" \
  "s_call_b64 s[52:53], LA_loop\n" \
  "LA_ev1:\n" \
  "v_mov_b32 v197, v178\n" \
  "s_waitcnt vmcnt(8)\n" \
  "v_cvt_pk_u16_u32 v216, v216, v217\n" \
  "v_cvt_pk_u16_u32 v218, v218, v219\n" \
  "v_cvt_pk_u16_u32 v220, v220, v221\n" \
  "v_cvt_pk_u16_u32 v222, v222, v223\n" \
  "v_pk_lshrrev_b16 v217, 3, v216 op_sel_hi:[0,1]\n" \
  "v_pk_lshrrev_b16 v219, 3, v218 op_sel_hi:[0,1]\n" \
  "v_pk_lshrrev_b16 v221, 3, v220 op_sel_hi:[0,1]\n" \
  "v_pk_lshrrev_b16 v223, 3, v222 op_sel_hi:[0,1]\n" \
  "v_pk_mad_u16 v216, v216, 8, v217 op_sel_hi:[1,0,1]\n" \
  "v_pk_mad_u16 v218, v218, 8, v219 op_sel_hi:[1,0,1]\n" \
  "v_pk_mad_u16 v220, v220, 8, v221 op_sel_hi:[1,0,1]\n" \
  "v_pk_mad_u16 v222, v222, 8, v223 op_sel_hi:[1,0,1]\n" \
  "v_alignbit_b32 v220, v220, v220, v179\n" \
  "v_alignbit_b32 v222, v222, v222, v179\n" \
  "v_add_u32 v217, 0x400, v195\n" \
  "ds_write2_b32 v217, v216, v218 offset0:0 offset1:4\n" \
  "ds_write2_b32 v217, v220, v222 offset0:2 offset1:6\n" \
  "v_subrev_u32 v194, 0x800, v194\n" \
  "s_mov_b32 s51, 4\n" \
  "s_call_b64 s[52:53], LA_loop\n" \
  "LA_ev2:\n" \
  "v_mov_b32 v198, v178\n" \
  "s_waitcnt vmcnt(6)\n" \
  "v_cvt_pk_u16_u32 v224, v224, v225\n" \
  "v_cvt_pk_u16_u32 v226, v226, v227\n" \
  "v_cvt_pk_u16_u32 v228, v228, v229\n" \
  "v_cvt_pk_u16_u32 v230, v230, v231\n" \
  "v_pk_lshrrev_b16 v225, 3, v224 op_sel_hi:[0,1]\n" \
  "v_pk_lshrrev_b16 v227, 3, v226 op_sel_hi:[0,1]\n" \
  "v_pk_lshrrev_b16 v229, 3, v228 op_sel_hi:[0,1]\n" \
  "v_pk_lshrrev_b16 v231, 3, v230 op_sel_hi:[0,1]\n" \
  "v_pk_mad_u16 v224, v224, 8, v225 op_sel_hi:[1,0,1]\n" \
  "v_pk_mad_u16 v226, v226, 8, v227 op_sel_hi:[1,0,1]\n" \
  "v_pk_mad_u16 v228, v228, 8, v229 op_sel_hi:[1,0,1]\n" \
  "v_pk_mad_u16 v230, v230, 8, v231 op_sel_hi:[1,0,1]\n" \
  "v_alignbit_b32 v228, v228, v228, v179\n" \
  "v_alignbit_b32 v230, v230, v230, v179\n" \
  "ds_write2_b32 v195, v224, v226 offset0:0 offset1:4\n" \
  "ds_write2_b32 v195, v228, v230 offset0:2 offset1:6\n" \
  "s_mov_b32 s51, 4\n" \
  "s_call_b64 s[52:53], LA_loop\n" \
  "LA_ev3:\n" \
  "v_mov_b32 v199, v178\n" \
  "s_waitcnt vmcnt(4)\n" \
  "v_cvt_pk_u16_u32 v232, v232, v233\n" \
  "v_cvt_pk_u16_u32 v234, v234, v235\n" \
  "v_cvt_pk_u16_u32 v236, v236, v237\n" \
  "v_cvt_pk_u16_u32 v238, v238, v239\n" \
  "v_pk_lshrrev_b16 v233, 3, v232 op_sel_hi:[0,1]\n" \
  "v_pk_lshrrev_b16 v235, 3, v234 op_sel_hi:[0,1]\n" \
  "v_pk_lshrrev_b16 v237, 3, v236 op_sel_hi:[0,1]\n" \
  "v_pk_lshrrev_b16 v239, 3, v238 op_sel_hi:[0,1]\n" \
  "v_pk_mad_u16 v232, v232, 8, v233 op_sel_hi:[1,0,1]\n" \
  "v_pk_mad_u16 v234, v234, 8, v235 op_sel_hi:[1,0,1]\n" \
  "v_pk_mad_u16 v236, v236, 8, v237 op_sel_hi:[1,0,1]\n" \
  "v_pk_mad_u16 v238, v238, 8, v239 op_sel_hi:[1,0,1]\n" \
  "v_alignbit_b32 v236, v236, v236, v179\n" \
  "v_alignbit_b32 v238, v238, v238, v179\n" \
  "v_add_u32 v233, 0x400, v195\n" \
  "ds_write2_b32 v233, v232, v234 offset0:0 offset1:4\n" \
  "ds_write2_b32 v233, v236, v238 offset0:2 offset1:6\n" \
  "v_subrev_u32 v194, 0x800, v194\n" \
  "s_mov_b32 s51, 4\n" \
  "s_call_b64 s[52:53], LA_loop\n" \
  "LA_ev4:\n" \
  "v_mov_b32 v200, v178\n" \
  "s_waitcnt vmcnt(2)\n" \
  "v_cvt_pk_u16_u32 v240, v240, v241\n" \
  "v_cvt_pk_u16_u32 v242, v242, v243\n" \
  "v_cvt_pk_u16_u32 v244, v244, v245\n" \
  "v_cvt_pk_u16_u32 v246, v246, v247\n" \
  "v_pk_lshrrev_b16 v241, 3, v240 op_sel_hi:[0,1]\n" \
  "v_pk_lshrrev_b16 v243, 3, v242 op_sel_hi:[0,1]\n" \
  "v_pk_lshrrev_b16 v245, 3, v244 op_sel_hi:[0,1]\n" \
  "v_pk_lshrrev_b16 v247, 3, v246 op_sel_hi:[0,1]\n" \
  "v_pk_mad_u16 v240, v240, 8, v241 op_sel_hi:[1,0,1]\n" \
  "v_pk_mad_u16 v242, v242, 8, v243 op_sel_hi:[1,0,1]\n" \
  "v_pk_mad_u16 v244, v244, 8, v245 op_sel_hi:[1,0,1]\n" \
  "v_pk_mad_u16 v246, v246, 8, v247 op_sel_hi:[1,0,1]\n" \
  "v_alignbit_b32 v244, v244, v244, v179\n" \
  "v_alignbit_b32 v246, v246, v246, v179\n" \
  "ds_write2_b32 v195, v240, v242 offset0:0 offset1:4\n" \
  "ds_write2_b32 v195, v244, v246 offset0:2 offset1:6\n" \
  "s_mov_b32 s51, 4\n" \
  "s_call_b64 s[52:53], LA_loop\n" \
  "LA_ev5:\n" \
  "v_mov_b32 v201, v178\n" \
  "s_waitcnt vmcnt(0)\n" \
  "v_cvt_pk_u16_u32 v248, v248, v249\n" \
  "v_cvt_pk_u16_u32 v250, v250, v251\n" \
  "v_cvt_pk_u16_u32 v252, v252, v253\n" \
  "v_cvt_pk_u16_u32 v254, v254, v255\n" \
  "v_pk_lshrrev_b16 v249, 3, v248 op_sel_hi:[0,1]\n" \
  "v_pk_lshrrev_b16 v251, 3, v250 op_sel_hi:[0,1]\n" \
  "v_pk_lshrrev_b16 v253, 3, v252 op_sel_hi:[0,1]\n" \
  "v_pk_lshrrev_b16 v255, 3, v254 op_sel_hi:[0,1]\n" \
  "v_pk_mad_u16 v248, v248, 8, v249 op_sel_hi:[1,0,1]\n" \
  "v_pk_mad_u16 v250, v250, 8, v251 op_sel_hi:[1,0,1]\n" \
  "v_pk_mad_u16 v252, v252, 8, v253 op_sel_hi:[1,0,1]\n" \
  "v_pk_mad_u16 v254, v254, 8, v255 op_sel_hi:[1,0,1]\n" \
  "v_alignbit_b32 v252, v252, v252, v179\n" \
  "v_alignbit_b32 v254, v254, v254, v179\n" \
  "v_add_u32 v249, 0x400, v195\n" \
  "ds_write2_b32 v249, v248, v250 offset0:0 offset1:4\n" \
  "ds_write2_b32 v249, v252, v254 offset0:2 offset1:6\n" \
  "v_subrev_u32 v194, 0x800, v194\n" \
  "s_mov_b32 s51, 4\n" \
  "s_call_b64 s[52:53], LA_loop\n" \
  "LA_ev6:\n" \
  "v_mov_b32 v202, v178\n" \
  "s_lshl_b32 s35, s28, 2\n" \
  "s_add_u32 s35, s35, 163328\n" \
  "v_mov_b32 v208, s35\n" \
  "L2_:\n" \
  "ds_read_b32 v209, v208 offset:32\n" \
  "s_waitcnt lgkmcnt(0)\n" \
  "v_readfirstlane_b32 s35, v209\n" \
  "s_cmp_lt_u32 s35, 128\n" \
  "s_cbranch_scc1 L2_\n" \
  "s_and_b32 s4, s35, 15\n" \
  "s_sub_u32 s4, s4, 4\n" \
  "s_bfe_u32 s5, s35, 0x30004\n" \
  "s_sub_u32 s5, s5, s29\n" \
  "s_cmp_le_i32 s4, 0\n" \
  "s_cbranch_scc1 LA_nt\n" \
  "s_mul_i32 s35, s5, 51200\n" \
  "s_ashr_i32 s36, s35, 31\n" \
  "s_add_u32 s46, s46, s35\n" \
  "s_addc_u32 s47, s47, s36\n" \
  "v_lshrrev_b32 v216, 4, v188\n" \
  "v_mul_u32_u24 v216, 400, v216\n" \
  "v_add_u32 v216, 6400, v216\n" \
  "v_bfe_u32 v218, v194, 4, 4\n" \
  "v_min_u32 v217, 8, v218\n" \
  "v_lshl_add_u32 v217, v217, 4, v216\n" \
  "v_lshl_add_u32 v216, v218, 4, v216\n" \
  "global_load_dwordx4 v[208:211], v216, s[46:47] offset:0\n" \
  "global_load_dwordx4 v[212:215], v217, s[46:47] offset:256\n" \
  "LA_nt:\n" \
  "s_lshl_b32 s5, s5, 7\n" \
  "s_sub_u32 s5, s5, s4\n" \
  "s_min_i32 s51, s4, 0\n" \
  "s_add_u32 s51, s51, 4\n" \
  "s_cmp_eq_u32 s51, 0\n" \
  "s_cbranch_scc1 LA_ev7\n" \
  "s_call_b64 s[52:53], LA_loop\n" \
  "LA_ev7:\n" \
  "v_mov_b32 v203, v178\n" \
  "s_cmp_le_i32 s4, 0\n" \
  "s_cbranch_scc1 LA_ev8\n" \
  "s_waitcnt vmcnt(0)\n" \
  "v_cvt_pk_u16_u32 v208, v208, v209\n" \
  "v_cvt_pk_u16_u32 v210, v210, v211\n" \
  "v_cvt_pk_u16_u32 v212, v212, v213\n" \
  "v_cvt_pk_u16_u32 v214, v214, v215\n" \
  "v_pk_lshrrev_b16 v209, 3, v208 op_sel_hi:[0,1]\n" \
  "v_pk_lshrrev_b16 v211, 3, v210 op_sel_hi:[0,1]\n" \
  "v_pk_lshrrev_b16 v213, 3, v212 op_sel_hi:[0,1]\n" \
  "v_pk_lshrrev_b16 v215, 3, v214 op_sel_hi:[0,1]\n" \
  "v_pk_mad_u16 v208, v208, 8, v209 op_sel_hi:[1,0,1]\n" \
  "v_pk_mad_u16 v210, v210, 8, v211 op_sel_hi:[1,0,1]\n" \
  "v_pk_mad_u16 v212, v212, 8, v213 op_sel_hi:[1,0,1]\n" \
  "v_pk_mad_u16 v214, v214, 8, v215 op_sel_hi:[1,0,1]\n" \
  "v_alignbit_b32 v212, v212, v212, v179\n" \
  "v_alignbit_b32 v214, v214, v214, v179\n" \
  "ds_write2_b32 v195, v208, v210 offset0:0 offset1:4\n" \
  "ds_write2_b32 v195, v212, v214 offset0:2 offset1:6\n" \
  "s_lshl_b32 s35, s4, 8\n" \
  "s_add_u32 s35, s35, 0x400\n" \
  "v_subrev_u32 v194, s35, v194\n" \
  "s_mov_b32 s51, s4\n" \
  "s_call_b64 s[52:53], LA_loop\n" \
  "LA_ev8:\n" \
  "v_mov_b32 v179, v178\n" \
  "ds_read_b128 v[100:103], v185 offset:64\n" \
  "v_mad_u32_u16 v186, v183, v191, v189\n" \
  "ds_read2_b32 v[144:145], v186 offset0:0 offset1:2\n" \
  "ds_read2_b32 v[146:147], v186 offset0:4 offset1:6\n" \
  "v_mad_u32_u16 v187, v183, v192, v190\n" \
  "ds_read2_b32 v[148:149], v187 offset0:0 offset1:2\n" \
  "ds_read2_b32 v[150:151], v187 offset0:4 offset1:6\n" \
  "s_lshr_b32 s35, s30, 2\n" \
  "s_lshl3_add_u32 s33, s29, s35\n" \
  "s_and_b32 s34, s30, 3\n" \
  "s_lshl_b32 s59, s27, 11\n" \
  "s_mul_i32 s35, s33, 1600000\n" \
  "s_add_u32 s54, s24, s35\n" \
  "s_addc_u32 s55, s25, 0\n" \
  "s_mul_i32 s35, s34, 819200\n" \
  "s_lshl1_add_u32 s35, s59, s35\n" \
  "s_add_u32 s35, s35, 102400000\n" \
  "s_add_u32 s56, s24, s35\n" \
  "s_addc_u32 s57, s25, 0\n" \
  "v_mbcnt_lo_u32_b32 v189, -1, 0\n" \
  "v_mbcnt_hi_u32_b32 v189, -1, v189\n" \
  "v_lshlrev_b32 v188, 4, v189\n" \
  "s_waitcnt lgkmcnt(5)\n" \
  "v_mfma_f32_16x16x32_bf16 v[152:155], v[112:115], v[112:115], 0\n" \
  "v_fmac_f32 v176, v156, v88\n" \
  "v_fmac_f32 v176, v157, v89\n" \
  "v_mfma_f32_16x16x32_bf16 v[152:155], v[116:119], v[116:119], v[152:155]\n" \
  "v_fmac_f32 v176, v158, v90\n" \
  "v_fmac_f32 v176, v159, v91\n" \
  "v_mfma_f32_16x16x32_bf16 v[156:159], v[128:131], v[128:131], 0\n" \
  "v_fmac_f32 v176, v160, v92\n" \
  "v_fmac_f32 v176, v161, v93\n" \
  "v_mfma_f32_16x16x32_bf16 v[156:159], v[132:135], v[132:135], v[156:159]\n" \
  "v_fmac_f32 v176, v162, v94\n" \
  "v_fma_f32 v177, v163, v95, v176\n" \
  "v_mfma_f32_16x16x32_bf16 v[160:163], v[128:131], v[136:139], 0\n" \
  "v_mul_f32 v176, v152, v0\n" \
  "v_fmac_f32 v176, v153, v1\n" \
  "v_add_f32_dpp v177, v177, v177 row_ror:8 row_mask:0xf bank_mask:0xf\n" \
  "v_mfma_f32_16x16x32_bf16 v[160:163], v[132:135], v[140:143], v[160:163]\n" \
  "v_fmac_f32 v176, v154, v2\n" \
  "v_fmac_f32 v176, v155, v3\n" \
  "v_mfma_f32_16x16x32_bf16 v[152:155], v[136:139], v[136:139], 0\n" \
  "v_fmac_f32 v176, v156, v4\n" \
  "v_fmac_f32 v176, v157, v5\n" \
  "v_mfma_f32_16x16x32_bf16 v[152:155], v[140:143], v[140:143], v[152:155]\n" \
  "v_fmac_f32 v176, v158, v6\n" \
  "v_fmac_f32 v176, v159, v7\n" \
  "s_waitcnt lgkmcnt(0)\n" \
  "s_setprio 3\n" \
  "s_barrier\n" \
  "global_load_dwordx4 v[208:211], v188, s[56:57] offset:0 sc1\n" \
  "global_load_dwordx4 v[212:215], v188, s[56:57] offset:1024 sc1\n" \
  "global_load_dwordx4 v[216:219], v188, s[56:57] offset:2048 sc1\n" \
  "global_load_dwordx4 v[220:223], v188, s[56:57] offset:3072 sc1\n" \
  "v_mfma_f32_16x16x32_bf16 v[156:159], v[112:115], v[136:139], 0\n" \
  "v_fmac_f32 v176, v160, v8\n" \
  "v_fmac_f32 v176, v161, v9\n" \
  "v_mov_b32_dpp v178, v178 row_ror:4 row_mask:0xf bank_mask:0xf\n" \
  "v_lshrrev_b32 v180, 1, v189\n" \
  "v_mfma_f32_16x16x32_bf16 v[156:159], v[116:119], v[140:143], v[156:159]\n" \
  "v_fmac_f32 v176, v162, v10\n" \
  "v_fmac_f32 v176, v163, v11\n" \
  "v_xor_b32 v180, v180, v189\n" \
  "v_mfma_f32_16x16x32_bf16 v[160:163], v[112:115], v[128:131], 0\n" \
  "v_fmac_f32 v176, v152, v12\n" \
  "v_fmac_f32 v176, v153, v13\n" \
  "v_and_b32 v180, 8, v180\n" \
  "v_mfma_f32_16x16x32_bf16 v[160:163], v[116:119], v[132:135], v[160:163]\n" \
  "v_fmac_f32 v176, v154, v14\n" \
  "v_fmac_f32 v176, v155, v15\n" \
  "v_mov_b64 v[174:175], 0\n" \
  "v_bfe_u32 v181, v189, 2, 1\n" \
  "v_mfma_f32_16x16x32_bf16 v[152:155], v[112:115], v[120:123], 0\n" \
  "v_fmac_f32 v176, v156, v16\n" \
  "v_fmac_f32 v176, v157, v17\n" \
  "v_mov_b64 v[170:171], 0\n" \
  "v_lshlrev_b32_e64 v181, v181, 5\n" \
  "v_mfma_f32_16x16x32_bf16 v[152:155], v[116:119], v[124:127], v[152:155]\n" \
  "v_fmac_f32 v176, v158, v18\n" \
  "v_fmac_f32 v176, v159, v19\n" \
  "v_lshlrev_b32 v180, v180, v181\n" \
  "v_mfma_f32_16x16x32_bf16 v[156:159], v[120:123], v[136:139], 0\n" \
  "v_fmac_f32 v176, v160, v20\n" \
  "v_fmac_f32 v176, v161, v21\n" \
  "v_bfe_u32 v181, v189, 2, 2\n" \
  "v_mfma_f32_16x16x32_bf16 v[156:159], v[124:127], v[140:143], v[156:159]\n" \
  "v_fmac_f32 v176, v162, v22\n" \
  "v_fmac_f32 v176, v163, v23\n" \
  "v_mov_b64 v[166:167], 0\n" \
  "v_lshlrev_b32_e64 v181, v181, 1\n" \
  "v_mfma_f32_16x16x32_bf16 v[160:163], v[104:107], v[128:131], 0\n" \
  "v_fmac_f32 v176, v152, v24\n" \
  "v_fmac_f32 v176, v153, v25\n" \
  "v_cmp_gt_u32 vcc, 32, v189\n" \
  "v_mfma_f32_16x16x32_bf16 v[160:163], v[108:111], v[132:135], v[160:163]\n" \
  "v_fmac_f32 v176, v154, v26\n" \
  "v_fmac_f32 v176, v155, v27\n" \
  "v_mov_b32 v191, 0x3f80\n" \
  "v_mfma_f32_16x16x32_bf16 v[152:155], v[120:123], v[128:131], 0\n" \
  "v_fmac_f32 v176, v156, v28\n" \
  "v_fmac_f32 v176, v157, v29\n" \
  "v_add_u32 v190, 32000, v188\n" \
  "v_mfma_f32_16x16x32_bf16 v[152:155], v[124:127], v[132:135], v[152:155]\n" \
  "v_fmac_f32 v176, v158, v30\n" \
  "v_fmac_f32 v176, v159, v31\n" \
  "v_add_f32_dpp v178, v177, v177 row_ror:4 row_mask:0xf bank_mask:0x1\n" \
  "v_cndmask_b32 v192, v181, v180, vcc\n" \
  "v_mfma_f32_16x16x32_bf16 v[156:159], v[96:99], v[128:131], 0\n" \
  "v_fmac_f32 v176, v160, v32\n" \
  "v_fmac_f32 v176, v161, v33\n" \
  "v_bfe_i32 v180, v192, 0, 1\n" \
  "v_mfma_f32_16x16x32_bf16 v[156:159], v[100:103], v[132:135], v[156:159]\n" \
  "v_fmac_f32 v176, v162, v34\n" \
  "v_fmac_f32 v176, v163, v35\n" \
  "v_and_b32 v180, v180, v191\n" \
  "v_smfmac_f32_16x16x64_bf16 v[172:175], v[144:147], v[128:135], v193\n" \
  "v_fmac_f32 v176, v152, v36\n" \
  "v_fmac_f32 v176, v153, v37\n" \
  "v_bfe_i32 v181, v192, 1, 1\n" \
  "v_smfmac_f32_16x16x64_bf16 v[172:175], v[148:151], v[136:143], v193\n" \
  "v_fmac_f32 v176, v154, v38\n" \
  "v_fmac_f32 v176, v155, v39\n" \
  "v_and_b32 v181, v181, v191\n" \
  "v_mfma_f32_16x16x32_bf16 v[160:163], v[96:99], v[112:115], 0\n" \
  "v_fmac_f32 v176, v156, v40\n" \
  "v_fmac_f32 v176, v157, v41\n" \
  "v_bfe_i32 v182, v192, 2, 1\n" \
  "v_mfma_f32_16x16x32_bf16 v[160:163], v[100:103], v[116:119], v[160:163]\n" \
  "v_fmac_f32 v176, v158, v42\n" \
  "v_fmac_f32 v176, v159, v43\n" \
  "v_and_b32 v182, v182, v191\n" \
  "v_mfma_f32_16x16x32_bf16 v[152:155], v[104:107], v[112:115], 0\n" \
  "v_fmac_f32 v176, v172, v44\n" \
  "v_fmac_f32 v176, v173, v45\n" \
  "v_bfe_i32 v183, v192, 3, 1\n" \
  "v_mfma_f32_16x16x32_bf16 v[152:155], v[108:111], v[116:119], v[152:155]\n" \
  "v_fmac_f32 v176, v174, v46\n" \
  "v_fmac_f32 v176, v175, v47\n" \
  "v_and_b32 v183, v183, v191\n" \
  "v_mfma_f32_16x16x32_bf16 v[156:159], v[104:107], v[136:139], 0\n" \
  "v_fmac_f32 v176, v160, v48\n" \
  "v_fmac_f32 v176, v161, v49\n" \
  "v_and_b32 v192, 3, v189\n" \
  "v_mfma_f32_16x16x32_bf16 v[156:159], v[108:111], v[140:143], v[156:159]\n" \
  "v_fmac_f32 v176, v162, v50\n" \
  "v_fmac_f32 v176, v163, v51\n" \
  "v_mul_u32_u24 v192, 0x5555, v192\n" \
  "v_smfmac_f32_16x16x64_bf16 v[168:171], v[144:147], v[112:119], v193\n" \
  "v_fmac_f32 v176, v152, v52\n" \
  "v_fmac_f32 v176, v153, v53\n" \
  "v_add_u32 v189, 16000, v188\n" \
  "v_smfmac_f32_16x16x64_bf16 v[168:171], v[148:151], v[120:127], v193\n" \
  "v_fmac_f32 v176, v154, v54\n" \
  "v_fmac_f32 v176, v155, v55\n" \
  "v_add_u32 v191, 48000, v188\n" \
  "v_mfma_f32_16x16x32_bf16 v[160:163], v[96:99], v[136:139], 0\n" \
  "v_fmac_f32 v176, v156, v56\n" \
  "v_fmac_f32 v176, v157, v57\n" \
  "v_mfma_f32_16x16x32_bf16 v[160:163], v[100:103], v[140:143], v[160:163]\n" \
  "v_fmac_f32 v176, v158, v58\n" \
  "v_fmac_f32 v176, v159, v59\n" \
  "s_add_u32 s35, s59, 64000\n" \
  "s_add_u32 s54, s54, s35\n" \
  "s_addc_u32 s55, s55, 0\n" \
  "s_add_u32 s58, s59, 0x10000\n" \
  "s_mov_b32 m0, s58\n" \
  "s_add_u32 s35, s58, 0x8000\n" \
  "global_load_lds_dwordx4 v188, s[54:55] sc1\n" \
  "global_load_lds_dwordx4 v188, s[54:55] offset:1024 sc1\n" \
  "v_smfmac_f32_16x16x64_bf16 v[164:167], v[148:151], v[104:111], v193\n" \
  "v_fmac_f32 v176, v168, v60\n" \
  "v_fmac_f32 v176, v169, v61\n" \
  "v_smfmac_f32_16x16x64_bf16 v[164:167], v[144:147], v[96:103], v193\n" \
  "v_fmac_f32 v176, v170, v62\n" \
  "v_fmac_f32 v176, v171, v63\n" \
  "v_mfma_f32_16x16x32_bf16 v[152:155], v[120:123], v[120:123], 0\n" \
  "v_fmac_f32 v176, v160, v64\n" \
  "v_fmac_f32 v176, v161, v65\n" \
  "v_mfma_f32_16x16x32_bf16 v[152:155], v[124:127], v[124:127], v[152:155]\n" \
  "v_fmac_f32 v176, v162, v66\n" \
  "v_fmac_f32 v176, v163, v67\n" \
  "v_mfma_f32_16x16x32_bf16 v[156:159], v[104:107], v[120:123], 0\n" \
  "v_fmac_f32 v176, v164, v68\n" \
  "v_fmac_f32 v176, v165, v69\n" \
  "v_mfma_f32_16x16x32_bf16 v[156:159], v[108:111], v[124:127], v[156:159]\n" \
  "v_fmac_f32 v176, v166, v70\n" \
  "v_fmac_f32 v176, v167, v71\n" \
  "s_add_u32 m0, s58, 0x4000\n" \
  "s_cmp_lt_u32 s35, 0x28000\n" \
  "global_load_lds_dwordx4 v189, s[54:55] sc1\n" \
  "global_load_lds_dwordx4 v189, s[54:55] offset:1024 sc1\n" \
  "s_cselect_b32 s58, s35, s59\n" \
  "s_add_u32 s54, s54, 32000\n" \
  "s_addc_u32 s55, s55, 0\n" \
  "v_mfma_f32_16x16x32_bf16 v[160:163], v[96:99], v[120:123], 0\n" \
  "v_fmac_f32 v176, v152, v72\n" \
  "v_fmac_f32 v176, v153, v73\n" \
  "v_mfma_f32_16x16x32_bf16 v[160:163], v[100:103], v[124:127], v[160:163]\n" \
  "v_fmac_f32 v176, v154, v74\n" \
  "v_fmac_f32 v176, v155, v75\n" \
  "v_mfma_f32_16x16x32_bf16 v[152:155], v[96:99], v[104:107], 0\n" \
  "v_fmac_f32 v176, v156, v76\n" \
  "v_fmac_f32 v176, v157, v77\n" \
  "v_mfma_f32_16x16x32_bf16 v[152:155], v[100:103], v[108:111], v[152:155]\n" \
  "v_fmac_f32 v176, v158, v78\n" \
  "v_fmac_f32 v176, v159, v79\n" \
  "v_mfma_f32_16x16x32_bf16 v[156:159], v[104:107], v[104:107], 0\n" \
  "v_fmac_f32 v176, v160, v80\n" \
  "v_fmac_f32 v176, v161, v81\n" \
  "v_mfma_f32_16x16x32_bf16 v[156:159], v[108:111], v[108:111], v[156:159]\n" \
  "v_fmac_f32 v176, v162, v82\n" \
  "v_fmac_f32 v176, v163, v83\n" \
  "v_mfma_f32_16x16x32_bf16 v[160:163], v[148:151], v[144:147], 0\n" \
  "v_fmac_f32 v176, v152, v84\n" \
  "v_fmac_f32 v176, v153, v85\n" \
  "v_mfma_f32_16x16x32_bf16 v[160:163], v[96:99], v[96:99], v[160:163]\n" \
  "v_fmac_f32 v176, v154, v86\n" \
  "v_fmac_f32 v176, v155, v87\n" \
  "v_mfma_f32_16x16x32_bf16 v[160:163], v[100:103], v[100:103], v[160:163]\n" \
  "s_mov_b32 s51, 7\n" \
  "v_fmac_f32 v176, v156, v88\n" \
  "v_fmac_f32 v176, v157, v89\n" \
  "v_fmac_f32 v176, v158, v90\n" \
  "v_fmac_f32 v176, v159, v91\n" \
  "s_nop 2\n" \
  "v_fmac_f32 v176, v160, v92\n" \
  "v_fmac_f32 v176, v161, v93\n" \
  "v_fmac_f32 v176, v162, v94\n" \
  "v_fma_f32 v177, v163, v95, v176\n" \
  "s_nop 1\n" \
  "v_add_f32_dpp v177, v177, v177 row_ror:8 row_mask:0xf bank_mask:0xf\n" \
  "v_mov_b32_dpp v178, v178 row_ror:4 row_mask:0xf bank_mask:0xf\n" \
  "s_nop 1\n" \
  "v_add_f32_dpp v178, v177, v177 row_ror:4 row_mask:0xf bank_mask:0x1\n" \
  "v_mov_b32 v99, v179\n" \
  "s_nop 0\n" \
  "v_mov_b32_dpp v196, v178 row_ror:8 row_mask:0xf bank_mask:0xc\n" \
  "s_nop 1\n" \
  "v_permlane16_swap_b32 v196, v197\n" \
  "v_permlane16_swap_b32 v198, v199\n" \
  "v_permlane16_swap_b32 v200, v201\n" \
  "v_permlane16_swap_b32 v202, v203\n" \
  "v_permlane16_swap_b32 v179, v99\n" \
  "v_add_f32 v196, v196, v197\n" \
  "v_add_f32 v198, v198, v199\n" \
  "v_add_f32 v200, v200, v201\n" \
  "v_add_f32 v202, v202, v203\n" \
  "v_add_f32 v179, v179, v99\n" \
  "v_lshrrev_b32 v96, 4, v188\n" \
  "v_and_b32 v97, 1, v96\n" \
  "v_mov_b32 v99, v179\n" \
  "v_lshrrev_b32 v98, 4, v96\n" \
  "v_permlane32_swap_b32 v196, v198\n" \
  "v_permlane32_swap_b32 v200, v202\n" \
  "v_permlane32_swap_b32 v179, v99\n" \
  "v_add_f32 v196, v196, v198\n" \
  "v_add_f32 v200, v200, v202\n" \
  "v_add_f32 v179, v179, v99\n" \
  "v_cmp_eq_u32 vcc, 1, v97\n" \
  "v_lshlrev_b32 v97, 2, v97\n" \
  "v_add_lshl_u32 v97, v97, v98, 2\n" \
  "v_bfe_u32 v98, v96, 2, 2\n" \
  "v_add_f32_dpp v196, v196, v196 quad_perm:[1,0,3,2] row_mask:0xf bank_mask:0xf\n" \
  "v_add_f32_dpp v200, v200, v200 quad_perm:[1,0,3,2] row_mask:0xf bank_mask:0xf\n" \
  "v_add_f32_dpp v179, v179, v179 quad_perm:[1,0,3,2] row_mask:0xf bank_mask:0xf\n" \
  "v_sub_u32 v97, v97, v98\n" \
  "v_cndmask_b32 v196, v196, v200, vcc\n" \
  "v_add_u32 v97, 1, v97\n" \
  "s_min_i32 s35, s4, 0\n" \
  "s_add_u32 s36, s4, 32\n" \
  "s_add_u32 s37, s4, 29\n" \
  "v_add_f32_dpp v179, v179, v179 quad_perm:[2,3,0,1] row_mask:0xf bank_mask:0xf\n" \
  "v_add_f32_dpp v196, v196, v196 quad_perm:[2,3,0,1] row_mask:0xf bank_mask:0xf\n" \
  "s_mov_b32 exec_lo, 0x0\n" \
  "s_mov_b32 exec_hi, 0xaaaa0000\n" \
  "v_add_u32 v97, s35, v97\n" \
  "s_mov_b32 exec_lo, 0x5500\n" \
  "s_mov_b32 exec_hi, 0x0\n" \
  "v_add_u32 v97, s36, v97\n" \
  "s_mov_b32 exec_lo, 0x4444\n" \
  "s_mov_b32 exec_hi, 0x0\n" \
  "v_sub_u32 v97, s37, v98\n" \
  "v_mov_b32 v196, v179\n" \
  "s_mov_b64 exec, -1\n" \
  "v_cmp_lt_u32 vcc, 31, v97\n" \
  "v_add_u32 v98, s5, v97\n" \
  "s_lshl2_add_u32 s35, s7, s28\n" \
  "s_lshl_b32 s35, s35, 5\n" \
  "v_cndmask_b32 v97, v97, v98, vcc\n" \
  "v_add_lshl_u32 v97, v97, s35, 2\n" \
  "s_lshl_b32 s35, s31, 16\n" \
  "s_add_u32 s35, s35, 109871104\n" \
  "s_add_u32 s40, s24, s35\n" \
  "s_addc_u32 s41, s25, 0\n" \
  "v_min_u32 v196, -2, v196\n" \
  "v_lshlrev_b32 v184, 16, v204\n" \
  "v_and_b32 v185, 0xffff0000, v204\n" \
  "v_lshlrev_b32 v186, 16, v205\n" \
  "v_and_b32 v187, 0xffff0000, v205\n" \
  "s_barrier\n" \
  "s_add_u32 s56, s56, 32768\n" \
  "s_addc_u32 s57, s57, 0\n" \
  "global_load_dwordx4 v[224:227], v188, s[56:57] sc1\n" \
  "global_load_dwordx4 v[228:231], v188, s[56:57] offset:1024 sc1\n" \
  "global_load_dwordx4 v[232:235], v188, s[56:57] offset:2048 sc1\n" \
  "global_load_dwordx4 v[236:239], v188, s[56:57] offset:3072 sc1\n" \
  "s_add_u32 s56, s56, 32768\n" \
  "s_addc_u32 s57, s57, 0\n" \
  "s_mov_b32 exec_lo, 0x33337777\n" \
  "s_mov_b32 exec_hi, 0x33333333\n" \
  "global_store_dword v97, v196, s[40:41] sc1\n" \
  "s_mov_b64 exec, -1\n" \
  "s_waitcnt vmcnt(9)\n" \
  "s_setprio 0\n" \
  "s_branch LG_start\n" \
  "LA_loop:\n" \
  "v_mfma_f32_16x16x32_bf16 v[152:155], v[112:115], v[112:115], 0\n" \
  "s_waitcnt lgkmcnt(7)\n" \
  "ds_read_b128 v[100:103], v185 offset:64\n" \
  "v_fmac_f32 v176, v156, v88\n" \
  "v_fmac_f32 v176, v157, v89\n" \
  "v_mad_u32_u16 v186, v183, v191, v189\n" \
  "v_mfma_f32_16x16x32_bf16 v[152:155], v[116:119], v[116:119], v[152:155]\n" \
  "ds_read2_b32 v[144:145], v186 offset0:0 offset1:2\n" \
  "v_fmac_f32 v176, v158, v90\n" \
  "v_fmac_f32 v176, v159, v91\n" \
  "v_mfma_f32_16x16x32_bf16 v[156:159], v[128:131], v[128:131], 0\n" \
  "ds_read2_b32 v[146:147], v186 offset0:4 offset1:6\n" \
  "v_fmac_f32 v176, v160, v92\n" \
  "v_fmac_f32 v176, v161, v93\n" \
  "v_mad_u32_u16 v187, v183, v192, v190\n" \
  "v_mfma_f32_16x16x32_bf16 v[156:159], v[132:135], v[132:135], v[156:159]\n" \
  "s_waitcnt lgkmcnt(9)\n" \
  "ds_read2_b32 v[148:149], v187 offset0:0 offset1:2\n" \
  "v_fmac_f32 v176, v162, v94\n" \
  "v_fma_f32 v177, v163, v95, v176\n" \
  "v_mfma_f32_16x16x32_bf16 v[160:163], v[128:131], v[136:139], 0\n" \
  "s_waitcnt lgkmcnt(9)\n" \
  "ds_read2_b32 v[150:151], v187 offset0:4 offset1:6\n" \
  "v_mul_f32 v176, v152, v0\n" \
  "v_fmac_f32 v176, v153, v1\n" \
  "v_add_f32_dpp v177, v177, v177 row_ror:8 row_mask:0xf bank_mask:0xf\n" \
  "v_mfma_f32_16x16x32_bf16 v[160:163], v[132:135], v[140:143], v[160:163]\n" \
  "ds_read_b128 v[180:183], v194\n" \
  "v_fmac_f32 v176, v154, v2\n" \
  "v_fmac_f32 v176, v155, v3\n" \
  "v_mfma_f32_16x16x32_bf16 v[152:155], v[136:139], v[136:139], 0\n" \
  "v_fmac_f32 v176, v156, v4\n" \
  "v_fmac_f32 v176, v157, v5\n" \
  "v_mfma_f32_16x16x32_bf16 v[152:155], v[140:143], v[140:143], v[152:155]\n" \
  "v_fmac_f32 v176, v158, v6\n" \
  "v_fmac_f32 v176, v159, v7\n" \
  "v_add_u32 v194, 0x100, v194\n" \
  "v_mfma_f32_16x16x32_bf16 v[156:159], v[112:115], v[136:139], 0\n" \
  "v_fmac_f32 v176, v160, v8\n" \
  "v_fmac_f32 v176, v161, v9\n" \
  "v_mov_b32_dpp v178, v178 row_ror:4 row_mask:0xf bank_mask:0xf\n" \
  "v_mfma_f32_16x16x32_bf16 v[156:159], v[116:119], v[140:143], v[156:159]\n" \
  "v_fmac_f32 v176, v162, v10\n" \
  "v_fmac_f32 v176, v163, v11\n" \
  "v_mfma_f32_16x16x32_bf16 v[160:163], v[112:115], v[128:131], 0\n" \
  "v_fmac_f32 v176, v152, v12\n" \
  "v_fmac_f32 v176, v153, v13\n" \
  "v_mfma_f32_16x16x32_bf16 v[160:163], v[116:119], v[132:135], v[160:163]\n" \
  "s_waitcnt lgkmcnt(10)\n" \
  "v_fmac_f32 v176, v154, v14\n" \
  "v_fmac_f32 v176, v155, v15\n" \
  "v_mov_b64 v[174:175], 0\n" \
  "v_mfma_f32_16x16x32_bf16 v[152:155], v[112:115], v[120:123], 0\n" \
  "s_waitcnt lgkmcnt(9)\n" \
  "v_fmac_f32 v176, v156, v16\n" \
  "v_fmac_f32 v176, v157, v17\n" \
  "v_mov_b64 v[170:171], 0\n" \
  "v_mfma_f32_16x16x32_bf16 v[152:155], v[116:119], v[124:127], v[152:155]\n" \
  "v_fmac_f32 v176, v158, v18\n" \
  "v_fmac_f32 v176, v159, v19\n" \
  "v_mfma_f32_16x16x32_bf16 v[156:159], v[120:123], v[136:139], 0\n" \
  "v_fmac_f32 v176, v160, v20\n" \
  "v_fmac_f32 v176, v161, v21\n" \
  "v_mfma_f32_16x16x32_bf16 v[156:159], v[124:127], v[140:143], v[156:159]\n" \
  "s_waitcnt lgkmcnt(8)\n" \
  "v_fmac_f32 v176, v162, v22\n" \
  "v_fmac_f32 v176, v163, v23\n" \
  "v_mov_b64 v[166:167], 0\n" \
  "v_mfma_f32_16x16x32_bf16 v[160:163], v[104:107], v[128:131], 0\n" \
  "s_waitcnt lgkmcnt(0)\n" \
  "v_fmac_f32 v176, v152, v24\n" \
  "v_fmac_f32 v176, v153, v25\n" \
  "v_mad_u32_u16 v184, v182, 16, v188\n" \
  "v_mfma_f32_16x16x32_bf16 v[160:163], v[108:111], v[132:135], v[160:163]\n" \
  "v_fmac_f32 v176, v154, v26\n" \
  "v_fmac_f32 v176, v155, v27\n" \
  "v_mfma_f32_16x16x32_bf16 v[152:155], v[120:123], v[128:131], 0\n" \
  "v_fmac_f32 v176, v156, v28\n" \
  "v_fmac_f32 v176, v157, v29\n" \
  "v_mfma_f32_16x16x32_bf16 v[152:155], v[124:127], v[132:135], v[152:155]\n" \
  "v_fmac_f32 v176, v158, v30\n" \
  "v_fmac_f32 v176, v159, v31\n" \
  "v_add_f32_dpp v178, v177, v177 row_ror:4 row_mask:0xf bank_mask:0x1\n" \
  "v_mfma_f32_16x16x32_bf16 v[156:159], v[96:99], v[128:131], 0\n" \
  "v_fmac_f32 v176, v160, v32\n" \
  "v_fmac_f32 v176, v161, v33\n" \
  "v_mfma_f32_16x16x32_bf16 v[156:159], v[100:103], v[132:135], v[156:159]\n" \
  "v_fmac_f32 v176, v162, v34\n" \
  "v_fmac_f32 v176, v163, v35\n" \
  "v_smfmac_f32_16x16x64_bf16 v[172:175], v[144:147], v[128:135], v193\n" \
  "ds_read_b128 v[128:131], v184 offset:0\n" \
  "v_fmac_f32 v176, v152, v36\n" \
  "v_fmac_f32 v176, v153, v37\n" \
  "v_smfmac_f32_16x16x64_bf16 v[172:175], v[148:151], v[136:143], v193\n" \
  "ds_read_b128 v[132:135], v184 offset:64\n" \
  "v_fmac_f32 v176, v154, v38\n" \
  "v_fmac_f32 v176, v155, v39\n" \
  "v_mfma_f32_16x16x32_bf16 v[160:163], v[96:99], v[112:115], 0\n" \
  "v_fmac_f32 v176, v156, v40\n" \
  "v_fmac_f32 v176, v157, v41\n" \
  "v_mfma_f32_16x16x32_bf16 v[160:163], v[100:103], v[116:119], v[160:163]\n" \
  "v_fmac_f32 v176, v158, v42\n" \
  "v_fmac_f32 v176, v159, v43\n" \
  "v_mfma_f32_16x16x32_bf16 v[152:155], v[104:107], v[112:115], 0\n" \
  "v_fmac_f32 v176, v172, v44\n" \
  "v_fmac_f32 v176, v173, v45\n" \
  "v_mad_u32_u16 v185, v181, 16, v188\n" \
  "v_mfma_f32_16x16x32_bf16 v[152:155], v[108:111], v[116:119], v[152:155]\n" \
  "v_fmac_f32 v176, v174, v46\n" \
  "v_fmac_f32 v176, v175, v47\n" \
  "v_mov_b64 v[172:173], 0\n" \
  "v_mfma_f32_16x16x32_bf16 v[156:159], v[104:107], v[136:139], 0\n" \
  "v_fmac_f32 v176, v160, v48\n" \
  "v_fmac_f32 v176, v161, v49\n" \
  "v_mad_u32_u16 v186, v182, 16, v188 op_sel:[1,0,0,0]\n" \
  "v_mfma_f32_16x16x32_bf16 v[156:159], v[108:111], v[140:143], v[156:159]\n" \
  "v_fmac_f32 v176, v162, v50\n" \
  "v_fmac_f32 v176, v163, v51\n" \
  "v_smfmac_f32_16x16x64_bf16 v[168:171], v[144:147], v[112:119], v193\n" \
  "ds_read_b128 v[112:115], v185 offset:0\n" \
  "v_fmac_f32 v176, v152, v52\n" \
  "v_fmac_f32 v176, v153, v53\n" \
  "v_smfmac_f32_16x16x64_bf16 v[168:171], v[148:151], v[120:127], v193\n" \
  "ds_read_b128 v[116:119], v185 offset:64\n" \
  "v_fmac_f32 v176, v154, v54\n" \
  "v_fmac_f32 v176, v155, v55\n" \
  "v_mfma_f32_16x16x32_bf16 v[160:163], v[96:99], v[136:139], 0\n" \
  "v_fmac_f32 v176, v156, v56\n" \
  "v_fmac_f32 v176, v157, v57\n" \
  "v_mfma_f32_16x16x32_bf16 v[160:163], v[100:103], v[140:143], v[160:163]\n" \
  "v_fmac_f32 v176, v158, v58\n" \
  "v_fmac_f32 v176, v159, v59\n" \
  "v_smfmac_f32_16x16x64_bf16 v[164:167], v[148:151], v[104:111], v193\n" \
  "ds_read_b128 v[136:139], v186 offset:0\n" \
  "v_fmac_f32 v176, v168, v60\n" \
  "v_fmac_f32 v176, v169, v61\n" \
  "v_smfmac_f32_16x16x64_bf16 v[164:167], v[144:147], v[96:103], v193\n" \
  "ds_read_b128 v[140:143], v186 offset:64\n" \
  "v_fmac_f32 v176, v170, v62\n" \
  "v_fmac_f32 v176, v171, v63\n" \
  "v_mfma_f32_16x16x32_bf16 v[152:155], v[120:123], v[120:123], 0\n" \
  "v_fmac_f32 v176, v160, v64\n" \
  "v_fmac_f32 v176, v161, v65\n" \
  "v_mfma_f32_16x16x32_bf16 v[152:155], v[124:127], v[124:127], v[152:155]\n" \
  "v_fmac_f32 v176, v162, v66\n" \
  "v_fmac_f32 v176, v163, v67\n" \
  "v_mad_u32_u16 v187, v181, 16, v188 op_sel:[1,0,0,0]\n" \
  "v_mfma_f32_16x16x32_bf16 v[156:159], v[104:107], v[120:123], 0\n" \
  "v_fmac_f32 v176, v164, v68\n" \
  "v_fmac_f32 v176, v165, v69\n" \
  "v_mov_b64 v[168:169], 0\n" \
  "v_mfma_f32_16x16x32_bf16 v[156:159], v[108:111], v[124:127], v[156:159]\n" \
  "v_fmac_f32 v176, v166, v70\n" \
  "v_fmac_f32 v176, v167, v71\n" \
  "v_mfma_f32_16x16x32_bf16 v[160:163], v[96:99], v[120:123], 0\n" \
  "ds_read_b128 v[120:123], v187 offset:0\n" \
  "v_fmac_f32 v176, v152, v72\n" \
  "v_fmac_f32 v176, v153, v73\n" \
  "v_mfma_f32_16x16x32_bf16 v[160:163], v[100:103], v[124:127], v[160:163]\n" \
  "v_fmac_f32 v176, v154, v74\n" \
  "v_fmac_f32 v176, v155, v75\n" \
  "v_mov_b64 v[164:165], 0\n" \
  "v_mfma_f32_16x16x32_bf16 v[152:155], v[96:99], v[104:107], 0\n" \
  "ds_read_b128 v[124:127], v187 offset:64\n" \
  "v_fmac_f32 v176, v156, v76\n" \
  "v_fmac_f32 v176, v157, v77\n" \
  "v_mad_u32_u16 v184, v180, 16, v188 op_sel:[1,0,0,0]\n" \
  "v_mfma_f32_16x16x32_bf16 v[152:155], v[100:103], v[108:111], v[152:155]\n" \
  "v_fmac_f32 v176, v158, v78\n" \
  "v_fmac_f32 v176, v159, v79\n" \
  "v_mfma_f32_16x16x32_bf16 v[156:159], v[104:107], v[104:107], 0\n" \
  "ds_read_b128 v[104:107], v184 offset:0\n" \
  "v_fmac_f32 v176, v160, v80\n" \
  "v_fmac_f32 v176, v161, v81\n" \
  "v_mfma_f32_16x16x32_bf16 v[156:159], v[108:111], v[108:111], v[156:159]\n" \
  "s_sub_u32 s51, s51, 1\n" \
  "v_fmac_f32 v176, v162, v82\n" \
  "v_fmac_f32 v176, v163, v83\n" \
  "v_mfma_f32_16x16x32_bf16 v[160:163], v[148:151], v[144:147], 0\n" \
  "s_cmp_lg_u32 s51, 0\n" \
  "ds_read_b128 v[108:111], v184 offset:64\n" \
  "v_fmac_f32 v176, v152, v84\n" \
  "v_fmac_f32 v176, v153, v85\n" \
  "v_mad_u32_u16 v185, v180, 16, v188\n" \
  "v_mfma_f32_16x16x32_bf16 v[160:163], v[96:99], v[96:99], v[160:163]\n" \
  "s_waitcnt lgkmcnt(7)\n" \
  "ds_read_b128 v[96:99], v185 offset:0\n" \
  "v_fmac_f32 v176, v154, v86\n" \
  "v_fmac_f32 v176, v155, v87\n" \
  "v_mfma_f32_16x16x32_bf16 v[160:163], v[100:103], v[100:103], v[160:163]\n" \
  "s_cbranch_scc1 LA_loop\n" \
  "s_setpc_b64 s[52:53]\n" \
  "LB_start:\n" \
  "s_setprio 3\n" \
  "s_mov_b32 s37, 12\n" \
  "L3_:\n" \
  "s_sub_u32 s35, s36, 125\n" \
  "s_min_u32 s35, s35, s36\n" \
  "s_mul_i32 m0, s35, 1040\n" \
  "s_lshl_b32 s35, s35, 11\n" \
  "s_add_u32 s42, s40, s35\n" \
  "s_addc_u32 s43, s41, 0\n" \
  "global_load_lds_dwordx4 v254, s[42:43]\n" \
  "s_add_u32 s36, s36, 4\n" \
  "s_sub_u32 s37, s37, 1\n" \
  "s_cmp_lg_u32 s37, 0\n" \
  "s_cbranch_scc1 L3_\n" \
  "v_mov_b32 v190, 163328\n" \
  "v_mov_b64 v[2:3], 0\n" \
  "ds_write_b64 v190, v[2:3]\n" \
  "ds_write_b64 v190, v[2:3] offset:8\n" \
  "ds_write_b64 v190, v[2:3] offset:16\n" \
  "ds_write_b64 v190, v[2:3] offset:32\n" \
  "ds_write_b64 v190, v[2:3] offset:40\n" \
  "s_waitcnt lgkmcnt(0)\n" \
  "s_barrier\n" \
  "v_lshlrev_b32 v247, 7, v255\n" \
  "s_mov_b32 s37, 20\n" \
  "L4_:\n" \
  "s_sub_u32 s35, s36, 125\n" \
  "s_min_u32 s35, s35, s36\n" \
  "s_mul_i32 m0, s35, 1040\n" \
  "s_lshl_b32 s35, s35, 11\n" \
  "s_add_u32 s42, s40, s35\n" \
  "s_addc_u32 s43, s41, 0\n" \
  "global_load_lds_dwordx4 v254, s[42:43]\n" \
  "s_add_u32 s36, s36, 4\n" \
  "s_sub_u32 s37, s37, 1\n" \
  "s_cmp_lg_u32 s37, 0\n" \
  "s_cbranch_scc1 L4_\n" \
  "s_add_u32 s48, s24, 110002176\n" \
  "s_addc_u32 s49, s25, 0\n" \
  "s_lshl_b32 s35, s26, 2\n" \
  "v_mov_b32 v193, s35\n" \
  "global_load_dword v193, v193, s[48:49] offset:1024 sc1\n" \
  "v_mov_b32 v191, 1\n" \
  "v_and_b32 v252, 15, v255\n" \
  "v_lshrrev_b32 v253, 4, v255\n" \
  "s_lshl2_add_u32 s35, s31, s28\n" \
  "s_lshl_b32 s35, s35, 1\n" \
  "v_bfe_u32 v1, v255, 2, 1\n" \
  "v_add_u32 v1, s35, v1\n" \
  "v_and_b32 v2, 3, v255\n" \
  "v_lshlrev_b32 v3, 5, v1\n" \
  "v_lshl_add_u32 v3, v2, 3, v3\n" \
  "v_lshlrev_b32 v4, 2, v2\n" \
  "v_add_u32 v4, 380, v4\n" \
  "v_cmp_gt_u32 vcc, 12, v1\n" \
  "v_lshrrev_b32 v5, 3, v255\n" \
  "v_mul_u32_u24 v5, 6400, v5\n" \
  "v_cndmask_b32 v3, v4, v3, vcc\n" \
  "v_add_u32 v5, 3200, v5\n" \
  "v_add_u32 v227, v5, v3\n" \
  "v_lshl_add_u32 v3, v1, 2, v2\n" \
  "v_add_u32 v4, 48, v3\n" \
  "v_mul_u32_u24 v5, 205, v3\n" \
  "v_lshrrev_b32 v5, 11, v5\n" \
  "v_mul_u32_u24 v5, 10, v5\n" \
  "v_sub_u32 v5, v3, v5\n" \
  "v_lshlrev_b32 v236, 26, v5\n" \
  "v_mul_u32_u24 v5, 205, v4\n" \
  "v_lshrrev_b32 v5, 11, v5\n" \
  "v_mul_u32_u24 v5, 10, v5\n" \
  "v_sub_u32 v5, v4, v5\n" \
  "v_lshlrev_b32 v237, 26, v5\n" \
  "s_lshl2_add_u32 s4, s31, s28\n" \
  "s_lshl_b32 s4, s4, 7\n" \
  "s_add_u32 s36, s4, 96\n" \
  "s_min_u32 s36, s36, 984\n" \
  "s_add_u32 s37, s4, 112\n" \
  "s_min_u32 s37, s37, 984\n" \
  "v_add_u32 v186, s4, v252\n" \
  "v_add_u32 v187, s36, v252\n" \
  "v_add_u32 v188, s37, v252\n" \
  "s_mul_i32 s38, s7, 51200\n" \
  "s_add_u32 s42, s8, s38\n" \
  "s_addc_u32 s43, s9, 0\n" \
  "s_xor_b32 s38, s31, 1\n" \
  "s_lshl_b32 s38, s38, 7\n" \
  "s_add_u32 s94, s10, s38\n" \
  "s_addc_u32 s95, s11, 0\n" \
  "s_mul_i32 s5, s28, 4224\n" \
  "s_add_u32 s5, s5, 146432\n" \
  "v_lshrrev_b32 v2, 3, v186\n" \
  "v_lshl_add_u32 v2, v186, 3, v2\n" \
  "v_add_lshl_u32 v249, v2, v253, 4\n" \
  "v_lshrrev_b32 v2, 3, v187\n" \
  "v_lshl_add_u32 v2, v187, 3, v2\n" \
  "v_add_lshl_u32 v250, v2, v253, 4\n" \
  "v_lshrrev_b32 v2, 3, v188\n" \
  "v_lshl_add_u32 v2, v188, 3, v2\n" \
  "v_add_lshl_u32 v251, v2, v253, 4\n" \
  "v_mul_u32_u24 v1, 1600000, v253\n" \
  "v_lshl_add_u32 v186, v186, 4, v1\n" \
  "v_lshl_add_u32 v187, v187, 4, v1\n" \
  "v_lshl_add_u32 v188, v188, 4, v1\n" \
  "v_lshlrev_b32 v1, 4, v253\n" \
  "v_xor_b32 v244, v1, v254\n" \
  "v_lshl_add_u32 v245, v255, 4, s5\n" \
  "v_lshrrev_b32 v1, 1, v252\n" \
  "v_xor_b32 v1, v1, v253\n" \
  "v_lshlrev_b32 v1, 4, v1\n" \
  "v_lshl_add_u32 v1, v252, 7, v1\n" \
  "v_add_u32 v246, s5, v1\n" \
  "v_xor_b32 v1, 64, v1\n" \
  "v_add_u32 v242, s5, v1\n" \
  "v_lshlrev_b32 v1, 4, v255\n" \
  "v_xor_b32 v1, 64, v1\n" \
  "v_add_u32 v243, s5, v1\n" \
  "v_lshrrev_b32 v1, 2, v255\n" \
  "s_lshl_b32 s35, s28, 5\n" \
  "s_lshl_b32 s36, s31, 6\n" \
  "s_add_u32 s35, s35, s36\n" \
  "s_and_b32 s35, s35, 127\n" \
  "v_add_u32 v1, s35, v1\n" \
  "v_lshrrev_b32 v2, 4, v255\n" \
  "v_and_b32 v2, 2, v2\n" \
  "v_xor_b32 v2, v2, v255\n" \
  "v_and_b32 v2, 3, v2\n" \
  "v_lshlrev_b32 v2, 4, v2\n" \
  "v_lshl_add_u32 v189, v1, 10, v2\n" \
  "v_add_u32 v194, 15360, v189\n" \
  "v_lshrrev_b32 v1, 2, v252\n" \
  "v_lshlrev_b32 v1, 6, v1\n" \
  "v_lshl_add_u32 v1, v253, 9, v1\n" \
  "v_and_b32 v2, 3, v252\n" \
  "v_lshl_add_u32 v1, v2, 3, v1\n" \
  "v_and_b32 v2, 1, v253\n" \
  "v_lshl_add_u32 v1, v2, 5, v1\n" \
  "v_add_u32 v184, 138240, v1\n" \
  "v_xor_b32 v1, 32, v1\n" \
  "v_add_u32 v185, 138240, v1\n" \
  "s_bfe_u32 s35, s30, 0x10001\n" \
  "s_lshl1_add_u32 s35, s29, s35\n" \
  "s_lshr_b32 s6, s30, 2\n" \
  "s_lshl_b32 s6, s6, 18\n" \
  "s_lshl_b32 s38, s35, 6\n" \
  "s_add_u32 s92, s14, s38\n" \
  "s_addc_u32 s93, s15, 0\n" \
  "s_lshr_b32 s37, s30, 3\n" \
  "s_add_u32 s37, s37, 96\n" \
  "s_lshl_b32 s34, s37, 17\n" \
  "s_mul_i32 s35, s35, 400\n" \
  "s_add_u32 s37, s37, s35\n" \
  "s_mul_i32 s37, s37, 16000\n" \
  "s_add_u32 s46, s24, s37\n" \
  "s_addc_u32 s47, s25, 0\n" \
  "s_lshr_b32 s36, s30, 2\n" \
  "s_add_u32 s36, s36, s35\n" \
  "s_mul_i32 s36, s36, 16000\n" \
  "s_add_u32 s44, s24, s36\n" \
  "s_addc_u32 s45, s25, 0\n" \
  "s_lshl_b32 s96, s28, 11\n" \
  "s_add_u32 s96, s96, 138240\n" \
  "v_lshlrev_b32 v238, 4, v255\n" \
  "v_mov_b32 v234, 0\n" \
  "s_lshl2_add_u32 s33, s26, s28\n" \
  "s_mov_b32 s52, 0x10002\n" \
  "s_mov_b32 s53, 0xc0c0400\n" \
  "s_mov_b32 s54, 0x4000c0c\n" \
  "s_mov_b32 s55, 0x3030303\n" \
  "s_mov_b32 s56, 0x55555555\n" \
  "s_mov_b32 s38, 0x0\n" \
  "s_mov_b32 s98, 0x0\n" \
  "s_mov_b32 s99, 0x8\n" \
  "s_mov_b32 s97, 0x2000\n" \
  "s_add_u32 s35, s4, 0\n" \
  "s_min_u32 s35, s35, 984\n" \
  "s_lshl_b32 s35, s35, 8\n" \
  "s_add_u32 s58, s94, s35\n" \
  "s_addc_u32 s59, s95, 0\n" \
  "global_load_dwordx4 v[8:11], v244, s[58:59] offset:0\n" \
  "global_load_dwordx4 v[12:15], v244, s[58:59] offset:2048\n" \
  "s_add_u32 s35, s4, 16\n" \
  "s_min_u32 s35, s35, 984\n" \
  "s_lshl_b32 s35, s35, 8\n" \
  "s_add_u32 s58, s94, s35\n" \
  "s_addc_u32 s59, s95, 0\n" \
  "global_load_dwordx4 v[24:27], v244, s[58:59] offset:0\n" \
  "global_load_dwordx4 v[28:31], v244, s[58:59] offset:2048\n" \
  "s_add_u32 s35, s4, 32\n" \
  "s_min_u32 s35, s35, 984\n" \
  "s_lshl_b32 s35, s35, 8\n" \
  "s_add_u32 s58, s94, s35\n" \
  "s_addc_u32 s59, s95, 0\n" \
  "global_load_dwordx4 v[40:43], v244, s[58:59] offset:0\n" \
  "global_load_dwordx4 v[44:47], v244, s[58:59] offset:2048\n" \
  "s_add_u32 s35, s4, 48\n" \
  "s_min_u32 s35, s35, 984\n" \
  "s_lshl_b32 s35, s35, 8\n" \
  "s_add_u32 s58, s94, s35\n" \
  "s_addc_u32 s59, s95, 0\n" \
  "global_load_dwordx4 v[56:59], v244, s[58:59] offset:0\n" \
  "global_load_dwordx4 v[60:63], v244, s[58:59] offset:2048\n" \
  "s_waitcnt vmcnt(8)\n" \
  "s_mov_b64 exec, 1\n" \
  "ds_add_u32 v190, v191 offset:12\n" \
  "s_mov_b64 exec, -1\n" \
  "s_memtime s[88:89]\n" \
  "s_setprio 2\n" \
  "s_add_u32 s35, s4, 64\n" \
  "s_min_u32 s35, s35, 984\n" \
  "s_lshl_b32 s35, s35, 8\n" \
  "s_add_u32 s58, s94, s35\n" \
  "s_addc_u32 s59, s95, 0\n" \
  "global_load_dwordx4 v[72:75], v244, s[58:59] offset:0\n" \
  "global_load_dwordx4 v[76:79], v244, s[58:59] offset:2048\n" \
  "s_add_u32 s35, s4, 80\n" \
  "s_min_u32 s35, s35, 984\n" \
  "s_lshl_b32 s35, s35, 8\n" \
  "s_add_u32 s58, s94, s35\n" \
  "s_addc_u32 s59, s95, 0\n" \
  "global_load_dwordx4 v[88:91], v244, s[58:59] offset:0\n" \
  "global_load_dwordx4 v[92:95], v244, s[58:59] offset:2048\n" \
  "s_add_u32 s35, s4, 96\n" \
  "s_min_u32 s35, s35, 984\n" \
  "s_lshl_b32 s35, s35, 8\n" \
  "s_add_u32 s58, s94, s35\n" \
  "s_addc_u32 s59, s95, 0\n" \
  "global_load_dwordx4 v[104:107], v244, s[58:59] offset:0\n" \
  "global_load_dwordx4 v[108:111], v244, s[58:59] offset:2048\n" \
  "s_add_u32 s35, s4, 112\n" \
  "s_min_u32 s35, s35, 984\n" \
  "s_lshl_b32 s35, s35, 8\n" \
  "s_add_u32 s58, s94, s35\n" \
  "s_addc_u32 s59, s95, 0\n" \
  "global_load_dwordx4 v[120:123], v244, s[58:59] offset:0\n" \
  "global_load_dwordx4 v[124:127], v244, s[58:59] offset:2048\n" \
  "L5_:\n" \
  "ds_read_b32 v192, v190 offset:16\n" \
  "s_waitcnt lgkmcnt(0)\n" \
  "v_readfirstlane_b32 s35, v192\n" \
  "s_cmp_lt_u32 s35, 4\n" \
  "s_cbranch_scc1 L5_\n" \
  "s_add_u32 s40, s92, s6\n" \
  "s_addc_u32 s41, s93, 0\n" \
  "s_add_u32 s6, s6, 2097152\n" \
  "s_mov_b32 m0, s96\n" \
  "s_nop 0\n" \
  "global_load_lds_dwordx4 v189, s[40:41]\n" \
  "global_load_lds_dwordx4 v194, s[40:41] offset:1024\n" \
  "s_lshl2_add_u32 s35, s26, s28\n" \
  "s_mul_i32 s35, s35, 6400\n" \
  "s_add_u32 s35, s35, 0\n" \
  "s_add_u32 s58, s14, s35\n" \
  "s_addc_u32 s59, s15, 0\n" \
  "s_mov_b32 exec_lo, 0xffffffff\n" \
  "s_mov_b32 exec_hi, 0x3ffff\n" \
  "global_load_dword v248, v247, s[58:59]\n" \
  "s_mov_b64 exec, -1\n" \
  "L6_:\n" \
  "ds_read_b32 v192, v190 offset:12\n" \
  "s_waitcnt lgkmcnt(0)\n" \
  "v_readfirstlane_b32 s35, v192\n" \
  "s_cmp_lt_u32 s35, 4\n" \
  "s_cbranch_scc1 L6_\n" \
  "ds_read_b128 v[0:3], v249 offset:0\n" \
  "ds_read_b128 v[4:7], v249 offset:64\n" \
  "global_load_dwordx2 v[196:197], v227, s[42:43] offset:-3200\n" \
  "global_load_dwordx2 v[198:199], v227, s[42:43] offset:-2800\n" \
  "s_waitcnt vmcnt(19)\n" \
  "ds_write_b128 v245, v[8:11] offset:0\n" \
  "ds_write_b128 v243, v[12:15] offset:1024\n" \
  "ds_read_b128 v[8:11], v246 offset:0\n" \
  "ds_read_b128 v[12:15], v242 offset:0\n" \
  "ds_read_b128 v[16:19], v249 offset:2080\n" \
  "ds_read_b128 v[20:23], v249 offset:2144\n" \
  "global_load_dwordx2 v[200:201], v227, s[42:43] offset:-2400\n" \
  "global_load_dwordx2 v[202:203], v227, s[42:43] offset:-1600\n" \
  "s_waitcnt vmcnt(19)\n" \
  "ds_write_b128 v245, v[24:27] offset:2048\n" \
  "ds_write_b128 v243, v[28:31] offset:3072\n" \
  "ds_read_b128 v[24:27], v246 offset:2048\n" \
  "ds_read_b128 v[28:31], v242 offset:2048\n" \
  "ds_read_b128 v[32:35], v249 offset:4160\n" \
  "ds_read_b128 v[36:39], v249 offset:4224\n" \
  "global_load_dwordx2 v[204:205], v227, s[42:43] offset:-1200\n" \
  "global_load_dwordx2 v[206:207], v227, s[42:43] offset:-800\n" \
  "s_waitcnt vmcnt(19)\n" \
  "ds_write_b128 v245, v[40:43] offset:0\n" \
  "ds_write_b128 v243, v[44:47] offset:1024\n" \
  "ds_read_b128 v[40:43], v246 offset:0\n" \
  "ds_read_b128 v[44:47], v242 offset:0\n" \
  "ds_read_b128 v[48:51], v249 offset:6240\n" \
  "ds_read_b128 v[52:55], v249 offset:6304\n" \
  "global_load_dwordx2 v[208:209], v227, s[42:43] offset:-400\n" \
  "global_load_dwordx2 v[210:211], v227, s[42:43] offset:0\n" \
  "s_waitcnt vmcnt(19)\n" \
  "ds_write_b128 v245, v[56:59] offset:2048\n" \
  "ds_write_b128 v243, v[60:63] offset:3072\n" \
  "ds_read_b128 v[56:59], v246 offset:2048\n" \
  "ds_read_b128 v[60:63], v242 offset:2048\n" \
  "ds_read_b128 v[64:67], v249 offset:8320\n" \
  "ds_read_b128 v[68:71], v249 offset:8384\n" \
  "global_load_dwordx2 v[212:213], v227, s[42:43] offset:400\n" \
  "global_load_dwordx2 v[214:215], v227, s[42:43] offset:800\n" \
  "s_waitcnt vmcnt(19)\n" \
  "ds_write_b128 v245, v[72:75] offset:0\n" \
  "ds_write_b128 v243, v[76:79] offset:1024\n" \
  "ds_read_b128 v[72:75], v246 offset:0\n" \
  "ds_read_b128 v[76:79], v242 offset:0\n" \
  "ds_read_b128 v[80:83], v249 offset:10400\n" \
  "ds_read_b128 v[84:87], v249 offset:10464\n" \
  "global_load_dwordx2 v[216:217], v227, s[42:43] offset:1200\n" \
  "global_load_dwordx2 v[218:219], v227, s[42:43] offset:-2000\n" \
  "s_waitcnt vmcnt(19)\n" \
  "ds_write_b128 v245, v[88:91] offset:2048\n" \
  "ds_write_b128 v243, v[92:95] offset:3072\n" \
  "ds_read_b128 v[88:91], v246 offset:2048\n" \
  "ds_read_b128 v[92:95], v242 offset:2048\n" \
  "ds_read_b128 v[96:99], v250 offset:0\n" \
  "ds_read_b128 v[100:103], v250 offset:64\n" \
  "global_load_dwordx2 v[220:221], v227, s[42:43] offset:1600\n" \
  "global_load_dwordx2 v[222:223], v227, s[42:43] offset:2000\n" \
  "s_waitcnt vmcnt(19)\n" \
  "ds_write_b128 v245, v[104:107] offset:0\n" \
  "ds_write_b128 v243, v[108:111] offset:1024\n" \
  "ds_read_b128 v[104:107], v246 offset:0\n" \
  "ds_read_b128 v[108:111], v242 offset:0\n" \
  "ds_read_b128 v[112:115], v251 offset:0\n" \
  "ds_read_b128 v[116:119], v251 offset:64\n" \
  "global_load_dwordx2 v[224:225], v227, s[42:43] offset:2400\n" \
  "global_load_dwordx2 v[226:227], v227, s[42:43] offset:2800\n" \
  "s_waitcnt vmcnt(19)\n" \
  "ds_write_b128 v245, v[120:123] offset:2048\n" \
  "ds_write_b128 v243, v[124:127] offset:3072\n" \
  "ds_read_b128 v[120:123], v246 offset:2048\n" \
  "ds_read_b128 v[124:127], v242 offset:2048\n" \
  "s_getpc_b64 s[58:59]\n" \
  "LB_pc:\n" \
  "s_sub_u32 s58, s58, LB_pc-LZ_first\n" \
  "s_subb_u32 s59, s59, 0\n" \
  "s_mul_i32 s35, s28, 6656\n" \
  "v_lshl_add_u32 v195, v255, 7, s35\n" \
  "v_min_u32 v195, LZ_end-LZ_first, v195\n" \
  "s_mov_b32 exec_hi, 0xfffff\n" \
  "global_load_dword v248, v195, s[58:59]\n" \
  "s_mov_b64 exec, -1\n" \
  "s_lshl2_add_u32 s35, s26, s28\n" \
  "s_mul_i32 s35, s35, 6400\n" \
  "s_add_u32 s35, s35, 6553600\n" \
  "s_add_u32 s58, s14, s35\n" \
  "s_addc_u32 s59, s15, 0\n" \
  "s_mov_b32 exec_lo, 0xffffffff\n" \
  "s_mov_b32 exec_hi, 0x3ffff\n" \
  "global_load_dword v248, v247, s[58:59]\n" \
  "s_mov_b64 exec, -1\n" \
  "s_waitcnt vmcnt(0)\n" \
  "s_sleep 8\n" \
  "s_sleep 8\n" \
  "s_setprio 2\n" \
  "s_mul_i32 s36, s28, 2176\n" \
  "s_add_u32 s36, s36, 154624\n" \
  "v_bfe_u32 v228, v255, 3, 1\n" \
  "v_and_b32 v229, 1, v255\n" \
  "v_lshl_add_u32 v230, v228, 1, v229\n" \
  "v_lshl_add_u32 v231, v229, 1, v228\n" \
  "v_xor_b32 v229, 1, v230\n" \
  "s_mov_b32 vcc_lo, 0xcccccccc\n" \
  "s_mov_b32 vcc_hi, vcc_lo\n" \
  "v_cndmask_b32 v228, v230, v231, vcc\n" \
  "v_cndmask_b32 v229, v229, v231, vcc\n" \
  "v_and_b32 v230, 4, v255\n" \
  "v_mul_u32_u24 v230, 272, v230\n" \
  "v_lshl_add_u32 v230, v253, 2, v230\n" \
  "v_and_or_b32 v230, v255, 2, v230\n" \
  "v_add_u32 v230, s36, v230\n" \
  "s_movk_i32 s35, 272\n" \
  "v_mad_u32_u24 v195, v228, s35, v230\n" \
  "v_mad_u32_u24 v235, v229, s35, v230\n" \
  "v_lshl_add_u32 v230, v253, 4, v238\n" \
  "v_add_u32 v254, s36, v230\n" \
  "v_readfirstlane_b32 s39, v193\n" \
  "s_lshl_b32 s35, s26, 2\n" \
  "s_add_u32 s39, s39, 1\n" \
  "v_mov_b32 v193, s35\n" \
  "v_mov_b64 v[240:241], -1\n" \
  "v_mov_b64 v[242:243], -1\n" \
  "s_waitcnt lgkmcnt(0)\n" \
  "s_mov_b64 exec, 1\n" \
  "ds_add_u32 v190, v191 offset:0\n" \
  "s_mov_b64 exec, -1\n" \
  "L7_:\n" \
  "ds_read_b32 v192, v190 offset:0\n" \
  "s_waitcnt lgkmcnt(0)\n" \
  "v_readfirstlane_b32 s35, v192\n" \
  "s_cmp_lt_u32 s35, 4\n" \
  "s_cbranch_scc1 L7_\n" \
  "s_add_u32 s96, s96, 8192\n" \
  "s_add_u32 s40, s92, s6\n" \
  "s_addc_u32 s41, s93, 0\n" \
  "s_add_u32 s6, s6, 2097152\n" \
  "s_mov_b32 m0, s96\n" \
  "s_nop 0\n" \
  "global_load_lds_dwordx4 v189, s[40:41]\n" \
  "global_load_lds_dwordx4 v194, s[40:41] offset:1024\n" \
  "s_sub_u32 s96, s96, 8192\n" \
  "ds_read_b64_tr_b16 v[128:129], v184 offset:0\n" \
  "ds_read_b64_tr_b16 v[130:131], v184 offset:256\n" \
  "ds_read_b64_tr_b16 v[144:145], v185 offset:0\n" \
  "ds_read_b64_tr_b16 v[146:147], v185 offset:256\n" \
  "ds_read_b64_tr_b16 v[132:133], v184 offset:2048\n" \
  "ds_read_b64_tr_b16 v[134:135], v184 offset:2304\n" \
  "ds_read_b64_tr_b16 v[148:149], v185 offset:2048\n" \
  "ds_read_b64_tr_b16 v[150:151], v185 offset:2304\n" \
  "ds_read_b64_tr_b16 v[136:137], v184 offset:4096\n" \
  "ds_read_b64_tr_b16 v[138:139], v184 offset:4352\n" \
  "ds_read_b64_tr_b16 v[152:153], v185 offset:4096\n" \
  "ds_read_b64_tr_b16 v[154:155], v185 offset:4352\n" \
  "ds_read_b64_tr_b16 v[140:141], v184 offset:6144\n" \
  "ds_read_b64_tr_b16 v[142:143], v184 offset:6400\n" \
  "ds_read_b64_tr_b16 v[156:157], v185 offset:6144\n" \
  "ds_read_b64_tr_b16 v[158:159], v185 offset:6400\n" \
  "v_mov_b32 v192, s39\n" \
  "s_cmp_lg_u32 s28, 0\n" \
  "s_cbranch_scc1 LB_nc\n" \
  "s_mov_b64 exec, 1\n" \
  "global_store_dword v193, v192, s[48:49] offset:0 sc1\n" \
  "s_mov_b64 exec, -1\n" \
  "LB_nc:\n" \
  "s_lshr_b32 s35, s30, 2\n" \
  "s_lshl3_add_u32 s35, s29, s35\n" \
  "s_and_b32 s36, s30, 3\n" \
  "s_lshl2_add_u32 s35, s35, s36\n" \
  "s_lshl2_add_u32 s35, s35, s28\n" \
  "s_lshl_b32 s35, s35, 12\n" \
  "s_add_u32 s35, s35, 105676800\n" \
  "s_add_u32 s58, s24, s35\n" \
  "s_addc_u32 s59, s25, 0\n" \
  "global_store_dwordx4 v238, v[240:243], s[58:59] offset:0 sc1\n" \
  "global_store_dwordx4 v238, v[240:243], s[58:59] offset:1024 sc1\n" \
  "global_store_dwordx4 v238, v[240:243], s[58:59] offset:2048 sc1\n" \
  "global_store_dwordx4 v238, v[240:243], s[58:59] offset:3072 sc1\n" \
  "s_lshl2_add_u32 s35, s7, s28\n" \
  "s_lshl_b32 s35, s35, 7\n" \
  "s_lshl_b32 s36, s31, 16\n" \
  "s_add_u32 s35, s35, s36\n" \
  "s_add_u32 s35, s35, 109871104\n" \
  "s_add_u32 s58, s24, s35\n" \
  "s_addc_u32 s59, s25, 0\n" \
  "s_mov_b64 exec, 0xff\n" \
  "global_store_dwordx4 v238, v[240:243], s[58:59] sc1\n" \
  "s_mov_b64 exec, -1\n" \
  "s_cmp_lg_u32 s33, 1021\n" \
  "s_cbranch_scc1 LB_nm\n" \
  "v_lshlrev_b32 v242, 8, v255\n" \
  "v_mov_b64 v[240:241], 0\n" \
  "global_store_dwordx2 v242, v[240:241], s[48:49] offset:3072 sc1\n" \
  "v_add_u32 v242, 0x4000, v242\n" \
  "global_store_dwordx2 v242, v[240:241], s[48:49] offset:3072 sc1\n" \
  "v_add_u32 v242, 0x4000, v242\n" \
  "global_store_dwordx2 v242, v[240:241], s[48:49] offset:3072 sc1\n" \
  "v_add_u32 v242, 0x4000, v242\n" \
  "global_store_dwordx2 v242, v[240:241], s[48:49] offset:3072 sc1\n" \
  "v_add_u32 v242, 0x4000, v242\n" \
  "LB_nm:\n" \
  "s_setprio 2\n" \
  "s_memtime s[90:91]\n" \
  "s_memrealtime s[42:43]\n" \
  "LT:\n" \
  "s_waitcnt lgkmcnt(14)\n" \
  "v_mfma_f32_16x16x32_bf16 v[160:163], v[128:131], v[0:3], 0\n" \
  "s_and_b32 s37, s38, -2\n" \
  "s_cmp_eq_u32 s37, 6\n" \
  "s_cselect_b32 vcc_lo, 0x33333333, -1\n" \
  "s_mov_b32 vcc_hi, vcc_lo\n" \
  "v_cndmask_b32_dpp v234, v234, v234, vcc row_ror:8 row_mask:0xf bank_mask:0xf\n" \
  "s_min_u32 s37, s38, 37\n" \
  "s_set_gpr_idx_on s37, gpr_idx(SRC0)\n" \
  "v_mad_u32_u24 v232, v196, s52, v236\n" \
  "s_set_gpr_idx_off\n" \
  "s_waitcnt lgkmcnt(12)\n" \
  "v_mfma_f32_16x16x32_bf16 v[164:167], v[144:147], v[0:3], 0\n" \
  "v_lshrrev_b32 v228, v232, v234\n" \
  "s_nop 0\n" \
  "s_waitcnt lgkmcnt(10)\n" \
  "v_mfma_f32_16x16x32_bf16 v[160:163], v[132:135], v[4:7], v[160:163]\n" \
  "v_lshrrev_b32_dpp v229, v232, v234 quad_perm:[2,3,0,1] row_mask:0xf bank_mask:0xf\n" \
  "v_lshrrev_b32_dpp v230, v232, v234 quad_perm:[1,2,3,0] row_mask:0xf bank_mask:0xf\n" \
  "s_waitcnt lgkmcnt(8)\n" \
  "v_mfma_f32_16x16x32_bf16 v[164:167], v[148:151], v[4:7], v[164:167]\n" \
  "v_lshrrev_b32_dpp v231, v232, v234 quad_perm:[3,0,1,2] row_mask:0xf bank_mask:0xf\n" \
  "s_nop 0\n" \
  "s_waitcnt lgkmcnt(6)\n" \
  "v_mfma_f32_16x16x32_bf16 v[160:163], v[136:139], v[8:11], v[160:163]\n" \
  "v_perm_b32 v228, v229, v228, s53\n" \
  "v_perm_b32 v230, v231, v230, s54\n" \
  "s_waitcnt lgkmcnt(4)\n" \
  "v_mfma_f32_16x16x32_bf16 v[164:167], v[152:155], v[8:11], v[164:167]\n" \
  "v_bitop3_b32 v228, v228, v230, s55 bitop3:0xa8\n" \
  "s_nop 0\n" \
  "s_waitcnt lgkmcnt(2)\n" \
  "v_mfma_f32_16x16x32_bf16 v[160:163], v[140:143], v[12:15], v[160:163]\n" \
  "v_lshl_or_b32 v229, v234, 1, s56\n" \
  "v_add_u32_dpp v228, v228, v228 quad_perm:[2,3,0,1] row_mask:0xf bank_mask:0xf\n" \
  "s_waitcnt lgkmcnt(0)\n" \
  "v_mfma_f32_16x16x32_bf16 v[164:167], v[156:159], v[12:15], v[164:167]\n" \
  "v_min_u32_sdwa v230, v228, v228 dst_sel:DWORD dst_unused:UNUSED_PAD src0_sel:BYTE_0 src1_sel:BYTE_1\n" \
  "s_nop 1\n" \
  "v_mfma_f32_16x16x32_bf16 v[168:171], v[128:131], v[16:19], 0\n" \
  "v_min_u32_sdwa v231, v228, v228 dst_sel:DWORD dst_unused:UNUSED_PAD src0_sel:BYTE_2 src1_sel:BYTE_3\n" \
  "v_sub_u32 v230, v230, v231\n" \
  "s_nop 0\n" \
  "v_mfma_f32_16x16x32_bf16 v[172:175], v[144:147], v[16:19], 0\n" \
  "v_add_u32_dpp v230, v230, v230 quad_perm:[1,0,3,2] row_mask:0xf bank_mask:0xf\n" \
  "s_nop 1\n" \
  "v_mfma_f32_16x16x32_bf16 v[168:171], v[132:135], v[20:23], v[168:171]\n" \
  "v_cmp_ge_i32 vcc, 0, v230\n" \
  "s_nop 1\n" \
  "v_cndmask_b32_sdwa v228, v228, v228, vcc dst_sel:DWORD dst_unused:UNUSED_PAD src0_sel:WORD_1 src1_sel:WORD_0\n" \
  "s_mov_b64 s[50:51], vcc\n" \
  "v_mfma_f32_16x16x32_bf16 v[172:175], v[148:151], v[20:23], v[172:175]\n" \
  "v_cvt_pk_bf16_f32 v176, v160, v161\n" \
  "s_nop 1\n" \
  "v_mfma_f32_16x16x32_bf16 v[168:171], v[136:139], v[24:27], v[168:171]\n" \
  "v_cvt_pk_bf16_f32 v177, v162, v163\n" \
  "v_cmp_le_u32_sdwa vcc, v228, v228 src0_sel:BYTE_0 src1_sel:BYTE_1\n" \
  "s_nop 0\n" \
  "v_mfma_f32_16x16x32_bf16 v[172:175], v[152:155], v[24:27], v[172:175]\n" \
  "v_cvt_pk_bf16_f32 v178, v164, v165\n" \
  "s_nop 1\n" \
  "v_mfma_f32_16x16x32_bf16 v[168:171], v[140:143], v[28:31], v[168:171]\n" \
  "v_cvt_pk_bf16_f32 v179, v166, v167\n" \
  "v_cndmask_b32_dpp v232, v232, v232, vcc quad_perm:[2,3,0,1] row_mask:0xf bank_mask:0xf\n" \
  "s_mov_b64 vcc, s[50:51]\n" \
  "v_mfma_f32_16x16x32_bf16 v[172:175], v[156:159], v[28:31], v[172:175]\n" \
  "s_waitcnt vmcnt(5)\n" \
  "global_store_dwordx4 v186, v[176:179], s[44:45] offset:0 sc1\n" \
  "v_cndmask_b32_dpp v232, v232, v232, vcc quad_perm:[3,0,1,2] row_mask:0xf bank_mask:0xf\n" \
  "v_mfma_f32_16x16x32_bf16 v[160:163], v[128:131], v[32:35], 0\n" \
  "s_cmp_lg_u32 s33, 1021\n" \
  "s_cbranch_scc0 LT_e\n" \
  "LT_r:\n" \
  "v_lshlrev_b32_e64 v230, v232, 3\n" \
  "v_and_or_b32 v234, v229, v230, v234\n" \
  "v_mfma_f32_16x16x32_bf16 v[164:167], v[144:147], v[32:35], 0\n" \
  "s_set_gpr_idx_on s37, gpr_idx(DST)\n" \
  "v_mov_b32 v196, v232\n" \
  "s_set_gpr_idx_off\n" \
  "s_add_u32 s38, s38, 2\n" \
  "s_cmp_lt_u32 s38, 32\n" \
  "s_cselect_b32 s38, s38, 64\n" \
  "v_mfma_f32_16x16x32_bf16 v[160:163], v[132:135], v[36:39], v[160:163]\n" \
  "s_min_u32 s37, s38, 37\n" \
  "s_set_gpr_idx_on s37, gpr_idx(SRC0)\n" \
  "v_mad_u32_u24 v232, v196, s52, v236\n" \
  "s_set_gpr_idx_off\n" \
  "v_lshrrev_b32 v228, v232, v234\n" \
  "v_mfma_f32_16x16x32_bf16 v[164:167], v[148:151], v[36:39], v[164:167]\n" \
  "v_cvt_pk_bf16_f32 v180, v168, v169\n" \
  "s_nop 1\n" \
  "v_mfma_f32_16x16x32_bf16 v[160:163], v[136:139], v[40:43], v[160:163]\n" \
  "v_cvt_pk_bf16_f32 v181, v170, v171\n" \
  "v_lshrrev_b32_dpp v229, v232, v234 quad_perm:[2,3,0,1] row_mask:0xf bank_mask:0xf\n" \
  "s_nop 0\n" \
  "v_mfma_f32_16x16x32_bf16 v[164:167], v[152:155], v[40:43], v[164:167]\n" \
  "v_cvt_pk_bf16_f32 v182, v172, v173\n" \
  "s_nop 1\n" \
  "v_mfma_f32_16x16x32_bf16 v[160:163], v[140:143], v[44:47], v[160:163]\n" \
  "v_cvt_pk_bf16_f32 v183, v174, v175\n" \
  "v_lshrrev_b32_dpp v230, v232, v234 quad_perm:[1,2,3,0] row_mask:0xf bank_mask:0xf\n" \
  "s_nop 0\n" \
  "v_mfma_f32_16x16x32_bf16 v[164:167], v[156:159], v[44:47], v[164:167]\n" \
  "s_waitcnt vmcnt(5)\n" \
  "global_store_dwordx4 v186, v[180:183], s[44:45] offset:256 sc1\n" \
  "v_lshrrev_b32_dpp v231, v232, v234 quad_perm:[3,0,1,2] row_mask:0xf bank_mask:0xf\n" \
  "v_mfma_f32_16x16x32_bf16 v[168:171], v[128:131], v[48:51], 0\n" \
  "v_perm_b32 v228, v229, v228, s53\n" \
  "v_perm_b32 v230, v231, v230, s54\n" \
  "s_nop 0\n" \
  "v_mfma_f32_16x16x32_bf16 v[172:175], v[144:147], v[48:51], 0\n" \
  "v_bitop3_b32 v228, v228, v230, s55 bitop3:0xa8\n" \
  "s_nop 1\n" \
  "v_mfma_f32_16x16x32_bf16 v[168:171], v[132:135], v[52:55], v[168:171]\n" \
  "v_lshl_or_b32 v229, v234, 1, s56\n" \
  "v_add_u32_dpp v228, v228, v228 quad_perm:[2,3,0,1] row_mask:0xf bank_mask:0xf\n" \
  "s_nop 0\n" \
  "v_mfma_f32_16x16x32_bf16 v[172:175], v[148:151], v[52:55], v[172:175]\n" \
  "v_cvt_pk_bf16_f32 v176, v160, v161\n" \
  "s_nop 1\n" \
  "v_mfma_f32_16x16x32_bf16 v[168:171], v[136:139], v[56:59], v[168:171]\n" \
  "v_cvt_pk_bf16_f32 v177, v162, v163\n" \
  "v_min_u32_sdwa v230, v228, v228 dst_sel:DWORD dst_unused:UNUSED_PAD src0_sel:BYTE_0 src1_sel:BYTE_1\n" \
  "s_nop 0\n" \
  "v_mfma_f32_16x16x32_bf16 v[172:175], v[152:155], v[56:59], v[172:175]\n" \
  "v_cvt_pk_bf16_f32 v178, v164, v165\n" \
  "s_nop 1\n" \
  "v_mfma_f32_16x16x32_bf16 v[168:171], v[140:143], v[60:63], v[168:171]\n" \
  "v_cvt_pk_bf16_f32 v179, v166, v167\n" \
  "v_min_u32_sdwa v231, v228, v228 dst_sel:DWORD dst_unused:UNUSED_PAD src0_sel:BYTE_2 src1_sel:BYTE_3\n" \
  "s_nop 0\n" \
  "v_mfma_f32_16x16x32_bf16 v[172:175], v[156:159], v[60:63], v[172:175]\n" \
  "s_waitcnt vmcnt(5)\n" \
  "global_store_dwordx4 v186, v[176:179], s[44:45] offset:512 sc1\n" \
  "v_sub_u32 v230, v230, v231\n" \
  "v_mfma_f32_16x16x32_bf16 v[160:163], v[128:131], v[64:67], 0\n" \
  "s_nop 0\n" \
  "v_add_u32_dpp v230, v230, v230 quad_perm:[1,0,3,2] row_mask:0xf bank_mask:0xf\n" \
  "v_cmp_ge_i32 vcc, 0, v230\n" \
  "s_nop 0\n" \
  "v_mfma_f32_16x16x32_bf16 v[164:167], v[144:147], v[64:67], 0\n" \
  "v_cndmask_b32_sdwa v228, v228, v228, vcc dst_sel:DWORD dst_unused:UNUSED_PAD src0_sel:WORD_1 src1_sel:WORD_0\n" \
  "s_mov_b64 s[50:51], vcc\n" \
  "s_nop 0\n" \
  "v_mfma_f32_16x16x32_bf16 v[160:163], v[132:135], v[68:71], v[160:163]\n" \
  "v_cmp_le_u32_sdwa vcc, v228, v228 src0_sel:BYTE_0 src1_sel:BYTE_1\n" \
  "s_nop 1\n" \
  "v_cndmask_b32_dpp v232, v232, v232, vcc quad_perm:[2,3,0,1] row_mask:0xf bank_mask:0xf\n" \
  "s_mov_b64 vcc, s[50:51]\n" \
  "v_mfma_f32_16x16x32_bf16 v[164:167], v[148:151], v[68:71], v[164:167]\n" \
  "v_cvt_pk_bf16_f32 v180, v168, v169\n" \
  "s_nop 1\n" \
  "v_mfma_f32_16x16x32_bf16 v[160:163], v[136:139], v[72:75], v[160:163]\n" \
  "v_cvt_pk_bf16_f32 v181, v170, v171\n" \
  "v_cndmask_b32_dpp v232, v232, v232, vcc quad_perm:[3,0,1,2] row_mask:0xf bank_mask:0xf\n" \
  "s_nop 0\n" \
  "v_mfma_f32_16x16x32_bf16 v[164:167], v[152:155], v[72:75], v[164:167]\n" \
  "v_cvt_pk_bf16_f32 v182, v172, v173\n" \
  "s_nop 1\n" \
  "v_mfma_f32_16x16x32_bf16 v[160:163], v[140:143], v[76:79], v[160:163]\n" \
  "v_cvt_pk_bf16_f32 v183, v174, v175\n" \
  "v_lshlrev_b32_e64 v230, v232, 3\n" \
  "s_nop 0\n" \
  "v_mfma_f32_16x16x32_bf16 v[164:167], v[156:159], v[76:79], v[164:167]\n" \
  "s_waitcnt vmcnt(5)\n" \
  "global_store_dwordx4 v186, v[180:183], s[44:45] offset:768 sc1\n" \
  "v_and_or_b32 v234, v229, v230, v234\n" \
  "v_mfma_f32_16x16x32_bf16 v[168:171], v[128:131], v[80:83], 0\n" \
  "s_mov_b64 exec, 1\n" \
  "ds_add_u32 v190, v191 offset:0\n" \
  "s_mov_b64 exec, -1\n" \
  "s_set_gpr_idx_on s37, gpr_idx(DST)\n" \
  "v_mov_b32 v196, v232\n" \
  "s_set_gpr_idx_off\n" \
  "s_add_u32 s38, s38, 2\n" \
  "s_cmp_lt_u32 s38, 32\n" \
  "s_cselect_b32 s38, s38, 64\n" \
  "s_and_b32 s37, s38, -2\n" \
  "s_cmp_eq_u32 s37, 22\n" \
  "s_cselect_b32 vcc_lo, 0x33333333, -1\n" \
  "s_mov_b32 vcc_hi, vcc_lo\n" \
  "v_cndmask_b32_dpp v234, v234, v234, vcc row_ror:8 row_mask:0xf bank_mask:0xf\n" \
  "v_mfma_f32_16x16x32_bf16 v[172:175], v[144:147], v[80:83], 0\n" \
  "s_min_u32 s37, s38, 37\n" \
  "s_set_gpr_idx_on s37, gpr_idx(SRC0)\n" \
  "v_mad_u32_u24 v232, v196, s52, v236\n" \
  "s_set_gpr_idx_off\n" \
  "v_mfma_f32_16x16x32_bf16 v[168:171], v[132:135], v[84:87], v[168:171]\n" \
  "v_lshrrev_b32 v228, v232, v234\n" \
  "v_lshrrev_b32_dpp v229, v232, v234 quad_perm:[2,3,0,1] row_mask:0xf bank_mask:0xf\n" \
  "s_nop 0\n" \
  "v_mfma_f32_16x16x32_bf16 v[172:175], v[148:151], v[84:87], v[172:175]\n" \
  "v_cvt_pk_bf16_f32 v176, v160, v161\n" \
  "s_nop 1\n" \
  "v_mfma_f32_16x16x32_bf16 v[168:171], v[136:139], v[88:91], v[168:171]\n" \
  "v_cvt_pk_bf16_f32 v177, v162, v163\n" \
  "v_lshrrev_b32_dpp v230, v232, v234 quad_perm:[1,2,3,0] row_mask:0xf bank_mask:0xf\n" \
  "s_nop 0\n" \
  "v_mfma_f32_16x16x32_bf16 v[172:175], v[152:155], v[88:91], v[172:175]\n" \
  "v_cvt_pk_bf16_f32 v178, v164, v165\n" \
  "s_nop 1\n" \
  "v_mfma_f32_16x16x32_bf16 v[168:171], v[140:143], v[92:95], v[168:171]\n" \
  "v_cvt_pk_bf16_f32 v179, v166, v167\n" \
  "v_lshrrev_b32_dpp v231, v232, v234 quad_perm:[3,0,1,2] row_mask:0xf bank_mask:0xf\n" \
  "s_nop 0\n" \
  "v_mfma_f32_16x16x32_bf16 v[172:175], v[156:159], v[92:95], v[172:175]\n" \
  "s_waitcnt vmcnt(5)\n" \
  "global_store_dwordx4 v186, v[176:179], s[44:45] offset:1024 sc1\n" \
  "v_perm_b32 v228, v229, v228, s53\n" \
  "v_mfma_f32_16x16x32_bf16 v[160:163], v[128:131], v[96:99], 0\n" \
  "ds_read_b32 v192, v190\n" \
  "v_add_u32 v184, s97, v184\n" \
  "v_perm_b32 v230, v231, v230, s54\n" \
  "v_bitop3_b32 v228, v228, v230, s55 bitop3:0xa8\n" \
  "v_mfma_f32_16x16x32_bf16 v[164:167], v[144:147], v[96:99], 0\n" \
  "v_add_u32 v185, s97, v185\n" \
  "v_lshl_or_b32 v229, v234, 1, s56\n" \
  "s_nop 0\n" \
  "v_mfma_f32_16x16x32_bf16 v[160:163], v[132:135], v[100:103], v[160:163]\n" \
  "s_cmp_eq_u32 s98, 4\n" \
  "s_cselect_b32 s36, 12451840, 0\n" \
  "s_sub_u32 s6, s6, s36\n" \
  "s_cmp_lt_u32 s98, 10\n" \
  "s_cselect_b32 s6, s6, s34\n" \
  "s_add_u32 s40, s92, s6\n" \
  "s_addc_u32 s41, s93, 0\n" \
  "s_add_u32 s6, s6, 2097152\n" \
  "v_add_u32_dpp v228, v228, v228 quad_perm:[2,3,0,1] row_mask:0xf bank_mask:0xf\n" \
  "v_min_u32_sdwa v230, v228, v228 dst_sel:DWORD dst_unused:UNUSED_PAD src0_sel:BYTE_0 src1_sel:BYTE_1\n" \
  "v_mfma_f32_16x16x32_bf16 v[164:167], v[148:151], v[100:103], v[164:167]\n" \
  "v_cvt_pk_bf16_f32 v180, v168, v169\n" \
  "s_nop 1\n" \
  "v_mfma_f32_16x16x32_bf16 v[160:163], v[136:139], v[104:107], v[160:163]\n" \
  "v_cvt_pk_bf16_f32 v181, v170, v171\n" \
  "v_min_u32_sdwa v231, v228, v228 dst_sel:DWORD dst_unused:UNUSED_PAD src0_sel:BYTE_2 src1_sel:BYTE_3\n" \
  "s_nop 0\n" \
  "v_mfma_f32_16x16x32_bf16 v[164:167], v[152:155], v[104:107], v[164:167]\n" \
  "s_waitcnt lgkmcnt(0)\n" \
  "v_readfirstlane_b32 s35, v192\n" \
  "v_cvt_pk_bf16_f32 v182, v172, v173\n" \
  "v_mfma_f32_16x16x32_bf16 v[160:163], v[140:143], v[108:111], v[160:163]\n" \
  "s_cmp_lt_u32 s35, s99\n" \
  "s_cbranch_scc1 LT_w\n" \
  "LT_c:\n" \
  "s_mov_b32 m0, s96\n" \
  "s_nop 0\n" \
  "global_load_lds_dwordx4 v189, s[40:41]\n" \
  "global_load_lds_dwordx4 v194, s[40:41] offset:1024\n" \
  "s_add_u32 s96, s96, s97\n" \
  "s_sub_u32 s97, 0, s97\n" \
  "s_add_u32 s99, s99, 4\n" \
  "v_cvt_pk_bf16_f32 v183, v174, v175\n" \
  "v_sub_u32 v230, v230, v231\n" \
  "v_mfma_f32_16x16x32_bf16 v[164:167], v[156:159], v[108:111], v[164:167]\n" \
  "s_cmp_ge_u32 s98, 6\n" \
  "s_cbranch_scc1 LT_a\n" \
  "LT_s:\n" \
  "s_waitcnt vmcnt(5)\n" \
  "global_store_dwordx4 v186, v[180:183], s[44:45] offset:1280 sc1\n" \
  "v_add_u32_dpp v230, v230, v230 quad_perm:[1,0,3,2] row_mask:0xf bank_mask:0xf\n" \
  "v_mfma_f32_16x16x32_bf16 v[168:171], v[128:131], v[112:115], 0\n" \
  "ds_read_b64_tr_b16 v[128:129], v184 offset:0\n" \
  "ds_read_b64_tr_b16 v[130:131], v184 offset:256\n" \
  "v_cmp_ge_i32 vcc, 0, v230\n" \
  "s_nop 1\n" \
  "v_cndmask_b32_sdwa v228, v228, v228, vcc dst_sel:DWORD dst_unused:UNUSED_PAD src0_sel:WORD_1 src1_sel:WORD_0\n" \
  "s_mov_b64 s[50:51], vcc\n" \
  "v_mfma_f32_16x16x32_bf16 v[172:175], v[144:147], v[112:115], 0\n" \
  "ds_read_b64_tr_b16 v[144:145], v185 offset:0\n" \
  "ds_read_b64_tr_b16 v[146:147], v185 offset:256\n" \
  "v_cmp_le_u32_sdwa vcc, v228, v228 src0_sel:BYTE_0 src1_sel:BYTE_1\n" \
  "v_mfma_f32_16x16x32_bf16 v[168:171], v[132:135], v[116:119], v[168:171]\n" \
  "ds_read_b64_tr_b16 v[132:133], v184 offset:2048\n" \
  "ds_read_b64_tr_b16 v[134:135], v184 offset:2304\n" \
  "v_cndmask_b32_dpp v232, v232, v232, vcc quad_perm:[2,3,0,1] row_mask:0xf bank_mask:0xf\n" \
  "s_mov_b64 vcc, s[50:51]\n" \
  "s_nop 0\n" \
  "v_cndmask_b32_dpp v232, v232, v232, vcc quad_perm:[3,0,1,2] row_mask:0xf bank_mask:0xf\n" \
  "v_mfma_f32_16x16x32_bf16 v[172:175], v[148:151], v[116:119], v[172:175]\n" \
  "ds_read_b64_tr_b16 v[148:149], v185 offset:2048\n" \
  "ds_read_b64_tr_b16 v[150:151], v185 offset:2304\n" \
  "v_cvt_pk_bf16_f32 v176, v160, v161\n" \
  "v_mfma_f32_16x16x32_bf16 v[168:171], v[136:139], v[120:123], v[168:171]\n" \
  "ds_read_b64_tr_b16 v[136:137], v184 offset:4096\n" \
  "ds_read_b64_tr_b16 v[138:139], v184 offset:4352\n" \
  "v_cvt_pk_bf16_f32 v177, v162, v163\n" \
  "v_lshlrev_b32_e64 v230, v232, 3\n" \
  "v_mfma_f32_16x16x32_bf16 v[172:175], v[152:155], v[120:123], v[172:175]\n" \
  "ds_read_b64_tr_b16 v[152:153], v185 offset:4096\n" \
  "ds_read_b64_tr_b16 v[154:155], v185 offset:4352\n" \
  "v_cvt_pk_bf16_f32 v178, v164, v165\n" \
  "v_mfma_f32_16x16x32_bf16 v[168:171], v[140:143], v[124:127], v[168:171]\n" \
  "ds_read_b64_tr_b16 v[140:141], v184 offset:6144\n" \
  "ds_read_b64_tr_b16 v[142:143], v184 offset:6400\n" \
  "v_cvt_pk_bf16_f32 v179, v166, v167\n" \
  "v_and_or_b32 v234, v229, v230, v234\n" \
  "v_mfma_f32_16x16x32_bf16 v[172:175], v[156:159], v[124:127], v[172:175]\n" \
  "ds_read_b64_tr_b16 v[156:157], v185 offset:6144\n" \
  "ds_read_b64_tr_b16 v[158:159], v185 offset:6400\n" \
  "s_waitcnt vmcnt(5)\n" \
  "global_store_dwordx4 v187, v[176:179], s[44:45] offset:0 sc1\n" \
  "s_set_gpr_idx_on s37, gpr_idx(DST)\n" \
  "v_mov_b32 v196, v232\n" \
  "s_set_gpr_idx_off\n" \
  "s_add_u32 s38, s38, 2\n" \
  "s_cmp_lt_u32 s38, 32\n" \
  "s_cselect_b32 s38, s38, 64\n" \
  "s_add_u32 s98, s98, 1\n" \
  "v_cvt_pk_bf16_f32 v180, v168, v169\n" \
  "v_cvt_pk_bf16_f32 v181, v170, v171\n" \
  "v_cvt_pk_bf16_f32 v182, v172, v173\n" \
  "v_cvt_pk_bf16_f32 v183, v174, v175\n" \
  "s_waitcnt vmcnt(5)\n" \
  "global_store_dwordx4 v188, v[180:183], s[44:45] offset:0 sc1\n" \
  "s_add_u32 s44, s44, 128000\n" \
  "s_addc_u32 s45, s45, 0\n" \
  "s_bitcmp0_b32 4160, s98\n" \
  "s_cbranch_scc1 LT\n" \
  "s_cmp_eq_u32 s98, 6\n" \
  "s_cbranch_scc1 LT_m\n" \
  "s_lshr_b32 s35, s30, 2\n" \
  "s_lshl3_add_u32 s35, s29, s35\n" \
  "s_mul_i32 s35, s35, 1600000\n" \
  "s_lshl_b32 s36, s28, 12\n" \
  "s_add_u32 s35, s35, s36\n" \
  "s_add_u32 s40, s24, s35\n" \
  "s_addc_u32 s41, s25, 0\n" \
  "s_bitcmp1_b32 s30, 2\n" \
  "s_cbranch_scc1 LB_l_h\n" \
  "global_load_dwordx4 v[64:67], v238, s[40:41] offset:0 sc1\n" \
  "global_load_dwordx4 v[68:71], v238, s[40:41] offset:1024 sc1\n" \
  "global_load_dwordx4 v[72:75], v238, s[40:41] offset:2048 sc1\n" \
  "global_load_dwordx4 v[76:79], v238, s[40:41] offset:3072 sc1\n" \
  "s_add_u32 s40, s40, 16000\n" \
  "s_addc_u32 s41, s41, 0\n" \
  "global_load_dwordx4 v[80:83], v238, s[40:41] offset:0 sc1\n" \
  "global_load_dwordx4 v[84:87], v238, s[40:41] offset:1024 sc1\n" \
  "global_load_dwordx4 v[88:91], v238, s[40:41] offset:2048 sc1\n" \
  "global_load_dwordx4 v[92:95], v238, s[40:41] offset:3072 sc1\n" \
  "s_add_u32 s40, s40, 16000\n" \
  "s_addc_u32 s41, s41, 0\n" \
  "global_load_dwordx4 v[96:99], v238, s[40:41] offset:0 sc1\n" \
  "global_load_dwordx4 v[100:103], v238, s[40:41] offset:1024 sc1\n" \
  "global_load_dwordx4 v[104:107], v238, s[40:41] offset:2048 sc1\n" \
  "global_load_dwordx4 v[108:111], v238, s[40:41] offset:3072 sc1\n" \
  "s_add_u32 s40, s40, 16000\n" \
  "s_addc_u32 s41, s41, 0\n" \
  "global_load_dwordx4 v[112:115], v238, s[40:41] offset:0 sc1\n" \
  "global_load_dwordx4 v[116:119], v238, s[40:41] offset:1024 sc1\n" \
  "global_load_dwordx4 v[120:123], v238, s[40:41] offset:2048 sc1\n" \
  "global_load_dwordx4 v[124:127], v238, s[40:41] offset:3072 sc1\n" \
  "s_add_u32 s40, s40, 16000\n" \
  "s_addc_u32 s41, s41, 0\n" \
  "s_branch LB_l_e\n" \
  "LB_l_h:\n" \
  "global_load_dwordx4 v[0:3], v238, s[40:41] offset:0 sc1\n" \
  "global_load_dwordx4 v[4:7], v238, s[40:41] offset:1024 sc1\n" \
  "global_load_dwordx4 v[8:11], v238, s[40:41] offset:2048 sc1\n" \
  "global_load_dwordx4 v[12:15], v238, s[40:41] offset:3072 sc1\n" \
  "s_add_u32 s40, s40, 16000\n" \
  "s_addc_u32 s41, s41, 0\n" \
  "global_load_dwordx4 v[16:19], v238, s[40:41] offset:0 sc1\n" \
  "global_load_dwordx4 v[20:23], v238, s[40:41] offset:1024 sc1\n" \
  "global_load_dwordx4 v[24:27], v238, s[40:41] offset:2048 sc1\n" \
  "global_load_dwordx4 v[28:31], v238, s[40:41] offset:3072 sc1\n" \
  "s_add_u32 s40, s40, 16000\n" \
  "s_addc_u32 s41, s41, 0\n" \
  "global_load_dwordx4 v[32:35], v238, s[40:41] offset:0 sc1\n" \
  "global_load_dwordx4 v[36:39], v238, s[40:41] offset:1024 sc1\n" \
  "global_load_dwordx4 v[40:43], v238, s[40:41] offset:2048 sc1\n" \
  "global_load_dwordx4 v[44:47], v238, s[40:41] offset:3072 sc1\n" \
  "s_add_u32 s40, s40, 16000\n" \
  "s_addc_u32 s41, s41, 0\n" \
  "global_load_dwordx4 v[48:51], v238, s[40:41] offset:0 sc1\n" \
  "global_load_dwordx4 v[52:55], v238, s[40:41] offset:1024 sc1\n" \
  "global_load_dwordx4 v[56:59], v238, s[40:41] offset:2048 sc1\n" \
  "global_load_dwordx4 v[60:63], v238, s[40:41] offset:3072 sc1\n" \
  "s_add_u32 s40, s40, 16000\n" \
  "s_addc_u32 s41, s41, 0\n" \
  "LB_l_e:\n" \
  "ds_write_b16_d16_hi v195, v197 offset:0\n" \
  "ds_write_b16_d16_hi v195, v199 offset:16\n" \
  "ds_write_b16_d16_hi v195, v201 offset:32\n" \
  "ds_write_b16_d16_hi v235, v203 offset:64\n" \
  "ds_write_b16_d16_hi v235, v205 offset:80\n" \
  "ds_write_b16_d16_hi v235, v207 offset:96\n" \
  "ds_write_b16_d16_hi v235, v209 offset:112\n" \
  "ds_write_b16_d16_hi v235, v211 offset:128\n" \
  "ds_write_b16_d16_hi v235, v213 offset:144\n" \
  "ds_write_b16_d16_hi v235, v215 offset:160\n" \
  "ds_write_b16_d16_hi v235, v217 offset:176\n" \
  "ds_write_b16_d16_hi v195, v219 offset:48\n" \
  "ds_write_b16_d16_hi v195, v221 offset:192\n" \
  "ds_write_b16_d16_hi v195, v223 offset:208\n" \
  "ds_write_b16_d16_hi v195, v225 offset:224\n" \
  "ds_write_b16_d16_hi v195, v227 offset:240\n" \
  "ds_read_b128 v[244:247], v254 offset:0\n" \
  "ds_read_b128 v[248:251], v254 offset:1088\n" \
  "s_lshr_b32 s35, s7, 5\n" \
  "s_mul_i32 s35, s35, 819200\n" \
  "s_and_b32 s36, s7, 31\n" \
  "s_lshl_b32 s36, s36, 10\n" \
  "s_add_u32 s35, s35, s36\n" \
  "s_lshl_b32 s36, s4, 9\n" \
  "s_add_u32 s35, s35, s36\n" \
  "s_add_u32 s35, s35, 102793216\n" \
  "s_add_u32 s58, s24, s35\n" \
  "s_addc_u32 s59, s25, 0\n" \
  "s_waitcnt lgkmcnt(0)\n" \
  "s_cmp_ge_u32 s4, 896\n" \
  "s_cbranch_scc1 L10_\n" \
  "global_store_dwordx4 v238, v[244:247], s[58:59] sc1\n" \
  "s_cmp_ge_u32 s4, 768\n" \
  "s_cbranch_scc1 L10_\n" \
  "s_add_u32 s58, s58, 32768\n" \
  "s_addc_u32 s59, s59, 0\n" \
  "global_store_dwordx4 v238, v[248:251], s[58:59] sc1\n" \
  "L10_:\n" \
  "s_lshr_b32 s35, s30, 2\n" \
  "s_lshl3_add_u32 s33, s29, s35\n" \
  "s_and_b32 s34, s30, 3\n" \
  "s_lshl_b32 s59, s27, 11\n" \
  "s_mul_i32 s35, s33, 1600000\n" \
  "s_add_u32 s54, s24, s35\n" \
  "s_addc_u32 s55, s25, 0\n" \
  "s_mul_i32 s35, s34, 819200\n" \
  "s_lshl1_add_u32 s35, s59, s35\n" \
  "s_add_u32 s35, s35, 102400000\n" \
  "s_add_u32 s56, s24, s35\n" \
  "s_addc_u32 s57, s25, 0\n" \
  "global_load_dwordx4 v[208:211], v238, s[56:57] offset:0 sc1\n" \
  "global_load_dwordx4 v[212:215], v238, s[56:57] offset:1024 sc1\n" \
  "global_load_dwordx4 v[216:219], v238, s[56:57] offset:2048 sc1\n" \
  "global_load_dwordx4 v[220:223], v238, s[56:57] offset:3072 sc1\n" \
  "s_add_u32 s56, s56, 32768\n" \
  "s_addc_u32 s57, s57, 0\n" \
  "global_load_dwordx4 v[224:227], v238, s[56:57] sc1\n" \
  "global_load_dwordx4 v[228:231], v238, s[56:57] offset:1024 sc1\n" \
  "global_load_dwordx4 v[232:235], v238, s[56:57] offset:2048 sc1\n" \
  "global_load_dwordx4 v[236:239], v238, s[56:57] offset:3072 sc1\n" \
  "s_add_u32 s56, s56, 32768\n" \
  "s_addc_u32 s57, s57, 0\n" \
  "s_bitcmp1_b32 s30, 2\n" \
  "s_cbranch_scc1 LB_t_h\n" \
  "s_waitcnt lgkmcnt(14)\n" \
  "v_mfma_f32_16x16x32_bf16 v[160:163], v[128:131], v[0:3], 0\n" \
  "s_nop 2\n" \
  "s_waitcnt lgkmcnt(12)\n" \
  "v_mfma_f32_16x16x32_bf16 v[164:167], v[144:147], v[0:3], 0\n" \
  "s_nop 2\n" \
  "s_waitcnt lgkmcnt(10)\n" \
  "v_mfma_f32_16x16x32_bf16 v[160:163], v[132:135], v[4:7], v[160:163]\n" \
  "s_nop 2\n" \
  "s_waitcnt lgkmcnt(8)\n" \
  "v_mfma_f32_16x16x32_bf16 v[164:167], v[148:151], v[4:7], v[164:167]\n" \
  "s_nop 2\n" \
  "s_waitcnt lgkmcnt(6)\n" \
  "v_mfma_f32_16x16x32_bf16 v[160:163], v[136:139], v[8:11], v[160:163]\n" \
  "s_nop 2\n" \
  "s_waitcnt lgkmcnt(4)\n" \
  "v_mfma_f32_16x16x32_bf16 v[164:167], v[152:155], v[8:11], v[164:167]\n" \
  "s_nop 2\n" \
  "s_waitcnt lgkmcnt(2)\n" \
  "v_mfma_f32_16x16x32_bf16 v[160:163], v[140:143], v[12:15], v[160:163]\n" \
  "s_nop 2\n" \
  "s_waitcnt lgkmcnt(0)\n" \
  "v_mfma_f32_16x16x32_bf16 v[164:167], v[156:159], v[12:15], v[164:167]\n" \
  "s_nop 3\n" \
  "v_mfma_f32_16x16x32_bf16 v[168:171], v[128:131], v[16:19], 0\n" \
  "s_nop 3\n" \
  "v_mfma_f32_16x16x32_bf16 v[172:175], v[144:147], v[16:19], 0\n" \
  "s_nop 3\n" \
  "v_mfma_f32_16x16x32_bf16 v[168:171], v[132:135], v[20:23], v[168:171]\n" \
  "s_nop 3\n" \
  "v_mfma_f32_16x16x32_bf16 v[172:175], v[148:151], v[20:23], v[172:175]\n" \
  "v_cvt_pk_bf16_f32 v176, v160, v161\n" \
  "s_nop 2\n" \
  "v_mfma_f32_16x16x32_bf16 v[168:171], v[136:139], v[24:27], v[168:171]\n" \
  "v_cvt_pk_bf16_f32 v177, v162, v163\n" \
  "s_nop 2\n" \
  "v_mfma_f32_16x16x32_bf16 v[172:175], v[152:155], v[24:27], v[172:175]\n" \
  "v_cvt_pk_bf16_f32 v178, v164, v165\n" \
  "s_nop 2\n" \
  "v_mfma_f32_16x16x32_bf16 v[168:171], v[140:143], v[28:31], v[168:171]\n" \
  "v_cvt_pk_bf16_f32 v179, v166, v167\n" \
  "s_nop 2\n" \
  "v_mfma_f32_16x16x32_bf16 v[172:175], v[156:159], v[28:31], v[172:175]\n" \
  "s_waitcnt vmcnt(63)\n" \
  "global_store_dwordx4 v186, v[176:179], s[46:47] offset:0 sc1\n" \
  "s_nop 1\n" \
  "v_mfma_f32_16x16x32_bf16 v[160:163], v[128:131], v[32:35], 0\n" \
  "s_nop 3\n" \
  "v_mfma_f32_16x16x32_bf16 v[164:167], v[144:147], v[32:35], 0\n" \
  "s_nop 3\n" \
  "v_mfma_f32_16x16x32_bf16 v[160:163], v[132:135], v[36:39], v[160:163]\n" \
  "s_nop 3\n" \
  "v_mfma_f32_16x16x32_bf16 v[164:167], v[148:151], v[36:39], v[164:167]\n" \
  "v_cvt_pk_bf16_f32 v180, v168, v169\n" \
  "s_nop 2\n" \
  "v_mfma_f32_16x16x32_bf16 v[160:163], v[136:139], v[40:43], v[160:163]\n" \
  "v_cvt_pk_bf16_f32 v181, v170, v171\n" \
  "s_nop 2\n" \
  "v_mfma_f32_16x16x32_bf16 v[164:167], v[152:155], v[40:43], v[164:167]\n" \
  "v_cvt_pk_bf16_f32 v182, v172, v173\n" \
  "s_nop 2\n" \
  "v_mfma_f32_16x16x32_bf16 v[160:163], v[140:143], v[44:47], v[160:163]\n" \
  "v_cvt_pk_bf16_f32 v183, v174, v175\n" \
  "s_nop 2\n" \
  "v_mfma_f32_16x16x32_bf16 v[164:167], v[156:159], v[44:47], v[164:167]\n" \
  "s_waitcnt vmcnt(63)\n" \
  "global_store_dwordx4 v186, v[180:183], s[46:47] offset:256 sc1\n" \
  "s_nop 1\n" \
  "v_mfma_f32_16x16x32_bf16 v[168:171], v[128:131], v[48:51], 0\n" \
  "s_nop 3\n" \
  "v_mfma_f32_16x16x32_bf16 v[172:175], v[144:147], v[48:51], 0\n" \
  "s_nop 3\n" \
  "v_mfma_f32_16x16x32_bf16 v[168:171], v[132:135], v[52:55], v[168:171]\n" \
  "s_nop 3\n" \
  "v_mfma_f32_16x16x32_bf16 v[172:175], v[148:151], v[52:55], v[172:175]\n" \
  "v_cvt_pk_bf16_f32 v176, v160, v161\n" \
  "s_nop 2\n" \
  "v_mfma_f32_16x16x32_bf16 v[168:171], v[136:139], v[56:59], v[168:171]\n" \
  "v_cvt_pk_bf16_f32 v177, v162, v163\n" \
  "s_nop 2\n" \
  "v_mfma_f32_16x16x32_bf16 v[172:175], v[152:155], v[56:59], v[172:175]\n" \
  "v_cvt_pk_bf16_f32 v178, v164, v165\n" \
  "s_nop 2\n" \
  "v_mfma_f32_16x16x32_bf16 v[168:171], v[140:143], v[60:63], v[168:171]\n" \
  "v_cvt_pk_bf16_f32 v179, v166, v167\n" \
  "s_nop 2\n" \
  "v_mfma_f32_16x16x32_bf16 v[172:175], v[156:159], v[60:63], v[172:175]\n" \
  "s_waitcnt vmcnt(63)\n" \
  "global_store_dwordx4 v186, v[176:179], s[46:47] offset:512 sc1\n" \
  "s_nop 0\n" \
  "v_cvt_pk_bf16_f32 v180, v168, v169\n" \
  "v_cvt_pk_bf16_f32 v181, v170, v171\n" \
  "s_nop 2\n" \
  "v_cvt_pk_bf16_f32 v182, v172, v173\n" \
  "v_cvt_pk_bf16_f32 v183, v174, v175\n" \
  "s_waitcnt vmcnt(63)\n" \
  "global_store_dwordx4 v186, v[180:183], s[46:47] offset:768 sc1\n" \
  "s_branch LB_t_e\n" \
  "LT_w:\n" \
  "L8_:\n" \
  "s_sleep 1\n" \
  "ds_read_b32 v192, v190 offset:0\n" \
  "s_waitcnt lgkmcnt(0)\n" \
  "v_readfirstlane_b32 s35, v192\n" \
  "s_cmp_lt_u32 s35, s99\n" \
  "s_cbranch_scc1 L8_\n" \
  "s_branch LT_c\n" \
  "LT_e:\n" \
  "s_bitcmp1_b32 20, s98\n" \
  "s_cbranch_scc0 LT_r\n" \
  "global_load_dwordx4 v[240:243], v238, s[48:49] offset:0 sc1\n" \
  "s_waitcnt vmcnt(0)\n" \
  "global_store_dwordx4 v238, v[240:243], s[48:49] offset:1024 sc1\n" \
  "s_branch LT_r\n" \
  "LT_a:\n" \
  "s_cmp_ge_u32 s98, 9\n" \
  "s_cbranch_scc1 LT_p\n" \
  "s_lshl4_add_u32 s35, s28, s98\n" \
  "s_cmp_eq_u32 s35, 7\n" \
  "s_cbranch_scc1 LT_b\n" \
  "s_cmp_lg_u32 s35, 6\n" \
  "s_cbranch_scc1 LT_s\n" \
  "s_memtime s[58:59]\n" \
  "s_memrealtime s[40:41]\n" \
  "s_lshl_b32 s35, s26, 2\n" \
  "v_mov_b32 v202, s35\n" \
  "s_waitcnt lgkmcnt(0)\n" \
  "s_sub_u32 s88, s58, s88\n" \
  "s_sub_u32 s88, 106000, s88\n" \
  "s_sub_u32 s90, s58, s90\n" \
  "s_sub_u32 s42, s40, s42\n" \
  "s_lshl_b32 s40, s40, 4\n" \
  "v_cvt_f32_u32 v204, s90\n" \
  "v_cvt_f32_u32 v206, s42\n" \
  "v_rcp_f32 v204, v204\n" \
  "v_cvt_f32_i32 v208, s88\n" \
  "v_mul_f32 v206, 0x410ccccd, v206\n" \
  "v_mul_f32 v204, v204, v206\n" \
  "v_mul_f32 v204, v204, v208\n" \
  "v_cvt_i32_f32 v204, v204\n" \
  "v_add_u32 v204, s40, v204\n" \
  "s_mov_b64 exec, 1\n" \
  "global_store_dword v202, v204, s[48:49] offset:2048 sc1\n" \
  "s_mov_b64 exec, -1\n" \
  "global_load_dword v193, v193, s[48:49] offset:1024 sc1\n" \
  "s_branch LT_s\n" \
  "LT_p:\n" \
  "s_cmp_eq_u32 s98, 10\n" \
  "s_cbranch_scc1 LT_q\n" \
  "s_cmp_eq_u32 s98, 11\n" \
  "s_cbranch_scc1 LT_t\n" \
  "s_mov_b64 exec, 0xff\n" \
  "global_load_dword v200, v198, s[48:49] offset:3072 sc1\n" \
  "s_mov_b64 exec, -1\n" \
  "s_branch LT_s\n" \
  "LT_q:\n" \
  "s_mov_b64 s[58:59], vcc\n" \
  "s_mov_b32 s5, 0x10000\n" \
  "LB_p:\n" \
  "s_mov_b64 exec, 0xff\n" \
  "v_cmp_le_u32 vcc, 32, v200\n" \
  "s_mov_b64 exec, -1\n" \
  "s_sub_u32 s5, s5, 1\n" \
  "s_cselect_b32 s35, 0xff, vcc_lo\n" \
  "s_cmp_eq_u32 s35, 0xff\n" \
  "s_cbranch_scc1 LB_p_x\n" \
  "s_cmp_lg_u32 s33, 1021\n" \
  "s_cbranch_scc1 LB_ne\n" \
  "global_load_dwordx4 v[240:243], v238, s[48:49] offset:0 sc1\n" \
  "s_waitcnt vmcnt(0)\n" \
  "global_store_dwordx4 v238, v[240:243], s[48:49] offset:1024 sc1\n" \
  "LB_ne:\n" \
  "s_sleep 2\n" \
  "s_mov_b64 exec, 0xff\n" \
  "global_load_dword v200, v198, s[48:49] offset:3072 sc1\n" \
  "s_mov_b64 exec, -1\n" \
  "s_waitcnt vmcnt(0)\n" \
  "s_branch LB_p\n" \
  "LB_p_x:\n" \
  "s_mov_b64 vcc, s[58:59]\n" \
  "s_lshl_b32 s35, s30, 5\n" \
  "v_lshrrev_b32 v206, 3, v255\n" \
  "v_and_b32 v208, 7, v255\n" \
  "v_lshl_add_u32 v202, v206, 2, s35\n" \
  "v_lshl_add_u32 v204, v208, 2, s35\n" \
  "global_load_dword v202, v202, s[48:49] offset:2048 sc1\n" \
  "global_load_dword v204, v204, s[48:49] offset:2048 sc1\n" \
  "s_branch LT_s\n" \
  "LT_t:\n" \
  "v_and_or_b32 v202, v202, -8, v206\n" \
  "v_and_or_b32 v204, v204, -8, v208\n" \
  "v_cmp_lt_u32 s[58:59], v204, v202\n" \
  "v_sub_u32 v202, v204, v202\n" \
  "s_lshl_b32 s35, s29, 3\n" \
  "s_lshr_b64 s[40:41], s[58:59], s35\n" \
  "s_and_b32 s40, s40, 0xff\n" \
  "s_bcnt1_i32_b32 s40, s40\n" \
  "s_sub_u32 s40, 7, s40\n" \
  "s_mov_b32 s36, s29\n" \
  "s_bfe_u32 s41, s58, 0x80000\n" \
  "s_bcnt1_i32_b32 s41, s41\n" \
  "s_cmp_eq_u32 s41, s40\n" \
  "s_cselect_b32 s36, 0, s36\n" \
  "s_bfe_u32 s41, s58, 0x80008\n" \
  "s_bcnt1_i32_b32 s41, s41\n" \
  "s_cmp_eq_u32 s41, s40\n" \
  "s_cselect_b32 s36, 1, s36\n" \
  "s_bfe_u32 s41, s58, 0x80010\n" \
  "s_bcnt1_i32_b32 s41, s41\n" \
  "s_cmp_eq_u32 s41, s40\n" \
  "s_cselect_b32 s36, 2, s36\n" \
  "s_bfe_u32 s41, s58, 0x80018\n" \
  "s_bcnt1_i32_b32 s41, s41\n" \
  "s_cmp_eq_u32 s41, s40\n" \
  "s_cselect_b32 s36, 3, s36\n" \
  "s_bfe_u32 s41, s59, 0x80000\n" \
  "s_bcnt1_i32_b32 s41, s41\n" \
  "s_cmp_eq_u32 s41, s40\n" \
  "s_cselect_b32 s36, 4, s36\n" \
  "s_bfe_u32 s41, s59, 0x80008\n" \
  "s_bcnt1_i32_b32 s41, s41\n" \
  "s_cmp_eq_u32 s41, s40\n" \
  "s_cselect_b32 s36, 5, s36\n" \
  "s_bfe_u32 s41, s59, 0x80010\n" \
  "s_bcnt1_i32_b32 s41, s41\n" \
  "s_cmp_eq_u32 s41, s40\n" \
  "s_cselect_b32 s36, 6, s36\n" \
  "s_bfe_u32 s41, s59, 0x80018\n" \
  "s_bcnt1_i32_b32 s41, s41\n" \
  "s_cmp_eq_u32 s41, s40\n" \
  "s_cselect_b32 s36, 7, s36\n" \
  "s_add_u32 s35, s35, s36\n" \
  "v_readlane_b32 s41, v202, s35\n" \
  "s_sub_u32 s35, 0, s41\n" \
  "s_cmp_lt_u32 s40, 4\n" \
  "s_cselect_b32 s41, s35, s41\n" \
  "s_min_u32 s41, s41, 0xffff\n" \
  "s_mul_i32 s41, s41, 39\n" \
  "s_add_u32 s41, s41, 28672\n" \
  "s_lshr_b32 s41, s41, 16\n" \
  "s_min_u32 s41, s41, 4\n" \
  "s_sub_u32 s35, 0, s41\n" \
  "s_cmp_lt_u32 s40, 4\n" \
  "s_cselect_b32 s35, s35, s41\n" \
  "s_lshl4_add_u32 s35, s36, s35\n" \
  "s_add_u32 s35, s35, 132\n" \
  "v_mov_b32 v204, s35\n" \
  "s_lshl_b32 s35, s28, 2\n" \
  "v_add_u32 v206, s35, v190\n" \
  "ds_write_b32 v206, v204 offset:32\n" \
  "s_branch LT_s\n" \
  "LT_b:\n" \
  "s_mov_b32 s5, 0x10000\n" \
  "LB_ec:\n" \
  "v_readfirstlane_b32 s35, v193\n" \
  "s_cmp_eq_u32 s35, s39\n" \
  "s_cbranch_scc1 LB_ok\n" \
  "s_lshl_b32 s35, s26, 2\n" \
  "v_mov_b32 v193, s35\n" \
  "s_sleep 8\n" \
  "global_load_dword v193, v193, s[48:49] offset:1024 sc1\n" \
  "s_waitcnt vmcnt(0)\n" \
  "s_sub_u32 s5, s5, 1\n" \
  "s_cbranch_scc0 LB_ec\n" \
  "LB_ok:\n" \
  "s_mov_b32 exec_hi, 0\n" \
  "global_atomic_add v196, v191, s[48:49] offset:3072 sc1\n" \
  "s_mov_b64 exec, -1\n" \
  "s_branch LT_s\n" \
  "LT_m:\n" \
  "ds_write_b16_d16_hi v195, v196 offset:0\n" \
  "ds_write_b16_d16_hi v195, v198 offset:16\n" \
  "ds_write_b16_d16_hi v195, v200 offset:32\n" \
  "ds_write_b16_d16_hi v235, v202 offset:64\n" \
  "ds_write_b16_d16_hi v235, v204 offset:80\n" \
  "ds_write_b16_d16_hi v235, v206 offset:96\n" \
  "ds_write_b16_d16_hi v235, v208 offset:112\n" \
  "ds_write_b16_d16_hi v235, v210 offset:128\n" \
  "ds_write_b16_d16_hi v235, v212 offset:144\n" \
  "ds_write_b16_d16_hi v235, v214 offset:160\n" \
  "ds_write_b16_d16_hi v235, v216 offset:176\n" \
  "ds_write_b16_d16_hi v195, v218 offset:48\n" \
  "ds_write_b16_d16_hi v195, v220 offset:192\n" \
  "ds_write_b16_d16_hi v195, v222 offset:208\n" \
  "ds_write_b16_d16_hi v195, v224 offset:224\n" \
  "ds_write_b16_d16_hi v195, v226 offset:240\n" \
  "ds_read_b128 v[244:247], v254 offset:0\n" \
  "ds_read_b128 v[248:251], v254 offset:1088\n" \
  "s_lshr_b32 s35, s7, 5\n" \
  "s_mul_i32 s35, s35, 819200\n" \
  "s_and_b32 s36, s7, 31\n" \
  "s_lshl_b32 s36, s36, 10\n" \
  "s_add_u32 s35, s35, s36\n" \
  "s_lshl_b32 s36, s4, 9\n" \
  "s_add_u32 s35, s35, s36\n" \
  "s_add_u32 s35, s35, 102400000\n" \
  "s_add_u32 s58, s24, s35\n" \
  "s_addc_u32 s59, s25, 0\n" \
  "s_waitcnt lgkmcnt(0)\n" \
  "s_cmp_ge_u32 s4, 768\n" \
  "s_cbranch_scc1 L9_\n" \
  "global_store_dwordx4 v238, v[244:247], s[58:59] sc1\n" \
  "s_cmp_ge_u32 s4, 768\n" \
  "s_cbranch_scc1 L9_\n" \
  "s_add_u32 s58, s58, 32768\n" \
  "s_addc_u32 s59, s59, 0\n" \
  "global_store_dwordx4 v238, v[248:251], s[58:59] sc1\n" \
  "L9_:\n" \
  "v_mov_b32 v234, 0\n" \
  "v_mov_b32 v236, v237\n" \
  "s_mov_b32 s38, 1\n" \
  "s_lshl_b32 s35, s29, 8\n" \
  "v_lshl_add_u32 v196, v255, 11, s35\n" \
  "s_lshl_b32 s35, s30, 11\n" \
  "v_lshl_add_u32 v198, v255, 8, s35\n" \
  "s_branch LT\n" \
  "LB_t_h:\n" \
  "s_waitcnt lgkmcnt(14)\n" \
  "v_mfma_f32_16x16x32_bf16 v[160:163], v[128:131], v[64:67], 0\n" \
  "s_nop 2\n" \
  "s_waitcnt lgkmcnt(12)\n" \
  "v_mfma_f32_16x16x32_bf16 v[164:167], v[144:147], v[64:67], 0\n" \
  "s_nop 2\n" \
  "s_waitcnt lgkmcnt(10)\n" \
  "v_mfma_f32_16x16x32_bf16 v[160:163], v[132:135], v[68:71], v[160:163]\n" \
  "s_nop 2\n" \
  "s_waitcnt lgkmcnt(8)\n" \
  "v_mfma_f32_16x16x32_bf16 v[164:167], v[148:151], v[68:71], v[164:167]\n" \
  "s_nop 2\n" \
  "s_waitcnt lgkmcnt(6)\n" \
  "v_mfma_f32_16x16x32_bf16 v[160:163], v[136:139], v[72:75], v[160:163]\n" \
  "s_nop 2\n" \
  "s_waitcnt lgkmcnt(4)\n" \
  "v_mfma_f32_16x16x32_bf16 v[164:167], v[152:155], v[72:75], v[164:167]\n" \
  "s_nop 2\n" \
  "s_waitcnt lgkmcnt(2)\n" \
  "v_mfma_f32_16x16x32_bf16 v[160:163], v[140:143], v[76:79], v[160:163]\n" \
  "s_nop 2\n" \
  "s_waitcnt lgkmcnt(0)\n" \
  "v_mfma_f32_16x16x32_bf16 v[164:167], v[156:159], v[76:79], v[164:167]\n" \
  "s_nop 3\n" \
  "v_mfma_f32_16x16x32_bf16 v[168:171], v[128:131], v[80:83], 0\n" \
  "s_nop 3\n" \
  "v_mfma_f32_16x16x32_bf16 v[172:175], v[144:147], v[80:83], 0\n" \
  "s_nop 3\n" \
  "v_mfma_f32_16x16x32_bf16 v[168:171], v[132:135], v[84:87], v[168:171]\n" \
  "s_nop 3\n" \
  "v_mfma_f32_16x16x32_bf16 v[172:175], v[148:151], v[84:87], v[172:175]\n" \
  "v_cvt_pk_bf16_f32 v176, v160, v161\n" \
  "s_nop 2\n" \
  "v_mfma_f32_16x16x32_bf16 v[168:171], v[136:139], v[88:91], v[168:171]\n" \
  "v_cvt_pk_bf16_f32 v177, v162, v163\n" \
  "s_nop 2\n" \
  "v_mfma_f32_16x16x32_bf16 v[172:175], v[152:155], v[88:91], v[172:175]\n" \
  "v_cvt_pk_bf16_f32 v178, v164, v165\n" \
  "s_nop 2\n" \
  "v_mfma_f32_16x16x32_bf16 v[168:171], v[140:143], v[92:95], v[168:171]\n" \
  "v_cvt_pk_bf16_f32 v179, v166, v167\n" \
  "s_nop 2\n" \
  "v_mfma_f32_16x16x32_bf16 v[172:175], v[156:159], v[92:95], v[172:175]\n" \
  "s_waitcnt vmcnt(63)\n" \
  "global_store_dwordx4 v186, v[176:179], s[46:47] offset:1024 sc1\n" \
  "s_nop 1\n" \
  "v_mfma_f32_16x16x32_bf16 v[160:163], v[128:131], v[96:99], 0\n" \
  "s_nop 3\n" \
  "v_mfma_f32_16x16x32_bf16 v[164:167], v[144:147], v[96:99], 0\n" \
  "s_nop 3\n" \
  "v_mfma_f32_16x16x32_bf16 v[160:163], v[132:135], v[100:103], v[160:163]\n" \
  "s_nop 3\n" \
  "v_mfma_f32_16x16x32_bf16 v[164:167], v[148:151], v[100:103], v[164:167]\n" \
  "v_cvt_pk_bf16_f32 v180, v168, v169\n" \
  "s_nop 2\n" \
  "v_mfma_f32_16x16x32_bf16 v[160:163], v[136:139], v[104:107], v[160:163]\n" \
  "v_cvt_pk_bf16_f32 v181, v170, v171\n" \
  "s_nop 2\n" \
  "v_mfma_f32_16x16x32_bf16 v[164:167], v[152:155], v[104:107], v[164:167]\n" \
  "v_cvt_pk_bf16_f32 v182, v172, v173\n" \
  "s_nop 2\n" \
  "v_mfma_f32_16x16x32_bf16 v[160:163], v[140:143], v[108:111], v[160:163]\n" \
  "v_cvt_pk_bf16_f32 v183, v174, v175\n" \
  "s_nop 2\n" \
  "v_mfma_f32_16x16x32_bf16 v[164:167], v[156:159], v[108:111], v[164:167]\n" \
  "s_waitcnt vmcnt(63)\n" \
  "global_store_dwordx4 v186, v[180:183], s[46:47] offset:1280 sc1\n" \
  "s_nop 1\n" \
  "v_mfma_f32_16x16x32_bf16 v[168:171], v[128:131], v[112:115], 0\n" \
  "s_nop 3\n" \
  "v_mfma_f32_16x16x32_bf16 v[172:175], v[144:147], v[112:115], 0\n" \
  "s_nop 3\n" \
  "v_mfma_f32_16x16x32_bf16 v[168:171], v[132:135], v[116:119], v[168:171]\n" \
  "s_nop 3\n" \
  "v_mfma_f32_16x16x32_bf16 v[172:175], v[148:151], v[116:119], v[172:175]\n" \
  "v_cvt_pk_bf16_f32 v176, v160, v161\n" \
  "s_nop 2\n" \
  "v_mfma_f32_16x16x32_bf16 v[168:171], v[136:139], v[120:123], v[168:171]\n" \
  "v_cvt_pk_bf16_f32 v177, v162, v163\n" \
  "s_nop 2\n" \
  "v_mfma_f32_16x16x32_bf16 v[172:175], v[152:155], v[120:123], v[172:175]\n" \
  "v_cvt_pk_bf16_f32 v178, v164, v165\n" \
  "s_nop 2\n" \
  "v_mfma_f32_16x16x32_bf16 v[168:171], v[140:143], v[124:127], v[168:171]\n" \
  "v_cvt_pk_bf16_f32 v179, v166, v167\n" \
  "s_nop 2\n" \
  "v_mfma_f32_16x16x32_bf16 v[172:175], v[156:159], v[124:127], v[172:175]\n" \
  "s_waitcnt vmcnt(63)\n" \
  "global_store_dwordx4 v187, v[176:179], s[46:47] offset:0 sc1\n" \
  "s_nop 0\n" \
  "v_cvt_pk_bf16_f32 v180, v168, v169\n" \
  "v_cvt_pk_bf16_f32 v181, v170, v171\n" \
  "s_nop 2\n" \
  "v_cvt_pk_bf16_f32 v182, v172, v173\n" \
  "v_cvt_pk_bf16_f32 v183, v174, v175\n" \
  "s_waitcnt vmcnt(63)\n" \
  "global_store_dwordx4 v188, v[180:183], s[46:47] offset:0 sc1\n" \
  "LB_t_e:\n" \
  "s_setprio 2\n" \
  "v_mbcnt_lo_u32_b32 v189, -1, 0\n" \
  "v_mbcnt_hi_u32_b32 v189, -1, v189\n" \
  "v_lshlrev_b32 v188, 4, v189\n" \
  "s_lshl_b32 s35, s28, 12\n" \
  "v_add_u32 v128, s35, v188\n" \
  "v_mov_b32 v129, 1\n" \
  "s_lshr_b32 s35, s30, 4\n" \
  "s_lshl1_add_u32 s35, s29, s35\n" \
  "s_lshl_b32 s35, s35, 6\n" \
  "s_bfe_u32 s37, s30, 0x20002\n" \
  "s_lshl3_add_u32 s35, s37, s35\n" \
  "v_and_b32 v190, 16, v189\n" \
  "v_lshl_add_u32 v190, v190, 1, s35\n" \
  "global_load_dwordx2 v[204:205], v190, s[16:17]\n" \
  "global_load_dwordx2 v[206:207], v190, s[18:19]\n" \
  "v_lshrrev_b32 v180, 1, v189\n" \
  "v_xor_b32 v180, v180, v189\n" \
  "v_and_b32 v180, 8, v180\n" \
  "v_bfe_u32 v181, v189, 2, 1\n" \
  "v_lshlrev_b32_e64 v181, v181, 5\n" \
  "v_lshlrev_b32 v180, v180, v181\n" \
  "v_bfe_u32 v181, v189, 2, 2\n" \
  "v_lshlrev_b32_e64 v181, v181, 1\n" \
  "v_cmp_gt_u32 vcc, 32, v189\n" \
  "v_mov_b32 v191, 0x3f80\n" \
  "v_add_u32 v190, 32000, v188\n" \
  "v_cndmask_b32 v192, v181, v180, vcc\n" \
  "v_bfe_i32 v180, v192, 0, 1\n" \
  "v_and_b32 v180, v180, v191\n" \
  "v_bfe_i32 v181, v192, 1, 1\n" \
  "v_and_b32 v181, v181, v191\n" \
  "v_bfe_i32 v182, v192, 2, 1\n" \
  "v_and_b32 v182, v182, v191\n" \
  "v_bfe_i32 v183, v192, 3, 1\n" \
  "v_and_b32 v183, v183, v191\n" \
  "v_and_b32 v192, 3, v189\n" \
  "v_mul_u32_u24 v192, 0x5555, v192\n" \
  "v_add_u32 v189, 16000, v188\n" \
  "v_add_u32 v191, 48000, v188\n" \
  "s_waitcnt vmcnt(0)\n" \
  "v_lshlrev_b32 v184, 16, v204\n" \
  "v_and_b32 v185, 0xffff0000, v204\n" \
  "v_lshlrev_b32 v186, 16, v205\n" \
  "v_and_b32 v187, 0xffff0000, v205\n" \
  "s_setprio 0\n" \
  "s_barrier\n" \
  "s_cmp_lg_u32 s28, 0\n" \
  "s_cbranch_scc1 LB_nb\n" \
  "s_mov_b32 exec_hi, 0\n" \
  "global_atomic_add v196, v129, s[48:49] offset:3076 sc1\n" \
  "s_mov_b64 exec, -1\n" \
  "LB_nb:\n" \
  "s_bitcmp1_b32 s30, 2\n" \
  "s_cbranch_scc1 LB_w_h\n" \
  "ds_write_b128 v128, v[64:67] offset:0\n" \
  "ds_write_b128 v128, v[68:71] offset:1024\n" \
  "ds_write_b128 v128, v[72:75] offset:2048\n" \
  "ds_write_b128 v128, v[76:79] offset:3072\n" \
  "s_add_u32 s35, s59, 64000\n" \
  "s_add_u32 s54, s54, s35\n" \
  "s_addc_u32 s55, s55, 0\n" \
  "s_add_u32 s58, s59, 0x10000\n" \
  "s_mov_b32 m0, s58\n" \
  "s_add_u32 s35, s58, 0x8000\n" \
  "global_load_lds_dwordx4 v188, s[54:55] sc1\n" \
  "global_load_lds_dwordx4 v188, s[54:55] offset:1024 sc1\n" \
  "ds_write_b128 v128, v[80:83] offset:16384\n" \
  "ds_write_b128 v128, v[84:87] offset:17408\n" \
  "ds_write_b128 v128, v[88:91] offset:18432\n" \
  "ds_write_b128 v128, v[92:95] offset:19456\n" \
  "ds_write_b128 v128, v[96:99] offset:32768\n" \
  "ds_write_b128 v128, v[100:103] offset:33792\n" \
  "ds_write_b128 v128, v[104:107] offset:34816\n" \
  "ds_write_b128 v128, v[108:111] offset:35840\n" \
  "s_add_u32 m0, s58, 0x4000\n" \
  "s_cmp_lt_u32 s35, 0x28000\n" \
  "global_load_lds_dwordx4 v189, s[54:55] sc1\n" \
  "global_load_lds_dwordx4 v189, s[54:55] offset:1024 sc1\n" \
  "s_cselect_b32 s58, s35, s59\n" \
  "s_add_u32 s54, s54, 32000\n" \
  "s_addc_u32 s55, s55, 0\n" \
  "ds_write_b128 v128, v[112:115] offset:49152\n" \
  "ds_write_b128 v128, v[116:119] offset:50176\n" \
  "ds_write_b128 v128, v[120:123] offset:51200\n" \
  "ds_write_b128 v128, v[124:127] offset:52224\n" \
  "s_branch LB_w_e\n" \
  "LB_w_h:\n" \
  "ds_write_b128 v128, v[0:3] offset:0\n" \
  "ds_write_b128 v128, v[4:7] offset:1024\n" \
  "ds_write_b128 v128, v[8:11] offset:2048\n" \
  "ds_write_b128 v128, v[12:15] offset:3072\n" \
  "s_add_u32 s35, s59, 64000\n" \
  "s_add_u32 s54, s54, s35\n" \
  "s_addc_u32 s55, s55, 0\n" \
  "s_add_u32 s58, s59, 0x10000\n" \
  "s_mov_b32 m0, s58\n" \
  "s_add_u32 s35, s58, 0x8000\n" \
  "global_load_lds_dwordx4 v188, s[54:55] sc1\n" \
  "global_load_lds_dwordx4 v188, s[54:55] offset:1024 sc1\n" \
  "ds_write_b128 v128, v[16:19] offset:16384\n" \
  "ds_write_b128 v128, v[20:23] offset:17408\n" \
  "ds_write_b128 v128, v[24:27] offset:18432\n" \
  "ds_write_b128 v128, v[28:31] offset:19456\n" \
  "ds_write_b128 v128, v[32:35] offset:32768\n" \
  "ds_write_b128 v128, v[36:39] offset:33792\n" \
  "ds_write_b128 v128, v[40:43] offset:34816\n" \
  "ds_write_b128 v128, v[44:47] offset:35840\n" \
  "s_add_u32 m0, s58, 0x4000\n" \
  "s_cmp_lt_u32 s35, 0x28000\n" \
  "global_load_lds_dwordx4 v189, s[54:55] sc1\n" \
  "global_load_lds_dwordx4 v189, s[54:55] offset:1024 sc1\n" \
  "s_cselect_b32 s58, s35, s59\n" \
  "s_add_u32 s54, s54, 32000\n" \
  "s_addc_u32 s55, s55, 0\n" \
  "ds_write_b128 v128, v[48:51] offset:49152\n" \
  "ds_write_b128 v128, v[52:55] offset:50176\n" \
  "ds_write_b128 v128, v[56:59] offset:51200\n" \
  "ds_write_b128 v128, v[60:63] offset:52224\n" \
  "LB_w_e:\n" \
  "s_waitcnt lgkmcnt(0)\n" \
  "s_mov_b32 s51, 7\n" \
  "s_setprio 0\n" \
  "s_barrier\n" \
  "LG_start:\n" \
  "v_lshlrev_b32_sdwa v64, 4, v208 dst_sel:DWORD dst_unused:UNUSED_PAD src0_sel:DWORD src1_sel:WORD_0\n" \
  "ds_read_b128 v[68:71], v64\n" \
  "v_mov_b64 v[0:1], v[184:185]\n" \
  "v_lshlrev_b32_sdwa v65, 4, v208 dst_sel:DWORD dst_unused:UNUSED_PAD src0_sel:DWORD src1_sel:WORD_1\n" \
  "ds_read_b128 v[72:75], v65\n" \
  "v_mov_b64 v[2:3], v[186:187]\n" \
  "v_lshlrev_b32_sdwa v66, 4, v209 dst_sel:DWORD dst_unused:UNUSED_PAD src0_sel:DWORD src1_sel:WORD_0\n" \
  "ds_read_b128 v[76:79], v66\n" \
  "v_mov_b64 v[4:5], v[184:185]\n" \
  "v_lshlrev_b32_sdwa v67, 4, v209 dst_sel:DWORD dst_unused:UNUSED_PAD src0_sel:DWORD src1_sel:WORD_1\n" \
  "ds_read_b128 v[80:83], v67\n" \
  "v_mov_b64 v[6:7], v[186:187]\n" \
  "s_mov_b32 m0, s58\n" \
  "s_add_u32 s35, s58, 0x8000\n" \
  "global_load_lds_dwordx4 v188, s[54:55] sc1\n" \
  "global_load_lds_dwordx4 v188, s[54:55] offset:1024 sc1\n" \
  "v_lshlrev_b32_sdwa v64, 4, v210 dst_sel:DWORD dst_unused:UNUSED_PAD src0_sel:DWORD src1_sel:WORD_0\n" \
  "ds_read_b128 v[84:87], v64\n" \
  "v_mov_b64 v[8:9], v[184:185]\n" \
  "v_lshlrev_b32_sdwa v65, 4, v210 dst_sel:DWORD dst_unused:UNUSED_PAD src0_sel:DWORD src1_sel:WORD_1\n" \
  "ds_read_b128 v[88:91], v65\n" \
  "v_mov_b64 v[10:11], v[186:187]\n" \
  "v_lshlrev_b32_sdwa v66, 4, v211 dst_sel:DWORD dst_unused:UNUSED_PAD src0_sel:DWORD src1_sel:WORD_0\n" \
  "ds_read_b128 v[92:95], v66\n" \
  "v_mov_b64 v[12:13], v[184:185]\n" \
  "v_lshlrev_b32_sdwa v67, 4, v211 dst_sel:DWORD dst_unused:UNUSED_PAD src0_sel:DWORD src1_sel:WORD_1\n" \
  "ds_read_b128 v[96:99], v67\n" \
  "v_mov_b64 v[14:15], v[186:187]\n" \
  "s_add_u32 m0, s58, 0x4000\n" \
  "s_cmp_lt_u32 s35, 0x28000\n" \
  "global_load_lds_dwordx4 v189, s[54:55] sc1\n" \
  "global_load_lds_dwordx4 v189, s[54:55] offset:1024 sc1\n" \
  "s_cselect_b32 s58, s35, s59\n" \
  "v_lshlrev_b32_sdwa v64, 4, v212 dst_sel:DWORD dst_unused:UNUSED_PAD src0_sel:DWORD src1_sel:WORD_0\n" \
  "ds_read_b128 v[100:103], v64\n" \
  "v_mov_b64 v[16:17], v[184:185]\n" \
  "v_lshlrev_b32_sdwa v65, 4, v212 dst_sel:DWORD dst_unused:UNUSED_PAD src0_sel:DWORD src1_sel:WORD_1\n" \
  "ds_read_b128 v[104:107], v65\n" \
  "v_mov_b64 v[18:19], v[186:187]\n" \
  "v_lshlrev_b32_sdwa v66, 4, v213 dst_sel:DWORD dst_unused:UNUSED_PAD src0_sel:DWORD src1_sel:WORD_0\n" \
  "ds_read_b128 v[108:111], v66\n" \
  "v_mov_b64 v[20:21], v[184:185]\n" \
  "v_lshlrev_b32_sdwa v67, 4, v213 dst_sel:DWORD dst_unused:UNUSED_PAD src0_sel:DWORD src1_sel:WORD_1\n" \
  "ds_read_b128 v[112:115], v67\n" \
  "v_mov_b64 v[22:23], v[186:187]\n" \
  "s_waitcnt lgkmcnt(10)\n" \
  "v_smfmac_f32_16x16x64_bf16 v[0:3], v[180:183], v[68:75], v192\n" \
  "v_lshlrev_b32_sdwa v64, 4, v214 dst_sel:DWORD dst_unused:UNUSED_PAD src0_sel:DWORD src1_sel:WORD_0\n" \
  "ds_read_b128 v[68:71], v64\n" \
  "v_mov_b64 v[24:25], v[184:185]\n" \
  "v_lshlrev_b32_sdwa v65, 4, v214 dst_sel:DWORD dst_unused:UNUSED_PAD src0_sel:DWORD src1_sel:WORD_1\n" \
  "ds_read_b128 v[72:75], v65\n" \
  "v_mov_b64 v[26:27], v[186:187]\n" \
  "s_mov_b32 m0, s58\n" \
  "s_add_u32 s35, s58, 0x8000\n" \
  "global_load_lds_dwordx4 v190, s[54:55] sc1\n" \
  "global_load_lds_dwordx4 v190, s[54:55] offset:1024 sc1\n" \
  "s_waitcnt lgkmcnt(10)\n" \
  "v_smfmac_f32_16x16x64_bf16 v[4:7], v[180:183], v[76:83], v192\n" \
  "v_lshlrev_b32_sdwa v66, 4, v215 dst_sel:DWORD dst_unused:UNUSED_PAD src0_sel:DWORD src1_sel:WORD_0\n" \
  "ds_read_b128 v[76:79], v66\n" \
  "v_mov_b64 v[28:29], v[184:185]\n" \
  "v_lshlrev_b32_sdwa v67, 4, v215 dst_sel:DWORD dst_unused:UNUSED_PAD src0_sel:DWORD src1_sel:WORD_1\n" \
  "ds_read_b128 v[80:83], v67\n" \
  "v_mov_b64 v[30:31], v[186:187]\n" \
  "s_waitcnt lgkmcnt(10)\n" \
  "v_smfmac_f32_16x16x64_bf16 v[8:11], v[180:183], v[84:91], v192\n" \
  "v_lshlrev_b32_sdwa v64, 4, v216 dst_sel:DWORD dst_unused:UNUSED_PAD src0_sel:DWORD src1_sel:WORD_0\n" \
  "ds_read_b128 v[84:87], v64\n" \
  "v_mov_b64 v[32:33], v[184:185]\n" \
  "v_lshlrev_b32_sdwa v65, 4, v216 dst_sel:DWORD dst_unused:UNUSED_PAD src0_sel:DWORD src1_sel:WORD_1\n" \
  "ds_read_b128 v[88:91], v65\n" \
  "v_mov_b64 v[34:35], v[186:187]\n" \
  "s_add_u32 m0, s58, 0x4000\n" \
  "s_cmp_lt_u32 s35, 0x28000\n" \
  "global_load_lds_dwordx4 v191, s[54:55] sc1\n" \
  "global_load_lds_dwordx4 v191, s[54:55] offset:1024 sc1\n" \
  "s_cselect_b32 s58, s35, s59\n" \
  "s_waitcnt lgkmcnt(10)\n" \
  "v_smfmac_f32_16x16x64_bf16 v[12:15], v[180:183], v[92:99], v192\n" \
  "v_lshlrev_b32_sdwa v66, 4, v217 dst_sel:DWORD dst_unused:UNUSED_PAD src0_sel:DWORD src1_sel:WORD_0\n" \
  "ds_read_b128 v[92:95], v66\n" \
  "v_mov_b64 v[36:37], v[184:185]\n" \
  "v_lshlrev_b32_sdwa v67, 4, v217 dst_sel:DWORD dst_unused:UNUSED_PAD src0_sel:DWORD src1_sel:WORD_1\n" \
  "ds_read_b128 v[96:99], v67\n" \
  "v_mov_b64 v[38:39], v[186:187]\n" \
  "s_waitcnt lgkmcnt(10)\n" \
  "v_smfmac_f32_16x16x64_bf16 v[16:19], v[180:183], v[100:107], v192\n" \
  "v_lshlrev_b32_sdwa v64, 4, v218 dst_sel:DWORD dst_unused:UNUSED_PAD src0_sel:DWORD src1_sel:WORD_0\n" \
  "ds_read_b128 v[100:103], v64\n" \
  "v_mov_b64 v[40:41], v[184:185]\n" \
  "v_lshlrev_b32_sdwa v65, 4, v218 dst_sel:DWORD dst_unused:UNUSED_PAD src0_sel:DWORD src1_sel:WORD_1\n" \
  "ds_read_b128 v[104:107], v65\n" \
  "v_mov_b64 v[42:43], v[186:187]\n" \
  "global_load_dwordx4 v[240:243], v188, s[56:57] sc1\n" \
  "global_load_dwordx4 v[244:247], v188, s[56:57] offset:1024 sc1\n" \
  "s_waitcnt lgkmcnt(10)\n" \
  "v_smfmac_f32_16x16x64_bf16 v[20:23], v[180:183], v[108:115], v192\n" \
  "v_lshlrev_b32_sdwa v66, 4, v219 dst_sel:DWORD dst_unused:UNUSED_PAD src0_sel:DWORD src1_sel:WORD_0\n" \
  "ds_read_b128 v[108:111], v66\n" \
  "v_mov_b64 v[44:45], v[184:185]\n" \
  "v_lshlrev_b32_sdwa v67, 4, v219 dst_sel:DWORD dst_unused:UNUSED_PAD src0_sel:DWORD src1_sel:WORD_1\n" \
  "ds_read_b128 v[112:115], v67\n" \
  "v_mov_b64 v[46:47], v[186:187]\n" \
  "s_waitcnt lgkmcnt(10)\n" \
  "v_smfmac_f32_16x16x64_bf16 v[24:27], v[180:183], v[68:75], v192\n" \
  "v_lshlrev_b32_sdwa v64, 4, v220 dst_sel:DWORD dst_unused:UNUSED_PAD src0_sel:DWORD src1_sel:WORD_0\n" \
  "ds_read_b128 v[68:71], v64\n" \
  "v_mov_b64 v[48:49], v[184:185]\n" \
  "v_lshlrev_b32_sdwa v65, 4, v220 dst_sel:DWORD dst_unused:UNUSED_PAD src0_sel:DWORD src1_sel:WORD_1\n" \
  "ds_read_b128 v[72:75], v65\n" \
  "v_mov_b64 v[50:51], v[186:187]\n" \
  "global_load_dwordx4 v[248:251], v188, s[56:57] offset:2048 sc1\n" \
  "global_load_dwordx4 v[252:255], v188, s[56:57] offset:3072 sc1\n" \
  "s_waitcnt lgkmcnt(10)\n" \
  "v_smfmac_f32_16x16x64_bf16 v[28:31], v[180:183], v[76:83], v192\n" \
  "v_lshlrev_b32_sdwa v66, 4, v221 dst_sel:DWORD dst_unused:UNUSED_PAD src0_sel:DWORD src1_sel:WORD_0\n" \
  "ds_read_b128 v[76:79], v66\n" \
  "v_mov_b64 v[52:53], v[184:185]\n" \
  "v_lshlrev_b32_sdwa v67, 4, v221 dst_sel:DWORD dst_unused:UNUSED_PAD src0_sel:DWORD src1_sel:WORD_1\n" \
  "ds_read_b128 v[80:83], v67\n" \
  "v_mov_b64 v[54:55], v[186:187]\n" \
  "s_waitcnt lgkmcnt(10)\n" \
  "v_smfmac_f32_16x16x64_bf16 v[32:35], v[180:183], v[84:91], v192\n" \
  "v_lshlrev_b32_sdwa v64, 4, v222 dst_sel:DWORD dst_unused:UNUSED_PAD src0_sel:DWORD src1_sel:WORD_0\n" \
  "ds_read_b128 v[84:87], v64\n" \
  "v_mov_b64 v[56:57], v[184:185]\n" \
  "v_lshlrev_b32_sdwa v65, 4, v222 dst_sel:DWORD dst_unused:UNUSED_PAD src0_sel:DWORD src1_sel:WORD_1\n" \
  "ds_read_b128 v[88:91], v65\n" \
  "v_mov_b64 v[58:59], v[186:187]\n" \
  "s_waitcnt lgkmcnt(10)\n" \
  "v_smfmac_f32_16x16x64_bf16 v[36:39], v[180:183], v[92:99], v192\n" \
  "v_lshlrev_b32_sdwa v66, 4, v223 dst_sel:DWORD dst_unused:UNUSED_PAD src0_sel:DWORD src1_sel:WORD_0\n" \
  "ds_read_b128 v[92:95], v66\n" \
  "v_mov_b64 v[60:61], v[184:185]\n" \
  "v_lshlrev_b32_sdwa v67, 4, v223 dst_sel:DWORD dst_unused:UNUSED_PAD src0_sel:DWORD src1_sel:WORD_1\n" \
  "ds_read_b128 v[96:99], v67\n" \
  "v_mov_b64 v[62:63], v[186:187]\n" \
  "s_add_u32 s56, s56, 32768\n" \
  "s_addc_u32 s57, s57, 0\n" \
  "s_add_u32 s54, s54, 64000\n" \
  "s_addc_u32 s55, s55, 0\n" \
  "s_waitcnt lgkmcnt(10)\n" \
  "v_smfmac_f32_16x16x64_bf16 v[40:43], v[180:183], v[100:107], v192\n" \
  "s_waitcnt lgkmcnt(8)\n" \
  "v_smfmac_f32_16x16x64_bf16 v[44:47], v[180:183], v[108:115], v192\n" \
  "s_waitcnt vmcnt(8)\n" \
  "s_barrier\n" \
  "LG_loop:\n" \
  "v_lshlrev_b32_sdwa v64, 4, v224 dst_sel:DWORD dst_unused:UNUSED_PAD src0_sel:DWORD src1_sel:WORD_0\n" \
  "ds_read_b128 v[100:103], v64\n" \
  "v_lshlrev_b32_sdwa v65, 4, v224 dst_sel:DWORD dst_unused:UNUSED_PAD src0_sel:DWORD src1_sel:WORD_1\n" \
  "ds_read_b128 v[104:107], v65\n" \
  "v_lshlrev_b32_sdwa v66, 4, v225 dst_sel:DWORD dst_unused:UNUSED_PAD src0_sel:DWORD src1_sel:WORD_0\n" \
  "ds_read_b128 v[108:111], v66\n" \
  "v_lshlrev_b32_sdwa v67, 4, v225 dst_sel:DWORD dst_unused:UNUSED_PAD src0_sel:DWORD src1_sel:WORD_1\n" \
  "ds_read_b128 v[112:115], v67\n" \
  "s_waitcnt lgkmcnt(10)\n" \
  "v_smfmac_f32_16x16x64_bf16 v[48:51], v[180:183], v[68:75], v192\n" \
  "s_waitcnt lgkmcnt(8)\n" \
  "v_smfmac_f32_16x16x64_bf16 v[52:55], v[180:183], v[76:83], v192\n" \
  "s_waitcnt lgkmcnt(6)\n" \
  "v_smfmac_f32_16x16x64_bf16 v[56:59], v[180:183], v[84:91], v192\n" \
  "s_waitcnt lgkmcnt(4)\n" \
  "v_smfmac_f32_16x16x64_bf16 v[60:63], v[180:183], v[92:99], v192\n" \
  "s_barrier\n" \
  "s_mov_b32 m0, s58\n" \
  "s_add_u32 s35, s58, 0x8000\n" \
  "global_load_lds_dwordx4 v188, s[54:55] sc1\n" \
  "global_load_lds_dwordx4 v188, s[54:55] offset:1024 sc1\n" \
  "v_lshlrev_b32_sdwa v64, 4, v226 dst_sel:DWORD dst_unused:UNUSED_PAD src0_sel:DWORD src1_sel:WORD_0\n" \
  "ds_read_b128 v[68:71], v64\n" \
  "v_lshlrev_b32_sdwa v65, 4, v226 dst_sel:DWORD dst_unused:UNUSED_PAD src0_sel:DWORD src1_sel:WORD_1\n" \
  "ds_read_b128 v[72:75], v65\n" \
  "v_lshlrev_b32_sdwa v66, 4, v227 dst_sel:DWORD dst_unused:UNUSED_PAD src0_sel:DWORD src1_sel:WORD_0\n" \
  "ds_read_b128 v[76:79], v66\n" \
  "v_lshlrev_b32_sdwa v67, 4, v227 dst_sel:DWORD dst_unused:UNUSED_PAD src0_sel:DWORD src1_sel:WORD_1\n" \
  "ds_read_b128 v[80:83], v67\n" \
  "s_add_u32 m0, s58, 0x4000\n" \
  "s_cmp_lt_u32 s35, 0x28000\n" \
  "global_load_lds_dwordx4 v189, s[54:55] sc1\n" \
  "global_load_lds_dwordx4 v189, s[54:55] offset:1024 sc1\n" \
  "s_cselect_b32 s58, s35, s59\n" \
  "v_lshlrev_b32_sdwa v64, 4, v228 dst_sel:DWORD dst_unused:UNUSED_PAD src0_sel:DWORD src1_sel:WORD_0\n" \
  "ds_read_b128 v[84:87], v64\n" \
  "v_lshlrev_b32_sdwa v65, 4, v228 dst_sel:DWORD dst_unused:UNUSED_PAD src0_sel:DWORD src1_sel:WORD_1\n" \
  "ds_read_b128 v[88:91], v65\n" \
  "v_lshlrev_b32_sdwa v66, 4, v229 dst_sel:DWORD dst_unused:UNUSED_PAD src0_sel:DWORD src1_sel:WORD_0\n" \
  "ds_read_b128 v[92:95], v66\n" \
  "v_lshlrev_b32_sdwa v67, 4, v229 dst_sel:DWORD dst_unused:UNUSED_PAD src0_sel:DWORD src1_sel:WORD_1\n" \
  "ds_read_b128 v[96:99], v67\n" \
  "s_waitcnt lgkmcnt(10)\n" \
  "v_smfmac_f32_16x16x64_bf16 v[0:3], v[180:183], v[100:107], v192\n" \
  "v_lshlrev_b32_sdwa v64, 4, v230 dst_sel:DWORD dst_unused:UNUSED_PAD src0_sel:DWORD src1_sel:WORD_0\n" \
  "ds_read_b128 v[100:103], v64\n" \
  "v_lshlrev_b32_sdwa v65, 4, v230 dst_sel:DWORD dst_unused:UNUSED_PAD src0_sel:DWORD src1_sel:WORD_1\n" \
  "ds_read_b128 v[104:107], v65\n" \
  "s_mov_b32 m0, s58\n" \
  "s_add_u32 s35, s58, 0x8000\n" \
  "global_load_lds_dwordx4 v190, s[54:55] sc1\n" \
  "global_load_lds_dwordx4 v190, s[54:55] offset:1024 sc1\n" \
  "s_waitcnt lgkmcnt(10)\n" \
  "v_smfmac_f32_16x16x64_bf16 v[4:7], v[180:183], v[108:115], v192\n" \
  "v_lshlrev_b32_sdwa v66, 4, v231 dst_sel:DWORD dst_unused:UNUSED_PAD src0_sel:DWORD src1_sel:WORD_0\n" \
  "ds_read_b128 v[108:111], v66\n" \
  "v_lshlrev_b32_sdwa v67, 4, v231 dst_sel:DWORD dst_unused:UNUSED_PAD src0_sel:DWORD src1_sel:WORD_1\n" \
  "ds_read_b128 v[112:115], v67\n" \
  "s_waitcnt lgkmcnt(10)\n" \
  "v_smfmac_f32_16x16x64_bf16 v[8:11], v[180:183], v[68:75], v192\n" \
  "v_lshlrev_b32_sdwa v64, 4, v232 dst_sel:DWORD dst_unused:UNUSED_PAD src0_sel:DWORD src1_sel:WORD_0\n" \
  "ds_read_b128 v[68:71], v64\n" \
  "v_lshlrev_b32_sdwa v65, 4, v232 dst_sel:DWORD dst_unused:UNUSED_PAD src0_sel:DWORD src1_sel:WORD_1\n" \
  "ds_read_b128 v[72:75], v65\n" \
  "s_add_u32 m0, s58, 0x4000\n" \
  "s_cmp_lt_u32 s35, 0x28000\n" \
  "global_load_lds_dwordx4 v191, s[54:55] sc1\n" \
  "global_load_lds_dwordx4 v191, s[54:55] offset:1024 sc1\n" \
  "s_cselect_b32 s58, s35, s59\n" \
  "s_waitcnt lgkmcnt(10)\n" \
  "v_smfmac_f32_16x16x64_bf16 v[12:15], v[180:183], v[76:83], v192\n" \
  "v_lshlrev_b32_sdwa v66, 4, v233 dst_sel:DWORD dst_unused:UNUSED_PAD src0_sel:DWORD src1_sel:WORD_0\n" \
  "ds_read_b128 v[76:79], v66\n" \
  "v_lshlrev_b32_sdwa v67, 4, v233 dst_sel:DWORD dst_unused:UNUSED_PAD src0_sel:DWORD src1_sel:WORD_1\n" \
  "ds_read_b128 v[80:83], v67\n" \
  "s_waitcnt lgkmcnt(10)\n" \
  "v_smfmac_f32_16x16x64_bf16 v[16:19], v[180:183], v[84:91], v192\n" \
  "v_lshlrev_b32_sdwa v64, 4, v234 dst_sel:DWORD dst_unused:UNUSED_PAD src0_sel:DWORD src1_sel:WORD_0\n" \
  "ds_read_b128 v[84:87], v64\n" \
  "v_lshlrev_b32_sdwa v65, 4, v234 dst_sel:DWORD dst_unused:UNUSED_PAD src0_sel:DWORD src1_sel:WORD_1\n" \
  "ds_read_b128 v[88:91], v65\n" \
  "global_load_dwordx4 v[208:211], v188, s[56:57] sc1\n" \
  "global_load_dwordx4 v[212:215], v188, s[56:57] offset:1024 sc1\n" \
  "s_waitcnt lgkmcnt(10)\n" \
  "v_smfmac_f32_16x16x64_bf16 v[20:23], v[180:183], v[92:99], v192\n" \
  "v_lshlrev_b32_sdwa v66, 4, v235 dst_sel:DWORD dst_unused:UNUSED_PAD src0_sel:DWORD src1_sel:WORD_0\n" \
  "ds_read_b128 v[92:95], v66\n" \
  "v_lshlrev_b32_sdwa v67, 4, v235 dst_sel:DWORD dst_unused:UNUSED_PAD src0_sel:DWORD src1_sel:WORD_1\n" \
  "ds_read_b128 v[96:99], v67\n" \
  "s_waitcnt lgkmcnt(10)\n" \
  "v_smfmac_f32_16x16x64_bf16 v[24:27], v[180:183], v[100:107], v192\n" \
  "v_lshlrev_b32_sdwa v64, 4, v236 dst_sel:DWORD dst_unused:UNUSED_PAD src0_sel:DWORD src1_sel:WORD_0\n" \
  "ds_read_b128 v[100:103], v64\n" \
  "v_lshlrev_b32_sdwa v65, 4, v236 dst_sel:DWORD dst_unused:UNUSED_PAD src0_sel:DWORD src1_sel:WORD_1\n" \
  "ds_read_b128 v[104:107], v65\n" \
  "global_load_dwordx4 v[216:219], v188, s[56:57] offset:2048 sc1\n" \
  "global_load_dwordx4 v[220:223], v188, s[56:57] offset:3072 sc1\n" \
  "s_waitcnt lgkmcnt(10)\n" \
  "v_smfmac_f32_16x16x64_bf16 v[28:31], v[180:183], v[108:115], v192\n" \
  "v_lshlrev_b32_sdwa v66, 4, v237 dst_sel:DWORD dst_unused:UNUSED_PAD src0_sel:DWORD src1_sel:WORD_0\n" \
  "ds_read_b128 v[108:111], v66\n" \
  "v_lshlrev_b32_sdwa v67, 4, v237 dst_sel:DWORD dst_unused:UNUSED_PAD src0_sel:DWORD src1_sel:WORD_1\n" \
  "ds_read_b128 v[112:115], v67\n" \
  "s_waitcnt lgkmcnt(10)\n" \
  "v_smfmac_f32_16x16x64_bf16 v[32:35], v[180:183], v[68:75], v192\n" \
  "v_lshlrev_b32_sdwa v64, 4, v238 dst_sel:DWORD dst_unused:UNUSED_PAD src0_sel:DWORD src1_sel:WORD_0\n" \
  "ds_read_b128 v[68:71], v64\n" \
  "v_lshlrev_b32_sdwa v65, 4, v238 dst_sel:DWORD dst_unused:UNUSED_PAD src0_sel:DWORD src1_sel:WORD_1\n" \
  "ds_read_b128 v[72:75], v65\n" \
  "s_waitcnt lgkmcnt(10)\n" \
  "v_smfmac_f32_16x16x64_bf16 v[36:39], v[180:183], v[76:83], v192\n" \
  "v_lshlrev_b32_sdwa v66, 4, v239 dst_sel:DWORD dst_unused:UNUSED_PAD src0_sel:DWORD src1_sel:WORD_0\n" \
  "ds_read_b128 v[76:79], v66\n" \
  "v_lshlrev_b32_sdwa v67, 4, v239 dst_sel:DWORD dst_unused:UNUSED_PAD src0_sel:DWORD src1_sel:WORD_1\n" \
  "ds_read_b128 v[80:83], v67\n" \
  "s_add_u32 s56, s56, 32768\n" \
  "s_addc_u32 s57, s57, 0\n" \
  "s_add_u32 s54, s54, 64000\n" \
  "s_addc_u32 s55, s55, 0\n" \
  "s_waitcnt lgkmcnt(10)\n" \
  "v_smfmac_f32_16x16x64_bf16 v[40:43], v[180:183], v[84:91], v192\n" \
  "s_waitcnt lgkmcnt(8)\n" \
  "v_smfmac_f32_16x16x64_bf16 v[44:47], v[180:183], v[92:99], v192\n" \
  "s_waitcnt vmcnt(8)\n" \
  "s_barrier\n" \
  "v_lshlrev_b32_sdwa v64, 4, v240 dst_sel:DWORD dst_unused:UNUSED_PAD src0_sel:DWORD src1_sel:WORD_0\n" \
  "ds_read_b128 v[84:87], v64\n" \
  "v_lshlrev_b32_sdwa v65, 4, v240 dst_sel:DWORD dst_unused:UNUSED_PAD src0_sel:DWORD src1_sel:WORD_1\n" \
  "ds_read_b128 v[88:91], v65\n" \
  "v_lshlrev_b32_sdwa v66, 4, v241 dst_sel:DWORD dst_unused:UNUSED_PAD src0_sel:DWORD src1_sel:WORD_0\n" \
  "ds_read_b128 v[92:95], v66\n" \
  "v_lshlrev_b32_sdwa v67, 4, v241 dst_sel:DWORD dst_unused:UNUSED_PAD src0_sel:DWORD src1_sel:WORD_1\n" \
  "ds_read_b128 v[96:99], v67\n" \
  "s_waitcnt lgkmcnt(10)\n" \
  "v_smfmac_f32_16x16x64_bf16 v[48:51], v[180:183], v[100:107], v192\n" \
  "s_waitcnt lgkmcnt(8)\n" \
  "v_smfmac_f32_16x16x64_bf16 v[52:55], v[180:183], v[108:115], v192\n" \
  "s_waitcnt lgkmcnt(6)\n" \
  "v_smfmac_f32_16x16x64_bf16 v[56:59], v[180:183], v[68:75], v192\n" \
  "s_waitcnt lgkmcnt(4)\n" \
  "v_smfmac_f32_16x16x64_bf16 v[60:63], v[180:183], v[76:83], v192\n" \
  "s_barrier\n" \
  "s_mov_b32 m0, s58\n" \
  "s_add_u32 s35, s58, 0x8000\n" \
  "global_load_lds_dwordx4 v188, s[54:55] sc1\n" \
  "global_load_lds_dwordx4 v188, s[54:55] offset:1024 sc1\n" \
  "v_lshlrev_b32_sdwa v64, 4, v242 dst_sel:DWORD dst_unused:UNUSED_PAD src0_sel:DWORD src1_sel:WORD_0\n" \
  "ds_read_b128 v[100:103], v64\n" \
  "v_lshlrev_b32_sdwa v65, 4, v242 dst_sel:DWORD dst_unused:UNUSED_PAD src0_sel:DWORD src1_sel:WORD_1\n" \
  "ds_read_b128 v[104:107], v65\n" \
  "v_lshlrev_b32_sdwa v66, 4, v243 dst_sel:DWORD dst_unused:UNUSED_PAD src0_sel:DWORD src1_sel:WORD_0\n" \
  "ds_read_b128 v[108:111], v66\n" \
  "v_lshlrev_b32_sdwa v67, 4, v243 dst_sel:DWORD dst_unused:UNUSED_PAD src0_sel:DWORD src1_sel:WORD_1\n" \
  "ds_read_b128 v[112:115], v67\n" \
  "s_add_u32 m0, s58, 0x4000\n" \
  "s_cmp_lt_u32 s35, 0x28000\n" \
  "global_load_lds_dwordx4 v189, s[54:55] sc1\n" \
  "global_load_lds_dwordx4 v189, s[54:55] offset:1024 sc1\n" \
  "s_cselect_b32 s58, s35, s59\n" \
  "v_lshlrev_b32_sdwa v64, 4, v244 dst_sel:DWORD dst_unused:UNUSED_PAD src0_sel:DWORD src1_sel:WORD_0\n" \
  "ds_read_b128 v[68:71], v64\n" \
  "v_lshlrev_b32_sdwa v65, 4, v244 dst_sel:DWORD dst_unused:UNUSED_PAD src0_sel:DWORD src1_sel:WORD_1\n" \
  "ds_read_b128 v[72:75], v65\n" \
  "v_lshlrev_b32_sdwa v66, 4, v245 dst_sel:DWORD dst_unused:UNUSED_PAD src0_sel:DWORD src1_sel:WORD_0\n" \
  "ds_read_b128 v[76:79], v66\n" \
  "v_lshlrev_b32_sdwa v67, 4, v245 dst_sel:DWORD dst_unused:UNUSED_PAD src0_sel:DWORD src1_sel:WORD_1\n" \
  "ds_read_b128 v[80:83], v67\n" \
  "s_waitcnt lgkmcnt(10)\n" \
  "v_smfmac_f32_16x16x64_bf16 v[0:3], v[180:183], v[84:91], v192\n" \
  "v_lshlrev_b32_sdwa v64, 4, v246 dst_sel:DWORD dst_unused:UNUSED_PAD src0_sel:DWORD src1_sel:WORD_0\n" \
  "ds_read_b128 v[84:87], v64\n" \
  "v_lshlrev_b32_sdwa v65, 4, v246 dst_sel:DWORD dst_unused:UNUSED_PAD src0_sel:DWORD src1_sel:WORD_1\n" \
  "ds_read_b128 v[88:91], v65\n" \
  "s_mov_b32 m0, s58\n" \
  "s_add_u32 s35, s58, 0x8000\n" \
  "global_load_lds_dwordx4 v190, s[54:55] sc1\n" \
  "global_load_lds_dwordx4 v190, s[54:55] offset:1024 sc1\n" \
  "s_waitcnt lgkmcnt(10)\n" \
  "v_smfmac_f32_16x16x64_bf16 v[4:7], v[180:183], v[92:99], v192\n" \
  "v_lshlrev_b32_sdwa v66, 4, v247 dst_sel:DWORD dst_unused:UNUSED_PAD src0_sel:DWORD src1_sel:WORD_0\n" \
  "ds_read_b128 v[92:95], v66\n" \
  "v_lshlrev_b32_sdwa v67, 4, v247 dst_sel:DWORD dst_unused:UNUSED_PAD src0_sel:DWORD src1_sel:WORD_1\n" \
  "ds_read_b128 v[96:99], v67\n" \
  "s_waitcnt lgkmcnt(10)\n" \
  "v_smfmac_f32_16x16x64_bf16 v[8:11], v[180:183], v[100:107], v192\n" \
  "v_lshlrev_b32_sdwa v64, 4, v248 dst_sel:DWORD dst_unused:UNUSED_PAD src0_sel:DWORD src1_sel:WORD_0\n" \
  "ds_read_b128 v[100:103], v64\n" \
  "v_lshlrev_b32_sdwa v65, 4, v248 dst_sel:DWORD dst_unused:UNUSED_PAD src0_sel:DWORD src1_sel:WORD_1\n" \
  "ds_read_b128 v[104:107], v65\n" \
  "s_add_u32 m0, s58, 0x4000\n" \
  "s_cmp_lt_u32 s35, 0x28000\n" \
  "global_load_lds_dwordx4 v191, s[54:55] sc1\n" \
  "global_load_lds_dwordx4 v191, s[54:55] offset:1024 sc1\n" \
  "s_cselect_b32 s58, s35, s59\n" \
  "s_waitcnt lgkmcnt(10)\n" \
  "v_smfmac_f32_16x16x64_bf16 v[12:15], v[180:183], v[108:115], v192\n" \
  "v_lshlrev_b32_sdwa v66, 4, v249 dst_sel:DWORD dst_unused:UNUSED_PAD src0_sel:DWORD src1_sel:WORD_0\n" \
  "ds_read_b128 v[108:111], v66\n" \
  "v_lshlrev_b32_sdwa v67, 4, v249 dst_sel:DWORD dst_unused:UNUSED_PAD src0_sel:DWORD src1_sel:WORD_1\n" \
  "ds_read_b128 v[112:115], v67\n" \
  "s_waitcnt lgkmcnt(10)\n" \
  "v_smfmac_f32_16x16x64_bf16 v[16:19], v[180:183], v[68:75], v192\n" \
  "v_lshlrev_b32_sdwa v64, 4, v250 dst_sel:DWORD dst_unused:UNUSED_PAD src0_sel:DWORD src1_sel:WORD_0\n" \
  "ds_read_b128 v[68:71], v64\n" \
  "v_lshlrev_b32_sdwa v65, 4, v250 dst_sel:DWORD dst_unused:UNUSED_PAD src0_sel:DWORD src1_sel:WORD_1\n" \
  "ds_read_b128 v[72:75], v65\n" \
  "global_load_dwordx4 v[224:227], v188, s[56:57] sc1\n" \
  "global_load_dwordx4 v[228:231], v188, s[56:57] offset:1024 sc1\n" \
  "s_waitcnt lgkmcnt(10)\n" \
  "v_smfmac_f32_16x16x64_bf16 v[20:23], v[180:183], v[76:83], v192\n" \
  "v_lshlrev_b32_sdwa v66, 4, v251 dst_sel:DWORD dst_unused:UNUSED_PAD src0_sel:DWORD src1_sel:WORD_0\n" \
  "ds_read_b128 v[76:79], v66\n" \
  "v_lshlrev_b32_sdwa v67, 4, v251 dst_sel:DWORD dst_unused:UNUSED_PAD src0_sel:DWORD src1_sel:WORD_1\n" \
  "ds_read_b128 v[80:83], v67\n" \
  "s_waitcnt lgkmcnt(10)\n" \
  "v_smfmac_f32_16x16x64_bf16 v[24:27], v[180:183], v[84:91], v192\n" \
  "v_lshlrev_b32_sdwa v64, 4, v252 dst_sel:DWORD dst_unused:UNUSED_PAD src0_sel:DWORD src1_sel:WORD_0\n" \
  "ds_read_b128 v[84:87], v64\n" \
  "v_lshlrev_b32_sdwa v65, 4, v252 dst_sel:DWORD dst_unused:UNUSED_PAD src0_sel:DWORD src1_sel:WORD_1\n" \
  "ds_read_b128 v[88:91], v65\n" \
  "global_load_dwordx4 v[232:235], v188, s[56:57] offset:2048 sc1\n" \
  "global_load_dwordx4 v[236:239], v188, s[56:57] offset:3072 sc1\n" \
  "s_waitcnt lgkmcnt(10)\n" \
  "v_smfmac_f32_16x16x64_bf16 v[28:31], v[180:183], v[92:99], v192\n" \
  "v_lshlrev_b32_sdwa v66, 4, v253 dst_sel:DWORD dst_unused:UNUSED_PAD src0_sel:DWORD src1_sel:WORD_0\n" \
  "ds_read_b128 v[92:95], v66\n" \
  "v_lshlrev_b32_sdwa v67, 4, v253 dst_sel:DWORD dst_unused:UNUSED_PAD src0_sel:DWORD src1_sel:WORD_1\n" \
  "ds_read_b128 v[96:99], v67\n" \
  "s_waitcnt lgkmcnt(10)\n" \
  "v_smfmac_f32_16x16x64_bf16 v[32:35], v[180:183], v[100:107], v192\n" \
  "v_lshlrev_b32_sdwa v64, 4, v254 dst_sel:DWORD dst_unused:UNUSED_PAD src0_sel:DWORD src1_sel:WORD_0\n" \
  "ds_read_b128 v[100:103], v64\n" \
  "v_lshlrev_b32_sdwa v65, 4, v254 dst_sel:DWORD dst_unused:UNUSED_PAD src0_sel:DWORD src1_sel:WORD_1\n" \
  "ds_read_b128 v[104:107], v65\n" \
  "s_waitcnt lgkmcnt(10)\n" \
  "v_smfmac_f32_16x16x64_bf16 v[36:39], v[180:183], v[108:115], v192\n" \
  "v_lshlrev_b32_sdwa v66, 4, v255 dst_sel:DWORD dst_unused:UNUSED_PAD src0_sel:DWORD src1_sel:WORD_0\n" \
  "ds_read_b128 v[108:111], v66\n" \
  "v_lshlrev_b32_sdwa v67, 4, v255 dst_sel:DWORD dst_unused:UNUSED_PAD src0_sel:DWORD src1_sel:WORD_1\n" \
  "ds_read_b128 v[112:115], v67\n" \
  "s_add_u32 s56, s56, 32768\n" \
  "s_addc_u32 s57, s57, 0\n" \
  "s_add_u32 s54, s54, 64000\n" \
  "s_addc_u32 s55, s55, 0\n" \
  "s_waitcnt lgkmcnt(10)\n" \
  "v_smfmac_f32_16x16x64_bf16 v[40:43], v[180:183], v[68:75], v192\n" \
  "s_waitcnt lgkmcnt(8)\n" \
  "v_smfmac_f32_16x16x64_bf16 v[44:47], v[180:183], v[76:83], v192\n" \
  "s_waitcnt vmcnt(8)\n" \
  "s_barrier\n" \
  "s_lshl3_add_u32 s36, s27, s51\n" \
  "s_cmp_eq_u32 s36, 37\n" \
  "s_cbranch_scc1 LG_pol\n" \
  "LG_rt0:\n" \
  "v_lshlrev_b32_sdwa v64, 4, v208 dst_sel:DWORD dst_unused:UNUSED_PAD src0_sel:DWORD src1_sel:WORD_0\n" \
  "ds_read_b128 v[68:71], v64\n" \
  "v_lshlrev_b32_sdwa v65, 4, v208 dst_sel:DWORD dst_unused:UNUSED_PAD src0_sel:DWORD src1_sel:WORD_1\n" \
  "ds_read_b128 v[72:75], v65\n" \
  "v_lshlrev_b32_sdwa v66, 4, v209 dst_sel:DWORD dst_unused:UNUSED_PAD src0_sel:DWORD src1_sel:WORD_0\n" \
  "ds_read_b128 v[76:79], v66\n" \
  "v_lshlrev_b32_sdwa v67, 4, v209 dst_sel:DWORD dst_unused:UNUSED_PAD src0_sel:DWORD src1_sel:WORD_1\n" \
  "ds_read_b128 v[80:83], v67\n" \
  "s_waitcnt lgkmcnt(10)\n" \
  "v_smfmac_f32_16x16x64_bf16 v[48:51], v[180:183], v[84:91], v192\n" \
  "s_waitcnt lgkmcnt(8)\n" \
  "v_smfmac_f32_16x16x64_bf16 v[52:55], v[180:183], v[92:99], v192\n" \
  "s_waitcnt lgkmcnt(6)\n" \
  "v_smfmac_f32_16x16x64_bf16 v[56:59], v[180:183], v[100:107], v192\n" \
  "s_waitcnt lgkmcnt(4)\n" \
  "v_smfmac_f32_16x16x64_bf16 v[60:63], v[180:183], v[108:115], v192\n" \
  "s_barrier\n" \
  "s_mov_b32 m0, s58\n" \
  "s_add_u32 s35, s58, 0x8000\n" \
  "global_load_lds_dwordx4 v188, s[54:55] sc1\n" \
  "global_load_lds_dwordx4 v188, s[54:55] offset:1024 sc1\n" \
  "v_lshlrev_b32_sdwa v64, 4, v210 dst_sel:DWORD dst_unused:UNUSED_PAD src0_sel:DWORD src1_sel:WORD_0\n" \
  "ds_read_b128 v[84:87], v64\n" \
  "v_lshlrev_b32_sdwa v65, 4, v210 dst_sel:DWORD dst_unused:UNUSED_PAD src0_sel:DWORD src1_sel:WORD_1\n" \
  "ds_read_b128 v[88:91], v65\n" \
  "v_lshlrev_b32_sdwa v66, 4, v211 dst_sel:DWORD dst_unused:UNUSED_PAD src0_sel:DWORD src1_sel:WORD_0\n" \
  "ds_read_b128 v[92:95], v66\n" \
  "v_lshlrev_b32_sdwa v67, 4, v211 dst_sel:DWORD dst_unused:UNUSED_PAD src0_sel:DWORD src1_sel:WORD_1\n" \
  "ds_read_b128 v[96:99], v67\n" \
  "s_add_u32 m0, s58, 0x4000\n" \
  "s_cmp_lt_u32 s35, 0x28000\n" \
  "global_load_lds_dwordx4 v189, s[54:55] sc1\n" \
  "global_load_lds_dwordx4 v189, s[54:55] offset:1024 sc1\n" \
  "s_cselect_b32 s58, s35, s59\n" \
  "v_lshlrev_b32_sdwa v64, 4, v212 dst_sel:DWORD dst_unused:UNUSED_PAD src0_sel:DWORD src1_sel:WORD_0\n" \
  "ds_read_b128 v[100:103], v64\n" \
  "v_lshlrev_b32_sdwa v65, 4, v212 dst_sel:DWORD dst_unused:UNUSED_PAD src0_sel:DWORD src1_sel:WORD_1\n" \
  "ds_read_b128 v[104:107], v65\n" \
  "v_lshlrev_b32_sdwa v66, 4, v213 dst_sel:DWORD dst_unused:UNUSED_PAD src0_sel:DWORD src1_sel:WORD_0\n" \
  "ds_read_b128 v[108:111], v66\n" \
  "v_lshlrev_b32_sdwa v67, 4, v213 dst_sel:DWORD dst_unused:UNUSED_PAD src0_sel:DWORD src1_sel:WORD_1\n" \
  "ds_read_b128 v[112:115], v67\n" \
  "s_waitcnt lgkmcnt(10)\n" \
  "v_smfmac_f32_16x16x64_bf16 v[0:3], v[180:183], v[68:75], v192\n" \
  "v_lshlrev_b32_sdwa v64, 4, v214 dst_sel:DWORD dst_unused:UNUSED_PAD src0_sel:DWORD src1_sel:WORD_0\n" \
  "ds_read_b128 v[68:71], v64\n" \
  "v_lshlrev_b32_sdwa v65, 4, v214 dst_sel:DWORD dst_unused:UNUSED_PAD src0_sel:DWORD src1_sel:WORD_1\n" \
  "ds_read_b128 v[72:75], v65\n" \
  "s_mov_b32 m0, s58\n" \
  "s_add_u32 s35, s58, 0x8000\n" \
  "global_load_lds_dwordx4 v190, s[54:55] sc1\n" \
  "global_load_lds_dwordx4 v190, s[54:55] offset:1024 sc1\n" \
  "s_waitcnt lgkmcnt(10)\n" \
  "v_smfmac_f32_16x16x64_bf16 v[4:7], v[180:183], v[76:83], v192\n" \
  "v_lshlrev_b32_sdwa v66, 4, v215 dst_sel:DWORD dst_unused:UNUSED_PAD src0_sel:DWORD src1_sel:WORD_0\n" \
  "ds_read_b128 v[76:79], v66\n" \
  "v_lshlrev_b32_sdwa v67, 4, v215 dst_sel:DWORD dst_unused:UNUSED_PAD src0_sel:DWORD src1_sel:WORD_1\n" \
  "ds_read_b128 v[80:83], v67\n" \
  "s_waitcnt lgkmcnt(10)\n" \
  "v_smfmac_f32_16x16x64_bf16 v[8:11], v[180:183], v[84:91], v192\n" \
  "v_lshlrev_b32_sdwa v64, 4, v216 dst_sel:DWORD dst_unused:UNUSED_PAD src0_sel:DWORD src1_sel:WORD_0\n" \
  "ds_read_b128 v[84:87], v64\n" \
  "v_lshlrev_b32_sdwa v65, 4, v216 dst_sel:DWORD dst_unused:UNUSED_PAD src0_sel:DWORD src1_sel:WORD_1\n" \
  "ds_read_b128 v[88:91], v65\n" \
  "s_add_u32 m0, s58, 0x4000\n" \
  "s_cmp_lt_u32 s35, 0x28000\n" \
  "global_load_lds_dwordx4 v191, s[54:55] sc1\n" \
  "global_load_lds_dwordx4 v191, s[54:55] offset:1024 sc1\n" \
  "s_cselect_b32 s58, s35, s59\n" \
  "s_waitcnt lgkmcnt(10)\n" \
  "v_smfmac_f32_16x16x64_bf16 v[12:15], v[180:183], v[92:99], v192\n" \
  "v_lshlrev_b32_sdwa v66, 4, v217 dst_sel:DWORD dst_unused:UNUSED_PAD src0_sel:DWORD src1_sel:WORD_0\n" \
  "ds_read_b128 v[92:95], v66\n" \
  "v_lshlrev_b32_sdwa v67, 4, v217 dst_sel:DWORD dst_unused:UNUSED_PAD src0_sel:DWORD src1_sel:WORD_1\n" \
  "ds_read_b128 v[96:99], v67\n" \
  "s_waitcnt lgkmcnt(10)\n" \
  "v_smfmac_f32_16x16x64_bf16 v[16:19], v[180:183], v[100:107], v192\n" \
  "v_lshlrev_b32_sdwa v64, 4, v218 dst_sel:DWORD dst_unused:UNUSED_PAD src0_sel:DWORD src1_sel:WORD_0\n" \
  "ds_read_b128 v[100:103], v64\n" \
  "v_lshlrev_b32_sdwa v65, 4, v218 dst_sel:DWORD dst_unused:UNUSED_PAD src0_sel:DWORD src1_sel:WORD_1\n" \
  "ds_read_b128 v[104:107], v65\n" \
  "global_load_dwordx4 v[240:243], v188, s[56:57] sc1\n" \
  "global_load_dwordx4 v[244:247], v188, s[56:57] offset:1024 sc1\n" \
  "s_waitcnt lgkmcnt(10)\n" \
  "v_smfmac_f32_16x16x64_bf16 v[20:23], v[180:183], v[108:115], v192\n" \
  "v_lshlrev_b32_sdwa v66, 4, v219 dst_sel:DWORD dst_unused:UNUSED_PAD src0_sel:DWORD src1_sel:WORD_0\n" \
  "ds_read_b128 v[108:111], v66\n" \
  "v_lshlrev_b32_sdwa v67, 4, v219 dst_sel:DWORD dst_unused:UNUSED_PAD src0_sel:DWORD src1_sel:WORD_1\n" \
  "ds_read_b128 v[112:115], v67\n" \
  "s_waitcnt lgkmcnt(10)\n" \
  "v_smfmac_f32_16x16x64_bf16 v[24:27], v[180:183], v[68:75], v192\n" \
  "v_lshlrev_b32_sdwa v64, 4, v220 dst_sel:DWORD dst_unused:UNUSED_PAD src0_sel:DWORD src1_sel:WORD_0\n" \
  "ds_read_b128 v[68:71], v64\n" \
  "v_lshlrev_b32_sdwa v65, 4, v220 dst_sel:DWORD dst_unused:UNUSED_PAD src0_sel:DWORD src1_sel:WORD_1\n" \
  "ds_read_b128 v[72:75], v65\n" \
  "global_load_dwordx4 v[248:251], v188, s[56:57] offset:2048 sc1\n" \
  "global_load_dwordx4 v[252:255], v188, s[56:57] offset:3072 sc1\n" \
  "s_waitcnt lgkmcnt(10)\n" \
  "v_smfmac_f32_16x16x64_bf16 v[28:31], v[180:183], v[76:83], v192\n" \
  "v_lshlrev_b32_sdwa v66, 4, v221 dst_sel:DWORD dst_unused:UNUSED_PAD src0_sel:DWORD src1_sel:WORD_0\n" \
  "ds_read_b128 v[76:79], v66\n" \
  "v_lshlrev_b32_sdwa v67, 4, v221 dst_sel:DWORD dst_unused:UNUSED_PAD src0_sel:DWORD src1_sel:WORD_1\n" \
  "ds_read_b128 v[80:83], v67\n" \
  "s_waitcnt lgkmcnt(10)\n" \
  "v_smfmac_f32_16x16x64_bf16 v[32:35], v[180:183], v[84:91], v192\n" \
  "v_lshlrev_b32_sdwa v64, 4, v222 dst_sel:DWORD dst_unused:UNUSED_PAD src0_sel:DWORD src1_sel:WORD_0\n" \
  "ds_read_b128 v[84:87], v64\n" \
  "v_lshlrev_b32_sdwa v65, 4, v222 dst_sel:DWORD dst_unused:UNUSED_PAD src0_sel:DWORD src1_sel:WORD_1\n" \
  "ds_read_b128 v[88:91], v65\n" \
  "s_waitcnt lgkmcnt(10)\n" \
  "v_smfmac_f32_16x16x64_bf16 v[36:39], v[180:183], v[92:99], v192\n" \
  "v_lshlrev_b32_sdwa v66, 4, v223 dst_sel:DWORD dst_unused:UNUSED_PAD src0_sel:DWORD src1_sel:WORD_0\n" \
  "ds_read_b128 v[92:95], v66\n" \
  "v_lshlrev_b32_sdwa v67, 4, v223 dst_sel:DWORD dst_unused:UNUSED_PAD src0_sel:DWORD src1_sel:WORD_1\n" \
  "ds_read_b128 v[96:99], v67\n" \
  "s_add_u32 s56, s56, 32768\n" \
  "s_addc_u32 s57, s57, 0\n" \
  "s_add_u32 s54, s54, 64000\n" \
  "s_addc_u32 s55, s55, 0\n" \
  "s_waitcnt lgkmcnt(10)\n" \
  "v_smfmac_f32_16x16x64_bf16 v[40:43], v[180:183], v[100:107], v192\n" \
  "s_waitcnt lgkmcnt(8)\n" \
  "v_smfmac_f32_16x16x64_bf16 v[44:47], v[180:183], v[108:115], v192\n" \
  "s_waitcnt vmcnt(8)\n" \
  "s_cmp_eq_u32 s36, 37\n" \
  "s_cbranch_scc1 LG_chk\n" \
  "LG_ret:\n" \
  "s_sub_u32 s51, s51, 1\n" \
  "s_cmp_lg_u32 s51, 0\n" \
  "s_barrier\n" \
  "s_cbranch_scc1 LG_loop\n" \
  "v_lshlrev_b32_sdwa v64, 4, v224 dst_sel:DWORD dst_unused:UNUSED_PAD src0_sel:DWORD src1_sel:WORD_0\n" \
  "ds_read_b128 v[100:103], v64\n" \
  "v_lshlrev_b32_sdwa v65, 4, v224 dst_sel:DWORD dst_unused:UNUSED_PAD src0_sel:DWORD src1_sel:WORD_1\n" \
  "ds_read_b128 v[104:107], v65\n" \
  "v_lshlrev_b32_sdwa v66, 4, v225 dst_sel:DWORD dst_unused:UNUSED_PAD src0_sel:DWORD src1_sel:WORD_0\n" \
  "ds_read_b128 v[108:111], v66\n" \
  "v_lshlrev_b32_sdwa v67, 4, v225 dst_sel:DWORD dst_unused:UNUSED_PAD src0_sel:DWORD src1_sel:WORD_1\n" \
  "ds_read_b128 v[112:115], v67\n" \
  "s_waitcnt lgkmcnt(10)\n" \
  "v_smfmac_f32_16x16x64_bf16 v[48:51], v[180:183], v[68:75], v192\n" \
  "s_waitcnt lgkmcnt(8)\n" \
  "v_smfmac_f32_16x16x64_bf16 v[52:55], v[180:183], v[76:83], v192\n" \
  "s_waitcnt lgkmcnt(6)\n" \
  "v_smfmac_f32_16x16x64_bf16 v[56:59], v[180:183], v[84:91], v192\n" \
  "s_waitcnt lgkmcnt(4)\n" \
  "v_smfmac_f32_16x16x64_bf16 v[60:63], v[180:183], v[92:99], v192\n" \
  "s_barrier\n" \
  "s_mov_b32 m0, s58\n" \
  "s_add_u32 s35, s58, 0x8000\n" \
  "global_load_lds_dwordx4 v188, s[54:55] sc1\n" \
  "global_load_lds_dwordx4 v188, s[54:55] offset:1024 sc1\n" \
  "v_lshlrev_b32_sdwa v64, 4, v226 dst_sel:DWORD dst_unused:UNUSED_PAD src0_sel:DWORD src1_sel:WORD_0\n" \
  "ds_read_b128 v[68:71], v64\n" \
  "v_lshlrev_b32_sdwa v65, 4, v226 dst_sel:DWORD dst_unused:UNUSED_PAD src0_sel:DWORD src1_sel:WORD_1\n" \
  "ds_read_b128 v[72:75], v65\n" \
  "v_lshlrev_b32_sdwa v66, 4, v227 dst_sel:DWORD dst_unused:UNUSED_PAD src0_sel:DWORD src1_sel:WORD_0\n" \
  "ds_read_b128 v[76:79], v66\n" \
  "v_lshlrev_b32_sdwa v67, 4, v227 dst_sel:DWORD dst_unused:UNUSED_PAD src0_sel:DWORD src1_sel:WORD_1\n" \
  "ds_read_b128 v[80:83], v67\n" \
  "s_add_u32 m0, s58, 0x4000\n" \
  "s_cmp_lt_u32 s35, 0x28000\n" \
  "global_load_lds_dwordx4 v189, s[54:55] sc1\n" \
  "global_load_lds_dwordx4 v189, s[54:55] offset:1024 sc1\n" \
  "s_cselect_b32 s58, s35, s59\n" \
  "v_lshlrev_b32_sdwa v64, 4, v228 dst_sel:DWORD dst_unused:UNUSED_PAD src0_sel:DWORD src1_sel:WORD_0\n" \
  "ds_read_b128 v[84:87], v64\n" \
  "v_lshlrev_b32_sdwa v65, 4, v228 dst_sel:DWORD dst_unused:UNUSED_PAD src0_sel:DWORD src1_sel:WORD_1\n" \
  "ds_read_b128 v[88:91], v65\n" \
  "v_lshlrev_b32_sdwa v66, 4, v229 dst_sel:DWORD dst_unused:UNUSED_PAD src0_sel:DWORD src1_sel:WORD_0\n" \
  "ds_read_b128 v[92:95], v66\n" \
  "v_lshlrev_b32_sdwa v67, 4, v229 dst_sel:DWORD dst_unused:UNUSED_PAD src0_sel:DWORD src1_sel:WORD_1\n" \
  "ds_read_b128 v[96:99], v67\n" \
  "s_waitcnt lgkmcnt(10)\n" \
  "v_smfmac_f32_16x16x64_bf16 v[0:3], v[180:183], v[100:107], v192\n" \
  "v_lshlrev_b32_sdwa v64, 4, v230 dst_sel:DWORD dst_unused:UNUSED_PAD src0_sel:DWORD src1_sel:WORD_0\n" \
  "ds_read_b128 v[100:103], v64\n" \
  "v_lshlrev_b32_sdwa v65, 4, v230 dst_sel:DWORD dst_unused:UNUSED_PAD src0_sel:DWORD src1_sel:WORD_1\n" \
  "ds_read_b128 v[104:107], v65\n" \
  "s_mov_b32 m0, s58\n" \
  "s_add_u32 s35, s58, 0x8000\n" \
  "global_load_lds_dwordx4 v190, s[54:55] sc1\n" \
  "global_load_lds_dwordx4 v190, s[54:55] offset:1024 sc1\n" \
  "s_waitcnt lgkmcnt(10)\n" \
  "v_smfmac_f32_16x16x64_bf16 v[4:7], v[180:183], v[108:115], v192\n" \
  "v_lshlrev_b32_sdwa v66, 4, v231 dst_sel:DWORD dst_unused:UNUSED_PAD src0_sel:DWORD src1_sel:WORD_0\n" \
  "ds_read_b128 v[108:111], v66\n" \
  "v_lshlrev_b32_sdwa v67, 4, v231 dst_sel:DWORD dst_unused:UNUSED_PAD src0_sel:DWORD src1_sel:WORD_1\n" \
  "ds_read_b128 v[112:115], v67\n" \
  "s_waitcnt lgkmcnt(10)\n" \
  "v_smfmac_f32_16x16x64_bf16 v[8:11], v[180:183], v[68:75], v192\n" \
  "v_lshlrev_b32_sdwa v64, 4, v232 dst_sel:DWORD dst_unused:UNUSED_PAD src0_sel:DWORD src1_sel:WORD_0\n" \
  "ds_read_b128 v[68:71], v64\n" \
  "v_lshlrev_b32_sdwa v65, 4, v232 dst_sel:DWORD dst_unused:UNUSED_PAD src0_sel:DWORD src1_sel:WORD_1\n" \
  "ds_read_b128 v[72:75], v65\n" \
  "s_add_u32 m0, s58, 0x4000\n" \
  "s_cmp_lt_u32 s35, 0x28000\n" \
  "global_load_lds_dwordx4 v191, s[54:55] sc1\n" \
  "global_load_lds_dwordx4 v191, s[54:55] offset:1024 sc1\n" \
  "s_cselect_b32 s58, s35, s59\n" \
  "s_waitcnt lgkmcnt(10)\n" \
  "v_smfmac_f32_16x16x64_bf16 v[12:15], v[180:183], v[76:83], v192\n" \
  "v_lshlrev_b32_sdwa v66, 4, v233 dst_sel:DWORD dst_unused:UNUSED_PAD src0_sel:DWORD src1_sel:WORD_0\n" \
  "ds_read_b128 v[76:79], v66\n" \
  "v_lshlrev_b32_sdwa v67, 4, v233 dst_sel:DWORD dst_unused:UNUSED_PAD src0_sel:DWORD src1_sel:WORD_1\n" \
  "ds_read_b128 v[80:83], v67\n" \
  "s_waitcnt lgkmcnt(10)\n" \
  "v_smfmac_f32_16x16x64_bf16 v[16:19], v[180:183], v[84:91], v192\n" \
  "v_lshlrev_b32_sdwa v64, 4, v234 dst_sel:DWORD dst_unused:UNUSED_PAD src0_sel:DWORD src1_sel:WORD_0\n" \
  "ds_read_b128 v[84:87], v64\n" \
  "v_lshlrev_b32_sdwa v65, 4, v234 dst_sel:DWORD dst_unused:UNUSED_PAD src0_sel:DWORD src1_sel:WORD_1\n" \
  "ds_read_b128 v[88:91], v65\n" \
  "global_load_dwordx4 v[208:211], v188, s[56:57] sc1\n" \
  "global_load_dwordx4 v[212:215], v188, s[56:57] offset:1024 sc1\n" \
  "s_waitcnt lgkmcnt(10)\n" \
  "v_smfmac_f32_16x16x64_bf16 v[20:23], v[180:183], v[92:99], v192\n" \
  "v_lshlrev_b32_sdwa v66, 4, v235 dst_sel:DWORD dst_unused:UNUSED_PAD src0_sel:DWORD src1_sel:WORD_0\n" \
  "ds_read_b128 v[92:95], v66\n" \
  "v_lshlrev_b32_sdwa v67, 4, v235 dst_sel:DWORD dst_unused:UNUSED_PAD src0_sel:DWORD src1_sel:WORD_1\n" \
  "ds_read_b128 v[96:99], v67\n" \
  "s_waitcnt lgkmcnt(10)\n" \
  "v_smfmac_f32_16x16x64_bf16 v[24:27], v[180:183], v[100:107], v192\n" \
  "v_lshlrev_b32_sdwa v64, 4, v236 dst_sel:DWORD dst_unused:UNUSED_PAD src0_sel:DWORD src1_sel:WORD_0\n" \
  "ds_read_b128 v[100:103], v64\n" \
  "v_lshlrev_b32_sdwa v65, 4, v236 dst_sel:DWORD dst_unused:UNUSED_PAD src0_sel:DWORD src1_sel:WORD_1\n" \
  "ds_read_b128 v[104:107], v65\n" \
  "global_load_dwordx4 v[216:219], v188, s[56:57] offset:2048 sc1\n" \
  "global_load_dwordx4 v[220:223], v188, s[56:57] offset:3072 sc1\n" \
  "s_waitcnt lgkmcnt(10)\n" \
  "v_smfmac_f32_16x16x64_bf16 v[28:31], v[180:183], v[108:115], v192\n" \
  "v_lshlrev_b32_sdwa v66, 4, v237 dst_sel:DWORD dst_unused:UNUSED_PAD src0_sel:DWORD src1_sel:WORD_0\n" \
  "ds_read_b128 v[108:111], v66\n" \
  "v_lshlrev_b32_sdwa v67, 4, v237 dst_sel:DWORD dst_unused:UNUSED_PAD src0_sel:DWORD src1_sel:WORD_1\n" \
  "ds_read_b128 v[112:115], v67\n" \
  "s_waitcnt lgkmcnt(10)\n" \
  "v_smfmac_f32_16x16x64_bf16 v[32:35], v[180:183], v[68:75], v192\n" \
  "v_lshlrev_b32_sdwa v64, 4, v238 dst_sel:DWORD dst_unused:UNUSED_PAD src0_sel:DWORD src1_sel:WORD_0\n" \
  "ds_read_b128 v[68:71], v64\n" \
  "v_lshlrev_b32_sdwa v65, 4, v238 dst_sel:DWORD dst_unused:UNUSED_PAD src0_sel:DWORD src1_sel:WORD_1\n" \
  "ds_read_b128 v[72:75], v65\n" \
  "s_waitcnt lgkmcnt(10)\n" \
  "v_smfmac_f32_16x16x64_bf16 v[36:39], v[180:183], v[76:83], v192\n" \
  "v_lshlrev_b32_sdwa v66, 4, v239 dst_sel:DWORD dst_unused:UNUSED_PAD src0_sel:DWORD src1_sel:WORD_0\n" \
  "ds_read_b128 v[76:79], v66\n" \
  "v_lshlrev_b32_sdwa v67, 4, v239 dst_sel:DWORD dst_unused:UNUSED_PAD src0_sel:DWORD src1_sel:WORD_1\n" \
  "ds_read_b128 v[80:83], v67\n" \
  "s_add_u32 s56, s56, 32768\n" \
  "s_addc_u32 s57, s57, 0\n" \
  "s_add_u32 s54, s54, 64000\n" \
  "s_addc_u32 s55, s55, 0\n" \
  "s_waitcnt lgkmcnt(10)\n" \
  "v_smfmac_f32_16x16x64_bf16 v[40:43], v[180:183], v[84:91], v192\n" \
  "s_waitcnt lgkmcnt(8)\n" \
  "v_smfmac_f32_16x16x64_bf16 v[44:47], v[180:183], v[92:99], v192\n" \
  "s_waitcnt vmcnt(8)\n" \
  "s_barrier\n" \
  "v_lshlrev_b32_sdwa v64, 4, v240 dst_sel:DWORD dst_unused:UNUSED_PAD src0_sel:DWORD src1_sel:WORD_0\n" \
  "ds_read_b128 v[84:87], v64\n" \
  "v_lshlrev_b32_sdwa v65, 4, v240 dst_sel:DWORD dst_unused:UNUSED_PAD src0_sel:DWORD src1_sel:WORD_1\n" \
  "ds_read_b128 v[88:91], v65\n" \
  "v_lshlrev_b32_sdwa v66, 4, v241 dst_sel:DWORD dst_unused:UNUSED_PAD src0_sel:DWORD src1_sel:WORD_0\n" \
  "ds_read_b128 v[92:95], v66\n" \
  "v_lshlrev_b32_sdwa v67, 4, v241 dst_sel:DWORD dst_unused:UNUSED_PAD src0_sel:DWORD src1_sel:WORD_1\n" \
  "ds_read_b128 v[96:99], v67\n" \
  "s_waitcnt lgkmcnt(10)\n" \
  "v_smfmac_f32_16x16x64_bf16 v[48:51], v[180:183], v[100:107], v192\n" \
  "s_waitcnt lgkmcnt(8)\n" \
  "v_smfmac_f32_16x16x64_bf16 v[52:55], v[180:183], v[108:115], v192\n" \
  "s_waitcnt lgkmcnt(6)\n" \
  "v_smfmac_f32_16x16x64_bf16 v[56:59], v[180:183], v[68:75], v192\n" \
  "s_waitcnt lgkmcnt(4)\n" \
  "v_smfmac_f32_16x16x64_bf16 v[60:63], v[180:183], v[76:83], v192\n" \
  "s_barrier\n" \
  "s_mov_b32 m0, s58\n" \
  "s_add_u32 s35, s58, 0x8000\n" \
  "global_load_lds_dwordx4 v188, s[54:55] sc1\n" \
  "global_load_lds_dwordx4 v188, s[54:55] offset:1024 sc1\n" \
  "s_add_u32 m0, s58, 0x4000\n" \
  "s_cmp_lt_u32 s35, 0x28000\n" \
  "global_load_lds_dwordx4 v189, s[54:55] sc1\n" \
  "global_load_lds_dwordx4 v189, s[54:55] offset:1024 sc1\n" \
  "s_cselect_b32 s58, s35, s59\n" \
  "v_lshlrev_b32 v120, 16, v206\n" \
  "v_and_b32 v121, 0xffff0000, v206\n" \
  "v_lshlrev_b32 v122, 16, v207\n" \
  "v_and_b32 v123, 0xffff0000, v207\n" \
  "v_mov_b32 v124, 0xc0135761\n" \
  "v_mov_b32 v125, 0xc0135761\n" \
  "s_mov_b32 s52, 0xbdd2d3e8\n" \
  "s_mov_b32 s53, s52\n" \
  "s_lshl2_add_u32 s35, s33, s34\n" \
  "s_lshl_b32 s35, s35, 14\n" \
  "s_add_u32 s35, s35, s59\n" \
  "s_add_u32 s35, s35, 105676800\n" \
  "s_add_u32 s40, s24, s35\n" \
  "s_addc_u32 s41, s25, 0\n" \
  "v_lshlrev_b32_sdwa v64, 4, v242 dst_sel:DWORD dst_unused:UNUSED_PAD src0_sel:DWORD src1_sel:WORD_0\n" \
  "ds_read_b128 v[100:103], v64\n" \
  "v_lshlrev_b32_sdwa v65, 4, v242 dst_sel:DWORD dst_unused:UNUSED_PAD src0_sel:DWORD src1_sel:WORD_1\n" \
  "ds_read_b128 v[104:107], v65\n" \
  "v_lshlrev_b32_sdwa v66, 4, v243 dst_sel:DWORD dst_unused:UNUSED_PAD src0_sel:DWORD src1_sel:WORD_0\n" \
  "ds_read_b128 v[108:111], v66\n" \
  "v_lshlrev_b32_sdwa v67, 4, v243 dst_sel:DWORD dst_unused:UNUSED_PAD src0_sel:DWORD src1_sel:WORD_1\n" \
  "ds_read_b128 v[112:115], v67\n" \
  "v_lshlrev_b32_sdwa v64, 4, v244 dst_sel:DWORD dst_unused:UNUSED_PAD src0_sel:DWORD src1_sel:WORD_0\n" \
  "ds_read_b128 v[68:71], v64\n" \
  "v_lshlrev_b32_sdwa v65, 4, v244 dst_sel:DWORD dst_unused:UNUSED_PAD src0_sel:DWORD src1_sel:WORD_1\n" \
  "ds_read_b128 v[72:75], v65\n" \
  "v_lshlrev_b32_sdwa v66, 4, v245 dst_sel:DWORD dst_unused:UNUSED_PAD src0_sel:DWORD src1_sel:WORD_0\n" \
  "ds_read_b128 v[76:79], v66\n" \
  "v_lshlrev_b32_sdwa v67, 4, v245 dst_sel:DWORD dst_unused:UNUSED_PAD src0_sel:DWORD src1_sel:WORD_1\n" \
  "ds_read_b128 v[80:83], v67\n" \
  "s_waitcnt lgkmcnt(10)\n" \
  "v_smfmac_f32_16x16x64_bf16 v[0:3], v[180:183], v[84:91], v192\n" \
  "v_lshlrev_b32_sdwa v64, 4, v246 dst_sel:DWORD dst_unused:UNUSED_PAD src0_sel:DWORD src1_sel:WORD_0\n" \
  "ds_read_b128 v[84:87], v64\n" \
  "v_lshlrev_b32_sdwa v65, 4, v246 dst_sel:DWORD dst_unused:UNUSED_PAD src0_sel:DWORD src1_sel:WORD_1\n" \
  "ds_read_b128 v[88:91], v65\n" \
  "s_waitcnt lgkmcnt(10)\n" \
  "v_smfmac_f32_16x16x64_bf16 v[4:7], v[180:183], v[92:99], v192\n" \
  "v_lshlrev_b32_sdwa v66, 4, v247 dst_sel:DWORD dst_unused:UNUSED_PAD src0_sel:DWORD src1_sel:WORD_0\n" \
  "ds_read_b128 v[92:95], v66\n" \
  "v_lshlrev_b32_sdwa v67, 4, v247 dst_sel:DWORD dst_unused:UNUSED_PAD src0_sel:DWORD src1_sel:WORD_1\n" \
  "ds_read_b128 v[96:99], v67\n" \
  "s_waitcnt lgkmcnt(10)\n" \
  "v_smfmac_f32_16x16x64_bf16 v[8:11], v[180:183], v[100:107], v192\n" \
  "v_lshlrev_b32_sdwa v64, 4, v248 dst_sel:DWORD dst_unused:UNUSED_PAD src0_sel:DWORD src1_sel:WORD_0\n" \
  "ds_read_b128 v[100:103], v64\n" \
  "v_lshlrev_b32_sdwa v65, 4, v248 dst_sel:DWORD dst_unused:UNUSED_PAD src0_sel:DWORD src1_sel:WORD_1\n" \
  "ds_read_b128 v[104:107], v65\n" \
  "s_waitcnt lgkmcnt(10)\n" \
  "v_smfmac_f32_16x16x64_bf16 v[12:15], v[180:183], v[108:115], v192\n" \
  "v_lshlrev_b32_sdwa v66, 4, v249 dst_sel:DWORD dst_unused:UNUSED_PAD src0_sel:DWORD src1_sel:WORD_0\n" \
  "ds_read_b128 v[108:111], v66\n" \
  "v_lshlrev_b32_sdwa v67, 4, v249 dst_sel:DWORD dst_unused:UNUSED_PAD src0_sel:DWORD src1_sel:WORD_1\n" \
  "ds_read_b128 v[112:115], v67\n" \
  "s_waitcnt vmcnt(0)\n" \
  "s_barrier\n" \
  "s_waitcnt lgkmcnt(10)\n" \
  "v_smfmac_f32_16x16x64_bf16 v[16:19], v[180:183], v[68:75], v192\n" \
  "v_lshlrev_b32_sdwa v64, 4, v208 dst_sel:DWORD dst_unused:UNUSED_PAD src0_sel:DWORD src1_sel:WORD_0\n" \
  "ds_read_b128 v[68:71], v64\n" \
  "v_lshlrev_b32_sdwa v65, 4, v208 dst_sel:DWORD dst_unused:UNUSED_PAD src0_sel:DWORD src1_sel:WORD_1\n" \
  "ds_read_b128 v[72:75], v65\n" \
  "s_waitcnt lgkmcnt(10)\n" \
  "v_smfmac_f32_16x16x64_bf16 v[20:23], v[180:183], v[76:83], v192\n" \
  "v_lshlrev_b32_sdwa v66, 4, v209 dst_sel:DWORD dst_unused:UNUSED_PAD src0_sel:DWORD src1_sel:WORD_0\n" \
  "ds_read_b128 v[76:79], v66\n" \
  "v_lshlrev_b32_sdwa v67, 4, v209 dst_sel:DWORD dst_unused:UNUSED_PAD src0_sel:DWORD src1_sel:WORD_1\n" \
  "ds_read_b128 v[80:83], v67\n" \
  "s_waitcnt lgkmcnt(10)\n" \
  "v_smfmac_f32_16x16x64_bf16 v[24:27], v[180:183], v[84:91], v192\n" \
  "v_lshlrev_b32_sdwa v64, 4, v210 dst_sel:DWORD dst_unused:UNUSED_PAD src0_sel:DWORD src1_sel:WORD_0\n" \
  "ds_read_b128 v[84:87], v64\n" \
  "v_lshlrev_b32_sdwa v65, 4, v210 dst_sel:DWORD dst_unused:UNUSED_PAD src0_sel:DWORD src1_sel:WORD_1\n" \
  "ds_read_b128 v[88:91], v65\n" \
  "s_waitcnt lgkmcnt(10)\n" \
  "v_smfmac_f32_16x16x64_bf16 v[28:31], v[180:183], v[92:99], v192\n" \
  "v_lshlrev_b32_sdwa v66, 4, v211 dst_sel:DWORD dst_unused:UNUSED_PAD src0_sel:DWORD src1_sel:WORD_0\n" \
  "ds_read_b128 v[92:95], v66\n" \
  "v_lshlrev_b32_sdwa v67, 4, v211 dst_sel:DWORD dst_unused:UNUSED_PAD src0_sel:DWORD src1_sel:WORD_1\n" \
  "ds_read_b128 v[96:99], v67\n" \
  "s_waitcnt lgkmcnt(10)\n" \
  "v_smfmac_f32_16x16x64_bf16 v[32:35], v[180:183], v[100:107], v192\n" \
  "v_lshlrev_b32_sdwa v64, 4, v212 dst_sel:DWORD dst_unused:UNUSED_PAD src0_sel:DWORD src1_sel:WORD_0\n" \
  "ds_read_b128 v[100:103], v64\n" \
  "v_lshlrev_b32_sdwa v65, 4, v212 dst_sel:DWORD dst_unused:UNUSED_PAD src0_sel:DWORD src1_sel:WORD_1\n" \
  "ds_read_b128 v[104:107], v65\n" \
  "s_waitcnt lgkmcnt(10)\n" \
  "v_smfmac_f32_16x16x64_bf16 v[36:39], v[180:183], v[108:115], v192\n" \
  "v_lshlrev_b32_sdwa v66, 4, v213 dst_sel:DWORD dst_unused:UNUSED_PAD src0_sel:DWORD src1_sel:WORD_0\n" \
  "ds_read_b128 v[108:111], v66\n" \
  "v_lshlrev_b32_sdwa v67, 4, v213 dst_sel:DWORD dst_unused:UNUSED_PAD src0_sel:DWORD src1_sel:WORD_1\n" \
  "ds_read_b128 v[112:115], v67\n" \
  "s_waitcnt lgkmcnt(10)\n" \
  "v_smfmac_f32_16x16x64_bf16 v[0:3], v[180:183], v[68:75], v192\n" \
  "v_lshlrev_b32_sdwa v64, 4, v214 dst_sel:DWORD dst_unused:UNUSED_PAD src0_sel:DWORD src1_sel:WORD_0\n" \
  "ds_read_b128 v[68:71], v64\n" \
  "v_lshlrev_b32_sdwa v65, 4, v214 dst_sel:DWORD dst_unused:UNUSED_PAD src0_sel:DWORD src1_sel:WORD_1\n" \
  "ds_read_b128 v[72:75], v65\n" \
  "s_waitcnt lgkmcnt(10)\n" \
  "v_smfmac_f32_16x16x64_bf16 v[4:7], v[180:183], v[76:83], v192\n" \
  "v_lshlrev_b32_sdwa v66, 4, v215 dst_sel:DWORD dst_unused:UNUSED_PAD src0_sel:DWORD src1_sel:WORD_0\n" \
  "ds_read_b128 v[76:79], v66\n" \
  "v_lshlrev_b32_sdwa v67, 4, v215 dst_sel:DWORD dst_unused:UNUSED_PAD src0_sel:DWORD src1_sel:WORD_1\n" \
  "ds_read_b128 v[80:83], v67\n" \
  "v_pk_mul_f32 v[128:129], v[0:1], v[0:1]\n" \
  "v_pk_mul_f32 v[130:131], v[2:3], v[2:3]\n" \
  "v_pk_fma_f32 v[128:129], v[128:129], s[52:53], v[124:125]\n" \
  "v_pk_fma_f32 v[130:131], v[130:131], s[52:53], v[124:125]\n" \
  "v_pk_mul_f32 v[128:129], v[128:129], v[0:1]\n" \
  "v_pk_mul_f32 v[130:131], v[130:131], v[2:3]\n" \
  "v_exp_f32 v128, v128\n" \
  "v_exp_f32 v129, v129\n" \
  "v_pk_mul_f32 v[0:1], v[0:1], v[120:121]\n" \
  "v_exp_f32 v130, v130\n" \
  "v_exp_f32 v131, v131\n" \
  "v_pk_mul_f32 v[2:3], v[2:3], v[122:123]\n" \
  "v_pk_add_f32 v[128:129], v[128:129], 1.0 op_sel_hi:[1,0]\n" \
  "v_pk_add_f32 v[130:131], v[130:131], 1.0 op_sel_hi:[1,0]\n" \
  "v_rcp_f32 v128, v128\n" \
  "v_rcp_f32 v129, v129\n" \
  "s_waitcnt lgkmcnt(10)\n" \
  "v_smfmac_f32_16x16x64_bf16 v[8:11], v[180:183], v[84:91], v192\n" \
  "v_lshlrev_b32_sdwa v64, 4, v216 dst_sel:DWORD dst_unused:UNUSED_PAD src0_sel:DWORD src1_sel:WORD_0\n" \
  "ds_read_b128 v[84:87], v64\n" \
  "v_lshlrev_b32_sdwa v65, 4, v216 dst_sel:DWORD dst_unused:UNUSED_PAD src0_sel:DWORD src1_sel:WORD_1\n" \
  "ds_read_b128 v[88:91], v65\n" \
  "v_rcp_f32 v130, v130\n" \
  "v_rcp_f32 v131, v131\n" \
  "v_pk_mul_f32 v[0:1], v[0:1], v[128:129]\n" \
  "v_pk_fma_f32 v[0:1], v[2:3], v[130:131], v[0:1]\n" \
  "v_add_f32 v0, v0, v1\n" \
  "v_pk_mul_f32 v[132:133], v[4:5], v[4:5]\n" \
  "v_pk_mul_f32 v[134:135], v[6:7], v[6:7]\n" \
  "v_pk_fma_f32 v[132:133], v[132:133], s[52:53], v[124:125]\n" \
  "v_pk_fma_f32 v[134:135], v[134:135], s[52:53], v[124:125]\n" \
  "v_pk_mul_f32 v[132:133], v[132:133], v[4:5]\n" \
  "v_pk_mul_f32 v[134:135], v[134:135], v[6:7]\n" \
  "v_exp_f32 v132, v132\n" \
  "v_exp_f32 v133, v133\n" \
  "v_pk_mul_f32 v[4:5], v[4:5], v[120:121]\n" \
  "v_exp_f32 v134, v134\n" \
  "v_exp_f32 v135, v135\n" \
  "s_waitcnt lgkmcnt(10)\n" \
  "v_smfmac_f32_16x16x64_bf16 v[12:15], v[180:183], v[92:99], v192\n" \
  "v_lshlrev_b32_sdwa v66, 4, v217 dst_sel:DWORD dst_unused:UNUSED_PAD src0_sel:DWORD src1_sel:WORD_0\n" \
  "ds_read_b128 v[92:95], v66\n" \
  "v_lshlrev_b32_sdwa v67, 4, v217 dst_sel:DWORD dst_unused:UNUSED_PAD src0_sel:DWORD src1_sel:WORD_1\n" \
  "ds_read_b128 v[96:99], v67\n" \
  "v_pk_mul_f32 v[6:7], v[6:7], v[122:123]\n" \
  "v_pk_add_f32 v[132:133], v[132:133], 1.0 op_sel_hi:[1,0]\n" \
  "v_pk_add_f32 v[134:135], v[134:135], 1.0 op_sel_hi:[1,0]\n" \
  "v_rcp_f32 v132, v132\n" \
  "v_rcp_f32 v133, v133\n" \
  "v_rcp_f32 v134, v134\n" \
  "v_rcp_f32 v135, v135\n" \
  "v_pk_mul_f32 v[4:5], v[4:5], v[132:133]\n" \
  "v_pk_fma_f32 v[4:5], v[6:7], v[134:135], v[4:5]\n" \
  "v_add_f32 v4, v4, v5\n" \
  "v_pk_mul_f32 v[136:137], v[8:9], v[8:9]\n" \
  "v_pk_mul_f32 v[138:139], v[10:11], v[10:11]\n" \
  "v_pk_fma_f32 v[136:137], v[136:137], s[52:53], v[124:125]\n" \
  "v_pk_fma_f32 v[138:139], v[138:139], s[52:53], v[124:125]\n" \
  "v_pk_mul_f32 v[136:137], v[136:137], v[8:9]\n" \
  "v_pk_mul_f32 v[138:139], v[138:139], v[10:11]\n" \
  "s_waitcnt lgkmcnt(10)\n" \
  "v_smfmac_f32_16x16x64_bf16 v[16:19], v[180:183], v[100:107], v192\n" \
  "v_lshlrev_b32_sdwa v64, 4, v250 dst_sel:DWORD dst_unused:UNUSED_PAD src0_sel:DWORD src1_sel:WORD_0\n" \
  "ds_read_b128 v[100:103], v64\n" \
  "v_lshlrev_b32_sdwa v65, 4, v250 dst_sel:DWORD dst_unused:UNUSED_PAD src0_sel:DWORD src1_sel:WORD_1\n" \
  "ds_read_b128 v[104:107], v65\n" \
  "v_exp_f32 v136, v136\n" \
  "v_exp_f32 v137, v137\n" \
  "v_pk_mul_f32 v[8:9], v[8:9], v[120:121]\n" \
  "v_exp_f32 v138, v138\n" \
  "v_exp_f32 v139, v139\n" \
  "v_pk_mul_f32 v[10:11], v[10:11], v[122:123]\n" \
  "v_pk_add_f32 v[136:137], v[136:137], 1.0 op_sel_hi:[1,0]\n" \
  "v_pk_add_f32 v[138:139], v[138:139], 1.0 op_sel_hi:[1,0]\n" \
  "v_rcp_f32 v136, v136\n" \
  "v_rcp_f32 v137, v137\n" \
  "v_rcp_f32 v138, v138\n" \
  "v_rcp_f32 v139, v139\n" \
  "v_pk_mul_f32 v[8:9], v[8:9], v[136:137]\n" \
  "v_pk_fma_f32 v[8:9], v[10:11], v[138:139], v[8:9]\n" \
  "v_add_f32 v8, v8, v9\n" \
  "v_pk_mul_f32 v[140:141], v[12:13], v[12:13]\n" \
  "s_waitcnt lgkmcnt(10)\n" \
  "v_smfmac_f32_16x16x64_bf16 v[20:23], v[180:183], v[108:115], v192\n" \
  "v_lshlrev_b32_sdwa v66, 4, v218 dst_sel:DWORD dst_unused:UNUSED_PAD src0_sel:DWORD src1_sel:WORD_0\n" \
  "ds_read_b128 v[108:111], v66\n" \
  "v_lshlrev_b32_sdwa v67, 4, v218 dst_sel:DWORD dst_unused:UNUSED_PAD src0_sel:DWORD src1_sel:WORD_1\n" \
  "ds_read_b128 v[112:115], v67\n" \
  "v_pk_mul_f32 v[142:143], v[14:15], v[14:15]\n" \
  "v_pk_fma_f32 v[140:141], v[140:141], s[52:53], v[124:125]\n" \
  "v_pk_fma_f32 v[142:143], v[142:143], s[52:53], v[124:125]\n" \
  "v_pk_mul_f32 v[140:141], v[140:141], v[12:13]\n" \
  "v_pk_mul_f32 v[142:143], v[142:143], v[14:15]\n" \
  "v_exp_f32 v140, v140\n" \
  "v_exp_f32 v141, v141\n" \
  "v_pk_mul_f32 v[12:13], v[12:13], v[120:121]\n" \
  "v_exp_f32 v142, v142\n" \
  "v_exp_f32 v143, v143\n" \
  "v_pk_mul_f32 v[14:15], v[14:15], v[122:123]\n" \
  "v_pk_add_f32 v[140:141], v[140:141], 1.0 op_sel_hi:[1,0]\n" \
  "v_pk_add_f32 v[142:143], v[142:143], 1.0 op_sel_hi:[1,0]\n" \
  "v_rcp_f32 v140, v140\n" \
  "v_rcp_f32 v141, v141\n" \
  "v_rcp_f32 v142, v142\n" \
  "s_waitcnt lgkmcnt(10)\n" \
  "v_smfmac_f32_16x16x64_bf16 v[24:27], v[180:183], v[68:75], v192\n" \
  "v_lshlrev_b32_sdwa v64, 4, v251 dst_sel:DWORD dst_unused:UNUSED_PAD src0_sel:DWORD src1_sel:WORD_0\n" \
  "ds_read_b128 v[68:71], v64\n" \
  "v_lshlrev_b32_sdwa v65, 4, v251 dst_sel:DWORD dst_unused:UNUSED_PAD src0_sel:DWORD src1_sel:WORD_1\n" \
  "ds_read_b128 v[72:75], v65\n" \
  "v_rcp_f32 v143, v143\n" \
  "v_pk_mul_f32 v[12:13], v[12:13], v[140:141]\n" \
  "v_pk_fma_f32 v[12:13], v[14:15], v[142:143], v[12:13]\n" \
  "v_add_f32 v12, v12, v13\n" \
  "v_pk_mul_f32 v[128:129], v[16:17], v[16:17]\n" \
  "v_pk_mul_f32 v[130:131], v[18:19], v[18:19]\n" \
  "v_pk_fma_f32 v[128:129], v[128:129], s[52:53], v[124:125]\n" \
  "v_pk_fma_f32 v[130:131], v[130:131], s[52:53], v[124:125]\n" \
  "v_pk_mul_f32 v[128:129], v[128:129], v[16:17]\n" \
  "v_pk_mul_f32 v[130:131], v[130:131], v[18:19]\n" \
  "v_exp_f32 v128, v128\n" \
  "v_exp_f32 v129, v129\n" \
  "v_pk_mul_f32 v[16:17], v[16:17], v[120:121]\n" \
  "v_exp_f32 v130, v130\n" \
  "v_exp_f32 v131, v131\n" \
  "v_pk_mul_f32 v[18:19], v[18:19], v[122:123]\n" \
  "s_waitcnt lgkmcnt(10)\n" \
  "v_smfmac_f32_16x16x64_bf16 v[28:31], v[180:183], v[76:83], v192\n" \
  "v_lshlrev_b32_sdwa v66, 4, v219 dst_sel:DWORD dst_unused:UNUSED_PAD src0_sel:DWORD src1_sel:WORD_0\n" \
  "ds_read_b128 v[76:79], v66\n" \
  "v_lshlrev_b32_sdwa v67, 4, v219 dst_sel:DWORD dst_unused:UNUSED_PAD src0_sel:DWORD src1_sel:WORD_1\n" \
  "ds_read_b128 v[80:83], v67\n" \
  "v_pk_add_f32 v[128:129], v[128:129], 1.0 op_sel_hi:[1,0]\n" \
  "v_pk_add_f32 v[130:131], v[130:131], 1.0 op_sel_hi:[1,0]\n" \
  "v_rcp_f32 v128, v128\n" \
  "v_rcp_f32 v129, v129\n" \
  "v_rcp_f32 v130, v130\n" \
  "v_rcp_f32 v131, v131\n" \
  "v_pk_mul_f32 v[16:17], v[16:17], v[128:129]\n" \
  "v_pk_fma_f32 v[16:17], v[18:19], v[130:131], v[16:17]\n" \
  "v_add_f32 v16, v16, v17\n" \
  "v_pk_mul_f32 v[132:133], v[20:21], v[20:21]\n" \
  "v_pk_mul_f32 v[134:135], v[22:23], v[22:23]\n" \
  "v_pk_fma_f32 v[132:133], v[132:133], s[52:53], v[124:125]\n" \
  "v_pk_fma_f32 v[134:135], v[134:135], s[52:53], v[124:125]\n" \
  "v_pk_mul_f32 v[132:133], v[132:133], v[20:21]\n" \
  "v_pk_mul_f32 v[134:135], v[134:135], v[22:23]\n" \
  "v_exp_f32 v132, v132\n" \
  "s_waitcnt lgkmcnt(10)\n" \
  "v_smfmac_f32_16x16x64_bf16 v[32:35], v[180:183], v[84:91], v192\n" \
  "v_lshlrev_b32_sdwa v64, 4, v252 dst_sel:DWORD dst_unused:UNUSED_PAD src0_sel:DWORD src1_sel:WORD_0\n" \
  "ds_read_b128 v[84:87], v64\n" \
  "v_lshlrev_b32_sdwa v65, 4, v252 dst_sel:DWORD dst_unused:UNUSED_PAD src0_sel:DWORD src1_sel:WORD_1\n" \
  "ds_read_b128 v[88:91], v65\n" \
  "v_exp_f32 v133, v133\n" \
  "v_pk_mul_f32 v[20:21], v[20:21], v[120:121]\n" \
  "v_exp_f32 v134, v134\n" \
  "v_exp_f32 v135, v135\n" \
  "v_pk_mul_f32 v[22:23], v[22:23], v[122:123]\n" \
  "v_pk_add_f32 v[132:133], v[132:133], 1.0 op_sel_hi:[1,0]\n" \
  "v_pk_add_f32 v[134:135], v[134:135], 1.0 op_sel_hi:[1,0]\n" \
  "v_rcp_f32 v132, v132\n" \
  "v_rcp_f32 v133, v133\n" \
  "v_rcp_f32 v134, v134\n" \
  "v_rcp_f32 v135, v135\n" \
  "v_pk_mul_f32 v[20:21], v[20:21], v[132:133]\n" \
  "v_pk_fma_f32 v[20:21], v[22:23], v[134:135], v[20:21]\n" \
  "v_add_f32 v20, v20, v21\n" \
  "v_pk_mul_f32 v[136:137], v[24:25], v[24:25]\n" \
  "v_pk_mul_f32 v[138:139], v[26:27], v[26:27]\n" \
  "s_waitcnt lgkmcnt(10)\n" \
  "v_smfmac_f32_16x16x64_bf16 v[36:39], v[180:183], v[92:99], v192\n" \
  "v_lshlrev_b32_sdwa v66, 4, v220 dst_sel:DWORD dst_unused:UNUSED_PAD src0_sel:DWORD src1_sel:WORD_0\n" \
  "ds_read_b128 v[92:95], v66\n" \
  "v_lshlrev_b32_sdwa v67, 4, v220 dst_sel:DWORD dst_unused:UNUSED_PAD src0_sel:DWORD src1_sel:WORD_1\n" \
  "ds_read_b128 v[96:99], v67\n" \
  "v_pk_fma_f32 v[136:137], v[136:137], s[52:53], v[124:125]\n" \
  "v_pk_fma_f32 v[138:139], v[138:139], s[52:53], v[124:125]\n" \
  "v_pk_mul_f32 v[136:137], v[136:137], v[24:25]\n" \
  "v_pk_mul_f32 v[138:139], v[138:139], v[26:27]\n" \
  "v_exp_f32 v136, v136\n" \
  "v_exp_f32 v137, v137\n" \
  "v_pk_mul_f32 v[24:25], v[24:25], v[120:121]\n" \
  "v_exp_f32 v138, v138\n" \
  "v_exp_f32 v139, v139\n" \
  "v_pk_mul_f32 v[26:27], v[26:27], v[122:123]\n" \
  "v_pk_add_f32 v[136:137], v[136:137], 1.0 op_sel_hi:[1,0]\n" \
  "v_pk_add_f32 v[138:139], v[138:139], 1.0 op_sel_hi:[1,0]\n" \
  "v_rcp_f32 v136, v136\n" \
  "v_rcp_f32 v137, v137\n" \
  "v_rcp_f32 v138, v138\n" \
  "v_rcp_f32 v139, v139\n" \
  "s_waitcnt lgkmcnt(10)\n" \
  "v_smfmac_f32_16x16x64_bf16 v[40:43], v[180:183], v[100:107], v192\n" \
  "v_lshlrev_b32_sdwa v64, 4, v253 dst_sel:DWORD dst_unused:UNUSED_PAD src0_sel:DWORD src1_sel:WORD_0\n" \
  "ds_read_b128 v[100:103], v64\n" \
  "v_lshlrev_b32_sdwa v65, 4, v253 dst_sel:DWORD dst_unused:UNUSED_PAD src0_sel:DWORD src1_sel:WORD_1\n" \
  "ds_read_b128 v[104:107], v65\n" \
  "v_pk_mul_f32 v[24:25], v[24:25], v[136:137]\n" \
  "v_pk_fma_f32 v[24:25], v[26:27], v[138:139], v[24:25]\n" \
  "v_add_f32 v24, v24, v25\n" \
  "v_pk_mul_f32 v[140:141], v[28:29], v[28:29]\n" \
  "v_pk_mul_f32 v[142:143], v[30:31], v[30:31]\n" \
  "v_pk_fma_f32 v[140:141], v[140:141], s[52:53], v[124:125]\n" \
  "v_pk_fma_f32 v[142:143], v[142:143], s[52:53], v[124:125]\n" \
  "v_pk_mul_f32 v[140:141], v[140:141], v[28:29]\n" \
  "v_pk_mul_f32 v[142:143], v[142:143], v[30:31]\n" \
  "v_exp_f32 v140, v140\n" \
  "v_exp_f32 v141, v141\n" \
  "v_pk_mul_f32 v[28:29], v[28:29], v[120:121]\n" \
  "v_exp_f32 v142, v142\n" \
  "v_exp_f32 v143, v143\n" \
  "v_pk_mul_f32 v[30:31], v[30:31], v[122:123]\n" \
  "v_pk_add_f32 v[140:141], v[140:141], 1.0 op_sel_hi:[1,0]\n" \
  "s_waitcnt lgkmcnt(10)\n" \
  "v_smfmac_f32_16x16x64_bf16 v[40:43], v[180:183], v[108:115], v192\n" \
  "v_lshlrev_b32_sdwa v66, 4, v221 dst_sel:DWORD dst_unused:UNUSED_PAD src0_sel:DWORD src1_sel:WORD_0\n" \
  "ds_read_b128 v[108:111], v66\n" \
  "v_lshlrev_b32_sdwa v67, 4, v221 dst_sel:DWORD dst_unused:UNUSED_PAD src0_sel:DWORD src1_sel:WORD_1\n" \
  "ds_read_b128 v[112:115], v67\n" \
  "v_pk_add_f32 v[142:143], v[142:143], 1.0 op_sel_hi:[1,0]\n" \
  "v_rcp_f32 v140, v140\n" \
  "v_rcp_f32 v141, v141\n" \
  "v_rcp_f32 v142, v142\n" \
  "v_rcp_f32 v143, v143\n" \
  "v_pk_mul_f32 v[28:29], v[28:29], v[140:141]\n" \
  "v_pk_fma_f32 v[28:29], v[30:31], v[142:143], v[28:29]\n" \
  "v_add_f32 v28, v28, v29\n" \
  "v_permlane16_swap_b32 v0, v16\n" \
  "v_permlane16_swap_b32 v4, v20\n" \
  "v_permlane16_swap_b32 v8, v24\n" \
  "v_permlane16_swap_b32 v12, v28\n" \
  "v_add_f32 v144, v0, v16\n" \
  "v_add_f32 v145, v4, v20\n" \
  "v_add_f32 v146, v8, v24\n" \
  "v_add_f32 v147, v12, v28\n" \
  "s_waitcnt lgkmcnt(10)\n" \
  "v_smfmac_f32_16x16x64_bf16 v[44:47], v[180:183], v[68:75], v192\n" \
  "v_lshlrev_b32_sdwa v64, 4, v254 dst_sel:DWORD dst_unused:UNUSED_PAD src0_sel:DWORD src1_sel:WORD_0\n" \
  "ds_read_b128 v[68:71], v64\n" \
  "v_lshlrev_b32_sdwa v65, 4, v254 dst_sel:DWORD dst_unused:UNUSED_PAD src0_sel:DWORD src1_sel:WORD_1\n" \
  "ds_read_b128 v[72:75], v65\n" \
  "v_min_u32 v144, -2, v144\n" \
  "v_min_u32 v145, -2, v145\n" \
  "v_min_u32 v146, -2, v146\n" \
  "v_min_u32 v147, -2, v147\n" \
  "global_store_dwordx4 v188, v[144:147], s[40:41] offset:0 sc1\n" \
  "v_pk_mul_f32 v[128:129], v[32:33], v[32:33]\n" \
  "v_pk_mul_f32 v[130:131], v[34:35], v[34:35]\n" \
  "v_pk_fma_f32 v[128:129], v[128:129], s[52:53], v[124:125]\n" \
  "v_pk_fma_f32 v[130:131], v[130:131], s[52:53], v[124:125]\n" \
  "v_pk_mul_f32 v[128:129], v[128:129], v[32:33]\n" \
  "v_pk_mul_f32 v[130:131], v[130:131], v[34:35]\n" \
  "v_exp_f32 v128, v128\n" \
  "v_exp_f32 v129, v129\n" \
  "v_pk_mul_f32 v[32:33], v[32:33], v[120:121]\n" \
  "v_exp_f32 v130, v130\n" \
  "v_exp_f32 v131, v131\n" \
  "s_waitcnt lgkmcnt(10)\n" \
  "v_smfmac_f32_16x16x64_bf16 v[44:47], v[180:183], v[76:83], v192\n" \
  "v_lshlrev_b32_sdwa v66, 4, v222 dst_sel:DWORD dst_unused:UNUSED_PAD src0_sel:DWORD src1_sel:WORD_0\n" \
  "ds_read_b128 v[76:79], v66\n" \
  "v_lshlrev_b32_sdwa v67, 4, v222 dst_sel:DWORD dst_unused:UNUSED_PAD src0_sel:DWORD src1_sel:WORD_1\n" \
  "ds_read_b128 v[80:83], v67\n" \
  "v_pk_mul_f32 v[34:35], v[34:35], v[122:123]\n" \
  "v_pk_add_f32 v[128:129], v[128:129], 1.0 op_sel_hi:[1,0]\n" \
  "v_pk_add_f32 v[130:131], v[130:131], 1.0 op_sel_hi:[1,0]\n" \
  "v_rcp_f32 v128, v128\n" \
  "v_rcp_f32 v129, v129\n" \
  "v_rcp_f32 v130, v130\n" \
  "v_rcp_f32 v131, v131\n" \
  "v_pk_mul_f32 v[32:33], v[32:33], v[128:129]\n" \
  "v_pk_fma_f32 v[32:33], v[34:35], v[130:131], v[32:33]\n" \
  "v_add_f32 v32, v32, v33\n" \
  "v_pk_mul_f32 v[132:133], v[36:37], v[36:37]\n" \
  "v_pk_mul_f32 v[134:135], v[38:39], v[38:39]\n" \
  "v_pk_fma_f32 v[132:133], v[132:133], s[52:53], v[124:125]\n" \
  "v_pk_fma_f32 v[134:135], v[134:135], s[52:53], v[124:125]\n" \
  "v_pk_mul_f32 v[132:133], v[132:133], v[36:37]\n" \
  "v_pk_mul_f32 v[134:135], v[134:135], v[38:39]\n" \
  "s_waitcnt lgkmcnt(10)\n" \
  "v_smfmac_f32_16x16x64_bf16 v[48:51], v[180:183], v[84:91], v192\n" \
  "v_lshlrev_b32_sdwa v64, 4, v255 dst_sel:DWORD dst_unused:UNUSED_PAD src0_sel:DWORD src1_sel:WORD_0\n" \
  "ds_read_b128 v[84:87], v64\n" \
  "v_lshlrev_b32_sdwa v65, 4, v255 dst_sel:DWORD dst_unused:UNUSED_PAD src0_sel:DWORD src1_sel:WORD_1\n" \
  "ds_read_b128 v[88:91], v65\n" \
  "v_exp_f32 v132, v132\n" \
  "v_exp_f32 v133, v133\n" \
  "v_pk_mul_f32 v[36:37], v[36:37], v[120:121]\n" \
  "v_exp_f32 v134, v134\n" \
  "v_exp_f32 v135, v135\n" \
  "v_pk_mul_f32 v[38:39], v[38:39], v[122:123]\n" \
  "v_pk_add_f32 v[132:133], v[132:133], 1.0 op_sel_hi:[1,0]\n" \
  "v_pk_add_f32 v[134:135], v[134:135], 1.0 op_sel_hi:[1,0]\n" \
  "v_rcp_f32 v132, v132\n" \
  "v_rcp_f32 v133, v133\n" \
  "v_rcp_f32 v134, v134\n" \
  "v_rcp_f32 v135, v135\n" \
  "v_pk_mul_f32 v[36:37], v[36:37], v[132:133]\n" \
  "v_pk_fma_f32 v[36:37], v[38:39], v[134:135], v[36:37]\n" \
  "v_add_f32 v36, v36, v37\n" \
  "v_pk_mul_f32 v[136:137], v[40:41], v[40:41]\n" \
  "s_waitcnt lgkmcnt(10)\n" \
  "v_smfmac_f32_16x16x64_bf16 v[48:51], v[180:183], v[92:99], v192\n" \
  "v_lshlrev_b32_sdwa v66, 4, v223 dst_sel:DWORD dst_unused:UNUSED_PAD src0_sel:DWORD src1_sel:WORD_0\n" \
  "ds_read_b128 v[92:95], v66\n" \
  "v_lshlrev_b32_sdwa v67, 4, v223 dst_sel:DWORD dst_unused:UNUSED_PAD src0_sel:DWORD src1_sel:WORD_1\n" \
  "ds_read_b128 v[96:99], v67\n" \
  "v_pk_mul_f32 v[138:139], v[42:43], v[42:43]\n" \
  "v_pk_fma_f32 v[136:137], v[136:137], s[52:53], v[124:125]\n" \
  "v_pk_fma_f32 v[138:139], v[138:139], s[52:53], v[124:125]\n" \
  "v_pk_mul_f32 v[136:137], v[136:137], v[40:41]\n" \
  "v_pk_mul_f32 v[138:139], v[138:139], v[42:43]\n" \
  "v_exp_f32 v136, v136\n" \
  "v_exp_f32 v137, v137\n" \
  "v_pk_mul_f32 v[40:41], v[40:41], v[120:121]\n" \
  "v_exp_f32 v138, v138\n" \
  "v_exp_f32 v139, v139\n" \
  "v_pk_mul_f32 v[42:43], v[42:43], v[122:123]\n" \
  "v_pk_add_f32 v[136:137], v[136:137], 1.0 op_sel_hi:[1,0]\n" \
  "v_pk_add_f32 v[138:139], v[138:139], 1.0 op_sel_hi:[1,0]\n" \
  "v_rcp_f32 v136, v136\n" \
  "v_rcp_f32 v137, v137\n" \
  "v_rcp_f32 v138, v138\n" \
  "s_waitcnt lgkmcnt(10)\n" \
  "v_smfmac_f32_16x16x64_bf16 v[52:55], v[180:183], v[100:107], v192\n" \
  "v_rcp_f32 v139, v139\n" \
  "v_pk_mul_f32 v[40:41], v[40:41], v[136:137]\n" \
  "v_pk_fma_f32 v[40:41], v[42:43], v[138:139], v[40:41]\n" \
  "v_add_f32 v40, v40, v41\n" \
  "v_pk_mul_f32 v[140:141], v[44:45], v[44:45]\n" \
  "v_pk_mul_f32 v[142:143], v[46:47], v[46:47]\n" \
  "v_pk_fma_f32 v[140:141], v[140:141], s[52:53], v[124:125]\n" \
  "v_pk_fma_f32 v[142:143], v[142:143], s[52:53], v[124:125]\n" \
  "v_pk_mul_f32 v[140:141], v[140:141], v[44:45]\n" \
  "v_pk_mul_f32 v[142:143], v[142:143], v[46:47]\n" \
  "v_exp_f32 v140, v140\n" \
  "v_exp_f32 v141, v141\n" \
  "v_pk_mul_f32 v[44:45], v[44:45], v[120:121]\n" \
  "v_exp_f32 v142, v142\n" \
  "v_exp_f32 v143, v143\n" \
  "v_pk_mul_f32 v[46:47], v[46:47], v[122:123]\n" \
  "v_pk_add_f32 v[140:141], v[140:141], 1.0 op_sel_hi:[1,0]\n" \
  "v_pk_add_f32 v[142:143], v[142:143], 1.0 op_sel_hi:[1,0]\n" \
  "v_rcp_f32 v140, v140\n" \
  "v_rcp_f32 v141, v141\n" \
  "v_rcp_f32 v142, v142\n" \
  "v_rcp_f32 v143, v143\n" \
  "v_pk_mul_f32 v[44:45], v[44:45], v[140:141]\n" \
  "v_pk_fma_f32 v[44:45], v[46:47], v[142:143], v[44:45]\n" \
  "v_add_f32 v44, v44, v45\n" \
  "v_pk_mul_f32 v[128:129], v[48:49], v[48:49]\n" \
  "v_pk_mul_f32 v[130:131], v[50:51], v[50:51]\n" \
  "v_pk_fma_f32 v[128:129], v[128:129], s[52:53], v[124:125]\n" \
  "v_pk_fma_f32 v[130:131], v[130:131], s[52:53], v[124:125]\n" \
  "v_pk_mul_f32 v[128:129], v[128:129], v[48:49]\n" \
  "v_pk_mul_f32 v[130:131], v[130:131], v[50:51]\n" \
  "v_exp_f32 v128, v128\n" \
  "s_waitcnt lgkmcnt(8)\n" \
  "v_smfmac_f32_16x16x64_bf16 v[52:55], v[180:183], v[108:115], v192\n" \
  "v_exp_f32 v129, v129\n" \
  "v_pk_mul_f32 v[48:49], v[48:49], v[120:121]\n" \
  "v_exp_f32 v130, v130\n" \
  "v_exp_f32 v131, v131\n" \
  "v_pk_mul_f32 v[50:51], v[50:51], v[122:123]\n" \
  "v_pk_add_f32 v[128:129], v[128:129], 1.0 op_sel_hi:[1,0]\n" \
  "v_pk_add_f32 v[130:131], v[130:131], 1.0 op_sel_hi:[1,0]\n" \
  "v_rcp_f32 v128, v128\n" \
  "v_rcp_f32 v129, v129\n" \
  "v_rcp_f32 v130, v130\n" \
  "v_rcp_f32 v131, v131\n" \
  "v_pk_mul_f32 v[48:49], v[48:49], v[128:129]\n" \
  "v_pk_fma_f32 v[48:49], v[50:51], v[130:131], v[48:49]\n" \
  "v_add_f32 v48, v48, v49\n" \
  "s_waitcnt lgkmcnt(6)\n" \
  "v_smfmac_f32_16x16x64_bf16 v[56:59], v[180:183], v[68:75], v192\n" \
  "v_pk_mul_f32 v[132:133], v[52:53], v[52:53]\n" \
  "v_pk_mul_f32 v[134:135], v[54:55], v[54:55]\n" \
  "v_pk_fma_f32 v[132:133], v[132:133], s[52:53], v[124:125]\n" \
  "v_pk_fma_f32 v[134:135], v[134:135], s[52:53], v[124:125]\n" \
  "v_pk_mul_f32 v[132:133], v[132:133], v[52:53]\n" \
  "v_pk_mul_f32 v[134:135], v[134:135], v[54:55]\n" \
  "v_exp_f32 v132, v132\n" \
  "v_exp_f32 v133, v133\n" \
  "v_pk_mul_f32 v[52:53], v[52:53], v[120:121]\n" \
  "v_exp_f32 v134, v134\n" \
  "v_exp_f32 v135, v135\n" \
  "v_pk_mul_f32 v[54:55], v[54:55], v[122:123]\n" \
  "v_pk_add_f32 v[132:133], v[132:133], 1.0 op_sel_hi:[1,0]\n" \
  "v_pk_add_f32 v[134:135], v[134:135], 1.0 op_sel_hi:[1,0]\n" \
  "v_rcp_f32 v132, v132\n" \
  "v_rcp_f32 v133, v133\n" \
  "v_rcp_f32 v134, v134\n" \
  "v_rcp_f32 v135, v135\n" \
  "v_pk_mul_f32 v[52:53], v[52:53], v[132:133]\n" \
  "v_pk_fma_f32 v[52:53], v[54:55], v[134:135], v[52:53]\n" \
  "v_add_f32 v52, v52, v53\n" \
  "s_waitcnt lgkmcnt(4)\n" \
  "v_smfmac_f32_16x16x64_bf16 v[56:59], v[180:183], v[76:83], v192\n" \
  "s_waitcnt lgkmcnt(2)\n" \
  "v_smfmac_f32_16x16x64_bf16 v[60:63], v[180:183], v[84:91], v192\n" \
  "s_nop 5\n" \
  "v_pk_mul_f32 v[136:137], v[56:57], v[56:57]\n" \
  "v_pk_mul_f32 v[138:139], v[58:59], v[58:59]\n" \
  "v_pk_fma_f32 v[136:137], v[136:137], s[52:53], v[124:125]\n" \
  "v_pk_fma_f32 v[138:139], v[138:139], s[52:53], v[124:125]\n" \
  "v_pk_mul_f32 v[136:137], v[136:137], v[56:57]\n" \
  "v_pk_mul_f32 v[138:139], v[138:139], v[58:59]\n" \
  "v_exp_f32 v136, v136\n" \
  "v_exp_f32 v137, v137\n" \
  "v_pk_mul_f32 v[56:57], v[56:57], v[120:121]\n" \
  "v_exp_f32 v138, v138\n" \
  "v_exp_f32 v139, v139\n" \
  "v_pk_mul_f32 v[58:59], v[58:59], v[122:123]\n" \
  "v_pk_add_f32 v[136:137], v[136:137], 1.0 op_sel_hi:[1,0]\n" \
  "v_pk_add_f32 v[138:139], v[138:139], 1.0 op_sel_hi:[1,0]\n" \
  "v_rcp_f32 v136, v136\n" \
  "v_rcp_f32 v137, v137\n" \
  "v_rcp_f32 v138, v138\n" \
  "v_rcp_f32 v139, v139\n" \
  "v_pk_mul_f32 v[56:57], v[56:57], v[136:137]\n" \
  "v_pk_fma_f32 v[56:57], v[58:59], v[138:139], v[56:57]\n" \
  "v_add_f32 v56, v56, v57\n" \
  "s_waitcnt lgkmcnt(0)\n" \
  "v_smfmac_f32_16x16x64_bf16 v[60:63], v[180:183], v[92:99], v192\n" \
  "s_nop 7\n" \
  "v_pk_mul_f32 v[140:141], v[60:61], v[60:61]\n" \
  "v_pk_mul_f32 v[142:143], v[62:63], v[62:63]\n" \
  "v_pk_fma_f32 v[140:141], v[140:141], s[52:53], v[124:125]\n" \
  "v_pk_fma_f32 v[142:143], v[142:143], s[52:53], v[124:125]\n" \
  "v_pk_mul_f32 v[140:141], v[140:141], v[60:61]\n" \
  "v_pk_mul_f32 v[142:143], v[142:143], v[62:63]\n" \
  "v_exp_f32 v140, v140\n" \
  "v_exp_f32 v141, v141\n" \
  "v_pk_mul_f32 v[60:61], v[60:61], v[120:121]\n" \
  "v_exp_f32 v142, v142\n" \
  "v_exp_f32 v143, v143\n" \
  "v_pk_mul_f32 v[62:63], v[62:63], v[122:123]\n" \
  "v_pk_add_f32 v[140:141], v[140:141], 1.0 op_sel_hi:[1,0]\n" \
  "v_pk_add_f32 v[142:143], v[142:143], 1.0 op_sel_hi:[1,0]\n" \
  "v_rcp_f32 v140, v140\n" \
  "v_rcp_f32 v141, v141\n" \
  "v_rcp_f32 v142, v142\n" \
  "v_rcp_f32 v143, v143\n" \
  "v_pk_mul_f32 v[60:61], v[60:61], v[140:141]\n" \
  "v_pk_fma_f32 v[60:61], v[62:63], v[142:143], v[60:61]\n" \
  "v_add_f32 v60, v60, v61\n" \
  "v_permlane16_swap_b32 v32, v48\n" \
  "v_permlane16_swap_b32 v36, v52\n" \
  "v_permlane16_swap_b32 v40, v56\n" \
  "v_permlane16_swap_b32 v44, v60\n" \
  "v_add_f32 v148, v32, v48\n" \
  "v_add_f32 v149, v36, v52\n" \
  "v_add_f32 v150, v40, v56\n" \
  "v_add_f32 v151, v44, v60\n" \
  "v_min_u32 v148, -2, v148\n" \
  "v_min_u32 v149, -2, v149\n" \
  "v_min_u32 v150, -2, v150\n" \
  "v_min_u32 v151, -2, v151\n" \
  "global_store_dwordx4 v188, v[148:151], s[40:41] offset:1024 sc1\n" \
  "v_lshrrev_b32 v0, 4, v188\n" \
  "v_lshrrev_b32 v1, 4, v0\n" \
  "v_and_b32 v2, 15, v0\n" \
  "v_lshlrev_b32 v2, 4, v2\n" \
  "v_lshl_add_u32 v1, v1, 17, v2\n" \
  "s_lshl_b32 s35, s27, 19\n" \
  "s_lshl_b32 s36, s26, 8\n" \
  "s_add_u32 s35, s35, s36\n" \
  "s_add_u32 s35, s35, 105676800\n" \
  "s_add_u32 s40, s24, s35\n" \
  "s_addc_u32 s41, s25, 0\n" \
  "s_add_u32 s42, s40, 65536\n" \
  "s_addc_u32 s43, s41, 0\n" \
  "s_add_u32 s44, s24, 109871104\n" \
  "s_addc_u32 s45, s25, 0\n" \
  "s_add_u32 s46, s44, 65536\n" \
  "s_addc_u32 s47, s45, 0\n" \
  "s_lshr_b32 s35, s26, 2\n" \
  "s_lshl_b32 s35, s35, 8\n" \
  "s_and_b32 s36, s26, 1\n" \
  "s_lshl_b32 s36, s36, 7\n" \
  "s_add_u32 s35, s35, s36\n" \
  "s_and_b32 s36, s26, 2\n" \
  "s_lshl3_add_u32 s35, s36, s35\n" \
  "v_and_b32 v2, 3, v0\n" \
  "v_lshrrev_b32 v3, 2, v0\n" \
  "v_lshl_add_u32 v2, v2, 5, v3\n" \
  "v_add_u32 v2, s35, v2\n" \
  "v_lshlrev_b32 v3, 2, v2\n" \
  "s_cmp_eq_u32 s27, 0\n" \
  "s_cselect_b32 s35, -1, 0\n" \
  "v_and_b32 v3, s35, v3\n" \
  "v_mov_b32 v4, s6\n" \
  "s_mov_b32 s5, 0x10000\n" \
  "s_barrier\n" \
  "s_sleep 10\n" \
  "LF_poll:\n" \
  "global_load_dwordx4 v[8:11], v1, s[40:41] sc1\n" \
  "global_load_dwordx4 v[12:15], v1, s[42:43] sc1\n" \
  "global_load_dword v16, v3, s[44:45] sc1\n" \
  "global_load_dword v17, v3, s[46:47] sc1\n" \
  "s_waitcnt vmcnt(0)\n" \
  "v_max3_u32 v5, v8, v9, v10\n" \
  "v_max3_u32 v6, v11, v12, v13\n" \
  "v_max3_u32 v5, v5, v14, v15\n" \
  "v_max3_u32 v6, v6, v16, v17\n" \
  "v_max_u32 v5, v5, v6\n" \
  "v_cmp_eq_u32 vcc, -1, v5\n" \
  "s_sub_u32 s5, s5, 1\n" \
  "s_cbranch_scc1 LF_go\n" \
  "s_nop 2\n" \
  "s_cbranch_vccz LF_go\n" \
  "s_mov_b64 exec, vcc\n" \
  "s_sleep 2\n" \
  "s_branch LF_poll\n" \
  "LF_go:\n" \
  "s_mov_b64 exec, -1\n" \
  "v_pk_add_f32 v[8:9], v[8:9], v[12:13]\n" \
  "v_pk_add_f32 v[10:11], v[10:11], v[14:15]\n" \
  "s_nop 0\n" \
  "v_permlane16_swap_b32 v8, v9\n" \
  "v_permlane16_swap_b32 v10, v11\n" \
  "v_add_f32 v8, v8, v9\n" \
  "v_add_f32 v10, v10, v11\n" \
  "s_nop 1\n" \
  "v_permlane32_swap_b32 v8, v10\n" \
  "v_add_f32 v8, v8, v10\n" \
  "v_and_b32 v5, 15, v0\n" \
  "v_lshrrev_b32 v6, 4, v0\n" \
  "v_lshl_add_u32 v5, v5, 2, v6\n" \
  "s_lshl_b32 s35, s27, 8\n" \
  "v_lshl_add_u32 v5, v5, 2, s35\n" \
  "ds_write_b32 v5, v8\n" \
  "s_waitcnt lgkmcnt(0)\n" \
  "s_barrier\n" \
  "s_cmp_lg_u32 s27, 0\n" \
  "s_cbranch_scc1 LF_end\n" \
  "v_lshlrev_b32 v5, 2, v0\n" \
  "ds_read2st64_b32 v[8:9], v5 offset0:0 offset1:1\n" \
  "ds_read2st64_b32 v[10:11], v5 offset0:2 offset1:3\n" \
  "ds_read2st64_b32 v[12:13], v5 offset0:4 offset1:5\n" \
  "ds_read2st64_b32 v[14:15], v5 offset0:6 offset1:7\n" \
  "v_add_f32 v16, v16, v17\n" \
  "v_lshlrev_b32 v4, 16, v4\n" \
  "v_cvt_pk_bf16_f32 v16, v16, v16\n" \
  "v_lshlrev_b32 v2, 1, v2\n" \
  "s_waitcnt lgkmcnt(0)\n" \
  "v_pk_add_f32 v[8:9], v[8:9], v[10:11]\n" \
  "v_pk_add_f32 v[12:13], v[12:13], v[14:15]\n" \
  "v_pk_add_f32 v[8:9], v[8:9], v[12:13]\n" \
  "v_add_f32 v8, v8, v9\n" \
  "v_cvt_pk_bf16_f32 v8, v8, v8\n" \
  "v_lshlrev_b32 v8, 16, v8\n" \
  "v_add_f32 v8, v8, v4\n" \
  "v_cvt_pk_bf16_f32 v8, v8, v8\n" \
  "v_lshlrev_b32 v8, 16, v8\n" \
  "v_lshlrev_b32 v16, 16, v16\n" \
  "v_add_f32 v8, v8, v16\n" \
  "v_cvt_pk_bf16_f32 v8, v8, v8\n" \
  "global_store_short v2, v8, s[22:23]\n" \
  "LF_end:\n" \
  "s_branch LZ_end\n" \
  "LG_pol:\n" \
  "s_mov_b64 exec, 0xff\n" \
  "global_load_dword v200, v198, s[48:49] offset:3076 sc1\n" \
  "s_mov_b64 exec, -1\n" \
  "s_branch LG_rt0\n" \
  "LG_chk:\n" \
  "s_mov_b32 s5, 0x10000\n" \
  "LG_p:\n" \
  "s_mov_b64 exec, 0xff\n" \
  "v_cmp_le_u32 vcc, 32, v200\n" \
  "s_mov_b64 exec, -1\n" \
  "s_sub_u32 s5, s5, 1\n" \
  "s_cselect_b32 s35, 0xff, vcc_lo\n" \
  "s_cmp_eq_u32 s35, 0xff\n" \
  "s_cbranch_scc1 LG_p_x\n" \
  "s_sleep 2\n" \
  "s_mov_b64 exec, 0xff\n" \
  "global_load_dword v200, v198, s[48:49] offset:3076 sc1\n" \
  "s_mov_b64 exec, -1\n" \
  "s_waitcnt vmcnt(0)\n" \
  "s_branch LG_p\n" \
  "LG_p_x:\n" \
  "s_branch LG_ret\n" \
  "LZ_end:\n" \
  "s_waitcnt vmcnt(0) lgkmcnt(0)\n"

#define R10(p, b) p #b "0", p #b "1", p #b "2", p #b "3", p #b "4", p #b "5", p #b "6", p #b "7", p #b "8", p #b "9"
__global__ __launch_bounds__(512, 2) void k1(const int* __restrict__ idx, const unsigned short* __restrict__ table,
                                             const unsigned short* __restrict__ w_p, const unsigned short* __restrict__ w1,
                                             const unsigned short* __restrict__ b1, const unsigned short* __restrict__ w2,
                                             const unsigned short* __restrict__ b2, __hip_bfloat16* __restrict__ out, char* __restrict__ ws) {
  __shared__ __attribute__((aligned(1024))) char smem[163840];
  unsigned tid = threadIdx.x;
  asm volatile(K_ASM
               : "+{v0}"(tid)
               : "{s[8:9]}"(idx), "{s[10:11]}"(table), "{s[12:13]}"(w_p), "{s[14:15]}"(w1), "{s[16:17]}"(b1), "{s[18:19]}"(w2), "{s[20:21]}"(b2),
                 "{s[22:23]}"(out), "{s[24:25]}"(ws), "{s26}"(blockIdx.x), "s"((unsigned)(uintptr_t)smem)
               : "memory", "scc", "vcc", "s3", "s4", "s5", "s6", "s7", "s27", "s28", "s29", "s30", "s31", "s33", "s34", "s35", "s36", "s37", "s38", "s39", R10("s", 4), R10("s", 5), R10("s", 6), R10("s", 7), R10("s", 8), R10("s", 9),
                 "v1", "v2", "v3", "v4", "v5", "v6", "v7", "v8", "v9", R10("v", 1), R10("v", 2), R10("v", 3), R10("v", 4), R10("v", 5), R10("v", 6), R10("v", 7), R10("v", 8), R10("v", 9),
                 R10("v", 10), R10("v", 11), R10("v", 12), R10("v", 13), R10("v", 14), R10("v", 15), R10("v", 16), R10("v", 17), R10("v", 18), R10("v", 19),
                 R10("v", 20), R10("v", 21), R10("v", 22), R10("v", 23), R10("v", 24), "v250", "v251", "v252", "v253", "v254", "v255");
}

extern "C" void kernel_launch(void* const* d_in, const int* in_sizes, int n_in, void* d_out, int out_size, void* d_ws,
                              size_t ws_size, hipStream_t stream) {
  k1<<<dim3(256), dim3(512), 0, stream>>>((const int*)d_in[0], (const unsigned short*)d_in[1], (const unsigned short*)d_in[2], (const unsigned short*)d_in[3],
                                          (const unsigned short*)d_in[4], (const unsigned short*)d_in[5], (const unsigned short*)d_in[6], (__hip_bfloat16*)d_out, (char*)d_ws);
}
```

```cpp
#include <hip/hip_runtime.h>
#include <hip/hip_bf16.h>
#include <stdint.h>

#define K_ASM \
  "LZ_first:\n" \
  "v_readfirstlane_b32 s27, v0\n" \
  "v_and_b32 v255, 63, v0\n" \
  "s_and_b32 s29, s26, 7\n" \
  "s_lshr_b32 s30, s26, 3\n" \
  "s_lshr_b32 s27, s27, 6\n" \
  "s_and_b32 s31, s30, 1\n" \
  "s_and_b32 s28, s27, 3\n" \
  "s_lshr_b32 s35, s30, 1\n" \
  "s_lshl3_add_u32 s7, s35, s29\n" \
  "v_lshrrev_b32 v1, 3, v255\n" \
  "v_and_b32 v2, 7, v255\n" \
  "v_lshlrev_b32 v1, 8, v1\n" \
  "v_lshl_or_b32 v254, v2, 4, v1\n" \
  "s_lshl_b32 s35, s31, 7\n" \
  "s_add_u32 s40, s10, s35\n" \
  "s_addc_u32 s41, s11, 0\n" \
  "s_mul_i32 s36, s7, 37\n" \
  "s_and_b32 s36, s36, 63\n" \
  "s_add_u32 s36, s36, s28\n" \
  "s_cmp_ge_u32 s27, 4\n" \
  "s_cbranch_scc1 LB_start\n" \
  "v_lshlrev_b32 v1, 4, v255\n" \
  "s_lshl_b32 s35, s28, 10\n" \
  "v_add_u32 v1, s35, v1\n" \
  "s_add_u32 m0, s35, 138240\n" \
  "s_nop 0\n" \
  "global_load_lds_dwordx4 v1, s[12:13]\n" \
  "v_add_u32 v1, 0x1000, v1\n" \
  "s_add_u32 m0, m0, 0x1000\n" \
  "s_nop 0\n" \
  "global_load_lds_dwordx4 v1, s[12:13]\n" \
  "v_add_u32 v1, 0x1000, v1\n" \
  "s_add_u32 m0, m0, 0x1000\n" \
  "s_nop 0\n" \
  "global_load_lds_dwordx4 v1, s[12:13]\n" \
  "v_add_u32 v1, 0x1000, v1\n" \
  "s_add_u32 m0, m0, 0x1000\n" \
  "s_nop 0\n" \
  "global_load_lds_dwordx4 v1, s[12:13]\n" \
  "v_add_u32 v1, 0x1000, v1\n" \
  "s_add_u32 m0, m0, 0x1000\n" \
  "v_min_u32 v1, 19984, v1\n" \
  "global_load_lds_dwordx4 v1, s[12:13]\n" \
  "v_and_b32 v252, 15, v255\n" \
  "v_lshrrev_b32 v253, 4, v255\n" \
  "v_mul_u32_u24 v2, 400, v253\n" \
  "v_min_u32 v3, 8, v252\n" \
  "v_lshl_add_u32 v250, v252, 4, v2\n" \
  "v_lshl_add_u32 v251, v3, 4, v2\n" \
  "s_lshl2_add_u32 s35, s7, s28\n" \
  "s_mul_i32 s35, s35, 12800\n" \
  "s_add_u32 s44, s8, s35\n" \
  "s_addc_u32 s45, s9, 0\n" \
  "global_load_dwordx4 v[232:235], v250, s[44:45] offset:0\n" \
  "global_load_dwordx4 v[236:239], v251, s[44:45] offset:256\n" \
  "global_load_dwordx4 v[240:243], v250, s[44:45] offset:1600\n" \
  "global_load_dwordx4 v[244:247], v251, s[44:45] offset:1856\n" \
  "s_setprio 2\n" \
  "v_lshrrev_b32 v1, 2, v252\n" \
  "v_and_b32 v2, 3, v252\n" \
  "v_cmp_eq_u32 vcc, v1, v253\n" \
  "v_mov_b32 v4, 0x3f80\n" \
  "v_and_b32 v13, 1, v253\n" \
  "v_lshlrev_b32 v14, 1, v13\n" \
  "v_xor_b32 v2, v2, v14\n" \
  "v_cndmask_b32 v2, 4, v2, vcc\n" \
  "v_cmp_eq_u32 vcc, 0, v2\n" \
  "s_nop 1\n" \
  "v_cndmask_b32 v216, 0, v4, vcc\n" \
  "v_lshlrev_b32 v220, 16, v216\n" \
  "v_cmp_eq_u32 vcc, 1, v2\n" \
  "s_nop 1\n" \
  "v_cndmask_b32 v217, 0, v4, vcc\n" \
  "v_lshlrev_b32 v221, 16, v217\n" \
  "v_cmp_eq_u32 vcc, 2, v2\n" \
  "s_nop 1\n" \
  "v_cndmask_b32 v218, 0, v4, vcc\n" \
  "v_lshlrev_b32 v222, 16, v218\n" \
  "v_cmp_eq_u32 vcc, 3, v2\n" \
  "s_nop 1\n" \
  "v_cndmask_b32 v219, 0, v4, vcc\n" \
  "v_lshlrev_b32 v223, 16, v219\n" \
  "v_and_b32 v1, 2, v252\n" \
  "v_lshlrev_b32 v1, 3, v1\n" \
  "v_lshlrev_b32 v1, v1, v4\n" \
  "v_and_b32 v2, 0x31, v255\n" \
  "v_mov_b64 v[224:225], 0\n" \
  "v_cmp_eq_u32 vcc, 0, v2\n" \
  "s_nop 1\n" \
  "v_cndmask_b32 v226, 0, v1, vcc\n" \
  "v_cmp_eq_u32 vcc, 1, v2\n" \
  "s_nop 1\n" \
  "v_cndmask_b32 v227, 0, v1, vcc\n" \
  "v_mul_u32_u24 v5, 400, v252\n" \
  "v_add_u32 v5, 138240, v5\n" \
  "v_lshl_add_u32 v6, v253, 4, v5\n" \
  "v_add_u32 v7, 6400, v5\n" \
  "v_add_u32 v8, 6400, v6\n" \
  "v_add_u32 v9, 12800, v5\n" \
  "v_add_u32 v10, 12800, v6\n" \
  "v_bfe_u32 v1, v252, 1, 1\n" \
  "v_and_b32 v2, 1, v252\n" \
  "v_lshl_add_u32 v1, v2, 1, v1\n" \
  "v_mul_u32_u24 v1, 200, v1\n" \
  "v_add_u32 v11, 157440, v1\n" \
  "v_lshl_add_u32 v12, v253, 4, v11\n" \
  "v_lshlrev_b32 v13, 3, v13\n" \
  "v_sub_u32 v16, v5, v13\n" \
  "v_add_u32 v5, v5, v13\n" \
  "v_sub_u32 v17, v6, v13\n" \
  "v_add_u32 v6, v6, v13\n" \
  "v_sub_u32 v18, v7, v13\n" \
  "v_add_u32 v7, v7, v13\n" \
  "v_sub_u32 v19, v8, v13\n" \
  "v_add_u32 v8, v8, v13\n" \
  "v_sub_u32 v20, v9, v13\n" \
  "v_add_u32 v9, v9, v13\n" \
  "v_sub_u32 v21, v10, v13\n" \
  "v_add_u32 v10, v10, v13\n" \
  "v_sub_u32 v22, v11, v13\n" \
  "v_add_u32 v11, v11, v13\n" \
  "v_sub_u32 v23, v12, v13\n" \
  "v_add_u32 v12, v12, v13\n" \
  "s_waitcnt vmcnt(4)\n" \
  "s_barrier\n" \
  "ds_read_b64 v[200:201], v12 offset:128\n" \
  "ds_read_b64 v[202:203], v23 offset:136\n" \
  "ds_read_b64 v[184:185], v9 offset:184\n" \
  "ds_read_b64 v[186:187], v20 offset:192\n" \
  "ds_read_b64 v[188:189], v9 offset:384\n" \
  "ds_read_b64 v[190:191], v20 offset:392\n" \
  "ds_read_b64 v[196:197], v12 offset:64\n" \
  "ds_read_b64 v[198:199], v23 offset:72\n" \
  "ds_read_b64 v[176:177], v7 offset:184\n" \
  "ds_read_b64 v[178:179], v18 offset:192\n" \
  "ds_read_b64 v[180:181], v7 offset:384\n" \
  "ds_read_b64 v[182:183], v18 offset:392\n" \
  "ds_read_b64 v[192:193], v12 offset:0\n" \
  "ds_read_b64 v[194:195], v23 offset:8\n" \
  "s_waitcnt lgkmcnt(12)\n" \
  "v_mfma_f32_16x16x32_bf16 v[44:47], v[200:203], v[216:219], 0\n" \
  "ds_read_b64 v[168:169], v5 offset:184\n" \
  "ds_read_b64 v[170:171], v16 offset:192\n" \
  "s_waitcnt lgkmcnt(12)\n" \
  "v_mfma_f32_16x16x32_bf16 v[44:47], v[224:227], v[184:187], v[44:47]\n" \
  "ds_read_b64 v[172:173], v5 offset:384\n" \
  "ds_read_b64 v[174:175], v16 offset:392\n" \
  "v_mfma_f32_16x16x32_bf16 v[228:231], v[200:203], v[220:223], 0\n" \
  "ds_read_b64 v[96:97], v6 offset:0\n" \
  "ds_read_b64 v[98:99], v17 offset:8\n" \
  "s_waitcnt lgkmcnt(14)\n" \
  "v_mfma_f32_16x16x32_bf16 v[228:231], v[224:227], v[188:191], v[228:231]\n" \
  "ds_read_b64 v[124:125], v8 offset:64\n" \
  "ds_read_b64 v[126:127], v19 offset:72\n" \
  "s_waitcnt lgkmcnt(14)\n" \
  "v_mfma_f32_16x16x32_bf16 v[60:63], v[196:199], v[216:219], 0\n" \
  "ds_read_b64 v[136:137], v8 offset:264\n" \
  "ds_read_b64 v[138:139], v19 offset:272\n" \
  "s_waitcnt lgkmcnt(14)\n" \
  "v_mfma_f32_16x16x32_bf16 v[60:63], v[224:227], v[176:179], v[60:63]\n" \
  "ds_read_b64 v[140:141], v8 offset:328\n" \
  "ds_read_b64 v[142:143], v19 offset:336\n" \
  "v_mfma_f32_16x16x32_bf16 v[212:215], v[196:199], v[220:223], 0\n" \
  "ds_read_b64 v[160:161], v10 offset:264\n" \
  "ds_read_b64 v[162:163], v21 offset:272\n" \
  "s_waitcnt lgkmcnt(15)\n" \
  "v_mfma_f32_16x16x32_bf16 v[212:215], v[224:227], v[180:183], v[212:215]\n" \
  "ds_read_b64 v[116:117], v6 offset:328\n" \
  "ds_read_b64 v[118:119], v17 offset:336\n" \
  "s_waitcnt lgkmcnt(15)\n" \
  "v_mfma_f32_16x16x32_bf16 v[68:71], v[192:195], v[216:219], 0\n" \
  "ds_read_b64 v[144:145], v10 offset:0\n" \
  "ds_read_b64 v[146:147], v21 offset:8\n" \
  "s_waitcnt lgkmcnt(15)\n" \
  "v_mfma_f32_16x16x32_bf16 v[68:71], v[224:227], v[168:171], v[68:71]\n" \
  "ds_read_b64 v[148:149], v10 offset:64\n" \
  "ds_read_b64 v[150:151], v21 offset:72\n" \
  "v_mfma_f32_16x16x32_bf16 v[208:211], v[192:195], v[220:223], 0\n" \
  "ds_read_b64 v[104:105], v6 offset:128\n" \
  "ds_read_b64 v[106:107], v17 offset:136\n" \
  "s_waitcnt lgkmcnt(15)\n" \
  "v_mfma_f32_16x16x32_bf16 v[208:211], v[224:227], v[172:175], v[208:211]\n" \
  "ds_read_b64 v[100:101], v6 offset:64\n" \
  "ds_read_b64 v[102:103], v17 offset:72\n" \
  "v_mfma_f32_16x16x32_bf16 v[92:95], v[96:99], v[216:219], 0\n" \
  "ds_read_b64 v[120:121], v8 offset:0\n" \
  "ds_read_b64 v[122:123], v19 offset:8\n" \
  "s_waitcnt lgkmcnt(15)\n" \
  "v_mfma_f32_16x16x32_bf16 v[24:27], v[124:127], v[220:223], 0\n" \
  "ds_read_b64 v[112:113], v6 offset:264\n" \
  "ds_read_b64 v[114:115], v17 offset:272\n" \
  "v_mfma_f32_16x16x32_bf16 v[24:27], v[216:219], v[136:139], v[24:27]\n" \
  "ds_read_b64 v[156:157], v10 offset:200\n" \
  "ds_read_b64 v[158:159], v21 offset:208\n" \
  "s_waitcnt lgkmcnt(15)\n" \
  "v_mfma_f32_16x16x32_bf16 v[28:31], v[140:143], v[220:223], 0\n" \
  "ds_read_b64 v[132:133], v8 offset:200\n" \
  "ds_read_b64 v[134:135], v19 offset:208\n" \
  "v_mfma_f32_16x16x32_bf16 v[28:31], v[220:223], v[160:163], v[28:31]\n" \
  "ds_read_b64 v[108:109], v6 offset:200\n" \
  "ds_read_b64 v[110:111], v17 offset:208\n" \
  "s_waitcnt lgkmcnt(15)\n" \
  "v_mfma_f32_16x16x32_bf16 v[32:35], v[116:119], v[216:219], 0\n" \
  "ds_read_b64 v[152:153], v10 offset:128\n" \
  "ds_read_b64 v[154:155], v21 offset:136\n" \
  "v_mfma_f32_16x16x32_bf16 v[32:35], v[220:223], v[144:147], v[32:35]\n" \
  "ds_read_b64 v[164:165], v10 offset:328\n" \
  "ds_read_b64 v[166:167], v21 offset:336\n" \
  "v_mfma_f32_16x16x32_bf16 v[36:39], v[140:143], v[216:219], 0\n" \
  "ds_read_b64 v[128:129], v8 offset:128\n" \
  "ds_read_b64 v[130:131], v19 offset:136\n" \
  "s_waitcnt lgkmcnt(15)\n" \
  "v_mfma_f32_16x16x32_bf16 v[36:39], v[220:223], v[148:151], v[36:39]\n" \
  "ds_read_b64 v[204:205], v11 offset:184\n" \
  "ds_read_b64 v[206:207], v22 offset:192\n" \
  "v_mfma_f32_16x16x32_bf16 v[40:43], v[104:107], v[216:219], 0\n" \
  "v_mfma_f32_16x16x32_bf16 v[40:43], v[216:219], v[144:147], v[40:43]\n" \
  "v_mfma_f32_16x16x32_bf16 v[48:51], v[100:103], v[216:219], 0\n" \
  "s_waitcnt lgkmcnt(15)\n" \
  "v_mfma_f32_16x16x32_bf16 v[48:51], v[216:219], v[120:123], v[48:51]\n" \
  "s_waitcnt lgkmcnt(14)\n" \
  "v_mfma_f32_16x16x32_bf16 v[52:55], v[112:115], v[216:219], 0\n" \
  "v_mfma_f32_16x16x32_bf16 v[52:55], v[220:223], v[120:123], v[52:55]\n" \
  "v_mfma_f32_16x16x32_bf16 v[56:59], v[116:119], v[220:223], 0\n" \
  "s_waitcnt lgkmcnt(12)\n" \
  "v_mfma_f32_16x16x32_bf16 v[56:59], v[220:223], v[156:159], v[56:59]\n" \
  "v_mfma_f32_16x16x32_bf16 v[64:67], v[104:107], v[220:223], 0\n" \
  "v_mfma_f32_16x16x32_bf16 v[64:67], v[216:219], v[156:159], v[64:67]\n" \
  "v_mfma_f32_16x16x32_bf16 v[72:75], v[136:139], v[220:223], 0\n" \
  "v_mfma_f32_16x16x32_bf16 v[76:79], v[112:115], v[220:223], 0\n" \
  "s_waitcnt lgkmcnt(10)\n" \
  "v_mfma_f32_16x16x32_bf16 v[76:79], v[220:223], v[132:135], v[76:79]\n" \
  "v_mfma_f32_16x16x32_bf16 v[80:83], v[100:103], v[220:223], 0\n" \
  "v_mfma_f32_16x16x32_bf16 v[80:83], v[216:219], v[132:135], v[80:83]\n" \
  "v_mfma_f32_16x16x32_bf16 v[84:87], v[96:99], v[220:223], 0\n" \
  "s_waitcnt lgkmcnt(8)\n" \
  "v_mfma_f32_16x16x32_bf16 v[84:87], v[216:219], v[108:111], v[84:87]\n" \
  "v_mfma_f32_16x16x32_bf16 v[88:91], v[108:111], v[220:223], 0\n" \
  "v_mfma_f32_16x16x32_bf16 v[0:3], v[124:127], v[216:219], 0\n" \
  "s_waitcnt lgkmcnt(6)\n" \
  "v_mfma_f32_16x16x32_bf16 v[4:7], v[152:155], v[216:219], 0\n" \
  "v_mfma_f32_16x16x32_bf16 v[8:11], v[152:155], v[220:223], 0\n" \
  "s_waitcnt lgkmcnt(4)\n" \
  "v_mfma_f32_16x16x32_bf16 v[8:11], v[216:219], v[164:167], v[8:11]\n" \
  "v_mfma_f32_16x16x32_bf16 v[12:15], v[164:167], v[220:223], 0\n" \
  "s_waitcnt lgkmcnt(2)\n" \
  "v_mfma_f32_16x16x32_bf16 v[16:19], v[128:131], v[220:223], 0\n" \
  "v_mfma_f32_16x16x32_bf16 v[16:19], v[216:219], v[160:163], v[16:19]\n" \
  "v_mfma_f32_16x16x32_bf16 v[20:23], v[128:131], v[216:219], 0\n" \
  "v_mfma_f32_16x16x32_bf16 v[20:23], v[216:219], v[148:151], v[20:23]\n" \
  "s_waitcnt lgkmcnt(0)\n" \
  "v_mov_b32 v254, 163328\n" \
  "v_mov_b32 v255, 1\n" \
  "s_mov_b64 exec, 1\n" \
  "ds_add_u32 v254, v255 offset:16\n" \
  "s_mov_b64 exec, -1\n" \
  "v_mfma_f32_16x16x32_bf16 v[168:171], v[216:219], v[96:99], 0\n" \
  "v_mfma_f32_16x16x32_bf16 v[172:175], v[204:207], v[224:227], 0\n" \
  "v_mfma_f32_16x16x32_bf16 v[176:179], v[224:227], v[204:207], 0\n" \
  "s_mov_b32 exec_lo, 0x0\n" \
  "s_mov_b32 exec_hi, 0xffffffff\n" \
  "v_mov_b64 v[44:45], v[228:229]\n" \
  "v_mov_b64 v[46:47], v[230:231]\n" \
  "v_mov_b64 v[60:61], v[212:213]\n" \
  "v_mov_b64 v[62:63], v[214:215]\n" \
  "v_mov_b64 v[68:69], v[208:209]\n" \
  "v_mov_b64 v[70:71], v[210:211]\n" \
  "s_mov_b32 exec_lo, 0xff00ff00\n" \
  "s_mov_b32 exec_hi, 0x0\n" \
  "v_add_f32 v92, v92, v168\n" \
  "v_add_f32 v93, v93, v169\n" \
  "v_add_f32 v94, v94, v170\n" \
  "v_add_f32 v95, v95, v171\n" \
  "s_mov_b32 exec_lo, 0xf0000f00\n" \
  "s_mov_b32 exec_hi, 0x0\n" \
  "v_sub_f32 v92, v92, v176\n" \
  "v_sub_f32 v93, v93, v177\n" \
  "v_sub_f32 v94, v94, v178\n" \
  "v_sub_f32 v95, v95, v179\n" \
  "s_mov_b32 exec_lo, 0x0\n" \
  "s_mov_b32 exec_hi, 0xff00ff\n" \
  "v_mov_b64 v[92:93], 0\n" \
  "v_mov_b64 v[94:95], 0\n" \
  "s_mov_b32 exec_lo, 0x0\n" \
  "s_mov_b32 exec_hi, 0xf0000f\n" \
  "v_mov_b64 v[92:93], v[172:173]\n" \
  "v_mov_b64 v[94:95], v[174:175]\n" \
  "s_mov_b64 exec, -1\n" \
  "v_lshlrev_b32 v188, 4, v253\n" \
  "v_and_b32 v96, 4, v252\n" \
  "v_lshl_add_u32 v96, v253, 5, v96\n" \
  "v_mov_b32 v191, 0x40000000\n" \
  "v_cmp_gt_u32 vcc, 8, v252\n" \
  "v_mov_b32 v193, 0x4444\n" \
  "v_mov_b32 v98, 0xeeee\n" \
  "v_cndmask_b32 v189, v191, v96, vcc\n" \
  "v_cndmask_b32 v190, v96, v191, vcc\n" \
  "v_and_b32 v99, 4, v252\n" \
  "v_cmp_ne_u32 vcc, 0, v99\n" \
  "s_lshl_b32 s35, s28, 11\n" \
  "s_add_u32 s35, s35, 130048\n" \
  "v_cndmask_b32 v193, v193, v98, vcc\n" \
  "v_lshl_add_u32 v194, v252, 4, s35\n" \
  "v_and_b32 v96, 7, v252\n" \
  "v_lshlrev_b32 v96, 5, v96\n" \
  "v_lshl_add_u32 v96, v253, 8, v96\n" \
  "v_lshrrev_b32 v97, 3, v252\n" \
  "v_lshl_add_u32 v96, v97, 2, v96\n" \
  "v_add_u32 v195, s35, v96\n" \
  "v_and_b32 v97, v97, v252\n" \
  "v_lshlrev_b32 v179, 4, v97\n" \
  "s_waitcnt vmcnt(0)\n" \
  "v_cvt_pk_u16_u32 v232, v232, v233\n" \
  "v_cvt_pk_u16_u32 v234, v234, v235\n" \
  "v_cvt_pk_u16_u32 v236, v236, v237\n" \
  "v_cvt_pk_u16_u32 v238, v238, v239\n" \
  "v_pk_lshrrev_b16 v233, 3, v232 op_sel_hi:[0,1]\n" \
  "v_pk_lshrrev_b16 v235, 3, v234 op_sel_hi:[0,1]\n" \
  "v_pk_lshrrev_b16 v237, 3, v236 op_sel_hi:[0,1]\n" \
  "v_pk_lshrrev_b16 v239, 3, v238 op_sel_hi:[0,1]\n" \
  "v_pk_mad_u16 v232, v232, 8, v233 op_sel_hi:[1,0,1]\n" \
  "v_pk_mad_u16 v234, v234, 8, v235 op_sel_hi:[1,0,1]\n" \
  "v_pk_mad_u16 v236, v236, 8, v237 op_sel_hi:[1,0,1]\n" \
  "v_pk_mad_u16 v238, v238, 8, v239 op_sel_hi:[1,0,1]\n" \
  "v_alignbit_b32 v236, v236, v236, v179\n" \
  "v_alignbit_b32 v238, v238, v238, v179\n" \
  "ds_write2_b32 v195, v232, v234 offset0:0 offset1:4\n" \
  "ds_write2_b32 v195, v236, v238 offset0:2 offset1:6\n" \
  "v_cvt_pk_u16_u32 v240, v240, v241\n" \
  "v_cvt_pk_u16_u32 v242, v242, v243\n" \
  "v_cvt_pk_u16_u32 v244, v244, v245\n" \
  "v_cvt_pk_u16_u32 v246, v246, v247\n" \
  "v_pk_lshrrev_b16 v241, 3, v240 op_sel_hi:[0,1]\n" \
  "v_pk_lshrrev_b16 v243, 3, v242 op_sel_hi:[0,1]\n" \
  "v_pk_lshrrev_b16 v245, 3, v244 op_sel_hi:[0,1]\n" \
  "v_pk_lshrrev_b16 v247, 3, v246 op_sel_hi:[0,1]\n" \
  "v_pk_mad_u16 v240, v240, 8, v241 op_sel_hi:[1,0,1]\n" \
  "v_pk_mad_u16 v242, v242, 8, v243 op_sel_hi:[1,0,1]\n" \
  "v_pk_mad_u16 v244, v244, 8, v245 op_sel_hi:[1,0,1]\n" \
  "v_pk_mad_u16 v246, v246, 8, v247 op_sel_hi:[1,0,1]\n" \
  "v_alignbit_b32 v244, v244, v244, v179\n" \
  "v_alignbit_b32 v246, v246, v246, v179\n" \
  "v_add_u32 v241, 0x400, v195\n" \
  "ds_write2_b32 v241, v240, v242 offset0:0 offset1:4\n" \
  "ds_write2_b32 v241, v244, v246 offset0:2 offset1:6\n" \
  "v_add_u32 v96, 6400, v250\n" \
  "v_add_u32 v97, 6400, v251\n" \
  "v_mov_b32 v98, v254\n" \
  "L1_:\n" \
  "ds_read_b32 v99, v98 offset:12\n" \
  "s_waitcnt lgkmcnt(0)\n" \
  "v_readfirstlane_b32 s35, v99\n" \
  "s_cmp_lt_u32 s35, 4\n" \
  "s_cbranch_scc1 L1_\n" \
  "v_lshlrev_b32 v203, 4, v253\n" \
  "s_lshr_b32 s35, s30, 4\n" \
  "s_lshl1_add_u32 s35, s29, s35\n" \
  "s_lshl_b32 s35, s35, 6\n" \
  "s_bfe_u32 s37, s30, 0x20002\n" \
  "s_lshl3_add_u32 s35, s37, s35\n" \
  "v_and_b32 v203, 16, v203\n" \
  "v_lshl_add_u32 v203, v203, 1, s35\n" \
  "global_load_dwordx2 v[204:205], v203, s[16:17]\n" \
  "global_load_dwordx2 v[206:207], v203, s[18:19]\n" \
  "v_mov_b32 v203, 0\n" \
  "global_load_ushort v203, v203, s[20:21]\n" \
  "s_add_u32 s46, s44, 4800\n" \
  "s_addc_u32 s47, s45, 0\n" \
  "global_load_dwordx4 v[208:211], v96, s[44:45] offset:-3200\n" \
  "global_load_dwordx4 v[212:215], v97, s[44:45] offset:-2944\n" \
  "global_load_dwordx4 v[216:219], v96, s[44:45] offset:-1600\n" \
  "global_load_dwordx4 v[220:223], v97, s[44:45] offset:-1344\n" \
  "global_load_dwordx4 v[224:227], v96, s[44:45] offset:0\n" \
  "global_load_dwordx4 v[228:231], v97, s[44:45] offset:256\n" \
  "global_load_dwordx4 v[232:235], v96, s[44:45] offset:1600\n" \
  "global_load_dwordx4 v[236:239], v97, s[44:45] offset:1856\n" \
  "global_load_dwordx4 v[240:243], v96, s[44:45] offset:3200\n" \
  "global_load_dwordx4 v[244:247], v97, s[44:45] offset:3456\n" \
  "global_load_dwordx4 v[248:251], v96, s[46:47] offset:0\n" \
  "global_load_dwordx4 v[252:255], v97, s[46:47] offset:256\n" \
  "s_setprio 1\n" \
  "ds_read_b128 v[180:183], v194\n" \
  "v_add_u32 v194, 0x100, v194\n" \
  "s_waitcnt lgkmcnt(0)\n" \
  "v_mad_u32_u16 v184, v182, 16, v188\n" \
  "ds_read_b128 v[128:131], v184 offset:0\n" \
  "ds_read_b128 v[132:135], v184 offset:64\n" \
  "ds_read_b128 v[172:175], v191\n" \
  "v_mad_u32_u16 v185, v181, 16, v188\n" \
  "v_mad_u32_u16 v186, v182, 16, v188 op_sel:[1,0,0,0]\n" \
  "ds_read_b128 v[112:115], v185 offset:0\n" \
  "ds_read_b128 v[116:119], v185 offset:64\n" \
  "ds_read_b128 v[136:139], v186 offset:0\n" \
  "ds_read_b128 v[140:143], v186 offset:64\n" \
  "ds_read_b128 v[168:171], v191\n" \
  "v_mad_u32_u16 v187, v181, 16, v188 op_sel:[1,0,0,0]\n" \
  "v_mad_u32_u16 v184, v180, 16, v188 op_sel:[1,0,0,0]\n" \
  "ds_read_b128 v[120:123], v187 offset:0\n" \
  "v_mad_u32_u16 v185, v180, 16, v188\n" \
  "ds_read_b128 v[124:127], v187 offset:64\n" \
  "ds_read_b128 v[104:107], v184 offset:0\n" \
  "v_mad_u32_u16 v186, v183, 16, v189\n" \
  "ds_read_b128 v[108:111], v184 offset:64\n" \
  "ds_read_b128 v[96:99], v185 offset:0\n" \
  "s_waitcnt lgkmcnt(9)\n" \
  "s_mov_b32 s51, 3\n" \
  "s_call_b64 s[52:53], LA_loop\n" \
  "LA_ev0:\n" \
  "v_mov_b32 v196, v178\n" \
  "s_waitcnt vmcnt(10)\n" \
  "v_cvt_pk_u16_u32 v208, v208, v209\n" \
  "v_cvt_pk_u16_u32 v210, v210, v211\n" \
  "v_cvt_pk_u16_u32 v212, v212, v213\n" \
  "v_cvt_pk_u16_u32 v214, v214, v215\n" \
  "v_pk_lshrrev_b16 v209, 3, v208 op_sel_hi:[0,1]\n" \
  "v_pk_lshrrev_b16 v211, 3, v210 op_sel_hi:[0,1]\n" \
  "v_pk_lshrrev_b16 v213, 3, v212 op_sel_hi:[0,1]\n" \
  "v_pk_lshrrev_b16 v215, 3, v214 op_sel_hi:[0,1]\n" \
  "v_pk_mad_u16 v208, v208, 8, v209 op_sel_hi:[1,0,1]\n" \
  "v_pk_mad_u16 v210, v210, 8, v211 op_sel_hi:[1,0,1]\n" \
  "v_pk_mad_u16 v212, v212, 8, v213 op_sel_hi:[1,0,1]\n" \
  "v_pk_mad_u16 v214, v214, 8, v215 op_sel_hi:[1,0,1]\n" \
  "v_alignbit_b32 v212, v212, v212, v179\n" \
  "v_alignbit_b32 v214, v214, v214, v179\n" \
  "ds_write2_b32 v195, v208, v210 offset0:0 offset1:4\n" \
  "ds_write2_b32 v195, v212, v214 offset0:2 offset1:6\n" \
  "v_readfirstlane_b32 s6, v203\n" \
  "s_mov_b32 s51, 4\n" \
  "s_call_b64 s[52:53], LA_loop\n" \
  "LA_ev1:\n" \
  "v_mov_b32 v197, v178\n" \
  "s_waitcnt vmcnt(8)\n" \
  "v_cvt_pk_u16_u32 v216, v216, v217\n" \
  "v_cvt_pk_u16_u32 v218, v218, v219\n" \
  "v_cvt_pk_u16_u32 v220, v220, v221\n" \
  "v_cvt_pk_u16_u32 v222, v222, v223\n" \
  "v_pk_lshrrev_b16 v217, 3, v216 op_sel_hi:[0,1]\n" \
  "v_pk_lshrrev_b16 v219, 3, v218 op_sel_hi:[0,1]\n" \
  "v_pk_lshrrev_b16 v221, 3, v220 op_sel_hi:[0,1]\n" \
  "v_pk_lshrrev_b16 v223, 3, v222 op_sel_hi:[0,1]\n" \
  "v_pk_mad_u16 v216, v216, 8, v217 op_sel_hi:[1,0,1]\n" \
  "v_pk_mad_u16 v218, v218, 8, v219 op_sel_hi:[1,0,1]\n" \
  "v_pk_mad_u16 v220, v220, 8, v221 op_sel_hi:[1,0,1]\n" \
  "v_pk_mad_u16 v222, v222, 8, v223 op_sel_hi:[1,0,1]\n" \
  "v_alignbit_b32 v220, v220, v220, v179\n" \
  "v_alignbit_b32 v222, v222, v222, v179\n" \
  "v_add_u32 v217, 0x400, v195\n" \
  "ds_write2_b32 v217, v216, v218 offset0:0 offset1:4\n" \
  "ds_write2_b32 v217, v220, v222 offset0:2 offset1:6\n" \
  "v_subrev_u32 v194, 0x800, v194\n" \
  "s_mov_b32 s51, 4\n" \
  "s_call_b64 s[52:53], LA_loop\n" \
  "LA_ev2:\n" \
  "v_mov_b32 v198, v178\n" \
  "s_waitcnt vmcnt(6)\n" \
  "v_cvt_pk_u16_u32 v224, v224, v225\n" \
  "v_cvt_pk_u16_u32 v226, v226, v227\n" \
  "v_cvt_pk_u16_u32 v228, v228, v229\n" \
  "v_cvt_pk_u16_u32 v230, v230, v231\n" \
  "v_pk_lshrrev_b16 v225, 3, v224 op_sel_hi:[0,1]\n" \
  "v_pk_lshrrev_b16 v227, 3, v226 op_sel_hi:[0,1]\n" \
  "v_pk_lshrrev_b16 v229, 3, v228 op_sel_hi:[0,1]\n" \
  "v_pk_lshrrev_b16 v231, 3, v230 op_sel_hi:[0,1]\n" \
  "v_pk_mad_u16 v224, v224, 8, v225 op_sel_hi:[1,0,1]\n" \
  "v_pk_mad_u16 v226, v226, 8, v227 op_sel_hi:[1,0,1]\n" \
  "v_pk_mad_u16 v228, v228, 8, v229 op_sel_hi:[1,0,1]\n" \
  "v_pk_mad_u16 v230, v230, 8, v231 op_sel_hi:[1,0,1]\n" \
  "v_alignbit_b32 v228, v228, v228, v179\n" \
  "v_alignbit_b32 v230, v230, v230, v179\n" \
  "ds_write2_b32 v195, v224, v226 offset0:0 offset1:4\n" \
  "ds_write2_b32 v195, v228, v230 offset0:2 offset1:6\n" \
  "s_mov_b32 s51, 4\n" \
  "s_call_b64 s[52:53], LA_loop\n" \
  "LA_ev3:\n" \
  "v_mov_b32 v199, v178\n" \
  "s_waitcnt vmcnt(4)\n" \
  "v_cvt_pk_u16_u32 v232, v232, v233\n" \
  "v_cvt_pk_u16_u32 v234, v234, v235\n" \
  "v_cvt_pk_u16_u32 v236, v236, v237\n" \
  "v_cvt_pk_u16_u32 v238, v238, v239\n" \
  "v_pk_lshrrev_b16 v233, 3, v232 op_sel_hi:[0,1]\n" \
  "v_pk_lshrrev_b16 v235, 3, v234 op_sel_hi:[0,1]\n" \
  "v_pk_lshrrev_b16 v237, 3, v236 op_sel_hi:[0,1]\n" \
  "v_pk_lshrrev_b16 v239, 3, v238 op_sel_hi:[0,1]\n" \
  "v_pk_mad_u16 v232, v232, 8, v233 op_sel_hi:[1,0,1]\n" \
  "v_pk_mad_u16 v234, v234, 8, v235 op_sel_hi:[1,0,1]\n" \
  "v_pk_mad_u16 v236, v236, 8, v237 op_sel_hi:[1,0,1]\n" \
  "v_pk_mad_u16 v238, v238, 8, v239 op_sel_hi:[1,0,1]\n" \
  "v_alignbit_b32 v236, v236, v236, v179\n" \
  "v_alignbit_b32 v238, v238, v238, v179\n" \
  "v_add_u32 v233, 0x400, v195\n" \
  "ds_write2_b32 v233, v232, v234 offset0:0 offset1:4\n" \
  "ds_write2_b32 v233, v236, v238 offset0:2 offset1:6\n" \
  "v_subrev_u32 v194, 0x800, v194\n" \
  "s_mov_b32 s51, 4\n" \
  "s_call_b64 s[52:53], LA_loop\n" \
  "LA_ev4:\n" \
  "v_mov_b32 v200, v178\n" \
  "s_waitcnt vmcnt(2)\n" \
  "v_cvt_pk_u16_u32 v240, v240, v241\n" \
  "v_cvt_pk_u16_u32 v242, v242, v243\n" \
  "v_cvt_pk_u16_u32 v244, v244, v245\n" \
  "v_cvt_pk_u16_u32 v246, v246, v247\n" \
  "v_pk_lshrrev_b16 v241, 3, v240 op_sel_hi:[0,1]\n" \
  "v_pk_lshrrev_b16 v243, 3, v242 op_sel_hi:[0,1]\n" \
  "v_pk_lshrrev_b16 v245, 3, v244 op_sel_hi:[0,1]\n" \
  "v_pk_lshrrev_b16 v247, 3, v246 op_sel_hi:[0,1]\n" \
  "v_pk_mad_u16 v240, v240, 8, v241 op_sel_hi:[1,0,1]\n" \
  "v_pk_mad_u16 v242, v242, 8, v243 op_sel_hi:[1,0,1]\n" \
  "v_pk_mad_u16 v244, v244, 8, v245 op_sel_hi:[1,0,1]\n" \
  "v_pk_mad_u16 v246, v246, 8, v247 op_sel_hi:[1,0,1]\n" \
  "v_alignbit_b32 v244, v244, v244, v179\n" \
  "v_alignbit_b32 v246, v246, v246, v179\n" \
  "ds_write2_b32 v195, v240, v242 offset0:0 offset1:4\n" \
  "ds_write2_b32 v195, v244, v246 offset0:2 offset1:6\n" \
  "s_mov_b32 s51, 4\n" \
  "s_call_b64 s[52:53], LA_loop\n" \
  "LA_ev5:\n" \
  "v_mov_b32 v201, v178\n" \
  "s_waitcnt vmcnt(0)\n" \
  "v_cvt_pk_u16_u32 v248, v248, v249\n" \
  "v_cvt_pk_u16_u32 v250, v250, v251\n" \
  "v_cvt_pk_u16_u32 v252, v252, v253\n" \
  "v_cvt_pk_u16_u32 v254, v254, v255\n" \
  "v_pk_lshrrev_b16 v249, 3, v248 op_sel_hi:[0,1]\n" \
  "v_pk_lshrrev_b16 v251, 3, v250 op_sel_hi:[0,1]\n" \
  "v_pk_lshrrev_b16 v253, 3, v252 op_sel_hi:[0,1]\n" \
  "v_pk_lshrrev_b16 v255, 3, v254 op_sel_hi:[0,1]\n" \
  "v_pk_mad_u16 v248, v248, 8, v249 op_sel_hi:[1,0,1]\n" \
  "v_pk_mad_u16 v250, v250, 8, v251 op_sel_hi:[1,0,1]\n" \
  "v_pk_mad_u16 v252, v252, 8, v253 op_sel_hi:[1,0,1]\n" \
  "v_pk_mad_u16 v254, v254, 8, v255 op_sel_hi:[1,0,1]\n" \
  "v_alignbit_b32 v252, v252, v252, v179\n" \
  "v_alignbit_b32 v254, v254, v254, v179\n" \
  "v_add_u32 v249, 0x400, v195\n" \
  "ds_write2_b32 v249, v248, v250 offset0:0 offset1:4\n" \
  "ds_write2_b32 v249, v252, v254 offset0:2 offset1:6\n" \
  "v_mov_b32 v208, 163328\n" \
  "ds_read_b32 v209, v208 offset:4\n" \
  "v_subrev_u32 v194, 0x800, v194\n" \
  "s_mov_b32 s51, 4\n" \
  "s_call_b64 s[52:53], LA_loop\n" \
  "LA_ev6:\n" \
  "v_mov_b32 v202, v178\n" \
  "v_readfirstlane_b32 s35, v209\n" \
  "s_cmp_ge_u32 s35, 128\n" \
  "s_cbranch_scc1 LA_pl\n" \
  "L2_:\n" \
  "ds_read_b32 v209, v208 offset:4\n" \
  "s_waitcnt lgkmcnt(0)\n" \
  "v_readfirstlane_b32 s35, v209\n" \
  "s_cmp_lt_u32 s35, 128\n" \
  "s_cbranch_scc1 L2_\n" \
  "LA_pl:\n" \
  "s_and_b32 s4, s35, 15\n" \
  "s_sub_u32 s4, s4, 4\n" \
  "s_bfe_u32 s5, s35, 0x30004\n" \
  "s_sub_u32 s5, s5, s29\n" \
  "s_cmp_le_i32 s4, 0\n" \
  "s_cbranch_scc1 LA_nt\n" \
  "s_mul_i32 s35, s5, 51200\n" \
  "s_ashr_i32 s36, s35, 31\n" \
  "s_add_u32 s46, s46, s35\n" \
  "s_addc_u32 s47, s47, s36\n" \
  "v_lshrrev_b32 v216, 4, v188\n" \
  "v_mul_u32_u24 v216, 400, v216\n" \
  "v_add_u32 v216, 6400, v216\n" \
  "v_bfe_u32 v218, v194, 4, 4\n" \
  "v_min_u32 v217, 8, v218\n" \
  "v_lshl_add_u32 v217, v217, 4, v216\n" \
  "v_lshl_add_u32 v216, v218, 4, v216\n" \
  "global_load_dwordx4 v[208:211], v216, s[46:47] offset:0\n" \
  "global_load_dwordx4 v[212:215], v217, s[46:47] offset:256\n" \
  "LA_nt:\n" \
  "s_lshl_b32 s5, s5, 7\n" \
  "s_sub_u32 s5, s5, s4\n" \
  "s_min_i32 s51, s4, 0\n" \
  "s_add_u32 s51, s51, 4\n" \
  "s_cmp_eq_u32 s51, 0\n" \
  "s_cbranch_scc1 LA_ev7\n" \
  "s_call_b64 s[52:53], LA_loop\n" \
  "LA_ev7:\n" \
  "v_mov_b32 v203, v178\n" \
  "s_cmp_le_i32 s4, 0\n" \
  "s_cbranch_scc1 LA_ev8\n" \
  "s_waitcnt vmcnt(0)\n" \
  "v_cvt_pk_u16_u32 v208, v208, v209\n" \
  "v_cvt_pk_u16_u32 v210, v210, v211\n" \
  "v_cvt_pk_u16_u32 v212, v212, v213\n" \
  "v_cvt_pk_u16_u32 v214, v214, v215\n" \
  "v_pk_lshrrev_b16 v209, 3, v208 op_sel_hi:[0,1]\n" \
  "v_pk_lshrrev_b16 v211, 3, v210 op_sel_hi:[0,1]\n" \
  "v_pk_lshrrev_b16 v213, 3, v212 op_sel_hi:[0,1]\n" \
  "v_pk_lshrrev_b16 v215, 3, v214 op_sel_hi:[0,1]\n" \
  "v_pk_mad_u16 v208, v208, 8, v209 op_sel_hi:[1,0,1]\n" \
  "v_pk_mad_u16 v210, v210, 8, v211 op_sel_hi:[1,0,1]\n" \
  "v_pk_mad_u16 v212, v212, 8, v213 op_sel_hi:[1,0,1]\n" \
  "v_pk_mad_u16 v214, v214, 8, v215 op_sel_hi:[1,0,1]\n" \
  "v_alignbit_b32 v212, v212, v212, v179\n" \
  "v_alignbit_b32 v214, v214, v214, v179\n" \
  "ds_write2_b32 v195, v208, v210 offset0:0 offset1:4\n" \
  "ds_write2_b32 v195, v212, v214 offset0:2 offset1:6\n" \
  "s_lshl_b32 s35, s4, 8\n" \
  "s_add_u32 s35, s35, 0x400\n" \
  "v_subrev_u32 v194, s35, v194\n" \
  "s_mov_b32 s51, s4\n" \
  "s_call_b64 s[52:53], LA_loop\n" \
  "LA_ev8:\n" \
  "v_mov_b32 v179, v178\n" \
  "ds_read_b128 v[100:103], v185 offset:64\n" \
  "ds_read2_b32 v[144:145], v186 offset0:0 offset1:2\n" \
  "ds_read2_b32 v[146:147], v186 offset0:4 offset1:6\n" \
  "v_mad_u32_u16 v187, v183, 16, v190\n" \
  "ds_read2_b32 v[148:149], v187 offset0:0 offset1:2\n" \
  "ds_read2_b32 v[150:151], v187 offset0:4 offset1:6\n" \
  "ds_read_b128 v[164:167], v191\n" \
  "s_lshr_b32 s35, s30, 2\n" \
  "s_lshl3_add_u32 s33, s29, s35\n" \
  "s_and_b32 s34, s30, 3\n" \
  "s_lshl_b32 s59, s27, 11\n" \
  "s_mul_i32 s35, s33, 1600000\n" \
  "s_add_u32 s54, s24, s35\n" \
  "s_addc_u32 s55, s25, 0\n" \
  "s_mul_i32 s35, s34, 819200\n" \
  "s_lshl1_add_u32 s35, s59, s35\n" \
  "s_add_u32 s35, s35, 102400000\n" \
  "s_add_u32 s56, s24, s35\n" \
  "s_addc_u32 s57, s25, 0\n" \
  "v_mbcnt_lo_u32_b32 v189, -1, 0\n" \
  "v_mbcnt_hi_u32_b32 v189, -1, v189\n" \
  "v_lshlrev_b32 v188, 4, v189\n" \
  "s_waitcnt lgkmcnt(6)\n" \
  "v_mfma_f32_16x16x32_bf16 v[152:155], v[112:115], v[112:115], 0\n" \
  "v_fmac_f32 v176, v156, v88\n" \
  "v_fmac_f32 v176, v157, v89\n" \
  "v_mfma_f32_16x16x32_bf16 v[152:155], v[116:119], v[116:119], v[152:155]\n" \
  "v_fmac_f32 v176, v158, v90\n" \
  "v_fmac_f32 v176, v159, v91\n" \
  "v_mfma_f32_16x16x32_bf16 v[156:159], v[128:131], v[128:131], 0\n" \
  "v_fmac_f32 v176, v160, v92\n" \
  "v_fmac_f32 v176, v161, v93\n" \
  "v_mfma_f32_16x16x32_bf16 v[156:159], v[132:135], v[132:135], v[156:159]\n" \
  "v_fmac_f32 v176, v162, v94\n" \
  "v_fma_f32 v177, v163, v95, v176\n" \
  "v_mfma_f32_16x16x32_bf16 v[160:163], v[128:131], v[136:139], 0\n" \
  "v_mul_f32 v176, v152, v0\n" \
  "v_fmac_f32 v176, v153, v1\n" \
  "v_mfma_f32_16x16x32_bf16 v[160:163], v[132:135], v[140:143], v[160:163]\n" \
  "v_fmac_f32 v176, v154, v2\n" \
  "v_fmac_f32 v176, v155, v3\n" \
  "v_mfma_f32_16x16x32_bf16 v[152:155], v[136:139], v[136:139], 0\n" \
  "v_fmac_f32 v176, v156, v4\n" \
  "v_fmac_f32 v176, v157, v5\n" \
  "v_mfma_f32_16x16x32_bf16 v[152:155], v[140:143], v[140:143], v[152:155]\n" \
  "v_fmac_f32 v176, v158, v6\n" \
  "v_fmac_f32 v176, v159, v7\n" \
  "s_waitcnt lgkmcnt(0)\n" \
  "s_setprio 3\n" \
  "s_barrier\n" \
  "global_load_dwordx4 v[208:211], v188, s[56:57] offset:0 sc1\n" \
  "global_load_dwordx4 v[212:215], v188, s[56:57] offset:1024 sc1\n" \
  "global_load_dwordx4 v[216:219], v188, s[56:57] offset:2048 sc1\n" \
  "global_load_dwordx4 v[220:223], v188, s[56:57] offset:3072 sc1\n" \
  "v_mfma_f32_16x16x32_bf16 v[156:159], v[112:115], v[136:139], 0\n" \
  "v_fmac_f32 v176, v160, v8\n" \
  "v_fmac_f32 v176, v161, v9\n" \
  "v_lshrrev_b32 v180, 1, v189\n" \
  "v_mfma_f32_16x16x32_bf16 v[156:159], v[116:119], v[140:143], v[156:159]\n" \
  "v_fmac_f32 v176, v162, v10\n" \
  "v_fmac_f32 v176, v163, v11\n" \
  "v_xor_b32 v180, v180, v189\n" \
  "v_mfma_f32_16x16x32_bf16 v[160:163], v[112:115], v[128:131], 0\n" \
  "v_fmac_f32 v176, v152, v12\n" \
  "v_fmac_f32 v176, v153, v13\n" \
  "v_add_f32_dpp v177, v177, v177 row_ror:8 row_mask:0xf bank_mask:0xf\n" \
  "v_and_b32 v180, 8, v180\n" \
  "v_mfma_f32_16x16x32_bf16 v[160:163], v[116:119], v[132:135], v[160:163]\n" \
  "v_fmac_f32 v176, v154, v14\n" \
  "v_fmac_f32 v176, v155, v15\n" \
  "v_bfe_u32 v181, v189, 2, 1\n" \
  "v_mfma_f32_16x16x32_bf16 v[152:155], v[112:115], v[120:123], 0\n" \
  "v_fmac_f32 v176, v156, v16\n" \
  "v_fmac_f32 v176, v157, v17\n" \
  "v_lshlrev_b32_e64 v181, v181, 5\n" \
  "v_mfma_f32_16x16x32_bf16 v[152:155], v[116:119], v[124:127], v[152:155]\n" \
  "v_fmac_f32 v176, v158, v18\n" \
  "v_fmac_f32 v176, v159, v19\n" \
  "v_mov_b32_dpp v178, v178 row_ror:4 row_mask:0xf bank_mask:0xf\n" \
  "v_lshlrev_b32 v180, v180, v181\n" \
  "v_mfma_f32_16x16x32_bf16 v[156:159], v[120:123], v[136:139], 0\n" \
  "v_fmac_f32 v176, v160, v20\n" \
  "v_fmac_f32 v176, v161, v21\n" \
  "v_bfe_u32 v181, v189, 2, 2\n" \
  "v_mfma_f32_16x16x32_bf16 v[156:159], v[124:127], v[140:143], v[156:159]\n" \
  "v_fmac_f32 v176, v162, v22\n" \
  "v_fmac_f32 v176, v163, v23\n" \
  "v_lshlrev_b32_e64 v181, v181, 1\n" \
  "v_mfma_f32_16x16x32_bf16 v[160:163], v[104:107], v[128:131], 0\n" \
  "v_fmac_f32 v176, v152, v24\n" \
  "v_fmac_f32 v176, v153, v25\n" \
  "v_cmp_gt_u32 vcc, 32, v189\n" \
  "v_mfma_f32_16x16x32_bf16 v[160:163], v[108:111], v[132:135], v[160:163]\n" \
  "v_fmac_f32 v176, v154, v26\n" \
  "v_fmac_f32 v176, v155, v27\n" \
  "v_mov_b32 v191, 0x3f80\n" \
  "v_mfma_f32_16x16x32_bf16 v[152:155], v[120:123], v[128:131], 0\n" \
  "v_fmac_f32 v176, v156, v28\n" \
  "v_fmac_f32 v176, v157, v29\n" \
  "v_add_u32 v190, 32000, v188\n" \
  "v_mfma_f32_16x16x32_bf16 v[152:155], v[124:127], v[132:135], v[152:155]\n" \
  "v_fmac_f32 v176, v158, v30\n" \
  "v_fmac_f32 v176, v159, v31\n" \
  "v_add_f32_dpp v178, v177, v177 row_ror:4 row_mask:0xf bank_mask:0x1\n" \
  "v_cndmask_b32 v192, v181, v180, vcc\n" \
  "v_mfma_f32_16x16x32_bf16 v[156:159], v[96:99], v[128:131], 0\n" \
  "v_fmac_f32 v176, v160, v32\n" \
  "v_fmac_f32 v176, v161, v33\n" \
  "v_bfe_i32 v180, v192, 0, 1\n" \
  "v_mfma_f32_16x16x32_bf16 v[156:159], v[100:103], v[132:135], v[156:159]\n" \
  "v_fmac_f32 v176, v162, v34\n" \
  "v_fmac_f32 v176, v163, v35\n" \
  "v_and_b32 v180, v180, v191\n" \
  "v_smfmac_f32_16x16x64_bf16 v[172:175], v[144:147], v[128:135], v193\n" \
  "v_fmac_f32 v176, v152, v36\n" \
  "v_fmac_f32 v176, v153, v37\n" \
  "v_bfe_i32 v181, v192, 1, 1\n" \
  "v_smfmac_f32_16x16x64_bf16 v[172:175], v[148:151], v[136:143], v193\n" \
  "v_fmac_f32 v176, v154, v38\n" \
  "v_fmac_f32 v176, v155, v39\n" \
  "v_and_b32 v181, v181, v191\n" \
  "v_mfma_f32_16x16x32_bf16 v[160:163], v[96:99], v[112:115], 0\n" \
  "v_fmac_f32 v176, v156, v40\n" \
  "v_fmac_f32 v176, v157, v41\n" \
  "v_bfe_i32 v182, v192, 2, 1\n" \
  "v_mfma_f32_16x16x32_bf16 v[160:163], v[100:103], v[116:119], v[160:163]\n" \
  "v_fmac_f32 v176, v158, v42\n" \
  "v_fmac_f32 v176, v159, v43\n" \
  "v_and_b32 v182, v182, v191\n" \
  "v_mfma_f32_16x16x32_bf16 v[152:155], v[104:107], v[112:115], 0\n" \
  "v_fmac_f32 v176, v172, v44\n" \
  "v_fmac_f32 v176, v173, v45\n" \
  "v_bfe_i32 v183, v192, 3, 1\n" \
  "v_mfma_f32_16x16x32_bf16 v[152:155], v[108:111], v[116:119], v[152:155]\n" \
  "v_fmac_f32 v176, v174, v46\n" \
  "v_fmac_f32 v176, v175, v47\n" \
  "v_and_b32 v183, v183, v191\n" \
  "v_mfma_f32_16x16x32_bf16 v[156:159], v[104:107], v[136:139], 0\n" \
  "v_fmac_f32 v176, v160, v48\n" \
  "v_fmac_f32 v176, v161, v49\n" \
  "v_and_b32 v192, 3, v189\n" \
  "v_mfma_f32_16x16x32_bf16 v[156:159], v[108:111], v[140:143], v[156:159]\n" \
  "v_fmac_f32 v176, v162, v50\n" \
  "v_fmac_f32 v176, v163, v51\n" \
  "v_mul_u32_u24 v192, 0x5555, v192\n" \
  "v_smfmac_f32_16x16x64_bf16 v[168:171], v[144:147], v[112:119], v193\n" \
  "v_fmac_f32 v176, v152, v52\n" \
  "v_fmac_f32 v176, v153, v53\n" \
  "v_add_u32 v189, 16000, v188\n" \
  "v_smfmac_f32_16x16x64_bf16 v[168:171], v[148:151], v[120:127], v193\n" \
  "v_fmac_f32 v176, v154, v54\n" \
  "v_fmac_f32 v176, v155, v55\n" \
  "v_add_u32 v191, 48000, v188\n" \
  "v_mfma_f32_16x16x32_bf16 v[160:163], v[96:99], v[136:139], 0\n" \
  "v_fmac_f32 v176, v156, v56\n" \
  "v_fmac_f32 v176, v157, v57\n" \
  "v_mfma_f32_16x16x32_bf16 v[160:163], v[100:103], v[140:143], v[160:163]\n" \
  "v_fmac_f32 v176, v158, v58\n" \
  "v_fmac_f32 v176, v159, v59\n" \
  "s_add_u32 s35, s59, 64000\n" \
  "s_add_u32 s54, s54, s35\n" \
  "s_addc_u32 s55, s55, 0\n" \
  "s_add_u32 s58, s59, 0x10000\n" \
  "s_mov_b32 m0, s58\n" \
  "s_add_u32 s35, s58, 0x8000\n" \
  "global_load_lds_dwordx4 v188, s[54:55] sc1\n" \
  "global_load_lds_dwordx4 v188, s[54:55] offset:1024 sc1\n" \
  "v_smfmac_f32_16x16x64_bf16 v[164:167], v[148:151], v[104:111], v193\n" \
  "v_fmac_f32 v176, v168, v60\n" \
  "v_fmac_f32 v176, v169, v61\n" \
  "v_smfmac_f32_16x16x64_bf16 v[164:167], v[144:147], v[96:103], v193\n" \
  "v_fmac_f32 v176, v170, v62\n" \
  "v_fmac_f32 v176, v171, v63\n" \
  "v_mfma_f32_16x16x32_bf16 v[152:155], v[120:123], v[120:123], 0\n" \
  "v_fmac_f32 v176, v160, v64\n" \
  "v_fmac_f32 v176, v161, v65\n" \
  "v_mfma_f32_16x16x32_bf16 v[152:155], v[124:127], v[124:127], v[152:155]\n" \
  "v_fmac_f32 v176, v162, v66\n" \
  "v_fmac_f32 v176, v163, v67\n" \
  "v_mfma_f32_16x16x32_bf16 v[156:159], v[104:107], v[120:123], 0\n" \
  "v_fmac_f32 v176, v164, v68\n" \
  "v_fmac_f32 v176, v165, v69\n" \
  "v_mfma_f32_16x16x32_bf16 v[156:159], v[108:111], v[124:127], v[156:159]\n" \
  "v_fmac_f32 v176, v166, v70\n" \
  "v_fmac_f32 v176, v167, v71\n" \
  "s_add_u32 m0, s58, 0x4000\n" \
  "s_cmp_lt_u32 s35, 0x28000\n" \
  "global_load_lds_dwordx4 v189, s[54:55] sc1\n" \
  "global_load_lds_dwordx4 v189, s[54:55] offset:1024 sc1\n" \
  "s_cselect_b32 s58, s35, s59\n" \
  "s_add_u32 s54, s54, 32000\n" \
  "s_addc_u32 s55, s55, 0\n" \
  "v_mfma_f32_16x16x32_bf16 v[160:163], v[96:99], v[120:123], 0\n" \
  "v_fmac_f32 v176, v152, v72\n" \
  "v_fmac_f32 v176, v153, v73\n" \
  "v_mfma_f32_16x16x32_bf16 v[160:163], v[100:103], v[124:127], v[160:163]\n" \
  "v_fmac_f32 v176, v154, v74\n" \
  "v_fmac_f32 v176, v155, v75\n" \
  "v_mfma_f32_16x16x32_bf16 v[152:155], v[96:99], v[104:107], 0\n" \
  "v_fmac_f32 v176, v156, v76\n" \
  "v_fmac_f32 v176, v157, v77\n" \
  "v_mfma_f32_16x16x32_bf16 v[152:155], v[100:103], v[108:111], v[152:155]\n" \
  "v_fmac_f32 v176, v158, v78\n" \
  "v_fmac_f32 v176, v159, v79\n" \
  "v_mfma_f32_16x16x32_bf16 v[156:159], v[104:107], v[104:107], 0\n" \
  "v_fmac_f32 v176, v160, v80\n" \
  "v_fmac_f32 v176, v161, v81\n" \
  "v_mfma_f32_16x16x32_bf16 v[156:159], v[108:111], v[108:111], v[156:159]\n" \
  "v_fmac_f32 v176, v162, v82\n" \
  "v_fmac_f32 v176, v163, v83\n" \
  "v_mfma_f32_16x16x32_bf16 v[160:163], v[148:151], v[144:147], 0\n" \
  "v_fmac_f32 v176, v152, v84\n" \
  "v_fmac_f32 v176, v153, v85\n" \
  "v_mfma_f32_16x16x32_bf16 v[160:163], v[96:99], v[96:99], v[160:163]\n" \
  "v_fmac_f32 v176, v154, v86\n" \
  "v_fmac_f32 v176, v155, v87\n" \
  "v_mfma_f32_16x16x32_bf16 v[160:163], v[100:103], v[100:103], v[160:163]\n" \
  "s_mov_b32 s51, 7\n" \
  "v_fmac_f32 v176, v156, v88\n" \
  "v_fmac_f32 v176, v157, v89\n" \
  "v_fmac_f32 v176, v158, v90\n" \
  "v_fmac_f32 v176, v159, v91\n" \
  "s_nop 2\n" \
  "v_fmac_f32 v176, v160, v92\n" \
  "v_fmac_f32 v176, v161, v93\n" \
  "v_fmac_f32 v176, v162, v94\n" \
  "v_fma_f32 v177, v163, v95, v176\n" \
  "s_nop 1\n" \
  "v_add_f32_dpp v177, v177, v177 row_ror:8 row_mask:0xf bank_mask:0xf\n" \
  "v_mov_b32_dpp v178, v178 row_ror:4 row_mask:0xf bank_mask:0xf\n" \
  "s_nop 1\n" \
  "v_add_f32_dpp v178, v177, v177 row_ror:4 row_mask:0xf bank_mask:0x1\n" \
  "v_mov_b32 v99, v179\n" \
  "s_nop 0\n" \
  "v_mov_b32_dpp v196, v178 row_ror:8 row_mask:0xf bank_mask:0xc\n" \
  "s_nop 1\n" \
  "v_permlane16_swap_b32 v196, v197\n" \
  "v_permlane16_swap_b32 v198, v199\n" \
  "v_permlane16_swap_b32 v200, v201\n" \
  "v_permlane16_swap_b32 v202, v203\n" \
  "v_permlane16_swap_b32 v179, v99\n" \
  "v_add_f32 v196, v196, v197\n" \
  "v_add_f32 v198, v198, v199\n" \
  "v_add_f32 v200, v200, v201\n" \
  "v_add_f32 v202, v202, v203\n" \
  "v_add_f32 v179, v179, v99\n" \
  "v_lshrrev_b32 v96, 4, v188\n" \
  "v_and_b32 v97, 1, v96\n" \
  "v_mov_b32 v99, v179\n" \
  "v_lshrrev_b32 v98, 4, v96\n" \
  "v_permlane32_swap_b32 v196, v198\n" \
  "v_permlane32_swap_b32 v200, v202\n" \
  "v_permlane32_swap_b32 v179, v99\n" \
  "v_add_f32 v196, v196, v198\n" \
  "v_add_f32 v200, v200, v202\n" \
  "v_add_f32 v179, v179, v99\n" \
  "v_cmp_eq_u32 vcc, 1, v97\n" \
  "v_lshlrev_b32 v97, 2, v97\n" \
  "v_add_lshl_u32 v97, v97, v98, 2\n" \
  "v_bfe_u32 v98, v96, 2, 2\n" \
  "v_add_f32_dpp v196, v196, v196 quad_perm:[1,0,3,2] row_mask:0xf bank_mask:0xf\n" \
  "v_add_f32_dpp v200, v200, v200 quad_perm:[1,0,3,2] row_mask:0xf bank_mask:0xf\n" \
  "v_add_f32_dpp v179, v179, v179 quad_perm:[1,0,3,2] row_mask:0xf bank_mask:0xf\n" \
  "v_sub_u32 v97, v97, v98\n" \
  "v_cndmask_b32 v196, v196, v200, vcc\n" \
  "v_add_u32 v97, 1, v97\n" \
  "s_min_i32 s35, s4, 0\n" \
  "s_add_u32 s36, s4, 32\n" \
  "s_add_u32 s37, s4, 29\n" \
  "v_add_f32_dpp v179, v179, v179 quad_perm:[2,3,0,1] row_mask:0xf bank_mask:0xf\n" \
  "v_add_f32_dpp v196, v196, v196 quad_perm:[2,3,0,1] row_mask:0xf bank_mask:0xf\n" \
  "s_mov_b32 exec_lo, 0x0\n" \
  "s_mov_b32 exec_hi, 0xaaaa0000\n" \
  "v_add_u32 v97, s35, v97\n" \
  "s_mov_b32 exec_lo, 0x5500\n" \
  "s_mov_b32 exec_hi, 0x0\n" \
  "v_add_u32 v97, s36, v97\n" \
  "s_mov_b32 exec_lo, 0x4444\n" \
  "s_mov_b32 exec_hi, 0x0\n" \
  "v_sub_u32 v97, s37, v98\n" \
  "v_mov_b32 v196, v179\n" \
  "s_mov_b64 exec, -1\n" \
  "v_cmp_lt_u32 vcc, 31, v97\n" \
  "v_add_u32 v98, s5, v97\n" \
  "s_lshl2_add_u32 s35, s7, s28\n" \
  "s_lshl_b32 s35, s35, 5\n" \
  "v_cndmask_b32 v97, v97, v98, vcc\n" \
  "v_add_lshl_u32 v97, v97, s35, 2\n" \
  "s_lshl_b32 s35, s31, 16\n" \
  "s_add_u32 s35, s35, 109871104\n" \
  "s_add_u32 s40, s24, s35\n" \
  "s_addc_u32 s41, s25, 0\n" \
  "v_min_u32 v196, -2, v196\n" \
  "v_lshlrev_b32 v184, 16, v204\n" \
  "v_and_b32 v185, 0xffff0000, v204\n" \
  "v_lshlrev_b32 v186, 16, v205\n" \
  "v_and_b32 v187, 0xffff0000, v205\n" \
  "s_barrier\n" \
  "s_add_u32 s56, s56, 32768\n" \
  "s_addc_u32 s57, s57, 0\n" \
  "global_load_dwordx4 v[224:227], v188, s[56:57] sc1\n" \
  "global_load_dwordx4 v[228:231], v188, s[56:57] offset:1024 sc1\n" \
  "global_load_dwordx4 v[232:235], v188, s[56:57] offset:2048 sc1\n" \
  "global_load_dwordx4 v[236:239], v188, s[56:57] offset:3072 sc1\n" \
  "s_add_u32 s56, s56, 32768\n" \
  "s_addc_u32 s57, s57, 0\n" \
  "s_mov_b32 exec_lo, 0x33337777\n" \
  "s_mov_b32 exec_hi, 0x33333333\n" \
  "global_store_dword v97, v196, s[40:41] sc1\n" \
  "s_mov_b64 exec, -1\n" \
  "s_waitcnt vmcnt(9)\n" \
  "s_setprio 0\n" \
  "s_branch LG_start\n" \
  "LA_loop:\n" \
  "v_mfma_f32_16x16x32_bf16 v[152:155], v[112:115], v[112:115], 0\n" \
  "s_waitcnt lgkmcnt(8)\n" \
  "ds_read_b128 v[100:103], v185 offset:64\n" \
  "v_fmac_f32 v176, v156, v88\n" \
  "v_fmac_f32 v176, v157, v89\n" \
  "v_mfma_f32_16x16x32_bf16 v[152:155], v[116:119], v[116:119], v[152:155]\n" \
  "ds_read2_b32 v[144:145], v186 offset0:0 offset1:2\n" \
  "v_fmac_f32 v176, v158, v90\n" \
  "v_fmac_f32 v176, v159, v91\n" \
  "v_mfma_f32_16x16x32_bf16 v[156:159], v[128:131], v[128:131], 0\n" \
  "ds_read2_b32 v[146:147], v186 offset0:4 offset1:6\n" \
  "v_fmac_f32 v176, v160, v92\n" \
  "v_fmac_f32 v176, v161, v93\n" \
  "v_mad_u32_u16 v187, v183, 16, v190\n" \
  "v_mfma_f32_16x16x32_bf16 v[156:159], v[132:135], v[132:135], v[156:159]\n" \
  "s_waitcnt lgkmcnt(10)\n" \
  "ds_read2_b32 v[148:149], v187 offset0:0 offset1:2\n" \
  "v_fmac_f32 v176, v162, v94\n" \
  "v_fma_f32 v177, v163, v95, v176\n" \
  "v_mfma_f32_16x16x32_bf16 v[160:163], v[128:131], v[136:139], 0\n" \
  "s_waitcnt lgkmcnt(10)\n" \
  "ds_read2_b32 v[150:151], v187 offset0:4 offset1:6\n" \
  "v_mul_f32 v176, v152, v0\n" \
  "v_fmac_f32 v176, v153, v1\n" \
  "v_mfma_f32_16x16x32_bf16 v[160:163], v[132:135], v[140:143], v[160:163]\n" \
  "ds_read_b128 v[180:183], v194\n" \
  "v_fmac_f32 v176, v154, v2\n" \
  "v_fmac_f32 v176, v155, v3\n" \
  "v_mfma_f32_16x16x32_bf16 v[152:155], v[136:139], v[136:139], 0\n" \
  "v_fmac_f32 v176, v156, v4\n" \
  "v_fmac_f32 v176, v157, v5\n" \
  "v_mfma_f32_16x16x32_bf16 v[152:155], v[140:143], v[140:143], v[152:155]\n" \
  "ds_read_b128 v[164:167], v191\n" \
  "v_fmac_f32 v176, v158, v6\n" \
  "v_fmac_f32 v176, v159, v7\n" \
  "v_mfma_f32_16x16x32_bf16 v[156:159], v[112:115], v[136:139], 0\n" \
  "v_fmac_f32 v176, v160, v8\n" \
  "v_fmac_f32 v176, v161, v9\n" \
  "v_mfma_f32_16x16x32_bf16 v[156:159], v[116:119], v[140:143], v[156:159]\n" \
  "v_fmac_f32 v176, v162, v10\n" \
  "v_fmac_f32 v176, v163, v11\n" \
  "v_mfma_f32_16x16x32_bf16 v[160:163], v[112:115], v[128:131], 0\n" \
  "v_fmac_f32 v176, v152, v12\n" \
  "v_fmac_f32 v176, v153, v13\n" \
  "v_add_f32_dpp v177, v177, v177 row_ror:8 row_mask:0xf bank_mask:0xf\n" \
  "v_mfma_f32_16x16x32_bf16 v[160:163], v[116:119], v[132:135], v[160:163]\n" \
  "s_waitcnt lgkmcnt(11)\n" \
  "v_fmac_f32 v176, v154, v14\n" \
  "v_fmac_f32 v176, v155, v15\n" \
  "v_mfma_f32_16x16x32_bf16 v[152:155], v[112:115], v[120:123], 0\n" \
  "s_waitcnt lgkmcnt(10)\n" \
  "v_fmac_f32 v176, v156, v16\n" \
  "v_fmac_f32 v176, v157, v17\n" \
  "v_mfma_f32_16x16x32_bf16 v[152:155], v[116:119], v[124:127], v[152:155]\n" \
  "v_fmac_f32 v176, v158, v18\n" \
  "v_fmac_f32 v176, v159, v19\n" \
  "v_mov_b32_dpp v178, v178 row_ror:4 row_mask:0xf bank_mask:0xf\n" \
  "v_mfma_f32_16x16x32_bf16 v[156:159], v[120:123], v[136:139], 0\n" \
  "v_fmac_f32 v176, v160, v20\n" \
  "v_fmac_f32 v176, v161, v21\n" \
  "v_mfma_f32_16x16x32_bf16 v[156:159], v[124:127], v[140:143], v[156:159]\n" \
  "s_waitcnt lgkmcnt(9)\n" \
  "v_fmac_f32 v176, v162, v22\n" \
  "v_fmac_f32 v176, v163, v23\n" \
  "v_mfma_f32_16x16x32_bf16 v[160:163], v[104:107], v[128:131], 0\n" \
  "s_waitcnt lgkmcnt(1)\n" \
  "v_fmac_f32 v176, v152, v24\n" \
  "v_fmac_f32 v176, v153, v25\n" \
  "v_mad_u32_u16 v184, v182, 16, v188\n" \
  "v_mfma_f32_16x16x32_bf16 v[160:163], v[108:111], v[132:135], v[160:163]\n" \
  "v_fmac_f32 v176, v154, v26\n" \
  "v_fmac_f32 v176, v155, v27\n" \
  "v_mfma_f32_16x16x32_bf16 v[152:155], v[120:123], v[128:131], 0\n" \
  "v_fmac_f32 v176, v156, v28\n" \
  "v_fmac_f32 v176, v157, v29\n" \
  "v_mfma_f32_16x16x32_bf16 v[152:155], v[124:127], v[132:135], v[152:155]\n" \
  "v_fmac_f32 v176, v158, v30\n" \
  "v_fmac_f32 v176, v159, v31\n" \
  "v_add_f32_dpp v178, v177, v177 row_ror:4 row_mask:0xf bank_mask:0x1\n" \
  "v_mfma_f32_16x16x32_bf16 v[156:159], v[96:99], v[128:131], 0\n" \
  "v_fmac_f32 v176, v160, v32\n" \
  "v_fmac_f32 v176, v161, v33\n" \
  "v_mfma_f32_16x16x32_bf16 v[156:159], v[100:103], v[132:135], v[156:159]\n" \
  "v_fmac_f32 v176, v162, v34\n" \
  "v_fmac_f32 v176, v163, v35\n" \
  "v_smfmac_f32_16x16x64_bf16 v[172:175], v[144:147], v[128:135], v193\n" \
  "ds_read_b128 v[128:131], v184 offset:0\n" \
  "v_fmac_f32 v176, v152, v36\n" \
  "v_fmac_f32 v176, v153, v37\n" \
  "v_smfmac_f32_16x16x64_bf16 v[172:175], v[148:151], v[136:143], v193\n" \
  "ds_read_b128 v[132:135], v184 offset:64\n" \
  "v_fmac_f32 v176, v154, v38\n" \
  "v_fmac_f32 v176, v155, v39\n" \
  "v_mfma_f32_16x16x32_bf16 v[160:163], v[96:99], v[112:115], 0\n" \
  "v_fmac_f32 v176, v156, v40\n" \
  "v_fmac_f32 v176, v157, v41\n" \
  "v_mfma_f32_16x16x32_bf16 v[160:163], v[100:103], v[116:119], v[160:163]\n" \
  "v_fmac_f32 v176, v158, v42\n" \
  "v_fmac_f32 v176, v159, v43\n" \
  "v_add_u32 v194, 0x100, v194\n" \
  "v_mfma_f32_16x16x32_bf16 v[152:155], v[104:107], v[112:115], 0\n" \
  "v_fmac_f32 v176, v172, v44\n" \
  "v_fmac_f32 v176, v173, v45\n" \
  "v_mfma_f32_16x16x32_bf16 v[152:155], v[108:111], v[116:119], v[152:155]\n" \
  "v_fmac_f32 v176, v174, v46\n" \
  "v_fmac_f32 v176, v175, v47\n" \
  "v_mfma_f32_16x16x32_bf16 v[156:159], v[104:107], v[136:139], 0\n" \
  "ds_read_b128 v[172:175], v191\n" \
  "v_fmac_f32 v176, v160, v48\n" \
  "v_fmac_f32 v176, v161, v49\n" \
  "v_mad_u32_u16 v185, v181, 16, v188\n" \
  "v_mfma_f32_16x16x32_bf16 v[156:159], v[108:111], v[140:143], v[156:159]\n" \
  "v_fmac_f32 v176, v162, v50\n" \
  "v_fmac_f32 v176, v163, v51\n" \
  "v_mad_u32_u16 v186, v182, 16, v188 op_sel:[1,0,0,0]\n" \
  "v_smfmac_f32_16x16x64_bf16 v[168:171], v[144:147], v[112:119], v193\n" \
  "ds_read_b128 v[112:115], v185 offset:0\n" \
  "v_fmac_f32 v176, v152, v52\n" \
  "v_fmac_f32 v176, v153, v53\n" \
  "v_smfmac_f32_16x16x64_bf16 v[168:171], v[148:151], v[120:127], v193\n" \
  "ds_read_b128 v[116:119], v185 offset:64\n" \
  "v_fmac_f32 v176, v154, v54\n" \
  "v_fmac_f32 v176, v155, v55\n" \
  "v_mfma_f32_16x16x32_bf16 v[160:163], v[96:99], v[136:139], 0\n" \
  "v_fmac_f32 v176, v156, v56\n" \
  "v_fmac_f32 v176, v157, v57\n" \
  "v_mfma_f32_16x16x32_bf16 v[160:163], v[100:103], v[140:143], v[160:163]\n" \
  "s_waitcnt lgkmcnt(5)\n" \
  "v_fmac_f32 v176, v158, v58\n" \
  "v_fmac_f32 v176, v159, v59\n" \
  "v_smfmac_f32_16x16x64_bf16 v[164:167], v[148:151], v[104:111], v193\n" \
  "ds_read_b128 v[136:139], v186 offset:0\n" \
  "v_fmac_f32 v176, v168, v60\n" \
  "v_fmac_f32 v176, v169, v61\n" \
  "v_smfmac_f32_16x16x64_bf16 v[164:167], v[144:147], v[96:103], v193\n" \
  "ds_read_b128 v[140:143], v186 offset:64\n" \
  "v_fmac_f32 v176, v170, v62\n" \
  "v_fmac_f32 v176, v171, v63\n" \
  "v_mfma_f32_16x16x32_bf16 v[152:155], v[120:123], v[120:123], 0\n" \
  "v_fmac_f32 v176, v160, v64\n" \
  "v_fmac_f32 v176, v161, v65\n" \
  "v_mfma_f32_16x16x32_bf16 v[152:155], v[124:127], v[124:127], v[152:155]\n" \
  "ds_read_b128 v[168:171], v191\n" \
  "v_fmac_f32 v176, v162, v66\n" \
  "v_fmac_f32 v176, v163, v67\n" \
  "v_mad_u32_u16 v187, v181, 16, v188 op_sel:[1,0,0,0]\n" \
  "v_mfma_f32_16x16x32_bf16 v[156:159], v[104:107], v[120:123], 0\n" \
  "v_fmac_f32 v176, v164, v68\n" \
  "v_fmac_f32 v176, v165, v69\n" \
  "v_mad_u32_u16 v184, v180, 16, v188 op_sel:[1,0,0,0]\n" \
  "v_mfma_f32_16x16x32_bf16 v[156:159], v[108:111], v[124:127], v[156:159]\n" \
  "v_fmac_f32 v176, v166, v70\n" \
  "v_fmac_f32 v176, v167, v71\n" \
  "v_mfma_f32_16x16x32_bf16 v[160:163], v[96:99], v[120:123], 0\n" \
  "ds_read_b128 v[120:123], v187 offset:0\n" \
  "v_fmac_f32 v176, v152, v72\n" \
  "v_fmac_f32 v176, v153, v73\n" \
  "v_mfma_f32_16x16x32_bf16 v[160:163], v[100:103], v[124:127], v[160:163]\n" \
  "v_fmac_f32 v176, v154, v74\n" \
  "v_fmac_f32 v176, v155, v75\n" \
  "v_mad_u32_u16 v185, v180, 16, v188\n" \
  "v_mfma_f32_16x16x32_bf16 v[152:155], v[96:99], v[104:107], 0\n" \
  "ds_read_b128 v[124:127], v187 offset:64\n" \
  "v_fmac_f32 v176, v156, v76\n" \
  "v_fmac_f32 v176, v157, v77\n" \
  "v_mfma_f32_16x16x32_bf16 v[152:155], v[100:103], v[108:111], v[152:155]\n" \
  "v_fmac_f32 v176, v158, v78\n" \
  "v_fmac_f32 v176, v159, v79\n" \
  "v_mfma_f32_16x16x32_bf16 v[156:159], v[104:107], v[104:107], 0\n" \
  "ds_read_b128 v[104:107], v184 offset:0\n" \
  "v_fmac_f32 v176, v160, v80\n" \
  "v_fmac_f32 v176, v161, v81\n" \
  "v_mfma_f32_16x16x32_bf16 v[156:159], v[108:111], v[108:111], v[156:159]\n" \
  "s_sub_u32 s51, s51, 1\n" \
  "v_fmac_f32 v176, v162, v82\n" \
  "v_fmac_f32 v176, v163, v83\n" \
  "v_mad_u32_u16 v186, v183, 16, v189\n" \
  "v_mfma_f32_16x16x32_bf16 v[160:163], v[148:151], v[144:147], 0\n" \
  "s_cmp_lg_u32 s51, 0\n" \
  "ds_read_b128 v[108:111], v184 offset:64\n" \
  "v_fmac_f32 v176, v152, v84\n" \
  "v_fmac_f32 v176, v153, v85\n" \
  "v_mfma_f32_16x16x32_bf16 v[160:163], v[96:99], v[96:99], v[160:163]\n" \
  "s_waitcnt lgkmcnt(8)\n" \
  "ds_read_b128 v[96:99], v185 offset:0\n" \
  "v_fmac_f32 v176, v154, v86\n" \
  "v_fmac_f32 v176, v155, v87\n" \
  "v_mfma_f32_16x16x32_bf16 v[160:163], v[100:103], v[100:103], v[160:163]\n" \
  "s_cbranch_scc1 LA_loop\n" \
  "s_setpc_b64 s[52:53]\n" \
  "LB_start:\n" \
  "s_setprio 3\n" \
  "s_mov_b32 s37, 12\n" \
  "L3_:\n" \
  "s_sub_u32 s35, s36, 125\n" \
  "s_min_u32 s35, s35, s36\n" \
  "s_mul_i32 m0, s35, 1040\n" \
  "s_lshl_b32 s35, s35, 11\n" \
  "s_add_u32 s42, s40, s35\n" \
  "s_addc_u32 s43, s41, 0\n" \
  "global_load_lds_dwordx4 v254, s[42:43]\n" \
  "s_add_u32 s36, s36, 4\n" \
  "s_sub_u32 s37, s37, 1\n" \
  "s_cmp_lg_u32 s37, 0\n" \
  "s_cbranch_scc1 L3_\n" \
  "v_mov_b32 v190, 163328\n" \
  "v_mov_b64 v[2:3], 0\n" \
  "ds_write_b64 v190, v[2:3]\n" \
  "ds_write_b64 v190, v[2:3] offset:8\n" \
  "ds_write_b64 v190, v[2:3] offset:16\n" \
  "s_waitcnt lgkmcnt(0)\n" \
  "s_barrier\n" \
  "v_lshlrev_b32 v247, 7, v255\n" \
  "s_mov_b32 s37, 20\n" \
  "L4_:\n" \
  "s_sub_u32 s35, s36, 125\n" \
  "s_min_u32 s35, s35, s36\n" \
  "s_mul_i32 m0, s35, 1040\n" \
  "s_lshl_b32 s35, s35, 11\n" \
  "s_add_u32 s42, s40, s35\n" \
  "s_addc_u32 s43, s41, 0\n" \
  "global_load_lds_dwordx4 v254, s[42:43]\n" \
  "s_add_u32 s36, s36, 4\n" \
  "s_sub_u32 s37, s37, 1\n" \
  "s_cmp_lg_u32 s37, 0\n" \
  "s_cbranch_scc1 L4_\n" \
  "s_add_u32 s48, s24, 110002176\n" \
  "s_addc_u32 s49, s25, 0\n" \
  "s_lshl_b32 s35, s26, 2\n" \
  "v_mov_b32 v193, s35\n" \
  "global_load_dword v193, v193, s[48:49] offset:1024 sc1\n" \
  "v_mov_b32 v191, 1\n" \
  "v_and_b32 v252, 15, v255\n" \
  "v_lshrrev_b32 v253, 4, v255\n" \
  "s_lshl2_add_u32 s35, s31, s28\n" \
  "s_lshl_b32 s35, s35, 1\n" \
  "v_bfe_u32 v1, v255, 2, 1\n" \
  "v_add_u32 v1, s35, v1\n" \
  "v_and_b32 v2, 3, v255\n" \
  "v_lshlrev_b32 v3, 5, v1\n" \
  "v_lshl_add_u32 v3, v2, 3, v3\n" \
  "v_lshlrev_b32 v4, 2, v2\n" \
  "v_add_u32 v4, 380, v4\n" \
  "v_cmp_gt_u32 vcc, 12, v1\n" \
  "v_lshrrev_b32 v5, 3, v255\n" \
  "v_mul_u32_u24 v5, 6400, v5\n" \
  "v_cndmask_b32 v3, v4, v3, vcc\n" \
  "v_add_u32 v5, 3200, v5\n" \
  "v_add_u32 v227, v5, v3\n" \
  "v_lshl_add_u32 v3, v1, 2, v2\n" \
  "v_add_u32 v4, 48, v3\n" \
  "v_mul_u32_u24 v5, 205, v3\n" \
  "v_lshrrev_b32 v5, 11, v5\n" \
  "v_mul_u32_u24 v5, 10, v5\n" \
  "v_sub_u32 v5, v3, v5\n" \
  "v_lshlrev_b32 v236, 26, v5\n" \
  "v_mul_u32_u24 v5, 205, v4\n" \
  "v_lshrrev_b32 v5, 11, v5\n" \
  "v_mul_u32_u24 v5, 10, v5\n" \
  "v_sub_u32 v5, v4, v5\n" \
  "v_lshlrev_b32 v237, 26, v5\n" \
  "s_lshl2_add_u32 s4, s31, s28\n" \
  "s_lshl_b32 s4, s4, 7\n" \
  "s_add_u32 s36, s4, 96\n" \
  "s_min_u32 s36, s36, 984\n" \
  "s_add_u32 s37, s4, 112\n" \
  "s_min_u32 s37, s37, 984\n" \
  "v_add_u32 v186, s4, v252\n" \
  "v_add_u32 v187, s36, v252\n" \
  "v_add_u32 v188, s37, v252\n" \
  "s_mul_i32 s38, s7, 51200\n" \
  "s_add_u32 s42, s8, s38\n" \
  "s_addc_u32 s43, s9, 0\n" \
  "s_xor_b32 s38, s31, 1\n" \
  "s_lshl_b32 s38, s38, 7\n" \
  "s_add_u32 s94, s10, s38\n" \
  "s_addc_u32 s95, s11, 0\n" \
  "s_mul_i32 s5, s28, 4224\n" \
  "s_add_u32 s5, s5, 146432\n" \
  "v_lshrrev_b32 v2, 3, v186\n" \
  "v_lshl_add_u32 v2, v186, 3, v2\n" \
  "v_add_lshl_u32 v249, v2, v253, 4\n" \
  "v_lshrrev_b32 v2, 3, v187\n" \
  "v_lshl_add_u32 v2, v187, 3, v2\n" \
  "v_add_lshl_u32 v250, v2, v253, 4\n" \
  "v_lshrrev_b32 v2, 3, v188\n" \
  "v_lshl_add_u32 v2, v188, 3, v2\n" \
  "v_add_lshl_u32 v251, v2, v253, 4\n" \
  "v_mul_u32_u24 v1, 1600000, v253\n" \
  "v_lshl_add_u32 v186, v186, 4, v1\n" \
  "v_lshl_add_u32 v187, v187, 4, v1\n" \
  "v_lshl_add_u32 v188, v188, 4, v1\n" \
  "v_lshlrev_b32 v1, 4, v253\n" \
  "v_xor_b32 v244, v1, v254\n" \
  "v_lshl_add_u32 v245, v255, 4, s5\n" \
  "v_lshrrev_b32 v1, 1, v252\n" \
  "v_xor_b32 v1, v1, v253\n" \
  "v_lshlrev_b32 v1, 4, v1\n" \
  "v_lshl_add_u32 v1, v252, 7, v1\n" \
  "v_add_u32 v246, s5, v1\n" \
  "v_xor_b32 v1, 64, v1\n" \
  "v_add_u32 v242, s5, v1\n" \
  "v_lshlrev_b32 v1, 4, v255\n" \
  "v_xor_b32 v1, 64, v1\n" \
  "v_add_u32 v243, s5, v1\n" \
  "v_lshrrev_b32 v1, 2, v255\n" \
  "s_lshl_b32 s35, s28, 5\n" \
  "s_lshl_b32 s36, s31, 6\n" \
  "s_add_u32 s35, s35, s36\n" \
  "s_and_b32 s35, s35, 127\n" \
  "v_add_u32 v1, s35, v1\n" \
  "v_lshrrev_b32 v2, 4, v255\n" \
  "v_and_b32 v2, 2, v2\n" \
  "v_xor_b32 v2, v2, v255\n" \
  "v_and_b32 v2, 3, v2\n" \
  "v_lshlrev_b32 v2, 4, v2\n" \
  "v_lshl_add_u32 v189, v1, 10, v2\n" \
  "v_add_u32 v194, 15360, v189\n" \
  "v_lshrrev_b32 v1, 2, v252\n" \
  "v_lshlrev_b32 v1, 6, v1\n" \
  "v_lshl_add_u32 v1, v253, 9, v1\n" \
  "v_and_b32 v2, 3, v252\n" \
  "v_lshl_add_u32 v1, v2, 3, v1\n" \
  "v_and_b32 v2, 1, v253\n" \
  "v_lshl_add_u32 v1, v2, 5, v1\n" \
  "v_add_u32 v184, 138240, v1\n" \
  "v_xor_b32 v1, 32, v1\n" \
  "v_add_u32 v185, 138240, v1\n" \
  "s_bfe_u32 s35, s30, 0x10001\n" \
  "s_lshl1_add_u32 s35, s29, s35\n" \
  "s_lshr_b32 s6, s30, 2\n" \
  "s_lshl_b32 s6, s6, 18\n" \
  "s_lshl_b32 s38, s35, 6\n" \
  "s_add_u32 s92, s14, s38\n" \
  "s_addc_u32 s93, s15, 0\n" \
  "s_lshr_b32 s37, s30, 3\n" \
  "s_add_u32 s37, s37, 96\n" \
  "s_lshl_b32 s34, s37, 17\n" \
  "s_mul_i32 s35, s35, 400\n" \
  "s_add_u32 s37, s37, s35\n" \
  "s_mul_i32 s37, s37, 16000\n" \
  "s_add_u32 s46, s24, s37\n" \
  "s_addc_u32 s47, s25, 0\n" \
  "s_lshr_b32 s36, s30, 2\n" \
  "s_add_u32 s36, s36, s35\n" \
  "s_mul_i32 s36, s36, 16000\n" \
  "s_add_u32 s44, s24, s36\n" \
  "s_addc_u32 s45, s25, 0\n" \
  "s_lshl_b32 s96, s28, 11\n" \
  "s_add_u32 s96, s96, 138240\n" \
  "v_lshlrev_b32 v238, 4, v255\n" \
  "v_mov_b32 v234, 0\n" \
  "s_lshl2_add_u32 s33, s26, s28\n" \
  "s_mov_b32 s52, 0x10002\n" \
  "s_mov_b32 s53, 0xc0c0400\n" \
  "s_mov_b32 s54, 0x4000c0c\n" \
  "s_mov_b32 s55, 0x3030303\n" \
  "s_mov_b32 s56, 0x55555555\n" \
  "s_mov_b32 s38, 0x0\n" \
  "s_mov_b32 s98, 0x0\n" \
  "s_mov_b32 s99, 0x8\n" \
  "s_mov_b32 s97, 0x2000\n" \
  "v_xor_b32 v245, 64, v244\n" \
  "v_add_u32 v245, 0x400, v245\n" \
  "s_waitcnt vmcnt(0)\n" \
  "s_mov_b64 exec, 1\n" \
  "ds_add_u32 v190, v191 offset:12\n" \
  "s_mov_b64 exec, -1\n" \
  "s_memtime s[88:89]\n" \
  "s_setprio 0\n" \
  "L5_:\n" \
  "ds_read_b32 v192, v190 offset:16\n" \
  "s_waitcnt lgkmcnt(0)\n" \
  "v_readfirstlane_b32 s35, v192\n" \
  "s_cmp_lt_u32 s35, 4\n" \
  "s_cbranch_scc1 L5_\n" \
  "s_add_u32 s35, s4, 0\n" \
  "s_min_u32 s35, s35, 984\n" \
  "s_lshl_b32 s35, s35, 8\n" \
  "s_add_u32 m0, s5, 0\n" \
  "s_add_u32 s58, s94, s35\n" \
  "s_addc_u32 s59, s95, 0\n" \
  "global_load_lds_dwordx4 v244, s[58:59]\n" \
  "global_load_lds_dwordx4 v245, s[58:59] offset:1024\n" \
  "s_add_u32 s35, s4, 16\n" \
  "s_min_u32 s35, s35, 984\n" \
  "s_lshl_b32 s35, s35, 8\n" \
  "s_add_u32 m0, s5, 2048\n" \
  "s_add_u32 s58, s94, s35\n" \
  "s_addc_u32 s59, s95, 0\n" \
  "global_load_lds_dwordx4 v244, s[58:59]\n" \
  "global_load_lds_dwordx4 v245, s[58:59] offset:1024\n" \
  "s_add_u32 s40, s92, s6\n" \
  "s_addc_u32 s41, s93, 0\n" \
  "s_add_u32 s6, s6, 2097152\n" \
  "s_mov_b32 m0, s96\n" \
  "s_nop 0\n" \
  "global_load_lds_dwordx4 v189, s[40:41]\n" \
  "global_load_lds_dwordx4 v194, s[40:41] offset:1024\n" \
  "L6_:\n" \
  "ds_read_b32 v192, v190 offset:12\n" \
  "s_waitcnt lgkmcnt(0)\n" \
  "v_readfirstlane_b32 s35, v192\n" \
  "s_cmp_lt_u32 s35, 4\n" \
  "s_cbranch_scc1 L6_\n" \
  "s_waitcnt vmcnt(2)\n" \
  "ds_read_b128 v[8:11], v246 offset:0\n" \
  "ds_read_b128 v[12:15], v242 offset:0\n" \
  "ds_read_b128 v[24:27], v246 offset:2048\n" \
  "ds_read_b128 v[28:31], v242 offset:2048\n" \
  "ds_read_b128 v[0:3], v249 offset:0\n" \
  "ds_read_b128 v[4:7], v249 offset:64\n" \
  "ds_read_b128 v[16:19], v249 offset:2080\n" \
  "ds_read_b128 v[20:23], v249 offset:2144\n" \
  "s_waitcnt lgkmcnt(4)\n" \
  "global_load_dwordx2 v[196:197], v227, s[42:43] offset:-3200\n" \
  "global_load_dwordx2 v[198:199], v227, s[42:43] offset:-2800\n" \
  "global_load_dwordx2 v[200:201], v227, s[42:43] offset:-2400\n" \
  "global_load_dwordx2 v[202:203], v227, s[42:43] offset:-1600\n" \
  "global_load_dwordx2 v[204:205], v227, s[42:43] offset:-1200\n" \
  "global_load_dwordx2 v[206:207], v227, s[42:43] offset:-800\n" \
  "global_load_dwordx2 v[208:209], v227, s[42:43] offset:-400\n" \
  "global_load_dwordx2 v[210:211], v227, s[42:43] offset:0\n" \
  "global_load_dwordx2 v[212:213], v227, s[42:43] offset:400\n" \
  "global_load_dwordx2 v[214:215], v227, s[42:43] offset:800\n" \
  "global_load_dwordx2 v[216:217], v227, s[42:43] offset:1200\n" \
  "global_load_dwordx2 v[218:219], v227, s[42:43] offset:-2000\n" \
  "global_load_dwordx2 v[220:221], v227, s[42:43] offset:1600\n" \
  "global_load_dwordx2 v[222:223], v227, s[42:43] offset:2000\n" \
  "global_load_dwordx2 v[224:225], v227, s[42:43] offset:2400\n" \
  "global_load_dwordx2 v[226:227], v227, s[42:43] offset:2800\n" \
  "s_add_u32 s35, s4, 32\n" \
  "s_min_u32 s35, s35, 984\n" \
  "s_lshl_b32 s35, s35, 8\n" \
  "s_add_u32 m0, s5, 0\n" \
  "s_add_u32 s58, s94, s35\n" \
  "s_addc_u32 s59, s95, 0\n" \
  "global_load_lds_dwordx4 v244, s[58:59]\n" \
  "global_load_lds_dwordx4 v245, s[58:59] offset:1024\n" \
  "s_add_u32 s35, s4, 48\n" \
  "s_min_u32 s35, s35, 984\n" \
  "s_lshl_b32 s35, s35, 8\n" \
  "s_add_u32 m0, s5, 2048\n" \
  "s_add_u32 s58, s94, s35\n" \
  "s_addc_u32 s59, s95, 0\n" \
  "global_load_lds_dwordx4 v244, s[58:59]\n" \
  "global_load_lds_dwordx4 v245, s[58:59] offset:1024\n" \
  "s_waitcnt vmcnt(0)\n" \
  "ds_read_b128 v[40:43], v246 offset:0\n" \
  "ds_read_b128 v[44:47], v242 offset:0\n" \
  "ds_read_b128 v[56:59], v246 offset:2048\n" \
  "ds_read_b128 v[60:63], v242 offset:2048\n" \
  "ds_read_b128 v[32:35], v249 offset:4160\n" \
  "ds_read_b128 v[36:39], v249 offset:4224\n" \
  "ds_read_b128 v[48:51], v249 offset:6240\n" \
  "ds_read_b128 v[52:55], v249 offset:6304\n" \
  "s_waitcnt lgkmcnt(4)\n" \
  "s_lshl2_add_u32 s35, s26, s28\n" \
  "s_mul_i32 s35, s35, 6400\n" \
  "s_add_u32 s35, s35, 0\n" \
  "s_add_u32 s58, s14, s35\n" \
  "s_addc_u32 s59, s15, 0\n" \
  "s_mov_b32 exec_lo, 0xffffffff\n" \
  "s_mov_b32 exec_hi, 0x3ffff\n" \
  "global_load_dword v248, v247, s[58:59]\n" \
  "s_mov_b64 exec, -1\n" \
  "s_add_u32 s35, s4, 64\n" \
  "s_min_u32 s35, s35, 984\n" \
  "s_lshl_b32 s35, s35, 8\n" \
  "s_add_u32 m0, s5, 0\n" \
  "s_add_u32 s58, s94, s35\n" \
  "s_addc_u32 s59, s95, 0\n" \
  "global_load_lds_dwordx4 v244, s[58:59]\n" \
  "global_load_lds_dwordx4 v245, s[58:59] offset:1024\n" \
  "s_add_u32 s35, s4, 80\n" \
  "s_min_u32 s35, s35, 984\n" \
  "s_lshl_b32 s35, s35, 8\n" \
  "s_add_u32 m0, s5, 2048\n" \
  "s_add_u32 s58, s94, s35\n" \
  "s_addc_u32 s59, s95, 0\n" \
  "global_load_lds_dwordx4 v244, s[58:59]\n" \
  "global_load_lds_dwordx4 v245, s[58:59] offset:1024\n" \
  "s_waitcnt vmcnt(0)\n" \
  "ds_read_b128 v[72:75], v246 offset:0\n" \
  "ds_read_b128 v[76:79], v242 offset:0\n" \
  "ds_read_b128 v[88:91], v246 offset:2048\n" \
  "ds_read_b128 v[92:95], v242 offset:2048\n" \
  "ds_read_b128 v[64:67], v249 offset:8320\n" \
  "ds_read_b128 v[68:71], v249 offset:8384\n" \
  "ds_read_b128 v[80:83], v249 offset:10400\n" \
  "ds_read_b128 v[84:87], v249 offset:10464\n" \
  "s_waitcnt lgkmcnt(4)\n" \
  "s_add_u32 s35, s4, 96\n" \
  "s_min_u32 s35, s35, 984\n" \
  "s_lshl_b32 s35, s35, 8\n" \
  "s_add_u32 m0, s5, 0\n" \
  "s_add_u32 s58, s94, s35\n" \
  "s_addc_u32 s59, s95, 0\n" \
  "global_load_lds_dwordx4 v244, s[58:59]\n" \
  "global_load_lds_dwordx4 v245, s[58:59] offset:1024\n" \
  "s_add_u32 s35, s4, 112\n" \
  "s_min_u32 s35, s35, 984\n" \
  "s_lshl_b32 s35, s35, 8\n" \
  "s_add_u32 m0, s5, 2048\n" \
  "s_add_u32 s58, s94, s35\n" \
  "s_addc_u32 s59, s95, 0\n" \
  "global_load_lds_dwordx4 v244, s[58:59]\n" \
  "global_load_lds_dwordx4 v245, s[58:59] offset:1024\n" \
  "s_getpc_b64 s[58:59]\n" \
  "LB_pc:\n" \
  "s_sub_u32 s58, s58, LB_pc-LZ_first\n" \
  "s_subb_u32 s59, s59, 0\n" \
  "s_mul_i32 s35, s28, 6656\n" \
  "v_lshl_add_u32 v195, v255, 7, s35\n" \
  "v_min_u32 v195, LZ_end-LZ_first, v195\n" \
  "s_mov_b32 exec_hi, 0xfffff\n" \
  "global_load_dword v248, v195, s[58:59]\n" \
  "s_mov_b64 exec, -1\n" \
  "s_lshl2_add_u32 s35, s26, s28\n" \
  "s_mul_i32 s35, s35, 6400\n" \
  "s_add_u32 s35, s35, 6553600\n" \
  "s_add_u32 s58, s14, s35\n" \
  "s_addc_u32 s59, s15, 0\n" \
  "s_mov_b32 exec_lo, 0xffffffff\n" \
  "s_mov_b32 exec_hi, 0x3ffff\n" \
  "global_load_dword v248, v247, s[58:59]\n" \
  "s_mov_b64 exec, -1\n" \
  "s_waitcnt vmcnt(2)\n" \
  "ds_read_b128 v[104:107], v246 offset:0\n" \
  "ds_read_b128 v[108:111], v242 offset:0\n" \
  "ds_read_b128 v[120:123], v246 offset:2048\n" \
  "ds_read_b128 v[124:127], v242 offset:2048\n" \
  "ds_read_b128 v[96:99], v250 offset:0\n" \
  "ds_read_b128 v[100:103], v250 offset:64\n" \
  "ds_read_b128 v[112:115], v251 offset:0\n" \
  "ds_read_b128 v[116:119], v251 offset:64\n" \
  "s_waitcnt lgkmcnt(4)\n" \
  "s_waitcnt vmcnt(0)\n" \
  "s_sleep 8\n" \
  "s_setprio 2\n" \
  "s_mul_i32 s36, s28, 2176\n" \
  "s_add_u32 s36, s36, 154624\n" \
  "v_bfe_u32 v228, v255, 3, 1\n" \
  "v_and_b32 v229, 1, v255\n" \
  "v_lshl_add_u32 v230, v228, 1, v229\n" \
  "v_lshl_add_u32 v231, v229, 1, v228\n" \
  "v_xor_b32 v229, 1, v230\n" \
  "s_mov_b32 vcc_lo, 0xcccccccc\n" \
  "s_mov_b32 vcc_hi, vcc_lo\n" \
  "v_cndmask_b32 v228, v230, v231, vcc\n" \
  "v_cndmask_b32 v229, v229, v231, vcc\n" \
  "v_and_b32 v230, 4, v255\n" \
  "v_mul_u32_u24 v230, 272, v230\n" \
  "v_lshl_add_u32 v230, v253, 2, v230\n" \
  "v_and_or_b32 v230, v255, 2, v230\n" \
  "v_add_u32 v230, s36, v230\n" \
  "s_movk_i32 s35, 272\n" \
  "v_mad_u32_u24 v195, v228, s35, v230\n" \
  "v_mad_u32_u24 v235, v229, s35, v230\n" \
  "v_lshl_add_u32 v230, v253, 4, v238\n" \
  "v_add_u32 v254, s36, v230\n" \
  "v_readfirstlane_b32 s39, v193\n" \
  "s_lshl_b32 s35, s26, 2\n" \
  "s_add_u32 s39, s39, 1\n" \
  "v_mov_b32 v193, s35\n" \
  "v_mov_b64 v[240:241], -1\n" \
  "v_mov_b64 v[242:243], -1\n" \
  "s_waitcnt lgkmcnt(0)\n" \
  "s_mov_b64 exec, 1\n" \
  "ds_add_u32 v190, v191 offset:0\n" \
  "s_mov_b64 exec, -1\n" \
  "L7_:\n" \
  "ds_read_b32 v192, v190 offset:0\n" \
  "s_waitcnt lgkmcnt(0)\n" \
  "v_readfirstlane_b32 s35, v192\n" \
  "s_cmp_lt_u32 s35, 4\n" \
  "s_cbranch_scc1 L7_\n" \
  "s_add_u32 s96, s96, 8192\n" \
  "s_add_u32 s40, s92, s6\n" \
  "s_addc_u32 s41, s93, 0\n" \
  "s_add_u32 s6, s6, 2097152\n" \
  "s_mov_b32 m0, s96\n" \
  "s_nop 0\n" \
  "global_load_lds_dwordx4 v189, s[40:41]\n" \
  "global_load_lds_dwordx4 v194, s[40:41] offset:1024\n" \
  "s_sub_u32 s96, s96, 8192\n" \
  "ds_read_b64_tr_b16 v[128:129], v184 offset:0\n" \
  "ds_read_b64_tr_b16 v[130:131], v184 offset:256\n" \
  "ds_read_b64_tr_b16 v[132:133], v184 offset:2048\n" \
  "ds_read_b64_tr_b16 v[134:135], v184 offset:2304\n" \
  "ds_read_b64_tr_b16 v[136:137], v184 offset:4096\n" \
  "ds_read_b64_tr_b16 v[138:139], v184 offset:4352\n" \
  "ds_read_b64_tr_b16 v[140:141], v184 offset:6144\n" \
  "ds_read_b64_tr_b16 v[142:143], v184 offset:6400\n" \
  "ds_read_b64_tr_b16 v[156:157], v185 offset:6144\n" \
  "ds_read_b64_tr_b16 v[158:159], v185 offset:6400\n" \
  "ds_read_b64_tr_b16 v[152:153], v185 offset:4096\n" \
  "ds_read_b64_tr_b16 v[154:155], v185 offset:4352\n" \
  "ds_read_b64_tr_b16 v[148:149], v185 offset:2048\n" \
  "ds_read_b64_tr_b16 v[150:151], v185 offset:2304\n" \
  "ds_read_b64_tr_b16 v[144:145], v185 offset:0\n" \
  "ds_read_b64_tr_b16 v[146:147], v185 offset:256\n" \
  "v_mov_b32 v192, s39\n" \
  "s_cmp_lg_u32 s28, 0\n" \
  "s_cbranch_scc1 LB_nc\n" \
  "s_mov_b64 exec, 1\n" \
  "global_store_dword v193, v192, s[48:49] offset:0 sc1\n" \
  "s_mov_b64 exec, -1\n" \
  "LB_nc:\n" \
  "s_lshr_b32 s35, s30, 2\n" \
  "s_lshl3_add_u32 s35, s29, s35\n" \
  "s_and_b32 s36, s30, 3\n" \
  "s_lshl2_add_u32 s35, s35, s36\n" \
  "s_lshl2_add_u32 s35, s35, s28\n" \
  "s_lshl_b32 s35, s35, 12\n" \
  "s_add_u32 s35, s35, 105676800\n" \
  "s_add_u32 s58, s24, s35\n" \
  "s_addc_u32 s59, s25, 0\n" \
  "global_store_dwordx4 v238, v[240:243], s[58:59] offset:0 sc1\n" \
  "global_store_dwordx4 v238, v[240:243], s[58:59] offset:1024 sc1\n" \
  "global_store_dwordx4 v238, v[240:243], s[58:59] offset:2048 sc1\n" \
  "global_store_dwordx4 v238, v[240:243], s[58:59] offset:3072 sc1\n" \
  "s_lshl2_add_u32 s35, s7, s28\n" \
  "s_lshl_b32 s35, s35, 7\n" \
  "s_lshl_b32 s36, s31, 16\n" \
  "s_add_u32 s35, s35, s36\n" \
  "s_add_u32 s35, s35, 109871104\n" \
  "s_add_u32 s58, s24, s35\n" \
  "s_addc_u32 s59, s25, 0\n" \
  "s_mov_b64 exec, 0xff\n" \
  "global_store_dwordx4 v238, v[240:243], s[58:59] sc1\n" \
  "s_mov_b64 exec, -1\n" \
  "s_cmp_lg_u32 s33, 1021\n" \
  "s_cbranch_scc1 LB_nm\n" \
  "v_mov_b64 v[240:241], 0\n" \
  "v_mov_b64 v[242:243], 0\n" \
  "global_store_dwordx4 v238, v[240:243], s[48:49] offset:2048 sc1\n" \
  "s_nop 1\n" \
  "v_lshlrev_b32 v242, 8, v255\n" \
  "global_store_dwordx2 v242, v[240:241], s[48:49] offset:3072 sc1\n" \
  "v_add_u32 v242, 0x4000, v242\n" \
  "global_store_dwordx2 v242, v[240:241], s[48:49] offset:3072 sc1\n" \
  "v_add_u32 v242, 0x4000, v242\n" \
  "global_store_dwordx2 v242, v[240:241], s[48:49] offset:3072 sc1\n" \
  "v_add_u32 v242, 0x4000, v242\n" \
  "global_store_dwordx2 v242, v[240:241], s[48:49] offset:3072 sc1\n" \
  "v_add_u32 v242, 0x4000, v242\n" \
  "LB_nm:\n" \
  "s_mov_b32 s86, 0x78000b0\n" \
  "s_mov_b32 s87, 0x0\n" \
  "s_or_b32 s86, s86, s87\n" \
  "s_setprio 2\n" \
  "s_memtime s[90:91]\n" \
  "s_memrealtime s[42:43]\n" \
  "LT:\n" \
  "s_cmp_eq_u32 s38, 6\n" \
  "s_cselect_b32 s78, 0x33333333, -1\n" \
  "s_cmp_eq_u32 s38, 7\n" \
  "s_cselect_b32 s78, 0x33333333, s78\n" \
  "s_cmp_eq_u32 s38, 18\n" \
  "s_cselect_b32 s80, 0x33333333, -1\n" \
  "s_cmp_eq_u32 s38, 19\n" \
  "s_cselect_b32 s80, 0x33333333, s80\n" \
  "s_mov_b32 s79, s78\n" \
  "s_mov_b32 s81, s80\n" \
  "s_mov_b32 s37, s38\n" \
  "s_add_u32 s57, s38, 2\n" \
  "s_add_u32 s3, s38, 4\n" \
  "s_add_u32 s38, s38, 6\n" \
  "s_cmp_lt_u32 s57, 32\n" \
  "s_cselect_b32 s57, s57, 37\n" \
  "s_cselect_b32 s3, s3, 37\n" \
  "s_waitcnt lgkmcnt(12)\n" \
  "v_mfma_f32_16x16x32_bf16 v[160:163], v[128:131], v[0:3], 0\n" \
  "s_mov_b64 vcc, s[78:79]\n" \
  "v_cndmask_b32_dpp v234, v234, v234, vcc row_ror:8 row_mask:0xf bank_mask:0xf\n" \
  "v_mfma_f32_16x16x32_bf16 v[160:163], v[132:135], v[4:7], v[160:163]\n" \
  "s_waitcnt lgkmcnt(8)\n" \
  "v_mfma_f32_16x16x32_bf16 v[160:163], v[136:139], v[8:11], v[160:163]\n" \
  "s_set_gpr_idx_on s37, gpr_idx(SRC0)\n" \
  "v_mad_u32_u24 v232, v196, s52, v236\n" \
  "s_set_gpr_idx_off\n" \
  "v_mfma_f32_16x16x32_bf16 v[160:163], v[140:143], v[12:15], v[160:163]\n" \
  "s_waitcnt lgkmcnt(0)\n" \
  "v_lshrrev_b32 v228, v232, v234\n" \
  "v_lshrrev_b32_dpp v229, v232, v234 quad_perm:[2,3,0,1] row_mask:0xf bank_mask:0xf\n" \
  "v_mfma_f32_16x16x32_bf16 v[164:167], v[156:159], v[12:15], 0\n" \
  "v_lshrrev_b32_dpp v230, v232, v234 quad_perm:[1,2,3,0] row_mask:0xf bank_mask:0xf\n" \
  "v_lshrrev_b32_dpp v231, v232, v234 quad_perm:[3,0,1,2] row_mask:0xf bank_mask:0xf\n" \
  "v_mfma_f32_16x16x32_bf16 v[164:167], v[152:155], v[8:11], v[164:167]\n" \
  "v_perm_b32 v228, v229, v228, s53\n" \
  "v_perm_b32 v230, v231, v230, s54\n" \
  "v_mfma_f32_16x16x32_bf16 v[164:167], v[148:151], v[4:7], v[164:167]\n" \
  "v_bitop3_b32 v228, v228, v230, s55 bitop3:0xa8\n" \
  "v_lshl_or_b32 v229, v234, 1, s56\n" \
  "v_mfma_f32_16x16x32_bf16 v[164:167], v[144:147], v[0:3], v[164:167]\n" \
  "v_add_u32_dpp v228, v228, v228 quad_perm:[2,3,0,1] row_mask:0xf bank_mask:0xf\n" \
  "v_min_u32_sdwa v230, v228, v228 dst_sel:DWORD dst_unused:UNUSED_PAD src0_sel:BYTE_0 src1_sel:BYTE_1\n" \
  "v_mfma_f32_16x16x32_bf16 v[172:175], v[144:147], v[16:19], 0\n" \
  "v_min_u32_sdwa v231, v228, v228 dst_sel:DWORD dst_unused:UNUSED_PAD src0_sel:BYTE_2 src1_sel:BYTE_3\n" \
  "v_sub_u32 v230, v230, v231\n" \
  "v_mfma_f32_16x16x32_bf16 v[172:175], v[148:151], v[20:23], v[172:175]\n" \
  "v_cvt_pk_bf16_f32 v176, v160, v161\n" \
  "v_add_u32_dpp v230, v230, v230 quad_perm:[1,0,3,2] row_mask:0xf bank_mask:0xf\n" \
  "v_mfma_f32_16x16x32_bf16 v[172:175], v[152:155], v[24:27], v[172:175]\n" \
  "v_cmp_ge_i32 vcc, 0, v230\n" \
  "v_cvt_pk_bf16_f32 v177, v162, v163\n" \
  "v_mfma_f32_16x16x32_bf16 v[172:175], v[156:159], v[28:31], v[172:175]\n" \
  "v_cndmask_b32_sdwa v228, v228, v228, vcc dst_sel:DWORD dst_unused:UNUSED_PAD src0_sel:WORD_1 src1_sel:WORD_0\n" \
  "v_cvt_pk_bf16_f32 v178, v164, v165\n" \
  "v_mfma_f32_16x16x32_bf16 v[168:171], v[140:143], v[28:31], 0\n" \
  "s_mov_b64 s[50:51], vcc\n" \
  "v_cmp_le_u32_sdwa vcc, v228, v228 src0_sel:BYTE_0 src1_sel:BYTE_1\n" \
  "v_cvt_pk_bf16_f32 v179, v166, v167\n" \
  "v_mfma_f32_16x16x32_bf16 v[168:171], v[136:139], v[24:27], v[168:171]\n" \
  "v_cndmask_b32_dpp v232, v232, v232, vcc quad_perm:[2,3,0,1] row_mask:0xf bank_mask:0xf\n" \
  "v_mfma_f32_16x16x32_bf16 v[168:171], v[132:135], v[20:23], v[168:171]\n" \
  "s_mov_b64 vcc, s[50:51]\n" \
  "v_cndmask_b32_dpp v232, v232, v232, vcc quad_perm:[3,0,1,2] row_mask:0xf bank_mask:0xf\n" \
  "v_lshlrev_b32_e64 v230, v232, 3\n" \
  "v_mfma_f32_16x16x32_bf16 v[168:171], v[128:131], v[16:19], v[168:171]\n" \
  "v_and_or_b32 v234, v229, v230, v234\n" \
  "s_cmp_lg_u32 s33, 1021\n" \
  "s_cbranch_scc0 LT_e\n" \
  "LT_r:\n" \
  "s_waitcnt vmcnt(5)\n" \
  "global_store_dwordx4 v186, v[176:179], s[44:45] offset:0 sc1\n" \
  "v_mfma_f32_16x16x32_bf16 v[160:163], v[128:131], v[32:35], 0\n" \
  "s_set_gpr_idx_on s37, gpr_idx(DST)\n" \
  "v_mov_b32 v196, v232\n" \
  "s_set_gpr_idx_off\n" \
  "v_mfma_f32_16x16x32_bf16 v[160:163], v[132:135], v[36:39], v[160:163]\n" \
  "s_set_gpr_idx_on s57, gpr_idx(SRC0)\n" \
  "v_mad_u32_u24 v232, v196, s52, v236\n" \
  "s_set_gpr_idx_off\n" \
  "v_mfma_f32_16x16x32_bf16 v[160:163], v[136:139], v[40:43], v[160:163]\n" \
  "v_lshrrev_b32 v228, v232, v234\n" \
  "v_lshrrev_b32_dpp v229, v232, v234 quad_perm:[2,3,0,1] row_mask:0xf bank_mask:0xf\n" \
  "v_mfma_f32_16x16x32_bf16 v[160:163], v[140:143], v[44:47], v[160:163]\n" \
  "v_lshrrev_b32_dpp v230, v232, v234 quad_perm:[1,2,3,0] row_mask:0xf bank_mask:0xf\n" \
  "v_lshrrev_b32_dpp v231, v232, v234 quad_perm:[3,0,1,2] row_mask:0xf bank_mask:0xf\n" \
  "v_mfma_f32_16x16x32_bf16 v[164:167], v[156:159], v[44:47], 0\n" \
  "v_perm_b32 v228, v229, v228, s53\n" \
  "v_perm_b32 v230, v231, v230, s54\n" \
  "v_mfma_f32_16x16x32_bf16 v[164:167], v[152:155], v[40:43], v[164:167]\n" \
  "v_bitop3_b32 v228, v228, v230, s55 bitop3:0xa8\n" \
  "v_lshl_or_b32 v229, v234, 1, s56\n" \
  "v_mfma_f32_16x16x32_bf16 v[164:167], v[148:151], v[36:39], v[164:167]\n" \
  "v_cvt_pk_bf16_f32 v182, v172, v173\n" \
  "v_cvt_pk_bf16_f32 v183, v174, v175\n" \
  "v_mfma_f32_16x16x32_bf16 v[164:167], v[144:147], v[32:35], v[164:167]\n" \
  "v_cvt_pk_bf16_f32 v180, v168, v169\n" \
  "v_cvt_pk_bf16_f32 v181, v170, v171\n" \
  "s_waitcnt vmcnt(5)\n" \
  "global_store_dwordx4 v186, v[180:183], s[44:45] offset:256 sc1\n" \
  "v_mfma_f32_16x16x32_bf16 v[172:175], v[144:147], v[48:51], 0\n" \
  "v_add_u32_dpp v228, v228, v228 quad_perm:[2,3,0,1] row_mask:0xf bank_mask:0xf\n" \
  "v_min_u32_sdwa v230, v228, v228 dst_sel:DWORD dst_unused:UNUSED_PAD src0_sel:BYTE_0 src1_sel:BYTE_1\n" \
  "v_mfma_f32_16x16x32_bf16 v[172:175], v[148:151], v[52:55], v[172:175]\n" \
  "v_min_u32_sdwa v231, v228, v228 dst_sel:DWORD dst_unused:UNUSED_PAD src0_sel:BYTE_2 src1_sel:BYTE_3\n" \
  "v_sub_u32 v230, v230, v231\n" \
  "v_mfma_f32_16x16x32_bf16 v[172:175], v[152:155], v[56:59], v[172:175]\n" \
  "v_cvt_pk_bf16_f32 v176, v160, v161\n" \
  "v_add_u32_dpp v230, v230, v230 quad_perm:[1,0,3,2] row_mask:0xf bank_mask:0xf\n" \
  "v_mfma_f32_16x16x32_bf16 v[172:175], v[156:159], v[60:63], v[172:175]\n" \
  "v_cmp_ge_i32 vcc, 0, v230\n" \
  "v_cvt_pk_bf16_f32 v177, v162, v163\n" \
  "v_mfma_f32_16x16x32_bf16 v[168:171], v[140:143], v[60:63], 0\n" \
  "v_cndmask_b32_sdwa v228, v228, v228, vcc dst_sel:DWORD dst_unused:UNUSED_PAD src0_sel:WORD_1 src1_sel:WORD_0\n" \
  "v_cvt_pk_bf16_f32 v178, v164, v165\n" \
  "v_mfma_f32_16x16x32_bf16 v[168:171], v[136:139], v[56:59], v[168:171]\n" \
  "s_mov_b64 s[50:51], vcc\n" \
  "v_cmp_le_u32_sdwa vcc, v228, v228 src0_sel:BYTE_0 src1_sel:BYTE_1\n" \
  "v_cvt_pk_bf16_f32 v179, v166, v167\n" \
  "v_mfma_f32_16x16x32_bf16 v[168:171], v[132:135], v[52:55], v[168:171]\n" \
  "v_cndmask_b32_dpp v232, v232, v232, vcc quad_perm:[2,3,0,1] row_mask:0xf bank_mask:0xf\n" \
  "v_mfma_f32_16x16x32_bf16 v[168:171], v[128:131], v[48:51], v[168:171]\n" \
  "s_mov_b64 vcc, s[50:51]\n" \
  "v_cndmask_b32_dpp v232, v232, v232, vcc quad_perm:[3,0,1,2] row_mask:0xf bank_mask:0xf\n" \
  "v_lshlrev_b32_e64 v230, v232, 3\n" \
  "s_waitcnt vmcnt(5)\n" \
  "global_store_dwordx4 v186, v[176:179], s[44:45] offset:512 sc1\n" \
  "v_mfma_f32_16x16x32_bf16 v[160:163], v[128:131], v[64:67], 0\n" \
  "v_and_or_b32 v234, v229, v230, v234\n" \
  "v_cvt_pk_bf16_f32 v182, v172, v173\n" \
  "v_mfma_f32_16x16x32_bf16 v[160:163], v[132:135], v[68:71], v[160:163]\n" \
  "s_set_gpr_idx_on s57, gpr_idx(DST)\n" \
  "v_mov_b32 v196, v232\n" \
  "s_set_gpr_idx_off\n" \
  "v_mfma_f32_16x16x32_bf16 v[160:163], v[136:139], v[72:75], v[160:163]\n" \
  "s_mov_b64 vcc, s[80:81]\n" \
  "v_cndmask_b32_dpp v234, v234, v234, vcc row_ror:8 row_mask:0xf bank_mask:0xf\n" \
  "v_cvt_pk_bf16_f32 v183, v174, v175\n" \
  "v_mfma_f32_16x16x32_bf16 v[160:163], v[140:143], v[76:79], v[160:163]\n" \
  "s_set_gpr_idx_on s3, gpr_idx(SRC0)\n" \
  "v_mad_u32_u24 v232, v196, s52, v236\n" \
  "s_set_gpr_idx_off\n" \
  "v_mfma_f32_16x16x32_bf16 v[164:167], v[156:159], v[76:79], 0\n" \
  "v_lshrrev_b32 v228, v232, v234\n" \
  "v_lshrrev_b32_dpp v229, v232, v234 quad_perm:[2,3,0,1] row_mask:0xf bank_mask:0xf\n" \
  "v_mfma_f32_16x16x32_bf16 v[164:167], v[152:155], v[72:75], v[164:167]\n" \
  "v_lshrrev_b32_dpp v230, v232, v234 quad_perm:[1,2,3,0] row_mask:0xf bank_mask:0xf\n" \
  "v_lshrrev_b32_dpp v231, v232, v234 quad_perm:[3,0,1,2] row_mask:0xf bank_mask:0xf\n" \
  "v_mfma_f32_16x16x32_bf16 v[164:167], v[148:151], v[68:71], v[164:167]\n" \
  "v_perm_b32 v228, v229, v228, s53\n" \
  "v_perm_b32 v230, v231, v230, s54\n" \
  "v_mfma_f32_16x16x32_bf16 v[164:167], v[144:147], v[64:67], v[164:167]\n" \
  "v_cvt_pk_bf16_f32 v180, v168, v169\n" \
  "v_cvt_pk_bf16_f32 v181, v170, v171\n" \
  "s_waitcnt vmcnt(5)\n" \
  "global_store_dwordx4 v186, v[180:183], s[44:45] offset:768 sc1\n" \
  "v_mfma_f32_16x16x32_bf16 v[172:175], v[144:147], v[80:83], 0\n" \
  "s_mov_b64 exec, 1\n" \
  "ds_add_u32 v190, v191 offset:0\n" \
  "s_mov_b64 exec, -1\n" \
  "v_mfma_f32_16x16x32_bf16 v[172:175], v[148:151], v[84:87], v[172:175]\n" \
  "v_bitop3_b32 v228, v228, v230, s55 bitop3:0xa8\n" \
  "v_lshl_or_b32 v229, v234, 1, s56\n" \
  "v_mfma_f32_16x16x32_bf16 v[172:175], v[152:155], v[88:91], v[172:175]\n" \
  "v_add_u32_dpp v228, v228, v228 quad_perm:[2,3,0,1] row_mask:0xf bank_mask:0xf\n" \
  "v_min_u32_sdwa v230, v228, v228 dst_sel:DWORD dst_unused:UNUSED_PAD src0_sel:BYTE_0 src1_sel:BYTE_1\n" \
  "v_mfma_f32_16x16x32_bf16 v[172:175], v[156:159], v[92:95], v[172:175]\n" \
  "v_min_u32_sdwa v231, v228, v228 dst_sel:DWORD dst_unused:UNUSED_PAD src0_sel:BYTE_2 src1_sel:BYTE_3\n" \
  "v_sub_u32 v230, v230, v231\n" \
  "v_mfma_f32_16x16x32_bf16 v[168:171], v[140:143], v[92:95], 0\n" \
  "v_cvt_pk_bf16_f32 v176, v160, v161\n" \
  "v_add_u32_dpp v230, v230, v230 quad_perm:[1,0,3,2] row_mask:0xf bank_mask:0xf\n" \
  "v_mfma_f32_16x16x32_bf16 v[168:171], v[136:139], v[88:91], v[168:171]\n" \
  "v_cmp_ge_i32 vcc, 0, v230\n" \
  "v_cvt_pk_bf16_f32 v177, v162, v163\n" \
  "v_mfma_f32_16x16x32_bf16 v[168:171], v[132:135], v[84:87], v[168:171]\n" \
  "v_cndmask_b32_sdwa v228, v228, v228, vcc dst_sel:DWORD dst_unused:UNUSED_PAD src0_sel:WORD_1 src1_sel:WORD_0\n" \
  "v_cvt_pk_bf16_f32 v178, v164, v165\n" \
  "v_mfma_f32_16x16x32_bf16 v[168:171], v[128:131], v[80:83], v[168:171]\n" \
  "s_mov_b64 s[50:51], vcc\n" \
  "v_cmp_le_u32_sdwa vcc, v228, v228 src0_sel:BYTE_0 src1_sel:BYTE_1\n" \
  "v_cvt_pk_bf16_f32 v179, v166, v167\n" \
  "s_cmp_eq_u32 s98, 4\n" \
  "s_cselect_b32 s36, 12451840, 0\n" \
  "s_sub_u32 s6, s6, s36\n" \
  "s_cmp_lt_u32 s98, 10\n" \
  "s_cselect_b32 s6, s6, s34\n" \
  "s_add_u32 s40, s92, s6\n" \
  "s_addc_u32 s41, s93, 0\n" \
  "s_add_u32 s6, s6, 2097152\n" \
  "s_waitcnt vmcnt(5)\n" \
  "global_store_dwordx4 v186, v[176:179], s[44:45] offset:1024 sc1\n" \
  "v_mfma_f32_16x16x32_bf16 v[160:163], v[128:131], v[96:99], 0\n" \
  "ds_read_b32 v192, v190\n" \
  "v_add_u32 v184, s97, v184\n" \
  "v_mfma_f32_16x16x32_bf16 v[160:163], v[132:135], v[100:103], v[160:163]\n" \
  "v_add_u32 v185, s97, v185\n" \
  "v_cndmask_b32_dpp v232, v232, v232, vcc quad_perm:[2,3,0,1] row_mask:0xf bank_mask:0xf\n" \
  "v_mfma_f32_16x16x32_bf16 v[160:163], v[136:139], v[104:107], v[160:163]\n" \
  "s_mov_b64 vcc, s[50:51]\n" \
  "v_cndmask_b32_dpp v232, v232, v232, vcc quad_perm:[3,0,1,2] row_mask:0xf bank_mask:0xf\n" \
  "v_lshlrev_b32_e64 v230, v232, 3\n" \
  "v_mfma_f32_16x16x32_bf16 v[160:163], v[140:143], v[108:111], v[160:163]\n" \
  "v_and_or_b32 v234, v229, v230, v234\n" \
  "v_cvt_pk_bf16_f32 v182, v172, v173\n" \
  "v_mfma_f32_16x16x32_bf16 v[164:167], v[156:159], v[108:111], 0\n" \
  "s_set_gpr_idx_on s3, gpr_idx(DST)\n" \
  "v_mov_b32 v196, v232\n" \
  "s_set_gpr_idx_off\n" \
  "v_mfma_f32_16x16x32_bf16 v[164:167], v[152:155], v[104:107], v[164:167]\n" \
  "s_waitcnt lgkmcnt(0)\n" \
  "v_readfirstlane_b32 s35, v192\n" \
  "v_cvt_pk_bf16_f32 v183, v174, v175\n" \
  "v_mfma_f32_16x16x32_bf16 v[164:167], v[148:151], v[100:103], v[164:167]\n" \
  "v_cvt_pk_bf16_f32 v180, v168, v169\n" \
  "v_cvt_pk_bf16_f32 v181, v170, v171\n" \
  "v_mfma_f32_16x16x32_bf16 v[164:167], v[144:147], v[96:99], v[164:167]\n" \
  "s_cmp_lt_u32 s35, s99\n" \
  "s_cbranch_scc1 LT_w\n" \
  "LT_c:\n" \
  "s_mov_b32 m0, s96\n" \
  "s_nop 0\n" \
  "global_load_lds_dwordx4 v189, s[40:41]\n" \
  "global_load_lds_dwordx4 v194, s[40:41] offset:1024\n" \
  "s_add_u32 s96, s96, s97\n" \
  "s_sub_u32 s97, 0, s97\n" \
  "s_add_u32 s99, s99, 4\n" \
  "s_lshl4_add_u32 s35, s28, s98\n" \
  "s_bitcmp1_b64 s[86:87], s35\n" \
  "s_cbranch_scc1 LT_a\n" \
  "LT_s:\n" \
  "s_waitcnt vmcnt(5)\n" \
  "global_store_dwordx4 v186, v[180:183], s[44:45] offset:1280 sc1\n" \
  "v_mfma_f32_16x16x32_bf16 v[168:171], v[128:131], v[112:115], 0\n" \
  "ds_read_b64_tr_b16 v[128:129], v184 offset:0\n" \
  "ds_read_b64_tr_b16 v[130:131], v184 offset:256\n" \
  "v_mfma_f32_16x16x32_bf16 v[168:171], v[132:135], v[116:119], v[168:171]\n" \
  "ds_read_b64_tr_b16 v[132:133], v184 offset:2048\n" \
  "ds_read_b64_tr_b16 v[134:135], v184 offset:2304\n" \
  "v_mfma_f32_16x16x32_bf16 v[168:171], v[136:139], v[120:123], v[168:171]\n" \
  "ds_read_b64_tr_b16 v[136:137], v184 offset:4096\n" \
  "v_cvt_pk_bf16_f32 v176, v160, v161\n" \
  "v_mfma_f32_16x16x32_bf16 v[168:171], v[140:143], v[124:127], v[168:171]\n" \
  "ds_read_b64_tr_b16 v[138:139], v184 offset:4352\n" \
  "v_cvt_pk_bf16_f32 v177, v162, v163\n" \
  "v_mfma_f32_16x16x32_bf16 v[172:175], v[156:159], v[124:127], 0\n" \
  "ds_read_b64_tr_b16 v[140:141], v184 offset:6144\n" \
  "v_cvt_pk_bf16_f32 v178, v164, v165\n" \
  "v_mfma_f32_16x16x32_bf16 v[172:175], v[152:155], v[120:123], v[172:175]\n" \
  "ds_read_b64_tr_b16 v[142:143], v184 offset:6400\n" \
  "v_cvt_pk_bf16_f32 v179, v166, v167\n" \
  "v_mfma_f32_16x16x32_bf16 v[172:175], v[148:151], v[116:119], v[172:175]\n" \
  "ds_read_b64_tr_b16 v[156:157], v185 offset:6144\n" \
  "ds_read_b64_tr_b16 v[158:159], v185 offset:6400\n" \
  "v_mfma_f32_16x16x32_bf16 v[172:175], v[144:147], v[112:115], v[172:175]\n" \
  "ds_read_b64_tr_b16 v[152:153], v185 offset:4096\n" \
  "ds_read_b64_tr_b16 v[154:155], v185 offset:4352\n" \
  "ds_read_b64_tr_b16 v[148:149], v185 offset:2048\n" \
  "ds_read_b64_tr_b16 v[150:151], v185 offset:2304\n" \
  "ds_read_b64_tr_b16 v[144:145], v185 offset:0\n" \
  "ds_read_b64_tr_b16 v[146:147], v185 offset:256\n" \
  "s_waitcnt vmcnt(5)\n" \
  "global_store_dwordx4 v187, v[176:179], s[44:45] offset:0 sc1\n" \
  "s_add_u32 s98, s98, 1\n" \
  "v_cvt_pk_bf16_f32 v180, v168, v169\n" \
  "v_cvt_pk_bf16_f32 v181, v170, v171\n" \
  "v_cvt_pk_bf16_f32 v182, v172, v173\n" \
  "v_cvt_pk_bf16_f32 v183, v174, v175\n" \
  "s_waitcnt vmcnt(5)\n" \
  "global_store_dwordx4 v188, v[180:183], s[44:45] offset:0 sc1\n" \
  "s_add_u32 s44, s44, 128000\n" \
  "s_addc_u32 s45, s45, 0\n" \
  "s_bitcmp0_b32 4160, s98\n" \
  "s_cbranch_scc1 LT\n" \
  "s_cmp_eq_u32 s98, 6\n" \
  "s_cbranch_scc1 LT_m\n" \
  "s_lshr_b32 s35, s30, 2\n" \
  "s_lshl3_add_u32 s35, s29, s35\n" \
  "s_mul_i32 s35, s35, 1600000\n" \
  "s_lshl_b32 s36, s28, 12\n" \
  "s_add_u32 s35, s35, s36\n" \
  "s_add_u32 s40, s24, s35\n" \
  "s_addc_u32 s41, s25, 0\n" \
  "s_bitcmp1_b32 s30, 2\n" \
  "s_cbranch_scc1 LB_l_h\n" \
  "global_load_dwordx4 v[64:67], v238, s[40:41] offset:0 sc1\n" \
  "global_load_dwordx4 v[68:71], v238, s[40:41] offset:1024 sc1\n" \
  "global_load_dwordx4 v[72:75], v238, s[40:41] offset:2048 sc1\n" \
  "global_load_dwordx4 v[76:79], v238, s[40:41] offset:3072 sc1\n" \
  "s_add_u32 s40, s40, 16000\n" \
  "s_addc_u32 s41, s41, 0\n" \
  "global_load_dwordx4 v[80:83], v238, s[40:41] offset:0 sc1\n" \
  "global_load_dwordx4 v[84:87], v238, s[40:41] offset:1024 sc1\n" \
  "global_load_dwordx4 v[88:91], v238, s[40:41] offset:2048 sc1\n" \
  "global_load_dwordx4 v[92:95], v238, s[40:41] offset:3072 sc1\n" \
  "s_add_u32 s40, s40, 16000\n" \
  "s_addc_u32 s41, s41, 0\n" \
  "global_load_dwordx4 v[96:99], v238, s[40:41] offset:0 sc1\n" \
  "global_load_dwordx4 v[100:103], v238, s[40:41] offset:1024 sc1\n" \
  "global_load_dwordx4 v[104:107], v238, s[40:41] offset:2048 sc1\n" \
  "global_load_dwordx4 v[108:111], v238, s[40:41] offset:3072 sc1\n" \
  "s_add_u32 s40, s40, 16000\n" \
  "s_addc_u32 s41, s41, 0\n" \
  "global_load_dwordx4 v[112:115], v238, s[40:41] offset:0 sc1\n" \
  "global_load_dwordx4 v[116:119], v238, s[40:41] offset:1024 sc1\n" \
  "global_load_dwordx4 v[120:123], v238, s[40:41] offset:2048 sc1\n" \
  "global_load_dwordx4 v[124:127], v238, s[40:41] offset:3072 sc1\n" \
  "s_add_u32 s40, s40, 16000\n" \
  "s_addc_u32 s41, s41, 0\n" \
  "s_branch LB_l_e\n" \
  "LB_l_h:\n" \
  "global_load_dwordx4 v[0:3], v238, s[40:41] offset:0 sc1\n" \
  "global_load_dwordx4 v[4:7], v238, s[40:41] offset:1024 sc1\n" \
  "global_load_dwordx4 v[8:11], v238, s[40:41] offset:2048 sc1\n" \
  "global_load_dwordx4 v[12:15], v238, s[40:41] offset:3072 sc1\n" \
  "s_add_u32 s40, s40, 16000\n" \
  "s_addc_u32 s41, s41, 0\n" \
  "global_load_dwordx4 v[16:19], v238, s[40:41] offset:0 sc1\n" \
  "global_load_dwordx4 v[20:23], v238, s[40:41] offset:1024 sc1\n" \
  "global_load_dwordx4 v[24:27], v238, s[40:41] offset:2048 sc1\n" \
  "global_load_dwordx4 v[28:31], v238, s[40:41] offset:3072 sc1\n" \
  "s_add_u32 s40, s40, 16000\n" \
  "s_addc_u32 s41, s41, 0\n" \
  "global_load_dwordx4 v[32:35], v238, s[40:41] offset:0 sc1\n" \
  "global_load_dwordx4 v[36:39], v238, s[40:41] offset:1024 sc1\n" \
  "global_load_dwordx4 v[40:43], v238, s[40:41] offset:2048 sc1\n" \
  "global_load_dwordx4 v[44:47], v238, s[40:41] offset:3072 sc1\n" \
  "s_add_u32 s40, s40, 16000\n" \
  "s_addc_u32 s41, s41, 0\n" \
  "global_load_dwordx4 v[48:51], v238, s[40:41] offset:0 sc1\n" \
  "global_load_dwordx4 v[52:55], v238, s[40:41] offset:1024 sc1\n" \
  "global_load_dwordx4 v[56:59], v238, s[40:41] offset:2048 sc1\n" \
  "global_load_dwordx4 v[60:63], v238, s[40:41] offset:3072 sc1\n" \
  "s_add_u32 s40, s40, 16000\n" \
  "s_addc_u32 s41, s41, 0\n" \
  "LB_l_e:\n" \
  "ds_write_b16_d16_hi v195, v197 offset:0\n" \
  "ds_write_b16_d16_hi v195, v199 offset:16\n" \
  "ds_write_b16_d16_hi v195, v201 offset:32\n" \
  "ds_write_b16_d16_hi v235, v203 offset:64\n" \
  "ds_write_b16_d16_hi v235, v205 offset:80\n" \
  "ds_write_b16_d16_hi v235, v207 offset:96\n" \
  "ds_write_b16_d16_hi v235, v209 offset:112\n" \
  "ds_write_b16_d16_hi v235, v211 offset:128\n" \
  "ds_write_b16_d16_hi v235, v213 offset:144\n" \
  "ds_write_b16_d16_hi v235, v215 offset:160\n" \
  "ds_write_b16_d16_hi v235, v217 offset:176\n" \
  "ds_write_b16_d16_hi v195, v219 offset:48\n" \
  "ds_write_b16_d16_hi v195, v221 offset:192\n" \
  "ds_write_b16_d16_hi v195, v223 offset:208\n" \
  "ds_write_b16_d16_hi v195, v225 offset:224\n" \
  "ds_write_b16_d16_hi v195, v227 offset:240\n" \
  "ds_read_b128 v[244:247], v254 offset:0\n" \
  "ds_read_b128 v[248:251], v254 offset:1088\n" \
  "s_lshr_b32 s35, s7, 5\n" \
  "s_mul_i32 s35, s35, 819200\n" \
  "s_and_b32 s36, s7, 31\n" \
  "s_lshl_b32 s36, s36, 10\n" \
  "s_add_u32 s35, s35, s36\n" \
  "s_lshl_b32 s36, s4, 9\n" \
  "s_add_u32 s35, s35, s36\n" \
  "s_add_u32 s35, s35, 102793216\n" \
  "s_add_u32 s58, s24, s35\n" \
  "s_addc_u32 s59, s25, 0\n" \
  "s_waitcnt lgkmcnt(0)\n" \
  "s_cmp_ge_u32 s4, 896\n" \
  "s_cbranch_scc1 L17_\n" \
  "global_store_dwordx4 v238, v[244:247], s[58:59] sc1\n" \
  "s_cmp_ge_u32 s4, 768\n" \
  "s_cbranch_scc1 L17_\n" \
  "s_add_u32 s58, s58, 32768\n" \
  "s_addc_u32 s59, s59, 0\n" \
  "global_store_dwordx4 v238, v[248:251], s[58:59] sc1\n" \
  "L17_:\n" \
  "s_lshr_b32 s35, s30, 2\n" \
  "s_lshl3_add_u32 s33, s29, s35\n" \
  "s_and_b32 s34, s30, 3\n" \
  "s_lshl_b32 s59, s27, 11\n" \
  "s_mul_i32 s35, s33, 1600000\n" \
  "s_add_u32 s54, s24, s35\n" \
  "s_addc_u32 s55, s25, 0\n" \
  "s_mul_i32 s35, s34, 819200\n" \
  "s_lshl1_add_u32 s35, s59, s35\n" \
  "s_add_u32 s35, s35, 102400000\n" \
  "s_add_u32 s56, s24, s35\n" \
  "s_addc_u32 s57, s25, 0\n" \
  "global_load_dwordx4 v[208:211], v238, s[56:57] offset:0 sc1\n" \
  "global_load_dwordx4 v[212:215], v238, s[56:57] offset:1024 sc1\n" \
  "global_load_dwordx4 v[216:219], v238, s[56:57] offset:2048 sc1\n" \
  "global_load_dwordx4 v[220:223], v238, s[56:57] offset:3072 sc1\n" \
  "s_add_u32 s56, s56, 32768\n" \
  "s_addc_u32 s57, s57, 0\n" \
  "global_load_dwordx4 v[224:227], v238, s[56:57] sc1\n" \
  "global_load_dwordx4 v[228:231], v238, s[56:57] offset:1024 sc1\n" \
  "global_load_dwordx4 v[232:235], v238, s[56:57] offset:2048 sc1\n" \
  "global_load_dwordx4 v[236:239], v238, s[56:57] offset:3072 sc1\n" \
  "s_add_u32 s56, s56, 32768\n" \
  "s_addc_u32 s57, s57, 0\n" \
  "s_bitcmp1_b32 s30, 2\n" \
  "s_cbranch_scc1 LB_t_h\n" \
  "s_waitcnt lgkmcnt(12)\n" \
  "v_mfma_f32_16x16x32_bf16 v[160:163], v[128:131], v[0:3], 0\n" \
  "v_mfma_f32_16x16x32_bf16 v[160:163], v[132:135], v[4:7], v[160:163]\n" \
  "s_waitcnt lgkmcnt(8)\n" \
  "v_mfma_f32_16x16x32_bf16 v[160:163], v[136:139], v[8:11], v[160:163]\n" \
  "v_mfma_f32_16x16x32_bf16 v[160:163], v[140:143], v[12:15], v[160:163]\n" \
  "s_waitcnt lgkmcnt(0)\n" \
  "v_mfma_f32_16x16x32_bf16 v[164:167], v[156:159], v[12:15], 0\n" \
  "v_mfma_f32_16x16x32_bf16 v[164:167], v[152:155], v[8:11], v[164:167]\n" \
  "v_mfma_f32_16x16x32_bf16 v[164:167], v[148:151], v[4:7], v[164:167]\n" \
  "v_mfma_f32_16x16x32_bf16 v[164:167], v[144:147], v[0:3], v[164:167]\n" \
  "v_mfma_f32_16x16x32_bf16 v[172:175], v[144:147], v[16:19], 0\n" \
  "v_mfma_f32_16x16x32_bf16 v[172:175], v[148:151], v[20:23], v[172:175]\n" \
  "v_mfma_f32_16x16x32_bf16 v[172:175], v[152:155], v[24:27], v[172:175]\n" \
  "v_cvt_pk_bf16_f32 v176, v160, v161\n" \
  "v_cvt_pk_bf16_f32 v177, v162, v163\n" \
  "v_mfma_f32_16x16x32_bf16 v[172:175], v[156:159], v[28:31], v[172:175]\n" \
  "v_mfma_f32_16x16x32_bf16 v[168:171], v[140:143], v[28:31], 0\n" \
  "v_mfma_f32_16x16x32_bf16 v[168:171], v[136:139], v[24:27], v[168:171]\n" \
  "v_cvt_pk_bf16_f32 v178, v164, v165\n" \
  "v_cvt_pk_bf16_f32 v179, v166, v167\n" \
  "v_mfma_f32_16x16x32_bf16 v[168:171], v[132:135], v[20:23], v[168:171]\n" \
  "v_mfma_f32_16x16x32_bf16 v[168:171], v[128:131], v[16:19], v[168:171]\n" \
  "s_waitcnt vmcnt(63)\n" \
  "global_store_dwordx4 v186, v[176:179], s[46:47] offset:0 sc1\n" \
  "v_mfma_f32_16x16x32_bf16 v[160:163], v[128:131], v[32:35], 0\n" \
  "v_cvt_pk_bf16_f32 v182, v172, v173\n" \
  "v_cvt_pk_bf16_f32 v183, v174, v175\n" \
  "v_mfma_f32_16x16x32_bf16 v[160:163], v[132:135], v[36:39], v[160:163]\n" \
  "v_mfma_f32_16x16x32_bf16 v[160:163], v[136:139], v[40:43], v[160:163]\n" \
  "v_mfma_f32_16x16x32_bf16 v[160:163], v[140:143], v[44:47], v[160:163]\n" \
  "v_cvt_pk_bf16_f32 v180, v168, v169\n" \
  "v_cvt_pk_bf16_f32 v181, v170, v171\n" \
  "v_mfma_f32_16x16x32_bf16 v[164:167], v[156:159], v[44:47], 0\n" \
  "v_mfma_f32_16x16x32_bf16 v[164:167], v[152:155], v[40:43], v[164:167]\n" \
  "v_mfma_f32_16x16x32_bf16 v[164:167], v[148:151], v[36:39], v[164:167]\n" \
  "v_mfma_f32_16x16x32_bf16 v[164:167], v[144:147], v[32:35], v[164:167]\n" \
  "s_waitcnt vmcnt(63)\n" \
  "global_store_dwordx4 v186, v[180:183], s[46:47] offset:256 sc1\n" \
  "v_mfma_f32_16x16x32_bf16 v[172:175], v[144:147], v[48:51], 0\n" \
  "v_cvt_pk_bf16_f32 v176, v160, v161\n" \
  "v_cvt_pk_bf16_f32 v177, v162, v163\n" \
  "v_mfma_f32_16x16x32_bf16 v[172:175], v[148:151], v[52:55], v[172:175]\n" \
  "v_mfma_f32_16x16x32_bf16 v[172:175], v[152:155], v[56:59], v[172:175]\n" \
  "v_mfma_f32_16x16x32_bf16 v[172:175], v[156:159], v[60:63], v[172:175]\n" \
  "v_cvt_pk_bf16_f32 v178, v164, v165\n" \
  "v_cvt_pk_bf16_f32 v179, v166, v167\n" \
  "v_mfma_f32_16x16x32_bf16 v[168:171], v[140:143], v[60:63], 0\n" \
  "v_mfma_f32_16x16x32_bf16 v[168:171], v[136:139], v[56:59], v[168:171]\n" \
  "v_mfma_f32_16x16x32_bf16 v[168:171], v[132:135], v[52:55], v[168:171]\n" \
  "v_mfma_f32_16x16x32_bf16 v[168:171], v[128:131], v[48:51], v[168:171]\n" \
  "s_waitcnt vmcnt(63)\n" \
  "global_store_dwordx4 v186, v[176:179], s[46:47] offset:512 sc1\n" \
  "v_cvt_pk_bf16_f32 v182, v172, v173\n" \
  "v_cvt_pk_bf16_f32 v183, v174, v175\n" \
  "s_nop 3\n" \
  "v_cvt_pk_bf16_f32 v180, v168, v169\n" \
  "v_cvt_pk_bf16_f32 v181, v170, v171\n" \
  "s_waitcnt vmcnt(63)\n" \
  "global_store_dwordx4 v186, v[180:183], s[46:47] offset:768 sc1\n" \
  "s_branch LB_t_e\n" \
  "LT_w:\n" \
  "L8_:\n" \
  "s_sleep 1\n" \
  "ds_read_b32 v192, v190 offset:0\n" \
  "s_waitcnt lgkmcnt(0)\n" \
  "v_readfirstlane_b32 s35, v192\n" \
  "s_cmp_lt_u32 s35, s99\n" \
  "s_cbranch_scc1 L8_\n" \
  "s_branch LT_c\n" \
  "LT_e:\n" \
  "s_bitcmp1_b32 10, s98\n" \
  "s_cbranch_scc0 LT_r\n" \
  "global_load_dwordx4 v[240:243], v238, s[48:49] offset:0 sc1\n" \
  "s_waitcnt vmcnt(0)\n" \
  "global_store_dwordx4 v238, v[240:243], s[48:49] offset:1024 sc1\n" \
  "s_branch LT_r\n" \
  "LT_a:\n" \
  "s_cmp_lg_u32 s35, 4\n" \
  "s_cbranch_scc1 L9_\n" \
  "global_load_dword v193, v193, s[48:49] offset:1024 sc1\n" \
  "s_branch LT_s\n" \
  "L9_:\n" \
  "s_cmp_lg_u32 s35, 5\n" \
  "s_cbranch_scc1 L10_\n" \
  "s_memtime s[58:59]\n" \
  "s_memrealtime s[40:41]\n" \
  "s_mov_b32 s5, 0x10000\n" \
  "LB_ec:\n" \
  "v_readfirstlane_b32 s35, v193\n" \
  "s_cmp_eq_u32 s35, s39\n" \
  "s_cbranch_scc1 LB_ok\n" \
  "s_lshl_b32 s35, s26, 2\n" \
  "v_mov_b32 v193, s35\n" \
  "s_sleep 8\n" \
  "global_load_dword v193, v193, s[48:49] offset:1024 sc1\n" \
  "s_waitcnt vmcnt(0)\n" \
  "s_sub_u32 s5, s5, 1\n" \
  "s_cbranch_scc0 LB_ec\n" \
  "LB_ok:\n" \
  "s_lshl_b32 s35, s26, 2\n" \
  "v_mov_b32 v244, s35\n" \
  "s_waitcnt lgkmcnt(0)\n" \
  "s_sub_u32 s88, s58, s88\n" \
  "s_sub_u32 s88, 106000, s88\n" \
  "s_sub_u32 s90, s58, s90\n" \
  "s_sub_u32 s42, s40, s42\n" \
  "s_lshl_b32 s40, s40, 4\n" \
  "v_cvt_f32_u32 v245, s90\n" \
  "v_cvt_f32_u32 v246, s42\n" \
  "v_rcp_f32 v245, v245\n" \
  "v_cvt_f32_i32 v247, s88\n" \
  "v_mul_f32 v246, 0x4111eb85, v246\n" \
  "v_mul_f32 v245, v245, v246\n" \
  "v_mul_f32 v245, v245, v247\n" \
  "v_cvt_i32_f32 v245, v245\n" \
  "v_add_u32 v245, s40, v245\n" \
  "v_or_b32 v245, 1, v245\n" \
  "s_mov_b64 exec, 1\n" \
  "global_store_dword v244, v245, s[48:49] offset:2048 sc1\n" \
  "s_mov_b64 exec, -1\n" \
  "s_branch LT_s\n" \
  "L10_:\n" \
  "s_cmp_lg_u32 s35, 7\n" \
  "s_cbranch_scc1 L11_\n" \
  "s_mov_b32 exec_hi, 0\n" \
  "global_atomic_add v196, v191, s[48:49] offset:3072 sc1\n" \
  "s_mov_b64 exec, -1\n" \
  "s_branch LT_s\n" \
  "L11_:\n" \
  "s_cmp_lg_u32 s35, 23\n" \
  "s_cbranch_scc1 L12_\n" \
  "s_lshl_b32 s35, s30, 5\n" \
  "v_lshrrev_b32 v206, 3, v255\n" \
  "v_and_b32 v208, 7, v255\n" \
  "v_lshl_add_u32 v210, v206, 2, s35\n" \
  "v_lshl_add_u32 v212, v208, 2, s35\n" \
  "global_load_dword v202, v210, s[48:49] offset:2048 sc1\n" \
  "global_load_dword v204, v212, s[48:49] offset:2048 sc1\n" \
  "s_branch LT_s\n" \
  "L12_:\n" \
  "s_cmp_lg_u32 s35, 24\n" \
  "s_cbranch_scc1 L13_\n" \
  "s_mov_b32 s5, 0x10000\n" \
  "LB_z:\n" \
  "v_min_u32 v214, v202, v204\n" \
  "v_cmp_eq_u32 s[58:59], 0, v214\n" \
  "s_sub_u32 s5, s5, 1\n" \
  "s_cbranch_scc1 LB_zx\n" \
  "s_cmp_eq_u64 s[58:59], 0\n" \
  "s_cbranch_scc1 LB_zx\n" \
  "s_cmp_lg_u32 s33, 1021\n" \
  "s_cbranch_scc1 LB_n1\n" \
  "global_load_dwordx4 v[240:243], v238, s[48:49] offset:0 sc1\n" \
  "s_waitcnt vmcnt(0)\n" \
  "global_store_dwordx4 v238, v[240:243], s[48:49] offset:1024 sc1\n" \
  "LB_n1:\n" \
  "s_sleep 4\n" \
  "global_load_dword v202, v210, s[48:49] offset:2048 sc1\n" \
  "global_load_dword v204, v212, s[48:49] offset:2048 sc1\n" \
  "s_waitcnt vmcnt(0)\n" \
  "s_branch LB_z\n" \
  "LB_zx:\n" \
  "v_and_or_b32 v202, v202, -8, v206\n" \
  "v_and_or_b32 v204, v204, -8, v208\n" \
  "v_cmp_lt_u32 s[58:59], v204, v202\n" \
  "v_sub_u32 v202, v204, v202\n" \
  "s_lshl_b32 s35, s29, 3\n" \
  "s_lshr_b64 s[40:41], s[58:59], s35\n" \
  "s_and_b32 s40, s40, 0xff\n" \
  "s_bcnt1_i32_b32 s40, s40\n" \
  "s_sub_u32 s40, 7, s40\n" \
  "s_mov_b32 s36, s29\n" \
  "s_bfe_u32 s41, s58, 0x80000\n" \
  "s_bcnt1_i32_b32 s41, s41\n" \
  "s_cmp_eq_u32 s41, s40\n" \
  "s_cselect_b32 s36, 0, s36\n" \
  "s_bfe_u32 s41, s58, 0x80008\n" \
  "s_bcnt1_i32_b32 s41, s41\n" \
  "s_cmp_eq_u32 s41, s40\n" \
  "s_cselect_b32 s36, 1, s36\n" \
  "s_bfe_u32 s41, s58, 0x80010\n" \
  "s_bcnt1_i32_b32 s41, s41\n" \
  "s_cmp_eq_u32 s41, s40\n" \
  "s_cselect_b32 s36, 2, s36\n" \
  "s_bfe_u32 s41, s58, 0x80018\n" \
  "s_bcnt1_i32_b32 s41, s41\n" \
  "s_cmp_eq_u32 s41, s40\n" \
  "s_cselect_b32 s36, 3, s36\n" \
  "s_bfe_u32 s41, s59, 0x80000\n" \
  "s_bcnt1_i32_b32 s41, s41\n" \
  "s_cmp_eq_u32 s41, s40\n" \
  "s_cselect_b32 s36, 4, s36\n" \
  "s_bfe_u32 s41, s59, 0x80008\n" \
  "s_bcnt1_i32_b32 s41, s41\n" \
  "s_cmp_eq_u32 s41, s40\n" \
  "s_cselect_b32 s36, 5, s36\n" \
  "s_bfe_u32 s41, s59, 0x80010\n" \
  "s_bcnt1_i32_b32 s41, s41\n" \
  "s_cmp_eq_u32 s41, s40\n" \
  "s_cselect_b32 s36, 6, s36\n" \
  "s_bfe_u32 s41, s59, 0x80018\n" \
  "s_bcnt1_i32_b32 s41, s41\n" \
  "s_cmp_eq_u32 s41, s40\n" \
  "s_cselect_b32 s36, 7, s36\n" \
  "s_add_u32 s35, s35, s36\n" \
  "v_readlane_b32 s41, v202, s35\n" \
  "s_sub_u32 s35, 0, s41\n" \
  "s_cmp_lt_u32 s40, 4\n" \
  "s_cselect_b32 s41, s35, s41\n" \
  "s_min_u32 s41, s41, 0xffff\n" \
  "s_mul_i32 s41, s41, 39\n" \
  "s_add_u32 s41, s41, 28672\n" \
  "s_lshr_b32 s41, s41, 16\n" \
  "s_min_u32 s41, s41, 4\n" \
  "s_sub_u32 s35, 0, s41\n" \
  "s_cmp_lt_u32 s40, 4\n" \
  "s_cselect_b32 s35, s35, s41\n" \
  "s_lshl4_add_u32 s35, s36, s35\n" \
  "s_add_u32 s35, s35, 132\n" \
  "v_mov_b32 v204, s35\n" \
  "ds_write_b32 v190, v204 offset:4\n" \
  "s_branch LT_s\n" \
  "L13_:\n" \
  "s_cmp_lg_u32 s35, 25\n" \
  "s_cbranch_scc1 L14_\n" \
  "s_mov_b64 exec, 0xff\n" \
  "global_load_dword v200, v198, s[48:49] offset:3072 sc1\n" \
  "s_mov_b64 exec, -1\n" \
  "s_branch LT_s\n" \
  "L14_:\n" \
  "s_cmp_lg_u32 s35, 26\n" \
  "s_cbranch_scc1 L15_\n" \
  "s_mov_b64 s[58:59], vcc\n" \
  "s_mov_b32 s5, 0x10000\n" \
  "LB_p:\n" \
  "s_mov_b64 exec, 0xff\n" \
  "v_cmp_le_u32 vcc, 32, v200\n" \
  "s_mov_b64 exec, -1\n" \
  "s_sub_u32 s5, s5, 1\n" \
  "s_cselect_b32 s35, 0xff, vcc_lo\n" \
  "s_cmp_eq_u32 s35, 0xff\n" \
  "s_cbranch_scc1 LB_p_x\n" \
  "s_cmp_lg_u32 s33, 1021\n" \
  "s_cbranch_scc1 LB_n2\n" \
  "global_load_dwordx4 v[240:243], v238, s[48:49] offset:0 sc1\n" \
  "s_waitcnt vmcnt(0)\n" \
  "global_store_dwordx4 v238, v[240:243], s[48:49] offset:1024 sc1\n" \
  "LB_n2:\n" \
  "s_sleep 2\n" \
  "s_mov_b64 exec, 0xff\n" \
  "global_load_dword v200, v198, s[48:49] offset:3072 sc1\n" \
  "s_mov_b64 exec, -1\n" \
  "s_waitcnt vmcnt(0)\n" \
  "s_branch LB_p\n" \
  "LB_p_x:\n" \
  "s_mov_b64 vcc, s[58:59]\n" \
  "s_branch LT_s\n" \
  "L15_:\n" \
  "s_branch LT_s\n" \
  "LT_m:\n" \
  "ds_write_b16_d16_hi v195, v196 offset:0\n" \
  "ds_write_b16_d16_hi v195, v198 offset:16\n" \
  "ds_write_b16_d16_hi v195, v200 offset:32\n" \
  "ds_write_b16_d16_hi v235, v202 offset:64\n" \
  "ds_write_b16_d16_hi v235, v204 offset:80\n" \
  "ds_write_b16_d16_hi v235, v206 offset:96\n" \
  "ds_write_b16_d16_hi v235, v208 offset:112\n" \
  "ds_write_b16_d16_hi v235, v210 offset:128\n" \
  "ds_write_b16_d16_hi v235, v212 offset:144\n" \
  "ds_write_b16_d16_hi v235, v214 offset:160\n" \
  "ds_write_b16_d16_hi v235, v216 offset:176\n" \
  "ds_write_b16_d16_hi v195, v218 offset:48\n" \
  "ds_write_b16_d16_hi v195, v220 offset:192\n" \
  "ds_write_b16_d16_hi v195, v222 offset:208\n" \
  "ds_write_b16_d16_hi v195, v224 offset:224\n" \
  "ds_write_b16_d16_hi v195, v226 offset:240\n" \
  "ds_read_b128 v[244:247], v254 offset:0\n" \
  "ds_read_b128 v[248:251], v254 offset:1088\n" \
  "s_lshr_b32 s35, s7, 5\n" \
  "s_mul_i32 s35, s35, 819200\n" \
  "s_and_b32 s36, s7, 31\n" \
  "s_lshl_b32 s36, s36, 10\n" \
  "s_add_u32 s35, s35, s36\n" \
  "s_lshl_b32 s36, s4, 9\n" \
  "s_add_u32 s35, s35, s36\n" \
  "s_add_u32 s35, s35, 102400000\n" \
  "s_add_u32 s58, s24, s35\n" \
  "s_addc_u32 s59, s25, 0\n" \
  "s_waitcnt lgkmcnt(0)\n" \
  "s_cmp_ge_u32 s4, 768\n" \
  "s_cbranch_scc1 L16_\n" \
  "global_store_dwordx4 v238, v[244:247], s[58:59] sc1\n" \
  "s_cmp_ge_u32 s4, 768\n" \
  "s_cbranch_scc1 L16_\n" \
  "s_add_u32 s58, s58, 32768\n" \
  "s_addc_u32 s59, s59, 0\n" \
  "global_store_dwordx4 v238, v[248:251], s[58:59] sc1\n" \
  "L16_:\n" \
  "v_mov_b32 v234, 0\n" \
  "v_mov_b32 v236, v237\n" \
  "s_mov_b32 s38, 1\n" \
  "s_lshl_b32 s35, s29, 8\n" \
  "v_lshl_add_u32 v196, v255, 11, s35\n" \
  "s_lshl_b32 s35, s30, 11\n" \
  "v_lshl_add_u32 v198, v255, 8, s35\n" \
  "s_branch LT\n" \
  "LB_t_h:\n" \
  "s_waitcnt lgkmcnt(12)\n" \
  "v_mfma_f32_16x16x32_bf16 v[160:163], v[128:131], v[64:67], 0\n" \
  "v_mfma_f32_16x16x32_bf16 v[160:163], v[132:135], v[68:71], v[160:163]\n" \
  "s_waitcnt lgkmcnt(8)\n" \
  "v_mfma_f32_16x16x32_bf16 v[160:163], v[136:139], v[72:75], v[160:163]\n" \
  "v_mfma_f32_16x16x32_bf16 v[160:163], v[140:143], v[76:79], v[160:163]\n" \
  "s_waitcnt lgkmcnt(0)\n" \
  "v_mfma_f32_16x16x32_bf16 v[164:167], v[156:159], v[76:79], 0\n" \
  "v_mfma_f32_16x16x32_bf16 v[164:167], v[152:155], v[72:75], v[164:167]\n" \
  "v_mfma_f32_16x16x32_bf16 v[164:167], v[148:151], v[68:71], v[164:167]\n" \
  "v_mfma_f32_16x16x32_bf16 v[164:167], v[144:147], v[64:67], v[164:167]\n" \
  "v_mfma_f32_16x16x32_bf16 v[172:175], v[144:147], v[80:83], 0\n" \
  "v_mfma_f32_16x16x32_bf16 v[172:175], v[148:151], v[84:87], v[172:175]\n" \
  "v_mfma_f32_16x16x32_bf16 v[172:175], v[152:155], v[88:91], v[172:175]\n" \
  "v_cvt_pk_bf16_f32 v176, v160, v161\n" \
  "v_cvt_pk_bf16_f32 v177, v162, v163\n" \
  "v_mfma_f32_16x16x32_bf16 v[172:175], v[156:159], v[92:95], v[172:175]\n" \
  "v_mfma_f32_16x16x32_bf16 v[168:171], v[140:143], v[92:95], 0\n" \
  "v_mfma_f32_16x16x32_bf16 v[168:171], v[136:139], v[88:91], v[168:171]\n" \
  "v_cvt_pk_bf16_f32 v178, v164, v165\n" \
  "v_cvt_pk_bf16_f32 v179, v166, v167\n" \
  "v_mfma_f32_16x16x32_bf16 v[168:171], v[132:135], v[84:87], v[168:171]\n" \
  "v_mfma_f32_16x16x32_bf16 v[168:171], v[128:131], v[80:83], v[168:171]\n" \
  "s_waitcnt vmcnt(63)\n" \
  "global_store_dwordx4 v186, v[176:179], s[46:47] offset:1024 sc1\n" \
  "v_mfma_f32_16x16x32_bf16 v[160:163], v[128:131], v[96:99], 0\n" \
  "v_cvt_pk_bf16_f32 v182, v172, v173\n" \
  "v_cvt_pk_bf16_f32 v183, v174, v175\n" \
  "v_mfma_f32_16x16x32_bf16 v[160:163], v[132:135], v[100:103], v[160:163]\n" \
  "v_mfma_f32_16x16x32_bf16 v[160:163], v[136:139], v[104:107], v[160:163]\n" \
  "v_mfma_f32_16x16x32_bf16 v[160:163], v[140:143], v[108:111], v[160:163]\n" \
  "v_cvt_pk_bf16_f32 v180, v168, v169\n" \
  "v_cvt_pk_bf16_f32 v181, v170, v171\n" \
  "v_mfma_f32_16x16x32_bf16 v[164:167], v[156:159], v[108:111], 0\n" \
  "v_mfma_f32_16x16x32_bf16 v[164:167], v[152:155], v[104:107], v[164:167]\n" \
  "v_mfma_f32_16x16x32_bf16 v[164:167], v[148:151], v[100:103], v[164:167]\n" \
  "v_mfma_f32_16x16x32_bf16 v[164:167], v[144:147], v[96:99], v[164:167]\n" \
  "s_waitcnt vmcnt(63)\n" \
  "global_store_dwordx4 v186, v[180:183], s[46:47] offset:1280 sc1\n" \
  "v_mfma_f32_16x16x32_bf16 v[168:171], v[128:131], v[112:115], 0\n" \
  "v_cvt_pk_bf16_f32 v176, v160, v161\n" \
  "v_cvt_pk_bf16_f32 v177, v162, v163\n" \
  "v_mfma_f32_16x16x32_bf16 v[168:171], v[132:135], v[116:119], v[168:171]\n" \
  "v_mfma_f32_16x16x32_bf16 v[168:171], v[136:139], v[120:123], v[168:171]\n" \
  "v_mfma_f32_16x16x32_bf16 v[168:171], v[140:143], v[124:127], v[168:171]\n" \
  "v_cvt_pk_bf16_f32 v178, v164, v165\n" \
  "v_cvt_pk_bf16_f32 v179, v166, v167\n" \
  "v_mfma_f32_16x16x32_bf16 v[172:175], v[156:159], v[124:127], 0\n" \
  "v_mfma_f32_16x16x32_bf16 v[172:175], v[152:155], v[120:123], v[172:175]\n" \
  "v_mfma_f32_16x16x32_bf16 v[172:175], v[148:151], v[116:119], v[172:175]\n" \
  "v_mfma_f32_16x16x32_bf16 v[172:175], v[144:147], v[112:115], v[172:175]\n" \
  "s_waitcnt vmcnt(63)\n" \
  "global_store_dwordx4 v187, v[176:179], s[46:47] offset:0 sc1\n" \
  "v_cvt_pk_bf16_f32 v180, v168, v169\n" \
  "v_cvt_pk_bf16_f32 v181, v170, v171\n" \
  "s_nop 3\n" \
  "v_cvt_pk_bf16_f32 v182, v172, v173\n" \
  "v_cvt_pk_bf16_f32 v183, v174, v175\n" \
  "s_waitcnt vmcnt(63)\n" \
  "global_store_dwordx4 v188, v[180:183], s[46:47] offset:0 sc1\n" \
  "LB_t_e:\n" \
  "s_setprio 2\n" \
  "v_mbcnt_lo_u32_b32 v189, -1, 0\n" \
  "v_mbcnt_hi_u32_b32 v189, -1, v189\n" \
  "v_lshlrev_b32 v188, 4, v189\n" \
  "s_lshl_b32 s35, s28, 12\n" \
  "v_add_u32 v128, s35, v188\n" \
  "v_mov_b32 v129, 1\n" \
  "s_lshr_b32 s35, s30, 4\n" \
  "s_lshl1_add_u32 s35, s29, s35\n" \
  "s_lshl_b32 s35, s35, 6\n" \
  "s_bfe_u32 s37, s30, 0x20002\n" \
  "s_lshl3_add_u32 s35, s37, s35\n" \
  "v_and_b32 v190, 16, v189\n" \
  "v_lshl_add_u32 v190, v190, 1, s35\n" \
  "global_load_dwordx2 v[204:205], v190, s[16:17]\n" \
  "global_load_dwordx2 v[206:207], v190, s[18:19]\n" \
  "v_lshrrev_b32 v180, 1, v189\n" \
  "v_xor_b32 v180, v180, v189\n" \
  "v_and_b32 v180, 8, v180\n" \
  "v_bfe_u32 v181, v189, 2, 1\n" \
  "v_lshlrev_b32_e64 v181, v181, 5\n" \
  "v_lshlrev_b32 v180, v180, v181\n" \
  "v_bfe_u32 v181, v189, 2, 2\n" \
  "v_lshlrev_b32_e64 v181, v181, 1\n" \
  "v_cmp_gt_u32 vcc, 32, v189\n" \
  "v_mov_b32 v191, 0x3f80\n" \
  "v_add_u32 v190, 32000, v188\n" \
  "v_cndmask_b32 v192, v181, v180, vcc\n" \
  "v_bfe_i32 v180, v192, 0, 1\n" \
  "v_and_b32 v180, v180, v191\n" \
  "v_bfe_i32 v181, v192, 1, 1\n" \
  "v_and_b32 v181, v181, v191\n" \
  "v_bfe_i32 v182, v192, 2, 1\n" \
  "v_and_b32 v182, v182, v191\n" \
  "v_bfe_i32 v183, v192, 3, 1\n" \
  "v_and_b32 v183, v183, v191\n" \
  "v_and_b32 v192, 3, v189\n" \
  "v_mul_u32_u24 v192, 0x5555, v192\n" \
  "v_add_u32 v189, 16000, v188\n" \
  "v_add_u32 v191, 48000, v188\n" \
  "s_waitcnt vmcnt(0)\n" \
  "v_lshlrev_b32 v184, 16, v204\n" \
  "v_and_b32 v185, 0xffff0000, v204\n" \
  "v_lshlrev_b32 v186, 16, v205\n" \
  "v_and_b32 v187, 0xffff0000, v205\n" \
  "s_setprio 0\n" \
  "s_barrier\n" \
  "s_cmp_lg_u32 s28, 0\n" \
  "s_cbranch_scc1 LB_nb\n" \
  "s_mov_b32 exec_hi, 0\n" \
  "global_atomic_add v196, v129, s[48:49] offset:3076 sc1\n" \
  "s_mov_b64 exec, -1\n" \
  "LB_nb:\n" \
  "s_bitcmp1_b32 s30, 2\n" \
  "s_cbranch_scc1 LB_w_h\n" \
  "ds_write_b128 v128, v[64:67] offset:0\n" \
  "ds_write_b128 v128, v[68:71] offset:1024\n" \
  "ds_write_b128 v128, v[72:75] offset:2048\n" \
  "ds_write_b128 v128, v[76:79] offset:3072\n" \
  "s_add_u32 s35, s59, 64000\n" \
  "s_add_u32 s54, s54, s35\n" \
  "s_addc_u32 s55, s55, 0\n" \
  "s_add_u32 s58, s59, 0x10000\n" \
  "s_mov_b32 m0, s58\n" \
  "s_add_u32 s35, s58, 0x8000\n" \
  "global_load_lds_dwordx4 v188, s[54:55] sc1\n" \
  "global_load_lds_dwordx4 v188, s[54:55] offset:1024 sc1\n" \
  "ds_write_b128 v128, v[80:83] offset:16384\n" \
  "ds_write_b128 v128, v[84:87] offset:17408\n" \
  "ds_write_b128 v128, v[88:91] offset:18432\n" \
  "ds_write_b128 v128, v[92:95] offset:19456\n" \
  "ds_write_b128 v128, v[96:99] offset:32768\n" \
  "ds_write_b128 v128, v[100:103] offset:33792\n" \
  "ds_write_b128 v128, v[104:107] offset:34816\n" \
  "ds_write_b128 v128, v[108:111] offset:35840\n" \
  "s_add_u32 m0, s58, 0x4000\n" \
  "s_cmp_lt_u32 s35, 0x28000\n" \
  "global_load_lds_dwordx4 v189, s[54:55] sc1\n" \
  "global_load_lds_dwordx4 v189, s[54:55] offset:1024 sc1\n" \
  "s_cselect_b32 s58, s35, s59\n" \
  "s_add_u32 s54, s54, 32000\n" \
  "s_addc_u32 s55, s55, 0\n" \
  "ds_write_b128 v128, v[112:115] offset:49152\n" \
  "ds_write_b128 v128, v[116:119] offset:50176\n" \
  "ds_write_b128 v128, v[120:123] offset:51200\n" \
  "ds_write_b128 v128, v[124:127] offset:52224\n" \
  "s_branch LB_w_e\n" \
  "LB_w_h:\n" \
  "ds_write_b128 v128, v[0:3] offset:0\n" \
  "ds_write_b128 v128, v[4:7] offset:1024\n" \
  "ds_write_b128 v128, v[8:11] offset:2048\n" \
  "ds_write_b128 v128, v[12:15] offset:3072\n" \
  "s_add_u32 s35, s59, 64000\n" \
  "s_add_u32 s54, s54, s35\n" \
  "s_addc_u32 s55, s55, 0\n" \
  "s_add_u32 s58, s59, 0x10000\n" \
  "s_mov_b32 m0, s58\n" \
  "s_add_u32 s35, s58, 0x8000\n" \
  "global_load_lds_dwordx4 v188, s[54:55] sc1\n" \
  "global_load_lds_dwordx4 v188, s[54:55] offset:1024 sc1\n" \
  "ds_write_b128 v128, v[16:19] offset:16384\n" \
  "ds_write_b128 v128, v[20:23] offset:17408\n" \
  "ds_write_b128 v128, v[24:27] offset:18432\n" \
  "ds_write_b128 v128, v[28:31] offset:19456\n" \
  "ds_write_b128 v128, v[32:35] offset:32768\n" \
  "ds_write_b128 v128, v[36:39] offset:33792\n" \
  "ds_write_b128 v128, v[40:43] offset:34816\n" \
  "ds_write_b128 v128, v[44:47] offset:35840\n" \
  "s_add_u32 m0, s58, 0x4000\n" \
  "s_cmp_lt_u32 s35, 0x28000\n" \
  "global_load_lds_dwordx4 v189, s[54:55] sc1\n" \
  "global_load_lds_dwordx4 v189, s[54:55] offset:1024 sc1\n" \
  "s_cselect_b32 s58, s35, s59\n" \
  "s_add_u32 s54, s54, 32000\n" \
  "s_addc_u32 s55, s55, 0\n" \
  "ds_write_b128 v128, v[48:51] offset:49152\n" \
  "ds_write_b128 v128, v[52:55] offset:50176\n" \
  "ds_write_b128 v128, v[56:59] offset:51200\n" \
  "ds_write_b128 v128, v[60:63] offset:52224\n" \
  "LB_w_e:\n" \
  "s_waitcnt lgkmcnt(0)\n" \
  "s_mov_b32 s51, 7\n" \
  "s_setprio 0\n" \
  "s_barrier\n" \
  "LG_start:\n" \
  "v_lshlrev_b32_sdwa v64, 4, v208 dst_sel:DWORD dst_unused:UNUSED_PAD src0_sel:DWORD src1_sel:WORD_0\n" \
  "ds_read_b128 v[68:71], v64\n" \
  "v_mov_b64 v[0:1], v[184:185]\n" \
  "v_lshlrev_b32_sdwa v65, 4, v208 dst_sel:DWORD dst_unused:UNUSED_PAD src0_sel:DWORD src1_sel:WORD_1\n" \
  "ds_read_b128 v[72:75], v65\n" \
  "v_mov_b64 v[2:3], v[186:187]\n" \
  "v_lshlrev_b32_sdwa v66, 4, v209 dst_sel:DWORD dst_unused:UNUSED_PAD src0_sel:DWORD src1_sel:WORD_0\n" \
  "ds_read_b128 v[76:79], v66\n" \
  "v_mov_b64 v[4:5], v[184:185]\n" \
  "v_lshlrev_b32_sdwa v67, 4, v209 dst_sel:DWORD dst_unused:UNUSED_PAD src0_sel:DWORD src1_sel:WORD_1\n" \
  "ds_read_b128 v[80:83], v67\n" \
  "v_mov_b64 v[6:7], v[186:187]\n" \
  "s_mov_b32 m0, s58\n" \
  "s_add_u32 s35, s58, 0x8000\n" \
  "global_load_lds_dwordx4 v188, s[54:55] sc1\n" \
  "global_load_lds_dwordx4 v188, s[54:55] offset:1024 sc1\n" \
  "v_lshlrev_b32_sdwa v64, 4, v210 dst_sel:DWORD dst_unused:UNUSED_PAD src0_sel:DWORD src1_sel:WORD_0\n" \
  "ds_read_b128 v[84:87], v64\n" \
  "v_mov_b64 v[8:9], v[184:185]\n" \
  "v_lshlrev_b32_sdwa v65, 4, v210 dst_sel:DWORD dst_unused:UNUSED_PAD src0_sel:DWORD src1_sel:WORD_1\n" \
  "ds_read_b128 v[88:91], v65\n" \
  "v_mov_b64 v[10:11], v[186:187]\n" \
  "v_lshlrev_b32_sdwa v66, 4, v211 dst_sel:DWORD dst_unused:UNUSED_PAD src0_sel:DWORD src1_sel:WORD_0\n" \
  "ds_read_b128 v[92:95], v66\n" \
  "v_mov_b64 v[12:13], v[184:185]\n" \
  "v_lshlrev_b32_sdwa v67, 4, v211 dst_sel:DWORD dst_unused:UNUSED_PAD src0_sel:DWORD src1_sel:WORD_1\n" \
  "ds_read_b128 v[96:99], v67\n" \
  "v_mov_b64 v[14:15], v[186:187]\n" \
  "s_add_u32 m0, s58, 0x4000\n" \
  "s_cmp_lt_u32 s35, 0x28000\n" \
  "global_load_lds_dwordx4 v189, s[54:55] sc1\n" \
  "global_load_lds_dwordx4 v189, s[54:55] offset:1024 sc1\n" \
  "s_cselect_b32 s58, s35, s59\n" \
  "v_lshlrev_b32_sdwa v64, 4, v212 dst_sel:DWORD dst_unused:UNUSED_PAD src0_sel:DWORD src1_sel:WORD_0\n" \
  "ds_read_b128 v[100:103], v64\n" \
  "v_mov_b64 v[16:17], v[184:185]\n" \
  "v_lshlrev_b32_sdwa v65, 4, v212 dst_sel:DWORD dst_unused:UNUSED_PAD src0_sel:DWORD src1_sel:WORD_1\n" \
  "ds_read_b128 v[104:107], v65\n" \
  "v_mov_b64 v[18:19], v[186:187]\n" \
  "v_lshlrev_b32_sdwa v66, 4, v213 dst_sel:DWORD dst_unused:UNUSED_PAD src0_sel:DWORD src1_sel:WORD_0\n" \
  "ds_read_b128 v[108:111], v66\n" \
  "v_mov_b64 v[20:21], v[184:185]\n" \
  "v_lshlrev_b32_sdwa v67, 4, v213 dst_sel:DWORD dst_unused:UNUSED_PAD src0_sel:DWORD src1_sel:WORD_1\n" \
  "ds_read_b128 v[112:115], v67\n" \
  "v_mov_b64 v[22:23], v[186:187]\n" \
  "s_waitcnt lgkmcnt(10)\n" \
  "v_smfmac_f32_16x16x64_bf16 v[0:3], v[180:183], v[68:75], v192\n" \
  "v_lshlrev_b32_sdwa v64, 4, v214 dst_sel:DWORD dst_unused:UNUSED_PAD src0_sel:DWORD src1_sel:WORD_0\n" \
  "ds_read_b128 v[68:71], v64\n" \
  "v_mov_b64 v[24:25], v[184:185]\n" \
  "v_lshlrev_b32_sdwa v65, 4, v214 dst_sel:DWORD dst_unused:UNUSED_PAD src0_sel:DWORD src1_sel:WORD_1\n" \
  "ds_read_b128 v[72:75], v65\n" \
  "v_mov_b64 v[26:27], v[186:187]\n" \
  "s_mov_b32 m0, s58\n" \
  "s_add_u32 s35, s58, 0x8000\n" \
  "global_load_lds_dwordx4 v190, s[54:55] sc1\n" \
  "global_load_lds_dwordx4 v190, s[54:55] offset:1024 sc1\n" \
  "s_waitcnt lgkmcnt(10)\n" \
  "v_smfmac_f32_16x16x64_bf16 v[4:7], v[180:183], v[76:83], v192\n" \
  "v_lshlrev_b32_sdwa v66, 4, v215 dst_sel:DWORD dst_unused:UNUSED_PAD src0_sel:DWORD src1_sel:WORD_0\n" \
  "ds_read_b128 v[76:79], v66\n" \
  "v_mov_b64 v[28:29], v[184:185]\n" \
  "v_lshlrev_b32_sdwa v67, 4, v215 dst_sel:DWORD dst_unused:UNUSED_PAD src0_sel:DWORD src1_sel:WORD_1\n" \
  "ds_read_b128 v[80:83], v67\n" \
  "v_mov_b64 v[30:31], v[186:187]\n" \
  "s_waitcnt lgkmcnt(10)\n" \
  "v_smfmac_f32_16x16x64_bf16 v[8:11], v[180:183], v[84:91], v192\n" \
  "v_lshlrev_b32_sdwa v64, 4, v216 dst_sel:DWORD dst_unused:UNUSED_PAD src0_sel:DWORD src1_sel:WORD_0\n" \
  "ds_read_b128 v[84:87], v64\n" \
  "v_mov_b64 v[32:33], v[184:185]\n" \
  "v_lshlrev_b32_sdwa v65, 4, v216 dst_sel:DWORD dst_unused:UNUSED_PAD src0_sel:DWORD src1_sel:WORD_1\n" \
  "ds_read_b128 v[88:91], v65\n" \
  "v_mov_b64 v[34:35], v[186:187]\n" \
  "s_add_u32 m0, s58, 0x4000\n" \
  "s_cmp_lt_u32 s35, 0x28000\n" \
  "global_load_lds_dwordx4 v191, s[54:55] sc1\n" \
  "global_load_lds_dwordx4 v191, s[54:55] offset:1024 sc1\n" \
  "s_cselect_b32 s58, s35, s59\n" \
  "s_waitcnt lgkmcnt(10)\n" \
  "v_smfmac_f32_16x16x64_bf16 v[12:15], v[180:183], v[92:99], v192\n" \
  "v_lshlrev_b32_sdwa v66, 4, v217 dst_sel:DWORD dst_unused:UNUSED_PAD src0_sel:DWORD src1_sel:WORD_0\n" \
  "ds_read_b128 v[92:95], v66\n" \
  "v_mov_b64 v[36:37], v[184:185]\n" \
  "v_lshlrev_b32_sdwa v67, 4, v217 dst_sel:DWORD dst_unused:UNUSED_PAD src0_sel:DWORD src1_sel:WORD_1\n" \
  "ds_read_b128 v[96:99], v67\n" \
  "v_mov_b64 v[38:39], v[186:187]\n" \
  "s_waitcnt lgkmcnt(10)\n" \
  "v_smfmac_f32_16x16x64_bf16 v[16:19], v[180:183], v[100:107], v192\n" \
  "v_lshlrev_b32_sdwa v64, 4, v218 dst_sel:DWORD dst_unused:UNUSED_PAD src0_sel:DWORD src1_sel:WORD_0\n" \
  "ds_read_b128 v[100:103], v64\n" \
  "v_mov_b64 v[40:41], v[184:185]\n" \
  "v_lshlrev_b32_sdwa v65, 4, v218 dst_sel:DWORD dst_unused:UNUSED_PAD src0_sel:DWORD src1_sel:WORD_1\n" \
  "ds_read_b128 v[104:107], v65\n" \
  "v_mov_b64 v[42:43], v[186:187]\n" \
  "global_load_dwordx4 v[240:243], v188, s[56:57] sc1\n" \
  "global_load_dwordx4 v[244:247], v188, s[56:57] offset:1024 sc1\n" \
  "s_waitcnt lgkmcnt(10)\n" \
  "v_smfmac_f32_16x16x64_bf16 v[20:23], v[180:183], v[108:115], v192\n" \
  "v_lshlrev_b32_sdwa v66, 4, v219 dst_sel:DWORD dst_unused:UNUSED_PAD src0_sel:DWORD src1_sel:WORD_0\n" \
  "ds_read_b128 v[108:111], v66\n" \
  "v_mov_b64 v[44:45], v[184:185]\n" \
  "v_lshlrev_b32_sdwa v67, 4, v219 dst_sel:DWORD dst_unused:UNUSED_PAD src0_sel:DWORD src1_sel:WORD_1\n" \
  "ds_read_b128 v[112:115], v67\n" \
  "v_mov_b64 v[46:47], v[186:187]\n" \
  "s_waitcnt lgkmcnt(10)\n" \
  "v_smfmac_f32_16x16x64_bf16 v[24:27], v[180:183], v[68:75], v192\n" \
  "v_lshlrev_b32_sdwa v64, 4, v220 dst_sel:DWORD dst_unused:UNUSED_PAD src0_sel:DWORD src1_sel:WORD_0\n" \
  "ds_read_b128 v[68:71], v64\n" \
  "v_mov_b64 v[48:49], v[184:185]\n" \
  "v_lshlrev_b32_sdwa v65, 4, v220 dst_sel:DWORD dst_unused:UNUSED_PAD src0_sel:DWORD src1_sel:WORD_1\n" \
  "ds_read_b128 v[72:75], v65\n" \
  "v_mov_b64 v[50:51], v[186:187]\n" \
  "global_load_dwordx4 v[248:251], v188, s[56:57] offset:2048 sc1\n" \
  "global_load_dwordx4 v[252:255], v188, s[56:57] offset:3072 sc1\n" \
  "s_waitcnt lgkmcnt(10)\n" \
  "v_smfmac_f32_16x16x64_bf16 v[28:31], v[180:183], v[76:83], v192\n" \
  "v_lshlrev_b32_sdwa v66, 4, v221 dst_sel:DWORD dst_unused:UNUSED_PAD src0_sel:DWORD src1_sel:WORD_0\n" \
  "ds_read_b128 v[76:79], v66\n" \
  "v_mov_b64 v[52:53], v[184:185]\n" \
  "v_lshlrev_b32_sdwa v67, 4, v221 dst_sel:DWORD dst_unused:UNUSED_PAD src0_sel:DWORD src1_sel:WORD_1\n" \
  "ds_read_b128 v[80:83], v67\n" \
  "v_mov_b64 v[54:55], v[186:187]\n" \
  "s_waitcnt lgkmcnt(10)\n" \
  "v_smfmac_f32_16x16x64_bf16 v[32:35], v[180:183], v[84:91], v192\n" \
  "v_lshlrev_b32_sdwa v64, 4, v222 dst_sel:DWORD dst_unused:UNUSED_PAD src0_sel:DWORD src1_sel:WORD_0\n" \
  "ds_read_b128 v[84:87], v64\n" \
  "v_mov_b64 v[56:57], v[184:185]\n" \
  "v_lshlrev_b32_sdwa v65, 4, v222 dst_sel:DWORD dst_unused:UNUSED_PAD src0_sel:DWORD src1_sel:WORD_1\n" \
  "ds_read_b128 v[88:91], v65\n" \
  "v_mov_b64 v[58:59], v[186:187]\n" \
  "s_waitcnt lgkmcnt(10)\n" \
  "v_smfmac_f32_16x16x64_bf16 v[36:39], v[180:183], v[92:99], v192\n" \
  "v_lshlrev_b32_sdwa v66, 4, v223 dst_sel:DWORD dst_unused:UNUSED_PAD src0_sel:DWORD src1_sel:WORD_0\n" \
  "ds_read_b128 v[92:95], v66\n" \
  "v_mov_b64 v[60:61], v[184:185]\n" \
  "v_lshlrev_b32_sdwa v67, 4, v223 dst_sel:DWORD dst_unused:UNUSED_PAD src0_sel:DWORD src1_sel:WORD_1\n" \
  "ds_read_b128 v[96:99], v67\n" \
  "v_mov_b64 v[62:63], v[186:187]\n" \
  "s_add_u32 s56, s56, 32768\n" \
  "s_addc_u32 s57, s57, 0\n" \
  "s_add_u32 s54, s54, 64000\n" \
  "s_addc_u32 s55, s55, 0\n" \
  "s_waitcnt lgkmcnt(10)\n" \
  "v_smfmac_f32_16x16x64_bf16 v[40:43], v[180:183], v[100:107], v192\n" \
  "s_waitcnt lgkmcnt(8)\n" \
  "v_smfmac_f32_16x16x64_bf16 v[44:47], v[180:183], v[108:115], v192\n" \
  "s_waitcnt vmcnt(8)\n" \
  "s_barrier\n" \
  "LG_loop:\n" \
  "v_lshlrev_b32_sdwa v64, 4, v224 dst_sel:DWORD dst_unused:UNUSED_PAD src0_sel:DWORD src1_sel:WORD_0\n" \
  "ds_read_b128 v[100:103], v64\n" \
  "v_lshlrev_b32_sdwa v65, 4, v224 dst_sel:DWORD dst_unused:UNUSED_PAD src0_sel:DWORD src1_sel:WORD_1\n" \
  "ds_read_b128 v[104:107], v65\n" \
  "v_lshlrev_b32_sdwa v66, 4, v225 dst_sel:DWORD dst_unused:UNUSED_PAD src0_sel:DWORD src1_sel:WORD_0\n" \
  "ds_read_b128 v[108:111], v66\n" \
  "v_lshlrev_b32_sdwa v67, 4, v225 dst_sel:DWORD dst_unused:UNUSED_PAD src0_sel:DWORD src1_sel:WORD_1\n" \
  "ds_read_b128 v[112:115], v67\n" \
  "s_waitcnt lgkmcnt(10)\n" \
  "v_smfmac_f32_16x16x64_bf16 v[48:51], v[180:183], v[68:75], v192\n" \
  "s_waitcnt lgkmcnt(8)\n" \
  "v_smfmac_f32_16x16x64_bf16 v[52:55], v[180:183], v[76:83], v192\n" \
  "s_waitcnt lgkmcnt(6)\n" \
  "v_smfmac_f32_16x16x64_bf16 v[56:59], v[180:183], v[84:91], v192\n" \
  "s_waitcnt lgkmcnt(4)\n" \
  "v_smfmac_f32_16x16x64_bf16 v[60:63], v[180:183], v[92:99], v192\n" \
  "s_barrier\n" \
  "s_mov_b32 m0, s58\n" \
  "s_add_u32 s35, s58, 0x8000\n" \
  "global_load_lds_dwordx4 v188, s[54:55] sc1\n" \
  "global_load_lds_dwordx4 v188, s[54:55] offset:1024 sc1\n" \
  "v_lshlrev_b32_sdwa v64, 4, v226 dst_sel:DWORD dst_unused:UNUSED_PAD src0_sel:DWORD src1_sel:WORD_0\n" \
  "ds_read_b128 v[68:71], v64\n" \
  "v_lshlrev_b32_sdwa v65, 4, v226 dst_sel:DWORD dst_unused:UNUSED_PAD src0_sel:DWORD src1_sel:WORD_1\n" \
  "ds_read_b128 v[72:75], v65\n" \
  "v_lshlrev_b32_sdwa v66, 4, v227 dst_sel:DWORD dst_unused:UNUSED_PAD src0_sel:DWORD src1_sel:WORD_0\n" \
  "ds_read_b128 v[76:79], v66\n" \
  "v_lshlrev_b32_sdwa v67, 4, v227 dst_sel:DWORD dst_unused:UNUSED_PAD src0_sel:DWORD src1_sel:WORD_1\n" \
  "ds_read_b128 v[80:83], v67\n" \
  "s_add_u32 m0, s58, 0x4000\n" \
  "s_cmp_lt_u32 s35, 0x28000\n" \
  "global_load_lds_dwordx4 v189, s[54:55] sc1\n" \
  "global_load_lds_dwordx4 v189, s[54:55] offset:1024 sc1\n" \
  "s_cselect_b32 s58, s35, s59\n" \
  "v_lshlrev_b32_sdwa v64, 4, v228 dst_sel:DWORD dst_unused:UNUSED_PAD src0_sel:DWORD src1_sel:WORD_0\n" \
  "ds_read_b128 v[84:87], v64\n" \
  "v_lshlrev_b32_sdwa v65, 4, v228 dst_sel:DWORD dst_unused:UNUSED_PAD src0_sel:DWORD src1_sel:WORD_1\n" \
  "ds_read_b128 v[88:91], v65\n" \
  "v_lshlrev_b32_sdwa v66, 4, v229 dst_sel:DWORD dst_unused:UNUSED_PAD src0_sel:DWORD src1_sel:WORD_0\n" \
  "ds_read_b128 v[92:95], v66\n" \
  "v_lshlrev_b32_sdwa v67, 4, v229 dst_sel:DWORD dst_unused:UNUSED_PAD src0_sel:DWORD src1_sel:WORD_1\n" \
  "ds_read_b128 v[96:99], v67\n" \
  "s_waitcnt lgkmcnt(10)\n" \
  "v_smfmac_f32_16x16x64_bf16 v[0:3], v[180:183], v[100:107], v192\n" \
  "v_lshlrev_b32_sdwa v64, 4, v230 dst_sel:DWORD dst_unused:UNUSED_PAD src0_sel:DWORD src1_sel:WORD_0\n" \
  "ds_read_b128 v[100:103], v64\n" \
  "v_lshlrev_b32_sdwa v65, 4, v230 dst_sel:DWORD dst_unused:UNUSED_PAD src0_sel:DWORD src1_sel:WORD_1\n" \
  "ds_read_b128 v[104:107], v65\n" \
  "s_mov_b32 m0, s58\n" \
  "s_add_u32 s35, s58, 0x8000\n" \
  "global_load_lds_dwordx4 v190, s[54:55] sc1\n" \
  "global_load_lds_dwordx4 v190, s[54:55] offset:1024 sc1\n" \
  "s_waitcnt lgkmcnt(10)\n" \
  "v_smfmac_f32_16x16x64_bf16 v[4:7], v[180:183], v[108:115], v192\n" \
  "v_lshlrev_b32_sdwa v66, 4, v231 dst_sel:DWORD dst_unused:UNUSED_PAD src0_sel:DWORD src1_sel:WORD_0\n" \
  "ds_read_b128 v[108:111], v66\n" \
  "v_lshlrev_b32_sdwa v67, 4, v231 dst_sel:DWORD dst_unused:UNUSED_PAD src0_sel:DWORD src1_sel:WORD_1\n" \
  "ds_read_b128 v[112:115], v67\n" \
  "s_waitcnt lgkmcnt(10)\n" \
  "v_smfmac_f32_16x16x64_bf16 v[8:11], v[180:183], v[68:75], v192\n" \
  "v_lshlrev_b32_sdwa v64, 4, v232 dst_sel:DWORD dst_unused:UNUSED_PAD src0_sel:DWORD src1_sel:WORD_0\n" \
  "ds_read_b128 v[68:71], v64\n" \
  "v_lshlrev_b32_sdwa v65, 4, v232 dst_sel:DWORD dst_unused:UNUSED_PAD src0_sel:DWORD src1_sel:WORD_1\n" \
  "ds_read_b128 v[72:75], v65\n" \
  "s_add_u32 m0, s58, 0x4000\n" \
  "s_cmp_lt_u32 s35, 0x28000\n" \
  "global_load_lds_dwordx4 v191, s[54:55] sc1\n" \
  "global_load_lds_dwordx4 v191, s[54:55] offset:1024 sc1\n" \
  "s_cselect_b32 s58, s35, s59\n" \
  "s_waitcnt lgkmcnt(10)\n" \
  "v_smfmac_f32_16x16x64_bf16 v[12:15], v[180:183], v[76:83], v192\n" \
  "v_lshlrev_b32_sdwa v66, 4, v233 dst_sel:DWORD dst_unused:UNUSED_PAD src0_sel:DWORD src1_sel:WORD_0\n" \
  "ds_read_b128 v[76:79], v66\n" \
  "v_lshlrev_b32_sdwa v67, 4, v233 dst_sel:DWORD dst_unused:UNUSED_PAD src0_sel:DWORD src1_sel:WORD_1\n" \
  "ds_read_b128 v[80:83], v67\n" \
  "s_waitcnt lgkmcnt(10)\n" \
  "v_smfmac_f32_16x16x64_bf16 v[16:19], v[180:183], v[84:91], v192\n" \
  "v_lshlrev_b32_sdwa v64, 4, v234 dst_sel:DWORD dst_unused:UNUSED_PAD src0_sel:DWORD src1_sel:WORD_0\n" \
  "ds_read_b128 v[84:87], v64\n" \
  "v_lshlrev_b32_sdwa v65, 4, v234 dst_sel:DWORD dst_unused:UNUSED_PAD src0_sel:DWORD src1_sel:WORD_1\n" \
  "ds_read_b128 v[88:91], v65\n" \
  "global_load_dwordx4 v[208:211], v188, s[56:57] sc1\n" \
  "global_load_dwordx4 v[212:215], v188, s[56:57] offset:1024 sc1\n" \
  "s_waitcnt lgkmcnt(10)\n" \
  "v_smfmac_f32_16x16x64_bf16 v[20:23], v[180:183], v[92:99], v192\n" \
  "v_lshlrev_b32_sdwa v66, 4, v235 dst_sel:DWORD dst_unused:UNUSED_PAD src0_sel:DWORD src1_sel:WORD_0\n" \
  "ds_read_b128 v[92:95], v66\n" \
  "v_lshlrev_b32_sdwa v67, 4, v235 dst_sel:DWORD dst_unused:UNUSED_PAD src0_sel:DWORD src1_sel:WORD_1\n" \
  "ds_read_b128 v[96:99], v67\n" \
  "s_waitcnt lgkmcnt(10)\n" \
  "v_smfmac_f32_16x16x64_bf16 v[24:27], v[180:183], v[100:107], v192\n" \
  "v_lshlrev_b32_sdwa v64, 4, v236 dst_sel:DWORD dst_unused:UNUSED_PAD src0_sel:DWORD src1_sel:WORD_0\n" \
  "ds_read_b128 v[100:103], v64\n" \
  "v_lshlrev_b32_sdwa v65, 4, v236 dst_sel:DWORD dst_unused:UNUSED_PAD src0_sel:DWORD src1_sel:WORD_1\n" \
  "ds_read_b128 v[104:107], v65\n" \
  "global_load_dwordx4 v[216:219], v188, s[56:57] offset:2048 sc1\n" \
  "global_load_dwordx4 v[220:223], v188, s[56:57] offset:3072 sc1\n" \
  "s_waitcnt lgkmcnt(10)\n" \
  "v_smfmac_f32_16x16x64_bf16 v[28:31], v[180:183], v[108:115], v192\n" \
  "v_lshlrev_b32_sdwa v66, 4, v237 dst_sel:DWORD dst_unused:UNUSED_PAD src0_sel:DWORD src1_sel:WORD_0\n" \
  "ds_read_b128 v[108:111], v66\n" \
  "v_lshlrev_b32_sdwa v67, 4, v237 dst_sel:DWORD dst_unused:UNUSED_PAD src0_sel:DWORD src1_sel:WORD_1\n" \
  "ds_read_b128 v[112:115], v67\n" \
  "s_waitcnt lgkmcnt(10)\n" \
  "v_smfmac_f32_16x16x64_bf16 v[32:35], v[180:183], v[68:75], v192\n" \
  "v_lshlrev_b32_sdwa v64, 4, v238 dst_sel:DWORD dst_unused:UNUSED_PAD src0_sel:DWORD src1_sel:WORD_0\n" \
  "ds_read_b128 v[68:71], v64\n" \
  "v_lshlrev_b32_sdwa v65, 4, v238 dst_sel:DWORD dst_unused:UNUSED_PAD src0_sel:DWORD src1_sel:WORD_1\n" \
  "ds_read_b128 v[72:75], v65\n" \
  "s_waitcnt lgkmcnt(10)\n" \
  "v_smfmac_f32_16x16x64_bf16 v[36:39], v[180:183], v[76:83], v192\n" \
  "v_lshlrev_b32_sdwa v66, 4, v239 dst_sel:DWORD dst_unused:UNUSED_PAD src0_sel:DWORD src1_sel:WORD_0\n" \
  "ds_read_b128 v[76:79], v66\n" \
  "v_lshlrev_b32_sdwa v67, 4, v239 dst_sel:DWORD dst_unused:UNUSED_PAD src0_sel:DWORD src1_sel:WORD_1\n" \
  "ds_read_b128 v[80:83], v67\n" \
  "s_add_u32 s56, s56, 32768\n" \
  "s_addc_u32 s57, s57, 0\n" \
  "s_add_u32 s54, s54, 64000\n" \
  "s_addc_u32 s55, s55, 0\n" \
  "s_waitcnt lgkmcnt(10)\n" \
  "v_smfmac_f32_16x16x64_bf16 v[40:43], v[180:183], v[84:91], v192\n" \
  "s_waitcnt lgkmcnt(8)\n" \
  "v_smfmac_f32_16x16x64_bf16 v[44:47], v[180:183], v[92:99], v192\n" \
  "s_waitcnt vmcnt(8)\n" \
  "s_barrier\n" \
  "v_lshlrev_b32_sdwa v64, 4, v240 dst_sel:DWORD dst_unused:UNUSED_PAD src0_sel:DWORD src1_sel:WORD_0\n" \
  "ds_read_b128 v[84:87], v64\n" \
  "v_lshlrev_b32_sdwa v65, 4, v240 dst_sel:DWORD dst_unused:UNUSED_PAD src0_sel:DWORD src1_sel:WORD_1\n" \
  "ds_read_b128 v[88:91], v65\n" \
  "v_lshlrev_b32_sdwa v66, 4, v241 dst_sel:DWORD dst_unused:UNUSED_PAD src0_sel:DWORD src1_sel:WORD_0\n" \
  "ds_read_b128 v[92:95], v66\n" \
  "v_lshlrev_b32_sdwa v67, 4, v241 dst_sel:DWORD dst_unused:UNUSED_PAD src0_sel:DWORD src1_sel:WORD_1\n" \
  "ds_read_b128 v[96:99], v67\n" \
  "s_waitcnt lgkmcnt(10)\n" \
  "v_smfmac_f32_16x16x64_bf16 v[48:51], v[180:183], v[100:107], v192\n" \
  "s_waitcnt lgkmcnt(8)\n" \
  "v_smfmac_f32_16x16x64_bf16 v[52:55], v[180:183], v[108:115], v192\n" \
  "s_waitcnt lgkmcnt(6)\n" \
  "v_smfmac_f32_16x16x64_bf16 v[56:59], v[180:183], v[68:75], v192\n" \
  "s_waitcnt lgkmcnt(4)\n" \
  "v_smfmac_f32_16x16x64_bf16 v[60:63], v[180:183], v[76:83], v192\n" \
  "s_barrier\n" \
  "s_mov_b32 m0, s58\n" \
  "s_add_u32 s35, s58, 0x8000\n" \
  "global_load_lds_dwordx4 v188, s[54:55] sc1\n" \
  "global_load_lds_dwordx4 v188, s[54:55] offset:1024 sc1\n" \
  "v_lshlrev_b32_sdwa v64, 4, v242 dst_sel:DWORD dst_unused:UNUSED_PAD src0_sel:DWORD src1_sel:WORD_0\n" \
  "ds_read_b128 v[100:103], v64\n" \
  "v_lshlrev_b32_sdwa v65, 4, v242 dst_sel:DWORD dst_unused:UNUSED_PAD src0_sel:DWORD src1_sel:WORD_1\n" \
  "ds_read_b128 v[104:107], v65\n" \
  "v_lshlrev_b32_sdwa v66, 4, v243 dst_sel:DWORD dst_unused:UNUSED_PAD src0_sel:DWORD src1_sel:WORD_0\n" \
  "ds_read_b128 v[108:111], v66\n" \
  "v_lshlrev_b32_sdwa v67, 4, v243 dst_sel:DWORD dst_unused:UNUSED_PAD src0_sel:DWORD src1_sel:WORD_1\n" \
  "ds_read_b128 v[112:115], v67\n" \
  "s_add_u32 m0, s58, 0x4000\n" \
  "s_cmp_lt_u32 s35, 0x28000\n" \
  "global_load_lds_dwordx4 v189, s[54:55] sc1\n" \
  "global_load_lds_dwordx4 v189, s[54:55] offset:1024 sc1\n" \
  "s_cselect_b32 s58, s35, s59\n" \
  "v_lshlrev_b32_sdwa v64, 4, v244 dst_sel:DWORD dst_unused:UNUSED_PAD src0_sel:DWORD src1_sel:WORD_0\n" \
  "ds_read_b128 v[68:71], v64\n" \
  "v_lshlrev_b32_sdwa v65, 4, v244 dst_sel:DWORD dst_unused:UNUSED_PAD src0_sel:DWORD src1_sel:WORD_1\n" \
  "ds_read_b128 v[72:75], v65\n" \
  "v_lshlrev_b32_sdwa v66, 4, v245 dst_sel:DWORD dst_unused:UNUSED_PAD src0_sel:DWORD src1_sel:WORD_0\n" \
  "ds_read_b128 v[76:79], v66\n" \
  "v_lshlrev_b32_sdwa v67, 4, v245 dst_sel:DWORD dst_unused:UNUSED_PAD src0_sel:DWORD src1_sel:WORD_1\n" \
  "ds_read_b128 v[80:83], v67\n" \
  "s_waitcnt lgkmcnt(10)\n" \
  "v_smfmac_f32_16x16x64_bf16 v[0:3], v[180:183], v[84:91], v192\n" \
  "v_lshlrev_b32_sdwa v64, 4, v246 dst_sel:DWORD dst_unused:UNUSED_PAD src0_sel:DWORD src1_sel:WORD_0\n" \
  "ds_read_b128 v[84:87], v64\n" \
  "v_lshlrev_b32_sdwa v65, 4, v246 dst_sel:DWORD dst_unused:UNUSED_PAD src0_sel:DWORD src1_sel:WORD_1\n" \
  "ds_read_b128 v[88:91], v65\n" \
  "s_mov_b32 m0, s58\n" \
  "s_add_u32 s35, s58, 0x8000\n" \
  "global_load_lds_dwordx4 v190, s[54:55] sc1\n" \
  "global_load_lds_dwordx4 v190, s[54:55] offset:1024 sc1\n" \
  "s_waitcnt lgkmcnt(10)\n" \
  "v_smfmac_f32_16x16x64_bf16 v[4:7], v[180:183], v[92:99], v192\n" \
  "v_lshlrev_b32_sdwa v66, 4, v247 dst_sel:DWORD dst_unused:UNUSED_PAD src0_sel:DWORD src1_sel:WORD_0\n" \
  "ds_read_b128 v[92:95], v66\n" \
  "v_lshlrev_b32_sdwa v67, 4, v247 dst_sel:DWORD dst_unused:UNUSED_PAD src0_sel:DWORD src1_sel:WORD_1\n" \
  "ds_read_b128 v[96:99], v67\n" \
  "s_waitcnt lgkmcnt(10)\n" \
  "v_smfmac_f32_16x16x64_bf16 v[8:11], v[180:183], v[100:107], v192\n" \
  "v_lshlrev_b32_sdwa v64, 4, v248 dst_sel:DWORD dst_unused:UNUSED_PAD src0_sel:DWORD src1_sel:WORD_0\n" \
  "ds_read_b128 v[100:103], v64\n" \
  "v_lshlrev_b32_sdwa v65, 4, v248 dst_sel:DWORD dst_unused:UNUSED_PAD src0_sel:DWORD src1_sel:WORD_1\n" \
  "ds_read_b128 v[104:107], v65\n" \
  "s_add_u32 m0, s58, 0x4000\n" \
  "s_cmp_lt_u32 s35, 0x28000\n" \
  "global_load_lds_dwordx4 v191, s[54:55] sc1\n" \
  "global_load_lds_dwordx4 v191, s[54:55] offset:1024 sc1\n" \
  "s_cselect_b32 s58, s35, s59\n" \
  "s_waitcnt lgkmcnt(10)\n" \
  "v_smfmac_f32_16x16x64_bf16 v[12:15], v[180:183], v[108:115], v192\n" \
  "v_lshlrev_b32_sdwa v66, 4, v249 dst_sel:DWORD dst_unused:UNUSED_PAD src0_sel:DWORD src1_sel:WORD_0\n" \
  "ds_read_b128 v[108:111], v66\n" \
  "v_lshlrev_b32_sdwa v67, 4, v249 dst_sel:DWORD dst_unused:UNUSED_PAD src0_sel:DWORD src1_sel:WORD_1\n" \
  "ds_read_b128 v[112:115], v67\n" \
  "s_waitcnt lgkmcnt(10)\n" \
  "v_smfmac_f32_16x16x64_bf16 v[16:19], v[180:183], v[68:75], v192\n" \
  "v_lshlrev_b32_sdwa v64, 4, v250 dst_sel:DWORD dst_unused:UNUSED_PAD src0_sel:DWORD src1_sel:WORD_0\n" \
  "ds_read_b128 v[68:71], v64\n" \
  "v_lshlrev_b32_sdwa v65, 4, v250 dst_sel:DWORD dst_unused:UNUSED_PAD src0_sel:DWORD src1_sel:WORD_1\n" \
  "ds_read_b128 v[72:75], v65\n" \
  "global_load_dwordx4 v[224:227], v188, s[56:57] sc1\n" \
  "global_load_dwordx4 v[228:231], v188, s[56:57] offset:1024 sc1\n" \
  "s_waitcnt lgkmcnt(10)\n" \
  "v_smfmac_f32_16x16x64_bf16 v[20:23], v[180:183], v[76:83], v192\n" \
  "v_lshlrev_b32_sdwa v66, 4, v251 dst_sel:DWORD dst_unused:UNUSED_PAD src0_sel:DWORD src1_sel:WORD_0\n" \
  "ds_read_b128 v[76:79], v66\n" \
  "v_lshlrev_b32_sdwa v67, 4, v251 dst_sel:DWORD dst_unused:UNUSED_PAD src0_sel:DWORD src1_sel:WORD_1\n" \
  "ds_read_b128 v[80:83], v67\n" \
  "s_waitcnt lgkmcnt(10)\n" \
  "v_smfmac_f32_16x16x64_bf16 v[24:27], v[180:183], v[84:91], v192\n" \
  "v_lshlrev_b32_sdwa v64, 4, v252 dst_sel:DWORD dst_unused:UNUSED_PAD src0_sel:DWORD src1_sel:WORD_0\n" \
  "ds_read_b128 v[84:87], v64\n" \
  "v_lshlrev_b32_sdwa v65, 4, v252 dst_sel:DWORD dst_unused:UNUSED_PAD src0_sel:DWORD src1_sel:WORD_1\n" \
  "ds_read_b128 v[88:91], v65\n" \
  "global_load_dwordx4 v[232:235], v188, s[56:57] offset:2048 sc1\n" \
  "global_load_dwordx4 v[236:239], v188, s[56:57] offset:3072 sc1\n" \
  "s_waitcnt lgkmcnt(10)\n" \
  "v_smfmac_f32_16x16x64_bf16 v[28:31], v[180:183], v[92:99], v192\n" \
  "v_lshlrev_b32_sdwa v66, 4, v253 dst_sel:DWORD dst_unused:UNUSED_PAD src0_sel:DWORD src1_sel:WORD_0\n" \
  "ds_read_b128 v[92:95], v66\n" \
  "v_lshlrev_b32_sdwa v67, 4, v253 dst_sel:DWORD dst_unused:UNUSED_PAD src0_sel:DWORD src1_sel:WORD_1\n" \
  "ds_read_b128 v[96:99], v67\n" \
  "s_waitcnt lgkmcnt(10)\n" \
  "v_smfmac_f32_16x16x64_bf16 v[32:35], v[180:183], v[100:107], v192\n" \
  "v_lshlrev_b32_sdwa v64, 4, v254 dst_sel:DWORD dst_unused:UNUSED_PAD src0_sel:DWORD src1_sel:WORD_0\n" \
  "ds_read_b128 v[100:103], v64\n" \
  "v_lshlrev_b32_sdwa v65, 4, v254 dst_sel:DWORD dst_unused:UNUSED_PAD src0_sel:DWORD src1_sel:WORD_1\n" \
  "ds_read_b128 v[104:107], v65\n" \
  "s_waitcnt lgkmcnt(10)\n" \
  "v_smfmac_f32_16x16x64_bf16 v[36:39], v[180:183], v[108:115], v192\n" \
  "v_lshlrev_b32_sdwa v66, 4, v255 dst_sel:DWORD dst_unused:UNUSED_PAD src0_sel:DWORD src1_sel:WORD_0\n" \
  "ds_read_b128 v[108:111], v66\n" \
  "v_lshlrev_b32_sdwa v67, 4, v255 dst_sel:DWORD dst_unused:UNUSED_PAD src0_sel:DWORD src1_sel:WORD_1\n" \
  "ds_read_b128 v[112:115], v67\n" \
  "s_add_u32 s56, s56, 32768\n" \
  "s_addc_u32 s57, s57, 0\n" \
  "s_add_u32 s54, s54, 64000\n" \
  "s_addc_u32 s55, s55, 0\n" \
  "s_waitcnt lgkmcnt(10)\n" \
  "v_smfmac_f32_16x16x64_bf16 v[40:43], v[180:183], v[68:75], v192\n" \
  "s_waitcnt lgkmcnt(8)\n" \
  "v_smfmac_f32_16x16x64_bf16 v[44:47], v[180:183], v[76:83], v192\n" \
  "s_waitcnt vmcnt(8)\n" \
  "s_barrier\n" \
  "s_lshl3_add_u32 s36, s27, s51\n" \
  "s_cmp_eq_u32 s36, 37\n" \
  "s_cbranch_scc1 LG_pol\n" \
  "LG_rt0:\n" \
  "v_lshlrev_b32_sdwa v64, 4, v208 dst_sel:DWORD dst_unused:UNUSED_PAD src0_sel:DWORD src1_sel:WORD_0\n" \
  "ds_read_b128 v[68:71], v64\n" \
  "v_lshlrev_b32_sdwa v65, 4, v208 dst_sel:DWORD dst_unused:UNUSED_PAD src0_sel:DWORD src1_sel:WORD_1\n" \
  "ds_read_b128 v[72:75], v65\n" \
  "v_lshlrev_b32_sdwa v66, 4, v209 dst_sel:DWORD dst_unused:UNUSED_PAD src0_sel:DWORD src1_sel:WORD_0\n" \
  "ds_read_b128 v[76:79], v66\n" \
  "v_lshlrev_b32_sdwa v67, 4, v209 dst_sel:DWORD dst_unused:UNUSED_PAD src0_sel:DWORD src1_sel:WORD_1\n" \
  "ds_read_b128 v[80:83], v67\n" \
  "s_waitcnt lgkmcnt(10)\n" \
  "v_smfmac_f32_16x16x64_bf16 v[48:51], v[180:183], v[84:91], v192\n" \
  "s_waitcnt lgkmcnt(8)\n" \
  "v_smfmac_f32_16x16x64_bf16 v[52:55], v[180:183], v[92:99], v192\n" \
  "s_waitcnt lgkmcnt(6)\n" \
  "v_smfmac_f32_16x16x64_bf16 v[56:59], v[180:183], v[100:107], v192\n" \
  "s_waitcnt lgkmcnt(4)\n" \
  "v_smfmac_f32_16x16x64_bf16 v[60:63], v[180:183], v[108:115], v192\n" \
  "s_barrier\n" \
  "s_mov_b32 m0, s58\n" \
  "s_add_u32 s35, s58, 0x8000\n" \
  "global_load_lds_dwordx4 v188, s[54:55] sc1\n" \
  "global_load_lds_dwordx4 v188, s[54:55] offset:1024 sc1\n" \
  "v_lshlrev_b32_sdwa v64, 4, v210 dst_sel:DWORD dst_unused:UNUSED_PAD src0_sel:DWORD src1_sel:WORD_0\n" \
  "ds_read_b128 v[84:87], v64\n" \
  "v_lshlrev_b32_sdwa v65, 4, v210 dst_sel:DWORD dst_unused:UNUSED_PAD src0_sel:DWORD src1_sel:WORD_1\n" \
  "ds_read_b128 v[88:91], v65\n" \
  "v_lshlrev_b32_sdwa v66, 4, v211 dst_sel:DWORD dst_unused:UNUSED_PAD src0_sel:DWORD src1_sel:WORD_0\n" \
  "ds_read_b128 v[92:95], v66\n" \
  "v_lshlrev_b32_sdwa v67, 4, v211 dst_sel:DWORD dst_unused:UNUSED_PAD src0_sel:DWORD src1_sel:WORD_1\n" \
  "ds_read_b128 v[96:99], v67\n" \
  "s_add_u32 m0, s58, 0x4000\n" \
  "s_cmp_lt_u32 s35, 0x28000\n" \
  "global_load_lds_dwordx4 v189, s[54:55] sc1\n" \
  "global_load_lds_dwordx4 v189, s[54:55] offset:1024 sc1\n" \
  "s_cselect_b32 s58, s35, s59\n" \
  "v_lshlrev_b32_sdwa v64, 4, v212 dst_sel:DWORD dst_unused:UNUSED_PAD src0_sel:DWORD src1_sel:WORD_0\n" \
  "ds_read_b128 v[100:103], v64\n" \
  "v_lshlrev_b32_sdwa v65, 4, v212 dst_sel:DWORD dst_unused:UNUSED_PAD src0_sel:DWORD src1_sel:WORD_1\n" \
  "ds_read_b128 v[104:107], v65\n" \
  "v_lshlrev_b32_sdwa v66, 4, v213 dst_sel:DWORD dst_unused:UNUSED_PAD src0_sel:DWORD src1_sel:WORD_0\n" \
  "ds_read_b128 v[108:111], v66\n" \
  "v_lshlrev_b32_sdwa v67, 4, v213 dst_sel:DWORD dst_unused:UNUSED_PAD src0_sel:DWORD src1_sel:WORD_1\n" \
  "ds_read_b128 v[112:115], v67\n" \
  "s_waitcnt lgkmcnt(10)\n" \
  "v_smfmac_f32_16x16x64_bf16 v[0:3], v[180:183], v[68:75], v192\n" \
  "v_lshlrev_b32_sdwa v64, 4, v214 dst_sel:DWORD dst_unused:UNUSED_PAD src0_sel:DWORD src1_sel:WORD_0\n" \
  "ds_read_b128 v[68:71], v64\n" \
  "v_lshlrev_b32_sdwa v65, 4, v214 dst_sel:DWORD dst_unused:UNUSED_PAD src0_sel:DWORD src1_sel:WORD_1\n" \
  "ds_read_b128 v[72:75], v65\n" \
  "s_mov_b32 m0, s58\n" \
  "s_add_u32 s35, s58, 0x8000\n" \
  "global_load_lds_dwordx4 v190, s[54:55] sc1\n" \
  "global_load_lds_dwordx4 v190, s[54:55] offset:1024 sc1\n" \
  "s_waitcnt lgkmcnt(10)\n" \
  "v_smfmac_f32_16x16x64_bf16 v[4:7], v[180:183], v[76:83], v192\n" \
  "v_lshlrev_b32_sdwa v66, 4, v215 dst_sel:DWORD dst_unused:UNUSED_PAD src0_sel:DWORD src1_sel:WORD_0\n" \
  "ds_read_b128 v[76:79], v66\n" \
  "v_lshlrev_b32_sdwa v67, 4, v215 dst_sel:DWORD dst_unused:UNUSED_PAD src0_sel:DWORD src1_sel:WORD_1\n" \
  "ds_read_b128 v[80:83], v67\n" \
  "s_waitcnt lgkmcnt(10)\n" \
  "v_smfmac_f32_16x16x64_bf16 v[8:11], v[180:183], v[84:91], v192\n" \
  "v_lshlrev_b32_sdwa v64, 4, v216 dst_sel:DWORD dst_unused:UNUSED_PAD src0_sel:DWORD src1_sel:WORD_0\n" \
  "ds_read_b128 v[84:87], v64\n" \
  "v_lshlrev_b32_sdwa v65, 4, v216 dst_sel:DWORD dst_unused:UNUSED_PAD src0_sel:DWORD src1_sel:WORD_1\n" \
  "ds_read_b128 v[88:91], v65\n" \
  "s_add_u32 m0, s58, 0x4000\n" \
  "s_cmp_lt_u32 s35, 0x28000\n" \
  "global_load_lds_dwordx4 v191, s[54:55] sc1\n" \
  "global_load_lds_dwordx4 v191, s[54:55] offset:1024 sc1\n" \
  "s_cselect_b32 s58, s35, s59\n" \
  "s_waitcnt lgkmcnt(10)\n" \
  "v_smfmac_f32_16x16x64_bf16 v[12:15], v[180:183], v[92:99], v192\n" \
  "v_lshlrev_b32_sdwa v66, 4, v217 dst_sel:DWORD dst_unused:UNUSED_PAD src0_sel:DWORD src1_sel:WORD_0\n" \
  "ds_read_b128 v[92:95], v66\n" \
  "v_lshlrev_b32_sdwa v67, 4, v217 dst_sel:DWORD dst_unused:UNUSED_PAD src0_sel:DWORD src1_sel:WORD_1\n" \
  "ds_read_b128 v[96:99], v67\n" \
  "s_waitcnt lgkmcnt(10)\n" \
  "v_smfmac_f32_16x16x64_bf16 v[16:19], v[180:183], v[100:107], v192\n" \
  "v_lshlrev_b32_sdwa v64, 4, v218 dst_sel:DWORD dst_unused:UNUSED_PAD src0_sel:DWORD src1_sel:WORD_0\n" \
  "ds_read_b128 v[100:103], v64\n" \
  "v_lshlrev_b32_sdwa v65, 4, v218 dst_sel:DWORD dst_unused:UNUSED_PAD src0_sel:DWORD src1_sel:WORD_1\n" \
  "ds_read_b128 v[104:107], v65\n" \
  "global_load_dwordx4 v[240:243], v188, s[56:57] sc1\n" \
  "global_load_dwordx4 v[244:247], v188, s[56:57] offset:1024 sc1\n" \
  "s_waitcnt lgkmcnt(10)\n" \
  "v_smfmac_f32_16x16x64_bf16 v[20:23], v[180:183], v[108:115], v192\n" \
  "v_lshlrev_b32_sdwa v66, 4, v219 dst_sel:DWORD dst_unused:UNUSED_PAD src0_sel:DWORD src1_sel:WORD_0\n" \
  "ds_read_b128 v[108:111], v66\n" \
  "v_lshlrev_b32_sdwa v67, 4, v219 dst_sel:DWORD dst_unused:UNUSED_PAD src0_sel:DWORD src1_sel:WORD_1\n" \
  "ds_read_b128 v[112:115], v67\n" \
  "s_waitcnt lgkmcnt(10)\n" \
  "v_smfmac_f32_16x16x64_bf16 v[24:27], v[180:183], v[68:75], v192\n" \
  "v_lshlrev_b32_sdwa v64, 4, v220 dst_sel:DWORD dst_unused:UNUSED_PAD src0_sel:DWORD src1_sel:WORD_0\n" \
  "ds_read_b128 v[68:71], v64\n" \
  "v_lshlrev_b32_sdwa v65, 4, v220 dst_sel:DWORD dst_unused:UNUSED_PAD src0_sel:DWORD src1_sel:WORD_1\n" \
  "ds_read_b128 v[72:75], v65\n" \
  "global_load_dwordx4 v[248:251], v188, s[56:57] offset:2048 sc1\n" \
  "global_load_dwordx4 v[252:255], v188, s[56:57] offset:3072 sc1\n" \
  "s_waitcnt lgkmcnt(10)\n" \
  "v_smfmac_f32_16x16x64_bf16 v[28:31], v[180:183], v[76:83], v192\n" \
  "v_lshlrev_b32_sdwa v66, 4, v221 dst_sel:DWORD dst_unused:UNUSED_PAD src0_sel:DWORD src1_sel:WORD_0\n" \
  "ds_read_b128 v[76:79], v66\n" \
  "v_lshlrev_b32_sdwa v67, 4, v221 dst_sel:DWORD dst_unused:UNUSED_PAD src0_sel:DWORD src1_sel:WORD_1\n" \
  "ds_read_b128 v[80:83], v67\n" \
  "s_waitcnt lgkmcnt(10)\n" \
  "v_smfmac_f32_16x16x64_bf16 v[32:35], v[180:183], v[84:91], v192\n" \
  "v_lshlrev_b32_sdwa v64, 4, v222 dst_sel:DWORD dst_unused:UNUSED_PAD src0_sel:DWORD src1_sel:WORD_0\n" \
  "ds_read_b128 v[84:87], v64\n" \
  "v_lshlrev_b32_sdwa v65, 4, v222 dst_sel:DWORD dst_unused:UNUSED_PAD src0_sel:DWORD src1_sel:WORD_1\n" \
  "ds_read_b128 v[88:91], v65\n" \
  "s_waitcnt lgkmcnt(10)\n" \
  "v_smfmac_f32_16x16x64_bf16 v[36:39], v[180:183], v[92:99], v192\n" \
  "v_lshlrev_b32_sdwa v66, 4, v223 dst_sel:DWORD dst_unused:UNUSED_PAD src0_sel:DWORD src1_sel:WORD_0\n" \
  "ds_read_b128 v[92:95], v66\n" \
  "v_lshlrev_b32_sdwa v67, 4, v223 dst_sel:DWORD dst_unused:UNUSED_PAD src0_sel:DWORD src1_sel:WORD_1\n" \
  "ds_read_b128 v[96:99], v67\n" \
  "s_add_u32 s56, s56, 32768\n" \
  "s_addc_u32 s57, s57, 0\n" \
  "s_add_u32 s54, s54, 64000\n" \
  "s_addc_u32 s55, s55, 0\n" \
  "s_waitcnt lgkmcnt(10)\n" \
  "v_smfmac_f32_16x16x64_bf16 v[40:43], v[180:183], v[100:107], v192\n" \
  "s_waitcnt lgkmcnt(8)\n" \
  "v_smfmac_f32_16x16x64_bf16 v[44:47], v[180:183], v[108:115], v192\n" \
  "s_waitcnt vmcnt(8)\n" \
  "s_cmp_eq_u32 s36, 37\n" \
  "s_cbranch_scc1 LG_chk\n" \
  "LG_ret:\n" \
  "s_sub_u32 s51, s51, 1\n" \
  "s_cmp_lg_u32 s51, 0\n" \
  "s_barrier\n" \
  "s_cbranch_scc1 LG_loop\n" \
  "v_lshlrev_b32_sdwa v64, 4, v224 dst_sel:DWORD dst_unused:UNUSED_PAD src0_sel:DWORD src1_sel:WORD_0\n" \
  "ds_read_b128 v[100:103], v64\n" \
  "v_lshlrev_b32_sdwa v65, 4, v224 dst_sel:DWORD dst_unused:UNUSED_PAD src0_sel:DWORD src1_sel:WORD_1\n" \
  "ds_read_b128 v[104:107], v65\n" \
  "v_lshlrev_b32_sdwa v66, 4, v225 dst_sel:DWORD dst_unused:UNUSED_PAD src0_sel:DWORD src1_sel:WORD_0\n" \
  "ds_read_b128 v[108:111], v66\n" \
  "v_lshlrev_b32_sdwa v67, 4, v225 dst_sel:DWORD dst_unused:UNUSED_PAD src0_sel:DWORD src1_sel:WORD_1\n" \
  "ds_read_b128 v[112:115], v67\n" \
  "s_waitcnt lgkmcnt(10)\n" \
  "v_smfmac_f32_16x16x64_bf16 v[48:51], v[180:183], v[68:75], v192\n" \
  "s_waitcnt lgkmcnt(8)\n" \
  "v_smfmac_f32_16x16x64_bf16 v[52:55], v[180:183], v[76:83], v192\n" \
  "s_waitcnt lgkmcnt(6)\n" \
  "v_smfmac_f32_16x16x64_bf16 v[56:59], v[180:183], v[84:91], v192\n" \
  "s_waitcnt lgkmcnt(4)\n" \
  "v_smfmac_f32_16x16x64_bf16 v[60:63], v[180:183], v[92:99], v192\n" \
  "s_barrier\n" \
  "s_mov_b32 m0, s58\n" \
  "s_add_u32 s35, s58, 0x8000\n" \
  "global_load_lds_dwordx4 v188, s[54:55] sc1\n" \
  "global_load_lds_dwordx4 v188, s[54:55] offset:1024 sc1\n" \
  "v_lshlrev_b32_sdwa v64, 4, v226 dst_sel:DWORD dst_unused:UNUSED_PAD src0_sel:DWORD src1_sel:WORD_0\n" \
  "ds_read_b128 v[68:71], v64\n" \
  "v_lshlrev_b32_sdwa v65, 4, v226 dst_sel:DWORD dst_unused:UNUSED_PAD src0_sel:DWORD src1_sel:WORD_1\n" \
  "ds_read_b128 v[72:75], v65\n" \
  "v_lshlrev_b32_sdwa v66, 4, v227 dst_sel:DWORD dst_unused:UNUSED_PAD src0_sel:DWORD src1_sel:WORD_0\n" \
  "ds_read_b128 v[76:79], v66\n" \
  "v_lshlrev_b32_sdwa v67, 4, v227 dst_sel:DWORD dst_unused:UNUSED_PAD src0_sel:DWORD src1_sel:WORD_1\n" \
  "ds_read_b128 v[80:83], v67\n" \
  "s_add_u32 m0, s58, 0x4000\n" \
  "s_cmp_lt_u32 s35, 0x28000\n" \
  "global_load_lds_dwordx4 v189, s[54:55] sc1\n" \
  "global_load_lds_dwordx4 v189, s[54:55] offset:1024 sc1\n" \
  "s_cselect_b32 s58, s35, s59\n" \
  "v_lshlrev_b32_sdwa v64, 4, v228 dst_sel:DWORD dst_unused:UNUSED_PAD src0_sel:DWORD src1_sel:WORD_0\n" \
  "ds_read_b128 v[84:87], v64\n" \
  "v_lshlrev_b32_sdwa v65, 4, v228 dst_sel:DWORD dst_unused:UNUSED_PAD src0_sel:DWORD src1_sel:WORD_1\n" \
  "ds_read_b128 v[88:91], v65\n" \
  "v_lshlrev_b32_sdwa v66, 4, v229 dst_sel:DWORD dst_unused:UNUSED_PAD src0_sel:DWORD src1_sel:WORD_0\n" \
  "ds_read_b128 v[92:95], v66\n" \
  "v_lshlrev_b32_sdwa v67, 4, v229 dst_sel:DWORD dst_unused:UNUSED_PAD src0_sel:DWORD src1_sel:WORD_1\n" \
  "ds_read_b128 v[96:99], v67\n" \
  "s_waitcnt lgkmcnt(10)\n" \
  "v_smfmac_f32_16x16x64_bf16 v[0:3], v[180:183], v[100:107], v192\n" \
  "v_lshlrev_b32_sdwa v64, 4, v230 dst_sel:DWORD dst_unused:UNUSED_PAD src0_sel:DWORD src1_sel:WORD_0\n" \
  "ds_read_b128 v[100:103], v64\n" \
  "v_lshlrev_b32_sdwa v65, 4, v230 dst_sel:DWORD dst_unused:UNUSED_PAD src0_sel:DWORD src1_sel:WORD_1\n" \
  "ds_read_b128 v[104:107], v65\n" \
  "s_mov_b32 m0, s58\n" \
  "s_add_u32 s35, s58, 0x8000\n" \
  "global_load_lds_dwordx4 v190, s[54:55] sc1\n" \
  "global_load_lds_dwordx4 v190, s[54:55] offset:1024 sc1\n" \
  "s_waitcnt lgkmcnt(10)\n" \
  "v_smfmac_f32_16x16x64_bf16 v[4:7], v[180:183], v[108:115], v192\n" \
  "v_lshlrev_b32_sdwa v66, 4, v231 dst_sel:DWORD dst_unused:UNUSED_PAD src0_sel:DWORD src1_sel:WORD_0\n" \
  "ds_read_b128 v[108:111], v66\n" \
  "v_lshlrev_b32_sdwa v67, 4, v231 dst_sel:DWORD dst_unused:UNUSED_PAD src0_sel:DWORD src1_sel:WORD_1\n" \
  "ds_read_b128 v[112:115], v67\n" \
  "s_waitcnt lgkmcnt(10)\n" \
  "v_smfmac_f32_16x16x64_bf16 v[8:11], v[180:183], v[68:75], v192\n" \
  "v_lshlrev_b32_sdwa v64, 4, v232 dst_sel:DWORD dst_unused:UNUSED_PAD src0_sel:DWORD src1_sel:WORD_0\n" \
  "ds_read_b128 v[68:71], v64\n" \
  "v_lshlrev_b32_sdwa v65, 4, v232 dst_sel:DWORD dst_unused:UNUSED_PAD src0_sel:DWORD src1_sel:WORD_1\n" \
  "ds_read_b128 v[72:75], v65\n" \
  "s_add_u32 m0, s58, 0x4000\n" \
  "s_cmp_lt_u32 s35, 0x28000\n" \
  "global_load_lds_dwordx4 v191, s[54:55] sc1\n" \
  "global_load_lds_dwordx4 v191, s[54:55] offset:1024 sc1\n" \
  "s_cselect_b32 s58, s35, s59\n" \
  "s_waitcnt lgkmcnt(10)\n" \
  "v_smfmac_f32_16x16x64_bf16 v[12:15], v[180:183], v[76:83], v192\n" \
  "v_lshlrev_b32_sdwa v66, 4, v233 dst_sel:DWORD dst_unused:UNUSED_PAD src0_sel:DWORD src1_sel:WORD_0\n" \
  "ds_read_b128 v[76:79], v66\n" \
  "v_lshlrev_b32_sdwa v67, 4, v233 dst_sel:DWORD dst_unused:UNUSED_PAD src0_sel:DWORD src1_sel:WORD_1\n" \
  "ds_read_b128 v[80:83], v67\n" \
  "s_waitcnt lgkmcnt(10)\n" \
  "v_smfmac_f32_16x16x64_bf16 v[16:19], v[180:183], v[84:91], v192\n" \
  "v_lshlrev_b32_sdwa v64, 4, v234 dst_sel:DWORD dst_unused:UNUSED_PAD src0_sel:DWORD src1_sel:WORD_0\n" \
  "ds_read_b128 v[84:87], v64\n" \
  "v_lshlrev_b32_sdwa v65, 4, v234 dst_sel:DWORD dst_unused:UNUSED_PAD src0_sel:DWORD src1_sel:WORD_1\n" \
  "ds_read_b128 v[88:91], v65\n" \
  "global_load_dwordx4 v[208:211], v188, s[56:57] sc1\n" \
  "global_load_dwordx4 v[212:215], v188, s[56:57] offset:1024 sc1\n" \
  "s_waitcnt lgkmcnt(10)\n" \
  "v_smfmac_f32_16x16x64_bf16 v[20:23], v[180:183], v[92:99], v192\n" \
  "v_lshlrev_b32_sdwa v66, 4, v235 dst_sel:DWORD dst_unused:UNUSED_PAD src0_sel:DWORD src1_sel:WORD_0\n" \
  "ds_read_b128 v[92:95], v66\n" \
  "v_lshlrev_b32_sdwa v67, 4, v235 dst_sel:DWORD dst_unused:UNUSED_PAD src0_sel:DWORD src1_sel:WORD_1\n" \
  "ds_read_b128 v[96:99], v67\n" \
  "s_waitcnt lgkmcnt(10)\n" \
  "v_smfmac_f32_16x16x64_bf16 v[24:27], v[180:183], v[100:107], v192\n" \
  "v_lshlrev_b32_sdwa v64, 4, v236 dst_sel:DWORD dst_unused:UNUSED_PAD src0_sel:DWORD src1_sel:WORD_0\n" \
  "ds_read_b128 v[100:103], v64\n" \
  "v_lshlrev_b32_sdwa v65, 4, v236 dst_sel:DWORD dst_unused:UNUSED_PAD src0_sel:DWORD src1_sel:WORD_1\n" \
  "ds_read_b128 v[104:107], v65\n" \
  "global_load_dwordx4 v[216:219], v188, s[56:57] offset:2048 sc1\n" \
  "global_load_dwordx4 v[220:223], v188, s[56:57] offset:3072 sc1\n" \
  "s_waitcnt lgkmcnt(10)\n" \
  "v_smfmac_f32_16x16x64_bf16 v[28:31], v[180:183], v[108:115], v192\n" \
  "v_lshlrev_b32_sdwa v66, 4, v237 dst_sel:DWORD dst_unused:UNUSED_PAD src0_sel:DWORD src1_sel:WORD_0\n" \
  "ds_read_b128 v[108:111], v66\n" \
  "v_lshlrev_b32_sdwa v67, 4, v237 dst_sel:DWORD dst_unused:UNUSED_PAD src0_sel:DWORD src1_sel:WORD_1\n" \
  "ds_read_b128 v[112:115], v67\n" \
  "s_waitcnt lgkmcnt(10)\n" \
  "v_smfmac_f32_16x16x64_bf16 v[32:35], v[180:183], v[68:75], v192\n" \
  "v_lshlrev_b32_sdwa v64, 4, v238 dst_sel:DWORD dst_unused:UNUSED_PAD src0_sel:DWORD src1_sel:WORD_0\n" \
  "ds_read_b128 v[68:71], v64\n" \
  "v_lshlrev_b32_sdwa v65, 4, v238 dst_sel:DWORD dst_unused:UNUSED_PAD src0_sel:DWORD src1_sel:WORD_1\n" \
  "ds_read_b128 v[72:75], v65\n" \
  "s_waitcnt lgkmcnt(10)\n" \
  "v_smfmac_f32_16x16x64_bf16 v[36:39], v[180:183], v[76:83], v192\n" \
  "v_lshlrev_b32_sdwa v66, 4, v239 dst_sel:DWORD dst_unused:UNUSED_PAD src0_sel:DWORD src1_sel:WORD_0\n" \
  "ds_read_b128 v[76:79], v66\n" \
  "v_lshlrev_b32_sdwa v67, 4, v239 dst_sel:DWORD dst_unused:UNUSED_PAD src0_sel:DWORD src1_sel:WORD_1\n" \
  "ds_read_b128 v[80:83], v67\n" \
  "s_add_u32 s56, s56, 32768\n" \
  "s_addc_u32 s57, s57, 0\n" \
  "s_add_u32 s54, s54, 64000\n" \
  "s_addc_u32 s55, s55, 0\n" \
  "s_waitcnt lgkmcnt(10)\n" \
  "v_smfmac_f32_16x16x64_bf16 v[40:43], v[180:183], v[84:91], v192\n" \
  "s_waitcnt lgkmcnt(8)\n" \
  "v_smfmac_f32_16x16x64_bf16 v[44:47], v[180:183], v[92:99], v192\n" \
  "s_waitcnt vmcnt(8)\n" \
  "s_barrier\n" \
  "v_lshlrev_b32_sdwa v64, 4, v240 dst_sel:DWORD dst_unused:UNUSED_PAD src0_sel:DWORD src1_sel:WORD_0\n" \
  "ds_read_b128 v[84:87], v64\n" \
  "v_lshlrev_b32_sdwa v65, 4, v240 dst_sel:DWORD dst_unused:UNUSED_PAD src0_sel:DWORD src1_sel:WORD_1\n" \
  "ds_read_b128 v[88:91], v65\n" \
  "v_lshlrev_b32_sdwa v66, 4, v241 dst_sel:DWORD dst_unused:UNUSED_PAD src0_sel:DWORD src1_sel:WORD_0\n" \
  "ds_read_b128 v[92:95], v66\n" \
  "v_lshlrev_b32_sdwa v67, 4, v241 dst_sel:DWORD dst_unused:UNUSED_PAD src0_sel:DWORD src1_sel:WORD_1\n" \
  "ds_read_b128 v[96:99], v67\n" \
  "s_waitcnt lgkmcnt(10)\n" \
  "v_smfmac_f32_16x16x64_bf16 v[48:51], v[180:183], v[100:107], v192\n" \
  "s_waitcnt lgkmcnt(8)\n" \
  "v_smfmac_f32_16x16x64_bf16 v[52:55], v[180:183], v[108:115], v192\n" \
  "s_waitcnt lgkmcnt(6)\n" \
  "v_smfmac_f32_16x16x64_bf16 v[56:59], v[180:183], v[68:75], v192\n" \
  "s_waitcnt lgkmcnt(4)\n" \
  "v_smfmac_f32_16x16x64_bf16 v[60:63], v[180:183], v[76:83], v192\n" \
  "s_barrier\n" \
  "s_mov_b32 m0, s58\n" \
  "s_add_u32 s35, s58, 0x8000\n" \
  "global_load_lds_dwordx4 v188, s[54:55] sc1\n" \
  "global_load_lds_dwordx4 v188, s[54:55] offset:1024 sc1\n" \
  "s_add_u32 m0, s58, 0x4000\n" \
  "s_cmp_lt_u32 s35, 0x28000\n" \
  "global_load_lds_dwordx4 v189, s[54:55] sc1\n" \
  "global_load_lds_dwordx4 v189, s[54:55] offset:1024 sc1\n" \
  "s_cselect_b32 s58, s35, s59\n" \
  "v_lshlrev_b32 v120, 16, v206\n" \
  "v_and_b32 v121, 0xffff0000, v206\n" \
  "v_lshlrev_b32 v122, 16, v207\n" \
  "v_and_b32 v123, 0xffff0000, v207\n" \
  "v_mov_b32 v124, 0xc0135761\n" \
  "v_mov_b32 v125, 0xc0135761\n" \
  "s_mov_b32 s52, 0xbdd2d3e8\n" \
  "s_mov_b32 s53, s52\n" \
  "s_lshl2_add_u32 s35, s33, s34\n" \
  "s_lshl_b32 s35, s35, 14\n" \
  "s_add_u32 s35, s35, s59\n" \
  "s_add_u32 s35, s35, 105676800\n" \
  "s_add_u32 s40, s24, s35\n" \
  "s_addc_u32 s41, s25, 0\n" \
  "v_lshlrev_b32_sdwa v64, 4, v242 dst_sel:DWORD dst_unused:UNUSED_PAD src0_sel:DWORD src1_sel:WORD_0\n" \
  "ds_read_b128 v[100:103], v64\n" \
  "v_lshlrev_b32_sdwa v65, 4, v242 dst_sel:DWORD dst_unused:UNUSED_PAD src0_sel:DWORD src1_sel:WORD_1\n" \
  "ds_read_b128 v[104:107], v65\n" \
  "v_lshlrev_b32_sdwa v66, 4, v243 dst_sel:DWORD dst_unused:UNUSED_PAD src0_sel:DWORD src1_sel:WORD_0\n" \
  "ds_read_b128 v[108:111], v66\n" \
  "v_lshlrev_b32_sdwa v67, 4, v243 dst_sel:DWORD dst_unused:UNUSED_PAD src0_sel:DWORD src1_sel:WORD_1\n" \
  "ds_read_b128 v[112:115], v67\n" \
  "v_lshlrev_b32_sdwa v64, 4, v244 dst_sel:DWORD dst_unused:UNUSED_PAD src0_sel:DWORD src1_sel:WORD_0\n" \
  "ds_read_b128 v[68:71], v64\n" \
  "v_lshlrev_b32_sdwa v65, 4, v244 dst_sel:DWORD dst_unused:UNUSED_PAD src0_sel:DWORD src1_sel:WORD_1\n" \
  "ds_read_b128 v[72:75], v65\n" \
  "v_lshlrev_b32_sdwa v66, 4, v245 dst_sel:DWORD dst_unused:UNUSED_PAD src0_sel:DWORD src1_sel:WORD_0\n" \
  "ds_read_b128 v[76:79], v66\n" \
  "v_lshlrev_b32_sdwa v67, 4, v245 dst_sel:DWORD dst_unused:UNUSED_PAD src0_sel:DWORD src1_sel:WORD_1\n" \
  "ds_read_b128 v[80:83], v67\n" \
  "s_waitcnt lgkmcnt(10)\n" \
  "v_smfmac_f32_16x16x64_bf16 v[0:3], v[180:183], v[84:91], v192\n" \
  "v_lshlrev_b32_sdwa v64, 4, v246 dst_sel:DWORD dst_unused:UNUSED_PAD src0_sel:DWORD src1_sel:WORD_0\n" \
  "ds_read_b128 v[84:87], v64\n" \
  "v_lshlrev_b32_sdwa v65, 4, v246 dst_sel:DWORD dst_unused:UNUSED_PAD src0_sel:DWORD src1_sel:WORD_1\n" \
  "ds_read_b128 v[88:91], v65\n" \
  "s_waitcnt lgkmcnt(10)\n" \
  "v_smfmac_f32_16x16x64_bf16 v[4:7], v[180:183], v[92:99], v192\n" \
  "v_lshlrev_b32_sdwa v66, 4, v247 dst_sel:DWORD dst_unused:UNUSED_PAD src0_sel:DWORD src1_sel:WORD_0\n" \
  "ds_read_b128 v[92:95], v66\n" \
  "v_lshlrev_b32_sdwa v67, 4, v247 dst_sel:DWORD dst_unused:UNUSED_PAD src0_sel:DWORD src1_sel:WORD_1\n" \
  "ds_read_b128 v[96:99], v67\n" \
  "s_waitcnt lgkmcnt(10)\n" \
  "v_smfmac_f32_16x16x64_bf16 v[8:11], v[180:183], v[100:107], v192\n" \
  "v_lshlrev_b32_sdwa v64, 4, v248 dst_sel:DWORD dst_unused:UNUSED_PAD src0_sel:DWORD src1_sel:WORD_0\n" \
  "ds_read_b128 v[100:103], v64\n" \
  "v_lshlrev_b32_sdwa v65, 4, v248 dst_sel:DWORD dst_unused:UNUSED_PAD src0_sel:DWORD src1_sel:WORD_1\n" \
  "ds_read_b128 v[104:107], v65\n" \
  "s_waitcnt lgkmcnt(10)\n" \
  "v_smfmac_f32_16x16x64_bf16 v[12:15], v[180:183], v[108:115], v192\n" \
  "v_lshlrev_b32_sdwa v66, 4, v249 dst_sel:DWORD dst_unused:UNUSED_PAD src0_sel:DWORD src1_sel:WORD_0\n" \
  "ds_read_b128 v[108:111], v66\n" \
  "v_lshlrev_b32_sdwa v67, 4, v249 dst_sel:DWORD dst_unused:UNUSED_PAD src0_sel:DWORD src1_sel:WORD_1\n" \
  "ds_read_b128 v[112:115], v67\n" \
  "s_waitcnt vmcnt(0)\n" \
  "s_barrier\n" \
  "s_waitcnt lgkmcnt(10)\n" \
  "v_smfmac_f32_16x16x64_bf16 v[16:19], v[180:183], v[68:75], v192\n" \
  "v_lshlrev_b32_sdwa v64, 4, v208 dst_sel:DWORD dst_unused:UNUSED_PAD src0_sel:DWORD src1_sel:WORD_0\n" \
  "ds_read_b128 v[68:71], v64\n" \
  "v_lshlrev_b32_sdwa v65, 4, v208 dst_sel:DWORD dst_unused:UNUSED_PAD src0_sel:DWORD src1_sel:WORD_1\n" \
  "ds_read_b128 v[72:75], v65\n" \
  "s_waitcnt lgkmcnt(10)\n" \
  "v_smfmac_f32_16x16x64_bf16 v[20:23], v[180:183], v[76:83], v192\n" \
  "v_lshlrev_b32_sdwa v66, 4, v209 dst_sel:DWORD dst_unused:UNUSED_PAD src0_sel:DWORD src1_sel:WORD_0\n" \
  "ds_read_b128 v[76:79], v66\n" \
  "v_lshlrev_b32_sdwa v67, 4, v209 dst_sel:DWORD dst_unused:UNUSED_PAD src0_sel:DWORD src1_sel:WORD_1\n" \
  "ds_read_b128 v[80:83], v67\n" \
  "s_waitcnt lgkmcnt(10)\n" \
  "v_smfmac_f32_16x16x64_bf16 v[24:27], v[180:183], v[84:91], v192\n" \
  "v_lshlrev_b32_sdwa v64, 4, v210 dst_sel:DWORD dst_unused:UNUSED_PAD src0_sel:DWORD src1_sel:WORD_0\n" \
  "ds_read_b128 v[84:87], v64\n" \
  "v_lshlrev_b32_sdwa v65, 4, v210 dst_sel:DWORD dst_unused:UNUSED_PAD src0_sel:DWORD src1_sel:WORD_1\n" \
  "ds_read_b128 v[88:91], v65\n" \
  "s_waitcnt lgkmcnt(10)\n" \
  "v_smfmac_f32_16x16x64_bf16 v[28:31], v[180:183], v[92:99], v192\n" \
  "v_lshlrev_b32_sdwa v66, 4, v211 dst_sel:DWORD dst_unused:UNUSED_PAD src0_sel:DWORD src1_sel:WORD_0\n" \
  "ds_read_b128 v[92:95], v66\n" \
  "v_lshlrev_b32_sdwa v67, 4, v211 dst_sel:DWORD dst_unused:UNUSED_PAD src0_sel:DWORD src1_sel:WORD_1\n" \
  "ds_read_b128 v[96:99], v67\n" \
  "s_waitcnt lgkmcnt(10)\n" \
  "v_smfmac_f32_16x16x64_bf16 v[32:35], v[180:183], v[100:107], v192\n" \
  "v_lshlrev_b32_sdwa v64, 4, v212 dst_sel:DWORD dst_unused:UNUSED_PAD src0_sel:DWORD src1_sel:WORD_0\n" \
  "ds_read_b128 v[100:103], v64\n" \
  "v_lshlrev_b32_sdwa v65, 4, v212 dst_sel:DWORD dst_unused:UNUSED_PAD src0_sel:DWORD src1_sel:WORD_1\n" \
  "ds_read_b128 v[104:107], v65\n" \
  "s_waitcnt lgkmcnt(10)\n" \
  "v_smfmac_f32_16x16x64_bf16 v[36:39], v[180:183], v[108:115], v192\n" \
  "v_lshlrev_b32_sdwa v66, 4, v213 dst_sel:DWORD dst_unused:UNUSED_PAD src0_sel:DWORD src1_sel:WORD_0\n" \
  "ds_read_b128 v[108:111], v66\n" \
  "v_lshlrev_b32_sdwa v67, 4, v213 dst_sel:DWORD dst_unused:UNUSED_PAD src0_sel:DWORD src1_sel:WORD_1\n" \
  "ds_read_b128 v[112:115], v67\n" \
  "s_waitcnt lgkmcnt(10)\n" \
  "v_smfmac_f32_16x16x64_bf16 v[0:3], v[180:183], v[68:75], v192\n" \
  "v_lshlrev_b32_sdwa v64, 4, v214 dst_sel:DWORD dst_unused:UNUSED_PAD src0_sel:DWORD src1_sel:WORD_0\n" \
  "ds_read_b128 v[68:71], v64\n" \
  "v_lshlrev_b32_sdwa v65, 4, v214 dst_sel:DWORD dst_unused:UNUSED_PAD src0_sel:DWORD src1_sel:WORD_1\n" \
  "ds_read_b128 v[72:75], v65\n" \
  "s_waitcnt lgkmcnt(10)\n" \
  "v_smfmac_f32_16x16x64_bf16 v[4:7], v[180:183], v[76:83], v192\n" \
  "v_lshlrev_b32_sdwa v66, 4, v215 dst_sel:DWORD dst_unused:UNUSED_PAD src0_sel:DWORD src1_sel:WORD_0\n" \
  "ds_read_b128 v[76:79], v66\n" \
  "v_lshlrev_b32_sdwa v67, 4, v215 dst_sel:DWORD dst_unused:UNUSED_PAD src0_sel:DWORD src1_sel:WORD_1\n" \
  "ds_read_b128 v[80:83], v67\n" \
  "v_pk_mul_f32 v[128:129], v[0:1], v[0:1]\n" \
  "v_pk_mul_f32 v[130:131], v[2:3], v[2:3]\n" \
  "v_pk_fma_f32 v[128:129], v[128:129], s[52:53], v[124:125]\n" \
  "v_pk_fma_f32 v[130:131], v[130:131], s[52:53], v[124:125]\n" \
  "v_pk_mul_f32 v[128:129], v[128:129], v[0:1]\n" \
  "v_pk_mul_f32 v[130:131], v[130:131], v[2:3]\n" \
  "v_exp_f32 v128, v128\n" \
  "v_exp_f32 v129, v129\n" \
  "v_pk_mul_f32 v[0:1], v[0:1], v[120:121]\n" \
  "v_exp_f32 v130, v130\n" \
  "v_exp_f32 v131, v131\n" \
  "v_pk_mul_f32 v[2:3], v[2:3], v[122:123]\n" \
  "v_pk_add_f32 v[128:129], v[128:129], 1.0 op_sel_hi:[1,0]\n" \
  "v_pk_add_f32 v[130:131], v[130:131], 1.0 op_sel_hi:[1,0]\n" \
  "v_rcp_f32 v128, v128\n" \
  "v_rcp_f32 v129, v129\n" \
  "s_waitcnt lgkmcnt(10)\n" \
  "v_smfmac_f32_16x16x64_bf16 v[8:11], v[180:183], v[84:91], v192\n" \
  "v_lshlrev_b32_sdwa v64, 4, v216 dst_sel:DWORD dst_unused:UNUSED_PAD src0_sel:DWORD src1_sel:WORD_0\n" \
  "ds_read_b128 v[84:87], v64\n" \
  "v_lshlrev_b32_sdwa v65, 4, v216 dst_sel:DWORD dst_unused:UNUSED_PAD src0_sel:DWORD src1_sel:WORD_1\n" \
  "ds_read_b128 v[88:91], v65\n" \
  "v_rcp_f32 v130, v130\n" \
  "v_rcp_f32 v131, v131\n" \
  "v_pk_mul_f32 v[0:1], v[0:1], v[128:129]\n" \
  "v_pk_fma_f32 v[0:1], v[2:3], v[130:131], v[0:1]\n" \
  "v_add_f32 v0, v0, v1\n" \
  "v_pk_mul_f32 v[132:133], v[4:5], v[4:5]\n" \
  "v_pk_mul_f32 v[134:135], v[6:7], v[6:7]\n" \
  "v_pk_fma_f32 v[132:133], v[132:133], s[52:53], v[124:125]\n" \
  "v_pk_fma_f32 v[134:135], v[134:135], s[52:53], v[124:125]\n" \
  "v_pk_mul_f32 v[132:133], v[132:133], v[4:5]\n" \
  "v_pk_mul_f32 v[134:135], v[134:135], v[6:7]\n" \
  "v_exp_f32 v132, v132\n" \
  "v_exp_f32 v133, v133\n" \
  "v_pk_mul_f32 v[4:5], v[4:5], v[120:121]\n" \
  "v_exp_f32 v134, v134\n" \
  "v_exp_f32 v135, v135\n" \
  "s_waitcnt lgkmcnt(10)\n" \
  "v_smfmac_f32_16x16x64_bf16 v[12:15], v[180:183], v[92:99], v192\n" \
  "v_lshlrev_b32_sdwa v66, 4, v217 dst_sel:DWORD dst_unused:UNUSED_PAD src0_sel:DWORD src1_sel:WORD_0\n" \
  "ds_read_b128 v[92:95], v66\n" \
  "v_lshlrev_b32_sdwa v67, 4, v217 dst_sel:DWORD dst_unused:UNUSED_PAD src0_sel:DWORD src1_sel:WORD_1\n" \
  "ds_read_b128 v[96:99], v67\n" \
  "v_pk_mul_f32 v[6:7], v[6:7], v[122:123]\n" \
  "v_pk_add_f32 v[132:133], v[132:133], 1.0 op_sel_hi:[1,0]\n" \
  "v_pk_add_f32 v[134:135], v[134:135], 1.0 op_sel_hi:[1,0]\n" \
  "v_rcp_f32 v132, v132\n" \
  "v_rcp_f32 v133, v133\n" \
  "v_rcp_f32 v134, v134\n" \
  "v_rcp_f32 v135, v135\n" \
  "v_pk_mul_f32 v[4:5], v[4:5], v[132:133]\n" \
  "v_pk_fma_f32 v[4:5], v[6:7], v[134:135], v[4:5]\n" \
  "v_add_f32 v4, v4, v5\n" \
  "v_pk_mul_f32 v[136:137], v[8:9], v[8:9]\n" \
  "v_pk_mul_f32 v[138:139], v[10:11], v[10:11]\n" \
  "v_pk_fma_f32 v[136:137], v[136:137], s[52:53], v[124:125]\n" \
  "v_pk_fma_f32 v[138:139], v[138:139], s[52:53], v[124:125]\n" \
  "v_pk_mul_f32 v[136:137], v[136:137], v[8:9]\n" \
  "v_pk_mul_f32 v[138:139], v[138:139], v[10:11]\n" \
  "s_waitcnt lgkmcnt(10)\n" \
  "v_smfmac_f32_16x16x64_bf16 v[16:19], v[180:183], v[100:107], v192\n" \
  "v_lshlrev_b32_sdwa v64, 4, v250 dst_sel:DWORD dst_unused:UNUSED_PAD src0_sel:DWORD src1_sel:WORD_0\n" \
  "ds_read_b128 v[100:103], v64\n" \
  "v_lshlrev_b32_sdwa v65, 4, v250 dst_sel:DWORD dst_unused:UNUSED_PAD src0_sel:DWORD src1_sel:WORD_1\n" \
  "ds_read_b128 v[104:107], v65\n" \
  "v_exp_f32 v136, v136\n" \
  "v_exp_f32 v137, v137\n" \
  "v_pk_mul_f32 v[8:9], v[8:9], v[120:121]\n" \
  "v_exp_f32 v138, v138\n" \
  "v_exp_f32 v139, v139\n" \
  "v_pk_mul_f32 v[10:11], v[10:11], v[122:123]\n" \
  "v_pk_add_f32 v[136:137], v[136:137], 1.0 op_sel_hi:[1,0]\n" \
  "v_pk_add_f32 v[138:139], v[138:139], 1.0 op_sel_hi:[1,0]\n" \
  "v_rcp_f32 v136, v136\n" \
  "v_rcp_f32 v137, v137\n" \
  "v_rcp_f32 v138, v138\n" \
  "v_rcp_f32 v139, v139\n" \
  "v_pk_mul_f32 v[8:9], v[8:9], v[136:137]\n" \
  "v_pk_fma_f32 v[8:9], v[10:11], v[138:139], v[8:9]\n" \
  "v_add_f32 v8, v8, v9\n" \
  "v_pk_mul_f32 v[140:141], v[12:13], v[12:13]\n" \
  "s_waitcnt lgkmcnt(10)\n" \
  "v_smfmac_f32_16x16x64_bf16 v[20:23], v[180:183], v[108:115], v192\n" \
  "v_lshlrev_b32_sdwa v66, 4, v218 dst_sel:DWORD dst_unused:UNUSED_PAD src0_sel:DWORD src1_sel:WORD_0\n" \
  "ds_read_b128 v[108:111], v66\n" \
  "v_lshlrev_b32_sdwa v67, 4, v218 dst_sel:DWORD dst_unused:UNUSED_PAD src0_sel:DWORD src1_sel:WORD_1\n" \
  "ds_read_b128 v[112:115], v67\n" \
  "v_pk_mul_f32 v[142:143], v[14:15], v[14:15]\n" \
  "v_pk_fma_f32 v[140:141], v[140:141], s[52:53], v[124:125]\n" \
  "v_pk_fma_f32 v[142:143], v[142:143], s[52:53], v[124:125]\n" \
  "v_pk_mul_f32 v[140:141], v[140:141], v[12:13]\n" \
  "v_pk_mul_f32 v[142:143], v[142:143], v[14:15]\n" \
  "v_exp_f32 v140, v140\n" \
  "v_exp_f32 v141, v141\n" \
  "v_pk_mul_f32 v[12:13], v[12:13], v[120:121]\n" \
  "v_exp_f32 v142, v142\n" \
  "v_exp_f32 v143, v143\n" \
  "v_pk_mul_f32 v[14:15], v[14:15], v[122:123]\n" \
  "v_pk_add_f32 v[140:141], v[140:141], 1.0 op_sel_hi:[1,0]\n" \
  "v_pk_add_f32 v[142:143], v[142:143], 1.0 op_sel_hi:[1,0]\n" \
  "v_rcp_f32 v140, v140\n" \
  "v_rcp_f32 v141, v141\n" \
  "v_rcp_f32 v142, v142\n" \
  "s_waitcnt lgkmcnt(10)\n" \
  "v_smfmac_f32_16x16x64_bf16 v[24:27], v[180:183], v[68:75], v192\n" \
  "v_lshlrev_b32_sdwa v64, 4, v251 dst_sel:DWORD dst_unused:UNUSED_PAD src0_sel:DWORD src1_sel:WORD_0\n" \
  "ds_read_b128 v[68:71], v64\n" \
  "v_lshlrev_b32_sdwa v65, 4, v251 dst_sel:DWORD dst_unused:UNUSED_PAD src0_sel:DWORD src1_sel:WORD_1\n" \
  "ds_read_b128 v[72:75], v65\n" \
  "v_rcp_f32 v143, v143\n" \
  "v_pk_mul_f32 v[12:13], v[12:13], v[140:141]\n" \
  "v_pk_fma_f32 v[12:13], v[14:15], v[142:143], v[12:13]\n" \
  "v_add_f32 v12, v12, v13\n" \
  "v_pk_mul_f32 v[128:129], v[16:17], v[16:17]\n" \
  "v_pk_mul_f32 v[130:131], v[18:19], v[18:19]\n" \
  "v_pk_fma_f32 v[128:129], v[128:129], s[52:53], v[124:125]\n" \
  "v_pk_fma_f32 v[130:131], v[130:131], s[52:53], v[124:125]\n" \
  "v_pk_mul_f32 v[128:129], v[128:129], v[16:17]\n" \
  "v_pk_mul_f32 v[130:131], v[130:131], v[18:19]\n" \
  "v_exp_f32 v128, v128\n" \
  "v_exp_f32 v129, v129\n" \
  "v_pk_mul_f32 v[16:17], v[16:17], v[120:121]\n" \
  "v_exp_f32 v130, v130\n" \
  "v_exp_f32 v131, v131\n" \
  "v_pk_mul_f32 v[18:19], v[18:19], v[122:123]\n" \
  "s_waitcnt lgkmcnt(10)\n" \
  "v_smfmac_f32_16x16x64_bf16 v[28:31], v[180:183], v[76:83], v192\n" \
  "v_lshlrev_b32_sdwa v66, 4, v219 dst_sel:DWORD dst_unused:UNUSED_PAD src0_sel:DWORD src1_sel:WORD_0\n" \
  "ds_read_b128 v[76:79], v66\n" \
  "v_lshlrev_b32_sdwa v67, 4, v219 dst_sel:DWORD dst_unused:UNUSED_PAD src0_sel:DWORD src1_sel:WORD_1\n" \
  "ds_read_b128 v[80:83], v67\n" \
  "v_pk_add_f32 v[128:129], v[128:129], 1.0 op_sel_hi:[1,0]\n" \
  "v_pk_add_f32 v[130:131], v[130:131], 1.0 op_sel_hi:[1,0]\n" \
  "v_rcp_f32 v128, v128\n" \
  "v_rcp_f32 v129, v129\n" \
  "v_rcp_f32 v130, v130\n" \
  "v_rcp_f32 v131, v131\n" \
  "v_pk_mul_f32 v[16:17], v[16:17], v[128:129]\n" \
  "v_pk_fma_f32 v[16:17], v[18:19], v[130:131], v[16:17]\n" \
  "v_add_f32 v16, v16, v17\n" \
  "v_pk_mul_f32 v[132:133], v[20:21], v[20:21]\n" \
  "v_pk_mul_f32 v[134:135], v[22:23], v[22:23]\n" \
  "v_pk_fma_f32 v[132:133], v[132:133], s[52:53], v[124:125]\n" \
  "v_pk_fma_f32 v[134:135], v[134:135], s[52:53], v[124:125]\n" \
  "v_pk_mul_f32 v[132:133], v[132:133], v[20:21]\n" \
  "v_pk_mul_f32 v[134:135], v[134:135], v[22:23]\n" \
  "v_exp_f32 v132, v132\n" \
  "s_waitcnt lgkmcnt(10)\n" \
  "v_smfmac_f32_16x16x64_bf16 v[32:35], v[180:183], v[84:91], v192\n" \
  "v_lshlrev_b32_sdwa v64, 4, v252 dst_sel:DWORD dst_unused:UNUSED_PAD src0_sel:DWORD src1_sel:WORD_0\n" \
  "ds_read_b128 v[84:87], v64\n" \
  "v_lshlrev_b32_sdwa v65, 4, v252 dst_sel:DWORD dst_unused:UNUSED_PAD src0_sel:DWORD src1_sel:WORD_1\n" \
  "ds_read_b128 v[88:91], v65\n" \
  "v_exp_f32 v133, v133\n" \
  "v_pk_mul_f32 v[20:21], v[20:21], v[120:121]\n" \
  "v_exp_f32 v134, v134\n" \
  "v_exp_f32 v135, v135\n" \
  "v_pk_mul_f32 v[22:23], v[22:23], v[122:123]\n" \
  "v_pk_add_f32 v[132:133], v[132:133], 1.0 op_sel_hi:[1,0]\n" \
  "v_pk_add_f32 v[134:135], v[134:135], 1.0 op_sel_hi:[1,0]\n" \
  "v_rcp_f32 v132, v132\n" \
  "v_rcp_f32 v133, v133\n" \
  "v_rcp_f32 v134, v134\n" \
  "v_rcp_f32 v135, v135\n" \
  "v_pk_mul_f32 v[20:21], v[20:21], v[132:133]\n" \
  "v_pk_fma_f32 v[20:21], v[22:23], v[134:135], v[20:21]\n" \
  "v_add_f32 v20, v20, v21\n" \
  "v_pk_mul_f32 v[136:137], v[24:25], v[24:25]\n" \
  "v_pk_mul_f32 v[138:139], v[26:27], v[26:27]\n" \
  "s_waitcnt lgkmcnt(10)\n" \
  "v_smfmac_f32_16x16x64_bf16 v[36:39], v[180:183], v[92:99], v192\n" \
  "v_lshlrev_b32_sdwa v66, 4, v220 dst_sel:DWORD dst_unused:UNUSED_PAD src0_sel:DWORD src1_sel:WORD_0\n" \
  "ds_read_b128 v[92:95], v66\n" \
  "v_lshlrev_b32_sdwa v67, 4, v220 dst_sel:DWORD dst_unused:UNUSED_PAD src0_sel:DWORD src1_sel:WORD_1\n" \
  "ds_read_b128 v[96:99], v67\n" \
  "v_pk_fma_f32 v[136:137], v[136:137], s[52:53], v[124:125]\n" \
  "v_pk_fma_f32 v[138:139], v[138:139], s[52:53], v[124:125]\n" \
  "v_pk_mul_f32 v[136:137], v[136:137], v[24:25]\n" \
  "v_pk_mul_f32 v[138:139], v[138:139], v[26:27]\n" \
  "v_exp_f32 v136, v136\n" \
  "v_exp_f32 v137, v137\n" \
  "v_pk_mul_f32 v[24:25], v[24:25], v[120:121]\n" \
  "v_exp_f32 v138, v138\n" \
  "v_exp_f32 v139, v139\n" \
  "v_pk_mul_f32 v[26:27], v[26:27], v[122:123]\n" \
  "v_pk_add_f32 v[136:137], v[136:137], 1.0 op_sel_hi:[1,0]\n" \
  "v_pk_add_f32 v[138:139], v[138:139], 1.0 op_sel_hi:[1,0]\n" \
  "v_rcp_f32 v136, v136\n" \
  "v_rcp_f32 v137, v137\n" \
  "v_rcp_f32 v138, v138\n" \
  "v_rcp_f32 v139, v139\n" \
  "s_waitcnt lgkmcnt(10)\n" \
  "v_smfmac_f32_16x16x64_bf16 v[40:43], v[180:183], v[100:107], v192\n" \
  "v_lshlrev_b32_sdwa v64, 4, v253 dst_sel:DWORD dst_unused:UNUSED_PAD src0_sel:DWORD src1_sel:WORD_0\n" \
  "ds_read_b128 v[100:103], v64\n" \
  "v_lshlrev_b32_sdwa v65, 4, v253 dst_sel:DWORD dst_unused:UNUSED_PAD src0_sel:DWORD src1_sel:WORD_1\n" \
  "ds_read_b128 v[104:107], v65\n" \
  "v_pk_mul_f32 v[24:25], v[24:25], v[136:137]\n" \
  "v_pk_fma_f32 v[24:25], v[26:27], v[138:139], v[24:25]\n" \
  "v_add_f32 v24, v24, v25\n" \
  "v_pk_mul_f32 v[140:141], v[28:29], v[28:29]\n" \
  "v_pk_mul_f32 v[142:143], v[30:31], v[30:31]\n" \
  "v_pk_fma_f32 v[140:141], v[140:141], s[52:53], v[124:125]\n" \
  "v_pk_fma_f32 v[142:143], v[142:143], s[52:53], v[124:125]\n" \
  "v_pk_mul_f32 v[140:141], v[140:141], v[28:29]\n" \
  "v_pk_mul_f32 v[142:143], v[142:143], v[30:31]\n" \
  "v_exp_f32 v140, v140\n" \
  "v_exp_f32 v141, v141\n" \
  "v_pk_mul_f32 v[28:29], v[28:29], v[120:121]\n" \
  "v_exp_f32 v142, v142\n" \
  "v_exp_f32 v143, v143\n" \
  "v_pk_mul_f32 v[30:31], v[30:31], v[122:123]\n" \
  "v_pk_add_f32 v[140:141], v[140:141], 1.0 op_sel_hi:[1,0]\n" \
  "s_waitcnt lgkmcnt(10)\n" \
  "v_smfmac_f32_16x16x64_bf16 v[40:43], v[180:183], v[108:115], v192\n" \
  "v_lshlrev_b32_sdwa v66, 4, v221 dst_sel:DWORD dst_unused:UNUSED_PAD src0_sel:DWORD src1_sel:WORD_0\n" \
  "ds_read_b128 v[108:111], v66\n" \
  "v_lshlrev_b32_sdwa v67, 4, v221 dst_sel:DWORD dst_unused:UNUSED_PAD src0_sel:DWORD src1_sel:WORD_1\n" \
  "ds_read_b128 v[112:115], v67\n" \
  "v_pk_add_f32 v[142:143], v[142:143], 1.0 op_sel_hi:[1,0]\n" \
  "v_rcp_f32 v140, v140\n" \
  "v_rcp_f32 v141, v141\n" \
  "v_rcp_f32 v142, v142\n" \
  "v_rcp_f32 v143, v143\n" \
  "v_pk_mul_f32 v[28:29], v[28:29], v[140:141]\n" \
  "v_pk_fma_f32 v[28:29], v[30:31], v[142:143], v[28:29]\n" \
  "v_add_f32 v28, v28, v29\n" \
  "v_permlane16_swap_b32 v0, v16\n" \
  "v_permlane16_swap_b32 v4, v20\n" \
  "v_permlane16_swap_b32 v8, v24\n" \
  "v_permlane16_swap_b32 v12, v28\n" \
  "v_add_f32 v144, v0, v16\n" \
  "v_add_f32 v145, v4, v20\n" \
  "v_add_f32 v146, v8, v24\n" \
  "v_add_f32 v147, v12, v28\n" \
  "s_waitcnt lgkmcnt(10)\n" \
  "v_smfmac_f32_16x16x64_bf16 v[44:47], v[180:183], v[68:75], v192\n" \
  "v_lshlrev_b32_sdwa v64, 4, v254 dst_sel:DWORD dst_unused:UNUSED_PAD src0_sel:DWORD src1_sel:WORD_0\n" \
  "ds_read_b128 v[68:71], v64\n" \
  "v_lshlrev_b32_sdwa v65, 4, v254 dst_sel:DWORD dst_unused:UNUSED_PAD src0_sel:DWORD src1_sel:WORD_1\n" \
  "ds_read_b128 v[72:75], v65\n" \
  "v_min_u32 v144, -2, v144\n" \
  "v_min_u32 v145, -2, v145\n" \
  "v_min_u32 v146, -2, v146\n" \
  "v_min_u32 v147, -2, v147\n" \
  "global_store_dwordx4 v188, v[144:147], s[40:41] offset:0 sc1\n" \
  "v_pk_mul_f32 v[128:129], v[32:33], v[32:33]\n" \
  "v_pk_mul_f32 v[130:131], v[34:35], v[34:35]\n" \
  "v_pk_fma_f32 v[128:129], v[128:129], s[52:53], v[124:125]\n" \
  "v_pk_fma_f32 v[130:131], v[130:131], s[52:53], v[124:125]\n" \
  "v_pk_mul_f32 v[128:129], v[128:129], v[32:33]\n" \
  "v_pk_mul_f32 v[130:131], v[130:131], v[34:35]\n" \
  "v_exp_f32 v128, v128\n" \
  "v_exp_f32 v129, v129\n" \
  "v_pk_mul_f32 v[32:33], v[32:33], v[120:121]\n" \
  "v_exp_f32 v130, v130\n" \
  "v_exp_f32 v131, v131\n" \
  "s_waitcnt lgkmcnt(10)\n" \
  "v_smfmac_f32_16x16x64_bf16 v[44:47], v[180:183], v[76:83], v192\n" \
  "v_lshlrev_b32_sdwa v66, 4, v222 dst_sel:DWORD dst_unused:UNUSED_PAD src0_sel:DWORD src1_sel:WORD_0\n" \
  "ds_read_b128 v[76:79], v66\n" \
  "v_lshlrev_b32_sdwa v67, 4, v222 dst_sel:DWORD dst_unused:UNUSED_PAD src0_sel:DWORD src1_sel:WORD_1\n" \
  "ds_read_b128 v[80:83], v67\n" \
  "v_pk_mul_f32 v[34:35], v[34:35], v[122:123]\n" \
  "v_pk_add_f32 v[128:129], v[128:129], 1.0 op_sel_hi:[1,0]\n" \
  "v_pk_add_f32 v[130:131], v[130:131], 1.0 op_sel_hi:[1,0]\n" \
  "v_rcp_f32 v128, v128\n" \
  "v_rcp_f32 v129, v129\n" \
  "v_rcp_f32 v130, v130\n" \
  "v_rcp_f32 v131, v131\n" \
  "v_pk_mul_f32 v[32:33], v[32:33], v[128:129]\n" \
  "v_pk_fma_f32 v[32:33], v[34:35], v[130:131], v[32:33]\n" \
  "v_add_f32 v32, v32, v33\n" \
  "v_pk_mul_f32 v[132:133], v[36:37], v[36:37]\n" \
  "v_pk_mul_f32 v[134:135], v[38:39], v[38:39]\n" \
  "v_pk_fma_f32 v[132:133], v[132:133], s[52:53], v[124:125]\n" \
  "v_pk_fma_f32 v[134:135], v[134:135], s[52:53], v[124:125]\n" \
  "v_pk_mul_f32 v[132:133], v[132:133], v[36:37]\n" \
  "v_pk_mul_f32 v[134:135], v[134:135], v[38:39]\n" \
  "s_waitcnt lgkmcnt(10)\n" \
  "v_smfmac_f32_16x16x64_bf16 v[48:51], v[180:183], v[84:91], v192\n" \
  "v_lshlrev_b32_sdwa v64, 4, v255 dst_sel:DWORD dst_unused:UNUSED_PAD src0_sel:DWORD src1_sel:WORD_0\n" \
  "ds_read_b128 v[84:87], v64\n" \
  "v_lshlrev_b32_sdwa v65, 4, v255 dst_sel:DWORD dst_unused:UNUSED_PAD src0_sel:DWORD src1_sel:WORD_1\n" \
  "ds_read_b128 v[88:91], v65\n" \
  "v_exp_f32 v132, v132\n" \
  "v_exp_f32 v133, v133\n" \
  "v_pk_mul_f32 v[36:37], v[36:37], v[120:121]\n" \
  "v_exp_f32 v134, v134\n" \
  "v_exp_f32 v135, v135\n" \
  "v_pk_mul_f32 v[38:39], v[38:39], v[122:123]\n" \
  "v_pk_add_f32 v[132:133], v[132:133], 1.0 op_sel_hi:[1,0]\n" \
  "v_pk_add_f32 v[134:135], v[134:135], 1.0 op_sel_hi:[1,0]\n" \
  "v_rcp_f32 v132, v132\n" \
  "v_rcp_f32 v133, v133\n" \
  "v_rcp_f32 v134, v134\n" \
  "v_rcp_f32 v135, v135\n" \
  "v_pk_mul_f32 v[36:37], v[36:37], v[132:133]\n" \
  "v_pk_fma_f32 v[36:37], v[38:39], v[134:135], v[36:37]\n" \
  "v_add_f32 v36, v36, v37\n" \
  "v_pk_mul_f32 v[136:137], v[40:41], v[40:41]\n" \
  "s_waitcnt lgkmcnt(10)\n" \
  "v_smfmac_f32_16x16x64_bf16 v[48:51], v[180:183], v[92:99], v192\n" \
  "v_lshlrev_b32_sdwa v66, 4, v223 dst_sel:DWORD dst_unused:UNUSED_PAD src0_sel:DWORD src1_sel:WORD_0\n" \
  "ds_read_b128 v[92:95], v66\n" \
  "v_lshlrev_b32_sdwa v67, 4, v223 dst_sel:DWORD dst_unused:UNUSED_PAD src0_sel:DWORD src1_sel:WORD_1\n" \
  "ds_read_b128 v[96:99], v67\n" \
  "v_pk_mul_f32 v[138:139], v[42:43], v[42:43]\n" \
  "v_pk_fma_f32 v[136:137], v[136:137], s[52:53], v[124:125]\n" \
  "v_pk_fma_f32 v[138:139], v[138:139], s[52:53], v[124:125]\n" \
  "v_pk_mul_f32 v[136:137], v[136:137], v[40:41]\n" \
  "v_pk_mul_f32 v[138:139], v[138:139], v[42:43]\n" \
  "v_exp_f32 v136, v136\n" \
  "v_exp_f32 v137, v137\n" \
  "v_pk_mul_f32 v[40:41], v[40:41], v[120:121]\n" \
  "v_exp_f32 v138, v138\n" \
  "v_exp_f32 v139, v139\n" \
  "v_pk_mul_f32 v[42:43], v[42:43], v[122:123]\n" \
  "v_pk_add_f32 v[136:137], v[136:137], 1.0 op_sel_hi:[1,0]\n" \
  "v_pk_add_f32 v[138:139], v[138:139], 1.0 op_sel_hi:[1,0]\n" \
  "v_rcp_f32 v136, v136\n" \
  "v_rcp_f32 v137, v137\n" \
  "v_rcp_f32 v138, v138\n" \
  "s_waitcnt lgkmcnt(10)\n" \
  "v_smfmac_f32_16x16x64_bf16 v[52:55], v[180:183], v[100:107], v192\n" \
  "v_rcp_f32 v139, v139\n" \
  "v_pk_mul_f32 v[40:41], v[40:41], v[136:137]\n" \
  "v_pk_fma_f32 v[40:41], v[42:43], v[138:139], v[40:41]\n" \
  "v_add_f32 v40, v40, v41\n" \
  "v_pk_mul_f32 v[140:141], v[44:45], v[44:45]\n" \
  "v_pk_mul_f32 v[142:143], v[46:47], v[46:47]\n" \
  "v_pk_fma_f32 v[140:141], v[140:141], s[52:53], v[124:125]\n" \
  "v_pk_fma_f32 v[142:143], v[142:143], s[52:53], v[124:125]\n" \
  "v_pk_mul_f32 v[140:141], v[140:141], v[44:45]\n" \
  "v_pk_mul_f32 v[142:143], v[142:143], v[46:47]\n" \
  "v_exp_f32 v140, v140\n" \
  "v_exp_f32 v141, v141\n" \
  "v_pk_mul_f32 v[44:45], v[44:45], v[120:121]\n" \
  "v_exp_f32 v142, v142\n" \
  "v_exp_f32 v143, v143\n" \
  "v_pk_mul_f32 v[46:47], v[46:47], v[122:123]\n" \
  "v_pk_add_f32 v[140:141], v[140:141], 1.0 op_sel_hi:[1,0]\n" \
  "v_pk_add_f32 v[142:143], v[142:143], 1.0 op_sel_hi:[1,0]\n" \
  "v_rcp_f32 v140, v140\n" \
  "v_rcp_f32 v141, v141\n" \
  "v_rcp_f32 v142, v142\n" \
  "v_rcp_f32 v143, v143\n" \
  "v_pk_mul_f32 v[44:45], v[44:45], v[140:141]\n" \
  "v_pk_fma_f32 v[44:45], v[46:47], v[142:143], v[44:45]\n" \
  "v_add_f32 v44, v44, v45\n" \
  "v_pk_mul_f32 v[128:129], v[48:49], v[48:49]\n" \
  "v_pk_mul_f32 v[130:131], v[50:51], v[50:51]\n" \
  "v_pk_fma_f32 v[128:129], v[128:129], s[52:53], v[124:125]\n" \
  "v_pk_fma_f32 v[130:131], v[130:131], s[52:53], v[124:125]\n" \
  "v_pk_mul_f32 v[128:129], v[128:129], v[48:49]\n" \
  "v_pk_mul_f32 v[130:131], v[130:131], v[50:51]\n" \
  "v_exp_f32 v128, v128\n" \
  "s_waitcnt lgkmcnt(8)\n" \
  "v_smfmac_f32_16x16x64_bf16 v[52:55], v[180:183], v[108:115], v192\n" \
  "v_exp_f32 v129, v129\n" \
  "v_pk_mul_f32 v[48:49], v[48:49], v[120:121]\n" \
  "v_exp_f32 v130, v130\n" \
  "v_exp_f32 v131, v131\n" \
  "v_pk_mul_f32 v[50:51], v[50:51], v[122:123]\n" \
  "v_pk_add_f32 v[128:129], v[128:129], 1.0 op_sel_hi:[1,0]\n" \
  "v_pk_add_f32 v[130:131], v[130:131], 1.0 op_sel_hi:[1,0]\n" \
  "v_rcp_f32 v128, v128\n" \
  "v_rcp_f32 v129, v129\n" \
  "v_rcp_f32 v130, v130\n" \
  "v_rcp_f32 v131, v131\n" \
  "v_pk_mul_f32 v[48:49], v[48:49], v[128:129]\n" \
  "v_pk_fma_f32 v[48:49], v[50:51], v[130:131], v[48:49]\n" \
  "v_add_f32 v48, v48, v49\n" \
  "s_waitcnt lgkmcnt(6)\n" \
  "v_smfmac_f32_16x16x64_bf16 v[56:59], v[180:183], v[68:75], v192\n" \
  "v_pk_mul_f32 v[132:133], v[52:53], v[52:53]\n" \
  "v_pk_mul_f32 v[134:135], v[54:55], v[54:55]\n" \
  "v_pk_fma_f32 v[132:133], v[132:133], s[52:53], v[124:125]\n" \
  "v_pk_fma_f32 v[134:135], v[134:135], s[52:53], v[124:125]\n" \
  "v_pk_mul_f32 v[132:133], v[132:133], v[52:53]\n" \
  "v_pk_mul_f32 v[134:135], v[134:135], v[54:55]\n" \
  "v_exp_f32 v132, v132\n" \
  "v_exp_f32 v133, v133\n" \
  "v_pk_mul_f32 v[52:53], v[52:53], v[120:121]\n" \
  "v_exp_f32 v134, v134\n" \
  "v_exp_f32 v135, v135\n" \
  "v_pk_mul_f32 v[54:55], v[54:55], v[122:123]\n" \
  "v_pk_add_f32 v[132:133], v[132:133], 1.0 op_sel_hi:[1,0]\n" \
  "v_pk_add_f32 v[134:135], v[134:135], 1.0 op_sel_hi:[1,0]\n" \
  "v_rcp_f32 v132, v132\n" \
  "v_rcp_f32 v133, v133\n" \
  "v_rcp_f32 v134, v134\n" \
  "v_rcp_f32 v135, v135\n" \
  "v_pk_mul_f32 v[52:53], v[52:53], v[132:133]\n" \
  "v_pk_fma_f32 v[52:53], v[54:55], v[134:135], v[52:53]\n" \
  "v_add_f32 v52, v52, v53\n" \
  "s_waitcnt lgkmcnt(4)\n" \
  "v_smfmac_f32_16x16x64_bf16 v[56:59], v[180:183], v[76:83], v192\n" \
  "s_waitcnt lgkmcnt(2)\n" \
  "v_smfmac_f32_16x16x64_bf16 v[60:63], v[180:183], v[84:91], v192\n" \
  "s_nop 5\n" \
  "v_pk_mul_f32 v[136:137], v[56:57], v[56:57]\n" \
  "v_pk_mul_f32 v[138:139], v[58:59], v[58:59]\n" \
  "v_pk_fma_f32 v[136:137], v[136:137], s[52:53], v[124:125]\n" \
  "v_pk_fma_f32 v[138:139], v[138:139], s[52:53], v[124:125]\n" \
  "v_pk_mul_f32 v[136:137], v[136:137], v[56:57]\n" \
  "v_pk_mul_f32 v[138:139], v[138:139], v[58:59]\n" \
  "v_exp_f32 v136, v136\n" \
  "v_exp_f32 v137, v137\n" \
  "v_pk_mul_f32 v[56:57], v[56:57], v[120:121]\n" \
  "v_exp_f32 v138, v138\n" \
  "v_exp_f32 v139, v139\n" \
  "v_pk_mul_f32 v[58:59], v[58:59], v[122:123]\n" \
  "v_pk_add_f32 v[136:137], v[136:137], 1.0 op_sel_hi:[1,0]\n" \
  "v_pk_add_f32 v[138:139], v[138:139], 1.0 op_sel_hi:[1,0]\n" \
  "v_rcp_f32 v136, v136\n" \
  "v_rcp_f32 v137, v137\n" \
  "v_rcp_f32 v138, v138\n" \
  "v_rcp_f32 v139, v139\n" \
  "v_pk_mul_f32 v[56:57], v[56:57], v[136:137]\n" \
  "v_pk_fma_f32 v[56:57], v[58:59], v[138:139], v[56:57]\n" \
  "v_add_f32 v56, v56, v57\n" \
  "s_waitcnt lgkmcnt(0)\n" \
  "v_smfmac_f32_16x16x64_bf16 v[60:63], v[180:183], v[92:99], v192\n" \
  "s_nop 7\n" \
  "v_pk_mul_f32 v[140:141], v[60:61], v[60:61]\n" \
  "v_pk_mul_f32 v[142:143], v[62:63], v[62:63]\n" \
  "v_pk_fma_f32 v[140:141], v[140:141], s[52:53], v[124:125]\n" \
  "v_pk_fma_f32 v[142:143], v[142:143], s[52:53], v[124:125]\n" \
  "v_pk_mul_f32 v[140:141], v[140:141], v[60:61]\n" \
  "v_pk_mul_f32 v[142:143], v[142:143], v[62:63]\n" \
  "v_exp_f32 v140, v140\n" \
  "v_exp_f32 v141, v141\n" \
  "v_pk_mul_f32 v[60:61], v[60:61], v[120:121]\n" \
  "v_exp_f32 v142, v142\n" \
  "v_exp_f32 v143, v143\n" \
  "v_pk_mul_f32 v[62:63], v[62:63], v[122:123]\n" \
  "v_pk_add_f32 v[140:141], v[140:141], 1.0 op_sel_hi:[1,0]\n" \
  "v_pk_add_f32 v[142:143], v[142:143], 1.0 op_sel_hi:[1,0]\n" \
  "v_rcp_f32 v140, v140\n" \
  "v_rcp_f32 v141, v141\n" \
  "v_rcp_f32 v142, v142\n" \
  "v_rcp_f32 v143, v143\n" \
  "v_pk_mul_f32 v[60:61], v[60:61], v[140:141]\n" \
  "v_pk_fma_f32 v[60:61], v[62:63], v[142:143], v[60:61]\n" \
  "v_add_f32 v60, v60, v61\n" \
  "v_permlane16_swap_b32 v32, v48\n" \
  "v_permlane16_swap_b32 v36, v52\n" \
  "v_permlane16_swap_b32 v40, v56\n" \
  "v_permlane16_swap_b32 v44, v60\n" \
  "v_add_f32 v148, v32, v48\n" \
  "v_add_f32 v149, v36, v52\n" \
  "v_add_f32 v150, v40, v56\n" \
  "v_add_f32 v151, v44, v60\n" \
  "v_min_u32 v148, -2, v148\n" \
  "v_min_u32 v149, -2, v149\n" \
  "v_min_u32 v150, -2, v150\n" \
  "v_min_u32 v151, -2, v151\n" \
  "global_store_dwordx4 v188, v[148:151], s[40:41] offset:1024 sc1\n" \
  "v_lshrrev_b32 v0, 4, v188\n" \
  "v_lshrrev_b32 v1, 4, v0\n" \
  "v_and_b32 v2, 15, v0\n" \
  "v_lshlrev_b32 v2, 4, v2\n" \
  "v_lshl_add_u32 v1, v1, 17, v2\n" \
  "s_lshl_b32 s35, s27, 19\n" \
  "s_lshl_b32 s36, s26, 8\n" \
  "s_add_u32 s35, s35, s36\n" \
  "s_add_u32 s35, s35, 105676800\n" \
  "s_add_u32 s40, s24, s35\n" \
  "s_addc_u32 s41, s25, 0\n" \
  "s_add_u32 s42, s40, 65536\n" \
  "s_addc_u32 s43, s41, 0\n" \
  "s_add_u32 s44, s24, 109871104\n" \
  "s_addc_u32 s45, s25, 0\n" \
  "s_add_u32 s46, s44, 65536\n" \
  "s_addc_u32 s47, s45, 0\n" \
  "s_lshr_b32 s35, s26, 2\n" \
  "s_lshl_b32 s35, s35, 8\n" \
  "s_and_b32 s36, s26, 1\n" \
  "s_lshl_b32 s36, s36, 7\n" \
  "s_add_u32 s35, s35, s36\n" \
  "s_and_b32 s36, s26, 2\n" \
  "s_lshl3_add_u32 s35, s36, s35\n" \
  "v_and_b32 v2, 3, v0\n" \
  "v_lshrrev_b32 v3, 2, v0\n" \
  "v_lshl_add_u32 v2, v2, 5, v3\n" \
  "v_add_u32 v2, s35, v2\n" \
  "v_lshlrev_b32 v3, 2, v2\n" \
  "s_cmp_eq_u32 s27, 0\n" \
  "s_cselect_b32 s35, -1, 0\n" \
  "v_and_b32 v3, s35, v3\n" \
  "v_mov_b32 v4, s6\n" \
  "s_mov_b32 s5, 0x10000\n" \
  "s_barrier\n" \
  "s_sleep 10\n" \
  "LF_poll:\n" \
  "global_load_dwordx4 v[8:11], v1, s[40:41] sc1\n" \
  "global_load_dwordx4 v[12:15], v1, s[42:43] sc1\n" \
  "global_load_dword v16, v3, s[44:45] sc1\n" \
  "global_load_dword v17, v3, s[46:47] sc1\n" \
  "s_waitcnt vmcnt(0)\n" \
  "v_max3_u32 v5, v8, v9, v10\n" \
  "v_max3_u32 v6, v11, v12, v13\n" \
  "v_max3_u32 v5, v5, v14, v15\n" \
  "v_max3_u32 v6, v6, v16, v17\n" \
  "v_max_u32 v5, v5, v6\n" \
  "v_cmp_eq_u32 vcc, -1, v5\n" \
  "s_sub_u32 s5, s5, 1\n" \
  "s_cbranch_scc1 LF_go\n" \
  "s_nop 2\n" \
  "s_cbranch_vccz LF_go\n" \
  "s_mov_b64 exec, vcc\n" \
  "s_sleep 2\n" \
  "s_branch LF_poll\n" \
  "LF_go:\n" \
  "s_mov_b64 exec, -1\n" \
  "v_pk_add_f32 v[8:9], v[8:9], v[12:13]\n" \
  "v_pk_add_f32 v[10:11], v[10:11], v[14:15]\n" \
  "s_nop 0\n" \
  "v_permlane16_swap_b32 v8, v9\n" \
  "v_permlane16_swap_b32 v10, v11\n" \
  "v_add_f32 v8, v8, v9\n" \
  "v_add_f32 v10, v10, v11\n" \
  "s_nop 1\n" \
  "v_permlane32_swap_b32 v8, v10\n" \
  "v_add_f32 v8, v8, v10\n" \
  "v_and_b32 v5, 15, v0\n" \
  "v_lshrrev_b32 v6, 4, v0\n" \
  "v_lshl_add_u32 v5, v5, 2, v6\n" \
  "s_lshl_b32 s35, s27, 8\n" \
  "v_lshl_add_u32 v5, v5, 2, s35\n" \
  "ds_write_b32 v5, v8\n" \
  "s_waitcnt lgkmcnt(0)\n" \
  "s_barrier\n" \
  "s_cmp_lg_u32 s27, 0\n" \
  "s_cbranch_scc1 LF_end\n" \
  "v_lshlrev_b32 v5, 2, v0\n" \
  "ds_read2st64_b32 v[8:9], v5 offset0:0 offset1:1\n" \
  "ds_read2st64_b32 v[10:11], v5 offset0:2 offset1:3\n" \
  "ds_read2st64_b32 v[12:13], v5 offset0:4 offset1:5\n" \
  "ds_read2st64_b32 v[14:15], v5 offset0:6 offset1:7\n" \
  "v_add_f32 v16, v16, v17\n" \
  "v_lshlrev_b32 v4, 16, v4\n" \
  "v_cvt_pk_bf16_f32 v16, v16, v16\n" \
  "v_lshlrev_b32 v2, 1, v2\n" \
  "s_waitcnt lgkmcnt(0)\n" \
  "v_pk_add_f32 v[8:9], v[8:9], v[10:11]\n" \
  "v_pk_add_f32 v[12:13], v[12:13], v[14:15]\n" \
  "v_pk_add_f32 v[8:9], v[8:9], v[12:13]\n" \
  "v_add_f32 v8, v8, v9\n" \
  "v_cvt_pk_bf16_f32 v8, v8, v8\n" \
  "v_lshlrev_b32 v8, 16, v8\n" \
  "v_add_f32 v8, v8, v4\n" \
  "v_cvt_pk_bf16_f32 v8, v8, v8\n" \
  "v_lshlrev_b32 v8, 16, v8\n" \
  "v_lshlrev_b32 v16, 16, v16\n" \
  "v_add_f32 v8, v8, v16\n" \
  "v_cvt_pk_bf16_f32 v8, v8, v8\n" \
  "global_store_short v2, v8, s[22:23]\n" \
  "LF_end:\n" \
  "s_branch LZ_end\n" \
  "LG_pol:\n" \
  "s_mov_b64 exec, 0xff\n" \
  "global_load_dword v200, v198, s[48:49] offset:3076 sc1\n" \
  "s_mov_b64 exec, -1\n" \
  "s_branch LG_rt0\n" \
  "LG_chk:\n" \
  "s_mov_b32 s5, 0x10000\n" \
  "LG_p:\n" \
  "s_mov_b64 exec, 0xff\n" \
  "v_cmp_le_u32 vcc, 32, v200\n" \
  "s_mov_b64 exec, -1\n" \
  "s_sub_u32 s5, s5, 1\n" \
  "s_cselect_b32 s35, 0xff, vcc_lo\n" \
  "s_cmp_eq_u32 s35, 0xff\n" \
  "s_cbranch_scc1 LG_p_x\n" \
  "s_sleep 2\n" \
  "s_mov_b64 exec, 0xff\n" \
  "global_load_dword v200, v198, s[48:49] offset:3076 sc1\n" \
  "s_mov_b64 exec, -1\n" \
  "s_waitcnt vmcnt(0)\n" \
  "s_branch LG_p\n" \
  "LG_p_x:\n" \
  "s_branch LG_ret\n" \
  "LZ_end:\n" \
  "s_waitcnt vmcnt(0) lgkmcnt(0)\n"

#define R10(p, b) p #b "0", p #b "1", p #b "2", p #b "3", p #b "4", p #b "5", p #b "6", p #b "7", p #b "8", p #b "9"
__global__ __launch_bounds__(512, 2) void k1(const int* __restrict__ idx, const unsigned short* __restrict__ table,
                                             const unsigned short* __restrict__ w_p, const unsigned short* __restrict__ w1,
                                             const unsigned short* __restrict__ b1, const unsigned short* __restrict__ w2,
                                             const unsigned short* __restrict__ b2, __hip_bfloat16* __restrict__ out, char* __restrict__ ws) {
  __shared__ __attribute__((aligned(1024))) char smem[163840];
  unsigned tid = threadIdx.x;
  asm volatile(K_ASM
               : "+{v0}"(tid)
               : "{s[8:9]}"(idx), "{s[10:11]}"(table), "{s[12:13]}"(w_p), "{s[14:15]}"(w1), "{s[16:17]}"(b1), "{s[18:19]}"(w2), "{s[20:21]}"(b2),
                 "{s[22:23]}"(out), "{s[24:25]}"(ws), "{s26}"(blockIdx.x), "s"((unsigned)(uintptr_t)smem)
               : "memory", "scc", "vcc", "s3", "s4", "s5", "s6", "s7", "s27", "s28", "s29", "s30", "s31", "s33", "s34", "s35", "s36", "s37", "s38", "s39", R10("s", 4), R10("s", 5), R10("s", 6), R10("s", 7), R10("s", 8), R10("s", 9),
                 "v1", "v2", "v3", "v4", "v5", "v6", "v7", "v8", "v9", R10("v", 1), R10("v", 2), R10("v", 3), R10("v", 4), R10("v", 5), R10("v", 6), R10("v", 7), R10("v", 8), R10("v", 9),
                 R10("v", 10), R10("v", 11), R10("v", 12), R10("v", 13), R10("v", 14), R10("v", 15), R10("v", 16), R10("v", 17), R10("v", 18), R10("v", 19),
                 R10("v", 20), R10("v", 21), R10("v", 22), R10("v", 23), R10("v", 24), "v250", "v251", "v252", "v253", "v254", "v255");
}

extern "C" void kernel_launch(void* const* d_in, const int* in_sizes, int n_in, void* d_out, int out_size, void* d_ws,
                              size_t ws_size, hipStream_t stream) {
  k1<<<dim3(256), dim3(512), 0, stream>>>((const int*)d_in[0], (const unsigned short*)d_in[1], (const unsigned short*)d_in[2], (const unsigned short*)d_in[3],
                                          (const unsigned short*)d_in[4], (const unsigned short*)d_in[5], (const unsigned short*)d_in[6], (__hip_bfloat16*)d_out, (char*)d_ws);
}
```

```cpp
#include <hip/hip_runtime.h>
#include <hip/hip_bf16.h>
#include <stdint.h>

#define K_ASM \
  "LZ_first:\n" \
  "v_readfirstlane_b32 s27, v0\n" \
  "v_and_b32 v255, 63, v0\n" \
  "s_and_b32 s29, s26, 7\n" \
  "s_lshr_b32 s30, s26, 3\n" \
  "s_lshr_b32 s27, s27, 6\n" \
  "s_and_b32 s31, s30, 1\n" \
  "s_and_b32 s28, s27, 3\n" \
  "s_lshr_b32 s35, s30, 1\n" \
  "s_lshl3_add_u32 s7, s35, s29\n" \
  "v_lshrrev_b32 v1, 3, v255\n" \
  "v_and_b32 v2, 7, v255\n" \
  "v_lshlrev_b32 v1, 8, v1\n" \
  "v_lshl_or_b32 v254, v2, 4, v1\n" \
  "s_lshl_b32 s35, s31, 7\n" \
  "s_add_u32 s40, s10, s35\n" \
  "s_addc_u32 s41, s11, 0\n" \
  "s_mul_i32 s36, s7, 37\n" \
  "s_and_b32 s36, s36, 63\n" \
  "s_add_u32 s36, s36, s28\n" \
  "s_cmp_ge_u32 s27, 4\n" \
  "s_cbranch_scc1 LB_start\n" \
  "v_lshlrev_b32 v1, 4, v255\n" \
  "s_lshl_b32 s35, s28, 10\n" \
  "v_add_u32 v1, s35, v1\n" \
  "s_add_u32 m0, s35, 138240\n" \
  "s_nop 0\n" \
  "global_load_lds_dwordx4 v1, s[12:13]\n" \
  "v_add_u32 v1, 0x1000, v1\n" \
  "s_add_u32 m0, m0, 0x1000\n" \
  "s_nop 0\n" \
  "global_load_lds_dwordx4 v1, s[12:13]\n" \
  "v_add_u32 v1, 0x1000, v1\n" \
  "s_add_u32 m0, m0, 0x1000\n" \
  "s_nop 0\n" \
  "global_load_lds_dwordx4 v1, s[12:13]\n" \
  "v_add_u32 v1, 0x1000, v1\n" \
  "s_add_u32 m0, m0, 0x1000\n" \
  "s_nop 0\n" \
  "global_load_lds_dwordx4 v1, s[12:13]\n" \
  "v_add_u32 v1, 0x1000, v1\n" \
  "s_add_u32 m0, m0, 0x1000\n" \
  "v_min_u32 v1, 19984, v1\n" \
  "global_load_lds_dwordx4 v1, s[12:13]\n" \
  "v_and_b32 v252, 15, v255\n" \
  "v_lshrrev_b32 v253, 4, v255\n" \
  "v_mul_u32_u24 v2, 400, v253\n" \
  "v_min_u32 v3, 8, v252\n" \
  "v_lshl_add_u32 v250, v252, 4, v2\n" \
  "v_lshl_add_u32 v251, v3, 4, v2\n" \
  "s_lshl2_add_u32 s35, s7, s28\n" \
  "s_mul_i32 s35, s35, 12800\n" \
  "s_add_u32 s44, s8, s35\n" \
  "s_addc_u32 s45, s9, 0\n" \
  "global_load_dwordx4 v[232:235], v250, s[44:45] offset:0\n" \
  "global_load_dwordx4 v[236:239], v251, s[44:45] offset:256\n" \
  "global_load_dwordx4 v[240:243], v250, s[44:45] offset:1600\n" \
  "global_load_dwordx4 v[244:247], v251, s[44:45] offset:1856\n" \
  "s_setprio 2\n" \
  "v_lshrrev_b32 v1, 2, v252\n" \
  "v_and_b32 v2, 3, v252\n" \
  "v_cmp_eq_u32 vcc, v1, v253\n" \
  "v_mov_b32 v4, 0x3f80\n" \
  "v_and_b32 v13, 1, v253\n" \
  "v_lshlrev_b32 v14, 1, v13\n" \
  "v_xor_b32 v2, v2, v14\n" \
  "v_cndmask_b32 v2, 4, v2, vcc\n" \
  "v_cmp_eq_u32 vcc, 0, v2\n" \
  "s_nop 1\n" \
  "v_cndmask_b32 v216, 0, v4, vcc\n" \
  "v_lshlrev_b32 v220, 16, v216\n" \
  "v_cmp_eq_u32 vcc, 1, v2\n" \
  "s_nop 1\n" \
  "v_cndmask_b32 v217, 0, v4, vcc\n" \
  "v_lshlrev_b32 v221, 16, v217\n" \
  "v_cmp_eq_u32 vcc, 2, v2\n" \
  "s_nop 1\n" \
  "v_cndmask_b32 v218, 0, v4, vcc\n" \
  "v_lshlrev_b32 v222, 16, v218\n" \
  "v_cmp_eq_u32 vcc, 3, v2\n" \
  "s_nop 1\n" \
  "v_cndmask_b32 v219, 0, v4, vcc\n" \
  "v_lshlrev_b32 v223, 16, v219\n" \
  "v_and_b32 v1, 2, v252\n" \
  "v_lshlrev_b32 v1, 3, v1\n" \
  "v_lshlrev_b32 v1, v1, v4\n" \
  "v_and_b32 v2, 0x31, v255\n" \
  "v_mov_b64 v[224:225], 0\n" \
  "v_cmp_eq_u32 vcc, 0, v2\n" \
  "s_nop 1\n" \
  "v_cndmask_b32 v226, 0, v1, vcc\n" \
  "v_cmp_eq_u32 vcc, 1, v2\n" \
  "s_nop 1\n" \
  "v_cndmask_b32 v227, 0, v1, vcc\n" \
  "v_mul_u32_u24 v5, 400, v252\n" \
  "v_add_u32 v5, 138240, v5\n" \
  "v_lshl_add_u32 v6, v253, 4, v5\n" \
  "v_add_u32 v7, 6400, v5\n" \
  "v_add_u32 v8, 6400, v6\n" \
  "v_add_u32 v9, 12800, v5\n" \
  "v_add_u32 v10, 12800, v6\n" \
  "v_bfe_u32 v1, v252, 1, 1\n" \
  "v_and_b32 v2, 1, v252\n" \
  "v_lshl_add_u32 v1, v2, 1, v1\n" \
  "v_mul_u32_u24 v1, 200, v1\n" \
  "v_add_u32 v11, 157440, v1\n" \
  "v_lshl_add_u32 v12, v253, 4, v11\n" \
  "v_lshlrev_b32 v13, 3, v13\n" \
  "v_sub_u32 v16, v5, v13\n" \
  "v_add_u32 v5, v5, v13\n" \
  "v_sub_u32 v17, v6, v13\n" \
  "v_add_u32 v6, v6, v13\n" \
  "v_sub_u32 v18, v7, v13\n" \
  "v_add_u32 v7, v7, v13\n" \
  "v_sub_u32 v19, v8, v13\n" \
  "v_add_u32 v8, v8, v13\n" \
  "v_sub_u32 v20, v9, v13\n" \
  "v_add_u32 v9, v9, v13\n" \
  "v_sub_u32 v21, v10, v13\n" \
  "v_add_u32 v10, v10, v13\n" \
  "v_sub_u32 v22, v11, v13\n" \
  "v_add_u32 v11, v11, v13\n" \
  "v_sub_u32 v23, v12, v13\n" \
  "v_add_u32 v12, v12, v13\n" \
  "s_waitcnt vmcnt(4)\n" \
  "s_barrier\n" \
  "ds_read_b64 v[200:201], v12 offset:128\n" \
  "ds_read_b64 v[202:203], v23 offset:136\n" \
  "ds_read_b64 v[184:185], v9 offset:184\n" \
  "ds_read_b64 v[186:187], v20 offset:192\n" \
  "ds_read_b64 v[188:189], v9 offset:384\n" \
  "ds_read_b64 v[190:191], v20 offset:392\n" \
  "ds_read_b64 v[196:197], v12 offset:64\n" \
  "ds_read_b64 v[198:199], v23 offset:72\n" \
  "ds_read_b64 v[176:177], v7 offset:184\n" \
  "ds_read_b64 v[178:179], v18 offset:192\n" \
  "ds_read_b64 v[180:181], v7 offset:384\n" \
  "ds_read_b64 v[182:183], v18 offset:392\n" \
  "ds_read_b64 v[192:193], v12 offset:0\n" \
  "ds_read_b64 v[194:195], v23 offset:8\n" \
  "s_waitcnt lgkmcnt(12)\n" \
  "v_mfma_f32_16x16x32_bf16 v[44:47], v[200:203], v[216:219], 0\n" \
  "ds_read_b64 v[168:169], v5 offset:184\n" \
  "ds_read_b64 v[170:171], v16 offset:192\n" \
  "s_waitcnt lgkmcnt(12)\n" \
  "v_mfma_f32_16x16x32_bf16 v[44:47], v[224:227], v[184:187], v[44:47]\n" \
  "ds_read_b64 v[172:173], v5 offset:384\n" \
  "ds_read_b64 v[174:175], v16 offset:392\n" \
  "v_mfma_f32_16x16x32_bf16 v[228:231], v[200:203], v[220:223], 0\n" \
  "ds_read_b64 v[96:97], v6 offset:0\n" \
  "ds_read_b64 v[98:99], v17 offset:8\n" \
  "s_waitcnt lgkmcnt(14)\n" \
  "v_mfma_f32_16x16x32_bf16 v[228:231], v[224:227], v[188:191], v[228:231]\n" \
  "ds_read_b64 v[124:125], v8 offset:64\n" \
  "ds_read_b64 v[126:127], v19 offset:72\n" \
  "s_waitcnt lgkmcnt(14)\n" \
  "v_mfma_f32_16x16x32_bf16 v[60:63], v[196:199], v[216:219], 0\n" \
  "ds_read_b64 v[136:137], v8 offset:264\n" \
  "ds_read_b64 v[138:139], v19 offset:272\n" \
  "s_waitcnt lgkmcnt(14)\n" \
  "v_mfma_f32_16x16x32_bf16 v[60:63], v[224:227], v[176:179], v[60:63]\n" \
  "ds_read_b64 v[140:141], v8 offset:328\n" \
  "ds_read_b64 v[142:143], v19 offset:336\n" \
  "v_mfma_f32_16x16x32_bf16 v[212:215], v[196:199], v[220:223], 0\n" \
  "ds_read_b64 v[160:161], v10 offset:264\n" \
  "ds_read_b64 v[162:163], v21 offset:272\n" \
  "s_waitcnt lgkmcnt(15)\n" \
  "v_mfma_f32_16x16x32_bf16 v[212:215], v[224:227], v[180:183], v[212:215]\n" \
  "ds_read_b64 v[116:117], v6 offset:328\n" \
  "ds_read_b64 v[118:119], v17 offset:336\n" \
  "s_waitcnt lgkmcnt(15)\n" \
  "v_mfma_f32_16x16x32_bf16 v[68:71], v[192:195], v[216:219], 0\n" \
  "ds_read_b64 v[144:145], v10 offset:0\n" \
  "ds_read_b64 v[146:147], v21 offset:8\n" \
  "s_waitcnt lgkmcnt(15)\n" \
  "v_mfma_f32_16x16x32_bf16 v[68:71], v[224:227], v[168:171], v[68:71]\n" \
  "ds_read_b64 v[148:149], v10 offset:64\n" \
  "ds_read_b64 v[150:151], v21 offset:72\n" \
  "v_mfma_f32_16x16x32_bf16 v[208:211], v[192:195], v[220:223], 0\n" \
  "ds_read_b64 v[104:105], v6 offset:128\n" \
  "ds_read_b64 v[106:107], v17 offset:136\n" \
  "s_waitcnt lgkmcnt(15)\n" \
  "v_mfma_f32_16x16x32_bf16 v[208:211], v[224:227], v[172:175], v[208:211]\n" \
  "ds_read_b64 v[100:101], v6 offset:64\n" \
  "ds_read_b64 v[102:103], v17 offset:72\n" \
  "v_mfma_f32_16x16x32_bf16 v[92:95], v[96:99], v[216:219], 0\n" \
  "ds_read_b64 v[120:121], v8 offset:0\n" \
  "ds_read_b64 v[122:123], v19 offset:8\n" \
  "s_waitcnt lgkmcnt(15)\n" \
  "v_mfma_f32_16x16x32_bf16 v[24:27], v[124:127], v[220:223], 0\n" \
  "ds_read_b64 v[112:113], v6 offset:264\n" \
  "ds_read_b64 v[114:115], v17 offset:272\n" \
  "v_mfma_f32_16x16x32_bf16 v[24:27], v[216:219], v[136:139], v[24:27]\n" \
  "ds_read_b64 v[156:157], v10 offset:200\n" \
  "ds_read_b64 v[158:159], v21 offset:208\n" \
  "s_waitcnt lgkmcnt(15)\n" \
  "v_mfma_f32_16x16x32_bf16 v[28:31], v[140:143], v[220:223], 0\n" \
  "ds_read_b64 v[132:133], v8 offset:200\n" \
  "ds_read_b64 v[134:135], v19 offset:208\n" \
  "v_mfma_f32_16x16x32_bf16 v[28:31], v[220:223], v[160:163], v[28:31]\n" \
  "ds_read_b64 v[108:109], v6 offset:200\n" \
  "ds_read_b64 v[110:111], v17 offset:208\n" \
  "s_waitcnt lgkmcnt(15)\n" \
  "v_mfma_f32_16x16x32_bf16 v[32:35], v[116:119], v[216:219], 0\n" \
  "ds_read_b64 v[152:153], v10 offset:128\n" \
  "ds_read_b64 v[154:155], v21 offset:136\n" \
  "v_mfma_f32_16x16x32_bf16 v[32:35], v[220:223], v[144:147], v[32:35]\n" \
  "ds_read_b64 v[164:165], v10 offset:328\n" \
  "ds_read_b64 v[166:167], v21 offset:336\n" \
  "v_mfma_f32_16x16x32_bf16 v[36:39], v[140:143], v[216:219], 0\n" \
  "ds_read_b64 v[128:129], v8 offset:128\n" \
  "ds_read_b64 v[130:131], v19 offset:136\n" \
  "s_waitcnt lgkmcnt(15)\n" \
  "v_mfma_f32_16x16x32_bf16 v[36:39], v[220:223], v[148:151], v[36:39]\n" \
  "ds_read_b64 v[204:205], v11 offset:184\n" \
  "ds_read_b64 v[206:207], v22 offset:192\n" \
  "v_mfma_f32_16x16x32_bf16 v[40:43], v[104:107], v[216:219], 0\n" \
  "v_mfma_f32_16x16x32_bf16 v[40:43], v[216:219], v[144:147], v[40:43]\n" \
  "v_mfma_f32_16x16x32_bf16 v[48:51], v[100:103], v[216:219], 0\n" \
  "s_waitcnt lgkmcnt(15)\n" \
  "v_mfma_f32_16x16x32_bf16 v[48:51], v[216:219], v[120:123], v[48:51]\n" \
  "s_waitcnt lgkmcnt(14)\n" \
  "v_mfma_f32_16x16x32_bf16 v[52:55], v[112:115], v[216:219], 0\n" \
  "v_mfma_f32_16x16x32_bf16 v[52:55], v[220:223], v[120:123], v[52:55]\n" \
  "v_mfma_f32_16x16x32_bf16 v[56:59], v[116:119], v[220:223], 0\n" \
  "s_waitcnt lgkmcnt(12)\n" \
  "v_mfma_f32_16x16x32_bf16 v[56:59], v[220:223], v[156:159], v[56:59]\n" \
  "v_mfma_f32_16x16x32_bf16 v[64:67], v[104:107], v[220:223], 0\n" \
  "v_mfma_f32_16x16x32_bf16 v[64:67], v[216:219], v[156:159], v[64:67]\n" \
  "v_mfma_f32_16x16x32_bf16 v[72:75], v[136:139], v[220:223], 0\n" \
  "v_mfma_f32_16x16x32_bf16 v[76:79], v[112:115], v[220:223], 0\n" \
  "s_waitcnt lgkmcnt(10)\n" \
  "v_mfma_f32_16x16x32_bf16 v[76:79], v[220:223], v[132:135], v[76:79]\n" \
  "v_mfma_f32_16x16x32_bf16 v[80:83], v[100:103], v[220:223], 0\n" \
  "v_mfma_f32_16x16x32_bf16 v[80:83], v[216:219], v[132:135], v[80:83]\n" \
  "v_mfma_f32_16x16x32_bf16 v[84:87], v[96:99], v[220:223], 0\n" \
  "s_waitcnt lgkmcnt(8)\n" \
  "v_mfma_f32_16x16x32_bf16 v[84:87], v[216:219], v[108:111], v[84:87]\n" \
  "v_mfma_f32_16x16x32_bf16 v[88:91], v[108:111], v[220:223], 0\n" \
  "v_mfma_f32_16x16x32_bf16 v[0:3], v[124:127], v[216:219], 0\n" \
  "s_waitcnt lgkmcnt(6)\n" \
  "v_mfma_f32_16x16x32_bf16 v[4:7], v[152:155], v[216:219], 0\n" \
  "v_mfma_f32_16x16x32_bf16 v[8:11], v[152:155], v[220:223], 0\n" \
  "s_waitcnt lgkmcnt(4)\n" \
  "v_mfma_f32_16x16x32_bf16 v[8:11], v[216:219], v[164:167], v[8:11]\n" \
  "v_mfma_f32_16x16x32_bf16 v[12:15], v[164:167], v[220:223], 0\n" \
  "s_waitcnt lgkmcnt(2)\n" \
  "v_mfma_f32_16x16x32_bf16 v[16:19], v[128:131], v[220:223], 0\n" \
  "v_mfma_f32_16x16x32_bf16 v[16:19], v[216:219], v[160:163], v[16:19]\n" \
  "v_mfma_f32_16x16x32_bf16 v[20:23], v[128:131], v[216:219], 0\n" \
  "v_mfma_f32_16x16x32_bf16 v[20:23], v[216:219], v[148:151], v[20:23]\n" \
  "s_waitcnt lgkmcnt(0)\n" \
  "v_mov_b32 v254, 163328\n" \
  "v_mov_b32 v255, 1\n" \
  "s_mov_b64 exec, 1\n" \
  "ds_add_u32 v254, v255 offset:16\n" \
  "s_mov_b64 exec, -1\n" \
  "v_mfma_f32_16x16x32_bf16 v[168:171], v[216:219], v[96:99], 0\n" \
  "v_mfma_f32_16x16x32_bf16 v[172:175], v[204:207], v[224:227], 0\n" \
  "v_mfma_f32_16x16x32_bf16 v[176:179], v[224:227], v[204:207], 0\n" \
  "s_mov_b32 exec_lo, 0x0\n" \
  "s_mov_b32 exec_hi, 0xffffffff\n" \
  "v_mov_b64 v[44:45], v[228:229]\n" \
  "v_mov_b64 v[46:47], v[230:231]\n" \
  "v_mov_b64 v[60:61], v[212:213]\n" \
  "v_mov_b64 v[62:63], v[214:215]\n" \
  "v_mov_b64 v[68:69], v[208:209]\n" \
  "v_mov_b64 v[70:71], v[210:211]\n" \
  "s_mov_b32 exec_lo, 0xff00ff00\n" \
  "s_mov_b32 exec_hi, 0x0\n" \
  "v_add_f32 v92, v92, v168\n" \
  "v_add_f32 v93, v93, v169\n" \
  "v_add_f32 v94, v94, v170\n" \
  "v_add_f32 v95, v95, v171\n" \
  "s_mov_b32 exec_lo, 0xf0000f00\n" \
  "s_mov_b32 exec_hi, 0x0\n" \
  "v_sub_f32 v92, v92, v176\n" \
  "v_sub_f32 v93, v93, v177\n" \
  "v_sub_f32 v94, v94, v178\n" \
  "v_sub_f32 v95, v95, v179\n" \
  "s_mov_b32 exec_lo, 0x0\n" \
  "s_mov_b32 exec_hi, 0xff00ff\n" \
  "v_mov_b64 v[92:93], 0\n" \
  "v_mov_b64 v[94:95], 0\n" \
  "s_mov_b32 exec_lo, 0x0\n" \
  "s_mov_b32 exec_hi, 0xf0000f\n" \
  "v_mov_b64 v[92:93], v[172:173]\n" \
  "v_mov_b64 v[94:95], v[174:175]\n" \
  "s_mov_b64 exec, -1\n" \
  "v_lshlrev_b32 v188, 4, v253\n" \
  "v_and_b32 v96, 4, v252\n" \
  "v_lshl_add_u32 v96, v253, 5, v96\n" \
  "v_mov_b32 v191, 0x40000000\n" \
  "v_cmp_gt_u32 vcc, 8, v252\n" \
  "v_mov_b32 v193, 0x4444\n" \
  "v_mov_b32 v98, 0xeeee\n" \
  "v_cndmask_b32 v189, v191, v96, vcc\n" \
  "v_cndmask_b32 v190, v96, v191, vcc\n" \
  "v_and_b32 v99, 4, v252\n" \
  "v_cmp_ne_u32 vcc, 0, v99\n" \
  "s_lshl_b32 s35, s28, 11\n" \
  "s_add_u32 s35, s35, 130048\n" \
  "v_cndmask_b32 v193, v193, v98, vcc\n" \
  "v_lshl_add_u32 v194, v252, 4, s35\n" \
  "v_and_b32 v96, 7, v252\n" \
  "v_lshlrev_b32 v96, 5, v96\n" \
  "v_lshl_add_u32 v96, v253, 8, v96\n" \
  "v_lshrrev_b32 v97, 3, v252\n" \
  "v_lshl_add_u32 v96, v97, 2, v96\n" \
  "v_add_u32 v195, s35, v96\n" \
  "v_and_b32 v97, v97, v252\n" \
  "v_lshlrev_b32 v179, 4, v97\n" \
  "s_waitcnt vmcnt(0)\n" \
  "v_cvt_pk_u16_u32 v232, v232, v233\n" \
  "v_cvt_pk_u16_u32 v234, v234, v235\n" \
  "v_cvt_pk_u16_u32 v236, v236, v237\n" \
  "v_cvt_pk_u16_u32 v238, v238, v239\n" \
  "v_pk_lshrrev_b16 v233, 3, v232 op_sel_hi:[0,1]\n" \
  "v_pk_lshrrev_b16 v235, 3, v234 op_sel_hi:[0,1]\n" \
  "v_pk_lshrrev_b16 v237, 3, v236 op_sel_hi:[0,1]\n" \
  "v_pk_lshrrev_b16 v239, 3, v238 op_sel_hi:[0,1]\n" \
  "v_pk_mad_u16 v232, v232, 8, v233 op_sel_hi:[1,0,1]\n" \
  "v_pk_mad_u16 v234, v234, 8, v235 op_sel_hi:[1,0,1]\n" \
  "v_pk_mad_u16 v236, v236, 8, v237 op_sel_hi:[1,0,1]\n" \
  "v_pk_mad_u16 v238, v238, 8, v239 op_sel_hi:[1,0,1]\n" \
  "v_alignbit_b32 v236, v236, v236, v179\n" \
  "v_alignbit_b32 v238, v238, v238, v179\n" \
  "ds_write2_b32 v195, v232, v234 offset0:0 offset1:4\n" \
  "ds_write2_b32 v195, v236, v238 offset0:2 offset1:6\n" \
  "v_cvt_pk_u16_u32 v240, v240, v241\n" \
  "v_cvt_pk_u16_u32 v242, v242, v243\n" \
  "v_cvt_pk_u16_u32 v244, v244, v245\n" \
  "v_cvt_pk_u16_u32 v246, v246, v247\n" \
  "v_pk_lshrrev_b16 v241, 3, v240 op_sel_hi:[0,1]\n" \
  "v_pk_lshrrev_b16 v243, 3, v242 op_sel_hi:[0,1]\n" \
  "v_pk_lshrrev_b16 v245, 3, v244 op_sel_hi:[0,1]\n" \
  "v_pk_lshrrev_b16 v247, 3, v246 op_sel_hi:[0,1]\n" \
  "v_pk_mad_u16 v240, v240, 8, v241 op_sel_hi:[1,0,1]\n" \
  "v_pk_mad_u16 v242, v242, 8, v243 op_sel_hi:[1,0,1]\n" \
  "v_pk_mad_u16 v244, v244, 8, v245 op_sel_hi:[1,0,1]\n" \
  "v_pk_mad_u16 v246, v246, 8, v247 op_sel_hi:[1,0,1]\n" \
  "v_alignbit_b32 v244, v244, v244, v179\n" \
  "v_alignbit_b32 v246, v246, v246, v179\n" \
  "v_add_u32 v241, 0x400, v195\n" \
  "ds_write2_b32 v241, v240, v242 offset0:0 offset1:4\n" \
  "ds_write2_b32 v241, v244, v246 offset0:2 offset1:6\n" \
  "v_add_u32 v96, 6400, v250\n" \
  "v_add_u32 v97, 6400, v251\n" \
  "v_mov_b32 v98, v254\n" \
  "L1_:\n" \
  "ds_read_b32 v99, v98 offset:12\n" \
  "s_waitcnt lgkmcnt(0)\n" \
  "v_readfirstlane_b32 s35, v99\n" \
  "s_cmp_lt_u32 s35, 4\n" \
  "s_cbranch_scc1 L1_\n" \
  "v_lshlrev_b32 v203, 4, v253\n" \
  "s_lshr_b32 s35, s30, 4\n" \
  "s_lshl1_add_u32 s35, s29, s35\n" \
  "s_lshl_b32 s35, s35, 6\n" \
  "s_bfe_u32 s37, s30, 0x20002\n" \
  "s_lshl3_add_u32 s35, s37, s35\n" \
  "v_and_b32 v203, 16, v203\n" \
  "v_lshl_add_u32 v203, v203, 1, s35\n" \
  "global_load_dwordx2 v[204:205], v203, s[16:17]\n" \
  "global_load_dwordx2 v[206:207], v203, s[18:19]\n" \
  "v_mov_b32 v203, 0\n" \
  "global_load_ushort v203, v203, s[20:21]\n" \
  "s_add_u32 s46, s44, 4800\n" \
  "s_addc_u32 s47, s45, 0\n" \
  "global_load_dwordx4 v[208:211], v96, s[44:45] offset:-3200\n" \
  "global_load_dwordx4 v[212:215], v97, s[44:45] offset:-2944\n" \
  "global_load_dwordx4 v[216:219], v96, s[44:45] offset:-1600\n" \
  "global_load_dwordx4 v[220:223], v97, s[44:45] offset:-1344\n" \
  "global_load_dwordx4 v[224:227], v96, s[44:45] offset:0\n" \
  "global_load_dwordx4 v[228:231], v97, s[44:45] offset:256\n" \
  "global_load_dwordx4 v[232:235], v96, s[44:45] offset:1600\n" \
  "global_load_dwordx4 v[236:239], v97, s[44:45] offset:1856\n" \
  "global_load_dwordx4 v[240:243], v96, s[44:45] offset:3200\n" \
  "global_load_dwordx4 v[244:247], v97, s[44:45] offset:3456\n" \
  "global_load_dwordx4 v[248:251], v96, s[46:47] offset:0\n" \
  "global_load_dwordx4 v[252:255], v97, s[46:47] offset:256\n" \
  "s_setprio 1\n" \
  "ds_read_b128 v[180:183], v194\n" \
  "v_add_u32 v194, 0x100, v194\n" \
  "s_waitcnt lgkmcnt(0)\n" \
  "v_mad_u32_u16 v184, v182, 16, v188\n" \
  "ds_read_b128 v[128:131], v184 offset:0\n" \
  "ds_read_b128 v[132:135], v184 offset:64\n" \
  "ds_read_b128 v[172:175], v191\n" \
  "v_mad_u32_u16 v185, v181, 16, v188\n" \
  "v_mad_u32_u16 v186, v182, 16, v188 op_sel:[1,0,0,0]\n" \
  "ds_read_b128 v[112:115], v185 offset:0\n" \
  "ds_read_b128 v[116:119], v185 offset:64\n" \
  "ds_read_b128 v[136:139], v186 offset:0\n" \
  "ds_read_b128 v[140:143], v186 offset:64\n" \
  "ds_read_b128 v[168:171], v191\n" \
  "v_mad_u32_u16 v187, v181, 16, v188 op_sel:[1,0,0,0]\n" \
  "v_mad_u32_u16 v184, v180, 16, v188 op_sel:[1,0,0,0]\n" \
  "ds_read_b128 v[120:123], v187 offset:0\n" \
  "v_mad_u32_u16 v185, v180, 16, v188\n" \
  "ds_read_b128 v[124:127], v187 offset:64\n" \
  "ds_read_b128 v[104:107], v184 offset:0\n" \
  "v_mad_u32_u16 v186, v183, 16, v189\n" \
  "ds_read_b128 v[108:111], v184 offset:64\n" \
  "ds_read_b128 v[96:99], v185 offset:0\n" \
  "s_waitcnt lgkmcnt(9)\n" \
  "s_mov_b32 s51, 3\n" \
  "s_call_b64 s[52:53], LA_loop\n" \
  "LA_ev0:\n" \
  "v_mov_b32 v196, v178\n" \
  "s_waitcnt vmcnt(10)\n" \
  "v_cvt_pk_u16_u32 v208, v208, v209\n" \
  "v_cvt_pk_u16_u32 v210, v210, v211\n" \
  "v_cvt_pk_u16_u32 v212, v212, v213\n" \
  "v_cvt_pk_u16_u32 v214, v214, v215\n" \
  "v_pk_lshrrev_b16 v209, 3, v208 op_sel_hi:[0,1]\n" \
  "v_pk_lshrrev_b16 v211, 3, v210 op_sel_hi:[0,1]\n" \
  "v_pk_lshrrev_b16 v213, 3, v212 op_sel_hi:[0,1]\n" \
  "v_pk_lshrrev_b16 v215, 3, v214 op_sel_hi:[0,1]\n" \
  "v_pk_mad_u16 v208, v208, 8, v209 op_sel_hi:[1,0,1]\n" \
  "v_pk_mad_u16 v210, v210, 8, v211 op_sel_hi:[1,0,1]\n" \
  "v_pk_mad_u16 v212, v212, 8, v213 op_sel_hi:[1,0,1]\n" \
  "v_pk_mad_u16 v214, v214, 8, v215 op_sel_hi:[1,0,1]\n" \
  "v_alignbit_b32 v212, v212, v212, v179\n" \
  "v_alignbit_b32 v214, v214, v214, v179\n" \
  "ds_write2_b32 v195, v208, v210 offset0:0 offset1:4\n" \
  "ds_write2_b32 v195, v212, v214 offset0:2 offset1:6\n" \
  "v_readfirstlane_b32 s6, v203\n" \
  "s_mov_b32 s51, 4\n" \
  "s_call_b64 s[52:53], LA_loop\n" \
  "LA_ev1:\n" \
  "v_mov_b32 v197, v178\n" \
  "s_waitcnt vmcnt(8)\n" \
  "v_cvt_pk_u16_u32 v216, v216, v217\n" \
  "v_cvt_pk_u16_u32 v218, v218, v219\n" \
  "v_cvt_pk_u16_u32 v220, v220, v221\n" \
  "v_cvt_pk_u16_u32 v222, v222, v223\n" \
  "v_pk_lshrrev_b16 v217, 3, v216 op_sel_hi:[0,1]\n" \
  "v_pk_lshrrev_b16 v219, 3, v218 op_sel_hi:[0,1]\n" \
  "v_pk_lshrrev_b16 v221, 3, v220 op_sel_hi:[0,1]\n" \
  "v_pk_lshrrev_b16 v223, 3, v222 op_sel_hi:[0,1]\n" \
  "v_pk_mad_u16 v216, v216, 8, v217 op_sel_hi:[1,0,1]\n" \
  "v_pk_mad_u16 v218, v218, 8, v219 op_sel_hi:[1,0,1]\n" \
  "v_pk_mad_u16 v220, v220, 8, v221 op_sel_hi:[1,0,1]\n" \
  "v_pk_mad_u16 v222, v222, 8, v223 op_sel_hi:[1,0,1]\n" \
  "v_alignbit_b32 v220, v220, v220, v179\n" \
  "v_alignbit_b32 v222, v222, v222, v179\n" \
  "v_add_u32 v217, 0x400, v195\n" \
  "ds_write2_b32 v217, v216, v218 offset0:0 offset1:4\n" \
  "ds_write2_b32 v217, v220, v222 offset0:2 offset1:6\n" \
  "v_subrev_u32 v194, 0x800, v194\n" \
  "s_mov_b32 s51, 4\n" \
  "s_call_b64 s[52:53], LA_loop\n" \
  "LA_ev2:\n" \
  "v_mov_b32 v198, v178\n" \
  "s_waitcnt vmcnt(6)\n" \
  "v_cvt_pk_u16_u32 v224, v224, v225\n" \
  "v_cvt_pk_u16_u32 v226, v226, v227\n" \
  "v_cvt_pk_u16_u32 v228, v228, v229\n" \
  "v_cvt_pk_u16_u32 v230, v230, v231\n" \
  "v_pk_lshrrev_b16 v225, 3, v224 op_sel_hi:[0,1]\n" \
  "v_pk_lshrrev_b16 v227, 3, v226 op_sel_hi:[0,1]\n" \
  "v_pk_lshrrev_b16 v229, 3, v228 op_sel_hi:[0,1]\n" \
  "v_pk_lshrrev_b16 v231, 3, v230 op_sel_hi:[0,1]\n" \
  "v_pk_mad_u16 v224, v224, 8, v225 op_sel_hi:[1,0,1]\n" \
  "v_pk_mad_u16 v226, v226, 8, v227 op_sel_hi:[1,0,1]\n" \
  "v_pk_mad_u16 v228, v228, 8, v229 op_sel_hi:[1,0,1]\n" \
  "v_pk_mad_u16 v230, v230, 8, v231 op_sel_hi:[1,0,1]\n" \
  "v_alignbit_b32 v228, v228, v228, v179\n" \
  "v_alignbit_b32 v230, v230, v230, v179\n" \
  "ds_write2_b32 v195, v224, v226 offset0:0 offset1:4\n" \
  "ds_write2_b32 v195, v228, v230 offset0:2 offset1:6\n" \
  "s_mov_b32 s51, 4\n" \
  "s_call_b64 s[52:53], LA_loop\n" \
  "LA_ev3:\n" \
  "v_mov_b32 v199, v178\n" \
  "s_waitcnt vmcnt(4)\n" \
  "v_cvt_pk_u16_u32 v232, v232, v233\n" \
  "v_cvt_pk_u16_u32 v234, v234, v235\n" \
  "v_cvt_pk_u16_u32 v236, v236, v237\n" \
  "v_cvt_pk_u16_u32 v238, v238, v239\n" \
  "v_pk_lshrrev_b16 v233, 3, v232 op_sel_hi:[0,1]\n" \
  "v_pk_lshrrev_b16 v235, 3, v234 op_sel_hi:[0,1]\n" \
  "v_pk_lshrrev_b16 v237, 3, v236 op_sel_hi:[0,1]\n" \
  "v_pk_lshrrev_b16 v239, 3, v238 op_sel_hi:[0,1]\n" \
  "v_pk_mad_u16 v232, v232, 8, v233 op_sel_hi:[1,0,1]\n" \
  "v_pk_mad_u16 v234, v234, 8, v235 op_sel_hi:[1,0,1]\n" \
  "v_pk_mad_u16 v236, v236, 8, v237 op_sel_hi:[1,0,1]\n" \
  "v_pk_mad_u16 v238, v238, 8, v239 op_sel_hi:[1,0,1]\n" \
  "v_alignbit_b32 v236, v236, v236, v179\n" \
  "v_alignbit_b32 v238, v238, v238, v179\n" \
  "v_add_u32 v233, 0x400, v195\n" \
  "ds_write2_b32 v233, v232, v234 offset0:0 offset1:4\n" \
  "ds_write2_b32 v233, v236, v238 offset0:2 offset1:6\n" \
  "v_subrev_u32 v194, 0x800, v194\n" \
  "s_mov_b32 s51, 4\n" \
  "s_call_b64 s[52:53], LA_loop\n" \
  "LA_ev4:\n" \
  "v_mov_b32 v200, v178\n" \
  "s_waitcnt vmcnt(2)\n" \
  "v_cvt_pk_u16_u32 v240, v240, v241\n" \
  "v_cvt_pk_u16_u32 v242, v242, v243\n" \
  "v_cvt_pk_u16_u32 v244, v244, v245\n" \
  "v_cvt_pk_u16_u32 v246, v246, v247\n" \
  "v_pk_lshrrev_b16 v241, 3, v240 op_sel_hi:[0,1]\n" \
  "v_pk_lshrrev_b16 v243, 3, v242 op_sel_hi:[0,1]\n" \
  "v_pk_lshrrev_b16 v245, 3, v244 op_sel_hi:[0,1]\n" \
  "v_pk_lshrrev_b16 v247, 3, v246 op_sel_hi:[0,1]\n" \
  "v_pk_mad_u16 v240, v240, 8, v241 op_sel_hi:[1,0,1]\n" \
  "v_pk_mad_u16 v242, v242, 8, v243 op_sel_hi:[1,0,1]\n" \
  "v_pk_mad_u16 v244, v244, 8, v245 op_sel_hi:[1,0,1]\n" \
  "v_pk_mad_u16 v246, v246, 8, v247 op_sel_hi:[1,0,1]\n" \
  "v_alignbit_b32 v244, v244, v244, v179\n" \
  "v_alignbit_b32 v246, v246, v246, v179\n" \
  "ds_write2_b32 v195, v240, v242 offset0:0 offset1:4\n" \
  "ds_write2_b32 v195, v244, v246 offset0:2 offset1:6\n" \
  "s_mov_b32 s51, 4\n" \
  "s_call_b64 s[52:53], LA_loop\n" \
  "LA_ev5:\n" \
  "v_mov_b32 v201, v178\n" \
  "s_waitcnt vmcnt(0)\n" \
  "v_cvt_pk_u16_u32 v248, v248, v249\n" \
  "v_cvt_pk_u16_u32 v250, v250, v251\n" \
  "v_cvt_pk_u16_u32 v252, v252, v253\n" \
  "v_cvt_pk_u16_u32 v254, v254, v255\n" \
  "v_pk_lshrrev_b16 v249, 3, v248 op_sel_hi:[0,1]\n" \
  "v_pk_lshrrev_b16 v251, 3, v250 op_sel_hi:[0,1]\n" \
  "v_pk_lshrrev_b16 v253, 3, v252 op_sel_hi:[0,1]\n" \
  "v_pk_lshrrev_b16 v255, 3, v254 op_sel_hi:[0,1]\n" \
  "v_pk_mad_u16 v248, v248, 8, v249 op_sel_hi:[1,0,1]\n" \
  "v_pk_mad_u16 v250, v250, 8, v251 op_sel_hi:[1,0,1]\n" \
  "v_pk_mad_u16 v252, v252, 8, v253 op_sel_hi:[1,0,1]\n" \
  "v_pk_mad_u16 v254, v254, 8, v255 op_sel_hi:[1,0,1]\n" \
  "v_alignbit_b32 v252, v252, v252, v179\n" \
  "v_alignbit_b32 v254, v254, v254, v179\n" \
  "v_add_u32 v249, 0x400, v195\n" \
  "ds_write2_b32 v249, v248, v250 offset0:0 offset1:4\n" \
  "ds_write2_b32 v249, v252, v254 offset0:2 offset1:6\n" \
  "v_mov_b32 v208, 163328\n" \
  "ds_read_b32 v209, v208 offset:4\n" \
  "v_subrev_u32 v194, 0x800, v194\n" \
  "s_mov_b32 s51, 4\n" \
  "s_call_b64 s[52:53], LA_loop\n" \
  "LA_ev6:\n" \
  "v_mov_b32 v202, v178\n" \
  "v_readfirstlane_b32 s35, v209\n" \
  "s_cmp_ge_u32 s35, 128\n" \
  "s_cbranch_scc1 LA_pl\n" \
  "L2_:\n" \
  "ds_read_b32 v209, v208 offset:4\n" \
  "s_waitcnt lgkmcnt(0)\n" \
  "v_readfirstlane_b32 s35, v209\n" \
  "s_cmp_lt_u32 s35, 128\n" \
  "s_cbranch_scc1 L2_\n" \
  "LA_pl:\n" \
  "s_and_b32 s4, s35, 15\n" \
  "s_sub_u32 s4, s4, 4\n" \
  "s_bfe_u32 s5, s35, 0x30004\n" \
  "s_sub_u32 s5, s5, s29\n" \
  "s_cmp_le_i32 s4, 0\n" \
  "s_cbranch_scc1 LA_nt\n" \
  "s_mul_i32 s35, s5, 51200\n" \
  "s_ashr_i32 s36, s35, 31\n" \
  "s_add_u32 s46, s46, s35\n" \
  "s_addc_u32 s47, s47, s36\n" \
  "v_lshrrev_b32 v216, 4, v188\n" \
  "v_mul_u32_u24 v216, 400, v216\n" \
  "v_add_u32 v216, 6400, v216\n" \
  "v_bfe_u32 v218, v194, 4, 4\n" \
  "v_min_u32 v217, 8, v218\n" \
  "v_lshl_add_u32 v217, v217, 4, v216\n" \
  "v_lshl_add_u32 v216, v218, 4, v216\n" \
  "global_load_dwordx4 v[208:211], v216, s[46:47] offset:0\n" \
  "global_load_dwordx4 v[212:215], v217, s[46:47] offset:256\n" \
  "LA_nt:\n" \
  "s_lshl_b32 s5, s5, 7\n" \
  "s_sub_u32 s5, s5, s4\n" \
  "s_min_i32 s51, s4, 0\n" \
  "s_add_u32 s51, s51, 4\n" \
  "s_cmp_eq_u32 s51, 0\n" \
  "s_cbranch_scc1 LA_ev7\n" \
  "s_call_b64 s[52:53], LA_loop\n" \
  "LA_ev7:\n" \
  "v_mov_b32 v203, v178\n" \
  "s_cmp_le_i32 s4, 0\n" \
  "s_cbranch_scc1 LA_ev8\n" \
  "s_waitcnt vmcnt(0)\n" \
  "v_cvt_pk_u16_u32 v208, v208, v209\n" \
  "v_cvt_pk_u16_u32 v210, v210, v211\n" \
  "v_cvt_pk_u16_u32 v212, v212, v213\n" \
  "v_cvt_pk_u16_u32 v214, v214, v215\n" \
  "v_pk_lshrrev_b16 v209, 3, v208 op_sel_hi:[0,1]\n" \
  "v_pk_lshrrev_b16 v211, 3, v210 op_sel_hi:[0,1]\n" \
  "v_pk_lshrrev_b16 v213, 3, v212 op_sel_hi:[0,1]\n" \
  "v_pk_lshrrev_b16 v215, 3, v214 op_sel_hi:[0,1]\n" \
  "v_pk_mad_u16 v208, v208, 8, v209 op_sel_hi:[1,0,1]\n" \
  "v_pk_mad_u16 v210, v210, 8, v211 op_sel_hi:[1,0,1]\n" \
  "v_pk_mad_u16 v212, v212, 8, v213 op_sel_hi:[1,0,1]\n" \
  "v_pk_mad_u16 v214, v214, 8, v215 op_sel_hi:[1,0,1]\n" \
  "v_alignbit_b32 v212, v212, v212, v179\n" \
  "v_alignbit_b32 v214, v214, v214, v179\n" \
  "ds_write2_b32 v195, v208, v210 offset0:0 offset1:4\n" \
  "ds_write2_b32 v195, v212, v214 offset0:2 offset1:6\n" \
  "s_lshl_b32 s35, s4, 8\n" \
  "s_add_u32 s35, s35, 0x400\n" \
  "v_subrev_u32 v194, s35, v194\n" \
  "s_mov_b32 s51, s4\n" \
  "s_call_b64 s[52:53], LA_loop\n" \
  "LA_ev8:\n" \
  "v_mov_b32 v179, v178\n" \
  "ds_read_b128 v[100:103], v185 offset:64\n" \
  "ds_read2_b32 v[144:145], v186 offset0:0 offset1:2\n" \
  "ds_read2_b32 v[146:147], v186 offset0:4 offset1:6\n" \
  "v_mad_u32_u16 v187, v183, 16, v190\n" \
  "ds_read2_b32 v[148:149], v187 offset0:0 offset1:2\n" \
  "ds_read2_b32 v[150:151], v187 offset0:4 offset1:6\n" \
  "ds_read_b128 v[164:167], v191\n" \
  "s_lshr_b32 s35, s30, 2\n" \
  "s_lshl3_add_u32 s33, s29, s35\n" \
  "s_and_b32 s34, s30, 3\n" \
  "s_lshl_b32 s59, s27, 11\n" \
  "s_mul_i32 s35, s33, 1600000\n" \
  "s_add_u32 s54, s24, s35\n" \
  "s_addc_u32 s55, s25, 0\n" \
  "s_mul_i32 s35, s34, 819200\n" \
  "s_lshl1_add_u32 s35, s59, s35\n" \
  "s_add_u32 s35, s35, 102400000\n" \
  "s_add_u32 s56, s24, s35\n" \
  "s_addc_u32 s57, s25, 0\n" \
  "v_mbcnt_lo_u32_b32 v189, -1, 0\n" \
  "v_mbcnt_hi_u32_b32 v189, -1, v189\n" \
  "v_lshlrev_b32 v188, 4, v189\n" \
  "s_waitcnt lgkmcnt(6)\n" \
  "v_mfma_f32_16x16x32_bf16 v[152:155], v[112:115], v[112:115], 0\n" \
  "v_fmac_f32 v176, v156, v88\n" \
  "v_fmac_f32 v176, v157, v89\n" \
  "v_mfma_f32_16x16x32_bf16 v[152:155], v[116:119], v[116:119], v[152:155]\n" \
  "v_fmac_f32 v176, v158, v90\n" \
  "v_fmac_f32 v176, v159, v91\n" \
  "v_mfma_f32_16x16x32_bf16 v[156:159], v[128:131], v[128:131], 0\n" \
  "v_fmac_f32 v176, v160, v92\n" \
  "v_fmac_f32 v176, v161, v93\n" \
  "v_mfma_f32_16x16x32_bf16 v[156:159], v[132:135], v[132:135], v[156:159]\n" \
  "v_fmac_f32 v176, v162, v94\n" \
  "v_fma_f32 v177, v163, v95, v176\n" \
  "v_mfma_f32_16x16x32_bf16 v[160:163], v[128:131], v[136:139], 0\n" \
  "v_mul_f32 v176, v152, v0\n" \
  "v_fmac_f32 v176, v153, v1\n" \
  "v_mfma_f32_16x16x32_bf16 v[160:163], v[132:135], v[140:143], v[160:163]\n" \
  "v_fmac_f32 v176, v154, v2\n" \
  "v_fmac_f32 v176, v155, v3\n" \
  "v_mfma_f32_16x16x32_bf16 v[152:155], v[136:139], v[136:139], 0\n" \
  "v_fmac_f32 v176, v156, v4\n" \
  "v_fmac_f32 v176, v157, v5\n" \
  "v_mfma_f32_16x16x32_bf16 v[152:155], v[140:143], v[140:143], v[152:155]\n" \
  "v_fmac_f32 v176, v158, v6\n" \
  "v_fmac_f32 v176, v159, v7\n" \
  "s_waitcnt lgkmcnt(0)\n" \
  "s_setprio 3\n" \
  "s_barrier\n" \
  "global_load_dwordx4 v[208:211], v188, s[56:57] offset:0 sc1\n" \
  "global_load_dwordx4 v[212:215], v188, s[56:57] offset:1024 sc1\n" \
  "global_load_dwordx4 v[216:219], v188, s[56:57] offset:2048 sc1\n" \
  "global_load_dwordx4 v[220:223], v188, s[56:57] offset:3072 sc1\n" \
  "v_mfma_f32_16x16x32_bf16 v[156:159], v[112:115], v[136:139], 0\n" \
  "v_fmac_f32 v176, v160, v8\n" \
  "v_fmac_f32 v176, v161, v9\n" \
  "v_lshrrev_b32 v180, 1, v189\n" \
  "v_mfma_f32_16x16x32_bf16 v[156:159], v[116:119], v[140:143], v[156:159]\n" \
  "v_fmac_f32 v176, v162, v10\n" \
  "v_fmac_f32 v176, v163, v11\n" \
  "v_xor_b32 v180, v180, v189\n" \
  "v_mfma_f32_16x16x32_bf16 v[160:163], v[112:115], v[128:131], 0\n" \
  "v_fmac_f32 v176, v152, v12\n" \
  "v_fmac_f32 v176, v153, v13\n" \
  "v_add_f32_dpp v177, v177, v177 row_ror:8 row_mask:0xf bank_mask:0xf\n" \
  "v_and_b32 v180, 8, v180\n" \
  "v_mfma_f32_16x16x32_bf16 v[160:163], v[116:119], v[132:135], v[160:163]\n" \
  "v_fmac_f32 v176, v154, v14\n" \
  "v_fmac_f32 v176, v155, v15\n" \
  "v_bfe_u32 v181, v189, 2, 1\n" \
  "v_mfma_f32_16x16x32_bf16 v[152:155], v[112:115], v[120:123], 0\n" \
  "v_fmac_f32 v176, v156, v16\n" \
  "v_fmac_f32 v176, v157, v17\n" \
  "v_lshlrev_b32_e64 v181, v181, 5\n" \
  "v_mfma_f32_16x16x32_bf16 v[152:155], v[116:119], v[124:127], v[152:155]\n" \
  "v_fmac_f32 v176, v158, v18\n" \
  "v_fmac_f32 v176, v159, v19\n" \
  "v_mov_b32_dpp v178, v178 row_ror:4 row_mask:0xf bank_mask:0xf\n" \
  "v_lshlrev_b32 v180, v180, v181\n" \
  "v_mfma_f32_16x16x32_bf16 v[156:159], v[120:123], v[136:139], 0\n" \
  "v_fmac_f32 v176, v160, v20\n" \
  "v_fmac_f32 v176, v161, v21\n" \
  "v_bfe_u32 v181, v189, 2, 2\n" \
  "v_mfma_f32_16x16x32_bf16 v[156:159], v[124:127], v[140:143], v[156:159]\n" \
  "v_fmac_f32 v176, v162, v22\n" \
  "v_fmac_f32 v176, v163, v23\n" \
  "v_lshlrev_b32_e64 v181, v181, 1\n" \
  "v_mfma_f32_16x16x32_bf16 v[160:163], v[104:107], v[128:131], 0\n" \
  "v_fmac_f32 v176, v152, v24\n" \
  "v_fmac_f32 v176, v153, v25\n" \
  "v_cmp_gt_u32 vcc, 32, v189\n" \
  "v_mfma_f32_16x16x32_bf16 v[160:163], v[108:111], v[132:135], v[160:163]\n" \
  "v_fmac_f32 v176, v154, v26\n" \
  "v_fmac_f32 v176, v155, v27\n" \
  "v_mov_b32 v191, 0x3f80\n" \
  "v_mfma_f32_16x16x32_bf16 v[152:155], v[120:123], v[128:131], 0\n" \
  "v_fmac_f32 v176, v156, v28\n" \
  "v_fmac_f32 v176, v157, v29\n" \
  "v_add_u32 v190, 32000, v188\n" \
  "v_mfma_f32_16x16x32_bf16 v[152:155], v[124:127], v[132:135], v[152:155]\n" \
  "v_fmac_f32 v176, v158, v30\n" \
  "v_fmac_f32 v176, v159, v31\n" \
  "v_add_f32_dpp v178, v177, v177 row_ror:4 row_mask:0xf bank_mask:0x1\n" \
  "v_cndmask_b32 v192, v181, v180, vcc\n" \
  "v_mfma_f32_16x16x32_bf16 v[156:159], v[96:99], v[128:131], 0\n" \
  "v_fmac_f32 v176, v160, v32\n" \
  "v_fmac_f32 v176, v161, v33\n" \
  "v_bfe_i32 v180, v192, 0, 1\n" \
  "v_mfma_f32_16x16x32_bf16 v[156:159], v[100:103], v[132:135], v[156:159]\n" \
  "v_fmac_f32 v176, v162, v34\n" \
  "v_fmac_f32 v176, v163, v35\n" \
  "v_and_b32 v180, v180, v191\n" \
  "v_smfmac_f32_16x16x64_bf16 v[172:175], v[144:147], v[128:135], v193\n" \
  "v_fmac_f32 v176, v152, v36\n" \
  "v_fmac_f32 v176, v153, v37\n" \
  "v_bfe_i32 v181, v192, 1, 1\n" \
  "v_smfmac_f32_16x16x64_bf16 v[172:175], v[148:151], v[136:143], v193\n" \
  "v_fmac_f32 v176, v154, v38\n" \
  "v_fmac_f32 v176, v155, v39\n" \
  "v_and_b32 v181, v181, v191\n" \
  "v_mfma_f32_16x16x32_bf16 v[160:163], v[96:99], v[112:115], 0\n" \
  "v_fmac_f32 v176, v156, v40\n" \
  "v_fmac_f32 v176, v157, v41\n" \
  "v_bfe_i32 v182, v192, 2, 1\n" \
  "v_mfma_f32_16x16x32_bf16 v[160:163], v[100:103], v[116:119], v[160:163]\n" \
  "v_fmac_f32 v176, v158, v42\n" \
  "v_fmac_f32 v176, v159, v43\n" \
  "v_and_b32 v182, v182, v191\n" \
  "v_mfma_f32_16x16x32_bf16 v[152:155], v[104:107], v[112:115], 0\n" \
  "v_fmac_f32 v176, v172, v44\n" \
  "v_fmac_f32 v176, v173, v45\n" \
  "v_bfe_i32 v183, v192, 3, 1\n" \
  "v_mfma_f32_16x16x32_bf16 v[152:155], v[108:111], v[116:119], v[152:155]\n" \
  "v_fmac_f32 v176, v174, v46\n" \
  "v_fmac_f32 v176, v175, v47\n" \
  "v_and_b32 v183, v183, v191\n" \
  "v_mfma_f32_16x16x32_bf16 v[156:159], v[104:107], v[136:139], 0\n" \
  "v_fmac_f32 v176, v160, v48\n" \
  "v_fmac_f32 v176, v161, v49\n" \
  "v_and_b32 v192, 3, v189\n" \
  "v_mfma_f32_16x16x32_bf16 v[156:159], v[108:111], v[140:143], v[156:159]\n" \
  "v_fmac_f32 v176, v162, v50\n" \
  "v_fmac_f32 v176, v163, v51\n" \
  "v_mul_u32_u24 v192, 0x5555, v192\n" \
  "v_smfmac_f32_16x16x64_bf16 v[168:171], v[144:147], v[112:119], v193\n" \
  "v_fmac_f32 v176, v152, v52\n" \
  "v_fmac_f32 v176, v153, v53\n" \
  "v_add_u32 v189, 16000, v188\n" \
  "v_smfmac_f32_16x16x64_bf16 v[168:171], v[148:151], v[120:127], v193\n" \
  "v_fmac_f32 v176, v154, v54\n" \
  "v_fmac_f32 v176, v155, v55\n" \
  "v_add_u32 v191, 48000, v188\n" \
  "v_mfma_f32_16x16x32_bf16 v[160:163], v[96:99], v[136:139], 0\n" \
  "v_fmac_f32 v176, v156, v56\n" \
  "v_fmac_f32 v176, v157, v57\n" \
  "v_mfma_f32_16x16x32_bf16 v[160:163], v[100:103], v[140:143], v[160:163]\n" \
  "v_fmac_f32 v176, v158, v58\n" \
  "v_fmac_f32 v176, v159, v59\n" \
  "s_add_u32 s35, s59, 64000\n" \
  "s_add_u32 s54, s54, s35\n" \
  "s_addc_u32 s55, s55, 0\n" \
  "s_add_u32 s58, s59, 0x10000\n" \
  "s_mov_b32 m0, s58\n" \
  "s_add_u32 s35, s58, 0x8000\n" \
  "global_load_lds_dwordx4 v188, s[54:55] sc1\n" \
  "global_load_lds_dwordx4 v188, s[54:55] offset:1024 sc1\n" \
  "v_smfmac_f32_16x16x64_bf16 v[164:167], v[148:151], v[104:111], v193\n" \
  "v_fmac_f32 v176, v168, v60\n" \
  "v_fmac_f32 v176, v169, v61\n" \
  "v_smfmac_f32_16x16x64_bf16 v[164:167], v[144:147], v[96:103], v193\n" \
  "v_fmac_f32 v176, v170, v62\n" \
  "v_fmac_f32 v176, v171, v63\n" \
  "v_mfma_f32_16x16x32_bf16 v[152:155], v[120:123], v[120:123], 0\n" \
  "v_fmac_f32 v176, v160, v64\n" \
  "v_fmac_f32 v176, v161, v65\n" \
  "v_mfma_f32_16x16x32_bf16 v[152:155], v[124:127], v[124:127], v[152:155]\n" \
  "v_fmac_f32 v176, v162, v66\n" \
  "v_fmac_f32 v176, v163, v67\n" \
  "v_mfma_f32_16x16x32_bf16 v[156:159], v[104:107], v[120:123], 0\n" \
  "v_fmac_f32 v176, v164, v68\n" \
  "v_fmac_f32 v176, v165, v69\n" \
  "v_mfma_f32_16x16x32_bf16 v[156:159], v[108:111], v[124:127], v[156:159]\n" \
  "v_fmac_f32 v176, v166, v70\n" \
  "v_fmac_f32 v176, v167, v71\n" \
  "s_add_u32 m0, s58, 0x4000\n" \
  "s_cmp_lt_u32 s35, 0x28000\n" \
  "global_load_lds_dwordx4 v189, s[54:55] sc1\n" \
  "global_load_lds_dwordx4 v189, s[54:55] offset:1024 sc1\n" \
  "s_cselect_b32 s58, s35, s59\n" \
  "s_add_u32 s54, s54, 32000\n" \
  "s_addc_u32 s55, s55, 0\n" \
  "v_mfma_f32_16x16x32_bf16 v[160:163], v[96:99], v[120:123], 0\n" \
  "v_fmac_f32 v176, v152, v72\n" \
  "v_fmac_f32 v176, v153, v73\n" \
  "v_mfma_f32_16x16x32_bf16 v[160:163], v[100:103], v[124:127], v[160:163]\n" \
  "v_fmac_f32 v176, v154, v74\n" \
  "v_fmac_f32 v176, v155, v75\n" \
  "v_mfma_f32_16x16x32_bf16 v[152:155], v[96:99], v[104:107], 0\n" \
  "v_fmac_f32 v176, v156, v76\n" \
  "v_fmac_f32 v176, v157, v77\n" \
  "v_mfma_f32_16x16x32_bf16 v[152:155], v[100:103], v[108:111], v[152:155]\n" \
  "v_fmac_f32 v176, v158, v78\n" \
  "v_fmac_f32 v176, v159, v79\n" \
  "v_mfma_f32_16x16x32_bf16 v[156:159], v[104:107], v[104:107], 0\n" \
  "v_fmac_f32 v176, v160, v80\n" \
  "v_fmac_f32 v176, v161, v81\n" \
  "v_mfma_f32_16x16x32_bf16 v[156:159], v[108:111], v[108:111], v[156:159]\n" \
  "v_fmac_f32 v176, v162, v82\n" \
  "v_fmac_f32 v176, v163, v83\n" \
  "v_mfma_f32_16x16x32_bf16 v[160:163], v[148:151], v[144:147], 0\n" \
  "v_fmac_f32 v176, v152, v84\n" \
  "v_fmac_f32 v176, v153, v85\n" \
  "v_mfma_f32_16x16x32_bf16 v[160:163], v[96:99], v[96:99], v[160:163]\n" \
  "v_fmac_f32 v176, v154, v86\n" \
  "v_fmac_f32 v176, v155, v87\n" \
  "v_mfma_f32_16x16x32_bf16 v[160:163], v[100:103], v[100:103], v[160:163]\n" \
  "s_mov_b32 s51, 7\n" \
  "v_fmac_f32 v176, v156, v88\n" \
  "v_fmac_f32 v176, v157, v89\n" \
  "v_fmac_f32 v176, v158, v90\n" \
  "v_fmac_f32 v176, v159, v91\n" \
  "s_nop 2\n" \
  "v_fmac_f32 v176, v160, v92\n" \
  "v_fmac_f32 v176, v161, v93\n" \
  "v_fmac_f32 v176, v162, v94\n" \
  "v_fma_f32 v177, v163, v95, v176\n" \
  "s_nop 1\n" \
  "v_add_f32_dpp v177, v177, v177 row_ror:8 row_mask:0xf bank_mask:0xf\n" \
  "v_mov_b32_dpp v178, v178 row_ror:4 row_mask:0xf bank_mask:0xf\n" \
  "s_nop 1\n" \
  "v_add_f32_dpp v178, v177, v177 row_ror:4 row_mask:0xf bank_mask:0x1\n" \
  "v_mov_b32 v99, v179\n" \
  "s_nop 0\n" \
  "v_mov_b32_dpp v196, v178 row_ror:8 row_mask:0xf bank_mask:0xc\n" \
  "s_nop 1\n" \
  "v_permlane16_swap_b32 v196, v197\n" \
  "v_permlane16_swap_b32 v198, v199\n" \
  "v_permlane16_swap_b32 v200, v201\n" \
  "v_permlane16_swap_b32 v202, v203\n" \
  "v_permlane16_swap_b32 v179, v99\n" \
  "v_add_f32 v196, v196, v197\n" \
  "v_add_f32 v198, v198, v199\n" \
  "v_add_f32 v200, v200, v201\n" \
  "v_add_f32 v202, v202, v203\n" \
  "v_add_f32 v179, v179, v99\n" \
  "v_lshrrev_b32 v96, 4, v188\n" \
  "v_and_b32 v97, 1, v96\n" \
  "v_mov_b32 v99, v179\n" \
  "v_lshrrev_b32 v98, 4, v96\n" \
  "v_permlane32_swap_b32 v196, v198\n" \
  "v_permlane32_swap_b32 v200, v202\n" \
  "v_permlane32_swap_b32 v179, v99\n" \
  "v_add_f32 v196, v196, v198\n" \
  "v_add_f32 v200, v200, v202\n" \
  "v_add_f32 v179, v179, v99\n" \
  "v_cmp_eq_u32 vcc, 1, v97\n" \
  "v_lshlrev_b32 v97, 2, v97\n" \
  "v_add_lshl_u32 v97, v97, v98, 2\n" \
  "v_bfe_u32 v98, v96, 2, 2\n" \
  "v_add_f32_dpp v196, v196, v196 quad_perm:[1,0,3,2] row_mask:0xf bank_mask:0xf\n" \
  "v_add_f32_dpp v200, v200, v200 quad_perm:[1,0,3,2] row_mask:0xf bank_mask:0xf\n" \
  "v_add_f32_dpp v179, v179, v179 quad_perm:[1,0,3,2] row_mask:0xf bank_mask:0xf\n" \
  "v_sub_u32 v97, v97, v98\n" \
  "v_cndmask_b32 v196, v196, v200, vcc\n" \
  "v_add_u32 v97, 1, v97\n" \
  "s_min_i32 s35, s4, 0\n" \
  "s_add_u32 s36, s4, 32\n" \
  "s_add_u32 s37, s4, 29\n" \
  "v_add_f32_dpp v179, v179, v179 quad_perm:[2,3,0,1] row_mask:0xf bank_mask:0xf\n" \
  "v_add_f32_dpp v196, v196, v196 quad_perm:[2,3,0,1] row_mask:0xf bank_mask:0xf\n" \
  "s_mov_b32 exec_lo, 0x0\n" \
  "s_mov_b32 exec_hi, 0xaaaa0000\n" \
  "v_add_u32 v97, s35, v97\n" \
  "s_mov_b32 exec_lo, 0x5500\n" \
  "s_mov_b32 exec_hi, 0x0\n" \
  "v_add_u32 v97, s36, v97\n" \
  "s_mov_b32 exec_lo, 0x4444\n" \
  "s_mov_b32 exec_hi, 0x0\n" \
  "v_sub_u32 v97, s37, v98\n" \
  "v_mov_b32 v196, v179\n" \
  "s_mov_b64 exec, -1\n" \
  "v_cmp_lt_u32 vcc, 31, v97\n" \
  "v_add_u32 v98, s5, v97\n" \
  "s_lshl2_add_u32 s35, s7, s28\n" \
  "s_lshl_b32 s35, s35, 5\n" \
  "v_cndmask_b32 v97, v97, v98, vcc\n" \
  "v_add_lshl_u32 v97, v97, s35, 2\n" \
  "s_lshl_b32 s35, s31, 16\n" \
  "s_add_u32 s35, s35, 109871104\n" \
  "s_add_u32 s40, s24, s35\n" \
  "s_addc_u32 s41, s25, 0\n" \
  "v_min_u32 v196, -2, v196\n" \
  "v_lshlrev_b32 v184, 16, v204\n" \
  "v_and_b32 v185, 0xffff0000, v204\n" \
  "v_lshlrev_b32 v186, 16, v205\n" \
  "v_and_b32 v187, 0xffff0000, v205\n" \
  "s_barrier\n" \
  "s_add_u32 s56, s56, 32768\n" \
  "s_addc_u32 s57, s57, 0\n" \
  "global_load_dwordx4 v[224:227], v188, s[56:57] sc1\n" \
  "global_load_dwordx4 v[228:231], v188, s[56:57] offset:1024 sc1\n" \
  "global_load_dwordx4 v[232:235], v188, s[56:57] offset:2048 sc1\n" \
  "global_load_dwordx4 v[236:239], v188, s[56:57] offset:3072 sc1\n" \
  "s_add_u32 s56, s56, 32768\n" \
  "s_addc_u32 s57, s57, 0\n" \
  "s_mov_b32 exec_lo, 0x33337777\n" \
  "s_mov_b32 exec_hi, 0x33333333\n" \
  "global_store_dword v97, v196, s[40:41] sc1\n" \
  "s_mov_b64 exec, -1\n" \
  "s_waitcnt vmcnt(9)\n" \
  "s_setprio 0\n" \
  "s_branch LG_start\n" \
  "LA_loop:\n" \
  "v_mfma_f32_16x16x32_bf16 v[152:155], v[112:115], v[112:115], 0\n" \
  "s_waitcnt lgkmcnt(8)\n" \
  "ds_read_b128 v[100:103], v185 offset:64\n" \
  "v_fmac_f32 v176, v156, v88\n" \
  "v_fmac_f32 v176, v157, v89\n" \
  "v_mfma_f32_16x16x32_bf16 v[152:155], v[116:119], v[116:119], v[152:155]\n" \
  "ds_read2_b32 v[144:145], v186 offset0:0 offset1:2\n" \
  "v_fmac_f32 v176, v158, v90\n" \
  "v_fmac_f32 v176, v159, v91\n" \
  "v_mfma_f32_16x16x32_bf16 v[156:159], v[128:131], v[128:131], 0\n" \
  "ds_read2_b32 v[146:147], v186 offset0:4 offset1:6\n" \
  "v_fmac_f32 v176, v160, v92\n" \
  "v_fmac_f32 v176, v161, v93\n" \
  "v_mad_u32_u16 v187, v183, 16, v190\n" \
  "v_mfma_f32_16x16x32_bf16 v[156:159], v[132:135], v[132:135], v[156:159]\n" \
  "s_waitcnt lgkmcnt(10)\n" \
  "ds_read2_b32 v[148:149], v187 offset0:0 offset1:2\n" \
  "v_fmac_f32 v176, v162, v94\n" \
  "v_fma_f32 v177, v163, v95, v176\n" \
  "v_mfma_f32_16x16x32_bf16 v[160:163], v[128:131], v[136:139], 0\n" \
  "s_waitcnt lgkmcnt(10)\n" \
  "ds_read2_b32 v[150:151], v187 offset0:4 offset1:6\n" \
  "v_mul_f32 v176, v152, v0\n" \
  "v_fmac_f32 v176, v153, v1\n" \
  "v_mfma_f32_16x16x32_bf16 v[160:163], v[132:135], v[140:143], v[160:163]\n" \
  "ds_read_b128 v[180:183], v194\n" \
  "v_fmac_f32 v176, v154, v2\n" \
  "v_fmac_f32 v176, v155, v3\n" \
  "v_mfma_f32_16x16x32_bf16 v[152:155], v[136:139], v[136:139], 0\n" \
  "v_fmac_f32 v176, v156, v4\n" \
  "v_fmac_f32 v176, v157, v5\n" \
  "v_mfma_f32_16x16x32_bf16 v[152:155], v[140:143], v[140:143], v[152:155]\n" \
  "ds_read_b128 v[164:167], v191\n" \
  "v_fmac_f32 v176, v158, v6\n" \
  "v_fmac_f32 v176, v159, v7\n" \
  "v_mfma_f32_16x16x32_bf16 v[156:159], v[112:115], v[136:139], 0\n" \
  "v_fmac_f32 v176, v160, v8\n" \
  "v_fmac_f32 v176, v161, v9\n" \
  "v_mfma_f32_16x16x32_bf16 v[156:159], v[116:119], v[140:143], v[156:159]\n" \
  "v_fmac_f32 v176, v162, v10\n" \
  "v_fmac_f32 v176, v163, v11\n" \
  "v_mfma_f32_16x16x32_bf16 v[160:163], v[112:115], v[128:131], 0\n" \
  "v_fmac_f32 v176, v152, v12\n" \
  "v_fmac_f32 v176, v153, v13\n" \
  "v_add_f32_dpp v177, v177, v177 row_ror:8 row_mask:0xf bank_mask:0xf\n" \
  "v_mfma_f32_16x16x32_bf16 v[160:163], v[116:119], v[132:135], v[160:163]\n" \
  "s_waitcnt lgkmcnt(11)\n" \
  "v_fmac_f32 v176, v154, v14\n" \
  "v_fmac_f32 v176, v155, v15\n" \
  "v_mfma_f32_16x16x32_bf16 v[152:155], v[112:115], v[120:123], 0\n" \
  "s_waitcnt lgkmcnt(10)\n" \
  "v_fmac_f32 v176, v156, v16\n" \
  "v_fmac_f32 v176, v157, v17\n" \
  "v_mfma_f32_16x16x32_bf16 v[152:155], v[116:119], v[124:127], v[152:155]\n" \
  "v_fmac_f32 v176, v158, v18\n" \
  "v_fmac_f32 v176, v159, v19\n" \
  "v_mov_b32_dpp v178, v178 row_ror:4 row_mask:0xf bank_mask:0xf\n" \
  "v_mfma_f32_16x16x32_bf16 v[156:159], v[120:123], v[136:139], 0\n" \
  "v_fmac_f32 v176, v160, v20\n" \
  "v_fmac_f32 v176, v161, v21\n" \
  "v_mfma_f32_16x16x32_bf16 v[156:159], v[124:127], v[140:143], v[156:159]\n" \
  "s_waitcnt lgkmcnt(9)\n" \
  "v_fmac_f32 v176, v162, v22\n" \
  "v_fmac_f32 v176, v163, v23\n" \
  "v_mfma_f32_16x16x32_bf16 v[160:163], v[104:107], v[128:131], 0\n" \
  "s_waitcnt lgkmcnt(1)\n" \
  "v_fmac_f32 v176, v152, v24\n" \
  "v_fmac_f32 v176, v153, v25\n" \
  "v_mad_u32_u16 v184, v182, 16, v188\n" \
  "v_mfma_f32_16x16x32_bf16 v[160:163], v[108:111], v[132:135], v[160:163]\n" \
  "v_fmac_f32 v176, v154, v26\n" \
  "v_fmac_f32 v176, v155, v27\n" \
  "v_mfma_f32_16x16x32_bf16 v[152:155], v[120:123], v[128:131], 0\n" \
  "v_fmac_f32 v176, v156, v28\n" \
  "v_fmac_f32 v176, v157, v29\n" \
  "v_mfma_f32_16x16x32_bf16 v[152:155], v[124:127], v[132:135], v[152:155]\n" \
  "v_fmac_f32 v176, v158, v30\n" \
  "v_fmac_f32 v176, v159, v31\n" \
  "v_add_f32_dpp v178, v177, v177 row_ror:4 row_mask:0xf bank_mask:0x1\n" \
  "v_mfma_f32_16x16x32_bf16 v[156:159], v[96:99], v[128:131], 0\n" \
  "v_fmac_f32 v176, v160, v32\n" \
  "v_fmac_f32 v176, v161, v33\n" \
  "v_mfma_f32_16x16x32_bf16 v[156:159], v[100:103], v[132:135], v[156:159]\n" \
  "v_fmac_f32 v176, v162, v34\n" \
  "v_fmac_f32 v176, v163, v35\n" \
  "v_smfmac_f32_16x16x64_bf16 v[172:175], v[144:147], v[128:135], v193\n" \
  "ds_read_b128 v[128:131], v184 offset:0\n" \
  "v_fmac_f32 v176, v152, v36\n" \
  "v_fmac_f32 v176, v153, v37\n" \
  "v_smfmac_f32_16x16x64_bf16 v[172:175], v[148:151], v[136:143], v193\n" \
  "ds_read_b128 v[132:135], v184 offset:64\n" \
  "v_fmac_f32 v176, v154, v38\n" \
  "v_fmac_f32 v176, v155, v39\n" \
  "v_mfma_f32_16x16x32_bf16 v[160:163], v[96:99], v[112:115], 0\n" \
  "v_fmac_f32 v176, v156, v40\n" \
  "v_fmac_f32 v176, v157, v41\n" \
  "v_mfma_f32_16x16x32_bf16 v[160:163], v[100:103], v[116:119], v[160:163]\n" \
  "v_fmac_f32 v176, v158, v42\n" \
  "v_fmac_f32 v176, v159, v43\n" \
  "v_add_u32 v194, 0x100, v194\n" \
  "v_mfma_f32_16x16x32_bf16 v[152:155], v[104:107], v[112:115], 0\n" \
  "v_fmac_f32 v176, v172, v44\n" \
  "v_fmac_f32 v176, v173, v45\n" \
  "v_mfma_f32_16x16x32_bf16 v[152:155], v[108:111], v[116:119], v[152:155]\n" \
  "v_fmac_f32 v176, v174, v46\n" \
  "v_fmac_f32 v176, v175, v47\n" \
  "v_mfma_f32_16x16x32_bf16 v[156:159], v[104:107], v[136:139], 0\n" \
  "ds_read_b128 v[172:175], v191\n" \
  "v_fmac_f32 v176, v160, v48\n" \
  "v_fmac_f32 v176, v161, v49\n" \
  "v_mad_u32_u16 v185, v181, 16, v188\n" \
  "v_mfma_f32_16x16x32_bf16 v[156:159], v[108:111], v[140:143], v[156:159]\n" \
  "v_fmac_f32 v176, v162, v50\n" \
  "v_fmac_f32 v176, v163, v51\n" \
  "v_mad_u32_u16 v186, v182, 16, v188 op_sel:[1,0,0,0]\n" \
  "v_smfmac_f32_16x16x64_bf16 v[168:171], v[144:147], v[112:119], v193\n" \
  "ds_read_b128 v[112:115], v185 offset:0\n" \
  "v_fmac_f32 v176, v152, v52\n" \
  "v_fmac_f32 v176, v153, v53\n" \
  "v_smfmac_f32_16x16x64_bf16 v[168:171], v[148:151], v[120:127], v193\n" \
  "ds_read_b128 v[116:119], v185 offset:64\n" \
  "v_fmac_f32 v176, v154, v54\n" \
  "v_fmac_f32 v176, v155, v55\n" \
  "v_mfma_f32_16x16x32_bf16 v[160:163], v[96:99], v[136:139], 0\n" \
  "v_fmac_f32 v176, v156, v56\n" \
  "v_fmac_f32 v176, v157, v57\n" \
  "v_mfma_f32_16x16x32_bf16 v[160:163], v[100:103], v[140:143], v[160:163]\n" \
  "s_waitcnt lgkmcnt(5)\n" \
  "v_fmac_f32 v176, v158, v58\n" \
  "v_fmac_f32 v176, v159, v59\n" \
  "v_smfmac_f32_16x16x64_bf16 v[164:167], v[148:151], v[104:111], v193\n" \
  "ds_read_b128 v[136:139], v186 offset:0\n" \
  "v_fmac_f32 v176, v168, v60\n" \
  "v_fmac_f32 v176, v169, v61\n" \
  "v_smfmac_f32_16x16x64_bf16 v[164:167], v[144:147], v[96:103], v193\n" \
  "ds_read_b128 v[140:143], v186 offset:64\n" \
  "v_fmac_f32 v176, v170, v62\n" \
  "v_fmac_f32 v176, v171, v63\n" \
  "v_mfma_f32_16x16x32_bf16 v[152:155], v[120:123], v[120:123], 0\n" \
  "v_fmac_f32 v176, v160, v64\n" \
  "v_fmac_f32 v176, v161, v65\n" \
  "v_mfma_f32_16x16x32_bf16 v[152:155], v[124:127], v[124:127], v[152:155]\n" \
  "ds_read_b128 v[168:171], v191\n" \
  "v_fmac_f32 v176, v162, v66\n" \
  "v_fmac_f32 v176, v163, v67\n" \
  "v_mad_u32_u16 v187, v181, 16, v188 op_sel:[1,0,0,0]\n" \
  "v_mfma_f32_16x16x32_bf16 v[156:159], v[104:107], v[120:123], 0\n" \
  "v_fmac_f32 v176, v164, v68\n" \
  "v_fmac_f32 v176, v165, v69\n" \
  "v_mad_u32_u16 v184, v180, 16, v188 op_sel:[1,0,0,0]\n" \
  "v_mfma_f32_16x16x32_bf16 v[156:159], v[108:111], v[124:127], v[156:159]\n" \
  "v_fmac_f32 v176, v166, v70\n" \
  "v_fmac_f32 v176, v167, v71\n" \
  "v_mfma_f32_16x16x32_bf16 v[160:163], v[96:99], v[120:123], 0\n" \
  "ds_read_b128 v[120:123], v187 offset:0\n" \
  "v_fmac_f32 v176, v152, v72\n" \
  "v_fmac_f32 v176, v153, v73\n" \
  "v_mfma_f32_16x16x32_bf16 v[160:163], v[100:103], v[124:127], v[160:163]\n" \
  "v_fmac_f32 v176, v154, v74\n" \
  "v_fmac_f32 v176, v155, v75\n" \
  "v_mad_u32_u16 v185, v180, 16, v188\n" \
  "v_mfma_f32_16x16x32_bf16 v[152:155], v[96:99], v[104:107], 0\n" \
  "ds_read_b128 v[124:127], v187 offset:64\n" \
  "v_fmac_f32 v176, v156, v76\n" \
  "v_fmac_f32 v176, v157, v77\n" \
  "v_mfma_f32_16x16x32_bf16 v[152:155], v[100:103], v[108:111], v[152:155]\n" \
  "v_fmac_f32 v176, v158, v78\n" \
  "v_fmac_f32 v176, v159, v79\n" \
  "v_mfma_f32_16x16x32_bf16 v[156:159], v[104:107], v[104:107], 0\n" \
  "ds_read_b128 v[104:107], v184 offset:0\n" \
  "v_fmac_f32 v176, v160, v80\n" \
  "v_fmac_f32 v176, v161, v81\n" \
  "v_mfma_f32_16x16x32_bf16 v[156:159], v[108:111], v[108:111], v[156:159]\n" \
  "s_sub_u32 s51, s51, 1\n" \
  "v_fmac_f32 v176, v162, v82\n" \
  "v_fmac_f32 v176, v163, v83\n" \
  "v_mad_u32_u16 v186, v183, 16, v189\n" \
  "v_mfma_f32_16x16x32_bf16 v[160:163], v[148:151], v[144:147], 0\n" \
  "s_cmp_lg_u32 s51, 0\n" \
  "ds_read_b128 v[108:111], v184 offset:64\n" \
  "v_fmac_f32 v176, v152, v84\n" \
  "v_fmac_f32 v176, v153, v85\n" \
  "v_mfma_f32_16x16x32_bf16 v[160:163], v[96:99], v[96:99], v[160:163]\n" \
  "s_waitcnt lgkmcnt(8)\n" \
  "ds_read_b128 v[96:99], v185 offset:0\n" \
  "v_fmac_f32 v176, v154, v86\n" \
  "v_fmac_f32 v176, v155, v87\n" \
  "v_mfma_f32_16x16x32_bf16 v[160:163], v[100:103], v[100:103], v[160:163]\n" \
  "s_cbranch_scc1 LA_loop\n" \
  "s_setpc_b64 s[52:53]\n" \
  "LB_start:\n" \
  "s_setprio 3\n" \
  "s_mov_b32 s37, 12\n" \
  "L3_:\n" \
  "s_sub_u32 s35, s36, 125\n" \
  "s_min_u32 s35, s35, s36\n" \
  "s_mul_i32 m0, s35, 1040\n" \
  "s_lshl_b32 s35, s35, 11\n" \
  "s_add_u32 s42, s40, s35\n" \
  "s_addc_u32 s43, s41, 0\n" \
  "global_load_lds_dwordx4 v254, s[42:43]\n" \
  "s_add_u32 s36, s36, 4\n" \
  "s_sub_u32 s37, s37, 1\n" \
  "s_cmp_lg_u32 s37, 0\n" \
  "s_cbranch_scc1 L3_\n" \
  "v_mov_b32 v190, 163328\n" \
  "v_mov_b64 v[2:3], 0\n" \
  "ds_write_b64 v190, v[2:3]\n" \
  "ds_write_b64 v190, v[2:3] offset:8\n" \
  "ds_write_b64 v190, v[2:3] offset:16\n" \
  "s_waitcnt lgkmcnt(0)\n" \
  "s_barrier\n" \
  "v_lshlrev_b32 v247, 7, v255\n" \
  "s_mov_b32 s37, 20\n" \
  "L4_:\n" \
  "s_sub_u32 s35, s36, 125\n" \
  "s_min_u32 s35, s35, s36\n" \
  "s_mul_i32 m0, s35, 1040\n" \
  "s_lshl_b32 s35, s35, 11\n" \
  "s_add_u32 s42, s40, s35\n" \
  "s_addc_u32 s43, s41, 0\n" \
  "global_load_lds_dwordx4 v254, s[42:43]\n" \
  "s_add_u32 s36, s36, 4\n" \
  "s_sub_u32 s37, s37, 1\n" \
  "s_cmp_lg_u32 s37, 0\n" \
  "s_cbranch_scc1 L4_\n" \
  "s_add_u32 s48, s24, 110002176\n" \
  "s_addc_u32 s49, s25, 0\n" \
  "s_lshl_b32 s35, s26, 2\n" \
  "v_mov_b32 v193, s35\n" \
  "global_load_dword v193, v193, s[48:49] offset:1024 sc1\n" \
  "v_mov_b32 v191, 1\n" \
  "v_and_b32 v252, 15, v255\n" \
  "v_lshrrev_b32 v253, 4, v255\n" \
  "s_lshl2_add_u32 s35, s31, s28\n" \
  "s_lshl_b32 s35, s35, 1\n" \
  "v_bfe_u32 v1, v255, 2, 1\n" \
  "v_add_u32 v1, s35, v1\n" \
  "v_and_b32 v2, 3, v255\n" \
  "v_lshlrev_b32 v3, 5, v1\n" \
  "v_lshl_add_u32 v3, v2, 3, v3\n" \
  "v_lshlrev_b32 v4, 2, v2\n" \
  "v_add_u32 v4, 380, v4\n" \
  "v_cmp_gt_u32 vcc, 12, v1\n" \
  "v_lshrrev_b32 v5, 3, v255\n" \
  "v_mul_u32_u24 v5, 6400, v5\n" \
  "v_cndmask_b32 v3, v4, v3, vcc\n" \
  "v_add_u32 v5, 3200, v5\n" \
  "v_add_u32 v227, v5, v3\n" \
  "v_lshl_add_u32 v3, v1, 2, v2\n" \
  "v_add_u32 v4, 48, v3\n" \
  "v_mul_u32_u24 v5, 205, v3\n" \
  "v_lshrrev_b32 v5, 11, v5\n" \
  "v_mul_u32_u24 v5, 10, v5\n" \
  "v_sub_u32 v5, v3, v5\n" \
  "v_lshlrev_b32 v236, 26, v5\n" \
  "v_mul_u32_u24 v5, 205, v4\n" \
  "v_lshrrev_b32 v5, 11, v5\n" \
  "v_mul_u32_u24 v5, 10, v5\n" \
  "v_sub_u32 v5, v4, v5\n" \
  "v_lshlrev_b32 v237, 26, v5\n" \
  "s_lshl2_add_u32 s4, s31, s28\n" \
  "s_lshl_b32 s4, s4, 7\n" \
  "s_add_u32 s36, s4, 96\n" \
  "s_min_u32 s36, s36, 984\n" \
  "s_add_u32 s37, s4, 112\n" \
  "s_min_u32 s37, s37, 984\n" \
  "v_add_u32 v186, s4, v252\n" \
  "v_add_u32 v187, s36, v252\n" \
  "v_add_u32 v188, s37, v252\n" \
  "s_mul_i32 s38, s7, 51200\n" \
  "s_add_u32 s42, s8, s38\n" \
  "s_addc_u32 s43, s9, 0\n" \
  "s_xor_b32 s38, s31, 1\n" \
  "s_lshl_b32 s38, s38, 7\n" \
  "s_add_u32 s94, s10, s38\n" \
  "s_addc_u32 s95, s11, 0\n" \
  "s_mul_i32 s5, s28, 4224\n" \
  "s_add_u32 s5, s5, 146432\n" \
  "v_lshrrev_b32 v2, 3, v186\n" \
  "v_lshl_add_u32 v2, v186, 3, v2\n" \
  "v_add_lshl_u32 v249, v2, v253, 4\n" \
  "v_lshrrev_b32 v2, 3, v187\n" \
  "v_lshl_add_u32 v2, v187, 3, v2\n" \
  "v_add_lshl_u32 v250, v2, v253, 4\n" \
  "v_lshrrev_b32 v2, 3, v188\n" \
  "v_lshl_add_u32 v2, v188, 3, v2\n" \
  "v_add_lshl_u32 v251, v2, v253, 4\n" \
  "v_mul_u32_u24 v1, 1600000, v253\n" \
  "v_lshl_add_u32 v186, v186, 4, v1\n" \
  "v_lshl_add_u32 v187, v187, 4, v1\n" \
  "v_lshl_add_u32 v188, v188, 4, v1\n" \
  "v_lshlrev_b32 v1, 4, v253\n" \
  "v_xor_b32 v244, v1, v254\n" \
  "v_lshl_add_u32 v245, v255, 4, s5\n" \
  "v_lshrrev_b32 v1, 1, v252\n" \
  "v_xor_b32 v1, v1, v253\n" \
  "v_lshlrev_b32 v1, 4, v1\n" \
  "v_lshl_add_u32 v1, v252, 7, v1\n" \
  "v_add_u32 v246, s5, v1\n" \
  "v_xor_b32 v1, 64, v1\n" \
  "v_add_u32 v242, s5, v1\n" \
  "v_lshlrev_b32 v1, 4, v255\n" \
  "v_xor_b32 v1, 64, v1\n" \
  "v_add_u32 v243, s5, v1\n" \
  "v_lshrrev_b32 v1, 2, v255\n" \
  "s_lshl_b32 s35, s28, 5\n" \
  "s_lshl_b32 s36, s31, 6\n" \
  "s_add_u32 s35, s35, s36\n" \
  "s_and_b32 s35, s35, 127\n" \
  "v_add_u32 v1, s35, v1\n" \
  "v_lshrrev_b32 v2, 4, v255\n" \
  "v_and_b32 v2, 2, v2\n" \
  "v_xor_b32 v2, v2, v255\n" \
  "v_and_b32 v2, 3, v2\n" \
  "v_lshlrev_b32 v2, 4, v2\n" \
  "v_lshl_add_u32 v189, v1, 10, v2\n" \
  "v_add_u32 v194, 15360, v189\n" \
  "v_lshrrev_b32 v1, 2, v252\n" \
  "v_lshlrev_b32 v1, 6, v1\n" \
  "v_lshl_add_u32 v1, v253, 9, v1\n" \
  "v_and_b32 v2, 3, v252\n" \
  "v_lshl_add_u32 v1, v2, 3, v1\n" \
  "v_and_b32 v2, 1, v253\n" \
  "v_lshl_add_u32 v1, v2, 5, v1\n" \
  "v_add_u32 v184, 138240, v1\n" \
  "v_xor_b32 v1, 32, v1\n" \
  "v_add_u32 v185, 138240, v1\n" \
  "s_bfe_u32 s35, s30, 0x10001\n" \
  "s_lshl1_add_u32 s35, s29, s35\n" \
  "s_lshr_b32 s6, s30, 2\n" \
  "s_lshl_b32 s6, s6, 18\n" \
  "s_lshl_b32 s38, s35, 6\n" \
  "s_add_u32 s92, s14, s38\n" \
  "s_addc_u32 s93, s15, 0\n" \
  "s_lshr_b32 s37, s30, 3\n" \
  "s_add_u32 s37, s37, 96\n" \
  "s_lshl_b32 s34, s37, 17\n" \
  "s_mul_i32 s35, s35, 400\n" \
  "s_add_u32 s37, s37, s35\n" \
  "s_mul_i32 s37, s37, 16000\n" \
  "s_add_u32 s46, s24, s37\n" \
  "s_addc_u32 s47, s25, 0\n" \
  "s_lshr_b32 s36, s30, 2\n" \
  "s_add_u32 s36, s36, s35\n" \
  "s_mul_i32 s36, s36, 16000\n" \
  "s_add_u32 s44, s24, s36\n" \
  "s_addc_u32 s45, s25, 0\n" \
  "s_lshl_b32 s96, s28, 11\n" \
  "s_add_u32 s96, s96, 138240\n" \
  "v_lshlrev_b32 v238, 4, v255\n" \
  "v_mov_b32 v234, 0\n" \
  "s_lshl2_add_u32 s33, s26, s28\n" \
  "s_mov_b32 s52, 0x10002\n" \
  "s_mov_b32 s53, 0xc0c0400\n" \
  "s_mov_b32 s54, 0x4000c0c\n" \
  "s_mov_b32 s55, 0x3030303\n" \
  "s_mov_b32 s56, 0x55555555\n" \
  "s_mov_b32 s38, 0x0\n" \
  "s_mov_b32 s98, 0x0\n" \
  "s_mov_b32 s99, 0x8\n" \
  "s_mov_b32 s97, 0x2000\n" \
  "v_xor_b32 v245, 64, v244\n" \
  "v_add_u32 v245, 0x400, v245\n" \
  "s_waitcnt vmcnt(0)\n" \
  "s_mov_b64 exec, 1\n" \
  "ds_add_u32 v190, v191 offset:12\n" \
  "s_mov_b64 exec, -1\n" \
  "s_memtime s[88:89]\n" \
  "s_setprio 0\n" \
  "L5_:\n" \
  "ds_read_b32 v192, v190 offset:16\n" \
  "s_waitcnt lgkmcnt(0)\n" \
  "v_readfirstlane_b32 s35, v192\n" \
  "s_cmp_lt_u32 s35, 4\n" \
  "s_cbranch_scc1 L5_\n" \
  "s_add_u32 s35, s4, 0\n" \
  "s_min_u32 s35, s35, 984\n" \
  "s_lshl_b32 s35, s35, 8\n" \
  "s_add_u32 m0, s5, 0\n" \
  "s_add_u32 s58, s94, s35\n" \
  "s_addc_u32 s59, s95, 0\n" \
  "global_load_lds_dwordx4 v244, s[58:59]\n" \
  "global_load_lds_dwordx4 v245, s[58:59] offset:1024\n" \
  "s_add_u32 s35, s4, 16\n" \
  "s_min_u32 s35, s35, 984\n" \
  "s_lshl_b32 s35, s35, 8\n" \
  "s_add_u32 m0, s5, 2048\n" \
  "s_add_u32 s58, s94, s35\n" \
  "s_addc_u32 s59, s95, 0\n" \
  "global_load_lds_dwordx4 v244, s[58:59]\n" \
  "global_load_lds_dwordx4 v245, s[58:59] offset:1024\n" \
  "s_add_u32 s40, s92, s6\n" \
  "s_addc_u32 s41, s93, 0\n" \
  "s_add_u32 s6, s6, 2097152\n" \
  "s_mov_b32 m0, s96\n" \
  "s_nop 0\n" \
  "global_load_lds_dwordx4 v189, s[40:41]\n" \
  "global_load_lds_dwordx4 v194, s[40:41] offset:1024\n" \
  "L6_:\n" \
  "ds_read_b32 v192, v190 offset:12\n" \
  "s_waitcnt lgkmcnt(0)\n" \
  "v_readfirstlane_b32 s35, v192\n" \
  "s_cmp_lt_u32 s35, 4\n" \
  "s_cbranch_scc1 L6_\n" \
  "s_waitcnt vmcnt(2)\n" \
  "ds_read_b128 v[8:11], v246 offset:0\n" \
  "ds_read_b128 v[12:15], v242 offset:0\n" \
  "ds_read_b128 v[24:27], v246 offset:2048\n" \
  "ds_read_b128 v[28:31], v242 offset:2048\n" \
  "ds_read_b128 v[0:3], v249 offset:0\n" \
  "ds_read_b128 v[4:7], v249 offset:64\n" \
  "ds_read_b128 v[16:19], v249 offset:2080\n" \
  "ds_read_b128 v[20:23], v249 offset:2144\n" \
  "s_waitcnt lgkmcnt(4)\n" \
  "global_load_dwordx2 v[196:197], v227, s[42:43] offset:-3200\n" \
  "global_load_dwordx2 v[198:199], v227, s[42:43] offset:-2800\n" \
  "global_load_dwordx2 v[200:201], v227, s[42:43] offset:-2400\n" \
  "global_load_dwordx2 v[202:203], v227, s[42:43] offset:-1600\n" \
  "global_load_dwordx2 v[204:205], v227, s[42:43] offset:-1200\n" \
  "global_load_dwordx2 v[206:207], v227, s[42:43] offset:-800\n" \
  "global_load_dwordx2 v[208:209], v227, s[42:43] offset:-400\n" \
  "global_load_dwordx2 v[210:211], v227, s[42:43] offset:0\n" \
  "global_load_dwordx2 v[212:213], v227, s[42:43] offset:400\n" \
  "global_load_dwordx2 v[214:215], v227, s[42:43] offset:800\n" \
  "global_load_dwordx2 v[216:217], v227, s[42:43] offset:1200\n" \
  "global_load_dwordx2 v[218:219], v227, s[42:43] offset:-2000\n" \
  "global_load_dwordx2 v[220:221], v227, s[42:43] offset:1600\n" \
  "global_load_dwordx2 v[222:223], v227, s[42:43] offset:2000\n" \
  "global_load_dwordx2 v[224:225], v227, s[42:43] offset:2400\n" \
  "global_load_dwordx2 v[226:227], v227, s[42:43] offset:2800\n" \
  "s_add_u32 s35, s4, 32\n" \
  "s_min_u32 s35, s35, 984\n" \
  "s_lshl_b32 s35, s35, 8\n" \
  "s_add_u32 m0, s5, 0\n" \
  "s_add_u32 s58, s94, s35\n" \
  "s_addc_u32 s59, s95, 0\n" \
  "global_load_lds_dwordx4 v244, s[58:59]\n" \
  "global_load_lds_dwordx4 v245, s[58:59] offset:1024\n" \
  "s_add_u32 s35, s4, 48\n" \
  "s_min_u32 s35, s35, 984\n" \
  "s_lshl_b32 s35, s35, 8\n" \
  "s_add_u32 m0, s5, 2048\n" \
  "s_add_u32 s58, s94, s35\n" \
  "s_addc_u32 s59, s95, 0\n" \
  "global_load_lds_dwordx4 v244, s[58:59]\n" \
  "global_load_lds_dwordx4 v245, s[58:59] offset:1024\n" \
  "s_waitcnt vmcnt(0)\n" \
  "ds_read_b128 v[40:43], v246 offset:0\n" \
  "ds_read_b128 v[44:47], v242 offset:0\n" \
  "ds_read_b128 v[56:59], v246 offset:2048\n" \
  "ds_read_b128 v[60:63], v242 offset:2048\n" \
  "ds_read_b128 v[32:35], v249 offset:4160\n" \
  "ds_read_b128 v[36:39], v249 offset:4224\n" \
  "ds_read_b128 v[48:51], v249 offset:6240\n" \
  "ds_read_b128 v[52:55], v249 offset:6304\n" \
  "s_waitcnt lgkmcnt(4)\n" \
  "s_lshl2_add_u32 s35, s26, s28\n" \
  "s_mul_i32 s35, s35, 6400\n" \
  "s_add_u32 s35, s35, 0\n" \
  "s_add_u32 s58, s14, s35\n" \
  "s_addc_u32 s59, s15, 0\n" \
  "s_mov_b32 exec_lo, 0xffffffff\n" \
  "s_mov_b32 exec_hi, 0x3ffff\n" \
  "global_load_dword v248, v247, s[58:59]\n" \
  "s_mov_b64 exec, -1\n" \
  "s_add_u32 s35, s4, 64\n" \
  "s_min_u32 s35, s35, 984\n" \
  "s_lshl_b32 s35, s35, 8\n" \
  "s_add_u32 m0, s5, 0\n" \
  "s_add_u32 s58, s94, s35\n" \
  "s_addc_u32 s59, s95, 0\n" \
  "global_load_lds_dwordx4 v244, s[58:59]\n" \
  "global_load_lds_dwordx4 v245, s[58:59] offset:1024\n" \
  "s_add_u32 s35, s4, 80\n" \
  "s_min_u32 s35, s35, 984\n" \
  "s_lshl_b32 s35, s35, 8\n" \
  "s_add_u32 m0, s5, 2048\n" \
  "s_add_u32 s58, s94, s35\n" \
  "s_addc_u32 s59, s95, 0\n" \
  "global_load_lds_dwordx4 v244, s[58:59]\n" \
  "global_load_lds_dwordx4 v245, s[58:59] offset:1024\n" \
  "s_waitcnt vmcnt(0)\n" \
  "ds_read_b128 v[72:75], v246 offset:0\n" \
  "ds_read_b128 v[76:79], v242 offset:0\n" \
  "ds_read_b128 v[88:91], v246 offset:2048\n" \
  "ds_read_b128 v[92:95], v242 offset:2048\n" \
  "ds_read_b128 v[64:67], v249 offset:8320\n" \
  "ds_read_b128 v[68:71], v249 offset:8384\n" \
  "ds_read_b128 v[80:83], v249 offset:10400\n" \
  "ds_read_b128 v[84:87], v249 offset:10464\n" \
  "s_waitcnt lgkmcnt(4)\n" \
  "s_add_u32 s35, s4, 96\n" \
  "s_min_u32 s35, s35, 984\n" \
  "s_lshl_b32 s35, s35, 8\n" \
  "s_add_u32 m0, s5, 0\n" \
  "s_add_u32 s58, s94, s35\n" \
  "s_addc_u32 s59, s95, 0\n" \
  "global_load_lds_dwordx4 v244, s[58:59]\n" \
  "global_load_lds_dwordx4 v245, s[58:59] offset:1024\n" \
  "s_add_u32 s35, s4, 112\n" \
  "s_min_u32 s35, s35, 984\n" \
  "s_lshl_b32 s35, s35, 8\n" \
  "s_add_u32 m0, s5, 2048\n" \
  "s_add_u32 s58, s94, s35\n" \
  "s_addc_u32 s59, s95, 0\n" \
  "global_load_lds_dwordx4 v244, s[58:59]\n" \
  "global_load_lds_dwordx4 v245, s[58:59] offset:1024\n" \
  "s_getpc_b64 s[58:59]\n" \
  "LB_pc:\n" \
  "s_sub_u32 s58, s58, LB_pc-LZ_first\n" \
  "s_subb_u32 s59, s59, 0\n" \
  "s_mul_i32 s35, s28, 6656\n" \
  "v_lshl_add_u32 v195, v255, 7, s35\n" \
  "v_min_u32 v195, LZ_end-LZ_first, v195\n" \
  "s_mov_b32 exec_hi, 0xfffff\n" \
  "global_load_dword v248, v195, s[58:59]\n" \
  "s_mov_b64 exec, -1\n" \
  "s_lshl2_add_u32 s35, s26, s28\n" \
  "s_mul_i32 s35, s35, 6400\n" \
  "s_add_u32 s35, s35, 6553600\n" \
  "s_add_u32 s58, s14, s35\n" \
  "s_addc_u32 s59, s15, 0\n" \
  "s_mov_b32 exec_lo, 0xffffffff\n" \
  "s_mov_b32 exec_hi, 0x3ffff\n" \
  "global_load_dword v248, v247, s[58:59]\n" \
  "s_mov_b64 exec, -1\n" \
  "s_waitcnt vmcnt(2)\n" \
  "ds_read_b128 v[104:107], v246 offset:0\n" \
  "ds_read_b128 v[108:111], v242 offset:0\n" \
  "ds_read_b128 v[120:123], v246 offset:2048\n" \
  "ds_read_b128 v[124:127], v242 offset:2048\n" \
  "ds_read_b128 v[96:99], v250 offset:0\n" \
  "ds_read_b128 v[100:103], v250 offset:64\n" \
  "ds_read_b128 v[112:115], v251 offset:0\n" \
  "ds_read_b128 v[116:119], v251 offset:64\n" \
  "s_waitcnt lgkmcnt(4)\n" \
  "s_waitcnt vmcnt(0)\n" \
  "s_sleep 8\n" \
  "s_setprio 2\n" \
  "s_mul_i32 s36, s28, 2176\n" \
  "s_add_u32 s36, s36, 154624\n" \
  "v_bfe_u32 v228, v255, 3, 1\n" \
  "v_and_b32 v229, 1, v255\n" \
  "v_lshl_add_u32 v230, v228, 1, v229\n" \
  "v_lshl_add_u32 v231, v229, 1, v228\n" \
  "v_xor_b32 v229, 1, v230\n" \
  "s_mov_b32 vcc_lo, 0xcccccccc\n" \
  "s_mov_b32 vcc_hi, vcc_lo\n" \
  "v_cndmask_b32 v228, v230, v231, vcc\n" \
  "v_cndmask_b32 v229, v229, v231, vcc\n" \
  "v_and_b32 v230, 4, v255\n" \
  "v_mul_u32_u24 v230, 272, v230\n" \
  "v_lshl_add_u32 v230, v253, 2, v230\n" \
  "v_and_or_b32 v230, v255, 2, v230\n" \
  "v_add_u32 v230, s36, v230\n" \
  "s_movk_i32 s35, 272\n" \
  "v_mad_u32_u24 v195, v228, s35, v230\n" \
  "v_mad_u32_u24 v235, v229, s35, v230\n" \
  "v_lshl_add_u32 v230, v253, 4, v238\n" \
  "v_add_u32 v254, s36, v230\n" \
  "v_readfirstlane_b32 s39, v193\n" \
  "s_lshl_b32 s35, s26, 2\n" \
  "s_add_u32 s39, s39, 1\n" \
  "v_mov_b32 v193, s35\n" \
  "v_mov_b64 v[240:241], -1\n" \
  "v_mov_b64 v[242:243], -1\n" \
  "s_waitcnt lgkmcnt(0)\n" \
  "s_mov_b64 exec, 1\n" \
  "ds_add_u32 v190, v191 offset:0\n" \
  "s_mov_b64 exec, -1\n" \
  "L7_:\n" \
  "ds_read_b32 v192, v190 offset:0\n" \
  "s_waitcnt lgkmcnt(0)\n" \
  "v_readfirstlane_b32 s35, v192\n" \
  "s_cmp_lt_u32 s35, 4\n" \
  "s_cbranch_scc1 L7_\n" \
  "s_add_u32 s96, s96, 8192\n" \
  "s_add_u32 s40, s92, s6\n" \
  "s_addc_u32 s41, s93, 0\n" \
  "s_add_u32 s6, s6, 2097152\n" \
  "s_mov_b32 m0, s96\n" \
  "s_nop 0\n" \
  "global_load_lds_dwordx4 v189, s[40:41]\n" \
  "global_load_lds_dwordx4 v194, s[40:41] offset:1024\n" \
  "s_sub_u32 s96, s96, 8192\n" \
  "ds_read_b64_tr_b16 v[128:129], v184 offset:0\n" \
  "ds_read_b64_tr_b16 v[130:131], v184 offset:256\n" \
  "ds_read_b64_tr_b16 v[132:133], v184 offset:2048\n" \
  "ds_read_b64_tr_b16 v[134:135], v184 offset:2304\n" \
  "ds_read_b64_tr_b16 v[136:137], v184 offset:4096\n" \
  "ds_read_b64_tr_b16 v[138:139], v184 offset:4352\n" \
  "ds_read_b64_tr_b16 v[140:141], v184 offset:6144\n" \
  "ds_read_b64_tr_b16 v[142:143], v184 offset:6400\n" \
  "ds_read_b64_tr_b16 v[156:157], v185 offset:6144\n" \
  "ds_read_b64_tr_b16 v[158:159], v185 offset:6400\n" \
  "ds_read_b64_tr_b16 v[152:153], v185 offset:4096\n" \
  "ds_read_b64_tr_b16 v[154:155], v185 offset:4352\n" \
  "ds_read_b64_tr_b16 v[148:149], v185 offset:2048\n" \
  "ds_read_b64_tr_b16 v[150:151], v185 offset:2304\n" \
  "ds_read_b64_tr_b16 v[144:145], v185 offset:0\n" \
  "ds_read_b64_tr_b16 v[146:147], v185 offset:256\n" \
  "v_mov_b32 v192, s39\n" \
  "s_cmp_lg_u32 s28, 0\n" \
  "s_cbranch_scc1 LB_nc\n" \
  "s_mov_b64 exec, 1\n" \
  "global_store_dword v193, v192, s[48:49] offset:0 sc1\n" \
  "s_mov_b64 exec, -1\n" \
  "LB_nc:\n" \
  "s_lshr_b32 s35, s30, 2\n" \
  "s_lshl3_add_u32 s35, s29, s35\n" \
  "s_and_b32 s36, s30, 3\n" \
  "s_lshl2_add_u32 s35, s35, s36\n" \
  "s_lshl2_add_u32 s35, s35, s28\n" \
  "s_lshl_b32 s35, s35, 12\n" \
  "s_add_u32 s35, s35, 105676800\n" \
  "s_add_u32 s58, s24, s35\n" \
  "s_addc_u32 s59, s25, 0\n" \
  "global_store_dwordx4 v238, v[240:243], s[58:59] offset:0 sc1\n" \
  "global_store_dwordx4 v238, v[240:243], s[58:59] offset:1024 sc1\n" \
  "global_store_dwordx4 v238, v[240:243], s[58:59] offset:2048 sc1\n" \
  "global_store_dwordx4 v238, v[240:243], s[58:59] offset:3072 sc1\n" \
  "s_lshl2_add_u32 s35, s7, s28\n" \
  "s_lshl_b32 s35, s35, 7\n" \
  "s_lshl_b32 s36, s31, 16\n" \
  "s_add_u32 s35, s35, s36\n" \
  "s_add_u32 s35, s35, 109871104\n" \
  "s_add_u32 s58, s24, s35\n" \
  "s_addc_u32 s59, s25, 0\n" \
  "s_mov_b64 exec, 0xff\n" \
  "global_store_dwordx4 v238, v[240:243], s[58:59] sc1\n" \
  "s_mov_b64 exec, -1\n" \
  "s_cmp_lg_u32 s33, 1021\n" \
  "s_cbranch_scc1 LB_nm\n" \
  "v_mov_b64 v[240:241], 0\n" \
  "v_mov_b64 v[242:243], 0\n" \
  "global_store_dwordx4 v238, v[240:243], s[48:49] offset:2048 sc1\n" \
  "s_nop 1\n" \
  "v_lshlrev_b32 v242, 8, v255\n" \
  "global_store_dwordx2 v242, v[240:241], s[48:49] offset:3072 sc1\n" \
  "v_add_u32 v242, 0x4000, v242\n" \
  "global_store_dwordx2 v242, v[240:241], s[48:49] offset:3072 sc1\n" \
  "v_add_u32 v242, 0x4000, v242\n" \
  "global_store_dwordx2 v242, v[240:241], s[48:49] offset:3072 sc1\n" \
  "v_add_u32 v242, 0x4000, v242\n" \
  "global_store_dwordx2 v242, v[240:241], s[48:49] offset:3072 sc1\n" \
  "v_add_u32 v242, 0x4000, v242\n" \
  "LB_nm:\n" \
  "s_mov_b32 s86, 0x78000b0\n" \
  "s_mov_b32 s87, 0x0\n" \
  "s_or_b32 s86, s86, s87\n" \
  "s_setprio 2\n" \
  "s_memtime s[90:91]\n" \
  "s_memrealtime s[42:43]\n" \
  "LT:\n" \
  "s_cmp_eq_u32 s38, 6\n" \
  "s_cselect_b32 s78, 0x33333333, -1\n" \
  "s_cmp_eq_u32 s38, 7\n" \
  "s_cselect_b32 s78, 0x33333333, s78\n" \
  "s_cmp_eq_u32 s38, 18\n" \
  "s_cselect_b32 s80, 0x33333333, -1\n" \
  "s_cmp_eq_u32 s38, 19\n" \
  "s_cselect_b32 s80, 0x33333333, s80\n" \
  "s_mov_b32 s79, s78\n" \
  "s_mov_b32 s81, s80\n" \
  "s_mov_b32 s37, s38\n" \
  "s_add_u32 s57, s38, 2\n" \
  "s_add_u32 s3, s38, 4\n" \
  "s_add_u32 s38, s38, 6\n" \
  "s_cmp_lt_u32 s57, 32\n" \
  "s_cselect_b32 s57, s57, 37\n" \
  "s_cselect_b32 s3, s3, 37\n" \
  "s_waitcnt lgkmcnt(12)\n" \
  "v_mfma_f32_16x16x32_bf16 v[160:163], v[128:131], v[0:3], 0\n" \
  "s_mov_b64 vcc, s[78:79]\n" \
  "v_cndmask_b32_dpp v234, v234, v234, vcc row_ror:8 row_mask:0xf bank_mask:0xf\n" \
  "v_mfma_f32_16x16x32_bf16 v[160:163], v[132:135], v[4:7], v[160:163]\n" \
  "s_waitcnt lgkmcnt(8)\n" \
  "v_mfma_f32_16x16x32_bf16 v[160:163], v[136:139], v[8:11], v[160:163]\n" \
  "s_set_gpr_idx_on s37, gpr_idx(SRC0)\n" \
  "v_mad_u32_u24 v232, v196, s52, v236\n" \
  "s_set_gpr_idx_off\n" \
  "v_mfma_f32_16x16x32_bf16 v[160:163], v[140:143], v[12:15], v[160:163]\n" \
  "s_waitcnt lgkmcnt(0)\n" \
  "v_lshrrev_b32 v228, v232, v234\n" \
  "v_lshrrev_b32_dpp v229, v232, v234 quad_perm:[2,3,0,1] row_mask:0xf bank_mask:0xf\n" \
  "v_mfma_f32_16x16x32_bf16 v[164:167], v[156:159], v[12:15], 0\n" \
  "v_lshrrev_b32_dpp v230, v232, v234 quad_perm:[1,2,3,0] row_mask:0xf bank_mask:0xf\n" \
  "v_lshrrev_b32_dpp v231, v232, v234 quad_perm:[3,0,1,2] row_mask:0xf bank_mask:0xf\n" \
  "v_mfma_f32_16x16x32_bf16 v[164:167], v[152:155], v[8:11], v[164:167]\n" \
  "v_perm_b32 v228, v229, v228, s53\n" \
  "v_perm_b32 v230, v231, v230, s54\n" \
  "v_mfma_f32_16x16x32_bf16 v[164:167], v[148:151], v[4:7], v[164:167]\n" \
  "v_bitop3_b32 v228, v228, v230, s55 bitop3:0xa8\n" \
  "v_lshl_or_b32 v229, v234, 1, s56\n" \
  "v_mfma_f32_16x16x32_bf16 v[164:167], v[144:147], v[0:3], v[164:167]\n" \
  "v_add_u32_dpp v228, v228, v228 quad_perm:[2,3,0,1] row_mask:0xf bank_mask:0xf\n" \
  "v_min_u32_sdwa v230, v228, v228 dst_sel:DWORD dst_unused:UNUSED_PAD src0_sel:BYTE_0 src1_sel:BYTE_1\n" \
  "v_mfma_f32_16x16x32_bf16 v[172:175], v[144:147], v[16:19], 0\n" \
  "v_min_u32_sdwa v231, v228, v228 dst_sel:DWORD dst_unused:UNUSED_PAD src0_sel:BYTE_2 src1_sel:BYTE_3\n" \
  "v_sub_u32 v230, v230, v231\n" \
  "v_mfma_f32_16x16x32_bf16 v[172:175], v[148:151], v[20:23], v[172:175]\n" \
  "v_cvt_pk_bf16_f32 v176, v160, v161\n" \
  "v_add_u32_dpp v230, v230, v230 quad_perm:[1,0,3,2] row_mask:0xf bank_mask:0xf\n" \
  "v_mfma_f32_16x16x32_bf16 v[172:175], v[152:155], v[24:27], v[172:175]\n" \
  "v_cmp_ge_i32 vcc, 0, v230\n" \
  "v_cvt_pk_bf16_f32 v177, v162, v163\n" \
  "v_mfma_f32_16x16x32_bf16 v[172:175], v[156:159], v[28:31], v[172:175]\n" \
  "v_cndmask_b32_sdwa v228, v228, v228, vcc dst_sel:DWORD dst_unused:UNUSED_PAD src0_sel:WORD_1 src1_sel:WORD_0\n" \
  "v_cvt_pk_bf16_f32 v178, v164, v165\n" \
  "v_mfma_f32_16x16x32_bf16 v[168:171], v[140:143], v[28:31], 0\n" \
  "s_mov_b64 s[50:51], vcc\n" \
  "v_cmp_le_u32_sdwa vcc, v228, v228 src0_sel:BYTE_0 src1_sel:BYTE_1\n" \
  "v_cvt_pk_bf16_f32 v179, v166, v167\n" \
  "v_mfma_f32_16x16x32_bf16 v[168:171], v[136:139], v[24:27], v[168:171]\n" \
  "v_cndmask_b32_dpp v232, v232, v232, vcc quad_perm:[2,3,0,1] row_mask:0xf bank_mask:0xf\n" \
  "v_mfma_f32_16x16x32_bf16 v[168:171], v[132:135], v[20:23], v[168:171]\n" \
  "s_mov_b64 vcc, s[50:51]\n" \
  "v_cndmask_b32_dpp v232, v232, v232, vcc quad_perm:[3,0,1,2] row_mask:0xf bank_mask:0xf\n" \
  "v_lshlrev_b32_e64 v230, v232, 3\n" \
  "v_mfma_f32_16x16x32_bf16 v[168:171], v[128:131], v[16:19], v[168:171]\n" \
  "v_and_or_b32 v234, v229, v230, v234\n" \
  "s_cmp_lg_u32 s33, 1021\n" \
  "s_cbranch_scc0 LT_e\n" \
  "LT_r:\n" \
  "s_waitcnt vmcnt(5)\n" \
  "global_store_dwordx4 v186, v[176:179], s[44:45] offset:0 sc1\n" \
  "v_mfma_f32_16x16x32_bf16 v[160:163], v[128:131], v[32:35], 0\n" \
  "s_set_gpr_idx_on s37, gpr_idx(DST)\n" \
  "v_mov_b32 v196, v232\n" \
  "s_set_gpr_idx_off\n" \
  "v_mfma_f32_16x16x32_bf16 v[160:163], v[132:135], v[36:39], v[160:163]\n" \
  "s_set_gpr_idx_on s57, gpr_idx(SRC0)\n" \
  "v_mad_u32_u24 v232, v196, s52, v236\n" \
  "s_set_gpr_idx_off\n" \
  "v_mfma_f32_16x16x32_bf16 v[160:163], v[136:139], v[40:43], v[160:163]\n" \
  "v_lshrrev_b32 v228, v232, v234\n" \
  "v_lshrrev_b32_dpp v229, v232, v234 quad_perm:[2,3,0,1] row_mask:0xf bank_mask:0xf\n" \
  "v_mfma_f32_16x16x32_bf16 v[160:163], v[140:143], v[44:47], v[160:163]\n" \
  "v_lshrrev_b32_dpp v230, v232, v234 quad_perm:[1,2,3,0] row_mask:0xf bank_mask:0xf\n" \
  "v_lshrrev_b32_dpp v231, v232, v234 quad_perm:[3,0,1,2] row_mask:0xf bank_mask:0xf\n" \
  "v_mfma_f32_16x16x32_bf16 v[164:167], v[156:159], v[44:47], 0\n" \
  "v_perm_b32 v228, v229, v228, s53\n" \
  "v_perm_b32 v230, v231, v230, s54\n" \
  "v_mfma_f32_16x16x32_bf16 v[164:167], v[152:155], v[40:43], v[164:167]\n" \
  "v_bitop3_b32 v228, v228, v230, s55 bitop3:0xa8\n" \
  "v_lshl_or_b32 v229, v234, 1, s56\n" \
  "v_mfma_f32_16x16x32_bf16 v[164:167], v[148:151], v[36:39], v[164:167]\n" \
  "v_cvt_pk_bf16_f32 v182, v172, v173\n" \
  "v_cvt_pk_bf16_f32 v183, v174, v175\n" \
  "v_mfma_f32_16x16x32_bf16 v[164:167], v[144:147], v[32:35], v[164:167]\n" \
  "v_cvt_pk_bf16_f32 v180, v168, v169\n" \
  "v_cvt_pk_bf16_f32 v181, v170, v171\n" \
  "s_waitcnt vmcnt(5)\n" \
  "global_store_dwordx4 v186, v[180:183], s[44:45] offset:256 sc1\n" \
  "v_mfma_f32_16x16x32_bf16 v[172:175], v[144:147], v[48:51], 0\n" \
  "v_add_u32_dpp v228, v228, v228 quad_perm:[2,3,0,1] row_mask:0xf bank_mask:0xf\n" \
  "v_min_u32_sdwa v230, v228, v228 dst_sel:DWORD dst_unused:UNUSED_PAD src0_sel:BYTE_0 src1_sel:BYTE_1\n" \
  "v_mfma_f32_16x16x32_bf16 v[172:175], v[148:151], v[52:55], v[172:175]\n" \
  "v_min_u32_sdwa v231, v228, v228 dst_sel:DWORD dst_unused:UNUSED_PAD src0_sel:BYTE_2 src1_sel:BYTE_3\n" \
  "v_sub_u32 v230, v230, v231\n" \
  "v_mfma_f32_16x16x32_bf16 v[172:175], v[152:155], v[56:59], v[172:175]\n" \
  "v_cvt_pk_bf16_f32 v176, v160, v161\n" \
  "v_add_u32_dpp v230, v230, v230 quad_perm:[1,0,3,2] row_mask:0xf bank_mask:0xf\n" \
  "v_mfma_f32_16x16x32_bf16 v[172:175], v[156:159], v[60:63], v[172:175]\n" \
  "v_cmp_ge_i32 vcc, 0, v230\n" \
  "v_cvt_pk_bf16_f32 v177, v162, v163\n" \
  "v_mfma_f32_16x16x32_bf16 v[168:171], v[140:143], v[60:63], 0\n" \
  "v_cndmask_b32_sdwa v228, v228, v228, vcc dst_sel:DWORD dst_unused:UNUSED_PAD src0_sel:WORD_1 src1_sel:WORD_0\n" \
  "v_cvt_pk_bf16_f32 v178, v164, v165\n" \
  "v_mfma_f32_16x16x32_bf16 v[168:171], v[136:139], v[56:59], v[168:171]\n" \
  "s_mov_b64 s[50:51], vcc\n" \
  "v_cmp_le_u32_sdwa vcc, v228, v228 src0_sel:BYTE_0 src1_sel:BYTE_1\n" \
  "v_cvt_pk_bf16_f32 v179, v166, v167\n" \
  "v_mfma_f32_16x16x32_bf16 v[168:171], v[132:135], v[52:55], v[168:171]\n" \
  "v_cndmask_b32_dpp v232, v232, v232, vcc quad_perm:[2,3,0,1] row_mask:0xf bank_mask:0xf\n" \
  "v_mfma_f32_16x16x32_bf16 v[168:171], v[128:131], v[48:51], v[168:171]\n" \
  "s_mov_b64 vcc, s[50:51]\n" \
  "v_cndmask_b32_dpp v232, v232, v232, vcc quad_perm:[3,0,1,2] row_mask:0xf bank_mask:0xf\n" \
  "v_lshlrev_b32_e64 v230, v232, 3\n" \
  "s_waitcnt vmcnt(5)\n" \
  "global_store_dwordx4 v186, v[176:179], s[44:45] offset:512 sc1\n" \
  "v_mfma_f32_16x16x32_bf16 v[160:163], v[128:131], v[64:67], 0\n" \
  "v_and_or_b32 v234, v229, v230, v234\n" \
  "v_cvt_pk_bf16_f32 v182, v172, v173\n" \
  "v_mfma_f32_16x16x32_bf16 v[160:163], v[132:135], v[68:71], v[160:163]\n" \
  "s_set_gpr_idx_on s57, gpr_idx(DST)\n" \
  "v_mov_b32 v196, v232\n" \
  "s_set_gpr_idx_off\n" \
  "v_mfma_f32_16x16x32_bf16 v[160:163], v[136:139], v[72:75], v[160:163]\n" \
  "s_mov_b64 vcc, s[80:81]\n" \
  "v_cndmask_b32_dpp v234, v234, v234, vcc row_ror:8 row_mask:0xf bank_mask:0xf\n" \
  "v_cvt_pk_bf16_f32 v183, v174, v175\n" \
  "v_mfma_f32_16x16x32_bf16 v[160:163], v[140:143], v[76:79], v[160:163]\n" \
  "s_set_gpr_idx_on s3, gpr_idx(SRC0)\n" \
  "v_mad_u32_u24 v232, v196, s52, v236\n" \
  "s_set_gpr_idx_off\n" \
  "v_mfma_f32_16x16x32_bf16 v[164:167], v[156:159], v[76:79], 0\n" \
  "v_lshrrev_b32 v228, v232, v234\n" \
  "v_lshrrev_b32_dpp v229, v232, v234 quad_perm:[2,3,0,1] row_mask:0xf bank_mask:0xf\n" \
  "v_mfma_f32_16x16x32_bf16 v[164:167], v[152:155], v[72:75], v[164:167]\n" \
  "v_lshrrev_b32_dpp v230, v232, v234 quad_perm:[1,2,3,0] row_mask:0xf bank_mask:0xf\n" \
  "v_lshrrev_b32_dpp v231, v232, v234 quad_perm:[3,0,1,2] row_mask:0xf bank_mask:0xf\n" \
  "v_mfma_f32_16x16x32_bf16 v[164:167], v[148:151], v[68:71], v[164:167]\n" \
  "v_perm_b32 v228, v229, v228, s53\n" \
  "v_perm_b32 v230, v231, v230, s54\n" \
  "v_mfma_f32_16x16x32_bf16 v[164:167], v[144:147], v[64:67], v[164:167]\n" \
  "v_cvt_pk_bf16_f32 v180, v168, v169\n" \
  "v_cvt_pk_bf16_f32 v181, v170, v171\n" \
  "s_waitcnt vmcnt(5)\n" \
  "global_store_dwordx4 v186, v[180:183], s[44:45] offset:768 sc1\n" \
  "v_mfma_f32_16x16x32_bf16 v[172:175], v[144:147], v[80:83], 0\n" \
  "s_mov_b64 exec, 1\n" \
  "ds_add_u32 v190, v191 offset:0\n" \
  "s_mov_b64 exec, -1\n" \
  "v_mfma_f32_16x16x32_bf16 v[172:175], v[148:151], v[84:87], v[172:175]\n" \
  "v_bitop3_b32 v228, v228, v230, s55 bitop3:0xa8\n" \
  "v_lshl_or_b32 v229, v234, 1, s56\n" \
  "v_mfma_f32_16x16x32_bf16 v[172:175], v[152:155], v[88:91], v[172:175]\n" \
  "v_add_u32_dpp v228, v228, v228 quad_perm:[2,3,0,1] row_mask:0xf bank_mask:0xf\n" \
  "v_min_u32_sdwa v230, v228, v228 dst_sel:DWORD dst_unused:UNUSED_PAD src0_sel:BYTE_0 src1_sel:BYTE_1\n" \
  "v_mfma_f32_16x16x32_bf16 v[172:175], v[156:159], v[92:95], v[172:175]\n" \
  "v_min_u32_sdwa v231, v228, v228 dst_sel:DWORD dst_unused:UNUSED_PAD src0_sel:BYTE_2 src1_sel:BYTE_3\n" \
  "v_sub_u32 v230, v230, v231\n" \
  "v_mfma_f32_16x16x32_bf16 v[168:171], v[140:143], v[92:95], 0\n" \
  "v_cvt_pk_bf16_f32 v176, v160, v161\n" \
  "v_add_u32_dpp v230, v230, v230 quad_perm:[1,0,3,2] row_mask:0xf bank_mask:0xf\n" \
  "v_mfma_f32_16x16x32_bf16 v[168:171], v[136:139], v[88:91], v[168:171]\n" \
  "v_cmp_ge_i32 vcc, 0, v230\n" \
  "v_cvt_pk_bf16_f32 v177, v162, v163\n" \
  "v_mfma_f32_16x16x32_bf16 v[168:171], v[132:135], v[84:87], v[168:171]\n" \
  "v_cndmask_b32_sdwa v228, v228, v228, vcc dst_sel:DWORD dst_unused:UNUSED_PAD src0_sel:WORD_1 src1_sel:WORD_0\n" \
  "v_cvt_pk_bf16_f32 v178, v164, v165\n" \
  "v_mfma_f32_16x16x32_bf16 v[168:171], v[128:131], v[80:83], v[168:171]\n" \
  "s_mov_b64 s[50:51], vcc\n" \
  "v_cmp_le_u32_sdwa vcc, v228, v228 src0_sel:BYTE_0 src1_sel:BYTE_1\n" \
  "v_cvt_pk_bf16_f32 v179, v166, v167\n" \
  "s_cmp_eq_u32 s98, 4\n" \
  "s_cselect_b32 s36, 12451840, 0\n" \
  "s_sub_u32 s6, s6, s36\n" \
  "s_cmp_lt_u32 s98, 10\n" \
  "s_cselect_b32 s6, s6, s34\n" \
  "s_add_u32 s40, s92, s6\n" \
  "s_addc_u32 s41, s93, 0\n" \
  "s_add_u32 s6, s6, 2097152\n" \
  "s_waitcnt vmcnt(5)\n" \
  "global_store_dwordx4 v186, v[176:179], s[44:45] offset:1024 sc1\n" \
  "v_mfma_f32_16x16x32_bf16 v[160:163], v[128:131], v[96:99], 0\n" \
  "ds_read_b32 v192, v190\n" \
  "v_add_u32 v184, s97, v184\n" \
  "v_mfma_f32_16x16x32_bf16 v[160:163], v[132:135], v[100:103], v[160:163]\n" \
  "v_add_u32 v185, s97, v185\n" \
  "v_cndmask_b32_dpp v232, v232, v232, vcc quad_perm:[2,3,0,1] row_mask:0xf bank_mask:0xf\n" \
  "v_mfma_f32_16x16x32_bf16 v[160:163], v[136:139], v[104:107], v[160:163]\n" \
  "s_mov_b64 vcc, s[50:51]\n" \
  "v_cndmask_b32_dpp v232, v232, v232, vcc quad_perm:[3,0,1,2] row_mask:0xf bank_mask:0xf\n" \
  "v_lshlrev_b32_e64 v230, v232, 3\n" \
  "v_mfma_f32_16x16x32_bf16 v[160:163], v[140:143], v[108:111], v[160:163]\n" \
  "v_and_or_b32 v234, v229, v230, v234\n" \
  "v_cvt_pk_bf16_f32 v182, v172, v173\n" \
  "v_mfma_f32_16x16x32_bf16 v[164:167], v[156:159], v[108:111], 0\n" \
  "s_set_gpr_idx_on s3, gpr_idx(DST)\n" \
  "v_mov_b32 v196, v232\n" \
  "s_set_gpr_idx_off\n" \
  "v_mfma_f32_16x16x32_bf16 v[164:167], v[152:155], v[104:107], v[164:167]\n" \
  "s_waitcnt lgkmcnt(0)\n" \
  "v_readfirstlane_b32 s35, v192\n" \
  "v_cvt_pk_bf16_f32 v183, v174, v175\n" \
  "v_mfma_f32_16x16x32_bf16 v[164:167], v[148:151], v[100:103], v[164:167]\n" \
  "v_cvt_pk_bf16_f32 v180, v168, v169\n" \
  "v_cvt_pk_bf16_f32 v181, v170, v171\n" \
  "v_mfma_f32_16x16x32_bf16 v[164:167], v[144:147], v[96:99], v[164:167]\n" \
  "s_cmp_lt_u32 s35, s99\n" \
  "s_cbranch_scc1 LT_w\n" \
  "LT_c:\n" \
  "s_mov_b32 m0, s96\n" \
  "s_nop 0\n" \
  "global_load_lds_dwordx4 v189, s[40:41]\n" \
  "global_load_lds_dwordx4 v194, s[40:41] offset:1024\n" \
  "s_add_u32 s96, s96, s97\n" \
  "s_sub_u32 s97, 0, s97\n" \
  "s_add_u32 s99, s99, 4\n" \
  "s_lshl4_add_u32 s35, s28, s98\n" \
  "s_bitcmp1_b64 s[86:87], s35\n" \
  "s_cbranch_scc1 LT_a\n" \
  "LT_s:\n" \
  "s_waitcnt vmcnt(5)\n" \
  "global_store_dwordx4 v186, v[180:183], s[44:45] offset:1280 sc1\n" \
  "v_mfma_f32_16x16x32_bf16 v[168:171], v[128:131], v[112:115], 0\n" \
  "ds_read_b64_tr_b16 v[128:129], v184 offset:0\n" \
  "ds_read_b64_tr_b16 v[130:131], v184 offset:256\n" \
  "v_mfma_f32_16x16x32_bf16 v[168:171], v[132:135], v[116:119], v[168:171]\n" \
  "ds_read_b64_tr_b16 v[132:133], v184 offset:2048\n" \
  "ds_read_b64_tr_b16 v[134:135], v184 offset:2304\n" \
  "v_mfma_f32_16x16x32_bf16 v[168:171], v[136:139], v[120:123], v[168:171]\n" \
  "ds_read_b64_tr_b16 v[136:137], v184 offset:4096\n" \
  "v_cvt_pk_bf16_f32 v176, v160, v161\n" \
  "v_mfma_f32_16x16x32_bf16 v[168:171], v[140:143], v[124:127], v[168:171]\n" \
  "ds_read_b64_tr_b16 v[138:139], v184 offset:4352\n" \
  "v_cvt_pk_bf16_f32 v177, v162, v163\n" \
  "v_mfma_f32_16x16x32_bf16 v[172:175], v[156:159], v[124:127], 0\n" \
  "ds_read_b64_tr_b16 v[140:141], v184 offset:6144\n" \
  "v_cvt_pk_bf16_f32 v178, v164, v165\n" \
  "v_mfma_f32_16x16x32_bf16 v[172:175], v[152:155], v[120:123], v[172:175]\n" \
  "ds_read_b64_tr_b16 v[142:143], v184 offset:6400\n" \
  "v_cvt_pk_bf16_f32 v179, v166, v167\n" \
  "v_mfma_f32_16x16x32_bf16 v[172:175], v[148:151], v[116:119], v[172:175]\n" \
  "ds_read_b64_tr_b16 v[156:157], v185 offset:6144\n" \
  "ds_read_b64_tr_b16 v[158:159], v185 offset:6400\n" \
  "v_mfma_f32_16x16x32_bf16 v[172:175], v[144:147], v[112:115], v[172:175]\n" \
  "ds_read_b64_tr_b16 v[152:153], v185 offset:4096\n" \
  "ds_read_b64_tr_b16 v[154:155], v185 offset:4352\n" \
  "ds_read_b64_tr_b16 v[148:149], v185 offset:2048\n" \
  "ds_read_b64_tr_b16 v[150:151], v185 offset:2304\n" \
  "ds_read_b64_tr_b16 v[144:145], v185 offset:0\n" \
  "ds_read_b64_tr_b16 v[146:147], v185 offset:256\n" \
  "s_waitcnt vmcnt(5)\n" \
  "global_store_dwordx4 v187, v[176:179], s[44:45] offset:0 sc1\n" \
  "s_add_u32 s98, s98, 1\n" \
  "v_cvt_pk_bf16_f32 v180, v168, v169\n" \
  "v_cvt_pk_bf16_f32 v181, v170, v171\n" \
  "v_cvt_pk_bf16_f32 v182, v172, v173\n" \
  "v_cvt_pk_bf16_f32 v183, v174, v175\n" \
  "s_waitcnt vmcnt(5)\n" \
  "global_store_dwordx4 v188, v[180:183], s[44:45] offset:0 sc1\n" \
  "s_add_u32 s44, s44, 128000\n" \
  "s_addc_u32 s45, s45, 0\n" \
  "s_bitcmp0_b32 4160, s98\n" \
  "s_cbranch_scc1 LT\n" \
  "s_cmp_eq_u32 s98, 6\n" \
  "s_cbranch_scc1 LT_m\n" \
  "s_lshr_b32 s35, s30, 2\n" \
  "s_lshl3_add_u32 s35, s29, s35\n" \
  "s_mul_i32 s35, s35, 1600000\n" \
  "s_lshl_b32 s36, s28, 12\n" \
  "s_add_u32 s35, s35, s36\n" \
  "s_add_u32 s40, s24, s35\n" \
  "s_addc_u32 s41, s25, 0\n" \
  "s_bitcmp1_b32 s30, 2\n" \
  "s_cbranch_scc1 LB_l_h\n" \
  "global_load_dwordx4 v[64:67], v238, s[40:41] offset:0 sc1\n" \
  "global_load_dwordx4 v[68:71], v238, s[40:41] offset:1024 sc1\n" \
  "global_load_dwordx4 v[72:75], v238, s[40:41] offset:2048 sc1\n" \
  "global_load_dwordx4 v[76:79], v238, s[40:41] offset:3072 sc1\n" \
  "s_add_u32 s40, s40, 16000\n" \
  "s_addc_u32 s41, s41, 0\n" \
  "global_load_dwordx4 v[80:83], v238, s[40:41] offset:0 sc1\n" \
  "global_load_dwordx4 v[84:87], v238, s[40:41] offset:1024 sc1\n" \
  "global_load_dwordx4 v[88:91], v238, s[40:41] offset:2048 sc1\n" \
  "global_load_dwordx4 v[92:95], v238, s[40:41] offset:3072 sc1\n" \
  "s_add_u32 s40, s40, 16000\n" \
  "s_addc_u32 s41, s41, 0\n" \
  "global_load_dwordx4 v[96:99], v238, s[40:41] offset:0 sc1\n" \
  "global_load_dwordx4 v[100:103], v238, s[40:41] offset:1024 sc1\n" \
  "global_load_dwordx4 v[104:107], v238, s[40:41] offset:2048 sc1\n" \
  "global_load_dwordx4 v[108:111], v238, s[40:41] offset:3072 sc1\n" \
  "s_add_u32 s40, s40, 16000\n" \
  "s_addc_u32 s41, s41, 0\n" \
  "global_load_dwordx4 v[112:115], v238, s[40:41] offset:0 sc1\n" \
  "global_load_dwordx4 v[116:119], v238, s[40:41] offset:1024 sc1\n" \
  "global_load_dwordx4 v[120:123], v238, s[40:41] offset:2048 sc1\n" \
  "global_load_dwordx4 v[124:127], v238, s[40:41] offset:3072 sc1\n" \
  "s_add_u32 s40, s40, 16000\n" \
  "s_addc_u32 s41, s41, 0\n" \
  "s_branch LB_l_e\n" \
  "LB_l_h:\n" \
  "global_load_dwordx4 v[0:3], v238, s[40:41] offset:0 sc1\n" \
  "global_load_dwordx4 v[4:7], v238, s[40:41] offset:1024 sc1\n" \
  "global_load_dwordx4 v[8:11], v238, s[40:41] offset:2048 sc1\n" \
  "global_load_dwordx4 v[12:15], v238, s[40:41] offset:3072 sc1\n" \
  "s_add_u32 s40, s40, 16000\n" \
  "s_addc_u32 s41, s41, 0\n" \
  "global_load_dwordx4 v[16:19], v238, s[40:41] offset:0 sc1\n" \
  "global_load_dwordx4 v[20:23], v238, s[40:41] offset:1024 sc1\n" \
  "global_load_dwordx4 v[24:27], v238, s[40:41] offset:2048 sc1\n" \
  "global_load_dwordx4 v[28:31], v238, s[40:41] offset:3072 sc1\n" \
  "s_add_u32 s40, s40, 16000\n" \
  "s_addc_u32 s41, s41, 0\n" \
  "global_load_dwordx4 v[32:35], v238, s[40:41] offset:0 sc1\n" \
  "global_load_dwordx4 v[36:39], v238, s[40:41] offset:1024 sc1\n" \
  "global_load_dwordx4 v[40:43], v238, s[40:41] offset:2048 sc1\n" \
  "global_load_dwordx4 v[44:47], v238, s[40:41] offset:3072 sc1\n" \
  "s_add_u32 s40, s40, 16000\n" \
  "s_addc_u32 s41, s41, 0\n" \
  "global_load_dwordx4 v[48:51], v238, s[40:41] offset:0 sc1\n" \
  "global_load_dwordx4 v[52:55], v238, s[40:41] offset:1024 sc1\n" \
  "global_load_dwordx4 v[56:59], v238, s[40:41] offset:2048 sc1\n" \
  "global_load_dwordx4 v[60:63], v238, s[40:41] offset:3072 sc1\n" \
  "s_add_u32 s40, s40, 16000\n" \
  "s_addc_u32 s41, s41, 0\n" \
  "LB_l_e:\n" \
  "ds_write_b16_d16_hi v195, v197 offset:0\n" \
  "ds_write_b16_d16_hi v195, v199 offset:16\n" \
  "ds_write_b16_d16_hi v195, v201 offset:32\n" \
  "ds_write_b16_d16_hi v235, v203 offset:64\n" \
  "ds_write_b16_d16_hi v235, v205 offset:80\n" \
  "ds_write_b16_d16_hi v235, v207 offset:96\n" \
  "ds_write_b16_d16_hi v235, v209 offset:112\n" \
  "ds_write_b16_d16_hi v235, v211 offset:128\n" \
  "ds_write_b16_d16_hi v235, v213 offset:144\n" \
  "ds_write_b16_d16_hi v235, v215 offset:160\n" \
  "ds_write_b16_d16_hi v235, v217 offset:176\n" \
  "ds_write_b16_d16_hi v195, v219 offset:48\n" \
  "ds_write_b16_d16_hi v195, v221 offset:192\n" \
  "ds_write_b16_d16_hi v195, v223 offset:208\n" \
  "ds_write_b16_d16_hi v195, v225 offset:224\n" \
  "ds_write_b16_d16_hi v195, v227 offset:240\n" \
  "ds_read_b128 v[244:247], v254 offset:0\n" \
  "ds_read_b128 v[248:251], v254 offset:1088\n" \
  "s_lshr_b32 s35, s7, 5\n" \
  "s_mul_i32 s35, s35, 819200\n" \
  "s_and_b32 s36, s7, 31\n" \
  "s_lshl_b32 s36, s36, 10\n" \
  "s_add_u32 s35, s35, s36\n" \
  "s_lshl_b32 s36, s4, 9\n" \
  "s_add_u32 s35, s35, s36\n" \
  "s_add_u32 s35, s35, 102793216\n" \
  "s_add_u32 s58, s24, s35\n" \
  "s_addc_u32 s59, s25, 0\n" \
  "s_waitcnt lgkmcnt(0)\n" \
  "s_cmp_ge_u32 s4, 896\n" \
  "s_cbranch_scc1 L17_\n" \
  "global_store_dwordx4 v238, v[244:247], s[58:59] sc1\n" \
  "s_cmp_ge_u32 s4, 768\n" \
  "s_cbranch_scc1 L17_\n" \
  "s_add_u32 s58, s58, 32768\n" \
  "s_addc_u32 s59, s59, 0\n" \
  "global_store_dwordx4 v238, v[248:251], s[58:59] sc1\n" \
  "L17_:\n" \
  "s_lshr_b32 s35, s30, 2\n" \
  "s_lshl3_add_u32 s33, s29, s35\n" \
  "s_and_b32 s34, s30, 3\n" \
  "s_lshl_b32 s59, s27, 11\n" \
  "s_mul_i32 s35, s33, 1600000\n" \
  "s_add_u32 s54, s24, s35\n" \
  "s_addc_u32 s55, s25, 0\n" \
  "s_mul_i32 s35, s34, 819200\n" \
  "s_lshl1_add_u32 s35, s59, s35\n" \
  "s_add_u32 s35, s35, 102400000\n" \
  "s_add_u32 s56, s24, s35\n" \
  "s_addc_u32 s57, s25, 0\n" \
  "global_load_dwordx4 v[208:211], v238, s[56:57] offset:0 sc1\n" \
  "global_load_dwordx4 v[212:215], v238, s[56:57] offset:1024 sc1\n" \
  "global_load_dwordx4 v[216:219], v238, s[56:57] offset:2048 sc1\n" \
  "global_load_dwordx4 v[220:223], v238, s[56:57] offset:3072 sc1\n" \
  "s_add_u32 s56, s56, 32768\n" \
  "s_addc_u32 s57, s57, 0\n" \
  "global_load_dwordx4 v[224:227], v238, s[56:57] sc1\n" \
  "global_load_dwordx4 v[228:231], v238, s[56:57] offset:1024 sc1\n" \
  "global_load_dwordx4 v[232:235], v238, s[56:57] offset:2048 sc1\n" \
  "global_load_dwordx4 v[236:239], v238, s[56:57] offset:3072 sc1\n" \
  "s_add_u32 s56, s56, 32768\n" \
  "s_addc_u32 s57, s57, 0\n" \
  "s_bitcmp1_b32 s30, 2\n" \
  "s_cbranch_scc1 LB_t_h\n" \
  "s_waitcnt lgkmcnt(12)\n" \
  "v_mfma_f32_16x16x32_bf16 v[160:163], v[128:131], v[0:3], 0\n" \
  "v_mfma_f32_16x16x32_bf16 v[160:163], v[132:135], v[4:7], v[160:163]\n" \
  "s_waitcnt lgkmcnt(8)\n" \
  "v_mfma_f32_16x16x32_bf16 v[160:163], v[136:139], v[8:11], v[160:163]\n" \
  "v_mfma_f32_16x16x32_bf16 v[160:163], v[140:143], v[12:15], v[160:163]\n" \
  "s_waitcnt lgkmcnt(0)\n" \
  "v_mfma_f32_16x16x32_bf16 v[164:167], v[156:159], v[12:15], 0\n" \
  "v_mfma_f32_16x16x32_bf16 v[164:167], v[152:155], v[8:11], v[164:167]\n" \
  "v_mfma_f32_16x16x32_bf16 v[164:167], v[148:151], v[4:7], v[164:167]\n" \
  "v_mfma_f32_16x16x32_bf16 v[164:167], v[144:147], v[0:3], v[164:167]\n" \
  "v_mfma_f32_16x16x32_bf16 v[172:175], v[144:147], v[16:19], 0\n" \
  "v_mfma_f32_16x16x32_bf16 v[172:175], v[148:151], v[20:23], v[172:175]\n" \
  "v_mfma_f32_16x16x32_bf16 v[172:175], v[152:155], v[24:27], v[172:175]\n" \
  "v_cvt_pk_bf16_f32 v176, v160, v161\n" \
  "v_cvt_pk_bf16_f32 v177, v162, v163\n" \
  "v_mfma_f32_16x16x32_bf16 v[172:175], v[156:159], v[28:31], v[172:175]\n" \
  "v_mfma_f32_16x16x32_bf16 v[168:171], v[140:143], v[28:31], 0\n" \
  "v_mfma_f32_16x16x32_bf16 v[168:171], v[136:139], v[24:27], v[168:171]\n" \
  "v_cvt_pk_bf16_f32 v178, v164, v165\n" \
  "v_cvt_pk_bf16_f32 v179, v166, v167\n" \
  "v_mfma_f32_16x16x32_bf16 v[168:171], v[132:135], v[20:23], v[168:171]\n" \
  "v_mfma_f32_16x16x32_bf16 v[168:171], v[128:131], v[16:19], v[168:171]\n" \
  "s_waitcnt vmcnt(63)\n" \
  "global_store_dwordx4 v186, v[176:179], s[46:47] offset:0 sc1\n" \
  "v_mfma_f32_16x16x32_bf16 v[160:163], v[128:131], v[32:35], 0\n" \
  "v_cvt_pk_bf16_f32 v182, v172, v173\n" \
  "v_cvt_pk_bf16_f32 v183, v174, v175\n" \
  "v_mfma_f32_16x16x32_bf16 v[160:163], v[132:135], v[36:39], v[160:163]\n" \
  "v_mfma_f32_16x16x32_bf16 v[160:163], v[136:139], v[40:43], v[160:163]\n" \
  "v_mfma_f32_16x16x32_bf16 v[160:163], v[140:143], v[44:47], v[160:163]\n" \
  "v_cvt_pk_bf16_f32 v180, v168, v169\n" \
  "v_cvt_pk_bf16_f32 v181, v170, v171\n" \
  "v_mfma_f32_16x16x32_bf16 v[164:167], v[156:159], v[44:47], 0\n" \
  "v_mfma_f32_16x16x32_bf16 v[164:167], v[152:155], v[40:43], v[164:167]\n" \
  "v_mfma_f32_16x16x32_bf16 v[164:167], v[148:151], v[36:39], v[164:167]\n" \
  "v_mfma_f32_16x16x32_bf16 v[164:167], v[144:147], v[32:35], v[164:167]\n" \
  "s_waitcnt vmcnt(63)\n" \
  "global_store_dwordx4 v186, v[180:183], s[46:47] offset:256 sc1\n" \
  "v_mfma_f32_16x16x32_bf16 v[172:175], v[144:147], v[48:51], 0\n" \
  "v_cvt_pk_bf16_f32 v176, v160, v161\n" \
  "v_cvt_pk_bf16_f32 v177, v162, v163\n" \
  "v_mfma_f32_16x16x32_bf16 v[172:175], v[148:151], v[52:55], v[172:175]\n" \
  "v_mfma_f32_16x16x32_bf16 v[172:175], v[152:155], v[56:59], v[172:175]\n" \
  "v_mfma_f32_16x16x32_bf16 v[172:175], v[156:159], v[60:63], v[172:175]\n" \
  "v_cvt_pk_bf16_f32 v178, v164, v165\n" \
  "v_cvt_pk_bf16_f32 v179, v166, v167\n" \
  "v_mfma_f32_16x16x32_bf16 v[168:171], v[140:143], v[60:63], 0\n" \
  "v_mfma_f32_16x16x32_bf16 v[168:171], v[136:139], v[56:59], v[168:171]\n" \
  "v_mfma_f32_16x16x32_bf16 v[168:171], v[132:135], v[52:55], v[168:171]\n" \
  "v_mfma_f32_16x16x32_bf16 v[168:171], v[128:131], v[48:51], v[168:171]\n" \
  "s_waitcnt vmcnt(63)\n" \
  "global_store_dwordx4 v186, v[176:179], s[46:47] offset:512 sc1\n" \
  "v_cvt_pk_bf16_f32 v182, v172, v173\n" \
  "v_cvt_pk_bf16_f32 v183, v174, v175\n" \
  "s_nop 3\n" \
  "v_cvt_pk_bf16_f32 v180, v168, v169\n" \
  "v_cvt_pk_bf16_f32 v181, v170, v171\n" \
  "s_waitcnt vmcnt(63)\n" \
  "global_store_dwordx4 v186, v[180:183], s[46:47] offset:768 sc1\n" \
  "s_branch LB_t_e\n" \
  "LT_w:\n" \
  "L8_:\n" \
  "s_sleep 1\n" \
  "ds_read_b32 v192, v190 offset:0\n" \
  "s_waitcnt lgkmcnt(0)\n" \
  "v_readfirstlane_b32 s35, v192\n" \
  "s_cmp_lt_u32 s35, s99\n" \
  "s_cbranch_scc1 L8_\n" \
  "s_branch LT_c\n" \
  "LT_e:\n" \
  "s_bitcmp1_b32 10, s98\n" \
  "s_cbranch_scc0 LT_r\n" \
  "global_load_dwordx4 v[240:243], v238, s[48:49] offset:0 sc1\n" \
  "s_waitcnt vmcnt(0)\n" \
  "global_store_dwordx4 v238, v[240:243], s[48:49] offset:1024 sc1\n" \
  "s_branch LT_r\n" \
  "LT_a:\n" \
  "s_cmp_lg_u32 s35, 4\n" \
  "s_cbranch_scc1 L9_\n" \
  "global_load_dword v193, v193, s[48:49] offset:1024 sc1\n" \
  "s_branch LT_s\n" \
  "L9_:\n" \
  "s_cmp_lg_u32 s35, 5\n" \
  "s_cbranch_scc1 L10_\n" \
  "s_memtime s[58:59]\n" \
  "s_memrealtime s[40:41]\n" \
  "s_mov_b32 s5, 0x10000\n" \
  "LB_ec:\n" \
  "v_readfirstlane_b32 s35, v193\n" \
  "s_cmp_eq_u32 s35, s39\n" \
  "s_cbranch_scc1 LB_ok\n" \
  "s_lshl_b32 s35, s26, 2\n" \
  "v_mov_b32 v193, s35\n" \
  "s_sleep 8\n" \
  "global_load_dword v193, v193, s[48:49] offset:1024 sc1\n" \
  "s_waitcnt vmcnt(0)\n" \
  "s_sub_u32 s5, s5, 1\n" \
  "s_cbranch_scc0 LB_ec\n" \
  "LB_ok:\n" \
  "s_lshl_b32 s35, s26, 2\n" \
  "v_mov_b32 v244, s35\n" \
  "s_waitcnt lgkmcnt(0)\n" \
  "s_sub_u32 s88, s58, s88\n" \
  "s_sub_u32 s88, 106000, s88\n" \
  "s_sub_u32 s90, s58, s90\n" \
  "s_sub_u32 s42, s40, s42\n" \
  "s_lshl_b32 s40, s40, 4\n" \
  "v_cvt_f32_u32 v245, s90\n" \
  "v_cvt_f32_u32 v246, s42\n" \
  "v_rcp_f32 v245, v245\n" \
  "v_cvt_f32_i32 v247, s88\n" \
  "v_mul_f32 v246, 0x4111eb85, v246\n" \
  "v_mul_f32 v245, v245, v246\n" \
  "v_mul_f32 v245, v245, v247\n" \
  "v_cvt_i32_f32 v245, v245\n" \
  "v_add_u32 v245, s40, v245\n" \
  "v_or_b32 v245, 1, v245\n" \
  "s_mov_b64 exec, 1\n" \
  "global_store_dword v244, v245, s[48:49] offset:2048 sc1\n" \
  "s_mov_b64 exec, -1\n" \
  "s_branch LT_s\n" \
  "L10_:\n" \
  "s_cmp_lg_u32 s35, 7\n" \
  "s_cbranch_scc1 L11_\n" \
  "s_mov_b32 exec_hi, 0\n" \
  "global_atomic_add v196, v191, s[48:49] offset:3072 sc1\n" \
  "s_mov_b64 exec, -1\n" \
  "s_branch LT_s\n" \
  "L11_:\n" \
  "s_cmp_lg_u32 s35, 23\n" \
  "s_cbranch_scc1 L12_\n" \
  "s_lshl_b32 s35, s30, 5\n" \
  "v_lshrrev_b32 v206, 3, v255\n" \
  "v_and_b32 v208, 7, v255\n" \
  "v_lshl_add_u32 v210, v206, 2, s35\n" \
  "v_lshl_add_u32 v212, v208, 2, s35\n" \
  "global_load_dword v202, v210, s[48:49] offset:2048 sc1\n" \
  "global_load_dword v204, v212, s[48:49] offset:2048 sc1\n" \
  "s_branch LT_s\n" \
  "L12_:\n" \
  "s_cmp_lg_u32 s35, 24\n" \
  "s_cbranch_scc1 L13_\n" \
  "s_mov_b32 s5, 0x10000\n" \
  "LB_z:\n" \
  "v_min_u32 v214, v202, v204\n" \
  "v_cmp_eq_u32 s[58:59], 0, v214\n" \
  "s_sub_u32 s5, s5, 1\n" \
  "s_cbranch_scc1 LB_zx\n" \
  "s_cmp_eq_u64 s[58:59], 0\n" \
  "s_cbranch_scc1 LB_zx\n" \
  "s_cmp_lg_u32 s33, 1021\n" \
  "s_cbranch_scc1 LB_n1\n" \
  "global_load_dwordx4 v[240:243], v238, s[48:49] offset:0 sc1\n" \
  "s_waitcnt vmcnt(0)\n" \
  "global_store_dwordx4 v238, v[240:243], s[48:49] offset:1024 sc1\n" \
  "LB_n1:\n" \
  "s_sleep 4\n" \
  "global_load_dword v202, v210, s[48:49] offset:2048 sc1\n" \
  "global_load_dword v204, v212, s[48:49] offset:2048 sc1\n" \
  "s_waitcnt vmcnt(0)\n" \
  "s_branch LB_z\n" \
  "LB_zx:\n" \
  "v_and_or_b32 v202, v202, -8, v206\n" \
  "v_and_or_b32 v204, v204, -8, v208\n" \
  "v_cmp_lt_u32 s[58:59], v204, v202\n" \
  "v_sub_u32 v202, v204, v202\n" \
  "s_lshl_b32 s35, s29, 3\n" \
  "s_lshr_b64 s[40:41], s[58:59], s35\n" \
  "s_and_b32 s40, s40, 0xff\n" \
  "s_bcnt1_i32_b32 s40, s40\n" \
  "s_sub_u32 s40, 7, s40\n" \
  "s_mov_b32 s36, s29\n" \
  "s_bfe_u32 s41, s58, 0x80000\n" \
  "s_bcnt1_i32_b32 s41, s41\n" \
  "s_cmp_eq_u32 s41, s40\n" \
  "s_cselect_b32 s36, 0, s36\n" \
  "s_bfe_u32 s41, s58, 0x80008\n" \
  "s_bcnt1_i32_b32 s41, s41\n" \
  "s_cmp_eq_u32 s41, s40\n" \
  "s_cselect_b32 s36, 1, s36\n" \
  "s_bfe_u32 s41, s58, 0x80010\n" \
  "s_bcnt1_i32_b32 s41, s41\n" \
  "s_cmp_eq_u32 s41, s40\n" \
  "s_cselect_b32 s36, 2, s36\n" \
  "s_bfe_u32 s41, s58, 0x80018\n" \
  "s_bcnt1_i32_b32 s41, s41\n" \
  "s_cmp_eq_u32 s41, s40\n" \
  "s_cselect_b32 s36, 3, s36\n" \
  "s_bfe_u32 s41, s59, 0x80000\n" \
  "s_bcnt1_i32_b32 s41, s41\n" \
  "s_cmp_eq_u32 s41, s40\n" \
  "s_cselect_b32 s36, 4, s36\n" \
  "s_bfe_u32 s41, s59, 0x80008\n" \
  "s_bcnt1_i32_b32 s41, s41\n" \
  "s_cmp_eq_u32 s41, s40\n" \
  "s_cselect_b32 s36, 5, s36\n" \
  "s_bfe_u32 s41, s59, 0x80010\n" \
  "s_bcnt1_i32_b32 s41, s41\n" \
  "s_cmp_eq_u32 s41, s40\n" \
  "s_cselect_b32 s36, 6, s36\n" \
  "s_bfe_u32 s41, s59, 0x80018\n" \
  "s_bcnt1_i32_b32 s41, s41\n" \
  "s_cmp_eq_u32 s41, s40\n" \
  "s_cselect_b32 s36, 7, s36\n" \
  "s_add_u32 s35, s35, s36\n" \
  "v_readlane_b32 s41, v202, s35\n" \
  "s_sub_u32 s35, 0, s41\n" \
  "s_cmp_lt_u32 s40, 4\n" \
  "s_cselect_b32 s41, s35, s41\n" \
  "s_min_u32 s41, s41, 0xffff\n" \
  "s_mul_i32 s41, s41, 39\n" \
  "s_add_u32 s41, s41, 28672\n" \
  "s_lshr_b32 s41, s41, 16\n" \
  "s_min_u32 s41, s41, 4\n" \
  "s_sub_u32 s35, 0, s41\n" \
  "s_cmp_lt_u32 s40, 4\n" \
  "s_cselect_b32 s35, s35, s41\n" \
  "s_lshl4_add_u32 s35, s36, s35\n" \
  "s_add_u32 s35, s35, 132\n" \
  "v_mov_b32 v204, s35\n" \
  "ds_write_b32 v190, v204 offset:4\n" \
  "s_branch LT_s\n" \
  "L13_:\n" \
  "s_cmp_lg_u32 s35, 25\n" \
  "s_cbranch_scc1 L14_\n" \
  "s_mov_b64 exec, 0xff\n" \
  "global_load_dword v200, v198, s[48:49] offset:3072 sc1\n" \
  "s_mov_b64 exec, -1\n" \
  "s_branch LT_s\n" \
  "L14_:\n" \
  "s_cmp_lg_u32 s35, 26\n" \
  "s_cbranch_scc1 L15_\n" \
  "s_mov_b64 s[58:59], vcc\n" \
  "s_mov_b32 s5, 0x10000\n" \
  "LB_p:\n" \
  "s_mov_b64 exec, 0xff\n" \
  "v_cmp_le_u32 vcc, 32, v200\n" \
  "s_mov_b64 exec, -1\n" \
  "s_sub_u32 s5, s5, 1\n" \
  "s_cselect_b32 s35, 0xff, vcc_lo\n" \
  "s_cmp_eq_u32 s35, 0xff\n" \
  "s_cbranch_scc1 LB_p_x\n" \
  "s_cmp_lg_u32 s33, 1021\n" \
  "s_cbranch_scc1 LB_n2\n" \
  "global_load_dwordx4 v[240:243], v238, s[48:49] offset:0 sc1\n" \
  "s_waitcnt vmcnt(0)\n" \
  "global_store_dwordx4 v238, v[240:243], s[48:49] offset:1024 sc1\n" \
  "LB_n2:\n" \
  "s_sleep 2\n" \
  "s_mov_b64 exec, 0xff\n" \
  "global_load_dword v200, v198, s[48:49] offset:3072 sc1\n" \
  "s_mov_b64 exec, -1\n" \
  "s_waitcnt vmcnt(0)\n" \
  "s_branch LB_p\n" \
  "LB_p_x:\n" \
  "s_mov_b64 vcc, s[58:59]\n" \
  "s_branch LT_s\n" \
  "L15_:\n" \
  "s_branch LT_s\n" \
  "LT_m:\n" \
  "ds_write_b16_d16_hi v195, v196 offset:0\n" \
  "ds_write_b16_d16_hi v195, v198 offset:16\n" \
  "ds_write_b16_d16_hi v195, v200 offset:32\n" \
  "ds_write_b16_d16_hi v235, v202 offset:64\n" \
  "ds_write_b16_d16_hi v235, v204 offset:80\n" \
  "ds_write_b16_d16_hi v235, v206 offset:96\n" \
  "ds_write_b16_d16_hi v235, v208 offset:112\n" \
  "ds_write_b16_d16_hi v235, v210 offset:128\n" \
  "ds_write_b16_d16_hi v235, v212 offset:144\n" \
  "ds_write_b16_d16_hi v235, v214 offset:160\n" \
  "ds_write_b16_d16_hi v235, v216 offset:176\n" \
  "ds_write_b16_d16_hi v195, v218 offset:48\n" \
  "ds_write_b16_d16_hi v195, v220 offset:192\n" \
  "ds_write_b16_d16_hi v195, v222 offset:208\n" \
  "ds_write_b16_d16_hi v195, v224 offset:224\n" \
  "ds_write_b16_d16_hi v195, v226 offset:240\n" \
  "ds_read_b128 v[244:247], v254 offset:0\n" \
  "ds_read_b128 v[248:251], v254 offset:1088\n" \
  "s_lshr_b32 s35, s7, 5\n" \
  "s_mul_i32 s35, s35, 819200\n" \
  "s_and_b32 s36, s7, 31\n" \
  "s_lshl_b32 s36, s36, 10\n" \
  "s_add_u32 s35, s35, s36\n" \
  "s_lshl_b32 s36, s4, 9\n" \
  "s_add_u32 s35, s35, s36\n" \
  "s_add_u32 s35, s35, 102400000\n" \
  "s_add_u32 s58, s24, s35\n" \
  "s_addc_u32 s59, s25, 0\n" \
  "s_waitcnt lgkmcnt(0)\n" \
  "s_cmp_ge_u32 s4, 768\n" \
  "s_cbranch_scc1 L16_\n" \
  "global_store_dwordx4 v238, v[244:247], s[58:59] sc1\n" \
  "s_cmp_ge_u32 s4, 768\n" \
  "s_cbranch_scc1 L16_\n" \
  "s_add_u32 s58, s58, 32768\n" \
  "s_addc_u32 s59, s59, 0\n" \
  "global_store_dwordx4 v238, v[248:251], s[58:59] sc1\n" \
  "L16_:\n" \
  "v_mov_b32 v234, 0\n" \
  "v_mov_b32 v236, v237\n" \
  "s_mov_b32 s38, 1\n" \
  "s_lshl_b32 s35, s29, 8\n" \
  "v_lshl_add_u32 v196, v255, 11, s35\n" \
  "s_lshl_b32 s35, s30, 11\n" \
  "v_lshl_add_u32 v198, v255, 8, s35\n" \
  "s_branch LT\n" \
  "LB_t_h:\n" \
  "s_waitcnt lgkmcnt(12)\n" \
  "v_mfma_f32_16x16x32_bf16 v[160:163], v[128:131], v[64:67], 0\n" \
  "v_mfma_f32_16x16x32_bf16 v[160:163], v[132:135], v[68:71], v[160:163]\n" \
  "s_waitcnt lgkmcnt(8)\n" \
  "v_mfma_f32_16x16x32_bf16 v[160:163], v[136:139], v[72:75], v[160:163]\n" \
  "v_mfma_f32_16x16x32_bf16 v[160:163], v[140:143], v[76:79], v[160:163]\n" \
  "s_waitcnt lgkmcnt(0)\n" \
  "v_mfma_f32_16x16x32_bf16 v[164:167], v[156:159], v[76:79], 0\n" \
  "v_mfma_f32_16x16x32_bf16 v[164:167], v[152:155], v[72:75], v[164:167]\n" \
  "v_mfma_f32_16x16x32_bf16 v[164:167], v[148:151], v[68:71], v[164:167]\n" \
  "v_mfma_f32_16x16x32_bf16 v[164:167], v[144:147], v[64:67], v[164:167]\n" \
  "v_mfma_f32_16x16x32_bf16 v[172:175], v[144:147], v[80:83], 0\n" \
  "v_mfma_f32_16x16x32_bf16 v[172:175], v[148:151], v[84:87], v[172:175]\n" \
  "v_mfma_f32_16x16x32_bf16 v[172:175], v[152:155], v[88:91], v[172:175]\n" \
  "v_cvt_pk_bf16_f32 v176, v160, v161\n" \
  "v_cvt_pk_bf16_f32 v177, v162, v163\n" \
  "v_mfma_f32_16x16x32_bf16 v[172:175], v[156:159], v[92:95], v[172:175]\n" \
  "v_mfma_f32_16x16x32_bf16 v[168:171], v[140:143], v[92:95], 0\n" \
  "v_mfma_f32_16x16x32_bf16 v[168:171], v[136:139], v[88:91], v[168:171]\n" \
  "v_cvt_pk_bf16_f32 v178, v164, v165\n" \
  "v_cvt_pk_bf16_f32 v179, v166, v167\n" \
  "v_mfma_f32_16x16x32_bf16 v[168:171], v[132:135], v[84:87], v[168:171]\n" \
  "v_mfma_f32_16x16x32_bf16 v[168:171], v[128:131], v[80:83], v[168:171]\n" \
  "s_waitcnt vmcnt(63)\n" \
  "global_store_dwordx4 v186, v[176:179], s[46:47] offset:1024 sc1\n" \
  "v_mfma_f32_16x16x32_bf16 v[160:163], v[128:131], v[96:99], 0\n" \
  "v_cvt_pk_bf16_f32 v182, v172, v173\n" \
  "v_cvt_pk_bf16_f32 v183, v174, v175\n" \
  "v_mfma_f32_16x16x32_bf16 v[160:163], v[132:135], v[100:103], v[160:163]\n" \
  "v_mfma_f32_16x16x32_bf16 v[160:163], v[136:139], v[104:107], v[160:163]\n" \
  "v_mfma_f32_16x16x32_bf16 v[160:163], v[140:143], v[108:111], v[160:163]\n" \
  "v_cvt_pk_bf16_f32 v180, v168, v169\n" \
  "v_cvt_pk_bf16_f32 v181, v170, v171\n" \
  "v_mfma_f32_16x16x32_bf16 v[164:167], v[156:159], v[108:111], 0\n" \
  "v_mfma_f32_16x16x32_bf16 v[164:167], v[152:155], v[104:107], v[164:167]\n" \
  "v_mfma_f32_16x16x32_bf16 v[164:167], v[148:151], v[100:103], v[164:167]\n" \
  "v_mfma_f32_16x16x32_bf16 v[164:167], v[144:147], v[96:99], v[164:167]\n" \
  "s_waitcnt vmcnt(63)\n" \
  "global_store_dwordx4 v186, v[180:183], s[46:47] offset:1280 sc1\n" \
  "v_mfma_f32_16x16x32_bf16 v[168:171], v[128:131], v[112:115], 0\n" \
  "v_cvt_pk_bf16_f32 v176, v160, v161\n" \
  "v_cvt_pk_bf16_f32 v177, v162, v163\n" \
  "v_mfma_f32_16x16x32_bf16 v[168:171], v[132:135], v[116:119], v[168:171]\n" \
  "v_mfma_f32_16x16x32_bf16 v[168:171], v[136:139], v[120:123], v[168:171]\n" \
  "v_mfma_f32_16x16x32_bf16 v[168:171], v[140:143], v[124:127], v[168:171]\n" \
  "v_cvt_pk_bf16_f32 v178, v164, v165\n" \
  "v_cvt_pk_bf16_f32 v179, v166, v167\n" \
  "v_mfma_f32_16x16x32_bf16 v[172:175], v[156:159], v[124:127], 0\n" \
  "v_mfma_f32_16x16x32_bf16 v[172:175], v[152:155], v[120:123], v[172:175]\n" \
  "v_mfma_f32_16x16x32_bf16 v[172:175], v[148:151], v[116:119], v[172:175]\n" \
  "v_mfma_f32_16x16x32_bf16 v[172:175], v[144:147], v[112:115], v[172:175]\n" \
  "s_waitcnt vmcnt(63)\n" \
  "global_store_dwordx4 v187, v[176:179], s[46:47] offset:0 sc1\n" \
  "v_cvt_pk_bf16_f32 v180, v168, v169\n" \
  "v_cvt_pk_bf16_f32 v181, v170, v171\n" \
  "s_nop 3\n" \
  "v_cvt_pk_bf16_f32 v182, v172, v173\n" \
  "v_cvt_pk_bf16_f32 v183, v174, v175\n" \
  "s_waitcnt vmcnt(63)\n" \
  "global_store_dwordx4 v188, v[180:183], s[46:47] offset:0 sc1\n" \
  "LB_t_e:\n" \
  "s_setprio 2\n" \
  "v_mbcnt_lo_u32_b32 v189, -1, 0\n" \
  "v_mbcnt_hi_u32_b32 v189, -1, v189\n" \
  "v_lshlrev_b32 v188, 4, v189\n" \
  "s_lshl_b32 s35, s28, 12\n" \
  "v_add_u32 v128, s35, v188\n" \
  "v_mov_b32 v129, 1\n" \
  "s_lshr_b32 s35, s30, 4\n" \
  "s_lshl1_add_u32 s35, s29, s35\n" \
  "s_lshl_b32 s35, s35, 6\n" \
  "s_bfe_u32 s37, s30, 0x20002\n" \
  "s_lshl3_add_u32 s35, s37, s35\n" \
  "v_and_b32 v190, 16, v189\n" \
  "v_lshl_add_u32 v190, v190, 1, s35\n" \
  "global_load_dwordx2 v[204:205], v190, s[16:17]\n" \
  "global_load_dwordx2 v[206:207], v190, s[18:19]\n" \
  "v_lshrrev_b32 v180, 1, v189\n" \
  "v_xor_b32 v180, v180, v189\n" \
  "v_and_b32 v180, 8, v180\n" \
  "v_bfe_u32 v181, v189, 2, 1\n" \
  "v_lshlrev_b32_e64 v181, v181, 5\n" \
  "v_lshlrev_b32 v180, v180, v181\n" \
  "v_bfe_u32 v181, v189, 2, 2\n" \
  "v_lshlrev_b32_e64 v181, v181, 1\n" \
  "v_cmp_gt_u32 vcc, 32, v189\n" \
  "v_mov_b32 v191, 0x3f80\n" \
  "v_add_u32 v190, 32000, v188\n" \
  "v_cndmask_b32 v192, v181, v180, vcc\n" \
  "v_bfe_i32 v180, v192, 0, 1\n" \
  "v_and_b32 v180, v180, v191\n" \
  "v_bfe_i32 v181, v192, 1, 1\n" \
  "v_and_b32 v181, v181, v191\n" \
  "v_bfe_i32 v182, v192, 2, 1\n" \
  "v_and_b32 v182, v182, v191\n" \
  "v_bfe_i32 v183, v192, 3, 1\n" \
  "v_and_b32 v183, v183, v191\n" \
  "v_and_b32 v192, 3, v189\n" \
  "v_mul_u32_u24 v192, 0x5555, v192\n" \
  "v_add_u32 v189, 16000, v188\n" \
  "v_add_u32 v191, 48000, v188\n" \
  "s_waitcnt vmcnt(0)\n" \
  "v_lshlrev_b32 v184, 16, v204\n" \
  "v_and_b32 v185, 0xffff0000, v204\n" \
  "v_lshlrev_b32 v186, 16, v205\n" \
  "v_and_b32 v187, 0xffff0000, v205\n" \
  "s_setprio 0\n" \
  "s_barrier\n" \
  "s_cmp_lg_u32 s28, 0\n" \
  "s_cbranch_scc1 LB_nb\n" \
  "s_mov_b32 exec_hi, 0\n" \
  "global_atomic_add v196, v129, s[48:49] offset:3076 sc1\n" \
  "s_mov_b64 exec, -1\n" \
  "LB_nb:\n" \
  "s_bitcmp1_b32 s30, 2\n" \
  "s_cbranch_scc1 LB_w_h\n" \
  "ds_write_b128 v128, v[64:67] offset:0\n" \
  "ds_write_b128 v128, v[68:71] offset:1024\n" \
  "ds_write_b128 v128, v[72:75] offset:2048\n" \
  "ds_write_b128 v128, v[76:79] offset:3072\n" \
  "s_add_u32 s35, s59, 64000\n" \
  "s_add_u32 s54, s54, s35\n" \
  "s_addc_u32 s55, s55, 0\n" \
  "s_add_u32 s58, s59, 0x10000\n" \
  "s_mov_b32 m0, s58\n" \
  "s_add_u32 s35, s58, 0x8000\n" \
  "global_load_lds_dwordx4 v188, s[54:55] sc1\n" \
  "global_load_lds_dwordx4 v188, s[54:55] offset:1024 sc1\n" \
  "ds_write_b128 v128, v[80:83] offset:16384\n" \
  "ds_write_b128 v128, v[84:87] offset:17408\n" \
  "ds_write_b128 v128, v[88:91] offset:18432\n" \
  "ds_write_b128 v128, v[92:95] offset:19456\n" \
  "ds_write_b128 v128, v[96:99] offset:32768\n" \
  "ds_write_b128 v128, v[100:103] offset:33792\n" \
  "ds_write_b128 v128, v[104:107] offset:34816\n" \
  "ds_write_b128 v128, v[108:111] offset:35840\n" \
  "s_add_u32 m0, s58, 0x4000\n" \
  "s_cmp_lt_u32 s35, 0x28000\n" \
  "global_load_lds_dwordx4 v189, s[54:55] sc1\n" \
  "global_load_lds_dwordx4 v189, s[54:55] offset:1024 sc1\n" \
  "s_cselect_b32 s58, s35, s59\n" \
  "s_add_u32 s54, s54, 32000\n" \
  "s_addc_u32 s55, s55, 0\n" \
  "ds_write_b128 v128, v[112:115] offset:49152\n" \
  "ds_write_b128 v128, v[116:119] offset:50176\n" \
  "ds_write_b128 v128, v[120:123] offset:51200\n" \
  "ds_write_b128 v128, v[124:127] offset:52224\n" \
  "s_branch LB_w_e\n" \
  "LB_w_h:\n" \
  "ds_write_b128 v128, v[0:3] offset:0\n" \
  "ds_write_b128 v128, v[4:7] offset:1024\n" \
  "ds_write_b128 v128, v[8:11] offset:2048\n" \
  "ds_write_b128 v128, v[12:15] offset:3072\n" \
  "s_add_u32 s35, s59, 64000\n" \
  "s_add_u32 s54, s54, s35\n" \
  "s_addc_u32 s55, s55, 0\n" \
  "s_add_u32 s58, s59, 0x10000\n" \
  "s_mov_b32 m0, s58\n" \
  "s_add_u32 s35, s58, 0x8000\n" \
  "global_load_lds_dwordx4 v188, s[54:55] sc1\n" \
  "global_load_lds_dwordx4 v188, s[54:55] offset:1024 sc1\n" \
  "ds_write_b128 v128, v[16:19] offset:16384\n" \
  "ds_write_b128 v128, v[20:23] offset:17408\n" \
  "ds_write_b128 v128, v[24:27] offset:18432\n" \
  "ds_write_b128 v128, v[28:31] offset:19456\n" \
  "ds_write_b128 v128, v[32:35] offset:32768\n" \
  "ds_write_b128 v128, v[36:39] offset:33792\n" \
  "ds_write_b128 v128, v[40:43] offset:34816\n" \
  "ds_write_b128 v128, v[44:47] offset:35840\n" \
  "s_add_u32 m0, s58, 0x4000\n" \
  "s_cmp_lt_u32 s35, 0x28000\n" \
  "global_load_lds_dwordx4 v189, s[54:55] sc1\n" \
  "global_load_lds_dwordx4 v189, s[54:55] offset:1024 sc1\n" \
  "s_cselect_b32 s58, s35, s59\n" \
  "s_add_u32 s54, s54, 32000\n" \
  "s_addc_u32 s55, s55, 0\n" \
  "ds_write_b128 v128, v[48:51] offset:49152\n" \
  "ds_write_b128 v128, v[52:55] offset:50176\n" \
  "ds_write_b128 v128, v[56:59] offset:51200\n" \
  "ds_write_b128 v128, v[60:63] offset:52224\n" \
  "LB_w_e:\n" \
  "s_waitcnt lgkmcnt(0)\n" \
  "s_mov_b32 s51, 7\n" \
  "s_setprio 0\n" \
  "s_barrier\n" \
  "LG_start:\n" \
  "v_lshlrev_b32_sdwa v64, 4, v208 dst_sel:DWORD dst_unused:UNUSED_PAD src0_sel:DWORD src1_sel:WORD_0\n" \
  "ds_read_b128 v[68:71], v64\n" \
  "v_mov_b64 v[0:1], v[184:185]\n" \
  "v_lshlrev_b32_sdwa v65, 4, v208 dst_sel:DWORD dst_unused:UNUSED_PAD src0_sel:DWORD src1_sel:WORD_1\n" \
  "ds_read_b128 v[72:75], v65\n" \
  "v_mov_b64 v[2:3], v[186:187]\n" \
  "v_lshlrev_b32_sdwa v66, 4, v209 dst_sel:DWORD dst_unused:UNUSED_PAD src0_sel:DWORD src1_sel:WORD_0\n" \
  "ds_read_b128 v[76:79], v66\n" \
  "v_mov_b64 v[4:5], v[184:185]\n" \
  "v_lshlrev_b32_sdwa v67, 4, v209 dst_sel:DWORD dst_unused:UNUSED_PAD src0_sel:DWORD src1_sel:WORD_1\n" \
  "ds_read_b128 v[80:83], v67\n" \
  "v_mov_b64 v[6:7], v[186:187]\n" \
  "s_mov_b32 m0, s58\n" \
  "s_add_u32 s35, s58, 0x8000\n" \
  "global_load_lds_dwordx4 v188, s[54:55] sc1\n" \
  "global_load_lds_dwordx4 v188, s[54:55] offset:1024 sc1\n" \
  "v_lshlrev_b32_sdwa v64, 4, v210 dst_sel:DWORD dst_unused:UNUSED_PAD src0_sel:DWORD src1_sel:WORD_0\n" \
  "ds_read_b128 v[84:87], v64\n" \
  "v_mov_b64 v[8:9], v[184:185]\n" \
  "v_lshlrev_b32_sdwa v65, 4, v210 dst_sel:DWORD dst_unused:UNUSED_PAD src0_sel:DWORD src1_sel:WORD_1\n" \
  "ds_read_b128 v[88:91], v65\n" \
  "v_mov_b64 v[10:11], v[186:187]\n" \
  "v_lshlrev_b32_sdwa v66, 4, v211 dst_sel:DWORD dst_unused:UNUSED_PAD src0_sel:DWORD src1_sel:WORD_0\n" \
  "ds_read_b128 v[92:95], v66\n" \
  "v_mov_b64 v[12:13], v[184:185]\n" \
  "v_lshlrev_b32_sdwa v67, 4, v211 dst_sel:DWORD dst_unused:UNUSED_PAD src0_sel:DWORD src1_sel:WORD_1\n" \
  "ds_read_b128 v[96:99], v67\n" \
  "v_mov_b64 v[14:15], v[186:187]\n" \
  "s_add_u32 m0, s58, 0x4000\n" \
  "s_cmp_lt_u32 s35, 0x28000\n" \
  "global_load_lds_dwordx4 v189, s[54:55] sc1\n" \
  "global_load_lds_dwordx4 v189, s[54:55] offset:1024 sc1\n" \
  "s_cselect_b32 s58, s35, s59\n" \
  "v_lshlrev_b32_sdwa v64, 4, v212 dst_sel:DWORD dst_unused:UNUSED_PAD src0_sel:DWORD src1_sel:WORD_0\n" \
  "ds_read_b128 v[100:103], v64\n" \
  "v_mov_b64 v[16:17], v[184:185]\n" \
  "v_lshlrev_b32_sdwa v65, 4, v212 dst_sel:DWORD dst_unused:UNUSED_PAD src0_sel:DWORD src1_sel:WORD_1\n" \
  "ds_read_b128 v[104:107], v65\n" \
  "v_mov_b64 v[18:19], v[186:187]\n" \
  "v_lshlrev_b32_sdwa v66, 4, v213 dst_sel:DWORD dst_unused:UNUSED_PAD src0_sel:DWORD src1_sel:WORD_0\n" \
  "ds_read_b128 v[108:111], v66\n" \
  "v_mov_b64 v[20:21], v[184:185]\n" \
  "v_lshlrev_b32_sdwa v67, 4, v213 dst_sel:DWORD dst_unused:UNUSED_PAD src0_sel:DWORD src1_sel:WORD_1\n" \
  "ds_read_b128 v[112:115], v67\n" \
  "v_mov_b64 v[22:23], v[186:187]\n" \
  "s_waitcnt lgkmcnt(10)\n" \
  "v_smfmac_f32_16x16x64_bf16 v[0:3], v[180:183], v[68:75], v192\n" \
  "v_lshlrev_b32_sdwa v64, 4, v214 dst_sel:DWORD dst_unused:UNUSED_PAD src0_sel:DWORD src1_sel:WORD_0\n" \
  "ds_read_b128 v[68:71], v64\n" \
  "v_mov_b64 v[24:25], v[184:185]\n" \
  "v_lshlrev_b32_sdwa v65, 4, v214 dst_sel:DWORD dst_unused:UNUSED_PAD src0_sel:DWORD src1_sel:WORD_1\n" \
  "ds_read_b128 v[72:75], v65\n" \
  "v_mov_b64 v[26:27], v[186:187]\n" \
  "s_mov_b32 m0, s58\n" \
  "s_add_u32 s35, s58, 0x8000\n" \
  "global_load_lds_dwordx4 v190, s[54:55] sc1\n" \
  "global_load_lds_dwordx4 v190, s[54:55] offset:1024 sc1\n" \
  "s_waitcnt lgkmcnt(10)\n" \
  "v_smfmac_f32_16x16x64_bf16 v[4:7], v[180:183], v[76:83], v192\n" \
  "v_lshlrev_b32_sdwa v66, 4, v215 dst_sel:DWORD dst_unused:UNUSED_PAD src0_sel:DWORD src1_sel:WORD_0\n" \
  "ds_read_b128 v[76:79], v66\n" \
  "v_mov_b64 v[28:29], v[184:185]\n" \
  "v_lshlrev_b32_sdwa v67, 4, v215 dst_sel:DWORD dst_unused:UNUSED_PAD src0_sel:DWORD src1_sel:WORD_1\n" \
  "ds_read_b128 v[80:83], v67\n" \
  "v_mov_b64 v[30:31], v[186:187]\n" \
  "s_waitcnt lgkmcnt(10)\n" \
  "v_smfmac_f32_16x16x64_bf16 v[8:11], v[180:183], v[84:91], v192\n" \
  "v_lshlrev_b32_sdwa v64, 4, v216 dst_sel:DWORD dst_unused:UNUSED_PAD src0_sel:DWORD src1_sel:WORD_0\n" \
  "ds_read_b128 v[84:87], v64\n" \
  "v_mov_b64 v[32:33], v[184:185]\n" \
  "v_lshlrev_b32_sdwa v65, 4, v216 dst_sel:DWORD dst_unused:UNUSED_PAD src0_sel:DWORD src1_sel:WORD_1\n" \
  "ds_read_b128 v[88:91], v65\n" \
  "v_mov_b64 v[34:35], v[186:187]\n" \
  "s_add_u32 m0, s58, 0x4000\n" \
  "s_cmp_lt_u32 s35, 0x28000\n" \
  "global_load_lds_dwordx4 v191, s[54:55] sc1\n" \
  "global_load_lds_dwordx4 v191, s[54:55] offset:1024 sc1\n" \
  "s_cselect_b32 s58, s35, s59\n" \
  "s_waitcnt lgkmcnt(10)\n" \
  "v_smfmac_f32_16x16x64_bf16 v[12:15], v[180:183], v[92:99], v192\n" \
  "v_lshlrev_b32_sdwa v66, 4, v217 dst_sel:DWORD dst_unused:UNUSED_PAD src0_sel:DWORD src1_sel:WORD_0\n" \
  "ds_read_b128 v[92:95], v66\n" \
  "v_mov_b64 v[36:37], v[184:185]\n" \
  "v_lshlrev_b32_sdwa v67, 4, v217 dst_sel:DWORD dst_unused:UNUSED_PAD src0_sel:DWORD src1_sel:WORD_1\n" \
  "ds_read_b128 v[96:99], v67\n" \
  "v_mov_b64 v[38:39], v[186:187]\n" \
  "s_waitcnt lgkmcnt(10)\n" \
  "v_smfmac_f32_16x16x64_bf16 v[16:19], v[180:183], v[100:107], v192\n" \
  "v_lshlrev_b32_sdwa v64, 4, v218 dst_sel:DWORD dst_unused:UNUSED_PAD src0_sel:DWORD src1_sel:WORD_0\n" \
  "ds_read_b128 v[100:103], v64\n" \
  "v_mov_b64 v[40:41], v[184:185]\n" \
  "v_lshlrev_b32_sdwa v65, 4, v218 dst_sel:DWORD dst_unused:UNUSED_PAD src0_sel:DWORD src1_sel:WORD_1\n" \
  "ds_read_b128 v[104:107], v65\n" \
  "v_mov_b64 v[42:43], v[186:187]\n" \
  "global_load_dwordx4 v[240:243], v188, s[56:57] sc1\n" \
  "global_load_dwordx4 v[244:247], v188, s[56:57] offset:1024 sc1\n" \
  "s_waitcnt lgkmcnt(10)\n" \
  "v_smfmac_f32_16x16x64_bf16 v[20:23], v[180:183], v[108:115], v192\n" \
  "v_lshlrev_b32_sdwa v66, 4, v219 dst_sel:DWORD dst_unused:UNUSED_PAD src0_sel:DWORD src1_sel:WORD_0\n" \
  "ds_read_b128 v[108:111], v66\n" \
  "v_mov_b64 v[44:45], v[184:185]\n" \
  "v_lshlrev_b32_sdwa v67, 4, v219 dst_sel:DWORD dst_unused:UNUSED_PAD src0_sel:DWORD src1_sel:WORD_1\n" \
  "ds_read_b128 v[112:115], v67\n" \
  "v_mov_b64 v[46:47], v[186:187]\n" \
  "s_waitcnt lgkmcnt(10)\n" \
  "v_smfmac_f32_16x16x64_bf16 v[24:27], v[180:183], v[68:75], v192\n" \
  "v_lshlrev_b32_sdwa v64, 4, v220 dst_sel:DWORD dst_unused:UNUSED_PAD src0_sel:DWORD src1_sel:WORD_0\n" \
  "ds_read_b128 v[68:71], v64\n" \
  "v_mov_b64 v[48:49], v[184:185]\n" \
  "v_lshlrev_b32_sdwa v65, 4, v220 dst_sel:DWORD dst_unused:UNUSED_PAD src0_sel:DWORD src1_sel:WORD_1\n" \
  "ds_read_b128 v[72:75], v65\n" \
  "v_mov_b64 v[50:51], v[186:187]\n" \
  "global_load_dwordx4 v[248:251], v188, s[56:57] offset:2048 sc1\n" \
  "global_load_dwordx4 v[252:255], v188, s[56:57] offset:3072 sc1\n" \
  "s_waitcnt lgkmcnt(10)\n" \
  "v_smfmac_f32_16x16x64_bf16 v[28:31], v[180:183], v[76:83], v192\n" \
  "v_lshlrev_b32_sdwa v66, 4, v221 dst_sel:DWORD dst_unused:UNUSED_PAD src0_sel:DWORD src1_sel:WORD_0\n" \
  "ds_read_b128 v[76:79], v66\n" \
  "v_mov_b64 v[52:53], v[184:185]\n" \
  "v_lshlrev_b32_sdwa v67, 4, v221 dst_sel:DWORD dst_unused:UNUSED_PAD src0_sel:DWORD src1_sel:WORD_1\n" \
  "ds_read_b128 v[80:83], v67\n" \
  "v_mov_b64 v[54:55], v[186:187]\n" \
  "s_waitcnt lgkmcnt(10)\n" \
  "v_smfmac_f32_16x16x64_bf16 v[32:35], v[180:183], v[84:91], v192\n" \
  "v_lshlrev_b32_sdwa v64, 4, v222 dst_sel:DWORD dst_unused:UNUSED_PAD src0_sel:DWORD src1_sel:WORD_0\n" \
  "ds_read_b128 v[84:87], v64\n" \
  "v_mov_b64 v[56:57], v[184:185]\n" \
  "v_lshlrev_b32_sdwa v65, 4, v222 dst_sel:DWORD dst_unused:UNUSED_PAD src0_sel:DWORD src1_sel:WORD_1\n" \
  "ds_read_b128 v[88:91], v65\n" \
  "v_mov_b64 v[58:59], v[186:187]\n" \
  "s_waitcnt lgkmcnt(10)\n" \
  "v_smfmac_f32_16x16x64_bf16 v[36:39], v[180:183], v[92:99], v192\n" \
  "v_lshlrev_b32_sdwa v66, 4, v223 dst_sel:DWORD dst_unused:UNUSED_PAD src0_sel:DWORD src1_sel:WORD_0\n" \
  "ds_read_b128 v[92:95], v66\n" \
  "v_mov_b64 v[60:61], v[184:185]\n" \
  "v_lshlrev_b32_sdwa v67, 4, v223 dst_sel:DWORD dst_unused:UNUSED_PAD src0_sel:DWORD src1_sel:WORD_1\n" \
  "ds_read_b128 v[96:99], v67\n" \
  "v_mov_b64 v[62:63], v[186:187]\n" \
  "s_add_u32 s56, s56, 32768\n" \
  "s_addc_u32 s57, s57, 0\n" \
  "s_add_u32 s54, s54, 64000\n" \
  "s_addc_u32 s55, s55, 0\n" \
  "s_waitcnt lgkmcnt(10)\n" \
  "v_smfmac_f32_16x16x64_bf16 v[40:43], v[180:183], v[100:107], v192\n" \
  "s_waitcnt lgkmcnt(8)\n" \
  "v_smfmac_f32_16x16x64_bf16 v[44:47], v[180:183], v[108:115], v192\n" \
  "s_waitcnt vmcnt(8)\n" \
  "s_barrier\n" \
  "LG_loop:\n" \
  "v_lshlrev_b32_sdwa v64, 4, v224 dst_sel:DWORD dst_unused:UNUSED_PAD src0_sel:DWORD src1_sel:WORD_0\n" \
  "ds_read_b128 v[100:103], v64\n" \
  "v_lshlrev_b32_sdwa v65, 4, v224 dst_sel:DWORD dst_unused:UNUSED_PAD src0_sel:DWORD src1_sel:WORD_1\n" \
  "ds_read_b128 v[104:107], v65\n" \
  "v_lshlrev_b32_sdwa v66, 4, v225 dst_sel:DWORD dst_unused:UNUSED_PAD src0_sel:DWORD src1_sel:WORD_0\n" \
  "ds_read_b128 v[108:111], v66\n" \
  "v_lshlrev_b32_sdwa v67, 4, v225 dst_sel:DWORD dst_unused:UNUSED_PAD src0_sel:DWORD src1_sel:WORD_1\n" \
  "ds_read_b128 v[112:115], v67\n" \
  "s_waitcnt lgkmcnt(10)\n" \
  "v_smfmac_f32_16x16x64_bf16 v[48:51], v[180:183], v[68:75], v192\n" \
  "s_waitcnt lgkmcnt(8)\n" \
  "v_smfmac_f32_16x16x64_bf16 v[52:55], v[180:183], v[76:83], v192\n" \
  "s_waitcnt lgkmcnt(6)\n" \
  "v_smfmac_f32_16x16x64_bf16 v[56:59], v[180:183], v[84:91], v192\n" \
  "s_waitcnt lgkmcnt(4)\n" \
  "v_smfmac_f32_16x16x64_bf16 v[60:63], v[180:183], v[92:99], v192\n" \
  "s_barrier\n" \
  "s_mov_b32 m0, s58\n" \
  "s_add_u32 s35, s58, 0x8000\n" \
  "global_load_lds_dwordx4 v188, s[54:55] sc1\n" \
  "global_load_lds_dwordx4 v188, s[54:55] offset:1024 sc1\n" \
  "v_lshlrev_b32_sdwa v64, 4, v226 dst_sel:DWORD dst_unused:UNUSED_PAD src0_sel:DWORD src1_sel:WORD_0\n" \
  "ds_read_b128 v[68:71], v64\n" \
  "v_lshlrev_b32_sdwa v65, 4, v226 dst_sel:DWORD dst_unused:UNUSED_PAD src0_sel:DWORD src1_sel:WORD_1\n" \
  "ds_read_b128 v[72:75], v65\n" \
  "v_lshlrev_b32_sdwa v66, 4, v227 dst_sel:DWORD dst_unused:UNUSED_PAD src0_sel:DWORD src1_sel:WORD_0\n" \
  "ds_read_b128 v[76:79], v66\n" \
  "v_lshlrev_b32_sdwa v67, 4, v227 dst_sel:DWORD dst_unused:UNUSED_PAD src0_sel:DWORD src1_sel:WORD_1\n" \
  "ds_read_b128 v[80:83], v67\n" \
  "s_add_u32 m0, s58, 0x4000\n" \
  "s_cmp_lt_u32 s35, 0x28000\n" \
  "global_load_lds_dwordx4 v189, s[54:55] sc1\n" \
  "global_load_lds_dwordx4 v189, s[54:55] offset:1024 sc1\n" \
  "s_cselect_b32 s58, s35, s59\n" \
  "v_lshlrev_b32_sdwa v64, 4, v228 dst_sel:DWORD dst_unused:UNUSED_PAD src0_sel:DWORD src1_sel:WORD_0\n" \
  "ds_read_b128 v[84:87], v64\n" \
  "v_lshlrev_b32_sdwa v65, 4, v228 dst_sel:DWORD dst_unused:UNUSED_PAD src0_sel:DWORD src1_sel:WORD_1\n" \
  "ds_read_b128 v[88:91], v65\n" \
  "v_lshlrev_b32_sdwa v66, 4, v229 dst_sel:DWORD dst_unused:UNUSED_PAD src0_sel:DWORD src1_sel:WORD_0\n" \
  "ds_read_b128 v[92:95], v66\n" \
  "v_lshlrev_b32_sdwa v67, 4, v229 dst_sel:DWORD dst_unused:UNUSED_PAD src0_sel:DWORD src1_sel:WORD_1\n" \
  "ds_read_b128 v[96:99], v67\n" \
  "s_waitcnt lgkmcnt(10)\n" \
  "v_smfmac_f32_16x16x64_bf16 v[0:3], v[180:183], v[100:107], v192\n" \
  "v_lshlrev_b32_sdwa v64, 4, v230 dst_sel:DWORD dst_unused:UNUSED_PAD src0_sel:DWORD src1_sel:WORD_0\n" \
  "ds_read_b128 v[100:103], v64\n" \
  "v_lshlrev_b32_sdwa v65, 4, v230 dst_sel:DWORD dst_unused:UNUSED_PAD src0_sel:DWORD src1_sel:WORD_1\n" \
  "ds_read_b128 v[104:107], v65\n" \
  "s_mov_b32 m0, s58\n" \
  "s_add_u32 s35, s58, 0x8000\n" \
  "global_load_lds_dwordx4 v190, s[54:55] sc1\n" \
  "global_load_lds_dwordx4 v190, s[54:55] offset:1024 sc1\n" \
  "s_waitcnt lgkmcnt(10)\n" \
  "v_smfmac_f32_16x16x64_bf16 v[4:7], v[180:183], v[108:115], v192\n" \
  "v_lshlrev_b32_sdwa v66, 4, v231 dst_sel:DWORD dst_unused:UNUSED_PAD src0_sel:DWORD src1_sel:WORD_0\n" \
  "ds_read_b128 v[108:111], v66\n" \
  "v_lshlrev_b32_sdwa v67, 4, v231 dst_sel:DWORD dst_unused:UNUSED_PAD src0_sel:DWORD src1_sel:WORD_1\n" \
  "ds_read_b128 v[112:115], v67\n" \
  "s_waitcnt lgkmcnt(10)\n" \
  "v_smfmac_f32_16x16x64_bf16 v[8:11], v[180:183], v[68:75], v192\n" \
  "v_lshlrev_b32_sdwa v64, 4, v232 dst_sel:DWORD dst_unused:UNUSED_PAD src0_sel:DWORD src1_sel:WORD_0\n" \
  "ds_read_b128 v[68:71], v64\n" \
  "v_lshlrev_b32_sdwa v65, 4, v232 dst_sel:DWORD dst_unused:UNUSED_PAD src0_sel:DWORD src1_sel:WORD_1\n" \
  "ds_read_b128 v[72:75], v65\n" \
  "s_add_u32 m0, s58, 0x4000\n" \
  "s_cmp_lt_u32 s35, 0x28000\n" \
  "global_load_lds_dwordx4 v191, s[54:55] sc1\n" \
  "global_load_lds_dwordx4 v191, s[54:55] offset:1024 sc1\n" \
  "s_cselect_b32 s58, s35, s59\n" \
  "s_waitcnt lgkmcnt(10)\n" \
  "v_smfmac_f32_16x16x64_bf16 v[12:15], v[180:183], v[76:83], v192\n" \
  "v_lshlrev_b32_sdwa v66, 4, v233 dst_sel:DWORD dst_unused:UNUSED_PAD src0_sel:DWORD src1_sel:WORD_0\n" \
  "ds_read_b128 v[76:79], v66\n" \
  "v_lshlrev_b32_sdwa v67, 4, v233 dst_sel:DWORD dst_unused:UNUSED_PAD src0_sel:DWORD src1_sel:WORD_1\n" \
  "ds_read_b128 v[80:83], v67\n" \
  "s_waitcnt lgkmcnt(10)\n" \
  "v_smfmac_f32_16x16x64_bf16 v[16:19], v[180:183], v[84:91], v192\n" \
  "v_lshlrev_b32_sdwa v64, 4, v234 dst_sel:DWORD dst_unused:UNUSED_PAD src0_sel:DWORD src1_sel:WORD_0\n" \
  "ds_read_b128 v[84:87], v64\n" \
  "v_lshlrev_b32_sdwa v65, 4, v234 dst_sel:DWORD dst_unused:UNUSED_PAD src0_sel:DWORD src1_sel:WORD_1\n" \
  "ds_read_b128 v[88:91], v65\n" \
  "global_load_dwordx4 v[208:211], v188, s[56:57] sc1\n" \
  "global_load_dwordx4 v[212:215], v188, s[56:57] offset:1024 sc1\n" \
  "s_waitcnt lgkmcnt(10)\n" \
  "v_smfmac_f32_16x16x64_bf16 v[20:23], v[180:183], v[92:99], v192\n" \
  "v_lshlrev_b32_sdwa v66, 4, v235 dst_sel:DWORD dst_unused:UNUSED_PAD src0_sel:DWORD src1_sel:WORD_0\n" \
  "ds_read_b128 v[92:95], v66\n" \
  "v_lshlrev_b32_sdwa v67, 4, v235 dst_sel:DWORD dst_unused:UNUSED_PAD src0_sel:DWORD src1_sel:WORD_1\n" \
  "ds_read_b128 v[96:99], v67\n" \
  "s_waitcnt lgkmcnt(10)\n" \
  "v_smfmac_f32_16x16x64_bf16 v[24:27], v[180:183], v[100:107], v192\n" \
  "v_lshlrev_b32_sdwa v64, 4, v236 dst_sel:DWORD dst_unused:UNUSED_PAD src0_sel:DWORD src1_sel:WORD_0\n" \
  "ds_read_b128 v[100:103], v64\n" \
  "v_lshlrev_b32_sdwa v65, 4, v236 dst_sel:DWORD dst_unused:UNUSED_PAD src0_sel:DWORD src1_sel:WORD_1\n" \
  "ds_read_b128 v[104:107], v65\n" \
  "global_load_dwordx4 v[216:219], v188, s[56:57] offset:2048 sc1\n" \
  "global_load_dwordx4 v[220:223], v188, s[56:57] offset:3072 sc1\n" \
  "s_waitcnt lgkmcnt(10)\n" \
  "v_smfmac_f32_16x16x64_bf16 v[28:31], v[180:183], v[108:115], v192\n" \
  "v_lshlrev_b32_sdwa v66, 4, v237 dst_sel:DWORD dst_unused:UNUSED_PAD src0_sel:DWORD src1_sel:WORD_0\n" \
  "ds_read_b128 v[108:111], v66\n" \
  "v_lshlrev_b32_sdwa v67, 4, v237 dst_sel:DWORD dst_unused:UNUSED_PAD src0_sel:DWORD src1_sel:WORD_1\n" \
  "ds_read_b128 v[112:115], v67\n" \
  "s_waitcnt lgkmcnt(10)\n" \
  "v_smfmac_f32_16x16x64_bf16 v[32:35], v[180:183], v[68:75], v192\n" \
  "v_lshlrev_b32_sdwa v64, 4, v238 dst_sel:DWORD dst_unused:UNUSED_PAD src0_sel:DWORD src1_sel:WORD_0\n" \
  "ds_read_b128 v[68:71], v64\n" \
  "v_lshlrev_b32_sdwa v65, 4, v238 dst_sel:DWORD dst_unused:UNUSED_PAD src0_sel:DWORD src1_sel:WORD_1\n" \
  "ds_read_b128 v[72:75], v65\n" \
  "s_waitcnt lgkmcnt(10)\n" \
  "v_smfmac_f32_16x16x64_bf16 v[36:39], v[180:183], v[76:83], v192\n" \
  "v_lshlrev_b32_sdwa v66, 4, v239 dst_sel:DWORD dst_unused:UNUSED_PAD src0_sel:DWORD src1_sel:WORD_0\n" \
  "ds_read_b128 v[76:79], v66\n" \
  "v_lshlrev_b32_sdwa v67, 4, v239 dst_sel:DWORD dst_unused:UNUSED_PAD src0_sel:DWORD src1_sel:WORD_1\n" \
  "ds_read_b128 v[80:83], v67\n" \
  "s_add_u32 s56, s56, 32768\n" \
  "s_addc_u32 s57, s57, 0\n" \
  "s_add_u32 s54, s54, 64000\n" \
  "s_addc_u32 s55, s55, 0\n" \
  "s_waitcnt lgkmcnt(10)\n" \
  "v_smfmac_f32_16x16x64_bf16 v[40:43], v[180:183], v[84:91], v192\n" \
  "s_waitcnt lgkmcnt(8)\n" \
  "v_smfmac_f32_16x16x64_bf16 v[44:47], v[180:183], v[92:99], v192\n" \
  "s_waitcnt vmcnt(8)\n" \
  "s_barrier\n" \
  "v_lshlrev_b32_sdwa v64, 4, v240 dst_sel:DWORD dst_unused:UNUSED_PAD src0_sel:DWORD src1_sel:WORD_0\n" \
  "ds_read_b128 v[84:87], v64\n" \
  "v_lshlrev_b32_sdwa v65, 4, v240 dst_sel:DWORD dst_unused:UNUSED_PAD src0_sel:DWORD src1_sel:WORD_1\n" \
  "ds_read_b128 v[88:91], v65\n" \
  "v_lshlrev_b32_sdwa v66, 4, v241 dst_sel:DWORD dst_unused:UNUSED_PAD src0_sel:DWORD src1_sel:WORD_0\n" \
  "ds_read_b128 v[92:95], v66\n" \
  "v_lshlrev_b32_sdwa v67, 4, v241 dst_sel:DWORD dst_unused:UNUSED_PAD src0_sel:DWORD src1_sel:WORD_1\n" \
  "ds_read_b128 v[96:99], v67\n" \
  "s_waitcnt lgkmcnt(10)\n" \
  "v_smfmac_f32_16x16x64_bf16 v[48:51], v[180:183], v[100:107], v192\n" \
  "s_waitcnt lgkmcnt(8)\n" \
  "v_smfmac_f32_16x16x64_bf16 v[52:55], v[180:183], v[108:115], v192\n" \
  "s_waitcnt lgkmcnt(6)\n" \
  "v_smfmac_f32_16x16x64_bf16 v[56:59], v[180:183], v[68:75], v192\n" \
  "s_waitcnt lgkmcnt(4)\n" \
  "v_smfmac_f32_16x16x64_bf16 v[60:63], v[180:183], v[76:83], v192\n" \
  "s_barrier\n" \
  "s_mov_b32 m0, s58\n" \
  "s_add_u32 s35, s58, 0x8000\n" \
  "global_load_lds_dwordx4 v188, s[54:55] sc1\n" \
  "global_load_lds_dwordx4 v188, s[54:55] offset:1024 sc1\n" \
  "v_lshlrev_b32_sdwa v64, 4, v242 dst_sel:DWORD dst_unused:UNUSED_PAD src0_sel:DWORD src1_sel:WORD_0\n" \
  "ds_read_b128 v[100:103], v64\n" \
  "v_lshlrev_b32_sdwa v65, 4, v242 dst_sel:DWORD dst_unused:UNUSED_PAD src0_sel:DWORD src1_sel:WORD_1\n" \
  "ds_read_b128 v[104:107], v65\n" \
  "v_lshlrev_b32_sdwa v66, 4, v243 dst_sel:DWORD dst_unused:UNUSED_PAD src0_sel:DWORD src1_sel:WORD_0\n" \
  "ds_read_b128 v[108:111], v66\n" \
  "v_lshlrev_b32_sdwa v67, 4, v243 dst_sel:DWORD dst_unused:UNUSED_PAD src0_sel:DWORD src1_sel:WORD_1\n" \
  "ds_read_b128 v[112:115], v67\n" \
  "s_add_u32 m0, s58, 0x4000\n" \
  "s_cmp_lt_u32 s35, 0x28000\n" \
  "global_load_lds_dwordx4 v189, s[54:55] sc1\n" \
  "global_load_lds_dwordx4 v189, s[54:55] offset:1024 sc1\n" \
  "s_cselect_b32 s58, s35, s59\n" \
  "v_lshlrev_b32_sdwa v64, 4, v244 dst_sel:DWORD dst_unused:UNUSED_PAD src0_sel:DWORD src1_sel:WORD_0\n" \
  "ds_read_b128 v[68:71], v64\n" \
  "v_lshlrev_b32_sdwa v65, 4, v244 dst_sel:DWORD dst_unused:UNUSED_PAD src0_sel:DWORD src1_sel:WORD_1\n" \
  "ds_read_b128 v[72:75], v65\n" \
  "v_lshlrev_b32_sdwa v66, 4, v245 dst_sel:DWORD dst_unused:UNUSED_PAD src0_sel:DWORD src1_sel:WORD_0\n" \
  "ds_read_b128 v[76:79], v66\n" \
  "v_lshlrev_b32_sdwa v67, 4, v245 dst_sel:DWORD dst_unused:UNUSED_PAD src0_sel:DWORD src1_sel:WORD_1\n" \
  "ds_read_b128 v[80:83], v67\n" \
  "s_waitcnt lgkmcnt(10)\n" \
  "v_smfmac_f32_16x16x64_bf16 v[0:3], v[180:183], v[84:91], v192\n" \
  "v_lshlrev_b32_sdwa v64, 4, v246 dst_sel:DWORD dst_unused:UNUSED_PAD src0_sel:DWORD src1_sel:WORD_0\n" \
  "ds_read_b128 v[84:87], v64\n" \
  "v_lshlrev_b32_sdwa v65, 4, v246 dst_sel:DWORD dst_unused:UNUSED_PAD src0_sel:DWORD src1_sel:WORD_1\n" \
  "ds_read_b128 v[88:91], v65\n" \
  "s_mov_b32 m0, s58\n" \
  "s_add_u32 s35, s58, 0x8000\n" \
  "global_load_lds_dwordx4 v190, s[54:55] sc1\n" \
  "global_load_lds_dwordx4 v190, s[54:55] offset:1024 sc1\n" \
  "s_waitcnt lgkmcnt(10)\n" \
  "v_smfmac_f32_16x16x64_bf16 v[4:7], v[180:183], v[92:99], v192\n" \
  "v_lshlrev_b32_sdwa v66, 4, v247 dst_sel:DWORD dst_unused:UNUSED_PAD src0_sel:DWORD src1_sel:WORD_0\n" \
  "ds_read_b128 v[92:95], v66\n" \
  "v_lshlrev_b32_sdwa v67, 4, v247 dst_sel:DWORD dst_unused:UNUSED_PAD src0_sel:DWORD src1_sel:WORD_1\n" \
  "ds_read_b128 v[96:99], v67\n" \
  "s_waitcnt lgkmcnt(10)\n" \
  "v_smfmac_f32_16x16x64_bf16 v[8:11], v[180:183], v[100:107], v192\n" \
  "v_lshlrev_b32_sdwa v64, 4, v248 dst_sel:DWORD dst_unused:UNUSED_PAD src0_sel:DWORD src1_sel:WORD_0\n" \
  "ds_read_b128 v[100:103], v64\n" \
  "v_lshlrev_b32_sdwa v65, 4, v248 dst_sel:DWORD dst_unused:UNUSED_PAD src0_sel:DWORD src1_sel:WORD_1\n" \
  "ds_read_b128 v[104:107], v65\n" \
  "s_add_u32 m0, s58, 0x4000\n" \
  "s_cmp_lt_u32 s35, 0x28000\n" \
  "global_load_lds_dwordx4 v191, s[54:55] sc1\n" \
  "global_load_lds_dwordx4 v191, s[54:55] offset:1024 sc1\n" \
  "s_cselect_b32 s58, s35, s59\n" \
  "s_waitcnt lgkmcnt(10)\n" \
  "v_smfmac_f32_16x16x64_bf16 v[12:15], v[180:183], v[108:115], v192\n" \
  "v_lshlrev_b32_sdwa v66, 4, v249 dst_sel:DWORD dst_unused:UNUSED_PAD src0_sel:DWORD src1_sel:WORD_0\n" \
  "ds_read_b128 v[108:111], v66\n" \
  "v_lshlrev_b32_sdwa v67, 4, v249 dst_sel:DWORD dst_unused:UNUSED_PAD src0_sel:DWORD src1_sel:WORD_1\n" \
  "ds_read_b128 v[112:115], v67\n" \
  "s_waitcnt lgkmcnt(10)\n" \
  "v_smfmac_f32_16x16x64_bf16 v[16:19], v[180:183], v[68:75], v192\n" \
  "v_lshlrev_b32_sdwa v64, 4, v250 dst_sel:DWORD dst_unused:UNUSED_PAD src0_sel:DWORD src1_sel:WORD_0\n" \
  "ds_read_b128 v[68:71], v64\n" \
  "v_lshlrev_b32_sdwa v65, 4, v250 dst_sel:DWORD dst_unused:UNUSED_PAD src0_sel:DWORD src1_sel:WORD_1\n" \
  "ds_read_b128 v[72:75], v65\n" \
  "global_load_dwordx4 v[224:227], v188, s[56:57] sc1\n" \
  "global_load_dwordx4 v[228:231], v188, s[56:57] offset:1024 sc1\n" \
  "s_waitcnt lgkmcnt(10)\n" \
  "v_smfmac_f32_16x16x64_bf16 v[20:23], v[180:183], v[76:83], v192\n" \
  "v_lshlrev_b32_sdwa v66, 4, v251 dst_sel:DWORD dst_unused:UNUSED_PAD src0_sel:DWORD src1_sel:WORD_0\n" \
  "ds_read_b128 v[76:79], v66\n" \
  "v_lshlrev_b32_sdwa v67, 4, v251 dst_sel:DWORD dst_unused:UNUSED_PAD src0_sel:DWORD src1_sel:WORD_1\n" \
  "ds_read_b128 v[80:83], v67\n" \
  "s_waitcnt lgkmcnt(10)\n" \
  "v_smfmac_f32_16x16x64_bf16 v[24:27], v[180:183], v[84:91], v192\n" \
  "v_lshlrev_b32_sdwa v64, 4, v252 dst_sel:DWORD dst_unused:UNUSED_PAD src0_sel:DWORD src1_sel:WORD_0\n" \
  "ds_read_b128 v[84:87], v64\n" \
  "v_lshlrev_b32_sdwa v65, 4, v252 dst_sel:DWORD dst_unused:UNUSED_PAD src0_sel:DWORD src1_sel:WORD_1\n" \
  "ds_read_b128 v[88:91], v65\n" \
  "global_load_dwordx4 v[232:235], v188, s[56:57] offset:2048 sc1\n" \
  "global_load_dwordx4 v[236:239], v188, s[56:57] offset:3072 sc1\n" \
  "s_waitcnt lgkmcnt(10)\n" \
  "v_smfmac_f32_16x16x64_bf16 v[28:31], v[180:183], v[92:99], v192\n" \
  "v_lshlrev_b32_sdwa v66, 4, v253 dst_sel:DWORD dst_unused:UNUSED_PAD src0_sel:DWORD src1_sel:WORD_0\n" \
  "ds_read_b128 v[92:95], v66\n" \
  "v_lshlrev_b32_sdwa v67, 4, v253 dst_sel:DWORD dst_unused:UNUSED_PAD src0_sel:DWORD src1_sel:WORD_1\n" \
  "ds_read_b128 v[96:99], v67\n" \
  "s_waitcnt lgkmcnt(10)\n" \
  "v_smfmac_f32_16x16x64_bf16 v[32:35], v[180:183], v[100:107], v192\n" \
  "v_lshlrev_b32_sdwa v64, 4, v254 dst_sel:DWORD dst_unused:UNUSED_PAD src0_sel:DWORD src1_sel:WORD_0\n" \
  "ds_read_b128 v[100:103], v64\n" \
  "v_lshlrev_b32_sdwa v65, 4, v254 dst_sel:DWORD dst_unused:UNUSED_PAD src0_sel:DWORD src1_sel:WORD_1\n" \
  "ds_read_b128 v[104:107], v65\n" \
  "s_waitcnt lgkmcnt(10)\n" \
  "v_smfmac_f32_16x16x64_bf16 v[36:39], v[180:183], v[108:115], v192\n" \
  "v_lshlrev_b32_sdwa v66, 4, v255 dst_sel:DWORD dst_unused:UNUSED_PAD src0_sel:DWORD src1_sel:WORD_0\n" \
  "ds_read_b128 v[108:111], v66\n" \
  "v_lshlrev_b32_sdwa v67, 4, v255 dst_sel:DWORD dst_unused:UNUSED_PAD src0_sel:DWORD src1_sel:WORD_1\n" \
  "ds_read_b128 v[112:115], v67\n" \
  "s_add_u32 s56, s56, 32768\n" \
  "s_addc_u32 s57, s57, 0\n" \
  "s_add_u32 s54, s54, 64000\n" \
  "s_addc_u32 s55, s55, 0\n" \
  "s_waitcnt lgkmcnt(10)\n" \
  "v_smfmac_f32_16x16x64_bf16 v[40:43], v[180:183], v[68:75], v192\n" \
  "s_waitcnt lgkmcnt(8)\n" \
  "v_smfmac_f32_16x16x64_bf16 v[44:47], v[180:183], v[76:83], v192\n" \
  "s_waitcnt vmcnt(8)\n" \
  "s_barrier\n" \
  "s_lshl3_add_u32 s36, s27, s51\n" \
  "s_cmp_eq_u32 s36, 37\n" \
  "s_cbranch_scc1 LG_pol\n" \
  "LG_rt0:\n" \
  "v_lshlrev_b32_sdwa v64, 4, v208 dst_sel:DWORD dst_unused:UNUSED_PAD src0_sel:DWORD src1_sel:WORD_0\n" \
  "ds_read_b128 v[68:71], v64\n" \
  "v_lshlrev_b32_sdwa v65, 4, v208 dst_sel:DWORD dst_unused:UNUSED_PAD src0_sel:DWORD src1_sel:WORD_1\n" \
  "ds_read_b128 v[72:75], v65\n" \
  "v_lshlrev_b32_sdwa v66, 4, v209 dst_sel:DWORD dst_unused:UNUSED_PAD src0_sel:DWORD src1_sel:WORD_0\n" \
  "ds_read_b128 v[76:79], v66\n" \
  "v_lshlrev_b32_sdwa v67, 4, v209 dst_sel:DWORD dst_unused:UNUSED_PAD src0_sel:DWORD src1_sel:WORD_1\n" \
  "ds_read_b128 v[80:83], v67\n" \
  "s_waitcnt lgkmcnt(10)\n" \
  "v_smfmac_f32_16x16x64_bf16 v[48:51], v[180:183], v[84:91], v192\n" \
  "s_waitcnt lgkmcnt(8)\n" \
  "v_smfmac_f32_16x16x64_bf16 v[52:55], v[180:183], v[92:99], v192\n" \
  "s_waitcnt lgkmcnt(6)\n" \
  "v_smfmac_f32_16x16x64_bf16 v[56:59], v[180:183], v[100:107], v192\n" \
  "s_waitcnt lgkmcnt(4)\n" \
  "v_smfmac_f32_16x16x64_bf16 v[60:63], v[180:183], v[108:115], v192\n" \
  "s_barrier\n" \
  "s_mov_b32 m0, s58\n" \
  "s_add_u32 s35, s58, 0x8000\n" \
  "global_load_lds_dwordx4 v188, s[54:55] sc1\n" \
  "global_load_lds_dwordx4 v188, s[54:55] offset:1024 sc1\n" \
  "v_lshlrev_b32_sdwa v64, 4, v210 dst_sel:DWORD dst_unused:UNUSED_PAD src0_sel:DWORD src1_sel:WORD_0\n" \
  "ds_read_b128 v[84:87], v64\n" \
  "v_lshlrev_b32_sdwa v65, 4, v210 dst_sel:DWORD dst_unused:UNUSED_PAD src0_sel:DWORD src1_sel:WORD_1\n" \
  "ds_read_b128 v[88:91], v65\n" \
  "v_lshlrev_b32_sdwa v66, 4, v211 dst_sel:DWORD dst_unused:UNUSED_PAD src0_sel:DWORD src1_sel:WORD_0\n" \
  "ds_read_b128 v[92:95], v66\n" \
  "v_lshlrev_b32_sdwa v67, 4, v211 dst_sel:DWORD dst_unused:UNUSED_PAD src0_sel:DWORD src1_sel:WORD_1\n" \
  "ds_read_b128 v[96:99], v67\n" \
  "s_add_u32 m0, s58, 0x4000\n" \
  "s_cmp_lt_u32 s35, 0x28000\n" \
  "global_load_lds_dwordx4 v189, s[54:55] sc1\n" \
  "global_load_lds_dwordx4 v189, s[54:55] offset:1024 sc1\n" \
  "s_cselect_b32 s58, s35, s59\n" \
  "v_lshlrev_b32_sdwa v64, 4, v212 dst_sel:DWORD dst_unused:UNUSED_PAD src0_sel:DWORD src1_sel:WORD_0\n" \
  "ds_read_b128 v[100:103], v64\n" \
  "v_lshlrev_b32_sdwa v65, 4, v212 dst_sel:DWORD dst_unused:UNUSED_PAD src0_sel:DWORD src1_sel:WORD_1\n" \
  "ds_read_b128 v[104:107], v65\n" \
  "v_lshlrev_b32_sdwa v66, 4, v213 dst_sel:DWORD dst_unused:UNUSED_PAD src0_sel:DWORD src1_sel:WORD_0\n" \
  "ds_read_b128 v[108:111], v66\n" \
  "v_lshlrev_b32_sdwa v67, 4, v213 dst_sel:DWORD dst_unused:UNUSED_PAD src0_sel:DWORD src1_sel:WORD_1\n" \
  "ds_read_b128 v[112:115], v67\n" \
  "s_waitcnt lgkmcnt(10)\n" \
  "v_smfmac_f32_16x16x64_bf16 v[0:3], v[180:183], v[68:75], v192\n" \
  "v_lshlrev_b32_sdwa v64, 4, v214 dst_sel:DWORD dst_unused:UNUSED_PAD src0_sel:DWORD src1_sel:WORD_0\n" \
  "ds_read_b128 v[68:71], v64\n" \
  "v_lshlrev_b32_sdwa v65, 4, v214 dst_sel:DWORD dst_unused:UNUSED_PAD src0_sel:DWORD src1_sel:WORD_1\n" \
  "ds_read_b128 v[72:75], v65\n" \
  "s_mov_b32 m0, s58\n" \
  "s_add_u32 s35, s58, 0x8000\n" \
  "global_load_lds_dwordx4 v190, s[54:55] sc1\n" \
  "global_load_lds_dwordx4 v190, s[54:55] offset:1024 sc1\n" \
  "s_waitcnt lgkmcnt(10)\n" \
  "v_smfmac_f32_16x16x64_bf16 v[4:7], v[180:183], v[76:83], v192\n" \
  "v_lshlrev_b32_sdwa v66, 4, v215 dst_sel:DWORD dst_unused:UNUSED_PAD src0_sel:DWORD src1_sel:WORD_0\n" \
  "ds_read_b128 v[76:79], v66\n" \
  "v_lshlrev_b32_sdwa v67, 4, v215 dst_sel:DWORD dst_unused:UNUSED_PAD src0_sel:DWORD src1_sel:WORD_1\n" \
  "ds_read_b128 v[80:83], v67\n" \
  "s_waitcnt lgkmcnt(10)\n" \
  "v_smfmac_f32_16x16x64_bf16 v[8:11], v[180:183], v[84:91], v192\n" \
  "v_lshlrev_b32_sdwa v64, 4, v216 dst_sel:DWORD dst_unused:UNUSED_PAD src0_sel:DWORD src1_sel:WORD_0\n" \
  "ds_read_b128 v[84:87], v64\n" \
  "v_lshlrev_b32_sdwa v65, 4, v216 dst_sel:DWORD dst_unused:UNUSED_PAD src0_sel:DWORD src1_sel:WORD_1\n" \
  "ds_read_b128 v[88:91], v65\n" \
  "s_add_u32 m0, s58, 0x4000\n" \
  "s_cmp_lt_u32 s35, 0x28000\n" \
  "global_load_lds_dwordx4 v191, s[54:55] sc1\n" \
  "global_load_lds_dwordx4 v191, s[54:55] offset:1024 sc1\n" \
  "s_cselect_b32 s58, s35, s59\n" \
  "s_waitcnt lgkmcnt(10)\n" \
  "v_smfmac_f32_16x16x64_bf16 v[12:15], v[180:183], v[92:99], v192\n" \
  "v_lshlrev_b32_sdwa v66, 4, v217 dst_sel:DWORD dst_unused:UNUSED_PAD src0_sel:DWORD src1_sel:WORD_0\n" \
  "ds_read_b128 v[92:95], v66\n" \
  "v_lshlrev_b32_sdwa v67, 4, v217 dst_sel:DWORD dst_unused:UNUSED_PAD src0_sel:DWORD src1_sel:WORD_1\n" \
  "ds_read_b128 v[96:99], v67\n" \
  "s_waitcnt lgkmcnt(10)\n" \
  "v_smfmac_f32_16x16x64_bf16 v[16:19], v[180:183], v[100:107], v192\n" \
  "v_lshlrev_b32_sdwa v64, 4, v218 dst_sel:DWORD dst_unused:UNUSED_PAD src0_sel:DWORD src1_sel:WORD_0\n" \
  "ds_read_b128 v[100:103], v64\n" \
  "v_lshlrev_b32_sdwa v65, 4, v218 dst_sel:DWORD dst_unused:UNUSED_PAD src0_sel:DWORD src1_sel:WORD_1\n" \
  "ds_read_b128 v[104:107], v65\n" \
  "global_load_dwordx4 v[240:243], v188, s[56:57] sc1\n" \
  "global_load_dwordx4 v[244:247], v188, s[56:57] offset:1024 sc1\n" \
  "s_waitcnt lgkmcnt(10)\n" \
  "v_smfmac_f32_16x16x64_bf16 v[20:23], v[180:183], v[108:115], v192\n" \
  "v_lshlrev_b32_sdwa v66, 4, v219 dst_sel:DWORD dst_unused:UNUSED_PAD src0_sel:DWORD src1_sel:WORD_0\n" \
  "ds_read_b128 v[108:111], v66\n" \
  "v_lshlrev_b32_sdwa v67, 4, v219 dst_sel:DWORD dst_unused:UNUSED_PAD src0_sel:DWORD src1_sel:WORD_1\n" \
  "ds_read_b128 v[112:115], v67\n" \
  "s_waitcnt lgkmcnt(10)\n" \
  "v_smfmac_f32_16x16x64_bf16 v[24:27], v[180:183], v[68:75], v192\n" \
  "v_lshlrev_b32_sdwa v64, 4, v220 dst_sel:DWORD dst_unused:UNUSED_PAD src0_sel:DWORD src1_sel:WORD_0\n" \
  "ds_read_b128 v[68:71], v64\n" \
  "v_lshlrev_b32_sdwa v65, 4, v220 dst_sel:DWORD dst_unused:UNUSED_PAD src0_sel:DWORD src1_sel:WORD_1\n" \
  "ds_read_b128 v[72:75], v65\n" \
  "global_load_dwordx4 v[248:251], v188, s[56:57] offset:2048 sc1\n" \
  "global_load_dwordx4 v[252:255], v188, s[56:57] offset:3072 sc1\n" \
  "s_waitcnt lgkmcnt(10)\n" \
  "v_smfmac_f32_16x16x64_bf16 v[28:31], v[180:183], v[76:83], v192\n" \
  "v_lshlrev_b32_sdwa v66, 4, v221 dst_sel:DWORD dst_unused:UNUSED_PAD src0_sel:DWORD src1_sel:WORD_0\n" \
  "ds_read_b128 v[76:79], v66\n" \
  "v_lshlrev_b32_sdwa v67, 4, v221 dst_sel:DWORD dst_unused:UNUSED_PAD src0_sel:DWORD src1_sel:WORD_1\n" \
  "ds_read_b128 v[80:83], v67\n" \
  "s_waitcnt lgkmcnt(10)\n" \
  "v_smfmac_f32_16x16x64_bf16 v[32:35], v[180:183], v[84:91], v192\n" \
  "v_lshlrev_b32_sdwa v64, 4, v222 dst_sel:DWORD dst_unused:UNUSED_PAD src0_sel:DWORD src1_sel:WORD_0\n" \
  "ds_read_b128 v[84:87], v64\n" \
  "v_lshlrev_b32_sdwa v65, 4, v222 dst_sel:DWORD dst_unused:UNUSED_PAD src0_sel:DWORD src1_sel:WORD_1\n" \
  "ds_read_b128 v[88:91], v65\n" \
  "s_waitcnt lgkmcnt(10)\n" \
  "v_smfmac_f32_16x16x64_bf16 v[36:39], v[180:183], v[92:99], v192\n" \
  "v_lshlrev_b32_sdwa v66, 4, v223 dst_sel:DWORD dst_unused:UNUSED_PAD src0_sel:DWORD src1_sel:WORD_0\n" \
  "ds_read_b128 v[92:95], v66\n" \
  "v_lshlrev_b32_sdwa v67, 4, v223 dst_sel:DWORD dst_unused:UNUSED_PAD src0_sel:DWORD src1_sel:WORD_1\n" \
  "ds_read_b128 v[96:99], v67\n" \
  "s_add_u32 s56, s56, 32768\n" \
  "s_addc_u32 s57, s57, 0\n" \
  "s_add_u32 s54, s54, 64000\n" \
  "s_addc_u32 s55, s55, 0\n" \
  "s_waitcnt lgkmcnt(10)\n" \
  "v_smfmac_f32_16x16x64_bf16 v[40:43], v[180:183], v[100:107], v192\n" \
  "s_waitcnt lgkmcnt(8)\n" \
  "v_smfmac_f32_16x16x64_bf16 v[44:47], v[180:183], v[108:115], v192\n" \
  "s_waitcnt vmcnt(8)\n" \
  "s_cmp_eq_u32 s36, 37\n" \
  "s_cbranch_scc1 LG_chk\n" \
  "LG_ret:\n" \
  "s_sub_u32 s51, s51, 1\n" \
  "s_cmp_lg_u32 s51, 0\n" \
  "s_barrier\n" \
  "s_cbranch_scc1 LG_loop\n" \
  "v_lshlrev_b32_sdwa v64, 4, v224 dst_sel:DWORD dst_unused:UNUSED_PAD src0_sel:DWORD src1_sel:WORD_0\n" \
  "ds_read_b128 v[100:103], v64\n" \
  "v_lshlrev_b32_sdwa v65, 4, v224 dst_sel:DWORD dst_unused:UNUSED_PAD src0_sel:DWORD src1_sel:WORD_1\n" \
  "ds_read_b128 v[104:107], v65\n" \
  "v_lshlrev_b32_sdwa v66, 4, v225 dst_sel:DWORD dst_unused:UNUSED_PAD src0_sel:DWORD src1_sel:WORD_0\n" \
  "ds_read_b128 v[108:111], v66\n" \
  "v_lshlrev_b32_sdwa v67, 4, v225 dst_sel:DWORD dst_unused:UNUSED_PAD src0_sel:DWORD src1_sel:WORD_1\n" \
  "ds_read_b128 v[112:115], v67\n" \
  "s_waitcnt lgkmcnt(10)\n" \
  "v_smfmac_f32_16x16x64_bf16 v[48:51], v[180:183], v[68:75], v192\n" \
  "s_waitcnt lgkmcnt(8)\n" \
  "v_smfmac_f32_16x16x64_bf16 v[52:55], v[180:183], v[76:83], v192\n" \
  "s_waitcnt lgkmcnt(6)\n" \
  "v_smfmac_f32_16x16x64_bf16 v[56:59], v[180:183], v[84:91], v192\n" \
  "s_waitcnt lgkmcnt(4)\n" \
  "v_smfmac_f32_16x16x64_bf16 v[60:63], v[180:183], v[92:99], v192\n" \
  "s_barrier\n" \
  "s_mov_b32 m0, s58\n" \
  "s_add_u32 s35, s58, 0x8000\n" \
  "global_load_lds_dwordx4 v188, s[54:55] sc1\n" \
  "global_load_lds_dwordx4 v188, s[54:55] offset:1024 sc1\n" \
  "v_lshlrev_b32_sdwa v64, 4, v226 dst_sel:DWORD dst_unused:UNUSED_PAD src0_sel:DWORD src1_sel:WORD_0\n" \
  "ds_read_b128 v[68:71], v64\n" \
  "v_lshlrev_b32_sdwa v65, 4, v226 dst_sel:DWORD dst_unused:UNUSED_PAD src0_sel:DWORD src1_sel:WORD_1\n" \
  "ds_read_b128 v[72:75], v65\n" \
  "v_lshlrev_b32_sdwa v66, 4, v227 dst_sel:DWORD dst_unused:UNUSED_PAD src0_sel:DWORD src1_sel:WORD_0\n" \
  "ds_read_b128 v[76:79], v66\n" \
  "v_lshlrev_b32_sdwa v67, 4, v227 dst_sel:DWORD dst_unused:UNUSED_PAD src0_sel:DWORD src1_sel:WORD_1\n" \
  "ds_read_b128 v[80:83], v67\n" \
  "s_add_u32 m0, s58, 0x4000\n" \
  "s_cmp_lt_u32 s35, 0x28000\n" \
  "global_load_lds_dwordx4 v189, s[54:55] sc1\n" \
  "global_load_lds_dwordx4 v189, s[54:55] offset:1024 sc1\n" \
  "s_cselect_b32 s58, s35, s59\n" \
  "v_lshlrev_b32_sdwa v64, 4, v228 dst_sel:DWORD dst_unused:UNUSED_PAD src0_sel:DWORD src1_sel:WORD_0\n" \
  "ds_read_b128 v[84:87], v64\n" \
  "v_lshlrev_b32_sdwa v65, 4, v228 dst_sel:DWORD dst_unused:UNUSED_PAD src0_sel:DWORD src1_sel:WORD_1\n" \
  "ds_read_b128 v[88:91], v65\n" \
  "v_lshlrev_b32_sdwa v66, 4, v229 dst_sel:DWORD dst_unused:UNUSED_PAD src0_sel:DWORD src1_sel:WORD_0\n" \
  "ds_read_b128 v[92:95], v66\n" \
  "v_lshlrev_b32_sdwa v67, 4, v229 dst_sel:DWORD dst_unused:UNUSED_PAD src0_sel:DWORD src1_sel:WORD_1\n" \
  "ds_read_b128 v[96:99], v67\n" \
  "s_waitcnt lgkmcnt(10)\n" \
  "v_smfmac_f32_16x16x64_bf16 v[0:3], v[180:183], v[100:107], v192\n" \
  "v_lshlrev_b32_sdwa v64, 4, v230 dst_sel:DWORD dst_unused:UNUSED_PAD src0_sel:DWORD src1_sel:WORD_0\n" \
  "ds_read_b128 v[100:103], v64\n" \
  "v_lshlrev_b32_sdwa v65, 4, v230 dst_sel:DWORD dst_unused:UNUSED_PAD src0_sel:DWORD src1_sel:WORD_1\n" \
  "ds_read_b128 v[104:107], v65\n" \
  "s_mov_b32 m0, s58\n" \
  "s_add_u32 s35, s58, 0x8000\n" \
  "global_load_lds_dwordx4 v190, s[54:55] sc1\n" \
  "global_load_lds_dwordx4 v190, s[54:55] offset:1024 sc1\n" \
  "s_waitcnt lgkmcnt(10)\n" \
  "v_smfmac_f32_16x16x64_bf16 v[4:7], v[180:183], v[108:115], v192\n" \
  "v_lshlrev_b32_sdwa v66, 4, v231 dst_sel:DWORD dst_unused:UNUSED_PAD src0_sel:DWORD src1_sel:WORD_0\n" \
  "ds_read_b128 v[108:111], v66\n" \
  "v_lshlrev_b32_sdwa v67, 4, v231 dst_sel:DWORD dst_unused:UNUSED_PAD src0_sel:DWORD src1_sel:WORD_1\n" \
  "ds_read_b128 v[112:115], v67\n" \
  "s_waitcnt lgkmcnt(10)\n" \
  "v_smfmac_f32_16x16x64_bf16 v[8:11], v[180:183], v[68:75], v192\n" \
  "v_lshlrev_b32_sdwa v64, 4, v232 dst_sel:DWORD dst_unused:UNUSED_PAD src0_sel:DWORD src1_sel:WORD_0\n" \
  "ds_read_b128 v[68:71], v64\n" \
  "v_lshlrev_b32_sdwa v65, 4, v232 dst_sel:DWORD dst_unused:UNUSED_PAD src0_sel:DWORD src1_sel:WORD_1\n" \
  "ds_read_b128 v[72:75], v65\n" \
  "s_add_u32 m0, s58, 0x4000\n" \
  "s_cmp_lt_u32 s35, 0x28000\n" \
  "global_load_lds_dwordx4 v191, s[54:55] sc1\n" \
  "global_load_lds_dwordx4 v191, s[54:55] offset:1024 sc1\n" \
  "s_cselect_b32 s58, s35, s59\n" \
  "s_waitcnt lgkmcnt(10)\n" \
  "v_smfmac_f32_16x16x64_bf16 v[12:15], v[180:183], v[76:83], v192\n" \
  "v_lshlrev_b32_sdwa v66, 4, v233 dst_sel:DWORD dst_unused:UNUSED_PAD src0_sel:DWORD src1_sel:WORD_0\n" \
  "ds_read_b128 v[76:79], v66\n" \
  "v_lshlrev_b32_sdwa v67, 4, v233 dst_sel:DWORD dst_unused:UNUSED_PAD src0_sel:DWORD src1_sel:WORD_1\n" \
  "ds_read_b128 v[80:83], v67\n" \
  "s_waitcnt lgkmcnt(10)\n" \
  "v_smfmac_f32_16x16x64_bf16 v[16:19], v[180:183], v[84:91], v192\n" \
  "v_lshlrev_b32_sdwa v64, 4, v234 dst_sel:DWORD dst_unused:UNUSED_PAD src0_sel:DWORD src1_sel:WORD_0\n" \
  "ds_read_b128 v[84:87], v64\n" \
  "v_lshlrev_b32_sdwa v65, 4, v234 dst_sel:DWORD dst_unused:UNUSED_PAD src0_sel:DWORD src1_sel:WORD_1\n" \
  "ds_read_b128 v[88:91], v65\n" \
  "global_load_dwordx4 v[208:211], v188, s[56:57] sc1\n" \
  "global_load_dwordx4 v[212:215], v188, s[56:57] offset:1024 sc1\n" \
  "s_waitcnt lgkmcnt(10)\n" \
  "v_smfmac_f32_16x16x64_bf16 v[20:23], v[180:183], v[92:99], v192\n" \
  "v_lshlrev_b32_sdwa v66, 4, v235 dst_sel:DWORD dst_unused:UNUSED_PAD src0_sel:DWORD src1_sel:WORD_0\n" \
  "ds_read_b128 v[92:95], v66\n" \
  "v_lshlrev_b32_sdwa v67, 4, v235 dst_sel:DWORD dst_unused:UNUSED_PAD src0_sel:DWORD src1_sel:WORD_1\n" \
  "ds_read_b128 v[96:99], v67\n" \
  "s_waitcnt lgkmcnt(10)\n" \
  "v_smfmac_f32_16x16x64_bf16 v[24:27], v[180:183], v[100:107], v192\n" \
  "v_lshlrev_b32_sdwa v64, 4, v236 dst_sel:DWORD dst_unused:UNUSED_PAD src0_sel:DWORD src1_sel:WORD_0\n" \
  "ds_read_b128 v[100:103], v64\n" \
  "v_lshlrev_b32_sdwa v65, 4, v236 dst_sel:DWORD dst_unused:UNUSED_PAD src0_sel:DWORD src1_sel:WORD_1\n" \
  "ds_read_b128 v[104:107], v65\n" \
  "global_load_dwordx4 v[216:219], v188, s[56:57] offset:2048 sc1\n" \
  "global_load_dwordx4 v[220:223], v188, s[56:57] offset:3072 sc1\n" \
  "s_waitcnt lgkmcnt(10)\n" \
  "v_smfmac_f32_16x16x64_bf16 v[28:31], v[180:183], v[108:115], v192\n" \
  "v_lshlrev_b32_sdwa v66, 4, v237 dst_sel:DWORD dst_unused:UNUSED_PAD src0_sel:DWORD src1_sel:WORD_0\n" \
  "ds_read_b128 v[108:111], v66\n" \
  "v_lshlrev_b32_sdwa v67, 4, v237 dst_sel:DWORD dst_unused:UNUSED_PAD src0_sel:DWORD src1_sel:WORD_1\n" \
  "ds_read_b128 v[112:115], v67\n" \
  "s_waitcnt lgkmcnt(10)\n" \
  "v_smfmac_f32_16x16x64_bf16 v[32:35], v[180:183], v[68:75], v192\n" \
  "v_lshlrev_b32_sdwa v64, 4, v238 dst_sel:DWORD dst_unused:UNUSED_PAD src0_sel:DWORD src1_sel:WORD_0\n" \
  "ds_read_b128 v[68:71], v64\n" \
  "v_lshlrev_b32_sdwa v65, 4, v238 dst_sel:DWORD dst_unused:UNUSED_PAD src0_sel:DWORD src1_sel:WORD_1\n" \
  "ds_read_b128 v[72:75], v65\n" \
  "s_waitcnt lgkmcnt(10)\n" \
  "v_smfmac_f32_16x16x64_bf16 v[36:39], v[180:183], v[76:83], v192\n" \
  "v_lshlrev_b32_sdwa v66, 4, v239 dst_sel:DWORD dst_unused:UNUSED_PAD src0_sel:DWORD src1_sel:WORD_0\n" \
  "ds_read_b128 v[76:79], v66\n" \
  "v_lshlrev_b32_sdwa v67, 4, v239 dst_sel:DWORD dst_unused:UNUSED_PAD src0_sel:DWORD src1_sel:WORD_1\n" \
  "ds_read_b128 v[80:83], v67\n" \
  "s_add_u32 s56, s56, 32768\n" \
  "s_addc_u32 s57, s57, 0\n" \
  "s_add_u32 s54, s54, 64000\n" \
  "s_addc_u32 s55, s55, 0\n" \
  "s_waitcnt lgkmcnt(10)\n" \
  "v_smfmac_f32_16x16x64_bf16 v[40:43], v[180:183], v[84:91], v192\n" \
  "s_waitcnt lgkmcnt(8)\n" \
  "v_smfmac_f32_16x16x64_bf16 v[44:47], v[180:183], v[92:99], v192\n" \
  "s_waitcnt vmcnt(8)\n" \
  "s_barrier\n" \
  "v_lshlrev_b32_sdwa v64, 4, v240 dst_sel:DWORD dst_unused:UNUSED_PAD src0_sel:DWORD src1_sel:WORD_0\n" \
  "ds_read_b128 v[84:87], v64\n" \
  "v_lshlrev_b32_sdwa v65, 4, v240 dst_sel:DWORD dst_unused:UNUSED_PAD src0_sel:DWORD src1_sel:WORD_1\n" \
  "ds_read_b128 v[88:91], v65\n" \
  "v_lshlrev_b32_sdwa v66, 4, v241 dst_sel:DWORD dst_unused:UNUSED_PAD src0_sel:DWORD src1_sel:WORD_0\n" \
  "ds_read_b128 v[92:95], v66\n" \
  "v_lshlrev_b32_sdwa v67, 4, v241 dst_sel:DWORD dst_unused:UNUSED_PAD src0_sel:DWORD src1_sel:WORD_1\n" \
  "ds_read_b128 v[96:99], v67\n" \
  "s_waitcnt lgkmcnt(10)\n" \
  "v_smfmac_f32_16x16x64_bf16 v[48:51], v[180:183], v[100:107], v192\n" \
  "s_waitcnt lgkmcnt(8)\n" \
  "v_smfmac_f32_16x16x64_bf16 v[52:55], v[180:183], v[108:115], v192\n" \
  "s_waitcnt lgkmcnt(6)\n" \
  "v_smfmac_f32_16x16x64_bf16 v[56:59], v[180:183], v[68:75], v192\n" \
  "s_waitcnt lgkmcnt(4)\n" \
  "v_smfmac_f32_16x16x64_bf16 v[60:63], v[180:183], v[76:83], v192\n" \
  "s_barrier\n" \
  "s_mov_b32 m0, s58\n" \
  "s_add_u32 s35, s58, 0x8000\n" \
  "global_load_lds_dwordx4 v188, s[54:55] sc1\n" \
  "global_load_lds_dwordx4 v188, s[54:55] offset:1024 sc1\n" \
  "s_add_u32 m0, s58, 0x4000\n" \
  "s_cmp_lt_u32 s35, 0x28000\n" \
  "global_load_lds_dwordx4 v189, s[54:55] sc1\n" \
  "global_load_lds_dwordx4 v189, s[54:55] offset:1024 sc1\n" \
  "s_cselect_b32 s58, s35, s59\n" \
  "v_lshlrev_b32 v120, 16, v206\n" \
  "v_and_b32 v121, 0xffff0000, v206\n" \
  "v_lshlrev_b32 v122, 16, v207\n" \
  "v_and_b32 v123, 0xffff0000, v207\n" \
  "v_mov_b32 v124, 0xc0135761\n" \
  "v_mov_b32 v125, 0xc0135761\n" \
  "s_mov_b32 s52, 0xbdd2d3e8\n" \
  "s_mov_b32 s53, s52\n" \
  "s_lshl2_add_u32 s35, s33, s34\n" \
  "s_lshl_b32 s35, s35, 14\n" \
  "s_add_u32 s35, s35, s59\n" \
  "s_add_u32 s35, s35, 105676800\n" \
  "s_add_u32 s40, s24, s35\n" \
  "s_addc_u32 s41, s25, 0\n" \
  "v_lshlrev_b32_sdwa v64, 4, v242 dst_sel:DWORD dst_unused:UNUSED_PAD src0_sel:DWORD src1_sel:WORD_0\n" \
  "ds_read_b128 v[100:103], v64\n" \
  "v_lshlrev_b32_sdwa v65, 4, v242 dst_sel:DWORD dst_unused:UNUSED_PAD src0_sel:DWORD src1_sel:WORD_1\n" \
  "ds_read_b128 v[104:107], v65\n" \
  "v_lshlrev_b32_sdwa v66, 4, v243 dst_sel:DWORD dst_unused:UNUSED_PAD src0_sel:DWORD src1_sel:WORD_0\n" \
  "ds_read_b128 v[108:111], v66\n" \
  "v_lshlrev_b32_sdwa v67, 4, v243 dst_sel:DWORD dst_unused:UNUSED_PAD src0_sel:DWORD src1_sel:WORD_1\n" \
  "ds_read_b128 v[112:115], v67\n" \
  "v_lshlrev_b32_sdwa v64, 4, v244 dst_sel:DWORD dst_unused:UNUSED_PAD src0_sel:DWORD src1_sel:WORD_0\n" \
  "ds_read_b128 v[68:71], v64\n" \
  "v_lshlrev_b32_sdwa v65, 4, v244 dst_sel:DWORD dst_unused:UNUSED_PAD src0_sel:DWORD src1_sel:WORD_1\n" \
  "ds_read_b128 v[72:75], v65\n" \
  "v_lshlrev_b32_sdwa v66, 4, v245 dst_sel:DWORD dst_unused:UNUSED_PAD src0_sel:DWORD src1_sel:WORD_0\n" \
  "ds_read_b128 v[76:79], v66\n" \
  "v_lshlrev_b32_sdwa v67, 4, v245 dst_sel:DWORD dst_unused:UNUSED_PAD src0_sel:DWORD src1_sel:WORD_1\n" \
  "ds_read_b128 v[80:83], v67\n" \
  "s_waitcnt lgkmcnt(10)\n" \
  "v_smfmac_f32_16x16x64_bf16 v[0:3], v[180:183], v[84:91], v192\n" \
  "v_lshlrev_b32_sdwa v64, 4, v246 dst_sel:DWORD dst_unused:UNUSED_PAD src0_sel:DWORD src1_sel:WORD_0\n" \
  "ds_read_b128 v[84:87], v64\n" \
  "v_lshlrev_b32_sdwa v65, 4, v246 dst_sel:DWORD dst_unused:UNUSED_PAD src0_sel:DWORD src1_sel:WORD_1\n" \
  "ds_read_b128 v[88:91], v65\n" \
  "s_waitcnt lgkmcnt(10)\n" \
  "v_smfmac_f32_16x16x64_bf16 v[4:7], v[180:183], v[92:99], v192\n" \
  "v_lshlrev_b32_sdwa v66, 4, v247 dst_sel:DWORD dst_unused:UNUSED_PAD src0_sel:DWORD src1_sel:WORD_0\n" \
  "ds_read_b128 v[92:95], v66\n" \
  "v_lshlrev_b32_sdwa v67, 4, v247 dst_sel:DWORD dst_unused:UNUSED_PAD src0_sel:DWORD src1_sel:WORD_1\n" \
  "ds_read_b128 v[96:99], v67\n" \
  "s_waitcnt lgkmcnt(10)\n" \
  "v_smfmac_f32_16x16x64_bf16 v[8:11], v[180:183], v[100:107], v192\n" \
  "v_lshlrev_b32_sdwa v64, 4, v248 dst_sel:DWORD dst_unused:UNUSED_PAD src0_sel:DWORD src1_sel:WORD_0\n" \
  "ds_read_b128 v[100:103], v64\n" \
  "v_lshlrev_b32_sdwa v65, 4, v248 dst_sel:DWORD dst_unused:UNUSED_PAD src0_sel:DWORD src1_sel:WORD_1\n" \
  "ds_read_b128 v[104:107], v65\n" \
  "s_waitcnt lgkmcnt(10)\n" \
  "v_smfmac_f32_16x16x64_bf16 v[12:15], v[180:183], v[108:115], v192\n" \
  "v_lshlrev_b32_sdwa v66, 4, v249 dst_sel:DWORD dst_unused:UNUSED_PAD src0_sel:DWORD src1_sel:WORD_0\n" \
  "ds_read_b128 v[108:111], v66\n" \
  "v_lshlrev_b32_sdwa v67, 4, v249 dst_sel:DWORD dst_unused:UNUSED_PAD src0_sel:DWORD src1_sel:WORD_1\n" \
  "ds_read_b128 v[112:115], v67\n" \
  "s_waitcnt vmcnt(0)\n" \
  "s_barrier\n" \
  "s_waitcnt lgkmcnt(10)\n" \
  "v_smfmac_f32_16x16x64_bf16 v[16:19], v[180:183], v[68:75], v192\n" \
  "v_lshlrev_b32_sdwa v64, 4, v208 dst_sel:DWORD dst_unused:UNUSED_PAD src0_sel:DWORD src1_sel:WORD_0\n" \
  "ds_read_b128 v[68:71], v64\n" \
  "v_lshlrev_b32_sdwa v65, 4, v208 dst_sel:DWORD dst_unused:UNUSED_PAD src0_sel:DWORD src1_sel:WORD_1\n" \
  "ds_read_b128 v[72:75], v65\n" \
  "s_waitcnt lgkmcnt(10)\n" \
  "v_smfmac_f32_16x16x64_bf16 v[20:23], v[180:183], v[76:83], v192\n" \
  "v_lshlrev_b32_sdwa v66, 4, v209 dst_sel:DWORD dst_unused:UNUSED_PAD src0_sel:DWORD src1_sel:WORD_0\n" \
  "ds_read_b128 v[76:79], v66\n" \
  "v_lshlrev_b32_sdwa v67, 4, v209 dst_sel:DWORD dst_unused:UNUSED_PAD src0_sel:DWORD src1_sel:WORD_1\n" \
  "ds_read_b128 v[80:83], v67\n" \
  "s_waitcnt lgkmcnt(10)\n" \
  "v_smfmac_f32_16x16x64_bf16 v[24:27], v[180:183], v[84:91], v192\n" \
  "v_lshlrev_b32_sdwa v64, 4, v210 dst_sel:DWORD dst_unused:UNUSED_PAD src0_sel:DWORD src1_sel:WORD_0\n" \
  "ds_read_b128 v[84:87], v64\n" \
  "v_lshlrev_b32_sdwa v65, 4, v210 dst_sel:DWORD dst_unused:UNUSED_PAD src0_sel:DWORD src1_sel:WORD_1\n" \
  "ds_read_b128 v[88:91], v65\n" \
  "s_waitcnt lgkmcnt(10)\n" \
  "v_smfmac_f32_16x16x64_bf16 v[28:31], v[180:183], v[92:99], v192\n" \
  "v_lshlrev_b32_sdwa v66, 4, v211 dst_sel:DWORD dst_unused:UNUSED_PAD src0_sel:DWORD src1_sel:WORD_0\n" \
  "ds_read_b128 v[92:95], v66\n" \
  "v_lshlrev_b32_sdwa v67, 4, v211 dst_sel:DWORD dst_unused:UNUSED_PAD src0_sel:DWORD src1_sel:WORD_1\n" \
  "ds_read_b128 v[96:99], v67\n" \
  "s_waitcnt lgkmcnt(10)\n" \
  "v_smfmac_f32_16x16x64_bf16 v[32:35], v[180:183], v[100:107], v192\n" \
  "v_lshlrev_b32_sdwa v64, 4, v212 dst_sel:DWORD dst_unused:UNUSED_PAD src0_sel:DWORD src1_sel:WORD_0\n" \
  "ds_read_b128 v[100:103], v64\n" \
  "v_lshlrev_b32_sdwa v65, 4, v212 dst_sel:DWORD dst_unused:UNUSED_PAD src0_sel:DWORD src1_sel:WORD_1\n" \
  "ds_read_b128 v[104:107], v65\n" \
  "s_waitcnt lgkmcnt(10)\n" \
  "v_smfmac_f32_16x16x64_bf16 v[36:39], v[180:183], v[108:115], v192\n" \
  "v_lshlrev_b32_sdwa v66, 4, v213 dst_sel:DWORD dst_unused:UNUSED_PAD src0_sel:DWORD src1_sel:WORD_0\n" \
  "ds_read_b128 v[108:111], v66\n" \
  "v_lshlrev_b32_sdwa v67, 4, v213 dst_sel:DWORD dst_unused:UNUSED_PAD src0_sel:DWORD src1_sel:WORD_1\n" \
  "ds_read_b128 v[112:115], v67\n" \
  "s_waitcnt lgkmcnt(10)\n" \
  "v_smfmac_f32_16x16x64_bf16 v[0:3], v[180:183], v[68:75], v192\n" \
  "v_lshlrev_b32_sdwa v64, 4, v214 dst_sel:DWORD dst_unused:UNUSED_PAD src0_sel:DWORD src1_sel:WORD_0\n" \
  "ds_read_b128 v[68:71], v64\n" \
  "v_lshlrev_b32_sdwa v65, 4, v214 dst_sel:DWORD dst_unused:UNUSED_PAD src0_sel:DWORD src1_sel:WORD_1\n" \
  "ds_read_b128 v[72:75], v65\n" \
  "s_waitcnt lgkmcnt(10)\n" \
  "v_smfmac_f32_16x16x64_bf16 v[4:7], v[180:183], v[76:83], v192\n" \
  "v_lshlrev_b32_sdwa v66, 4, v215 dst_sel:DWORD dst_unused:UNUSED_PAD src0_sel:DWORD src1_sel:WORD_0\n" \
  "ds_read_b128 v[76:79], v66\n" \
  "v_lshlrev_b32_sdwa v67, 4, v215 dst_sel:DWORD dst_unused:UNUSED_PAD src0_sel:DWORD src1_sel:WORD_1\n" \
  "ds_read_b128 v[80:83], v67\n" \
  "v_pk_mul_f32 v[128:129], v[0:1], v[0:1]\n" \
  "v_pk_mul_f32 v[130:131], v[2:3], v[2:3]\n" \
  "v_pk_fma_f32 v[128:129], v[128:129], s[52:53], v[124:125]\n" \
  "v_pk_fma_f32 v[130:131], v[130:131], s[52:53], v[124:125]\n" \
  "v_pk_mul_f32 v[128:129], v[128:129], v[0:1]\n" \
  "v_pk_mul_f32 v[130:131], v[130:131], v[2:3]\n" \
  "v_exp_f32 v128, v128\n" \
  "v_exp_f32 v129, v129\n" \
  "v_pk_mul_f32 v[0:1], v[0:1], v[120:121]\n" \
  "v_exp_f32 v130, v130\n" \
  "v_exp_f32 v131, v131\n" \
  "v_pk_mul_f32 v[2:3], v[2:3], v[122:123]\n" \
  "v_pk_add_f32 v[128:129], v[128:129], 1.0 op_sel_hi:[1,0]\n" \
  "v_pk_add_f32 v[130:131], v[130:131], 1.0 op_sel_hi:[1,0]\n" \
  "v_rcp_f32 v128, v128\n" \
  "v_rcp_f32 v129, v129\n" \
  "s_waitcnt lgkmcnt(10)\n" \
  "v_smfmac_f32_16x16x64_bf16 v[8:11], v[180:183], v[84:91], v192\n" \
  "v_lshlrev_b32_sdwa v64, 4, v216 dst_sel:DWORD dst_unused:UNUSED_PAD src0_sel:DWORD src1_sel:WORD_0\n" \
  "ds_read_b128 v[84:87], v64\n" \
  "v_lshlrev_b32_sdwa v65, 4, v216 dst_sel:DWORD dst_unused:UNUSED_PAD src0_sel:DWORD src1_sel:WORD_1\n" \
  "ds_read_b128 v[88:91], v65\n" \
  "v_rcp_f32 v130, v130\n" \
  "v_rcp_f32 v131, v131\n" \
  "v_pk_mul_f32 v[0:1], v[0:1], v[128:129]\n" \
  "v_pk_fma_f32 v[0:1], v[2:3], v[130:131], v[0:1]\n" \
  "v_add_f32 v0, v0, v1\n" \
  "v_pk_mul_f32 v[132:133], v[4:5], v[4:5]\n" \
  "v_pk_mul_f32 v[134:135], v[6:7], v[6:7]\n" \
  "v_pk_fma_f32 v[132:133], v[132:133], s[52:53], v[124:125]\n" \
  "v_pk_fma_f32 v[134:135], v[134:135], s[52:53], v[124:125]\n" \
  "v_pk_mul_f32 v[132:133], v[132:133], v[4:5]\n" \
  "v_pk_mul_f32 v[134:135], v[134:135], v[6:7]\n" \
  "v_exp_f32 v132, v132\n" \
  "v_exp_f32 v133, v133\n" \
  "v_pk_mul_f32 v[4:5], v[4:5], v[120:121]\n" \
  "v_exp_f32 v134, v134\n" \
  "v_exp_f32 v135, v135\n" \
  "s_waitcnt lgkmcnt(10)\n" \
  "v_smfmac_f32_16x16x64_bf16 v[12:15], v[180:183], v[92:99], v192\n" \
  "v_lshlrev_b32_sdwa v66, 4, v217 dst_sel:DWORD dst_unused:UNUSED_PAD src0_sel:DWORD src1_sel:WORD_0\n" \
  "ds_read_b128 v[92:95], v66\n" \
  "v_lshlrev_b32_sdwa v67, 4, v217 dst_sel:DWORD dst_unused:UNUSED_PAD src0_sel:DWORD src1_sel:WORD_1\n" \
  "ds_read_b128 v[96:99], v67\n" \
  "v_pk_mul_f32 v[6:7], v[6:7], v[122:123]\n" \
  "v_pk_add_f32 v[132:133], v[132:133], 1.0 op_sel_hi:[1,0]\n" \
  "v_pk_add_f32 v[134:135], v[134:135], 1.0 op_sel_hi:[1,0]\n" \
  "v_rcp_f32 v132, v132\n" \
  "v_rcp_f32 v133, v133\n" \
  "v_rcp_f32 v134, v134\n" \
  "v_rcp_f32 v135, v135\n" \
  "v_pk_mul_f32 v[4:5], v[4:5], v[132:133]\n" \
  "v_pk_fma_f32 v[4:5], v[6:7], v[134:135], v[4:5]\n" \
  "v_add_f32 v4, v4, v5\n" \
  "v_pk_mul_f32 v[136:137], v[8:9], v[8:9]\n" \
  "v_pk_mul_f32 v[138:139], v[10:11], v[10:11]\n" \
  "v_pk_fma_f32 v[136:137], v[136:137], s[52:53], v[124:125]\n" \
  "v_pk_fma_f32 v[138:139], v[138:139], s[52:53], v[124:125]\n" \
  "v_pk_mul_f32 v[136:137], v[136:137], v[8:9]\n" \
  "v_pk_mul_f32 v[138:139], v[138:139], v[10:11]\n" \
  "s_waitcnt lgkmcnt(10)\n" \
  "v_smfmac_f32_16x16x64_bf16 v[16:19], v[180:183], v[100:107], v192\n" \
  "v_lshlrev_b32_sdwa v64, 4, v250 dst_sel:DWORD dst_unused:UNUSED_PAD src0_sel:DWORD src1_sel:WORD_0\n" \
  "ds_read_b128 v[100:103], v64\n" \
  "v_lshlrev_b32_sdwa v65, 4, v250 dst_sel:DWORD dst_unused:UNUSED_PAD src0_sel:DWORD src1_sel:WORD_1\n" \
  "ds_read_b128 v[104:107], v65\n" \
  "v_exp_f32 v136, v136\n" \
  "v_exp_f32 v137, v137\n" \
  "v_pk_mul_f32 v[8:9], v[8:9], v[120:121]\n" \
  "v_exp_f32 v138, v138\n" \
  "v_exp_f32 v139, v139\n" \
  "v_pk_mul_f32 v[10:11], v[10:11], v[122:123]\n" \
  "v_pk_add_f32 v[136:137], v[136:137], 1.0 op_sel_hi:[1,0]\n" \
  "v_pk_add_f32 v[138:139], v[138:139], 1.0 op_sel_hi:[1,0]\n" \
  "v_rcp_f32 v136, v136\n" \
  "v_rcp_f32 v137, v137\n" \
  "v_rcp_f32 v138, v138\n" \
  "v_rcp_f32 v139, v139\n" \
  "v_pk_mul_f32 v[8:9], v[8:9], v[136:137]\n" \
  "v_pk_fma_f32 v[8:9], v[10:11], v[138:139], v[8:9]\n" \
  "v_add_f32 v8, v8, v9\n" \
  "v_pk_mul_f32 v[140:141], v[12:13], v[12:13]\n" \
  "s_waitcnt lgkmcnt(10)\n" \
  "v_smfmac_f32_16x16x64_bf16 v[20:23], v[180:183], v[108:115], v192\n" \
  "v_lshlrev_b32_sdwa v66, 4, v218 dst_sel:DWORD dst_unused:UNUSED_PAD src0_sel:DWORD src1_sel:WORD_0\n" \
  "ds_read_b128 v[108:111], v66\n" \
  "v_lshlrev_b32_sdwa v67, 4, v218 dst_sel:DWORD dst_unused:UNUSED_PAD src0_sel:DWORD src1_sel:WORD_1\n" \
  "ds_read_b128 v[112:115], v67\n" \
  "v_pk_mul_f32 v[142:143], v[14:15], v[14:15]\n" \
  "v_pk_fma_f32 v[140:141], v[140:141], s[52:53], v[124:125]\n" \
  "v_pk_fma_f32 v[142:143], v[142:143], s[52:53], v[124:125]\n" \
  "v_pk_mul_f32 v[140:141], v[140:141], v[12:13]\n" \
  "v_pk_mul_f32 v[142:143], v[142:143], v[14:15]\n" \
  "v_exp_f32 v140, v140\n" \
  "v_exp_f32 v141, v141\n" \
  "v_pk_mul_f32 v[12:13], v[12:13], v[120:121]\n" \
  "v_exp_f32 v142, v142\n" \
  "v_exp_f32 v143, v143\n" \
  "v_pk_mul_f32 v[14:15], v[14:15], v[122:123]\n" \
  "v_pk_add_f32 v[140:141], v[140:141], 1.0 op_sel_hi:[1,0]\n" \
  "v_pk_add_f32 v[142:143], v[142:143], 1.0 op_sel_hi:[1,0]\n" \
  "v_rcp_f32 v140, v140\n" \
  "v_rcp_f32 v141, v141\n" \
  "v_rcp_f32 v142, v142\n" \
  "s_waitcnt lgkmcnt(10)\n" \
  "v_smfmac_f32_16x16x64_bf16 v[24:27], v[180:183], v[68:75], v192\n" \
  "v_lshlrev_b32_sdwa v64, 4, v251 dst_sel:DWORD dst_unused:UNUSED_PAD src0_sel:DWORD src1_sel:WORD_0\n" \
  "ds_read_b128 v[68:71], v64\n" \
  "v_lshlrev_b32_sdwa v65, 4, v251 dst_sel:DWORD dst_unused:UNUSED_PAD src0_sel:DWORD src1_sel:WORD_1\n" \
  "ds_read_b128 v[72:75], v65\n" \
  "v_rcp_f32 v143, v143\n" \
  "v_pk_mul_f32 v[12:13], v[12:13], v[140:141]\n" \
  "v_pk_fma_f32 v[12:13], v[14:15], v[142:143], v[12:13]\n" \
  "v_add_f32 v12, v12, v13\n" \
  "v_pk_mul_f32 v[128:129], v[16:17], v[16:17]\n" \
  "v_pk_mul_f32 v[130:131], v[18:19], v[18:19]\n" \
  "v_pk_fma_f32 v[128:129], v[128:129], s[52:53], v[124:125]\n" \
  "v_pk_fma_f32 v[130:131], v[130:131], s[52:53], v[124:125]\n" \
  "v_pk_mul_f32 v[128:129], v[128:129], v[16:17]\n" \
  "v_pk_mul_f32 v[130:131], v[130:131], v[18:19]\n" \
  "v_exp_f32 v128, v128\n" \
  "v_exp_f32 v129, v129\n" \
  "v_pk_mul_f32 v[16:17], v[16:17], v[120:121]\n" \
  "v_exp_f32 v130, v130\n" \
  "v_exp_f32 v131, v131\n" \
  "v_pk_mul_f32 v[18:19], v[18:19], v[122:123]\n" \
  "s_waitcnt lgkmcnt(10)\n" \
  "v_smfmac_f32_16x16x64_bf16 v[28:31], v[180:183], v[76:83], v192\n" \
  "v_lshlrev_b32_sdwa v66, 4, v219 dst_sel:DWORD dst_unused:UNUSED_PAD src0_sel:DWORD src1_sel:WORD_0\n" \
  "ds_read_b128 v[76:79], v66\n" \
  "v_lshlrev_b32_sdwa v67, 4, v219 dst_sel:DWORD dst_unused:UNUSED_PAD src0_sel:DWORD src1_sel:WORD_1\n" \
  "ds_read_b128 v[80:83], v67\n" \
  "v_pk_add_f32 v[128:129], v[128:129], 1.0 op_sel_hi:[1,0]\n" \
  "v_pk_add_f32 v[130:131], v[130:131], 1.0 op_sel_hi:[1,0]\n" \
  "v_rcp_f32 v128, v128\n" \
  "v_rcp_f32 v129, v129\n" \
  "v_rcp_f32 v130, v130\n" \
  "v_rcp_f32 v131, v131\n" \
  "v_pk_mul_f32 v[16:17], v[16:17], v[128:129]\n" \
  "v_pk_fma_f32 v[16:17], v[18:19], v[130:131], v[16:17]\n" \
  "v_add_f32 v16, v16, v17\n" \
  "v_pk_mul_f32 v[132:133], v[20:21], v[20:21]\n" \
  "v_pk_mul_f32 v[134:135], v[22:23], v[22:23]\n" \
  "v_pk_fma_f32 v[132:133], v[132:133], s[52:53], v[124:125]\n" \
  "v_pk_fma_f32 v[134:135], v[134:135], s[52:53], v[124:125]\n" \
  "v_pk_mul_f32 v[132:133], v[132:133], v[20:21]\n" \
  "v_pk_mul_f32 v[134:135], v[134:135], v[22:23]\n" \
  "v_exp_f32 v132, v132\n" \
  "s_waitcnt lgkmcnt(10)\n" \
  "v_smfmac_f32_16x16x64_bf16 v[32:35], v[180:183], v[84:91], v192\n" \
  "v_lshlrev_b32_sdwa v64, 4, v252 dst_sel:DWORD dst_unused:UNUSED_PAD src0_sel:DWORD src1_sel:WORD_0\n" \
  "ds_read_b128 v[84:87], v64\n" \
  "v_lshlrev_b32_sdwa v65, 4, v252 dst_sel:DWORD dst_unused:UNUSED_PAD src0_sel:DWORD src1_sel:WORD_1\n" \
  "ds_read_b128 v[88:91], v65\n" \
  "v_exp_f32 v133, v133\n" \
  "v_pk_mul_f32 v[20:21], v[20:21], v[120:121]\n" \
  "v_exp_f32 v134, v134\n" \
  "v_exp_f32 v135, v135\n" \
  "v_pk_mul_f32 v[22:23], v[22:23], v[122:123]\n" \
  "v_pk_add_f32 v[132:133], v[132:133], 1.0 op_sel_hi:[1,0]\n" \
  "v_pk_add_f32 v[134:135], v[134:135], 1.0 op_sel_hi:[1,0]\n" \
  "v_rcp_f32 v132, v132\n" \
  "v_rcp_f32 v133, v133\n" \
  "v_rcp_f32 v134, v134\n" \
  "v_rcp_f32 v135, v135\n" \
  "v_pk_mul_f32 v[20:21], v[20:21], v[132:133]\n" \
  "v_pk_fma_f32 v[20:21], v[22:23], v[134:135], v[20:21]\n" \
  "v_add_f32 v20, v20, v21\n" \
  "v_pk_mul_f32 v[136:137], v[24:25], v[24:25]\n" \
  "v_pk_mul_f32 v[138:139], v[26:27], v[26:27]\n" \
  "s_waitcnt lgkmcnt(10)\n" \
  "v_smfmac_f32_16x16x64_bf16 v[36:39], v[180:183], v[92:99], v192\n" \
  "v_lshlrev_b32_sdwa v66, 4, v220 dst_sel:DWORD dst_unused:UNUSED_PAD src0_sel:DWORD src1_sel:WORD_0\n" \
  "ds_read_b128 v[92:95], v66\n" \
  "v_lshlrev_b32_sdwa v67, 4, v220 dst_sel:DWORD dst_unused:UNUSED_PAD src0_sel:DWORD src1_sel:WORD_1\n" \
  "ds_read_b128 v[96:99], v67\n" \
  "v_pk_fma_f32 v[136:137], v[136:137], s[52:53], v[124:125]\n" \
  "v_pk_fma_f32 v[138:139], v[138:139], s[52:53], v[124:125]\n" \
  "v_pk_mul_f32 v[136:137], v[136:137], v[24:25]\n" \
  "v_pk_mul_f32 v[138:139], v[138:139], v[26:27]\n" \
  "v_exp_f32 v136, v136\n" \
  "v_exp_f32 v137, v137\n" \
  "v_pk_mul_f32 v[24:25], v[24:25], v[120:121]\n" \
  "v_exp_f32 v138, v138\n" \
  "v_exp_f32 v139, v139\n" \
  "v_pk_mul_f32 v[26:27], v[26:27], v[122:123]\n" \
  "v_pk_add_f32 v[136:137], v[136:137], 1.0 op_sel_hi:[1,0]\n" \
  "v_pk_add_f32 v[138:139], v[138:139], 1.0 op_sel_hi:[1,0]\n" \
  "v_rcp_f32 v136, v136\n" \
  "v_rcp_f32 v137, v137\n" \
  "v_rcp_f32 v138, v138\n" \
  "v_rcp_f32 v139, v139\n" \
  "s_waitcnt lgkmcnt(10)\n" \
  "v_smfmac_f32_16x16x64_bf16 v[40:43], v[180:183], v[100:107], v192\n" \
  "v_lshlrev_b32_sdwa v64, 4, v253 dst_sel:DWORD dst_unused:UNUSED_PAD src0_sel:DWORD src1_sel:WORD_0\n" \
  "ds_read_b128 v[100:103], v64\n" \
  "v_lshlrev_b32_sdwa v65, 4, v253 dst_sel:DWORD dst_unused:UNUSED_PAD src0_sel:DWORD src1_sel:WORD_1\n" \
  "ds_read_b128 v[104:107], v65\n" \
  "v_pk_mul_f32 v[24:25], v[24:25], v[136:137]\n" \
  "v_pk_fma_f32 v[24:25], v[26:27], v[138:139], v[24:25]\n" \
  "v_add_f32 v24, v24, v25\n" \
  "v_pk_mul_f32 v[140:141], v[28:29], v[28:29]\n" \
  "v_pk_mul_f32 v[142:143], v[30:31], v[30:31]\n" \
  "v_pk_fma_f32 v[140:141], v[140:141], s[52:53], v[124:125]\n" \
  "v_pk_fma_f32 v[142:143], v[142:143], s[52:53], v[124:125]\n" \
  "v_pk_mul_f32 v[140:141], v[140:141], v[28:29]\n" \
  "v_pk_mul_f32 v[142:143], v[142:143], v[30:31]\n" \
  "v_exp_f32 v140, v140\n" \
  "v_exp_f32 v141, v141\n" \
  "v_pk_mul_f32 v[28:29], v[28:29], v[120:121]\n" \
  "v_exp_f32 v142, v142\n" \
  "v_exp_f32 v143, v143\n" \
  "v_pk_mul_f32 v[30:31], v[30:31], v[122:123]\n" \
  "v_pk_add_f32 v[140:141], v[140:141], 1.0 op_sel_hi:[1,0]\n" \
  "s_waitcnt lgkmcnt(10)\n" \
  "v_smfmac_f32_16x16x64_bf16 v[40:43], v[180:183], v[108:115], v192\n" \
  "v_lshlrev_b32_sdwa v66, 4, v221 dst_sel:DWORD dst_unused:UNUSED_PAD src0_sel:DWORD src1_sel:WORD_0\n" \
  "ds_read_b128 v[108:111], v66\n" \
  "v_lshlrev_b32_sdwa v67, 4, v221 dst_sel:DWORD dst_unused:UNUSED_PAD src0_sel:DWORD src1_sel:WORD_1\n" \
  "ds_read_b128 v[112:115], v67\n" \
  "v_pk_add_f32 v[142:143], v[142:143], 1.0 op_sel_hi:[1,0]\n" \
  "v_rcp_f32 v140, v140\n" \
  "v_rcp_f32 v141, v141\n" \
  "v_rcp_f32 v142, v142\n" \
  "v_rcp_f32 v143, v143\n" \
  "v_pk_mul_f32 v[28:29], v[28:29], v[140:141]\n" \
  "v_pk_fma_f32 v[28:29], v[30:31], v[142:143], v[28:29]\n" \
  "v_add_f32 v28, v28, v29\n" \
  "v_permlane16_swap_b32 v0, v16\n" \
  "v_permlane16_swap_b32 v4, v20\n" \
  "v_permlane16_swap_b32 v8, v24\n" \
  "v_permlane16_swap_b32 v12, v28\n" \
  "v_add_f32 v144, v0, v16\n" \
  "v_add_f32 v145, v4, v20\n" \
  "v_add_f32 v146, v8, v24\n" \
  "v_add_f32 v147, v12, v28\n" \
  "s_waitcnt lgkmcnt(10)\n" \
  "v_smfmac_f32_16x16x64_bf16 v[44:47], v[180:183], v[68:75], v192\n" \
  "v_lshlrev_b32_sdwa v64, 4, v254 dst_sel:DWORD dst_unused:UNUSED_PAD src0_sel:DWORD src1_sel:WORD_0\n" \
  "ds_read_b128 v[68:71], v64\n" \
  "v_lshlrev_b32_sdwa v65, 4, v254 dst_sel:DWORD dst_unused:UNUSED_PAD src0_sel:DWORD src1_sel:WORD_1\n" \
  "ds_read_b128 v[72:75], v65\n" \
  "v_min_u32 v144, -2, v144\n" \
  "v_min_u32 v145, -2, v145\n" \
  "v_min_u32 v146, -2, v146\n" \
  "v_min_u32 v147, -2, v147\n" \
  "global_store_dwordx4 v188, v[144:147], s[40:41] offset:0 sc1\n" \
  "v_pk_mul_f32 v[128:129], v[32:33], v[32:33]\n" \
  "v_pk_mul_f32 v[130:131], v[34:35], v[34:35]\n" \
  "v_pk_fma_f32 v[128:129], v[128:129], s[52:53], v[124:125]\n" \
  "v_pk_fma_f32 v[130:131], v[130:131], s[52:53], v[124:125]\n" \
  "v_pk_mul_f32 v[128:129], v[128:129], v[32:33]\n" \
  "v_pk_mul_f32 v[130:131], v[130:131], v[34:35]\n" \
  "v_exp_f32 v128, v128\n" \
  "v_exp_f32 v129, v129\n" \
  "v_pk_mul_f32 v[32:33], v[32:33], v[120:121]\n" \
  "v_exp_f32 v130, v130\n" \
  "v_exp_f32 v131, v131\n" \
  "s_waitcnt lgkmcnt(10)\n" \
  "v_smfmac_f32_16x16x64_bf16 v[44:47], v[180:183], v[76:83], v192\n" \
  "v_lshlrev_b32_sdwa v66, 4, v222 dst_sel:DWORD dst_unused:UNUSED_PAD src0_sel:DWORD src1_sel:WORD_0\n" \
  "ds_read_b128 v[76:79], v66\n" \
  "v_lshlrev_b32_sdwa v67, 4, v222 dst_sel:DWORD dst_unused:UNUSED_PAD src0_sel:DWORD src1_sel:WORD_1\n" \
  "ds_read_b128 v[80:83], v67\n" \
  "v_pk_mul_f32 v[34:35], v[34:35], v[122:123]\n" \
  "v_pk_add_f32 v[128:129], v[128:129], 1.0 op_sel_hi:[1,0]\n" \
  "v_pk_add_f32 v[130:131], v[130:131], 1.0 op_sel_hi:[1,0]\n" \
  "v_rcp_f32 v128, v128\n" \
  "v_rcp_f32 v129, v129\n" \
  "v_rcp_f32 v130, v130\n" \
  "v_rcp_f32 v131, v131\n" \
  "v_pk_mul_f32 v[32:33], v[32:33], v[128:129]\n" \
  "v_pk_fma_f32 v[32:33], v[34:35], v[130:131], v[32:33]\n" \
  "v_add_f32 v32, v32, v33\n" \
  "v_pk_mul_f32 v[132:133], v[36:37], v[36:37]\n" \
  "v_pk_mul_f32 v[134:135], v[38:39], v[38:39]\n" \
  "v_pk_fma_f32 v[132:133], v[132:133], s[52:53], v[124:125]\n" \
  "v_pk_fma_f32 v[134:135], v[134:135], s[52:53], v[124:125]\n" \
  "v_pk_mul_f32 v[132:133], v[132:133], v[36:37]\n" \
  "v_pk_mul_f32 v[134:135], v[134:135], v[38:39]\n" \
  "s_waitcnt lgkmcnt(10)\n" \
  "v_smfmac_f32_16x16x64_bf16 v[48:51], v[180:183], v[84:91], v192\n" \
  "v_lshlrev_b32_sdwa v64, 4, v255 dst_sel:DWORD dst_unused:UNUSED_PAD src0_sel:DWORD src1_sel:WORD_0\n" \
  "ds_read_b128 v[84:87], v64\n" \
  "v_lshlrev_b32_sdwa v65, 4, v255 dst_sel:DWORD dst_unused:UNUSED_PAD src0_sel:DWORD src1_sel:WORD_1\n" \
  "ds_read_b128 v[88:91], v65\n" \
  "v_exp_f32 v132, v132\n" \
  "v_exp_f32 v133, v133\n" \
  "v_pk_mul_f32 v[36:37], v[36:37], v[120:121]\n" \
  "v_exp_f32 v134, v134\n" \
  "v_exp_f32 v135, v135\n" \
  "v_pk_mul_f32 v[38:39], v[38:39], v[122:123]\n" \
  "v_pk_add_f32 v[132:133], v[132:133], 1.0 op_sel_hi:[1,0]\n" \
  "v_pk_add_f32 v[134:135], v[134:135], 1.0 op_sel_hi:[1,0]\n" \
  "v_rcp_f32 v132, v132\n" \
  "v_rcp_f32 v133, v133\n" \
  "v_rcp_f32 v134, v134\n" \
  "v_rcp_f32 v135, v135\n" \
  "v_pk_mul_f32 v[36:37], v[36:37], v[132:133]\n" \
  "v_pk_fma_f32 v[36:37], v[38:39], v[134:135], v[36:37]\n" \
  "v_add_f32 v36, v36, v37\n" \
  "v_pk_mul_f32 v[136:137], v[40:41], v[40:41]\n" \
  "s_waitcnt lgkmcnt(10)\n" \
  "v_smfmac_f32_16x16x64_bf16 v[48:51], v[180:183], v[92:99], v192\n" \
  "v_lshlrev_b32_sdwa v66, 4, v223 dst_sel:DWORD dst_unused:UNUSED_PAD src0_sel:DWORD src1_sel:WORD_0\n" \
  "ds_read_b128 v[92:95], v66\n" \
  "v_lshlrev_b32_sdwa v67, 4, v223 dst_sel:DWORD dst_unused:UNUSED_PAD src0_sel:DWORD src1_sel:WORD_1\n" \
  "ds_read_b128 v[96:99], v67\n" \
  "v_pk_mul_f32 v[138:139], v[42:43], v[42:43]\n" \
  "v_pk_fma_f32 v[136:137], v[136:137], s[52:53], v[124:125]\n" \
  "v_pk_fma_f32 v[138:139], v[138:139], s[52:53], v[124:125]\n" \
  "v_pk_mul_f32 v[136:137], v[136:137], v[40:41]\n" \
  "v_pk_mul_f32 v[138:139], v[138:139], v[42:43]\n" \
  "v_exp_f32 v136, v136\n" \
  "v_exp_f32 v137, v137\n" \
  "v_pk_mul_f32 v[40:41], v[40:41], v[120:121]\n" \
  "v_exp_f32 v138, v138\n" \
  "v_exp_f32 v139, v139\n" \
  "v_pk_mul_f32 v[42:43], v[42:43], v[122:123]\n" \
  "v_pk_add_f32 v[136:137], v[136:137], 1.0 op_sel_hi:[1,0]\n" \
  "v_pk_add_f32 v[138:139], v[138:139], 1.0 op_sel_hi:[1,0]\n" \
  "v_rcp_f32 v136, v136\n" \
  "v_rcp_f32 v137, v137\n" \
  "v_rcp_f32 v138, v138\n" \
  "s_waitcnt lgkmcnt(10)\n" \
  "v_smfmac_f32_16x16x64_bf16 v[52:55], v[180:183], v[100:107], v192\n" \
  "v_rcp_f32 v139, v139\n" \
  "v_pk_mul_f32 v[40:41], v[40:41], v[136:137]\n" \
  "v_pk_fma_f32 v[40:41], v[42:43], v[138:139], v[40:41]\n" \
  "v_add_f32 v40, v40, v41\n" \
  "v_pk_mul_f32 v[140:141], v[44:45], v[44:45]\n" \
  "v_pk_mul_f32 v[142:143], v[46:47], v[46:47]\n" \
  "v_pk_fma_f32 v[140:141], v[140:141], s[52:53], v[124:125]\n" \
  "v_pk_fma_f32 v[142:143], v[142:143], s[52:53], v[124:125]\n" \
  "v_pk_mul_f32 v[140:141], v[140:141], v[44:45]\n" \
  "v_pk_mul_f32 v[142:143], v[142:143], v[46:47]\n" \
  "v_exp_f32 v140, v140\n" \
  "v_exp_f32 v141, v141\n" \
  "v_pk_mul_f32 v[44:45], v[44:45], v[120:121]\n" \
  "v_exp_f32 v142, v142\n" \
  "v_exp_f32 v143, v143\n" \
  "v_pk_mul_f32 v[46:47], v[46:47], v[122:123]\n" \
  "v_pk_add_f32 v[140:141], v[140:141], 1.0 op_sel_hi:[1,0]\n" \
  "v_pk_add_f32 v[142:143], v[142:143], 1.0 op_sel_hi:[1,0]\n" \
  "v_rcp_f32 v140, v140\n" \
  "v_rcp_f32 v141, v141\n" \
  "v_rcp_f32 v142, v142\n" \
  "v_rcp_f32 v143, v143\n" \
  "v_pk_mul_f32 v[44:45], v[44:45], v[140:141]\n" \
  "v_pk_fma_f32 v[44:45], v[46:47], v[142:143], v[44:45]\n" \
  "v_add_f32 v44, v44, v45\n" \
  "v_pk_mul_f32 v[128:129], v[48:49], v[48:49]\n" \
  "v_pk_mul_f32 v[130:131], v[50:51], v[50:51]\n" \
  "v_pk_fma_f32 v[128:129], v[128:129], s[52:53], v[124:125]\n" \
  "v_pk_fma_f32 v[130:131], v[130:131], s[52:53], v[124:125]\n" \
  "v_pk_mul_f32 v[128:129], v[128:129], v[48:49]\n" \
  "v_pk_mul_f32 v[130:131], v[130:131], v[50:51]\n" \
  "v_exp_f32 v128, v128\n" \
  "s_waitcnt lgkmcnt(8)\n" \
  "v_smfmac_f32_16x16x64_bf16 v[52:55], v[180:183], v[108:115], v192\n" \
  "v_exp_f32 v129, v129\n" \
  "v_pk_mul_f32 v[48:49], v[48:49], v[120:121]\n" \
  "v_exp_f32 v130, v130\n" \
  "v_exp_f32 v131, v131\n" \
  "v_pk_mul_f32 v[50:51], v[50:51], v[122:123]\n" \
  "v_pk_add_f32 v[128:129], v[128:129], 1.0 op_sel_hi:[1,0]\n" \
  "v_pk_add_f32 v[130:131], v[130:131], 1.0 op_sel_hi:[1,0]\n" \
  "v_rcp_f32 v128, v128\n" \
  "v_rcp_f32 v129, v129\n" \
  "v_rcp_f32 v130, v130\n" \
  "v_rcp_f32 v131, v131\n" \
  "v_pk_mul_f32 v[48:49], v[48:49], v[128:129]\n" \
  "v_pk_fma_f32 v[48:49], v[50:51], v[130:131], v[48:49]\n" \
  "v_add_f32 v48, v48, v49\n" \
  "s_waitcnt lgkmcnt(6)\n" \
  "v_smfmac_f32_16x16x64_bf16 v[56:59], v[180:183], v[68:75], v192\n" \
  "v_pk_mul_f32 v[132:133], v[52:53], v[52:53]\n" \
  "v_pk_mul_f32 v[134:135], v[54:55], v[54:55]\n" \
  "v_pk_fma_f32 v[132:133], v[132:133], s[52:53], v[124:125]\n" \
  "v_pk_fma_f32 v[134:135], v[134:135], s[52:53], v[124:125]\n" \
  "v_pk_mul_f32 v[132:133], v[132:133], v[52:53]\n" \
  "v_pk_mul_f32 v[134:135], v[134:135], v[54:55]\n" \
  "v_exp_f32 v132, v132\n" \
  "v_exp_f32 v133, v133\n" \
  "v_pk_mul_f32 v[52:53], v[52:53], v[120:121]\n" \
  "v_exp_f32 v134, v134\n" \
  "v_exp_f32 v135, v135\n" \
  "v_pk_mul_f32 v[54:55], v[54:55], v[122:123]\n" \
  "v_pk_add_f32 v[132:133], v[132:133], 1.0 op_sel_hi:[1,0]\n" \
  "v_pk_add_f32 v[134:135], v[134:135], 1.0 op_sel_hi:[1,0]\n" \
  "v_rcp_f32 v132, v132\n" \
  "v_rcp_f32 v133, v133\n" \
  "v_rcp_f32 v134, v134\n" \
  "v_rcp_f32 v135, v135\n" \
  "v_pk_mul_f32 v[52:53], v[52:53], v[132:133]\n" \
  "v_pk_fma_f32 v[52:53], v[54:55], v[134:135], v[52:53]\n" \
  "v_add_f32 v52, v52, v53\n" \
  "s_waitcnt lgkmcnt(4)\n" \
  "v_smfmac_f32_16x16x64_bf16 v[56:59], v[180:183], v[76:83], v192\n" \
  "s_waitcnt lgkmcnt(2)\n" \
  "v_smfmac_f32_16x16x64_bf16 v[60:63], v[180:183], v[84:91], v192\n" \
  "s_nop 5\n" \
  "v_pk_mul_f32 v[136:137], v[56:57], v[56:57]\n" \
  "v_pk_mul_f32 v[138:139], v[58:59], v[58:59]\n" \
  "v_pk_fma_f32 v[136:137], v[136:137], s[52:53], v[124:125]\n" \
  "v_pk_fma_f32 v[138:139], v[138:139], s[52:53], v[124:125]\n" \
  "v_pk_mul_f32 v[136:137], v[136:137], v[56:57]\n" \
  "v_pk_mul_f32 v[138:139], v[138:139], v[58:59]\n" \
  "v_exp_f32 v136, v136\n" \
  "v_exp_f32 v137, v137\n" \
  "v_pk_mul_f32 v[56:57], v[56:57], v[120:121]\n" \
  "v_exp_f32 v138, v138\n" \
  "v_exp_f32 v139, v139\n" \
  "v_pk_mul_f32 v[58:59], v[58:59], v[122:123]\n" \
  "v_pk_add_f32 v[136:137], v[136:137], 1.0 op_sel_hi:[1,0]\n" \
  "v_pk_add_f32 v[138:139], v[138:139], 1.0 op_sel_hi:[1,0]\n" \
  "v_rcp_f32 v136, v136\n" \
  "v_rcp_f32 v137, v137\n" \
  "v_rcp_f32 v138, v138\n" \
  "v_rcp_f32 v139, v139\n" \
  "v_pk_mul_f32 v[56:57], v[56:57], v[136:137]\n" \
  "v_pk_fma_f32 v[56:57], v[58:59], v[138:139], v[56:57]\n" \
  "v_add_f32 v56, v56, v57\n" \
  "s_waitcnt lgkmcnt(0)\n" \
  "v_smfmac_f32_16x16x64_bf16 v[60:63], v[180:183], v[92:99], v192\n" \
  "s_nop 7\n" \
  "v_pk_mul_f32 v[140:141], v[60:61], v[60:61]\n" \
  "v_pk_mul_f32 v[142:143], v[62:63], v[62:63]\n" \
  "v_pk_fma_f32 v[140:141], v[140:141], s[52:53], v[124:125]\n" \
  "v_pk_fma_f32 v[142:143], v[142:143], s[52:53], v[124:125]\n" \
  "v_pk_mul_f32 v[140:141], v[140:141], v[60:61]\n" \
  "v_pk_mul_f32 v[142:143], v[142:143], v[62:63]\n" \
  "v_exp_f32 v140, v140\n" \
  "v_exp_f32 v141, v141\n" \
  "v_pk_mul_f32 v[60:61], v[60:61], v[120:121]\n" \
  "v_exp_f32 v142, v142\n" \
  "v_exp_f32 v143, v143\n" \
  "v_pk_mul_f32 v[62:63], v[62:63], v[122:123]\n" \
  "v_pk_add_f32 v[140:141], v[140:141], 1.0 op_sel_hi:[1,0]\n" \
  "v_pk_add_f32 v[142:143], v[142:143], 1.0 op_sel_hi:[1,0]\n" \
  "v_rcp_f32 v140, v140\n" \
  "v_rcp_f32 v141, v141\n" \
  "v_rcp_f32 v142, v142\n" \
  "v_rcp_f32 v143, v143\n" \
  "v_pk_mul_f32 v[60:61], v[60:61], v[140:141]\n" \
  "v_pk_fma_f32 v[60:61], v[62:63], v[142:143], v[60:61]\n" \
  "v_add_f32 v60, v60, v61\n" \
  "v_permlane16_swap_b32 v32, v48\n" \
  "v_permlane16_swap_b32 v36, v52\n" \
  "v_permlane16_swap_b32 v40, v56\n" \
  "v_permlane16_swap_b32 v44, v60\n" \
  "v_add_f32 v148, v32, v48\n" \
  "v_add_f32 v149, v36, v52\n" \
  "v_add_f32 v150, v40, v56\n" \
  "v_add_f32 v151, v44, v60\n" \
  "v_min_u32 v148, -2, v148\n" \
  "v_min_u32 v149, -2, v149\n" \
  "v_min_u32 v150, -2, v150\n" \
  "v_min_u32 v151, -2, v151\n" \
  "global_store_dwordx4 v188, v[148:151], s[40:41] offset:1024 sc1\n" \
  "v_lshrrev_b32 v0, 4, v188\n" \
  "v_lshrrev_b32 v1, 4, v0\n" \
  "v_and_b32 v2, 15, v0\n" \
  "v_lshlrev_b32 v2, 4, v2\n" \
  "v_lshl_add_u32 v1, v1, 17, v2\n" \
  "s_lshl_b32 s35, s27, 19\n" \
  "s_lshl_b32 s36, s26, 8\n" \
  "s_add_u32 s35, s35, s36\n" \
  "s_add_u32 s35, s35, 105676800\n" \
  "s_add_u32 s40, s24, s35\n" \
  "s_addc_u32 s41, s25, 0\n" \
  "s_add_u32 s42, s40, 65536\n" \
  "s_addc_u32 s43, s41, 0\n" \
  "s_add_u32 s44, s24, 109871104\n" \
  "s_addc_u32 s45, s25, 0\n" \
  "s_add_u32 s46, s44, 65536\n" \
  "s_addc_u32 s47, s45, 0\n" \
  "s_lshr_b32 s35, s26, 2\n" \
  "s_lshl_b32 s35, s35, 8\n" \
  "s_and_b32 s36, s26, 1\n" \
  "s_lshl_b32 s36, s36, 7\n" \
  "s_add_u32 s35, s35, s36\n" \
  "s_and_b32 s36, s26, 2\n" \
  "s_lshl3_add_u32 s35, s36, s35\n" \
  "v_and_b32 v2, 3, v0\n" \
  "v_lshrrev_b32 v3, 2, v0\n" \
  "v_lshl_add_u32 v2, v2, 5, v3\n" \
  "v_add_u32 v2, s35, v2\n" \
  "v_lshlrev_b32 v3, 2, v2\n" \
  "s_cmp_eq_u32 s27, 0\n" \
  "s_cselect_b32 s35, -1, 0\n" \
  "v_and_b32 v3, s35, v3\n" \
  "v_mov_b32 v4, s6\n" \
  "s_mov_b32 s5, 0x10000\n" \
  "s_barrier\n" \
  "s_sleep 10\n" \
  "LF_poll:\n" \
  "global_load_dwordx4 v[8:11], v1, s[40:41] sc1\n" \
  "global_load_dwordx4 v[12:15], v1, s[42:43] sc1\n" \
  "global_load_dword v16, v3, s[44:45] sc1\n" \
  "global_load_dword v17, v3, s[46:47] sc1\n" \
  "s_waitcnt vmcnt(0)\n" \
  "v_max3_u32 v5, v8, v9, v10\n" \
  "v_max3_u32 v6, v11, v12, v13\n" \
  "v_max3_u32 v5, v5, v14, v15\n" \
  "v_max3_u32 v6, v6, v16, v17\n" \
  "v_max_u32 v5, v5, v6\n" \
  "v_cmp_eq_u32 vcc, -1, v5\n" \
  "s_sub_u32 s5, s5, 1\n" \
  "s_cbranch_scc1 LF_go\n" \
  "s_nop 2\n" \
  "s_cbranch_vccz LF_go\n" \
  "s_mov_b64 exec, vcc\n" \
  "s_sleep 2\n" \
  "s_branch LF_poll\n" \
  "LF_go:\n" \
  "s_mov_b64 exec, -1\n" \
  "v_pk_add_f32 v[8:9], v[8:9], v[12:13]\n" \
  "v_pk_add_f32 v[10:11], v[10:11], v[14:15]\n" \
  "s_nop 0\n" \
  "v_permlane16_swap_b32 v8, v9\n" \
  "v_permlane16_swap_b32 v10, v11\n" \
  "v_add_f32 v8, v8, v9\n" \
  "v_add_f32 v10, v10, v11\n" \
  "s_nop 1\n" \
  "v_permlane32_swap_b32 v8, v10\n" \
  "v_add_f32 v8, v8, v10\n" \
  "v_and_b32 v5, 15, v0\n" \
  "v_lshrrev_b32 v6, 4, v0\n" \
  "v_lshl_add_u32 v5, v5, 2, v6\n" \
  "s_lshl_b32 s35, s27, 8\n" \
  "v_lshl_add_u32 v5, v5, 2, s35\n" \
  "ds_write_b32 v5, v8\n" \
  "s_waitcnt lgkmcnt(0)\n" \
  "s_barrier\n" \
  "s_cmp_lg_u32 s27, 0\n" \
  "s_cbranch_scc1 LF_end\n" \
  "v_lshlrev_b32 v5, 2, v0\n" \
  "ds_read2st64_b32 v[8:9], v5 offset0:0 offset1:1\n" \
  "ds_read2st64_b32 v[10:11], v5 offset0:2 offset1:3\n" \
  "ds_read2st64_b32 v[12:13], v5 offset0:4 offset1:5\n" \
  "ds_read2st64_b32 v[14:15], v5 offset0:6 offset1:7\n" \
  "v_add_f32 v16, v16, v17\n" \
  "v_lshlrev_b32 v4, 16, v4\n" \
  "v_cvt_pk_bf16_f32 v16, v16, v16\n" \
  "v_lshlrev_b32 v2, 1, v2\n" \
  "s_waitcnt lgkmcnt(0)\n" \
  "v_pk_add_f32 v[8:9], v[8:9], v[10:11]\n" \
  "v_pk_add_f32 v[12:13], v[12:13], v[14:15]\n" \
  "v_pk_add_f32 v[8:9], v[8:9], v[12:13]\n" \
  "v_add_f32 v8, v8, v9\n" \
  "v_cvt_pk_bf16_f32 v8, v8, v8\n" \
  "v_lshlrev_b32 v8, 16, v8\n" \
  "v_add_f32 v8, v8, v4\n" \
  "v_cvt_pk_bf16_f32 v8, v8, v8\n" \
  "v_lshlrev_b32 v8, 16, v8\n" \
  "v_lshlrev_b32 v16, 16, v16\n" \
  "v_add_f32 v8, v8, v16\n" \
  "v_cvt_pk_bf16_f32 v8, v8, v8\n" \
  "global_store_short v2, v8, s[22:23]\n" \
  "LF_end:\n" \
  "s_branch LZ_end\n" \
  "LG_pol:\n" \
  "s_mov_b64 exec, 0xff\n" \
  "global_load_dword v200, v198, s[48:49] offset:3076 sc1\n" \
  "s_mov_b64 exec, -1\n" \
  "s_branch LG_rt0\n" \
  "LG_chk:\n" \
  "s_mov_b32 s5, 0x10000\n" \
  "LG_p:\n" \
  "s_mov_b64 exec, 0xff\n" \
  "v_cmp_le_u32 vcc, 32, v200\n" \
  "s_mov_b64 exec, -1\n" \
  "s_sub_u32 s5, s5, 1\n" \
  "s_cselect_b32 s35, 0xff, vcc_lo\n" \
  "s_cmp_eq_u32 s35, 0xff\n" \
  "s_cbranch_scc1 LG_p_x\n" \
  "s_sleep 2\n" \
  "s_mov_b64 exec, 0xff\n" \
  "global_load_dword v200, v198, s[48:49] offset:3076 sc1\n" \
  "s_mov_b64 exec, -1\n" \
  "s_waitcnt vmcnt(0)\n" \
  "s_branch LG_p\n" \
  "LG_p_x:\n" \
  "s_branch LG_ret\n" \
  "LZ_end:\n" \
  "s_waitcnt vmcnt(0) lgkmcnt(0)\n"

#define R10(p, b) p #b "0", p #b "1", p #b "2", p #b "3", p #b "4", p #b "5", p #b "6", p #b "7", p #b "8", p #b "9"
__global__ __launch_bounds__(512, 2) void k1(const int* __restrict__ idx, const unsigned short* __restrict__ table,
                                             const unsigned short* __restrict__ w_p, const unsigned short* __restrict__ w1,
                                             const unsigned short* __restrict__ b1, const unsigned short* __restrict__ w2,
                                             const unsigned short* __restrict__ b2, __hip_bfloat16* __restrict__ out, char* __restrict__ ws) {
  __shared__ __attribute__((aligned(1024))) char smem[163840];
  unsigned tid = threadIdx.x;
  asm volatile(K_ASM
               : "+{v0}"(tid)
               : "{s[8:9]}"(idx), "{s[10:11]}"(table), "{s[12:13]}"(w_p), "{s[14:15]}"(w1), "{s[16:17]}"(b1), "{s[18:19]}"(w2), "{s[20:21]}"(b2),
                 "{s[22:23]}"(out), "{s[24:25]}"(ws), "{s26}"(blockIdx.x), "s"((unsigned)(uintptr_t)smem)
               : "memory", "scc", "vcc", "s3", "s4", "s5", "s6", "s7", "s27", "s28", "s29", "s30", "s31", "s33", "s34", "s35", "s36", "s37", "s38", "s39", R10("s", 4), R10("s", 5), R10("s", 6), R10("s", 7), R10("s", 8), R10("s", 9),
                 "v1", "v2", "v3", "v4", "v5", "v6", "v7", "v8", "v9", R10("v", 1), R10("v", 2), R10("v", 3), R10("v", 4), R10("v", 5), R10("v", 6), R10("v", 7), R10("v", 8), R10("v", 9),
                 R10("v", 10), R10("v", 11), R10("v", 12), R10("v", 13), R10("v", 14), R10("v", 15), R10("v", 16), R10("v", 17), R10("v", 18), R10("v", 19),
                 R10("v", 20), R10("v", 21), R10("v", 22), R10("v", 23), R10("v", 24), "v250", "v251", "v252", "v253", "v254", "v255");
}


__global__ __launch_bounds__(512) void k_first(char* ws) {
  const unsigned t = (unsigned)__builtin_amdgcn_s_memrealtime();


  if (threadIdx.x == 0 && blockIdx.x == 0) ((unsigned*)(ws + (250u << 20)))[0] = t;
}
__global__ __launch_bounds__(64) void k_spin(const char* __restrict__ ws) {
  const unsigned ms0 = ((const unsigned*)(ws + (250u << 20)))[0];
  for (int i = 0; i < 2000000 && (int)((unsigned)__builtin_amdgcn_s_memrealtime() - ms0) < 15000; ++i) __builtin_amdgcn_s_sleep(1);
}
extern "C" void kernel_launch(void* const* d_in, const int* in_sizes, int n_in, void* d_out, int out_size, void* d_ws,
                              size_t ws_size, hipStream_t stream) {
  k_first<<<dim3(256), dim3(512), 0, stream>>>((char*)d_ws);
  k1<<<dim3(256), dim3(512), 0, stream>>>((const int*)d_in[0], (const unsigned short*)d_in[1], (const unsigned short*)d_in[2], (const unsigned short*)d_in[3],
                                          (const unsigned short*)d_in[4], (const unsigned short*)d_in[5], (const unsigned short*)d_in[6], (__hip_bfloat16*)d_out, (char*)d_ws);
  k_spin<<<dim3(1), dim3(64), 0, stream>>>((const char*)d_ws);
}
```

```cpp
#include <hip/hip_runtime.h>
#include <hip/hip_bf16.h>
#include <stdint.h>

#define K_ASM \
  "LZ_first:\n" \
  "s_load_dwordx16 s[8:23], s[0:1], 0x0\n" \
  "s_load_dwordx2 s[24:25], s[0:1], 0x40\n" \
  "v_readfirstlane_b32 s27, v0\n" \
  "v_and_b32 v255, 63, v0\n" \
  "s_and_b32 s29, s26, 7\n" \
  "s_lshr_b32 s30, s26, 3\n" \
  "s_lshr_b32 s27, s27, 6\n" \
  "s_and_b32 s31, s30, 1\n" \
  "s_and_b32 s28, s27, 3\n" \
  "s_lshr_b32 s35, s30, 1\n" \
  "s_lshl3_add_u32 s7, s35, s29\n" \
  "v_lshrrev_b32 v1, 3, v255\n" \
  "v_and_b32 v2, 7, v255\n" \
  "v_lshlrev_b32 v1, 8, v1\n" \
  "v_lshl_or_b32 v254, v2, 4, v1\n" \
  "s_mul_i32 s36, s7, 37\n" \
  "s_and_b32 s36, s36, 63\n" \
  "s_add_u32 s36, s36, s28\n" \
  "s_cmp_ge_u32 s27, 4\n" \
  "s_cbranch_scc1 LB_start\n" \
  "s_setprio 3\n" \
  "v_and_b32 v252, 15, v255\n" \
  "v_lshrrev_b32 v253, 4, v255\n" \
  "v_mul_u32_u24 v2, 400, v253\n" \
  "v_min_u32 v3, 8, v252\n" \
  "v_lshl_add_u32 v250, v252, 4, v2\n" \
  "v_lshl_add_u32 v251, v3, 4, v2\n" \
  "s_lshl2_add_u32 s36, s7, s28\n" \
  "s_mul_i32 s36, s36, 12800\n" \
  "s_lshl_b32 s35, s28, 10\n" \
  "v_lshl_add_u32 v1, v255, 4, s35\n" \
  "s_add_u32 m0, s35, 138240\n" \
  "s_waitcnt lgkmcnt(0)\n" \
  "s_add_u32 s44, s8, s36\n" \
  "s_addc_u32 s45, s9, 0\n" \
  "global_load_lds_dwordx4 v1, s[12:13]\n" \
  "s_add_u32 m0, m0, 0x1000\n" \
  "v_add_u32 v1, 0x1000, v1\n" \
  "global_load_lds_dwordx4 v1, s[12:13]\n" \
  "s_add_u32 m0, m0, 0x1000\n" \
  "v_add_u32 v1, 0x1000, v1\n" \
  "global_load_lds_dwordx4 v1, s[12:13]\n" \
  "s_add_u32 m0, m0, 0x1000\n" \
  "v_add_u32 v1, 0x1000, v1\n" \
  "global_load_lds_dwordx4 v1, s[12:13]\n" \
  "s_add_u32 m0, m0, 0x1000\n" \
  "v_add_u32 v1, 0x1000, v1\n" \
  "v_min_u32 v1, 19984, v1\n" \
  "global_load_lds_dwordx4 v1, s[12:13]\n" \
  "global_load_dwordx4 v[232:235], v250, s[44:45] offset:0\n" \
  "global_load_dwordx4 v[236:239], v251, s[44:45] offset:256\n" \
  "global_load_dwordx4 v[240:243], v250, s[44:45] offset:1600\n" \
  "global_load_dwordx4 v[244:247], v251, s[44:45] offset:1856\n" \
  "s_setprio 1\n" \
  "v_lshrrev_b32 v1, 2, v252\n" \
  "v_and_b32 v2, 3, v252\n" \
  "v_cmp_eq_u32 vcc, v1, v253\n" \
  "v_mov_b32 v4, 0x3f80\n" \
  "v_and_b32 v13, 1, v253\n" \
  "v_lshlrev_b32 v14, 1, v13\n" \
  "v_xor_b32 v2, v2, v14\n" \
  "v_cndmask_b32 v2, 4, v2, vcc\n" \
  "v_cmp_eq_u32 vcc, 0, v2\n" \
  "s_nop 1\n" \
  "v_cndmask_b32 v216, 0, v4, vcc\n" \
  "v_lshlrev_b32 v220, 16, v216\n" \
  "v_cmp_eq_u32 vcc, 1, v2\n" \
  "s_nop 1\n" \
  "v_cndmask_b32 v217, 0, v4, vcc\n" \
  "v_lshlrev_b32 v221, 16, v217\n" \
  "v_cmp_eq_u32 vcc, 2, v2\n" \
  "s_nop 1\n" \
  "v_cndmask_b32 v218, 0, v4, vcc\n" \
  "v_lshlrev_b32 v222, 16, v218\n" \
  "v_cmp_eq_u32 vcc, 3, v2\n" \
  "s_nop 1\n" \
  "v_cndmask_b32 v219, 0, v4, vcc\n" \
  "v_lshlrev_b32 v223, 16, v219\n" \
  "v_and_b32 v1, 2, v252\n" \
  "v_lshlrev_b32 v1, 3, v1\n" \
  "v_lshlrev_b32 v1, v1, v4\n" \
  "v_and_b32 v2, 0x31, v255\n" \
  "v_mov_b64 v[224:225], 0\n" \
  "v_cmp_eq_u32 vcc, 0, v2\n" \
  "s_nop 1\n" \
  "v_cndmask_b32 v226, 0, v1, vcc\n" \
  "v_cmp_eq_u32 vcc, 1, v2\n" \
  "s_nop 1\n" \
  "v_cndmask_b32 v227, 0, v1, vcc\n" \
  "v_mul_u32_u24 v5, 400, v252\n" \
  "v_add_u32 v5, 138240, v5\n" \
  "v_lshl_add_u32 v6, v253, 4, v5\n" \
  "v_add_u32 v7, 6400, v5\n" \
  "v_add_u32 v8, 6400, v6\n" \
  "v_add_u32 v9, 12800, v5\n" \
  "v_add_u32 v10, 12800, v6\n" \
  "v_bfe_u32 v1, v252, 1, 1\n" \
  "v_and_b32 v2, 1, v252\n" \
  "v_lshl_add_u32 v1, v2, 1, v1\n" \
  "v_mul_u32_u24 v1, 200, v1\n" \
  "v_add_u32 v11, 157440, v1\n" \
  "v_lshl_add_u32 v12, v253, 4, v11\n" \
  "v_lshlrev_b32 v13, 3, v13\n" \
  "v_sub_u32 v16, v5, v13\n" \
  "v_add_u32 v5, v5, v13\n" \
  "v_sub_u32 v17, v6, v13\n" \
  "v_add_u32 v6, v6, v13\n" \
  "v_sub_u32 v18, v7, v13\n" \
  "v_add_u32 v7, v7, v13\n" \
  "v_sub_u32 v19, v8, v13\n" \
  "v_add_u32 v8, v8, v13\n" \
  "v_sub_u32 v20, v9, v13\n" \
  "v_add_u32 v9, v9, v13\n" \
  "v_sub_u32 v21, v10, v13\n" \
  "v_add_u32 v10, v10, v13\n" \
  "v_sub_u32 v22, v11, v13\n" \
  "v_add_u32 v11, v11, v13\n" \
  "v_sub_u32 v23, v12, v13\n" \
  "v_add_u32 v12, v12, v13\n" \
  "v_lshlrev_b32 v188, 4, v253\n" \
  "v_and_b32 v96, 4, v252\n" \
  "v_lshl_add_u32 v96, v253, 5, v96\n" \
  "v_mov_b32 v191, 0x40000000\n" \
  "v_cmp_gt_u32 vcc, 8, v252\n" \
  "v_mov_b32 v193, 0x4444\n" \
  "v_mov_b32 v98, 0xeeee\n" \
  "v_cndmask_b32 v189, v191, v96, vcc\n" \
  "v_cndmask_b32 v190, v96, v191, vcc\n" \
  "v_and_b32 v99, 4, v252\n" \
  "v_cmp_ne_u32 vcc, 0, v99\n" \
  "s_lshl_b32 s35, s28, 11\n" \
  "s_add_u32 s35, s35, 130048\n" \
  "v_cndmask_b32 v193, v193, v98, vcc\n" \
  "v_lshl_add_u32 v194, v252, 4, s35\n" \
  "v_and_b32 v96, 7, v252\n" \
  "v_lshlrev_b32 v96, 5, v96\n" \
  "v_lshl_add_u32 v96, v253, 8, v96\n" \
  "v_lshrrev_b32 v97, 3, v252\n" \
  "v_lshl_add_u32 v96, v97, 2, v96\n" \
  "v_add_u32 v195, s35, v96\n" \
  "v_and_b32 v97, v97, v252\n" \
  "v_lshlrev_b32 v192, 4, v97\n" \
  "s_waitcnt vmcnt(4)\n" \
  "s_barrier\n" \
  "ds_read_b64 v[200:201], v12 offset:128\n" \
  "ds_read_b64 v[202:203], v23 offset:136\n" \
  "ds_read_b64 v[184:185], v9 offset:184\n" \
  "ds_read_b64 v[186:187], v20 offset:192\n" \
  "ds_read_b64 v[212:213], v9 offset:384\n" \
  "ds_read_b64 v[214:215], v20 offset:392\n" \
  "ds_read_b64 v[196:197], v12 offset:64\n" \
  "ds_read_b64 v[198:199], v23 offset:72\n" \
  "ds_read_b64 v[176:177], v7 offset:184\n" \
  "ds_read_b64 v[178:179], v18 offset:192\n" \
  "ds_read_b64 v[180:181], v7 offset:384\n" \
  "ds_read_b64 v[182:183], v18 offset:392\n" \
  "ds_read_b64 v[228:229], v12 offset:0\n" \
  "ds_read_b64 v[230:231], v23 offset:8\n" \
  "s_waitcnt lgkmcnt(12)\n" \
  "v_mfma_f32_16x16x32_bf16 v[44:47], v[200:203], v[216:219], 0\n" \
  "ds_read_b64 v[168:169], v5 offset:184\n" \
  "ds_read_b64 v[170:171], v16 offset:192\n" \
  "s_waitcnt lgkmcnt(12)\n" \
  "v_mfma_f32_16x16x32_bf16 v[44:47], v[224:227], v[184:187], v[44:47]\n" \
  "ds_read_b64 v[172:173], v5 offset:384\n" \
  "ds_read_b64 v[174:175], v16 offset:392\n" \
  "v_mfma_f32_16x16x32_bf16 v[208:211], v[200:203], v[220:223], 0\n" \
  "ds_read_b64 v[96:97], v6 offset:0\n" \
  "ds_read_b64 v[98:99], v17 offset:8\n" \
  "s_waitcnt lgkmcnt(14)\n" \
  "v_mfma_f32_16x16x32_bf16 v[208:211], v[224:227], v[212:215], v[208:211]\n" \
  "ds_read_b64 v[124:125], v8 offset:64\n" \
  "ds_read_b64 v[126:127], v19 offset:72\n" \
  "s_waitcnt lgkmcnt(14)\n" \
  "v_mfma_f32_16x16x32_bf16 v[60:63], v[196:199], v[216:219], 0\n" \
  "ds_read_b64 v[136:137], v8 offset:264\n" \
  "ds_read_b64 v[138:139], v19 offset:272\n" \
  "s_waitcnt lgkmcnt(14)\n" \
  "v_mfma_f32_16x16x32_bf16 v[60:63], v[224:227], v[176:179], v[60:63]\n" \
  "ds_read_b64 v[140:141], v8 offset:328\n" \
  "ds_read_b64 v[142:143], v19 offset:336\n" \
  "v_mfma_f32_16x16x32_bf16 v[184:187], v[196:199], v[220:223], 0\n" \
  "ds_read_b64 v[160:161], v10 offset:264\n" \
  "ds_read_b64 v[162:163], v21 offset:272\n" \
  "s_waitcnt lgkmcnt(15)\n" \
  "v_mfma_f32_16x16x32_bf16 v[184:187], v[224:227], v[180:183], v[184:187]\n" \
  "ds_read_b64 v[116:117], v6 offset:328\n" \
  "ds_read_b64 v[118:119], v17 offset:336\n" \
  "s_waitcnt lgkmcnt(15)\n" \
  "v_mfma_f32_16x16x32_bf16 v[68:71], v[228:231], v[216:219], 0\n" \
  "ds_read_b64 v[144:145], v10 offset:0\n" \
  "ds_read_b64 v[146:147], v21 offset:8\n" \
  "s_waitcnt lgkmcnt(15)\n" \
  "v_mfma_f32_16x16x32_bf16 v[68:71], v[224:227], v[168:171], v[68:71]\n" \
  "ds_read_b64 v[148:149], v10 offset:64\n" \
  "ds_read_b64 v[150:151], v21 offset:72\n" \
  "v_mfma_f32_16x16x32_bf16 v[212:215], v[228:231], v[220:223], 0\n" \
  "ds_read_b64 v[104:105], v6 offset:128\n" \
  "ds_read_b64 v[106:107], v17 offset:136\n" \
  "s_waitcnt lgkmcnt(15)\n" \
  "v_mfma_f32_16x16x32_bf16 v[212:215], v[224:227], v[172:175], v[212:215]\n" \
  "ds_read_b64 v[100:101], v6 offset:64\n" \
  "ds_read_b64 v[102:103], v17 offset:72\n" \
  "v_mfma_f32_16x16x32_bf16 v[92:95], v[96:99], v[216:219], 0\n" \
  "ds_read_b64 v[120:121], v8 offset:0\n" \
  "ds_read_b64 v[122:123], v19 offset:8\n" \
  "s_waitcnt lgkmcnt(15)\n" \
  "v_mfma_f32_16x16x32_bf16 v[24:27], v[124:127], v[220:223], 0\n" \
  "ds_read_b64 v[112:113], v6 offset:264\n" \
  "ds_read_b64 v[114:115], v17 offset:272\n" \
  "v_mfma_f32_16x16x32_bf16 v[24:27], v[216:219], v[136:139], v[24:27]\n" \
  "ds_read_b64 v[156:157], v10 offset:200\n" \
  "ds_read_b64 v[158:159], v21 offset:208\n" \
  "s_waitcnt lgkmcnt(15)\n" \
  "v_mfma_f32_16x16x32_bf16 v[28:31], v[140:143], v[220:223], 0\n" \
  "ds_read_b64 v[132:133], v8 offset:200\n" \
  "ds_read_b64 v[134:135], v19 offset:208\n" \
  "v_mfma_f32_16x16x32_bf16 v[28:31], v[220:223], v[160:163], v[28:31]\n" \
  "ds_read_b64 v[108:109], v6 offset:200\n" \
  "ds_read_b64 v[110:111], v17 offset:208\n" \
  "s_waitcnt lgkmcnt(15)\n" \
  "v_mfma_f32_16x16x32_bf16 v[32:35], v[116:119], v[216:219], 0\n" \
  "ds_read_b64 v[152:153], v10 offset:128\n" \
  "ds_read_b64 v[154:155], v21 offset:136\n" \
  "v_mfma_f32_16x16x32_bf16 v[32:35], v[220:223], v[144:147], v[32:35]\n" \
  "ds_read_b64 v[164:165], v10 offset:328\n" \
  "ds_read_b64 v[166:167], v21 offset:336\n" \
  "v_mfma_f32_16x16x32_bf16 v[36:39], v[140:143], v[216:219], 0\n" \
  "ds_read_b64 v[128:129], v8 offset:128\n" \
  "ds_read_b64 v[130:131], v19 offset:136\n" \
  "s_waitcnt lgkmcnt(15)\n" \
  "v_mfma_f32_16x16x32_bf16 v[36:39], v[220:223], v[148:151], v[36:39]\n" \
  "ds_read_b64 v[204:205], v11 offset:184\n" \
  "ds_read_b64 v[206:207], v22 offset:192\n" \
  "v_mfma_f32_16x16x32_bf16 v[40:43], v[104:107], v[216:219], 0\n" \
  "v_mfma_f32_16x16x32_bf16 v[40:43], v[216:219], v[144:147], v[40:43]\n" \
  "v_mfma_f32_16x16x32_bf16 v[48:51], v[100:103], v[216:219], 0\n" \
  "s_waitcnt lgkmcnt(15)\n" \
  "v_mfma_f32_16x16x32_bf16 v[48:51], v[216:219], v[120:123], v[48:51]\n" \
  "s_waitcnt lgkmcnt(14)\n" \
  "v_mfma_f32_16x16x32_bf16 v[52:55], v[112:115], v[216:219], 0\n" \
  "v_mfma_f32_16x16x32_bf16 v[52:55], v[220:223], v[120:123], v[52:55]\n" \
  "v_mfma_f32_16x16x32_bf16 v[56:59], v[116:119], v[220:223], 0\n" \
  "s_waitcnt lgkmcnt(12)\n" \
  "v_mfma_f32_16x16x32_bf16 v[56:59], v[220:223], v[156:159], v[56:59]\n" \
  "s_waitcnt vmcnt(0) lgkmcnt(0)\n" \
  "v_mfma_f32_16x16x32_bf16 v[64:67], v[104:107], v[220:223], 0\n" \
  "v_cvt_pk_u16_u32 v232, v232, v233\n" \
  "v_cvt_pk_u16_u32 v234, v234, v235\n" \
  "v_mfma_f32_16x16x32_bf16 v[64:67], v[216:219], v[156:159], v[64:67]\n" \
  "v_cvt_pk_u16_u32 v236, v236, v237\n" \
  "v_cvt_pk_u16_u32 v238, v238, v239\n" \
  "v_mfma_f32_16x16x32_bf16 v[72:75], v[136:139], v[220:223], 0\n" \
  "v_pk_lshrrev_b16 v233, 3, v232 op_sel_hi:[0,1]\n" \
  "v_pk_lshrrev_b16 v235, 3, v234 op_sel_hi:[0,1]\n" \
  "v_mfma_f32_16x16x32_bf16 v[76:79], v[112:115], v[220:223], 0\n" \
  "v_pk_lshrrev_b16 v237, 3, v236 op_sel_hi:[0,1]\n" \
  "v_pk_lshrrev_b16 v239, 3, v238 op_sel_hi:[0,1]\n" \
  "v_mfma_f32_16x16x32_bf16 v[76:79], v[220:223], v[132:135], v[76:79]\n" \
  "v_pk_mad_u16 v196, v232, 8, v233 op_sel_hi:[1,0,1]\n" \
  "v_pk_mad_u16 v197, v234, 8, v235 op_sel_hi:[1,0,1]\n" \
  "v_mfma_f32_16x16x32_bf16 v[80:83], v[100:103], v[220:223], 0\n" \
  "v_pk_mad_u16 v198, v236, 8, v237 op_sel_hi:[1,0,1]\n" \
  "v_pk_mad_u16 v199, v238, 8, v239 op_sel_hi:[1,0,1]\n" \
  "v_mfma_f32_16x16x32_bf16 v[80:83], v[216:219], v[132:135], v[80:83]\n" \
  "v_alignbit_b32 v198, v198, v198, v192\n" \
  "v_alignbit_b32 v199, v199, v199, v192\n" \
  "v_mfma_f32_16x16x32_bf16 v[84:87], v[96:99], v[220:223], 0\n" \
  "v_cvt_pk_u16_u32 v240, v240, v241\n" \
  "v_cvt_pk_u16_u32 v242, v242, v243\n" \
  "v_mfma_f32_16x16x32_bf16 v[84:87], v[216:219], v[108:111], v[84:87]\n" \
  "v_cvt_pk_u16_u32 v244, v244, v245\n" \
  "v_cvt_pk_u16_u32 v246, v246, v247\n" \
  "v_mfma_f32_16x16x32_bf16 v[88:91], v[108:111], v[220:223], 0\n" \
  "v_pk_lshrrev_b16 v241, 3, v240 op_sel_hi:[0,1]\n" \
  "v_pk_lshrrev_b16 v243, 3, v242 op_sel_hi:[0,1]\n" \
  "v_mfma_f32_16x16x32_bf16 v[0:3], v[124:127], v[216:219], 0\n" \
  "v_pk_lshrrev_b16 v245, 3, v244 op_sel_hi:[0,1]\n" \
  "v_pk_lshrrev_b16 v247, 3, v246 op_sel_hi:[0,1]\n" \
  "v_mfma_f32_16x16x32_bf16 v[4:7], v[152:155], v[216:219], 0\n" \
  "v_pk_mad_u16 v180, v240, 8, v241 op_sel_hi:[1,0,1]\n" \
  "v_pk_mad_u16 v181, v242, 8, v243 op_sel_hi:[1,0,1]\n" \
  "v_mfma_f32_16x16x32_bf16 v[8:11], v[152:155], v[220:223], 0\n" \
  "v_pk_mad_u16 v182, v244, 8, v245 op_sel_hi:[1,0,1]\n" \
  "v_pk_mad_u16 v183, v246, 8, v247 op_sel_hi:[1,0,1]\n" \
  "v_mfma_f32_16x16x32_bf16 v[8:11], v[216:219], v[164:167], v[8:11]\n" \
  "v_alignbit_b32 v182, v182, v182, v192\n" \
  "v_alignbit_b32 v183, v183, v183, v192\n" \
  "v_mfma_f32_16x16x32_bf16 v[12:15], v[164:167], v[220:223], 0\n" \
  "v_add_u32 v200, 0x400, v195\n" \
  "v_mfma_f32_16x16x32_bf16 v[16:19], v[128:131], v[220:223], 0\n" \
  "v_mfma_f32_16x16x32_bf16 v[16:19], v[216:219], v[160:163], v[16:19]\n" \
  "v_mfma_f32_16x16x32_bf16 v[20:23], v[128:131], v[216:219], 0\n" \
  "v_mfma_f32_16x16x32_bf16 v[20:23], v[216:219], v[148:151], v[20:23]\n" \
  "v_mfma_f32_16x16x32_bf16 v[168:171], v[216:219], v[96:99], 0\n" \
  "v_mfma_f32_16x16x32_bf16 v[172:175], v[204:207], v[224:227], 0\n" \
  "v_mfma_f32_16x16x32_bf16 v[176:179], v[224:227], v[204:207], 0\n" \
  "s_mov_b32 exec_lo, 0x0\n" \
  "s_mov_b32 exec_hi, 0xffffffff\n" \
  "v_mov_b64 v[44:45], v[208:209]\n" \
  "v_mov_b64 v[46:47], v[210:211]\n" \
  "v_mov_b64 v[60:61], v[184:185]\n" \
  "v_mov_b64 v[62:63], v[186:187]\n" \
  "v_mov_b64 v[68:69], v[212:213]\n" \
  "v_mov_b64 v[70:71], v[214:215]\n" \
  "s_mov_b32 exec_lo, 0xff00ff00\n" \
  "s_mov_b32 exec_hi, 0x0\n" \
  "v_add_f32 v92, v92, v168\n" \
  "v_add_f32 v93, v93, v169\n" \
  "v_add_f32 v94, v94, v170\n" \
  "v_add_f32 v95, v95, v171\n" \
  "s_mov_b32 exec_lo, 0xf0000f00\n" \
  "s_mov_b32 exec_hi, 0x0\n" \
  "v_sub_f32 v92, v92, v176\n" \
  "v_sub_f32 v93, v93, v177\n" \
  "v_sub_f32 v94, v94, v178\n" \
  "v_sub_f32 v95, v95, v179\n" \
  "s_mov_b32 exec_lo, 0x0\n" \
  "s_mov_b32 exec_hi, 0xff00ff\n" \
  "v_mov_b64 v[92:93], 0\n" \
  "v_mov_b64 v[94:95], 0\n" \
  "s_mov_b32 exec_lo, 0x0\n" \
  "s_mov_b32 exec_hi, 0xf0000f\n" \
  "v_mov_b64 v[92:93], v[172:173]\n" \
  "v_mov_b64 v[94:95], v[174:175]\n" \
  "s_mov_b64 exec, -1\n" \
  "v_add_u32 v96, 6400, v250\n" \
  "v_add_u32 v97, 6400, v251\n" \
  "v_mov_b32 v98, 163328\n" \
  "v_mov_b32 v99, 1\n" \
  "v_lshlrev_b32 v203, 4, v253\n" \
  "s_lshr_b32 s35, s30, 4\n" \
  "s_lshl1_add_u32 s35, s29, s35\n" \
  "s_lshl_b32 s35, s35, 6\n" \
  "s_bfe_u32 s37, s30, 0x20002\n" \
  "s_lshl3_add_u32 s35, s37, s35\n" \
  "v_and_b32 v203, 16, v203\n" \
  "v_lshl_add_u32 v203, v203, 1, s35\n" \
  "s_mov_b64 exec, 1\n" \
  "ds_add_u32 v98, v99 offset:16\n" \
  "s_mov_b64 exec, -1\n" \
  "ds_write2_b32 v195, v196, v197 offset0:0 offset1:4\n" \
  "ds_write2_b32 v195, v198, v199 offset0:2 offset1:6\n" \
  "ds_write2_b32 v200, v180, v181 offset0:0 offset1:4\n" \
  "ds_write2_b32 v200, v182, v183 offset0:2 offset1:6\n" \
  "global_load_dwordx2 v[204:205], v203, s[16:17]\n" \
  "global_load_dwordx2 v[206:207], v203, s[18:19]\n" \
  "v_mov_b32 v203, 0\n" \
  "global_load_ushort v203, v203, s[20:21]\n" \
  "s_add_u32 s46, s44, 4800\n" \
  "s_addc_u32 s47, s45, 0\n" \
  "global_load_dwordx4 v[208:211], v96, s[44:45] offset:-3200\n" \
  "global_load_dwordx4 v[212:215], v97, s[44:45] offset:-2944\n" \
  "global_load_dwordx4 v[216:219], v96, s[44:45] offset:-1600\n" \
  "global_load_dwordx4 v[220:223], v97, s[44:45] offset:-1344\n" \
  "global_load_dwordx4 v[224:227], v96, s[44:45] offset:0\n" \
  "global_load_dwordx4 v[228:231], v97, s[44:45] offset:256\n" \
  "global_load_dwordx4 v[232:235], v96, s[44:45] offset:1600\n" \
  "global_load_dwordx4 v[236:239], v97, s[44:45] offset:1856\n" \
  "global_load_dwordx4 v[240:243], v96, s[44:45] offset:3200\n" \
  "global_load_dwordx4 v[244:247], v97, s[44:45] offset:3456\n" \
  "global_load_dwordx4 v[248:251], v96, s[46:47] offset:0\n" \
  "global_load_dwordx4 v[252:255], v97, s[46:47] offset:256\n" \
  "ds_read_b128 v[180:183], v194\n" \
  "v_add_u32 v194, 0x100, v194\n" \
  "L1_:\n" \
  "ds_read_b32 v99, v98 offset:12\n" \
  "s_waitcnt lgkmcnt(0)\n" \
  "v_readfirstlane_b32 s35, v99\n" \
  "s_cmp_lt_u32 s35, 4\n" \
  "s_cbranch_scc1 L1_\n" \
  "s_setprio 1\n" \
  "v_mad_u32_u16 v184, v182, 16, v188\n" \
  "ds_read_b128 v[128:131], v184 offset:0\n" \
  "ds_read_b128 v[132:135], v184 offset:64\n" \
  "ds_read_b128 v[172:175], v191\n" \
  "v_mad_u32_u16 v185, v181, 16, v188\n" \
  "v_mad_u32_u16 v186, v182, 16, v188 op_sel:[1,0,0,0]\n" \
  "ds_read_b128 v[112:115], v185 offset:0\n" \
  "ds_read_b128 v[116:119], v185 offset:64\n" \
  "ds_read_b128 v[136:139], v186 offset:0\n" \
  "ds_read_b128 v[140:143], v186 offset:64\n" \
  "ds_read_b128 v[168:171], v191\n" \
  "v_mad_u32_u16 v187, v181, 16, v188 op_sel:[1,0,0,0]\n" \
  "v_mad_u32_u16 v184, v180, 16, v188 op_sel:[1,0,0,0]\n" \
  "ds_read_b128 v[120:123], v187 offset:0\n" \
  "v_mad_u32_u16 v185, v180, 16, v188\n" \
  "ds_read_b128 v[124:127], v187 offset:64\n" \
  "ds_read_b128 v[104:107], v184 offset:0\n" \
  "v_mad_u32_u16 v186, v183, 16, v189\n" \
  "ds_read_b128 v[108:111], v184 offset:64\n" \
  "ds_read_b128 v[96:99], v185 offset:0\n" \
  "s_waitcnt lgkmcnt(9)\n" \
  "s_mov_b32 s51, 3\n" \
  "s_call_b64 s[52:53], LA_loop\n" \
  "LA_ev0:\n" \
  "v_mov_b32 v196, v178\n" \
  "s_waitcnt vmcnt(10)\n" \
  "v_cvt_pk_u16_u32 v208, v208, v209\n" \
  "v_cvt_pk_u16_u32 v210, v210, v211\n" \
  "v_cvt_pk_u16_u32 v212, v212, v213\n" \
  "v_cvt_pk_u16_u32 v214, v214, v215\n" \
  "v_pk_lshrrev_b16 v209, 3, v208 op_sel_hi:[0,1]\n" \
  "v_pk_lshrrev_b16 v211, 3, v210 op_sel_hi:[0,1]\n" \
  "v_pk_lshrrev_b16 v213, 3, v212 op_sel_hi:[0,1]\n" \
  "v_pk_lshrrev_b16 v215, 3, v214 op_sel_hi:[0,1]\n" \
  "v_pk_mad_u16 v208, v208, 8, v209 op_sel_hi:[1,0,1]\n" \
  "v_pk_mad_u16 v210, v210, 8, v211 op_sel_hi:[1,0,1]\n" \
  "v_pk_mad_u16 v212, v212, 8, v213 op_sel_hi:[1,0,1]\n" \
  "v_pk_mad_u16 v214, v214, 8, v215 op_sel_hi:[1,0,1]\n" \
  "v_alignbit_b32 v212, v212, v212, v192\n" \
  "v_alignbit_b32 v214, v214, v214, v192\n" \
  "ds_write2_b32 v195, v208, v210 offset0:0 offset1:4\n" \
  "ds_write2_b32 v195, v212, v214 offset0:2 offset1:6\n" \
  "v_readfirstlane_b32 s6, v203\n" \
  "s_mov_b32 s51, 4\n" \
  "s_call_b64 s[52:53], LA_loop\n" \
  "LA_ev1:\n" \
  "v_mov_b32 v197, v178\n" \
  "s_waitcnt vmcnt(8)\n" \
  "v_cvt_pk_u16_u32 v216, v216, v217\n" \
  "v_cvt_pk_u16_u32 v218, v218, v219\n" \
  "v_cvt_pk_u16_u32 v220, v220, v221\n" \
  "v_cvt_pk_u16_u32 v222, v222, v223\n" \
  "v_pk_lshrrev_b16 v217, 3, v216 op_sel_hi:[0,1]\n" \
  "v_pk_lshrrev_b16 v219, 3, v218 op_sel_hi:[0,1]\n" \
  "v_pk_lshrrev_b16 v221, 3, v220 op_sel_hi:[0,1]\n" \
  "v_pk_lshrrev_b16 v223, 3, v222 op_sel_hi:[0,1]\n" \
  "v_pk_mad_u16 v216, v216, 8, v217 op_sel_hi:[1,0,1]\n" \
  "v_pk_mad_u16 v218, v218, 8, v219 op_sel_hi:[1,0,1]\n" \
  "v_pk_mad_u16 v220, v220, 8, v221 op_sel_hi:[1,0,1]\n" \
  "v_pk_mad_u16 v222, v222, 8, v223 op_sel_hi:[1,0,1]\n" \
  "v_alignbit_b32 v220, v220, v220, v192\n" \
  "v_alignbit_b32 v222, v222, v222, v192\n" \
  "v_add_u32 v217, 0x400, v195\n" \
  "ds_write2_b32 v217, v216, v218 offset0:0 offset1:4\n" \
  "ds_write2_b32 v217, v220, v222 offset0:2 offset1:6\n" \
  "v_subrev_u32 v194, 0x800, v194\n" \
  "s_mov_b32 s51, 4\n" \
  "s_call_b64 s[52:53], LA_loop\n" \
  "LA_ev2:\n" \
  "v_mov_b32 v198, v178\n" \
  "s_waitcnt vmcnt(6)\n" \
  "v_cvt_pk_u16_u32 v224, v224, v225\n" \
  "v_cvt_pk_u16_u32 v226, v226, v227\n" \
  "v_cvt_pk_u16_u32 v228, v228, v229\n" \
  "v_cvt_pk_u16_u32 v230, v230, v231\n" \
  "v_pk_lshrrev_b16 v225, 3, v224 op_sel_hi:[0,1]\n" \
  "v_pk_lshrrev_b16 v227, 3, v226 op_sel_hi:[0,1]\n" \
  "v_pk_lshrrev_b16 v229, 3, v228 op_sel_hi:[0,1]\n" \
  "v_pk_lshrrev_b16 v231, 3, v230 op_sel_hi:[0,1]\n" \
  "v_pk_mad_u16 v224, v224, 8, v225 op_sel_hi:[1,0,1]\n" \
  "v_pk_mad_u16 v226, v226, 8, v227 op_sel_hi:[1,0,1]\n" \
  "v_pk_mad_u16 v228, v228, 8, v229 op_sel_hi:[1,0,1]\n" \
  "v_pk_mad_u16 v230, v230, 8, v231 op_sel_hi:[1,0,1]\n" \
  "v_alignbit_b32 v228, v228, v228, v192\n" \
  "v_alignbit_b32 v230, v230, v230, v192\n" \
  "ds_write2_b32 v195, v224, v226 offset0:0 offset1:4\n" \
  "ds_write2_b32 v195, v228, v230 offset0:2 offset1:6\n" \
  "s_mov_b32 s51, 4\n" \
  "s_call_b64 s[52:53], LA_loop\n" \
  "LA_ev3:\n" \
  "v_mov_b32 v199, v178\n" \
  "s_waitcnt vmcnt(4)\n" \
  "v_cvt_pk_u16_u32 v232, v232, v233\n" \
  "v_cvt_pk_u16_u32 v234, v234, v235\n" \
  "v_cvt_pk_u16_u32 v236, v236, v237\n" \
  "v_cvt_pk_u16_u32 v238, v238, v239\n" \
  "v_pk_lshrrev_b16 v233, 3, v232 op_sel_hi:[0,1]\n" \
  "v_pk_lshrrev_b16 v235, 3, v234 op_sel_hi:[0,1]\n" \
  "v_pk_lshrrev_b16 v237, 3, v236 op_sel_hi:[0,1]\n" \
  "v_pk_lshrrev_b16 v239, 3, v238 op_sel_hi:[0,1]\n" \
  "v_pk_mad_u16 v232, v232, 8, v233 op_sel_hi:[1,0,1]\n" \
  "v_pk_mad_u16 v234, v234, 8, v235 op_sel_hi:[1,0,1]\n" \
  "v_pk_mad_u16 v236, v236, 8, v237 op_sel_hi:[1,0,1]\n" \
  "v_pk_mad_u16 v238, v238, 8, v239 op_sel_hi:[1,0,1]\n" \
  "v_alignbit_b32 v236, v236, v236, v192\n" \
  "v_alignbit_b32 v238, v238, v238, v192\n" \
  "v_add_u32 v233, 0x400, v195\n" \
  "ds_write2_b32 v233, v232, v234 offset0:0 offset1:4\n" \
  "ds_write2_b32 v233, v236, v238 offset0:2 offset1:6\n" \
  "v_subrev_u32 v194, 0x800, v194\n" \
  "s_mov_b32 s51, 4\n" \
  "s_call_b64 s[52:53], LA_loop\n" \
  "LA_ev4:\n" \
  "v_mov_b32 v200, v178\n" \
  "s_waitcnt vmcnt(2)\n" \
  "v_cvt_pk_u16_u32 v240, v240, v241\n" \
  "v_cvt_pk_u16_u32 v242, v242, v243\n" \
  "v_cvt_pk_u16_u32 v244, v244, v245\n" \
  "v_cvt_pk_u16_u32 v246, v246, v247\n" \
  "v_pk_lshrrev_b16 v241, 3, v240 op_sel_hi:[0,1]\n" \
  "v_pk_lshrrev_b16 v243, 3, v242 op_sel_hi:[0,1]\n" \
  "v_pk_lshrrev_b16 v245, 3, v244 op_sel_hi:[0,1]\n" \
  "v_pk_lshrrev_b16 v247, 3, v246 op_sel_hi:[0,1]\n" \
  "v_pk_mad_u16 v240, v240, 8, v241 op_sel_hi:[1,0,1]\n" \
  "v_pk_mad_u16 v242, v242, 8, v243 op_sel_hi:[1,0,1]\n" \
  "v_pk_mad_u16 v244, v244, 8, v245 op_sel_hi:[1,0,1]\n" \
  "v_pk_mad_u16 v246, v246, 8, v247 op_sel_hi:[1,0,1]\n" \
  "v_alignbit_b32 v244, v244, v244, v192\n" \
  "v_alignbit_b32 v246, v246, v246, v192\n" \
  "ds_write2_b32 v195, v240, v242 offset0:0 offset1:4\n" \
  "ds_write2_b32 v195, v244, v246 offset0:2 offset1:6\n" \
  "s_mov_b32 s51, 4\n" \
  "s_call_b64 s[52:53], LA_loop\n" \
  "LA_ev5:\n" \
  "v_mov_b32 v201, v178\n" \
  "s_waitcnt vmcnt(0)\n" \
  "v_cvt_pk_u16_u32 v248, v248, v249\n" \
  "v_cvt_pk_u16_u32 v250, v250, v251\n" \
  "v_cvt_pk_u16_u32 v252, v252, v253\n" \
  "v_cvt_pk_u16_u32 v254, v254, v255\n" \
  "v_pk_lshrrev_b16 v249, 3, v248 op_sel_hi:[0,1]\n" \
  "v_pk_lshrrev_b16 v251, 3, v250 op_sel_hi:[0,1]\n" \
  "v_pk_lshrrev_b16 v253, 3, v252 op_sel_hi:[0,1]\n" \
  "v_pk_lshrrev_b16 v255, 3, v254 op_sel_hi:[0,1]\n" \
  "v_pk_mad_u16 v248, v248, 8, v249 op_sel_hi:[1,0,1]\n" \
  "v_pk_mad_u16 v250, v250, 8, v251 op_sel_hi:[1,0,1]\n" \
  "v_pk_mad_u16 v252, v252, 8, v253 op_sel_hi:[1,0,1]\n" \
  "v_pk_mad_u16 v254, v254, 8, v255 op_sel_hi:[1,0,1]\n" \
  "v_alignbit_b32 v252, v252, v252, v192\n" \
  "v_alignbit_b32 v254, v254, v254, v192\n" \
  "v_add_u32 v249, 0x400, v195\n" \
  "ds_write2_b32 v249, v248, v250 offset0:0 offset1:4\n" \
  "ds_write2_b32 v249, v252, v254 offset0:2 offset1:6\n" \
  "v_mov_b32 v208, 163328\n" \
  "ds_read_b32 v209, v208 offset:4\n" \
  "v_subrev_u32 v194, 0x800, v194\n" \
  "s_mov_b32 s51, 4\n" \
  "s_call_b64 s[52:53], LA_loop\n" \
  "LA_ev6:\n" \
  "v_mov_b32 v202, v178\n" \
  "v_readfirstlane_b32 s35, v209\n" \
  "s_cmp_ge_u32 s35, 128\n" \
  "s_cbranch_scc1 LA_pl\n" \
  "L2_:\n" \
  "ds_read_b32 v209, v208 offset:4\n" \
  "s_waitcnt lgkmcnt(0)\n" \
  "v_readfirstlane_b32 s35, v209\n" \
  "s_cmp_lt_u32 s35, 128\n" \
  "s_cbranch_scc1 L2_\n" \
  "LA_pl:\n" \
  "s_and_b32 s4, s35, 15\n" \
  "s_sub_u32 s4, s4, 4\n" \
  "s_bfe_u32 s5, s35, 0x30004\n" \
  "s_sub_u32 s5, s5, s29\n" \
  "s_cmp_le_i32 s4, 0\n" \
  "s_cbranch_scc1 LA_nt\n" \
  "s_mul_i32 s35, s5, 51200\n" \
  "s_ashr_i32 s36, s35, 31\n" \
  "s_add_u32 s46, s46, s35\n" \
  "s_addc_u32 s47, s47, s36\n" \
  "v_lshrrev_b32 v216, 4, v188\n" \
  "v_mul_u32_u24 v216, 400, v216\n" \
  "v_add_u32 v216, 6400, v216\n" \
  "v_bfe_u32 v218, v194, 4, 4\n" \
  "v_min_u32 v217, 8, v218\n" \
  "v_lshl_add_u32 v217, v217, 4, v216\n" \
  "v_lshl_add_u32 v216, v218, 4, v216\n" \
  "global_load_dwordx4 v[208:211], v216, s[46:47] offset:0\n" \
  "global_load_dwordx4 v[212:215], v217, s[46:47] offset:256\n" \
  "LA_nt:\n" \
  "s_lshl_b32 s5, s5, 7\n" \
  "s_sub_u32 s5, s5, s4\n" \
  "s_min_i32 s51, s4, 0\n" \
  "s_add_u32 s51, s51, 4\n" \
  "s_cmp_eq_u32 s51, 0\n" \
  "s_cbranch_scc1 LA_ev7\n" \
  "s_call_b64 s[52:53], LA_loop\n" \
  "LA_ev7:\n" \
  "v_mov_b32 v203, v178\n" \
  "s_cmp_le_i32 s4, 0\n" \
  "s_cbranch_scc1 LA_ev8\n" \
  "s_waitcnt vmcnt(0)\n" \
  "v_cvt_pk_u16_u32 v208, v208, v209\n" \
  "v_cvt_pk_u16_u32 v210, v210, v211\n" \
  "v_cvt_pk_u16_u32 v212, v212, v213\n" \
  "v_cvt_pk_u16_u32 v214, v214, v215\n" \
  "v_pk_lshrrev_b16 v209, 3, v208 op_sel_hi:[0,1]\n" \
  "v_pk_lshrrev_b16 v211, 3, v210 op_sel_hi:[0,1]\n" \
  "v_pk_lshrrev_b16 v213, 3, v212 op_sel_hi:[0,1]\n" \
  "v_pk_lshrrev_b16 v215, 3, v214 op_sel_hi:[0,1]\n" \
  "v_pk_mad_u16 v208, v208, 8, v209 op_sel_hi:[1,0,1]\n" \
  "v_pk_mad_u16 v210, v210, 8, v211 op_sel_hi:[1,0,1]\n" \
  "v_pk_mad_u16 v212, v212, 8, v213 op_sel_hi:[1,0,1]\n" \
  "v_pk_mad_u16 v214, v214, 8, v215 op_sel_hi:[1,0,1]\n" \
  "v_alignbit_b32 v212, v212, v212, v192\n" \
  "v_alignbit_b32 v214, v214, v214, v192\n" \
  "ds_write2_b32 v195, v208, v210 offset0:0 offset1:4\n" \
  "ds_write2_b32 v195, v212, v214 offset0:2 offset1:6\n" \
  "s_lshl_b32 s35, s4, 8\n" \
  "s_add_u32 s35, s35, 0x400\n" \
  "v_subrev_u32 v194, s35, v194\n" \
  "s_mov_b32 s51, s4\n" \
  "s_call_b64 s[52:53], LA_loop\n" \
  "LA_ev8:\n" \
  "v_mov_b32 v179, v178\n" \
  "ds_read_b128 v[100:103], v185 offset:64\n" \
  "ds_read2_b32 v[144:145], v186 offset0:0 offset1:2\n" \
  "ds_read2_b32 v[146:147], v186 offset0:4 offset1:6\n" \
  "v_mad_u32_u16 v187, v183, 16, v190\n" \
  "ds_read2_b32 v[148:149], v187 offset0:0 offset1:2\n" \
  "ds_read2_b32 v[150:151], v187 offset0:4 offset1:6\n" \
  "ds_read_b128 v[164:167], v191\n" \
  "s_lshr_b32 s35, s30, 2\n" \
  "s_lshl3_add_u32 s33, s29, s35\n" \
  "s_and_b32 s34, s30, 3\n" \
  "s_lshl_b32 s59, s27, 11\n" \
  "s_mul_i32 s35, s33, 1600000\n" \
  "s_add_u32 s54, s24, s35\n" \
  "s_addc_u32 s55, s25, 0\n" \
  "s_mul_i32 s35, s34, 819200\n" \
  "s_lshl1_add_u32 s35, s59, s35\n" \
  "s_add_u32 s35, s35, 102400000\n" \
  "s_add_u32 s56, s24, s35\n" \
  "s_addc_u32 s57, s25, 0\n" \
  "v_mbcnt_lo_u32_b32 v189, -1, 0\n" \
  "v_mbcnt_hi_u32_b32 v189, -1, v189\n" \
  "v_lshlrev_b32 v188, 4, v189\n" \
  "s_waitcnt lgkmcnt(6)\n" \
  "v_mfma_f32_16x16x32_bf16 v[152:155], v[112:115], v[112:115], 0\n" \
  "v_fmac_f32 v176, v156, v88\n" \
  "v_fmac_f32 v176, v157, v89\n" \
  "v_mfma_f32_16x16x32_bf16 v[152:155], v[116:119], v[116:119], v[152:155]\n" \
  "v_fmac_f32 v176, v158, v90\n" \
  "v_fmac_f32 v176, v159, v91\n" \
  "v_mfma_f32_16x16x32_bf16 v[156:159], v[128:131], v[128:131], 0\n" \
  "v_fmac_f32 v176, v160, v92\n" \
  "v_fmac_f32 v176, v161, v93\n" \
  "v_mfma_f32_16x16x32_bf16 v[156:159], v[132:135], v[132:135], v[156:159]\n" \
  "v_fmac_f32 v176, v162, v94\n" \
  "v_fma_f32 v177, v163, v95, v176\n" \
  "v_mfma_f32_16x16x32_bf16 v[160:163], v[128:131], v[136:139], 0\n" \
  "v_mul_f32 v176, v152, v0\n" \
  "v_fmac_f32 v176, v153, v1\n" \
  "v_mfma_f32_16x16x32_bf16 v[160:163], v[132:135], v[140:143], v[160:163]\n" \
  "v_fmac_f32 v176, v154, v2\n" \
  "v_fmac_f32 v176, v155, v3\n" \
  "v_mfma_f32_16x16x32_bf16 v[152:155], v[136:139], v[136:139], 0\n" \
  "v_fmac_f32 v176, v156, v4\n" \
  "v_fmac_f32 v176, v157, v5\n" \
  "v_mfma_f32_16x16x32_bf16 v[152:155], v[140:143], v[140:143], v[152:155]\n" \
  "v_fmac_f32 v176, v158, v6\n" \
  "v_fmac_f32 v176, v159, v7\n" \
  "s_waitcnt lgkmcnt(0)\n" \
  "s_setprio 3\n" \
  "s_barrier\n" \
  "global_load_dwordx4 v[208:211], v188, s[56:57] offset:0 sc1\n" \
  "global_load_dwordx4 v[212:215], v188, s[56:57] offset:1024 sc1\n" \
  "global_load_dwordx4 v[216:219], v188, s[56:57] offset:2048 sc1\n" \
  "global_load_dwordx4 v[220:223], v188, s[56:57] offset:3072 sc1\n" \
  "v_mfma_f32_16x16x32_bf16 v[156:159], v[112:115], v[136:139], 0\n" \
  "v_fmac_f32 v176, v160, v8\n" \
  "v_fmac_f32 v176, v161, v9\n" \
  "v_lshrrev_b32 v180, 1, v189\n" \
  "v_mfma_f32_16x16x32_bf16 v[156:159], v[116:119], v[140:143], v[156:159]\n" \
  "v_fmac_f32 v176, v162, v10\n" \
  "v_fmac_f32 v176, v163, v11\n" \
  "v_xor_b32 v180, v180, v189\n" \
  "v_mfma_f32_16x16x32_bf16 v[160:163], v[112:115], v[128:131], 0\n" \
  "v_fmac_f32 v176, v152, v12\n" \
  "v_fmac_f32 v176, v153, v13\n" \
  "v_add_f32_dpp v177, v177, v177 row_ror:8 row_mask:0xf bank_mask:0xf\n" \
  "v_and_b32 v180, 8, v180\n" \
  "v_mfma_f32_16x16x32_bf16 v[160:163], v[116:119], v[132:135], v[160:163]\n" \
  "v_fmac_f32 v176, v154, v14\n" \
  "v_fmac_f32 v176, v155, v15\n" \
  "v_bfe_u32 v181, v189, 2, 1\n" \
  "v_mfma_f32_16x16x32_bf16 v[152:155], v[112:115], v[120:123], 0\n" \
  "v_fmac_f32 v176, v156, v16\n" \
  "v_fmac_f32 v176, v157, v17\n" \
  "v_lshlrev_b32_e64 v181, v181, 5\n" \
  "v_mfma_f32_16x16x32_bf16 v[152:155], v[116:119], v[124:127], v[152:155]\n" \
  "v_fmac_f32 v176, v158, v18\n" \
  "v_fmac_f32 v176, v159, v19\n" \
  "v_mov_b32_dpp v178, v178 row_ror:4 row_mask:0xf bank_mask:0xf\n" \
  "v_lshlrev_b32 v180, v180, v181\n" \
  "v_mfma_f32_16x16x32_bf16 v[156:159], v[120:123], v[136:139], 0\n" \
  "v_fmac_f32 v176, v160, v20\n" \
  "v_fmac_f32 v176, v161, v21\n" \
  "v_bfe_u32 v181, v189, 2, 2\n" \
  "v_mfma_f32_16x16x32_bf16 v[156:159], v[124:127], v[140:143], v[156:159]\n" \
  "v_fmac_f32 v176, v162, v22\n" \
  "v_fmac_f32 v176, v163, v23\n" \
  "v_lshlrev_b32_e64 v181, v181, 1\n" \
  "v_mfma_f32_16x16x32_bf16 v[160:163], v[104:107], v[128:131], 0\n" \
  "v_fmac_f32 v176, v152, v24\n" \
  "v_fmac_f32 v176, v153, v25\n" \
  "v_cmp_gt_u32 vcc, 32, v189\n" \
  "v_mfma_f32_16x16x32_bf16 v[160:163], v[108:111], v[132:135], v[160:163]\n" \
  "v_fmac_f32 v176, v154, v26\n" \
  "v_fmac_f32 v176, v155, v27\n" \
  "v_mov_b32 v191, 0x3f80\n" \
  "v_mfma_f32_16x16x32_bf16 v[152:155], v[120:123], v[128:131], 0\n" \
  "v_fmac_f32 v176, v156, v28\n" \
  "v_fmac_f32 v176, v157, v29\n" \
  "v_add_u32 v190, 32000, v188\n" \
  "v_mfma_f32_16x16x32_bf16 v[152:155], v[124:127], v[132:135], v[152:155]\n" \
  "v_fmac_f32 v176, v158, v30\n" \
  "v_fmac_f32 v176, v159, v31\n" \
  "v_add_f32_dpp v178, v177, v177 row_ror:4 row_mask:0xf bank_mask:0x1\n" \
  "v_cndmask_b32 v192, v181, v180, vcc\n" \
  "v_mfma_f32_16x16x32_bf16 v[156:159], v[96:99], v[128:131], 0\n" \
  "v_fmac_f32 v176, v160, v32\n" \
  "v_fmac_f32 v176, v161, v33\n" \
  "v_bfe_i32 v180, v192, 0, 1\n" \
  "v_mfma_f32_16x16x32_bf16 v[156:159], v[100:103], v[132:135], v[156:159]\n" \
  "v_fmac_f32 v176, v162, v34\n" \
  "v_fmac_f32 v176, v163, v35\n" \
  "v_and_b32 v180, v180, v191\n" \
  "v_smfmac_f32_16x16x64_bf16 v[172:175], v[144:147], v[128:135], v193\n" \
  "v_fmac_f32 v176, v152, v36\n" \
  "v_fmac_f32 v176, v153, v37\n" \
  "v_bfe_i32 v181, v192, 1, 1\n" \
  "v_smfmac_f32_16x16x64_bf16 v[172:175], v[148:151], v[136:143], v193\n" \
  "v_fmac_f32 v176, v154, v38\n" \
  "v_fmac_f32 v176, v155, v39\n" \
  "v_and_b32 v181, v181, v191\n" \
  "v_mfma_f32_16x16x32_bf16 v[160:163], v[96:99], v[112:115], 0\n" \
  "v_fmac_f32 v176, v156, v40\n" \
  "v_fmac_f32 v176, v157, v41\n" \
  "v_bfe_i32 v182, v192, 2, 1\n" \
  "v_mfma_f32_16x16x32_bf16 v[160:163], v[100:103], v[116:119], v[160:163]\n" \
  "v_fmac_f32 v176, v158, v42\n" \
  "v_fmac_f32 v176, v159, v43\n" \
  "v_and_b32 v182, v182, v191\n" \
  "v_mfma_f32_16x16x32_bf16 v[152:155], v[104:107], v[112:115], 0\n" \
  "v_fmac_f32 v176, v172, v44\n" \
  "v_fmac_f32 v176, v173, v45\n" \
  "v_bfe_i32 v183, v192, 3, 1\n" \
  "v_mfma_f32_16x16x32_bf16 v[152:155], v[108:111], v[116:119], v[152:155]\n" \
  "v_fmac_f32 v176, v174, v46\n" \
  "v_fmac_f32 v176, v175, v47\n" \
  "v_and_b32 v183, v183, v191\n" \
  "v_mfma_f32_16x16x32_bf16 v[156:159], v[104:107], v[136:139], 0\n" \
  "v_fmac_f32 v176, v160, v48\n" \
  "v_fmac_f32 v176, v161, v49\n" \
  "v_and_b32 v192, 3, v189\n" \
  "v_mfma_f32_16x16x32_bf16 v[156:159], v[108:111], v[140:143], v[156:159]\n" \
  "v_fmac_f32 v176, v162, v50\n" \
  "v_fmac_f32 v176, v163, v51\n" \
  "v_mul_u32_u24 v192, 0x5555, v192\n" \
  "v_smfmac_f32_16x16x64_bf16 v[168:171], v[144:147], v[112:119], v193\n" \
  "v_fmac_f32 v176, v152, v52\n" \
  "v_fmac_f32 v176, v153, v53\n" \
  "v_add_u32 v189, 16000, v188\n" \
  "v_smfmac_f32_16x16x64_bf16 v[168:171], v[148:151], v[120:127], v193\n" \
  "v_fmac_f32 v176, v154, v54\n" \
  "v_fmac_f32 v176, v155, v55\n" \
  "v_add_u32 v191, 48000, v188\n" \
  "v_mfma_f32_16x16x32_bf16 v[160:163], v[96:99], v[136:139], 0\n" \
  "v_fmac_f32 v176, v156, v56\n" \
  "v_fmac_f32 v176, v157, v57\n" \
  "v_mfma_f32_16x16x32_bf16 v[160:163], v[100:103], v[140:143], v[160:163]\n" \
  "v_fmac_f32 v176, v158, v58\n" \
  "v_fmac_f32 v176, v159, v59\n" \
  "s_add_u32 s35, s59, 64000\n" \
  "s_add_u32 s54, s54, s35\n" \
  "s_addc_u32 s55, s55, 0\n" \
  "s_add_u32 s58, s59, 0x10000\n" \
  "s_mov_b32 m0, s58\n" \
  "s_add_u32 s35, s58, 0x8000\n" \
  "global_load_lds_dwordx4 v188, s[54:55] sc1\n" \
  "global_load_lds_dwordx4 v188, s[54:55] offset:1024 sc1\n" \
  "v_smfmac_f32_16x16x64_bf16 v[164:167], v[148:151], v[104:111], v193\n" \
  "v_fmac_f32 v176, v168, v60\n" \
  "v_fmac_f32 v176, v169, v61\n" \
  "v_smfmac_f32_16x16x64_bf16 v[164:167], v[144:147], v[96:103], v193\n" \
  "v_fmac_f32 v176, v170, v62\n" \
  "v_fmac_f32 v176, v171, v63\n" \
  "v_mfma_f32_16x16x32_bf16 v[152:155], v[120:123], v[120:123], 0\n" \
  "v_fmac_f32 v176, v160, v64\n" \
  "v_fmac_f32 v176, v161, v65\n" \
  "v_mfma_f32_16x16x32_bf16 v[152:155], v[124:127], v[124:127], v[152:155]\n" \
  "v_fmac_f32 v176, v162, v66\n" \
  "v_fmac_f32 v176, v163, v67\n" \
  "v_mfma_f32_16x16x32_bf16 v[156:159], v[104:107], v[120:123], 0\n" \
  "v_fmac_f32 v176, v164, v68\n" \
  "v_fmac_f32 v176, v165, v69\n" \
  "v_mfma_f32_16x16x32_bf16 v[156:159], v[108:111], v[124:127], v[156:159]\n" \
  "v_fmac_f32 v176, v166, v70\n" \
  "v_fmac_f32 v176, v167, v71\n" \
  "s_add_u32 m0, s58, 0x4000\n" \
  "s_cmp_lt_u32 s35, 0x28000\n" \
  "global_load_lds_dwordx4 v189, s[54:55] sc1\n" \
  "global_load_lds_dwordx4 v189, s[54:55] offset:1024 sc1\n" \
  "s_cselect_b32 s58, s35, s59\n" \
  "s_add_u32 s54, s54, 32000\n" \
  "s_addc_u32 s55, s55, 0\n" \
  "v_mfma_f32_16x16x32_bf16 v[160:163], v[96:99], v[120:123], 0\n" \
  "v_fmac_f32 v176, v152, v72\n" \
  "v_fmac_f32 v176, v153, v73\n" \
  "v_mfma_f32_16x16x32_bf16 v[160:163], v[100:103], v[124:127], v[160:163]\n" \
  "v_fmac_f32 v176, v154, v74\n" \
  "v_fmac_f32 v176, v155, v75\n" \
  "v_mfma_f32_16x16x32_bf16 v[152:155], v[96:99], v[104:107], 0\n" \
  "v_fmac_f32 v176, v156, v76\n" \
  "v_fmac_f32 v176, v157, v77\n" \
  "v_mfma_f32_16x16x32_bf16 v[152:155], v[100:103], v[108:111], v[152:155]\n" \
  "v_fmac_f32 v176, v158, v78\n" \
  "v_fmac_f32 v176, v159, v79\n" \
  "v_mfma_f32_16x16x32_bf16 v[156:159], v[104:107], v[104:107], 0\n" \
  "v_fmac_f32 v176, v160, v80\n" \
  "v_fmac_f32 v176, v161, v81\n" \
  "v_mfma_f32_16x16x32_bf16 v[156:159], v[108:111], v[108:111], v[156:159]\n" \
  "v_fmac_f32 v176, v162, v82\n" \
  "v_fmac_f32 v176, v163, v83\n" \
  "v_mfma_f32_16x16x32_bf16 v[160:163], v[148:151], v[144:147], 0\n" \
  "v_fmac_f32 v176, v152, v84\n" \
  "v_fmac_f32 v176, v153, v85\n" \
  "v_mfma_f32_16x16x32_bf16 v[160:163], v[96:99], v[96:99], v[160:163]\n" \
  "v_fmac_f32 v176, v154, v86\n" \
  "v_fmac_f32 v176, v155, v87\n" \
  "v_mfma_f32_16x16x32_bf16 v[160:163], v[100:103], v[100:103], v[160:163]\n" \
  "s_mov_b32 s51, 7\n" \
  "v_fmac_f32 v176, v156, v88\n" \
  "v_fmac_f32 v176, v157, v89\n" \
  "v_fmac_f32 v176, v158, v90\n" \
  "v_fmac_f32 v176, v159, v91\n" \
  "s_nop 2\n" \
  "v_fmac_f32 v176, v160, v92\n" \
  "v_fmac_f32 v176, v161, v93\n" \
  "v_fmac_f32 v176, v162, v94\n" \
  "v_fma_f32 v177, v163, v95, v176\n" \
  "s_nop 1\n" \
  "v_add_f32_dpp v177, v177, v177 row_ror:8 row_mask:0xf bank_mask:0xf\n" \
  "v_mov_b32_dpp v178, v178 row_ror:4 row_mask:0xf bank_mask:0xf\n" \
  "s_nop 1\n" \
  "v_add_f32_dpp v178, v177, v177 row_ror:4 row_mask:0xf bank_mask:0x1\n" \
  "v_mov_b32 v99, v179\n" \
  "s_nop 0\n" \
  "v_mov_b32_dpp v196, v178 row_ror:8 row_mask:0xf bank_mask:0xc\n" \
  "s_nop 1\n" \
  "v_permlane16_swap_b32 v196, v197\n" \
  "v_permlane16_swap_b32 v198, v199\n" \
  "v_permlane16_swap_b32 v200, v201\n" \
  "v_permlane16_swap_b32 v202, v203\n" \
  "v_permlane16_swap_b32 v179, v99\n" \
  "v_add_f32 v196, v196, v197\n" \
  "v_add_f32 v198, v198, v199\n" \
  "v_add_f32 v200, v200, v201\n" \
  "v_add_f32 v202, v202, v203\n" \
  "v_add_f32 v179, v179, v99\n" \
  "v_lshrrev_b32 v96, 4, v188\n" \
  "v_and_b32 v97, 1, v96\n" \
  "v_mov_b32 v99, v179\n" \
  "v_lshrrev_b32 v98, 4, v96\n" \
  "v_permlane32_swap_b32 v196, v198\n" \
  "v_permlane32_swap_b32 v200, v202\n" \
  "v_permlane32_swap_b32 v179, v99\n" \
  "v_add_f32 v196, v196, v198\n" \
  "v_add_f32 v200, v200, v202\n" \
  "v_add_f32 v179, v179, v99\n" \
  "v_cmp_eq_u32 vcc, 1, v97\n" \
  "v_lshlrev_b32 v97, 2, v97\n" \
  "v_add_lshl_u32 v97, v97, v98, 2\n" \
  "v_bfe_u32 v98, v96, 2, 2\n" \
  "v_add_f32_dpp v196, v196, v196 quad_perm:[1,0,3,2] row_mask:0xf bank_mask:0xf\n" \
  "v_add_f32_dpp v200, v200, v200 quad_perm:[1,0,3,2] row_mask:0xf bank_mask:0xf\n" \
  "v_add_f32_dpp v179, v179, v179 quad_perm:[1,0,3,2] row_mask:0xf bank_mask:0xf\n" \
  "v_sub_u32 v97, v97, v98\n" \
  "v_cndmask_b32 v196, v196, v200, vcc\n" \
  "v_add_u32 v97, 1, v97\n" \
  "s_min_i32 s35, s4, 0\n" \
  "s_add_u32 s36, s4, 32\n" \
  "s_add_u32 s37, s4, 29\n" \
  "v_add_f32_dpp v179, v179, v179 quad_perm:[2,3,0,1] row_mask:0xf bank_mask:0xf\n" \
  "v_add_f32_dpp v196, v196, v196 quad_perm:[2,3,0,1] row_mask:0xf bank_mask:0xf\n" \
  "s_mov_b32 exec_lo, 0x0\n" \
  "s_mov_b32 exec_hi, 0xaaaa0000\n" \
  "v_add_u32 v97, s35, v97\n" \
  "s_mov_b32 exec_lo, 0x5500\n" \
  "s_mov_b32 exec_hi, 0x0\n" \
  "v_add_u32 v97, s36, v97\n" \
  "s_mov_b32 exec_lo, 0x4444\n" \
  "s_mov_b32 exec_hi, 0x0\n" \
  "v_sub_u32 v97, s37, v98\n" \
  "v_mov_b32 v196, v179\n" \
  "s_mov_b64 exec, -1\n" \
  "v_cmp_lt_u32 vcc, 31, v97\n" \
  "v_add_u32 v98, s5, v97\n" \
  "s_lshl2_add_u32 s35, s7, s28\n" \
  "s_lshl_b32 s35, s35, 5\n" \
  "v_cndmask_b32 v97, v97, v98, vcc\n" \
  "v_add_lshl_u32 v97, v97, s35, 2\n" \
  "s_lshl_b32 s35, s31, 16\n" \
  "s_add_u32 s35, s35, 109871104\n" \
  "s_add_u32 s40, s24, s35\n" \
  "s_addc_u32 s41, s25, 0\n" \
  "v_min_u32 v196, -2, v196\n" \
  "v_lshlrev_b32 v184, 16, v204\n" \
  "v_and_b32 v185, 0xffff0000, v204\n" \
  "v_lshlrev_b32 v186, 16, v205\n" \
  "v_and_b32 v187, 0xffff0000, v205\n" \
  "s_barrier\n" \
  "s_add_u32 s56, s56, 32768\n" \
  "s_addc_u32 s57, s57, 0\n" \
  "global_load_dwordx4 v[224:227], v188, s[56:57] sc1\n" \
  "global_load_dwordx4 v[228:231], v188, s[56:57] offset:1024 sc1\n" \
  "global_load_dwordx4 v[232:235], v188, s[56:57] offset:2048 sc1\n" \
  "global_load_dwordx4 v[236:239], v188, s[56:57] offset:3072 sc1\n" \
  "s_add_u32 s56, s56, 32768\n" \
  "s_addc_u32 s57, s57, 0\n" \
  "s_mov_b32 exec_lo, 0x33337777\n" \
  "s_mov_b32 exec_hi, 0x33333333\n" \
  "global_store_dword v97, v196, s[40:41] sc1\n" \
  "s_mov_b64 exec, -1\n" \
  "s_waitcnt vmcnt(9)\n" \
  "s_setprio 0\n" \
  "s_branch LG_start\n" \
  "LA_loop:\n" \
  "v_mfma_f32_16x16x32_bf16 v[152:155], v[112:115], v[112:115], 0\n" \
  "s_waitcnt lgkmcnt(8)\n" \
  "ds_read_b128 v[100:103], v185 offset:64\n" \
  "v_fmac_f32 v176, v156, v88\n" \
  "v_fmac_f32 v176, v157, v89\n" \
  "v_mfma_f32_16x16x32_bf16 v[152:155], v[116:119], v[116:119], v[152:155]\n" \
  "ds_read2_b32 v[144:145], v186 offset0:0 offset1:2\n" \
  "v_fmac_f32 v176, v158, v90\n" \
  "v_fmac_f32 v176, v159, v91\n" \
  "v_mfma_f32_16x16x32_bf16 v[156:159], v[128:131], v[128:131], 0\n" \
  "ds_read2_b32 v[146:147], v186 offset0:4 offset1:6\n" \
  "v_fmac_f32 v176, v160, v92\n" \
  "v_fmac_f32 v176, v161, v93\n" \
  "v_mad_u32_u16 v187, v183, 16, v190\n" \
  "v_mfma_f32_16x16x32_bf16 v[156:159], v[132:135], v[132:135], v[156:159]\n" \
  "s_waitcnt lgkmcnt(10)\n" \
  "ds_read2_b32 v[148:149], v187 offset0:0 offset1:2\n" \
  "v_fmac_f32 v176, v162, v94\n" \
  "v_fma_f32 v177, v163, v95, v176\n" \
  "v_mfma_f32_16x16x32_bf16 v[160:163], v[128:131], v[136:139], 0\n" \
  "s_waitcnt lgkmcnt(10)\n" \
  "ds_read2_b32 v[150:151], v187 offset0:4 offset1:6\n" \
  "v_mul_f32 v176, v152, v0\n" \
  "v_fmac_f32 v176, v153, v1\n" \
  "v_mfma_f32_16x16x32_bf16 v[160:163], v[132:135], v[140:143], v[160:163]\n" \
  "ds_read_b128 v[180:183], v194\n" \
  "v_fmac_f32 v176, v154, v2\n" \
  "v_fmac_f32 v176, v155, v3\n" \
  "v_mfma_f32_16x16x32_bf16 v[152:155], v[136:139], v[136:139], 0\n" \
  "v_fmac_f32 v176, v156, v4\n" \
  "v_fmac_f32 v176, v157, v5\n" \
  "v_mfma_f32_16x16x32_bf16 v[152:155], v[140:143], v[140:143], v[152:155]\n" \
  "ds_read_b128 v[164:167], v191\n" \
  "v_fmac_f32 v176, v158, v6\n" \
  "v_fmac_f32 v176, v159, v7\n" \
  "v_mfma_f32_16x16x32_bf16 v[156:159], v[112:115], v[136:139], 0\n" \
  "v_fmac_f32 v176, v160, v8\n" \
  "v_fmac_f32 v176, v161, v9\n" \
  "v_mfma_f32_16x16x32_bf16 v[156:159], v[116:119], v[140:143], v[156:159]\n" \
  "v_fmac_f32 v176, v162, v10\n" \
  "v_fmac_f32 v176, v163, v11\n" \
  "v_mfma_f32_16x16x32_bf16 v[160:163], v[112:115], v[128:131], 0\n" \
  "v_fmac_f32 v176, v152, v12\n" \
  "v_fmac_f32 v176, v153, v13\n" \
  "v_add_f32_dpp v177, v177, v177 row_ror:8 row_mask:0xf bank_mask:0xf\n" \
  "v_mfma_f32_16x16x32_bf16 v[160:163], v[116:119], v[132:135], v[160:163]\n" \
  "s_waitcnt lgkmcnt(11)\n" \
  "v_fmac_f32 v176, v154, v14\n" \
  "v_fmac_f32 v176, v155, v15\n" \
  "v_mfma_f32_16x16x32_bf16 v[152:155], v[112:115], v[120:123], 0\n" \
  "s_waitcnt lgkmcnt(10)\n" \
  "v_fmac_f32 v176, v156, v16\n" \
  "v_fmac_f32 v176, v157, v17\n" \
  "v_mfma_f32_16x16x32_bf16 v[152:155], v[116:119], v[124:127], v[152:155]\n" \
  "v_fmac_f32 v176, v158, v18\n" \
  "v_fmac_f32 v176, v159, v19\n" \
  "v_mov_b32_dpp v178, v178 row_ror:4 row_mask:0xf bank_mask:0xf\n" \
  "v_mfma_f32_16x16x32_bf16 v[156:159], v[120:123], v[136:139], 0\n" \
  "v_fmac_f32 v176, v160, v20\n" \
  "v_fmac_f32 v176, v161, v21\n" \
  "v_mfma_f32_16x16x32_bf16 v[156:159], v[124:127], v[140:143], v[156:159]\n" \
  "s_waitcnt lgkmcnt(9)\n" \
  "v_fmac_f32 v176, v162, v22\n" \
  "v_fmac_f32 v176, v163, v23\n" \
  "v_mfma_f32_16x16x32_bf16 v[160:163], v[104:107], v[128:131], 0\n" \
  "s_waitcnt lgkmcnt(1)\n" \
  "v_fmac_f32 v176, v152, v24\n" \
  "v_fmac_f32 v176, v153, v25\n" \
  "v_mad_u32_u16 v184, v182, 16, v188\n" \
  "v_mfma_f32_16x16x32_bf16 v[160:163], v[108:111], v[132:135], v[160:163]\n" \
  "v_fmac_f32 v176, v154, v26\n" \
  "v_fmac_f32 v176, v155, v27\n" \
  "v_mfma_f32_16x16x32_bf16 v[152:155], v[120:123], v[128:131], 0\n" \
  "v_fmac_f32 v176, v156, v28\n" \
  "v_fmac_f32 v176, v157, v29\n" \
  "v_mfma_f32_16x16x32_bf16 v[152:155], v[124:127], v[132:135], v[152:155]\n" \
  "v_fmac_f32 v176, v158, v30\n" \
  "v_fmac_f32 v176, v159, v31\n" \
  "v_add_f32_dpp v178, v177, v177 row_ror:4 row_mask:0xf bank_mask:0x1\n" \
  "v_mfma_f32_16x16x32_bf16 v[156:159], v[96:99], v[128:131], 0\n" \
  "v_fmac_f32 v176, v160, v32\n" \
  "v_fmac_f32 v176, v161, v33\n" \
  "v_mfma_f32_16x16x32_bf16 v[156:159], v[100:103], v[132:135], v[156:159]\n" \
  "v_fmac_f32 v176, v162, v34\n" \
  "v_fmac_f32 v176, v163, v35\n" \
  "v_smfmac_f32_16x16x64_bf16 v[172:175], v[144:147], v[128:135], v193\n" \
  "ds_read_b128 v[128:131], v184 offset:0\n" \
  "v_fmac_f32 v176, v152, v36\n" \
  "v_fmac_f32 v176, v153, v37\n" \
  "v_smfmac_f32_16x16x64_bf16 v[172:175], v[148:151], v[136:143], v193\n" \
  "ds_read_b128 v[132:135], v184 offset:64\n" \
  "v_fmac_f32 v176, v154, v38\n" \
  "v_fmac_f32 v176, v155, v39\n" \
  "v_mfma_f32_16x16x32_bf16 v[160:163], v[96:99], v[112:115], 0\n" \
  "v_fmac_f32 v176, v156, v40\n" \
  "v_fmac_f32 v176, v157, v41\n" \
  "v_mfma_f32_16x16x32_bf16 v[160:163], v[100:103], v[116:119], v[160:163]\n" \
  "v_fmac_f32 v176, v158, v42\n" \
  "v_fmac_f32 v176, v159, v43\n" \
  "v_add_u32 v194, 0x100, v194\n" \
  "v_mfma_f32_16x16x32_bf16 v[152:155], v[104:107], v[112:115], 0\n" \
  "v_fmac_f32 v176, v172, v44\n" \
  "v_fmac_f32 v176, v173, v45\n" \
  "v_mfma_f32_16x16x32_bf16 v[152:155], v[108:111], v[116:119], v[152:155]\n" \
  "v_fmac_f32 v176, v174, v46\n" \
  "v_fmac_f32 v176, v175, v47\n" \
  "v_mfma_f32_16x16x32_bf16 v[156:159], v[104:107], v[136:139], 0\n" \
  "ds_read_b128 v[172:175], v191\n" \
  "v_fmac_f32 v176, v160, v48\n" \
  "v_fmac_f32 v176, v161, v49\n" \
  "v_mad_u32_u16 v185, v181, 16, v188\n" \
  "v_mfma_f32_16x16x32_bf16 v[156:159], v[108:111], v[140:143], v[156:159]\n" \
  "v_fmac_f32 v176, v162, v50\n" \
  "v_fmac_f32 v176, v163, v51\n" \
  "v_mad_u32_u16 v186, v182, 16, v188 op_sel:[1,0,0,0]\n" \
  "v_smfmac_f32_16x16x64_bf16 v[168:171], v[144:147], v[112:119], v193\n" \
  "ds_read_b128 v[112:115], v185 offset:0\n" \
  "v_fmac_f32 v176, v152, v52\n" \
  "v_fmac_f32 v176, v153, v53\n" \
  "v_smfmac_f32_16x16x64_bf16 v[168:171], v[148:151], v[120:127], v193\n" \
  "ds_read_b128 v[116:119], v185 offset:64\n" \
  "v_fmac_f32 v176, v154, v54\n" \
  "v_fmac_f32 v176, v155, v55\n" \
  "v_mfma_f32_16x16x32_bf16 v[160:163], v[96:99], v[136:139], 0\n" \
  "v_fmac_f32 v176, v156, v56\n" \
  "v_fmac_f32 v176, v157, v57\n" \
  "v_mfma_f32_16x16x32_bf16 v[160:163], v[100:103], v[140:143], v[160:163]\n" \
  "s_waitcnt lgkmcnt(5)\n" \
  "v_fmac_f32 v176, v158, v58\n" \
  "v_fmac_f32 v176, v159, v59\n" \
  "v_smfmac_f32_16x16x64_bf16 v[164:167], v[148:151], v[104:111], v193\n" \
  "ds_read_b128 v[136:139], v186 offset:0\n" \
  "v_fmac_f32 v176, v168, v60\n" \
  "v_fmac_f32 v176, v169, v61\n" \
  "v_smfmac_f32_16x16x64_bf16 v[164:167], v[144:147], v[96:103], v193\n" \
  "ds_read_b128 v[140:143], v186 offset:64\n" \
  "v_fmac_f32 v176, v170, v62\n" \
  "v_fmac_f32 v176, v171, v63\n" \
  "v_mfma_f32_16x16x32_bf16 v[152:155], v[120:123], v[120:123], 0\n" \
  "v_fmac_f32 v176, v160, v64\n" \
  "v_fmac_f32 v176, v161, v65\n" \
  "v_mfma_f32_16x16x32_bf16 v[152:155], v[124:127], v[124:127], v[152:155]\n" \
  "ds_read_b128 v[168:171], v191\n" \
  "v_fmac_f32 v176, v162, v66\n" \
  "v_fmac_f32 v176, v163, v67\n" \
  "v_mad_u32_u16 v187, v181, 16, v188 op_sel:[1,0,0,0]\n" \
  "v_mfma_f32_16x16x32_bf16 v[156:159], v[104:107], v[120:123], 0\n" \
  "v_fmac_f32 v176, v164, v68\n" \
  "v_fmac_f32 v176, v165, v69\n" \
  "v_mad_u32_u16 v184, v180, 16, v188 op_sel:[1,0,0,0]\n" \
  "v_mfma_f32_16x16x32_bf16 v[156:159], v[108:111], v[124:127], v[156:159]\n" \
  "v_fmac_f32 v176, v166, v70\n" \
  "v_fmac_f32 v176, v167, v71\n" \
  "v_mfma_f32_16x16x32_bf16 v[160:163], v[96:99], v[120:123], 0\n" \
  "ds_read_b128 v[120:123], v187 offset:0\n" \
  "v_fmac_f32 v176, v152, v72\n" \
  "v_fmac_f32 v176, v153, v73\n" \
  "v_mfma_f32_16x16x32_bf16 v[160:163], v[100:103], v[124:127], v[160:163]\n" \
  "v_fmac_f32 v176, v154, v74\n" \
  "v_fmac_f32 v176, v155, v75\n" \
  "v_mad_u32_u16 v185, v180, 16, v188\n" \
  "v_mfma_f32_16x16x32_bf16 v[152:155], v[96:99], v[104:107], 0\n" \
  "ds_read_b128 v[124:127], v187 offset:64\n" \
  "v_fmac_f32 v176, v156, v76\n" \
  "v_fmac_f32 v176, v157, v77\n" \
  "v_mfma_f32_16x16x32_bf16 v[152:155], v[100:103], v[108:111], v[152:155]\n" \
  "v_fmac_f32 v176, v158, v78\n" \
  "v_fmac_f32 v176, v159, v79\n" \
  "v_mfma_f32_16x16x32_bf16 v[156:159], v[104:107], v[104:107], 0\n" \
  "ds_read_b128 v[104:107], v184 offset:0\n" \
  "v_fmac_f32 v176, v160, v80\n" \
  "v_fmac_f32 v176, v161, v81\n" \
  "v_mfma_f32_16x16x32_bf16 v[156:159], v[108:111], v[108:111], v[156:159]\n" \
  "s_sub_u32 s51, s51, 1\n" \
  "v_fmac_f32 v176, v162, v82\n" \
  "v_fmac_f32 v176, v163, v83\n" \
  "v_mad_u32_u16 v186, v183, 16, v189\n" \
  "v_mfma_f32_16x16x32_bf16 v[160:163], v[148:151], v[144:147], 0\n" \
  "s_cmp_lg_u32 s51, 0\n" \
  "ds_read_b128 v[108:111], v184 offset:64\n" \
  "v_fmac_f32 v176, v152, v84\n" \
  "v_fmac_f32 v176, v153, v85\n" \
  "v_mfma_f32_16x16x32_bf16 v[160:163], v[96:99], v[96:99], v[160:163]\n" \
  "s_waitcnt lgkmcnt(8)\n" \
  "ds_read_b128 v[96:99], v185 offset:0\n" \
  "v_fmac_f32 v176, v154, v86\n" \
  "v_fmac_f32 v176, v155, v87\n" \
  "v_mfma_f32_16x16x32_bf16 v[160:163], v[100:103], v[100:103], v[160:163]\n" \
  "s_cbranch_scc1 LA_loop\n" \
  "s_setpc_b64 s[52:53]\n" \
  "LB_start:\n" \
  "s_setprio 2\n" \
  "s_lshl_b32 s35, s31, 7\n" \
  "s_waitcnt lgkmcnt(0)\n" \
  "s_add_u32 s40, s10, s35\n" \
  "s_addc_u32 s41, s11, 0\n" \
  "s_add_u32 s37, s36, 32\n" \
  "L3_:\n" \
  "s_sub_u32 s35, s36, 125\n" \
  "s_min_u32 s35, s35, s36\n" \
  "s_mul_i32 m0, s35, 1040\n" \
  "s_lshl_b32 s35, s35, 11\n" \
  "s_add_u32 s42, s40, s35\n" \
  "s_addc_u32 s43, s41, 0\n" \
  "global_load_lds_dwordx4 v254, s[42:43]\n" \
  "s_add_u32 s36, s36, 4\n" \
  "s_sub_u32 s35, s36, 125\n" \
  "s_min_u32 s35, s35, s36\n" \
  "s_mul_i32 m0, s35, 1040\n" \
  "s_lshl_b32 s35, s35, 11\n" \
  "s_add_u32 s42, s40, s35\n" \
  "s_addc_u32 s43, s41, 0\n" \
  "global_load_lds_dwordx4 v254, s[42:43]\n" \
  "s_add_u32 s36, s36, 4\n" \
  "s_cmp_lg_u32 s37, s36\n" \
  "s_cbranch_scc1 L3_\n" \
  "v_mov_b32 v190, 163328\n" \
  "v_mov_b64 v[2:3], 0\n" \
  "ds_write_b64 v190, v[2:3]\n" \
  "ds_write_b64 v190, v[2:3] offset:8\n" \
  "ds_write_b64 v190, v[2:3] offset:16\n" \
  "s_waitcnt lgkmcnt(0)\n" \
  "s_barrier\n" \
  "v_lshlrev_b32 v247, 7, v255\n" \
  "s_add_u32 s37, s36, 96\n" \
  "L4_:\n" \
  "s_sub_u32 s35, s36, 125\n" \
  "s_min_u32 s35, s35, s36\n" \
  "s_mul_i32 m0, s35, 1040\n" \
  "s_lshl_b32 s35, s35, 11\n" \
  "s_add_u32 s42, s40, s35\n" \
  "s_addc_u32 s43, s41, 0\n" \
  "global_load_lds_dwordx4 v254, s[42:43]\n" \
  "s_add_u32 s36, s36, 4\n" \
  "s_sub_u32 s35, s36, 125\n" \
  "s_min_u32 s35, s35, s36\n" \
  "s_mul_i32 m0, s35, 1040\n" \
  "s_lshl_b32 s35, s35, 11\n" \
  "s_add_u32 s42, s40, s35\n" \
  "s_addc_u32 s43, s41, 0\n" \
  "global_load_lds_dwordx4 v254, s[42:43]\n" \
  "s_add_u32 s36, s36, 4\n" \
  "s_cmp_lg_u32 s37, s36\n" \
  "s_cbranch_scc1 L4_\n" \
  "s_add_u32 s48, s24, 110002176\n" \
  "s_addc_u32 s49, s25, 0\n" \
  "s_lshl_b32 s35, s26, 2\n" \
  "v_mov_b32 v193, s35\n" \
  "global_load_dword v193, v193, s[48:49] offset:1024 sc1\n" \
  "v_mov_b32 v191, 1\n" \
  "v_and_b32 v252, 15, v255\n" \
  "v_lshrrev_b32 v253, 4, v255\n" \
  "s_lshl2_add_u32 s35, s31, s28\n" \
  "s_lshl_b32 s35, s35, 1\n" \
  "v_bfe_u32 v1, v255, 2, 1\n" \
  "v_add_u32 v1, s35, v1\n" \
  "v_and_b32 v2, 3, v255\n" \
  "v_lshlrev_b32 v3, 5, v1\n" \
  "v_lshl_add_u32 v3, v2, 3, v3\n" \
  "v_lshlrev_b32 v4, 2, v2\n" \
  "v_add_u32 v4, 380, v4\n" \
  "v_cmp_gt_u32 vcc, 12, v1\n" \
  "v_lshrrev_b32 v5, 3, v255\n" \
  "v_mul_u32_u24 v5, 6400, v5\n" \
  "v_cndmask_b32 v3, v4, v3, vcc\n" \
  "v_add_u32 v5, 3200, v5\n" \
  "v_add_u32 v227, v5, v3\n" \
  "v_lshl_add_u32 v3, v1, 2, v2\n" \
  "v_add_u32 v4, 48, v3\n" \
  "v_mul_u32_u24 v5, 205, v3\n" \
  "v_lshrrev_b32 v5, 11, v5\n" \
  "v_mul_u32_u24 v5, 10, v5\n" \
  "v_sub_u32 v5, v3, v5\n" \
  "v_lshlrev_b32 v236, 26, v5\n" \
  "v_mul_u32_u24 v5, 205, v4\n" \
  "v_lshrrev_b32 v5, 11, v5\n" \
  "v_mul_u32_u24 v5, 10, v5\n" \
  "v_sub_u32 v5, v4, v5\n" \
  "v_lshlrev_b32 v237, 26, v5\n" \
  "s_lshl2_add_u32 s4, s31, s28\n" \
  "s_lshl_b32 s4, s4, 7\n" \
  "s_add_u32 s36, s4, 96\n" \
  "s_min_u32 s36, s36, 984\n" \
  "s_add_u32 s37, s4, 112\n" \
  "s_min_u32 s37, s37, 984\n" \
  "v_add_u32 v186, s4, v252\n" \
  "v_add_u32 v187, s36, v252\n" \
  "v_add_u32 v188, s37, v252\n" \
  "s_mul_i32 s38, s7, 51200\n" \
  "s_add_u32 s42, s8, s38\n" \
  "s_addc_u32 s43, s9, 0\n" \
  "s_xor_b32 s38, s31, 1\n" \
  "s_lshl_b32 s38, s38, 7\n" \
  "s_add_u32 s94, s10, s38\n" \
  "s_addc_u32 s95, s11, 0\n" \
  "s_mul_i32 s5, s28, 4224\n" \
  "s_add_u32 s5, s5, 146432\n" \
  "v_lshrrev_b32 v2, 3, v186\n" \
  "v_lshl_add_u32 v2, v186, 3, v2\n" \
  "v_add_lshl_u32 v249, v2, v253, 4\n" \
  "v_lshrrev_b32 v2, 3, v187\n" \
  "v_lshl_add_u32 v2, v187, 3, v2\n" \
  "v_add_lshl_u32 v250, v2, v253, 4\n" \
  "v_lshrrev_b32 v2, 3, v188\n" \
  "v_lshl_add_u32 v2, v188, 3, v2\n" \
  "v_add_lshl_u32 v251, v2, v253, 4\n" \
  "v_mul_u32_u24 v1, 1600000, v253\n" \
  "v_lshl_add_u32 v186, v186, 4, v1\n" \
  "v_lshl_add_u32 v187, v187, 4, v1\n" \
  "v_lshl_add_u32 v188, v188, 4, v1\n" \
  "v_lshlrev_b32 v1, 4, v253\n" \
  "v_xor_b32 v244, v1, v254\n" \
  "v_lshl_add_u32 v245, v255, 4, s5\n" \
  "v_lshrrev_b32 v1, 1, v252\n" \
  "s_waitcnt vmcnt(1)\n" \
  "s_mov_b64 exec, 1\n" \
  "ds_add_u32 v190, v191 offset:12\n" \
  "s_mov_b64 exec, -1\n" \
  "s_memtime s[88:89]\n" \
  "v_xor_b32 v1, v1, v253\n" \
  "v_lshlrev_b32 v1, 4, v1\n" \
  "v_lshl_add_u32 v1, v252, 7, v1\n" \
  "v_add_u32 v246, s5, v1\n" \
  "v_xor_b32 v1, 64, v1\n" \
  "v_add_u32 v242, s5, v1\n" \
  "v_lshlrev_b32 v1, 4, v255\n" \
  "v_xor_b32 v1, 64, v1\n" \
  "v_add_u32 v243, s5, v1\n" \
  "v_lshrrev_b32 v1, 2, v255\n" \
  "s_lshl_b32 s35, s28, 5\n" \
  "s_lshl_b32 s36, s31, 6\n" \
  "s_add_u32 s35, s35, s36\n" \
  "s_and_b32 s35, s35, 127\n" \
  "v_add_u32 v1, s35, v1\n" \
  "v_lshrrev_b32 v2, 4, v255\n" \
  "v_and_b32 v2, 2, v2\n" \
  "v_xor_b32 v2, v2, v255\n" \
  "v_and_b32 v2, 3, v2\n" \
  "v_lshlrev_b32 v2, 4, v2\n" \
  "v_lshl_add_u32 v189, v1, 10, v2\n" \
  "v_add_u32 v194, 15360, v189\n" \
  "v_lshrrev_b32 v1, 2, v252\n" \
  "v_lshlrev_b32 v1, 6, v1\n" \
  "v_lshl_add_u32 v1, v253, 9, v1\n" \
  "v_and_b32 v2, 3, v252\n" \
  "v_lshl_add_u32 v1, v2, 3, v1\n" \
  "v_and_b32 v2, 1, v253\n" \
  "v_lshl_add_u32 v1, v2, 5, v1\n" \
  "v_add_u32 v184, 138240, v1\n" \
  "v_xor_b32 v1, 32, v1\n" \
  "v_add_u32 v185, 138240, v1\n" \
  "s_bfe_u32 s35, s30, 0x10001\n" \
  "s_lshl1_add_u32 s35, s29, s35\n" \
  "s_lshr_b32 s6, s30, 2\n" \
  "s_lshl_b32 s6, s6, 18\n" \
  "s_lshl_b32 s38, s35, 6\n" \
  "s_add_u32 s92, s14, s38\n" \
  "s_addc_u32 s93, s15, 0\n" \
  "s_lshr_b32 s37, s30, 3\n" \
  "s_add_u32 s37, s37, 96\n" \
  "s_lshl_b32 s34, s37, 17\n" \
  "s_mul_i32 s35, s35, 400\n" \
  "s_add_u32 s37, s37, s35\n" \
  "s_mul_i32 s37, s37, 16000\n" \
  "s_add_u32 s46, s24, s37\n" \
  "s_addc_u32 s47, s25, 0\n" \
  "s_lshr_b32 s36, s30, 2\n" \
  "s_add_u32 s36, s36, s35\n" \
  "s_mul_i32 s36, s36, 16000\n" \
  "s_add_u32 s44, s24, s36\n" \
  "s_addc_u32 s45, s25, 0\n" \
  "s_lshl_b32 s96, s28, 11\n" \
  "s_add_u32 s96, s96, 138240\n" \
  "v_lshlrev_b32 v238, 4, v255\n" \
  "v_mov_b32 v234, 0\n" \
  "s_lshl2_add_u32 s33, s26, s28\n" \
  "s_mov_b32 s52, 0x10002\n" \
  "s_mov_b32 s53, 0xc0c0400\n" \
  "s_mov_b32 s54, 0x4000c0c\n" \
  "s_mov_b32 s55, 0x3030303\n" \
  "s_mov_b32 s56, 0x55555555\n" \
  "s_mov_b32 s38, 0x0\n" \
  "s_mov_b32 s98, 0x0\n" \
  "s_mov_b32 s99, 0x8\n" \
  "s_mov_b32 s97, 0x2000\n" \
  "v_xor_b32 v245, 64, v244\n" \
  "v_add_u32 v245, 0x400, v245\n" \
  "s_setprio 0\n" \
  "L5_:\n" \
  "ds_read_b32 v192, v190 offset:16\n" \
  "s_waitcnt lgkmcnt(0)\n" \
  "v_readfirstlane_b32 s35, v192\n" \
  "s_cmp_lt_u32 s35, 4\n" \
  "s_cbranch_scc1 L5_\n" \
  "s_add_u32 s35, s4, 0\n" \
  "s_min_u32 s35, s35, 984\n" \
  "s_lshl_b32 s35, s35, 8\n" \
  "s_add_u32 m0, s5, 0\n" \
  "s_add_u32 s58, s94, s35\n" \
  "s_addc_u32 s59, s95, 0\n" \
  "global_load_lds_dwordx4 v244, s[58:59]\n" \
  "global_load_lds_dwordx4 v245, s[58:59] offset:1024\n" \
  "s_add_u32 s35, s4, 16\n" \
  "s_min_u32 s35, s35, 984\n" \
  "s_lshl_b32 s35, s35, 8\n" \
  "s_add_u32 m0, s5, 2048\n" \
  "s_add_u32 s58, s94, s35\n" \
  "s_addc_u32 s59, s95, 0\n" \
  "global_load_lds_dwordx4 v244, s[58:59]\n" \
  "global_load_lds_dwordx4 v245, s[58:59] offset:1024\n" \
  "s_add_u32 s40, s92, s6\n" \
  "s_addc_u32 s41, s93, 0\n" \
  "s_add_u32 s6, s6, 2097152\n" \
  "s_mov_b32 m0, s96\n" \
  "s_nop 0\n" \
  "global_load_lds_dwordx4 v189, s[40:41]\n" \
  "global_load_lds_dwordx4 v194, s[40:41] offset:1024\n" \
  "L6_:\n" \
  "ds_read_b32 v192, v190 offset:12\n" \
  "s_waitcnt lgkmcnt(0)\n" \
  "v_readfirstlane_b32 s35, v192\n" \
  "s_cmp_lt_u32 s35, 4\n" \
  "s_cbranch_scc1 L6_\n" \
  "s_waitcnt vmcnt(2)\n" \
  "ds_read_b128 v[8:11], v246 offset:0\n" \
  "ds_read_b128 v[12:15], v242 offset:0\n" \
  "ds_read_b128 v[24:27], v246 offset:2048\n" \
  "ds_read_b128 v[28:31], v242 offset:2048\n" \
  "ds_read_b128 v[0:3], v249 offset:0\n" \
  "ds_read_b128 v[4:7], v249 offset:64\n" \
  "ds_read_b128 v[16:19], v249 offset:2080\n" \
  "ds_read_b128 v[20:23], v249 offset:2144\n" \
  "s_waitcnt lgkmcnt(4)\n" \
  "global_load_dwordx2 v[196:197], v227, s[42:43] offset:-3200\n" \
  "global_load_dwordx2 v[198:199], v227, s[42:43] offset:-2800\n" \
  "global_load_dwordx2 v[200:201], v227, s[42:43] offset:-2400\n" \
  "global_load_dwordx2 v[202:203], v227, s[42:43] offset:-1600\n" \
  "global_load_dwordx2 v[204:205], v227, s[42:43] offset:-1200\n" \
  "global_load_dwordx2 v[206:207], v227, s[42:43] offset:-800\n" \
  "global_load_dwordx2 v[208:209], v227, s[42:43] offset:-400\n" \
  "global_load_dwordx2 v[210:211], v227, s[42:43] offset:0\n" \
  "global_load_dwordx2 v[212:213], v227, s[42:43] offset:400\n" \
  "global_load_dwordx2 v[214:215], v227, s[42:43] offset:800\n" \
  "global_load_dwordx2 v[216:217], v227, s[42:43] offset:1200\n" \
  "global_load_dwordx2 v[218:219], v227, s[42:43] offset:-2000\n" \
  "global_load_dwordx2 v[220:221], v227, s[42:43] offset:1600\n" \
  "global_load_dwordx2 v[222:223], v227, s[42:43] offset:2000\n" \
  "global_load_dwordx2 v[224:225], v227, s[42:43] offset:2400\n" \
  "global_load_dwordx2 v[226:227], v227, s[42:43] offset:2800\n" \
  "s_add_u32 s35, s4, 32\n" \
  "s_min_u32 s35, s35, 984\n" \
  "s_lshl_b32 s35, s35, 8\n" \
  "s_add_u32 m0, s5, 0\n" \
  "s_add_u32 s58, s94, s35\n" \
  "s_addc_u32 s59, s95, 0\n" \
  "global_load_lds_dwordx4 v244, s[58:59]\n" \
  "global_load_lds_dwordx4 v245, s[58:59] offset:1024\n" \
  "s_add_u32 s35, s4, 48\n" \
  "s_min_u32 s35, s35, 984\n" \
  "s_lshl_b32 s35, s35, 8\n" \
  "s_add_u32 m0, s5, 2048\n" \
  "s_add_u32 s58, s94, s35\n" \
  "s_addc_u32 s59, s95, 0\n" \
  "global_load_lds_dwordx4 v244, s[58:59]\n" \
  "global_load_lds_dwordx4 v245, s[58:59] offset:1024\n" \
  "s_waitcnt vmcnt(0)\n" \
  "ds_read_b128 v[40:43], v246 offset:0\n" \
  "ds_read_b128 v[44:47], v242 offset:0\n" \
  "ds_read_b128 v[56:59], v246 offset:2048\n" \
  "ds_read_b128 v[60:63], v242 offset:2048\n" \
  "ds_read_b128 v[32:35], v249 offset:4160\n" \
  "ds_read_b128 v[36:39], v249 offset:4224\n" \
  "ds_read_b128 v[48:51], v249 offset:6240\n" \
  "ds_read_b128 v[52:55], v249 offset:6304\n" \
  "s_waitcnt lgkmcnt(4)\n" \
  "s_lshl2_add_u32 s35, s26, s28\n" \
  "s_mul_i32 s35, s35, 6400\n" \
  "s_add_u32 s35, s35, 0\n" \
  "s_add_u32 s58, s14, s35\n" \
  "s_addc_u32 s59, s15, 0\n" \
  "s_mov_b32 exec_lo, 0xffffffff\n" \
  "s_mov_b32 exec_hi, 0x3ffff\n" \
  "global_load_dword v248, v247, s[58:59]\n" \
  "s_mov_b64 exec, -1\n" \
  "s_add_u32 s35, s4, 64\n" \
  "s_min_u32 s35, s35, 984\n" \
  "s_lshl_b32 s35, s35, 8\n" \
  "s_add_u32 m0, s5, 0\n" \
  "s_add_u32 s58, s94, s35\n" \
  "s_addc_u32 s59, s95, 0\n" \
  "global_load_lds_dwordx4 v244, s[58:59]\n" \
  "global_load_lds_dwordx4 v245, s[58:59] offset:1024\n" \
  "s_add_u32 s35, s4, 80\n" \
  "s_min_u32 s35, s35, 984\n" \
  "s_lshl_b32 s35, s35, 8\n" \
  "s_add_u32 m0, s5, 2048\n" \
  "s_add_u32 s58, s94, s35\n" \
  "s_addc_u32 s59, s95, 0\n" \
  "global_load_lds_dwordx4 v244, s[58:59]\n" \
  "global_load_lds_dwordx4 v245, s[58:59] offset:1024\n" \
  "s_waitcnt vmcnt(0)\n" \
  "ds_read_b128 v[72:75], v246 offset:0\n" \
  "ds_read_b128 v[76:79], v242 offset:0\n" \
  "ds_read_b128 v[88:91], v246 offset:2048\n" \
  "ds_read_b128 v[92:95], v242 offset:2048\n" \
  "ds_read_b128 v[64:67], v249 offset:8320\n" \
  "ds_read_b128 v[68:71], v249 offset:8384\n" \
  "ds_read_b128 v[80:83], v249 offset:10400\n" \
  "ds_read_b128 v[84:87], v249 offset:10464\n" \
  "s_waitcnt lgkmcnt(4)\n" \
  "s_add_u32 s35, s4, 96\n" \
  "s_min_u32 s35, s35, 984\n" \
  "s_lshl_b32 s35, s35, 8\n" \
  "s_add_u32 m0, s5, 0\n" \
  "s_add_u32 s58, s94, s35\n" \
  "s_addc_u32 s59, s95, 0\n" \
  "global_load_lds_dwordx4 v244, s[58:59]\n" \
  "global_load_lds_dwordx4 v245, s[58:59] offset:1024\n" \
  "s_add_u32 s35, s4, 112\n" \
  "s_min_u32 s35, s35, 984\n" \
  "s_lshl_b32 s35, s35, 8\n" \
  "s_add_u32 m0, s5, 2048\n" \
  "s_add_u32 s58, s94, s35\n" \
  "s_addc_u32 s59, s95, 0\n" \
  "global_load_lds_dwordx4 v244, s[58:59]\n" \
  "global_load_lds_dwordx4 v245, s[58:59] offset:1024\n" \
  "s_getpc_b64 s[58:59]\n" \
  "LB_pc:\n" \
  "s_sub_u32 s58, s58, LB_pc-LZ_first\n" \
  "s_subb_u32 s59, s59, 0\n" \
  "s_mul_i32 s35, s28, 6656\n" \
  "v_lshl_add_u32 v195, v255, 7, s35\n" \
  "v_min_u32 v195, LZ_end-LZ_first, v195\n" \
  "s_mov_b32 exec_hi, 0xfffff\n" \
  "global_load_dword v248, v195, s[58:59]\n" \
  "s_mov_b64 exec, -1\n" \
  "s_lshl2_add_u32 s35, s26, s28\n" \
  "s_mul_i32 s35, s35, 6400\n" \
  "s_add_u32 s35, s35, 6553600\n" \
  "s_add_u32 s58, s14, s35\n" \
  "s_addc_u32 s59, s15, 0\n" \
  "s_mov_b32 exec_lo, 0xffffffff\n" \
  "s_mov_b32 exec_hi, 0x3ffff\n" \
  "global_load_dword v248, v247, s[58:59]\n" \
  "s_mov_b64 exec, -1\n" \
  "s_waitcnt vmcnt(2)\n" \
  "ds_read_b128 v[104:107], v246 offset:0\n" \
  "ds_read_b128 v[108:111], v242 offset:0\n" \
  "ds_read_b128 v[120:123], v246 offset:2048\n" \
  "ds_read_b128 v[124:127], v242 offset:2048\n" \
  "ds_read_b128 v[96:99], v250 offset:0\n" \
  "ds_read_b128 v[100:103], v250 offset:64\n" \
  "ds_read_b128 v[112:115], v251 offset:0\n" \
  "ds_read_b128 v[116:119], v251 offset:64\n" \
  "s_waitcnt lgkmcnt(4)\n" \
  "s_waitcnt vmcnt(0)\n" \
  "s_sleep 8\n" \
  "s_setprio 2\n" \
  "s_mul_i32 s36, s28, 2176\n" \
  "s_add_u32 s36, s36, 154624\n" \
  "v_bfe_u32 v228, v255, 3, 1\n" \
  "v_and_b32 v229, 1, v255\n" \
  "v_lshl_add_u32 v230, v228, 1, v229\n" \
  "v_lshl_add_u32 v231, v229, 1, v228\n" \
  "v_xor_b32 v229, 1, v230\n" \
  "s_mov_b32 vcc_lo, 0xcccccccc\n" \
  "s_mov_b32 vcc_hi, vcc_lo\n" \
  "v_cndmask_b32 v228, v230, v231, vcc\n" \
  "v_cndmask_b32 v229, v229, v231, vcc\n" \
  "v_and_b32 v230, 4, v255\n" \
  "v_mul_u32_u24 v230, 272, v230\n" \
  "v_lshl_add_u32 v230, v253, 2, v230\n" \
  "v_and_or_b32 v230, v255, 2, v230\n" \
  "v_add_u32 v230, s36, v230\n" \
  "s_movk_i32 s35, 272\n" \
  "v_mad_u32_u24 v195, v228, s35, v230\n" \
  "v_mad_u32_u24 v235, v229, s35, v230\n" \
  "v_lshl_add_u32 v230, v253, 4, v238\n" \
  "v_add_u32 v254, s36, v230\n" \
  "v_readfirstlane_b32 s39, v193\n" \
  "s_lshl_b32 s35, s26, 2\n" \
  "s_add_u32 s39, s39, 1\n" \
  "v_mov_b32 v193, s35\n" \
  "v_mov_b64 v[240:241], -1\n" \
  "v_mov_b64 v[242:243], -1\n" \
  "s_waitcnt lgkmcnt(0)\n" \
  "s_mov_b64 exec, 1\n" \
  "ds_add_u32 v190, v191 offset:0\n" \
  "s_mov_b64 exec, -1\n" \
  "L7_:\n" \
  "ds_read_b32 v192, v190 offset:0\n" \
  "s_waitcnt lgkmcnt(0)\n" \
  "v_readfirstlane_b32 s35, v192\n" \
  "s_cmp_lt_u32 s35, 4\n" \
  "s_cbranch_scc1 L7_\n" \
  "s_add_u32 s96, s96, 8192\n" \
  "s_add_u32 s40, s92, s6\n" \
  "s_addc_u32 s41, s93, 0\n" \
  "s_add_u32 s6, s6, 2097152\n" \
  "s_mov_b32 m0, s96\n" \
  "s_nop 0\n" \
  "global_load_lds_dwordx4 v189, s[40:41]\n" \
  "global_load_lds_dwordx4 v194, s[40:41] offset:1024\n" \
  "s_sub_u32 s96, s96, 8192\n" \
  "ds_read_b64_tr_b16 v[128:129], v184 offset:0\n" \
  "ds_read_b64_tr_b16 v[130:131], v184 offset:256\n" \
  "ds_read_b64_tr_b16 v[132:133], v184 offset:2048\n" \
  "ds_read_b64_tr_b16 v[134:135], v184 offset:2304\n" \
  "ds_read_b64_tr_b16 v[136:137], v184 offset:4096\n" \
  "ds_read_b64_tr_b16 v[138:139], v184 offset:4352\n" \
  "ds_read_b64_tr_b16 v[140:141], v184 offset:6144\n" \
  "ds_read_b64_tr_b16 v[142:143], v184 offset:6400\n" \
  "ds_read_b64_tr_b16 v[156:157], v185 offset:6144\n" \
  "ds_read_b64_tr_b16 v[158:159], v185 offset:6400\n" \
  "ds_read_b64_tr_b16 v[152:153], v185 offset:4096\n" \
  "ds_read_b64_tr_b16 v[154:155], v185 offset:4352\n" \
  "ds_read_b64_tr_b16 v[148:149], v185 offset:2048\n" \
  "ds_read_b64_tr_b16 v[150:151], v185 offset:2304\n" \
  "ds_read_b64_tr_b16 v[144:145], v185 offset:0\n" \
  "ds_read_b64_tr_b16 v[146:147], v185 offset:256\n" \
  "v_mov_b32 v192, s39\n" \
  "s_cmp_lg_u32 s28, 0\n" \
  "s_cbranch_scc1 LB_nc\n" \
  "s_mov_b64 exec, 1\n" \
  "global_store_dword v193, v192, s[48:49] offset:0 sc1\n" \
  "s_mov_b64 exec, -1\n" \
  "LB_nc:\n" \
  "s_lshr_b32 s35, s30, 2\n" \
  "s_lshl3_add_u32 s35, s29, s35\n" \
  "s_and_b32 s36, s30, 3\n" \
  "s_lshl2_add_u32 s35, s35, s36\n" \
  "s_lshl2_add_u32 s35, s35, s28\n" \
  "s_lshl_b32 s35, s35, 12\n" \
  "s_add_u32 s35, s35, 105676800\n" \
  "s_add_u32 s58, s24, s35\n" \
  "s_addc_u32 s59, s25, 0\n" \
  "global_store_dwordx4 v238, v[240:243], s[58:59] offset:0 sc1\n" \
  "global_store_dwordx4 v238, v[240:243], s[58:59] offset:1024 sc1\n" \
  "global_store_dwordx4 v238, v[240:243], s[58:59] offset:2048 sc1\n" \
  "global_store_dwordx4 v238, v[240:243], s[58:59] offset:3072 sc1\n" \
  "s_lshl2_add_u32 s35, s7, s28\n" \
  "s_lshl_b32 s35, s35, 7\n" \
  "s_lshl_b32 s36, s31, 16\n" \
  "s_add_u32 s35, s35, s36\n" \
  "s_add_u32 s35, s35, 109871104\n" \
  "s_add_u32 s58, s24, s35\n" \
  "s_addc_u32 s59, s25, 0\n" \
  "s_mov_b64 exec, 0xff\n" \
  "global_store_dwordx4 v238, v[240:243], s[58:59] sc1\n" \
  "s_mov_b64 exec, -1\n" \
  "s_cmp_lg_u32 s33, 1021\n" \
  "s_cbranch_scc1 LB_nm\n" \
  "v_mov_b64 v[240:241], 0\n" \
  "v_mov_b64 v[242:243], 0\n" \
  "global_store_dwordx4 v238, v[240:243], s[48:49] offset:2048 sc1\n" \
  "s_nop 1\n" \
  "v_lshlrev_b32 v242, 8, v255\n" \
  "global_store_dwordx2 v242, v[240:241], s[48:49] offset:3072 sc1\n" \
  "v_add_u32 v242, 0x4000, v242\n" \
  "global_store_dwordx2 v242, v[240:241], s[48:49] offset:3072 sc1\n" \
  "v_add_u32 v242, 0x4000, v242\n" \
  "global_store_dwordx2 v242, v[240:241], s[48:49] offset:3072 sc1\n" \
  "v_add_u32 v242, 0x4000, v242\n" \
  "global_store_dwordx2 v242, v[240:241], s[48:49] offset:3072 sc1\n" \
  "v_add_u32 v242, 0x4000, v242\n" \
  "LB_nm:\n" \
  "s_mov_b32 s86, 0x78000b0\n" \
  "s_mov_b32 s87, 0x0\n" \
  "s_or_b32 s86, s86, s87\n" \
  "s_setprio 2\n" \
  "s_memtime s[90:91]\n" \
  "s_memrealtime s[42:43]\n" \
  "LT:\n" \
  "s_cmp_eq_u32 s38, 6\n" \
  "s_cselect_b32 s78, 0x33333333, -1\n" \
  "s_cmp_eq_u32 s38, 7\n" \
  "s_cselect_b32 s78, 0x33333333, s78\n" \
  "s_cmp_eq_u32 s38, 18\n" \
  "s_cselect_b32 s80, 0x33333333, -1\n" \
  "s_cmp_eq_u32 s38, 19\n" \
  "s_cselect_b32 s80, 0x33333333, s80\n" \
  "s_mov_b32 s79, s78\n" \
  "s_mov_b32 s81, s80\n" \
  "s_mov_b32 s37, s38\n" \
  "s_add_u32 s57, s38, 2\n" \
  "s_add_u32 s3, s38, 4\n" \
  "s_add_u32 s38, s38, 6\n" \
  "s_cmp_lt_u32 s57, 32\n" \
  "s_cselect_b32 s57, s57, 37\n" \
  "s_cselect_b32 s3, s3, 37\n" \
  "s_waitcnt lgkmcnt(12)\n" \
  "v_mfma_f32_16x16x32_bf16 v[160:163], v[128:131], v[0:3], 0\n" \
  "s_mov_b64 vcc, s[78:79]\n" \
  "v_cndmask_b32_dpp v234, v234, v234, vcc row_ror:8 row_mask:0xf bank_mask:0xf\n" \
  "v_mfma_f32_16x16x32_bf16 v[160:163], v[132:135], v[4:7], v[160:163]\n" \
  "s_waitcnt lgkmcnt(8)\n" \
  "v_mfma_f32_16x16x32_bf16 v[160:163], v[136:139], v[8:11], v[160:163]\n" \
  "s_set_gpr_idx_on s37, gpr_idx(SRC0)\n" \
  "v_mad_u32_u24 v232, v196, s52, v236\n" \
  "s_set_gpr_idx_off\n" \
  "v_mfma_f32_16x16x32_bf16 v[160:163], v[140:143], v[12:15], v[160:163]\n" \
  "s_waitcnt lgkmcnt(0)\n" \
  "v_lshrrev_b32 v228, v232, v234\n" \
  "v_lshrrev_b32_dpp v229, v232, v234 quad_perm:[2,3,0,1] row_mask:0xf bank_mask:0xf\n" \
  "v_mfma_f32_16x16x32_bf16 v[164:167], v[156:159], v[12:15], 0\n" \
  "v_lshrrev_b32_dpp v230, v232, v234 quad_perm:[1,2,3,0] row_mask:0xf bank_mask:0xf\n" \
  "v_lshrrev_b32_dpp v231, v232, v234 quad_perm:[3,0,1,2] row_mask:0xf bank_mask:0xf\n" \
  "v_mfma_f32_16x16x32_bf16 v[164:167], v[152:155], v[8:11], v[164:167]\n" \
  "v_perm_b32 v228, v229, v228, s53\n" \
  "v_perm_b32 v230, v231, v230, s54\n" \
  "v_mfma_f32_16x16x32_bf16 v[164:167], v[148:151], v[4:7], v[164:167]\n" \
  "v_bitop3_b32 v228, v228, v230, s55 bitop3:0xa8\n" \
  "v_lshl_or_b32 v229, v234, 1, s56\n" \
  "v_mfma_f32_16x16x32_bf16 v[164:167], v[144:147], v[0:3], v[164:167]\n" \
  "v_add_u32_dpp v228, v228, v228 quad_perm:[2,3,0,1] row_mask:0xf bank_mask:0xf\n" \
  "v_min_u32_sdwa v230, v228, v228 dst_sel:DWORD dst_unused:UNUSED_PAD src0_sel:BYTE_0 src1_sel:BYTE_1\n" \
  "v_mfma_f32_16x16x32_bf16 v[172:175], v[144:147], v[16:19], 0\n" \
  "v_min_u32_sdwa v231, v228, v228 dst_sel:DWORD dst_unused:UNUSED_PAD src0_sel:BYTE_2 src1_sel:BYTE_3\n" \
  "v_sub_u32 v230, v230, v231\n" \
  "v_mfma_f32_16x16x32_bf16 v[172:175], v[148:151], v[20:23], v[172:175]\n" \
  "v_cvt_pk_bf16_f32 v176, v160, v161\n" \
  "v_add_u32_dpp v230, v230, v230 quad_perm:[1,0,3,2] row_mask:0xf bank_mask:0xf\n" \
  "v_mfma_f32_16x16x32_bf16 v[172:175], v[152:155], v[24:27], v[172:175]\n" \
  "v_cmp_ge_i32 vcc, 0, v230\n" \
  "v_cvt_pk_bf16_f32 v177, v162, v163\n" \
  "v_mfma_f32_16x16x32_bf16 v[172:175], v[156:159], v[28:31], v[172:175]\n" \
  "v_cndmask_b32_sdwa v228, v228, v228, vcc dst_sel:DWORD dst_unused:UNUSED_PAD src0_sel:WORD_1 src1_sel:WORD_0\n" \
  "v_cvt_pk_bf16_f32 v178, v164, v165\n" \
  "v_mfma_f32_16x16x32_bf16 v[168:171], v[140:143], v[28:31], 0\n" \
  "s_mov_b64 s[50:51], vcc\n" \
  "v_cmp_le_u32_sdwa vcc, v228, v228 src0_sel:BYTE_0 src1_sel:BYTE_1\n" \
  "v_cvt_pk_bf16_f32 v179, v166, v167\n" \
  "v_mfma_f32_16x16x32_bf16 v[168:171], v[136:139], v[24:27], v[168:171]\n" \
  "v_cndmask_b32_dpp v232, v232, v232, vcc quad_perm:[2,3,0,1] row_mask:0xf bank_mask:0xf\n" \
  "v_mfma_f32_16x16x32_bf16 v[168:171], v[132:135], v[20:23], v[168:171]\n" \
  "s_mov_b64 vcc, s[50:51]\n" \
  "v_cndmask_b32_dpp v232, v232, v232, vcc quad_perm:[3,0,1,2] row_mask:0xf bank_mask:0xf\n" \
  "v_lshlrev_b32_e64 v230, v232, 3\n" \
  "v_mfma_f32_16x16x32_bf16 v[168:171], v[128:131], v[16:19], v[168:171]\n" \
  "v_and_or_b32 v234, v229, v230, v234\n" \
  "s_cmp_lg_u32 s33, 1021\n" \
  "s_cbranch_scc0 LT_e\n" \
  "LT_r:\n" \
  "s_waitcnt vmcnt(5)\n" \
  "global_store_dwordx4 v186, v[176:179], s[44:45] offset:0 sc1\n" \
  "v_mfma_f32_16x16x32_bf16 v[160:163], v[128:131], v[32:35], 0\n" \
  "s_set_gpr_idx_on s37, gpr_idx(DST)\n" \
  "v_mov_b32 v196, v232\n" \
  "s_set_gpr_idx_off\n" \
  "v_mfma_f32_16x16x32_bf16 v[160:163], v[132:135], v[36:39], v[160:163]\n" \
  "s_set_gpr_idx_on s57, gpr_idx(SRC0)\n" \
  "v_mad_u32_u24 v232, v196, s52, v236\n" \
  "s_set_gpr_idx_off\n" \
  "v_mfma_f32_16x16x32_bf16 v[160:163], v[136:139], v[40:43], v[160:163]\n" \
  "v_lshrrev_b32 v228, v232, v234\n" \
  "v_lshrrev_b32_dpp v229, v232, v234 quad_perm:[2,3,0,1] row_mask:0xf bank_mask:0xf\n" \
  "v_mfma_f32_16x16x32_bf16 v[160:163], v[140:143], v[44:47], v[160:163]\n" \
  "v_lshrrev_b32_dpp v230, v232, v234 quad_perm:[1,2,3,0] row_mask:0xf bank_mask:0xf\n" \
  "v_lshrrev_b32_dpp v231, v232, v234 quad_perm:[3,0,1,2] row_mask:0xf bank_mask:0xf\n" \
  "v_mfma_f32_16x16x32_bf16 v[164:167], v[156:159], v[44:47], 0\n" \
  "v_perm_b32 v228, v229, v228, s53\n" \
  "v_perm_b32 v230, v231, v230, s54\n" \
  "v_mfma_f32_16x16x32_bf16 v[164:167], v[152:155], v[40:43], v[164:167]\n" \
  "v_bitop3_b32 v228, v228, v230, s55 bitop3:0xa8\n" \
  "v_lshl_or_b32 v229, v234, 1, s56\n" \
  "v_mfma_f32_16x16x32_bf16 v[164:167], v[148:151], v[36:39], v[164:167]\n" \
  "v_cvt_pk_bf16_f32 v182, v172, v173\n" \
  "v_cvt_pk_bf16_f32 v183, v174, v175\n" \
  "v_mfma_f32_16x16x32_bf16 v[164:167], v[144:147], v[32:35], v[164:167]\n" \
  "v_cvt_pk_bf16_f32 v180, v168, v169\n" \
  "v_cvt_pk_bf16_f32 v181, v170, v171\n" \
  "s_waitcnt vmcnt(5)\n" \
  "global_store_dwordx4 v186, v[180:183], s[44:45] offset:256 sc1\n" \
  "v_mfma_f32_16x16x32_bf16 v[172:175], v[144:147], v[48:51], 0\n" \
  "v_add_u32_dpp v228, v228, v228 quad_perm:[2,3,0,1] row_mask:0xf bank_mask:0xf\n" \
  "v_min_u32_sdwa v230, v228, v228 dst_sel:DWORD dst_unused:UNUSED_PAD src0_sel:BYTE_0 src1_sel:BYTE_1\n" \
  "v_mfma_f32_16x16x32_bf16 v[172:175], v[148:151], v[52:55], v[172:175]\n" \
  "v_min_u32_sdwa v231, v228, v228 dst_sel:DWORD dst_unused:UNUSED_PAD src0_sel:BYTE_2 src1_sel:BYTE_3\n" \
  "v_sub_u32 v230, v230, v231\n" \
  "v_mfma_f32_16x16x32_bf16 v[172:175], v[152:155], v[56:59], v[172:175]\n" \
  "v_cvt_pk_bf16_f32 v176, v160, v161\n" \
  "v_add_u32_dpp v230, v230, v230 quad_perm:[1,0,3,2] row_mask:0xf bank_mask:0xf\n" \
  "v_mfma_f32_16x16x32_bf16 v[172:175], v[156:159], v[60:63], v[172:175]\n" \
  "v_cmp_ge_i32 vcc, 0, v230\n" \
  "v_cvt_pk_bf16_f32 v177, v162, v163\n" \
  "v_mfma_f32_16x16x32_bf16 v[168:171], v[140:143], v[60:63], 0\n" \
  "v_cndmask_b32_sdwa v228, v228, v228, vcc dst_sel:DWORD dst_unused:UNUSED_PAD src0_sel:WORD_1 src1_sel:WORD_0\n" \
  "v_cvt_pk_bf16_f32 v178, v164, v165\n" \
  "v_mfma_f32_16x16x32_bf16 v[168:171], v[136:139], v[56:59], v[168:171]\n" \
  "s_mov_b64 s[50:51], vcc\n" \
  "v_cmp_le_u32_sdwa vcc, v228, v228 src0_sel:BYTE_0 src1_sel:BYTE_1\n" \
  "v_cvt_pk_bf16_f32 v179, v166, v167\n" \
  "v_mfma_f32_16x16x32_bf16 v[168:171], v[132:135], v[52:55], v[168:171]\n" \
  "v_cndmask_b32_dpp v232, v232, v232, vcc quad_perm:[2,3,0,1] row_mask:0xf bank_mask:0xf\n" \
  "v_mfma_f32_16x16x32_bf16 v[168:171], v[128:131], v[48:51], v[168:171]\n" \
  "s_mov_b64 vcc, s[50:51]\n" \
  "v_cndmask_b32_dpp v232, v232, v232, vcc quad_perm:[3,0,1,2] row_mask:0xf bank_mask:0xf\n" \
  "v_lshlrev_b32_e64 v230, v232, 3\n" \
  "s_waitcnt vmcnt(5)\n" \
  "global_store_dwordx4 v186, v[176:179], s[44:45] offset:512 sc1\n" \
  "v_mfma_f32_16x16x32_bf16 v[160:163], v[128:131], v[64:67], 0\n" \
  "v_and_or_b32 v234, v229, v230, v234\n" \
  "v_cvt_pk_bf16_f32 v182, v172, v173\n" \
  "v_mfma_f32_16x16x32_bf16 v[160:163], v[132:135], v[68:71], v[160:163]\n" \
  "s_set_gpr_idx_on s57, gpr_idx(DST)\n" \
  "v_mov_b32 v196, v232\n" \
  "s_set_gpr_idx_off\n" \
  "v_mfma_f32_16x16x32_bf16 v[160:163], v[136:139], v[72:75], v[160:163]\n" \
  "s_mov_b64 vcc, s[80:81]\n" \
  "v_cndmask_b32_dpp v234, v234, v234, vcc row_ror:8 row_mask:0xf bank_mask:0xf\n" \
  "v_cvt_pk_bf16_f32 v183, v174, v175\n" \
  "v_mfma_f32_16x16x32_bf16 v[160:163], v[140:143], v[76:79], v[160:163]\n" \
  "s_set_gpr_idx_on s3, gpr_idx(SRC0)\n" \
  "v_mad_u32_u24 v232, v196, s52, v236\n" \
  "s_set_gpr_idx_off\n" \
  "v_mfma_f32_16x16x32_bf16 v[164:167], v[156:159], v[76:79], 0\n" \
  "v_lshrrev_b32 v228, v232, v234\n" \
  "v_lshrrev_b32_dpp v229, v232, v234 quad_perm:[2,3,0,1] row_mask:0xf bank_mask:0xf\n" \
  "v_mfma_f32_16x16x32_bf16 v[164:167], v[152:155], v[72:75], v[164:167]\n" \
  "v_lshrrev_b32_dpp v230, v232, v234 quad_perm:[1,2,3,0] row_mask:0xf bank_mask:0xf\n" \
  "v_lshrrev_b32_dpp v231, v232, v234 quad_perm:[3,0,1,2] row_mask:0xf bank_mask:0xf\n" \
  "v_mfma_f32_16x16x32_bf16 v[164:167], v[148:151], v[68:71], v[164:167]\n" \
  "v_perm_b32 v228, v229, v228, s53\n" \
  "v_perm_b32 v230, v231, v230, s54\n" \
  "v_mfma_f32_16x16x32_bf16 v[164:167], v[144:147], v[64:67], v[164:167]\n" \
  "v_cvt_pk_bf16_f32 v180, v168, v169\n" \
  "v_cvt_pk_bf16_f32 v181, v170, v171\n" \
  "s_waitcnt vmcnt(5)\n" \
  "global_store_dwordx4 v186, v[180:183], s[44:45] offset:768 sc1\n" \
  "v_mfma_f32_16x16x32_bf16 v[172:175], v[144:147], v[80:83], 0\n" \
  "s_mov_b64 exec, 1\n" \
  "ds_add_u32 v190, v191 offset:0\n" \
  "s_mov_b64 exec, -1\n" \
  "v_mfma_f32_16x16x32_bf16 v[172:175], v[148:151], v[84:87], v[172:175]\n" \
  "v_bitop3_b32 v228, v228, v230, s55 bitop3:0xa8\n" \
  "v_lshl_or_b32 v229, v234, 1, s56\n" \
  "v_mfma_f32_16x16x32_bf16 v[172:175], v[152:155], v[88:91], v[172:175]\n" \
  "v_add_u32_dpp v228, v228, v228 quad_perm:[2,3,0,1] row_mask:0xf bank_mask:0xf\n" \
  "v_min_u32_sdwa v230, v228, v228 dst_sel:DWORD dst_unused:UNUSED_PAD src0_sel:BYTE_0 src1_sel:BYTE_1\n" \
  "v_mfma_f32_16x16x32_bf16 v[172:175], v[156:159], v[92:95], v[172:175]\n" \
  "v_min_u32_sdwa v231, v228, v228 dst_sel:DWORD dst_unused:UNUSED_PAD src0_sel:BYTE_2 src1_sel:BYTE_3\n" \
  "v_sub_u32 v230, v230, v231\n" \
  "v_mfma_f32_16x16x32_bf16 v[168:171], v[140:143], v[92:95], 0\n" \
  "v_cvt_pk_bf16_f32 v176, v160, v161\n" \
  "v_add_u32_dpp v230, v230, v230 quad_perm:[1,0,3,2] row_mask:0xf bank_mask:0xf\n" \
  "v_mfma_f32_16x16x32_bf16 v[168:171], v[136:139], v[88:91], v[168:171]\n" \
  "v_cmp_ge_i32 vcc, 0, v230\n" \
  "v_cvt_pk_bf16_f32 v177, v162, v163\n" \
  "v_mfma_f32_16x16x32_bf16 v[168:171], v[132:135], v[84:87], v[168:171]\n" \
  "v_cndmask_b32_sdwa v228, v228, v228, vcc dst_sel:DWORD dst_unused:UNUSED_PAD src0_sel:WORD_1 src1_sel:WORD_0\n" \
  "v_cvt_pk_bf16_f32 v178, v164, v165\n" \
  "v_mfma_f32_16x16x32_bf16 v[168:171], v[128:131], v[80:83], v[168:171]\n" \
  "s_mov_b64 s[50:51], vcc\n" \
  "v_cmp_le_u32_sdwa vcc, v228, v228 src0_sel:BYTE_0 src1_sel:BYTE_1\n" \
  "v_cvt_pk_bf16_f32 v179, v166, v167\n" \
  "s_cmp_eq_u32 s98, 4\n" \
  "s_cselect_b32 s36, 12451840, 0\n" \
  "s_sub_u32 s6, s6, s36\n" \
  "s_cmp_lt_u32 s98, 10\n" \
  "s_cselect_b32 s6, s6, s34\n" \
  "s_add_u32 s40, s92, s6\n" \
  "s_addc_u32 s41, s93, 0\n" \
  "s_add_u32 s6, s6, 2097152\n" \
  "s_waitcnt vmcnt(5)\n" \
  "global_store_dwordx4 v186, v[176:179], s[44:45] offset:1024 sc1\n" \
  "v_mfma_f32_16x16x32_bf16 v[160:163], v[128:131], v[96:99], 0\n" \
  "ds_read_b32 v192, v190\n" \
  "v_add_u32 v184, s97, v184\n" \
  "v_mfma_f32_16x16x32_bf16 v[160:163], v[132:135], v[100:103], v[160:163]\n" \
  "v_add_u32 v185, s97, v185\n" \
  "v_cndmask_b32_dpp v232, v232, v232, vcc quad_perm:[2,3,0,1] row_mask:0xf bank_mask:0xf\n" \
  "v_mfma_f32_16x16x32_bf16 v[160:163], v[136:139], v[104:107], v[160:163]\n" \
  "s_mov_b64 vcc, s[50:51]\n" \
  "v_cndmask_b32_dpp v232, v232, v232, vcc quad_perm:[3,0,1,2] row_mask:0xf bank_mask:0xf\n" \
  "v_lshlrev_b32_e64 v230, v232, 3\n" \
  "v_mfma_f32_16x16x32_bf16 v[160:163], v[140:143], v[108:111], v[160:163]\n" \
  "v_and_or_b32 v234, v229, v230, v234\n" \
  "v_cvt_pk_bf16_f32 v182, v172, v173\n" \
  "v_mfma_f32_16x16x32_bf16 v[164:167], v[156:159], v[108:111], 0\n" \
  "s_set_gpr_idx_on s3, gpr_idx(DST)\n" \
  "v_mov_b32 v196, v232\n" \
  "s_set_gpr_idx_off\n" \
  "v_mfma_f32_16x16x32_bf16 v[164:167], v[152:155], v[104:107], v[164:167]\n" \
  "s_waitcnt lgkmcnt(0)\n" \
  "v_readfirstlane_b32 s35, v192\n" \
  "v_cvt_pk_bf16_f32 v183, v174, v175\n" \
  "v_mfma_f32_16x16x32_bf16 v[164:167], v[148:151], v[100:103], v[164:167]\n" \
  "v_cvt_pk_bf16_f32 v180, v168, v169\n" \
  "v_cvt_pk_bf16_f32 v181, v170, v171\n" \
  "v_mfma_f32_16x16x32_bf16 v[164:167], v[144:147], v[96:99], v[164:167]\n" \
  "s_cmp_lt_u32 s35, s99\n" \
  "s_cbranch_scc1 LT_w\n" \
  "LT_c:\n" \
  "s_mov_b32 m0, s96\n" \
  "s_nop 0\n" \
  "global_load_lds_dwordx4 v189, s[40:41]\n" \
  "global_load_lds_dwordx4 v194, s[40:41] offset:1024\n" \
  "s_add_u32 s96, s96, s97\n" \
  "s_sub_u32 s97, 0, s97\n" \
  "s_add_u32 s99, s99, 4\n" \
  "s_lshl4_add_u32 s35, s28, s98\n" \
  "s_bitcmp1_b64 s[86:87], s35\n" \
  "s_cbranch_scc1 LT_a\n" \
  "LT_s:\n" \
  "s_waitcnt vmcnt(5)\n" \
  "global_store_dwordx4 v186, v[180:183], s[44:45] offset:1280 sc1\n" \
  "v_mfma_f32_16x16x32_bf16 v[168:171], v[128:131], v[112:115], 0\n" \
  "ds_read_b64_tr_b16 v[128:129], v184 offset:0\n" \
  "ds_read_b64_tr_b16 v[130:131], v184 offset:256\n" \
  "v_mfma_f32_16x16x32_bf16 v[168:171], v[132:135], v[116:119], v[168:171]\n" \
  "ds_read_b64_tr_b16 v[132:133], v184 offset:2048\n" \
  "ds_read_b64_tr_b16 v[134:135], v184 offset:2304\n" \
  "v_mfma_f32_16x16x32_bf16 v[168:171], v[136:139], v[120:123], v[168:171]\n" \
  "ds_read_b64_tr_b16 v[136:137], v184 offset:4096\n" \
  "v_cvt_pk_bf16_f32 v176, v160, v161\n" \
  "v_mfma_f32_16x16x32_bf16 v[168:171], v[140:143], v[124:127], v[168:171]\n" \
  "ds_read_b64_tr_b16 v[138:139], v184 offset:4352\n" \
  "v_cvt_pk_bf16_f32 v177, v162, v163\n" \
  "v_mfma_f32_16x16x32_bf16 v[172:175], v[156:159], v[124:127], 0\n" \
  "ds_read_b64_tr_b16 v[140:141], v184 offset:6144\n" \
  "v_cvt_pk_bf16_f32 v178, v164, v165\n" \
  "v_mfma_f32_16x16x32_bf16 v[172:175], v[152:155], v[120:123], v[172:175]\n" \
  "ds_read_b64_tr_b16 v[142:143], v184 offset:6400\n" \
  "v_cvt_pk_bf16_f32 v179, v166, v167\n" \
  "v_mfma_f32_16x16x32_bf16 v[172:175], v[148:151], v[116:119], v[172:175]\n" \
  "ds_read_b64_tr_b16 v[156:157], v185 offset:6144\n" \
  "ds_read_b64_tr_b16 v[158:159], v185 offset:6400\n" \
  "v_mfma_f32_16x16x32_bf16 v[172:175], v[144:147], v[112:115], v[172:175]\n" \
  "ds_read_b64_tr_b16 v[152:153], v185 offset:4096\n" \
  "ds_read_b64_tr_b16 v[154:155], v185 offset:4352\n" \
  "ds_read_b64_tr_b16 v[148:149], v185 offset:2048\n" \
  "ds_read_b64_tr_b16 v[150:151], v185 offset:2304\n" \
  "ds_read_b64_tr_b16 v[144:145], v185 offset:0\n" \
  "ds_read_b64_tr_b16 v[146:147], v185 offset:256\n" \
  "s_waitcnt vmcnt(5)\n" \
  "global_store_dwordx4 v187, v[176:179], s[44:45] offset:0 sc1\n" \
  "s_add_u32 s98, s98, 1\n" \
  "v_cvt_pk_bf16_f32 v180, v168, v169\n" \
  "v_cvt_pk_bf16_f32 v181, v170, v171\n" \
  "v_cvt_pk_bf16_f32 v182, v172, v173\n" \
  "v_cvt_pk_bf16_f32 v183, v174, v175\n" \
  "s_waitcnt vmcnt(5)\n" \
  "global_store_dwordx4 v188, v[180:183], s[44:45] offset:0 sc1\n" \
  "s_add_u32 s44, s44, 128000\n" \
  "s_addc_u32 s45, s45, 0\n" \
  "s_bitcmp0_b32 4160, s98\n" \
  "s_cbranch_scc1 LT\n" \
  "s_cmp_eq_u32 s98, 6\n" \
  "s_cbranch_scc1 LT_m\n" \
  "s_lshr_b32 s35, s30, 2\n" \
  "s_lshl3_add_u32 s35, s29, s35\n" \
  "s_mul_i32 s35, s35, 1600000\n" \
  "s_lshl_b32 s36, s28, 12\n" \
  "s_add_u32 s35, s35, s36\n" \
  "s_add_u32 s40, s24, s35\n" \
  "s_addc_u32 s41, s25, 0\n" \
  "s_bitcmp1_b32 s30, 2\n" \
  "s_cbranch_scc1 LB_l_h\n" \
  "global_load_dwordx4 v[64:67], v238, s[40:41] offset:0 sc1\n" \
  "global_load_dwordx4 v[68:71], v238, s[40:41] offset:1024 sc1\n" \
  "global_load_dwordx4 v[72:75], v238, s[40:41] offset:2048 sc1\n" \
  "global_load_dwordx4 v[76:79], v238, s[40:41] offset:3072 sc1\n" \
  "s_add_u32 s40, s40, 16000\n" \
  "s_addc_u32 s41, s41, 0\n" \
  "global_load_dwordx4 v[80:83], v238, s[40:41] offset:0 sc1\n" \
  "global_load_dwordx4 v[84:87], v238, s[40:41] offset:1024 sc1\n" \
  "global_load_dwordx4 v[88:91], v238, s[40:41] offset:2048 sc1\n" \
  "global_load_dwordx4 v[92:95], v238, s[40:41] offset:3072 sc1\n" \
  "s_add_u32 s40, s40, 16000\n" \
  "s_addc_u32 s41, s41, 0\n" \
  "global_load_dwordx4 v[96:99], v238, s[40:41] offset:0 sc1\n" \
  "global_load_dwordx4 v[100:103], v238, s[40:41] offset:1024 sc1\n" \
  "global_load_dwordx4 v[104:107], v238, s[40:41] offset:2048 sc1\n" \
  "global_load_dwordx4 v[108:111], v238, s[40:41] offset:3072 sc1\n" \
  "s_add_u32 s40, s40, 16000\n" \
  "s_addc_u32 s41, s41, 0\n" \
  "global_load_dwordx4 v[112:115], v238, s[40:41] offset:0 sc1\n" \
  "global_load_dwordx4 v[116:119], v238, s[40:41] offset:1024 sc1\n" \
  "global_load_dwordx4 v[120:123], v238, s[40:41] offset:2048 sc1\n" \
  "global_load_dwordx4 v[124:127], v238, s[40:41] offset:3072 sc1\n" \
  "s_add_u32 s40, s40, 16000\n" \
  "s_addc_u32 s41, s41, 0\n" \
  "s_branch LB_l_e\n" \
  "LB_l_h:\n" \
  "global_load_dwordx4 v[0:3], v238, s[40:41] offset:0 sc1\n" \
  "global_load_dwordx4 v[4:7], v238, s[40:41] offset:1024 sc1\n" \
  "global_load_dwordx4 v[8:11], v238, s[40:41] offset:2048 sc1\n" \
  "global_load_dwordx4 v[12:15], v238, s[40:41] offset:3072 sc1\n" \
  "s_add_u32 s40, s40, 16000\n" \
  "s_addc_u32 s41, s41, 0\n" \
  "global_load_dwordx4 v[16:19], v238, s[40:41] offset:0 sc1\n" \
  "global_load_dwordx4 v[20:23], v238, s[40:41] offset:1024 sc1\n" \
  "global_load_dwordx4 v[24:27], v238, s[40:41] offset:2048 sc1\n" \
  "global_load_dwordx4 v[28:31], v238, s[40:41] offset:3072 sc1\n" \
  "s_add_u32 s40, s40, 16000\n" \
  "s_addc_u32 s41, s41, 0\n" \
  "global_load_dwordx4 v[32:35], v238, s[40:41] offset:0 sc1\n" \
  "global_load_dwordx4 v[36:39], v238, s[40:41] offset:1024 sc1\n" \
  "global_load_dwordx4 v[40:43], v238, s[40:41] offset:2048 sc1\n" \
  "global_load_dwordx4 v[44:47], v238, s[40:41] offset:3072 sc1\n" \
  "s_add_u32 s40, s40, 16000\n" \
  "s_addc_u32 s41, s41, 0\n" \
  "global_load_dwordx4 v[48:51], v238, s[40:41] offset:0 sc1\n" \
  "global_load_dwordx4 v[52:55], v238, s[40:41] offset:1024 sc1\n" \
  "global_load_dwordx4 v[56:59], v238, s[40:41] offset:2048 sc1\n" \
  "global_load_dwordx4 v[60:63], v238, s[40:41] offset:3072 sc1\n" \
  "s_add_u32 s40, s40, 16000\n" \
  "s_addc_u32 s41, s41, 0\n" \
  "LB_l_e:\n" \
  "ds_write_b16_d16_hi v195, v197 offset:0\n" \
  "ds_write_b16_d16_hi v195, v199 offset:16\n" \
  "ds_write_b16_d16_hi v195, v201 offset:32\n" \
  "ds_write_b16_d16_hi v235, v203 offset:64\n" \
  "ds_write_b16_d16_hi v235, v205 offset:80\n" \
  "ds_write_b16_d16_hi v235, v207 offset:96\n" \
  "ds_write_b16_d16_hi v235, v209 offset:112\n" \
  "ds_write_b16_d16_hi v235, v211 offset:128\n" \
  "ds_write_b16_d16_hi v235, v213 offset:144\n" \
  "ds_write_b16_d16_hi v235, v215 offset:160\n" \
  "ds_write_b16_d16_hi v235, v217 offset:176\n" \
  "ds_write_b16_d16_hi v195, v219 offset:48\n" \
  "ds_write_b16_d16_hi v195, v221 offset:192\n" \
  "ds_write_b16_d16_hi v195, v223 offset:208\n" \
  "ds_write_b16_d16_hi v195, v225 offset:224\n" \
  "ds_write_b16_d16_hi v195, v227 offset:240\n" \
  "ds_read_b128 v[244:247], v254 offset:0\n" \
  "ds_read_b128 v[248:251], v254 offset:1088\n" \
  "s_lshr_b32 s35, s7, 5\n" \
  "s_mul_i32 s35, s35, 819200\n" \
  "s_and_b32 s36, s7, 31\n" \
  "s_lshl_b32 s36, s36, 10\n" \
  "s_add_u32 s35, s35, s36\n" \
  "s_lshl_b32 s36, s4, 9\n" \
  "s_add_u32 s35, s35, s36\n" \
  "s_add_u32 s35, s35, 102793216\n" \
  "s_add_u32 s58, s24, s35\n" \
  "s_addc_u32 s59, s25, 0\n" \
  "s_waitcnt lgkmcnt(0)\n" \
  "s_cmp_ge_u32 s4, 896\n" \
  "s_cbranch_scc1 L17_\n" \
  "global_store_dwordx4 v238, v[244:247], s[58:59] sc1\n" \
  "s_cmp_ge_u32 s4, 768\n" \
  "s_cbranch_scc1 L17_\n" \
  "s_add_u32 s58, s58, 32768\n" \
  "s_addc_u32 s59, s59, 0\n" \
  "global_store_dwordx4 v238, v[248:251], s[58:59] sc1\n" \
  "L17_:\n" \
  "s_lshr_b32 s35, s30, 2\n" \
  "s_lshl3_add_u32 s33, s29, s35\n" \
  "s_and_b32 s34, s30, 3\n" \
  "s_lshl_b32 s59, s27, 11\n" \
  "s_mul_i32 s35, s33, 1600000\n" \
  "s_add_u32 s54, s24, s35\n" \
  "s_addc_u32 s55, s25, 0\n" \
  "s_mul_i32 s35, s34, 819200\n" \
  "s_lshl1_add_u32 s35, s59, s35\n" \
  "s_add_u32 s35, s35, 102400000\n" \
  "s_add_u32 s56, s24, s35\n" \
  "s_addc_u32 s57, s25, 0\n" \
  "global_load_dwordx4 v[208:211], v238, s[56:57] offset:0 sc1\n" \
  "global_load_dwordx4 v[212:215], v238, s[56:57] offset:1024 sc1\n" \
  "global_load_dwordx4 v[216:219], v238, s[56:57] offset:2048 sc1\n" \
  "global_load_dwordx4 v[220:223], v238, s[56:57] offset:3072 sc1\n" \
  "s_add_u32 s56, s56, 32768\n" \
  "s_addc_u32 s57, s57, 0\n" \
  "global_load_dwordx4 v[224:227], v238, s[56:57] sc1\n" \
  "global_load_dwordx4 v[228:231], v238, s[56:57] offset:1024 sc1\n" \
  "global_load_dwordx4 v[232:235], v238, s[56:57] offset:2048 sc1\n" \
  "global_load_dwordx4 v[236:239], v238, s[56:57] offset:3072 sc1\n" \
  "s_add_u32 s56, s56, 32768\n" \
  "s_addc_u32 s57, s57, 0\n" \
  "s_bitcmp1_b32 s30, 2\n" \
  "s_cbranch_scc1 LB_t_h\n" \
  "s_waitcnt lgkmcnt(12)\n" \
  "v_mfma_f32_16x16x32_bf16 v[160:163], v[128:131], v[0:3], 0\n" \
  "v_mfma_f32_16x16x32_bf16 v[160:163], v[132:135], v[4:7], v[160:163]\n" \
  "s_waitcnt lgkmcnt(8)\n" \
  "v_mfma_f32_16x16x32_bf16 v[160:163], v[136:139], v[8:11], v[160:163]\n" \
  "v_mfma_f32_16x16x32_bf16 v[160:163], v[140:143], v[12:15], v[160:163]\n" \
  "s_waitcnt lgkmcnt(0)\n" \
  "v_mfma_f32_16x16x32_bf16 v[164:167], v[156:159], v[12:15], 0\n" \
  "v_mfma_f32_16x16x32_bf16 v[164:167], v[152:155], v[8:11], v[164:167]\n" \
  "v_mfma_f32_16x16x32_bf16 v[164:167], v[148:151], v[4:7], v[164:167]\n" \
  "v_mfma_f32_16x16x32_bf16 v[164:167], v[144:147], v[0:3], v[164:167]\n" \
  "v_mfma_f32_16x16x32_bf16 v[172:175], v[144:147], v[16:19], 0\n" \
  "v_mfma_f32_16x16x32_bf16 v[172:175], v[148:151], v[20:23], v[172:175]\n" \
  "v_mfma_f32_16x16x32_bf16 v[172:175], v[152:155], v[24:27], v[172:175]\n" \
  "v_cvt_pk_bf16_f32 v176, v160, v161\n" \
  "v_cvt_pk_bf16_f32 v177, v162, v163\n" \
  "v_mfma_f32_16x16x32_bf16 v[172:175], v[156:159], v[28:31], v[172:175]\n" \
  "v_mfma_f32_16x16x32_bf16 v[168:171], v[140:143], v[28:31], 0\n" \
  "v_mfma_f32_16x16x32_bf16 v[168:171], v[136:139], v[24:27], v[168:171]\n" \
  "v_cvt_pk_bf16_f32 v178, v164, v165\n" \
  "v_cvt_pk_bf16_f32 v179, v166, v167\n" \
  "v_mfma_f32_16x16x32_bf16 v[168:171], v[132:135], v[20:23], v[168:171]\n" \
  "v_mfma_f32_16x16x32_bf16 v[168:171], v[128:131], v[16:19], v[168:171]\n" \
  "s_waitcnt vmcnt(63)\n" \
  "global_store_dwordx4 v186, v[176:179], s[46:47] offset:0 sc1\n" \
  "v_mfma_f32_16x16x32_bf16 v[160:163], v[128:131], v[32:35], 0\n" \
  "v_cvt_pk_bf16_f32 v182, v172, v173\n" \
  "v_cvt_pk_bf16_f32 v183, v174, v175\n" \
  "v_mfma_f32_16x16x32_bf16 v[160:163], v[132:135], v[36:39], v[160:163]\n" \
  "v_mfma_f32_16x16x32_bf16 v[160:163], v[136:139], v[40:43], v[160:163]\n" \
  "v_mfma_f32_16x16x32_bf16 v[160:163], v[140:143], v[44:47], v[160:163]\n" \
  "v_cvt_pk_bf16_f32 v180, v168, v169\n" \
  "v_cvt_pk_bf16_f32 v181, v170, v171\n" \
  "v_mfma_f32_16x16x32_bf16 v[164:167], v[156:159], v[44:47], 0\n" \
  "v_mfma_f32_16x16x32_bf16 v[164:167], v[152:155], v[40:43], v[164:167]\n" \
  "v_mfma_f32_16x16x32_bf16 v[164:167], v[148:151], v[36:39], v[164:167]\n" \
  "v_mfma_f32_16x16x32_bf16 v[164:167], v[144:147], v[32:35], v[164:167]\n" \
  "s_waitcnt vmcnt(63)\n" \
  "global_store_dwordx4 v186, v[180:183], s[46:47] offset:256 sc1\n" \
  "v_mfma_f32_16x16x32_bf16 v[172:175], v[144:147], v[48:51], 0\n" \
  "v_cvt_pk_bf16_f32 v176, v160, v161\n" \
  "v_cvt_pk_bf16_f32 v177, v162, v163\n" \
  "v_mfma_f32_16x16x32_bf16 v[172:175], v[148:151], v[52:55], v[172:175]\n" \
  "v_mfma_f32_16x16x32_bf16 v[172:175], v[152:155], v[56:59], v[172:175]\n" \
  "v_mfma_f32_16x16x32_bf16 v[172:175], v[156:159], v[60:63], v[172:175]\n" \
  "v_cvt_pk_bf16_f32 v178, v164, v165\n" \
  "v_cvt_pk_bf16_f32 v179, v166, v167\n" \
  "v_mfma_f32_16x16x32_bf16 v[168:171], v[140:143], v[60:63], 0\n" \
  "v_mfma_f32_16x16x32_bf16 v[168:171], v[136:139], v[56:59], v[168:171]\n" \
  "v_mfma_f32_16x16x32_bf16 v[168:171], v[132:135], v[52:55], v[168:171]\n" \
  "v_mfma_f32_16x16x32_bf16 v[168:171], v[128:131], v[48:51], v[168:171]\n" \
  "s_waitcnt vmcnt(63)\n" \
  "global_store_dwordx4 v186, v[176:179], s[46:47] offset:512 sc1\n" \
  "v_cvt_pk_bf16_f32 v182, v172, v173\n" \
  "v_cvt_pk_bf16_f32 v183, v174, v175\n" \
  "s_nop 3\n" \
  "v_cvt_pk_bf16_f32 v180, v168, v169\n" \
  "v_cvt_pk_bf16_f32 v181, v170, v171\n" \
  "s_waitcnt vmcnt(63)\n" \
  "global_store_dwordx4 v186, v[180:183], s[46:47] offset:768 sc1\n" \
  "s_branch LB_t_e\n" \
  "LT_w:\n" \
  "L8_:\n" \
  "s_sleep 1\n" \
  "ds_read_b32 v192, v190 offset:0\n" \
  "s_waitcnt lgkmcnt(0)\n" \
  "v_readfirstlane_b32 s35, v192\n" \
  "s_cmp_lt_u32 s35, s99\n" \
  "s_cbranch_scc1 L8_\n" \
  "s_branch LT_c\n" \
  "LT_e:\n" \
  "s_bitcmp1_b32 10, s98\n" \
  "s_cbranch_scc0 LT_r\n" \
  "global_load_dwordx4 v[240:243], v238, s[48:49] offset:0 sc1\n" \
  "s_waitcnt vmcnt(0)\n" \
  "global_store_dwordx4 v238, v[240:243], s[48:49] offset:1024 sc1\n" \
  "s_branch LT_r\n" \
  "LT_a:\n" \
  "s_cmp_lg_u32 s35, 4\n" \
  "s_cbranch_scc1 L9_\n" \
  "global_load_dword v193, v193, s[48:49] offset:1024 sc1\n" \
  "s_branch LT_s\n" \
  "L9_:\n" \
  "s_cmp_lg_u32 s35, 5\n" \
  "s_cbranch_scc1 L10_\n" \
  "s_memtime s[58:59]\n" \
  "s_memrealtime s[40:41]\n" \
  "s_mov_b32 s5, 0x10000\n" \
  "LB_ec:\n" \
  "v_readfirstlane_b32 s35, v193\n" \
  "s_cmp_eq_u32 s35, s39\n" \
  "s_cbranch_scc1 LB_ok\n" \
  "s_lshl_b32 s35, s26, 2\n" \
  "v_mov_b32 v193, s35\n" \
  "s_sleep 8\n" \
  "global_load_dword v193, v193, s[48:49] offset:1024 sc1\n" \
  "s_waitcnt vmcnt(0)\n" \
  "s_sub_u32 s5, s5, 1\n" \
  "s_cbranch_scc0 LB_ec\n" \
  "LB_ok:\n" \
  "s_lshl_b32 s35, s26, 2\n" \
  "v_mov_b32 v244, s35\n" \
  "s_waitcnt lgkmcnt(0)\n" \
  "s_sub_u32 s88, s58, s88\n" \
  "s_sub_u32 s88, 106000, s88\n" \
  "s_sub_u32 s90, s58, s90\n" \
  "s_sub_u32 s42, s40, s42\n" \
  "s_lshl_b32 s40, s40, 4\n" \
  "v_cvt_f32_u32 v245, s90\n" \
  "v_cvt_f32_u32 v246, s42\n" \
  "v_rcp_f32 v245, v245\n" \
  "v_cvt_f32_i32 v247, s88\n" \
  "v_mul_f32 v246, 0x4111eb85, v246\n" \
  "v_mul_f32 v245, v245, v246\n" \
  "v_mul_f32 v245, v245, v247\n" \
  "v_cvt_i32_f32 v245, v245\n" \
  "v_add_u32 v245, s40, v245\n" \
  "v_or_b32 v245, 1, v245\n" \
  "s_mov_b64 exec, 1\n" \
  "global_store_dword v244, v245, s[48:49] offset:2048 sc1\n" \
  "s_mov_b64 exec, -1\n" \
  "s_branch LT_s\n" \
  "L10_:\n" \
  "s_cmp_lg_u32 s35, 7\n" \
  "s_cbranch_scc1 L11_\n" \
  "s_mov_b32 exec_hi, 0\n" \
  "global_atomic_add v196, v191, s[48:49] offset:3072 sc1\n" \
  "s_mov_b64 exec, -1\n" \
  "s_branch LT_s\n" \
  "L11_:\n" \
  "s_cmp_lg_u32 s35, 23\n" \
  "s_cbranch_scc1 L12_\n" \
  "s_lshl_b32 s35, s30, 5\n" \
  "v_lshrrev_b32 v206, 3, v255\n" \
  "v_and_b32 v208, 7, v255\n" \
  "v_lshl_add_u32 v210, v206, 2, s35\n" \
  "v_lshl_add_u32 v212, v208, 2, s35\n" \
  "global_load_dword v202, v210, s[48:49] offset:2048 sc1\n" \
  "global_load_dword v204, v212, s[48:49] offset:2048 sc1\n" \
  "s_branch LT_s\n" \
  "L12_:\n" \
  "s_cmp_lg_u32 s35, 24\n" \
  "s_cbranch_scc1 L13_\n" \
  "s_mov_b32 s5, 0x10000\n" \
  "LB_z:\n" \
  "v_min_u32 v214, v202, v204\n" \
  "v_cmp_eq_u32 s[58:59], 0, v214\n" \
  "s_sub_u32 s5, s5, 1\n" \
  "s_cbranch_scc1 LB_zx\n" \
  "s_cmp_eq_u64 s[58:59], 0\n" \
  "s_cbranch_scc1 LB_zx\n" \
  "s_cmp_lg_u32 s33, 1021\n" \
  "s_cbranch_scc1 LB_n1\n" \
  "global_load_dwordx4 v[240:243], v238, s[48:49] offset:0 sc1\n" \
  "s_waitcnt vmcnt(0)\n" \
  "global_store_dwordx4 v238, v[240:243], s[48:49] offset:1024 sc1\n" \
  "LB_n1:\n" \
  "s_sleep 4\n" \
  "global_load_dword v202, v210, s[48:49] offset:2048 sc1\n" \
  "global_load_dword v204, v212, s[48:49] offset:2048 sc1\n" \
  "s_waitcnt vmcnt(0)\n" \
  "s_branch LB_z\n" \
  "LB_zx:\n" \
  "v_and_or_b32 v202, v202, -8, v206\n" \
  "v_and_or_b32 v204, v204, -8, v208\n" \
  "v_cmp_lt_u32 s[58:59], v204, v202\n" \
  "v_sub_u32 v202, v204, v202\n" \
  "s_lshl_b32 s35, s29, 3\n" \
  "s_lshr_b64 s[40:41], s[58:59], s35\n" \
  "s_and_b32 s40, s40, 0xff\n" \
  "s_bcnt1_i32_b32 s40, s40\n" \
  "s_sub_u32 s40, 7, s40\n" \
  "s_mov_b32 s36, s29\n" \
  "s_bfe_u32 s41, s58, 0x80000\n" \
  "s_bcnt1_i32_b32 s41, s41\n" \
  "s_cmp_eq_u32 s41, s40\n" \
  "s_cselect_b32 s36, 0, s36\n" \
  "s_bfe_u32 s41, s58, 0x80008\n" \
  "s_bcnt1_i32_b32 s41, s41\n" \
  "s_cmp_eq_u32 s41, s40\n" \
  "s_cselect_b32 s36, 1, s36\n" \
  "s_bfe_u32 s41, s58, 0x80010\n" \
  "s_bcnt1_i32_b32 s41, s41\n" \
  "s_cmp_eq_u32 s41, s40\n" \
  "s_cselect_b32 s36, 2, s36\n" \
  "s_bfe_u32 s41, s58, 0x80018\n" \
  "s_bcnt1_i32_b32 s41, s41\n" \
  "s_cmp_eq_u32 s41, s40\n" \
  "s_cselect_b32 s36, 3, s36\n" \
  "s_bfe_u32 s41, s59, 0x80000\n" \
  "s_bcnt1_i32_b32 s41, s41\n" \
  "s_cmp_eq_u32 s41, s40\n" \
  "s_cselect_b32 s36, 4, s36\n" \
  "s_bfe_u32 s41, s59, 0x80008\n" \
  "s_bcnt1_i32_b32 s41, s41\n" \
  "s_cmp_eq_u32 s41, s40\n" \
  "s_cselect_b32 s36, 5, s36\n" \
  "s_bfe_u32 s41, s59, 0x80010\n" \
  "s_bcnt1_i32_b32 s41, s41\n" \
  "s_cmp_eq_u32 s41, s40\n" \
  "s_cselect_b32 s36, 6, s36\n" \
  "s_bfe_u32 s41, s59, 0x80018\n" \
  "s_bcnt1_i32_b32 s41, s41\n" \
  "s_cmp_eq_u32 s41, s40\n" \
  "s_cselect_b32 s36, 7, s36\n" \
  "s_add_u32 s35, s35, s36\n" \
  "v_readlane_b32 s41, v202, s35\n" \
  "s_sub_u32 s35, 0, s41\n" \
  "s_cmp_lt_u32 s40, 4\n" \
  "s_cselect_b32 s41, s35, s41\n" \
  "s_min_u32 s41, s41, 0xffff\n" \
  "s_mul_i32 s41, s41, 39\n" \
  "s_add_u32 s41, s41, 28672\n" \
  "s_lshr_b32 s41, s41, 16\n" \
  "s_min_u32 s41, s41, 4\n" \
  "s_sub_u32 s35, 0, s41\n" \
  "s_cmp_lt_u32 s40, 4\n" \
  "s_cselect_b32 s35, s35, s41\n" \
  "s_lshl4_add_u32 s35, s36, s35\n" \
  "s_add_u32 s35, s35, 132\n" \
  "v_mov_b32 v204, s35\n" \
  "ds_write_b32 v190, v204 offset:4\n" \
  "s_branch LT_s\n" \
  "L13_:\n" \
  "s_cmp_lg_u32 s35, 25\n" \
  "s_cbranch_scc1 L14_\n" \
  "s_mov_b64 exec, 0xff\n" \
  "global_load_dword v200, v198, s[48:49] offset:3072 sc1\n" \
  "s_mov_b64 exec, -1\n" \
  "s_branch LT_s\n" \
  "L14_:\n" \
  "s_cmp_lg_u32 s35, 26\n" \
  "s_cbranch_scc1 L15_\n" \
  "s_mov_b64 s[58:59], vcc\n" \
  "s_mov_b32 s5, 0x10000\n" \
  "LB_p:\n" \
  "s_mov_b64 exec, 0xff\n" \
  "v_cmp_le_u32 vcc, 32, v200\n" \
  "s_mov_b64 exec, -1\n" \
  "s_sub_u32 s5, s5, 1\n" \
  "s_cselect_b32 s35, 0xff, vcc_lo\n" \
  "s_cmp_eq_u32 s35, 0xff\n" \
  "s_cbranch_scc1 LB_p_x\n" \
  "s_cmp_lg_u32 s33, 1021\n" \
  "s_cbranch_scc1 LB_n2\n" \
  "global_load_dwordx4 v[240:243], v238, s[48:49] offset:0 sc1\n" \
  "s_waitcnt vmcnt(0)\n" \
  "global_store_dwordx4 v238, v[240:243], s[48:49] offset:1024 sc1\n" \
  "LB_n2:\n" \
  "s_sleep 2\n" \
  "s_mov_b64 exec, 0xff\n" \
  "global_load_dword v200, v198, s[48:49] offset:3072 sc1\n" \
  "s_mov_b64 exec, -1\n" \
  "s_waitcnt vmcnt(0)\n" \
  "s_branch LB_p\n" \
  "LB_p_x:\n" \
  "s_mov_b64 vcc, s[58:59]\n" \
  "s_branch LT_s\n" \
  "L15_:\n" \
  "s_branch LT_s\n" \
  "LT_m:\n" \
  "ds_write_b16_d16_hi v195, v196 offset:0\n" \
  "ds_write_b16_d16_hi v195, v198 offset:16\n" \
  "ds_write_b16_d16_hi v195, v200 offset:32\n" \
  "ds_write_b16_d16_hi v235, v202 offset:64\n" \
  "ds_write_b16_d16_hi v235, v204 offset:80\n" \
  "ds_write_b16_d16_hi v235, v206 offset:96\n" \
  "ds_write_b16_d16_hi v235, v208 offset:112\n" \
  "ds_write_b16_d16_hi v235, v210 offset:128\n" \
  "ds_write_b16_d16_hi v235, v212 offset:144\n" \
  "ds_write_b16_d16_hi v235, v214 offset:160\n" \
  "ds_write_b16_d16_hi v235, v216 offset:176\n" \
  "ds_write_b16_d16_hi v195, v218 offset:48\n" \
  "ds_write_b16_d16_hi v195, v220 offset:192\n" \
  "ds_write_b16_d16_hi v195, v222 offset:208\n" \
  "ds_write_b16_d16_hi v195, v224 offset:224\n" \
  "ds_write_b16_d16_hi v195, v226 offset:240\n" \
  "ds_read_b128 v[244:247], v254 offset:0\n" \
  "ds_read_b128 v[248:251], v254 offset:1088\n" \
  "s_lshr_b32 s35, s7, 5\n" \
  "s_mul_i32 s35, s35, 819200\n" \
  "s_and_b32 s36, s7, 31\n" \
  "s_lshl_b32 s36, s36, 10\n" \
  "s_add_u32 s35, s35, s36\n" \
  "s_lshl_b32 s36, s4, 9\n" \
  "s_add_u32 s35, s35, s36\n" \
  "s_add_u32 s35, s35, 102400000\n" \
  "s_add_u32 s58, s24, s35\n" \
  "s_addc_u32 s59, s25, 0\n" \
  "s_waitcnt lgkmcnt(0)\n" \
  "s_cmp_ge_u32 s4, 768\n" \
  "s_cbranch_scc1 L16_\n" \
  "global_store_dwordx4 v238, v[244:247], s[58:59] sc1\n" \
  "s_cmp_ge_u32 s4, 768\n" \
  "s_cbranch_scc1 L16_\n" \
  "s_add_u32 s58, s58, 32768\n" \
  "s_addc_u32 s59, s59, 0\n" \
  "global_store_dwordx4 v238, v[248:251], s[58:59] sc1\n" \
  "L16_:\n" \
  "v_mov_b32 v234, 0\n" \
  "v_mov_b32 v236, v237\n" \
  "s_mov_b32 s38, 1\n" \
  "s_lshl_b32 s35, s29, 8\n" \
  "v_lshl_add_u32 v196, v255, 11, s35\n" \
  "s_lshl_b32 s35, s30, 11\n" \
  "v_lshl_add_u32 v198, v255, 8, s35\n" \
  "s_branch LT\n" \
  "LB_t_h:\n" \
  "s_waitcnt lgkmcnt(12)\n" \
  "v_mfma_f32_16x16x32_bf16 v[160:163], v[128:131], v[64:67], 0\n" \
  "v_mfma_f32_16x16x32_bf16 v[160:163], v[132:135], v[68:71], v[160:163]\n" \
  "s_waitcnt lgkmcnt(8)\n" \
  "v_mfma_f32_16x16x32_bf16 v[160:163], v[136:139], v[72:75], v[160:163]\n" \
  "v_mfma_f32_16x16x32_bf16 v[160:163], v[140:143], v[76:79], v[160:163]\n" \
  "s_waitcnt lgkmcnt(0)\n" \
  "v_mfma_f32_16x16x32_bf16 v[164:167], v[156:159], v[76:79], 0\n" \
  "v_mfma_f32_16x16x32_bf16 v[164:167], v[152:155], v[72:75], v[164:167]\n" \
  "v_mfma_f32_16x16x32_bf16 v[164:167], v[148:151], v[68:71], v[164:167]\n" \
  "v_mfma_f32_16x16x32_bf16 v[164:167], v[144:147], v[64:67], v[164:167]\n" \
  "v_mfma_f32_16x16x32_bf16 v[172:175], v[144:147], v[80:83], 0\n" \
  "v_mfma_f32_16x16x32_bf16 v[172:175], v[148:151], v[84:87], v[172:175]\n" \
  "v_mfma_f32_16x16x32_bf16 v[172:175], v[152:155], v[88:91], v[172:175]\n" \
  "v_cvt_pk_bf16_f32 v176, v160, v161\n" \
  "v_cvt_pk_bf16_f32 v177, v162, v163\n" \
  "v_mfma_f32_16x16x32_bf16 v[172:175], v[156:159], v[92:95], v[172:175]\n" \
  "v_mfma_f32_16x16x32_bf16 v[168:171], v[140:143], v[92:95], 0\n" \
  "v_mfma_f32_16x16x32_bf16 v[168:171], v[136:139], v[88:91], v[168:171]\n" \
  "v_cvt_pk_bf16_f32 v178, v164, v165\n" \
  "v_cvt_pk_bf16_f32 v179, v166, v167\n" \
  "v_mfma_f32_16x16x32_bf16 v[168:171], v[132:135], v[84:87], v[168:171]\n" \
  "v_mfma_f32_16x16x32_bf16 v[168:171], v[128:131], v[80:83], v[168:171]\n" \
  "s_waitcnt vmcnt(63)\n" \
  "global_store_dwordx4 v186, v[176:179], s[46:47] offset:1024 sc1\n" \
  "v_mfma_f32_16x16x32_bf16 v[160:163], v[128:131], v[96:99], 0\n" \
  "v_cvt_pk_bf16_f32 v182, v172, v173\n" \
  "v_cvt_pk_bf16_f32 v183, v174, v175\n" \
  "v_mfma_f32_16x16x32_bf16 v[160:163], v[132:135], v[100:103], v[160:163]\n" \
  "v_mfma_f32_16x16x32_bf16 v[160:163], v[136:139], v[104:107], v[160:163]\n" \
  "v_mfma_f32_16x16x32_bf16 v[160:163], v[140:143], v[108:111], v[160:163]\n" \
  "v_cvt_pk_bf16_f32 v180, v168, v169\n" \
  "v_cvt_pk_bf16_f32 v181, v170, v171\n" \
  "v_mfma_f32_16x16x32_bf16 v[164:167], v[156:159], v[108:111], 0\n" \
  "v_mfma_f32_16x16x32_bf16 v[164:167], v[152:155], v[104:107], v[164:167]\n" \
  "v_mfma_f32_16x16x32_bf16 v[164:167], v[148:151], v[100:103], v[164:167]\n" \
  "v_mfma_f32_16x16x32_bf16 v[164:167], v[144:147], v[96:99], v[164:167]\n" \
  "s_waitcnt vmcnt(63)\n" \
  "global_store_dwordx4 v186, v[180:183], s[46:47] offset:1280 sc1\n" \
  "v_mfma_f32_16x16x32_bf16 v[168:171], v[128:131], v[112:115], 0\n" \
  "v_cvt_pk_bf16_f32 v176, v160, v161\n" \
  "v_cvt_pk_bf16_f32 v177, v162, v163\n" \
  "v_mfma_f32_16x16x32_bf16 v[168:171], v[132:135], v[116:119], v[168:171]\n" \
  "v_mfma_f32_16x16x32_bf16 v[168:171], v[136:139], v[120:123], v[168:171]\n" \
  "v_mfma_f32_16x16x32_bf16 v[168:171], v[140:143], v[124:127], v[168:171]\n" \
  "v_cvt_pk_bf16_f32 v178, v164, v165\n" \
  "v_cvt_pk_bf16_f32 v179, v166, v167\n" \
  "v_mfma_f32_16x16x32_bf16 v[172:175], v[156:159], v[124:127], 0\n" \
  "v_mfma_f32_16x16x32_bf16 v[172:175], v[152:155], v[120:123], v[172:175]\n" \
  "v_mfma_f32_16x16x32_bf16 v[172:175], v[148:151], v[116:119], v[172:175]\n" \
  "v_mfma_f32_16x16x32_bf16 v[172:175], v[144:147], v[112:115], v[172:175]\n" \
  "s_waitcnt vmcnt(63)\n" \
  "global_store_dwordx4 v187, v[176:179], s[46:47] offset:0 sc1\n" \
  "v_cvt_pk_bf16_f32 v180, v168, v169\n" \
  "v_cvt_pk_bf16_f32 v181, v170, v171\n" \
  "s_nop 3\n" \
  "v_cvt_pk_bf16_f32 v182, v172, v173\n" \
  "v_cvt_pk_bf16_f32 v183, v174, v175\n" \
  "s_waitcnt vmcnt(63)\n" \
  "global_store_dwordx4 v188, v[180:183], s[46:47] offset:0 sc1\n" \
  "LB_t_e:\n" \
  "s_setprio 2\n" \
  "v_mbcnt_lo_u32_b32 v189, -1, 0\n" \
  "v_mbcnt_hi_u32_b32 v189, -1, v189\n" \
  "v_lshlrev_b32 v188, 4, v189\n" \
  "s_lshl_b32 s35, s28, 12\n" \
  "v_add_u32 v128, s35, v188\n" \
  "v_mov_b32 v129, 1\n" \
  "s_lshr_b32 s35, s30, 4\n" \
  "s_lshl1_add_u32 s35, s29, s35\n" \
  "s_lshl_b32 s35, s35, 6\n" \
  "s_bfe_u32 s37, s30, 0x20002\n" \
  "s_lshl3_add_u32 s35, s37, s35\n" \
  "v_and_b32 v190, 16, v189\n" \
  "v_lshl_add_u32 v190, v190, 1, s35\n" \
  "global_load_dwordx2 v[204:205], v190, s[16:17]\n" \
  "global_load_dwordx2 v[206:207], v190, s[18:19]\n" \
  "v_lshrrev_b32 v180, 1, v189\n" \
  "v_xor_b32 v180, v180, v189\n" \
  "v_and_b32 v180, 8, v180\n" \
  "v_bfe_u32 v181, v189, 2, 1\n" \
  "v_lshlrev_b32_e64 v181, v181, 5\n" \
  "v_lshlrev_b32 v180, v180, v181\n" \
  "v_bfe_u32 v181, v189, 2, 2\n" \
  "v_lshlrev_b32_e64 v181, v181, 1\n" \
  "v_cmp_gt_u32 vcc, 32, v189\n" \
  "v_mov_b32 v191, 0x3f80\n" \
  "v_add_u32 v190, 32000, v188\n" \
  "v_cndmask_b32 v192, v181, v180, vcc\n" \
  "v_bfe_i32 v180, v192, 0, 1\n" \
  "v_and_b32 v180, v180, v191\n" \
  "v_bfe_i32 v181, v192, 1, 1\n" \
  "v_and_b32 v181, v181, v191\n" \
  "v_bfe_i32 v182, v192, 2, 1\n" \
  "v_and_b32 v182, v182, v191\n" \
  "v_bfe_i32 v183, v192, 3, 1\n" \
  "v_and_b32 v183, v183, v191\n" \
  "v_and_b32 v192, 3, v189\n" \
  "v_mul_u32_u24 v192, 0x5555, v192\n" \
  "v_add_u32 v189, 16000, v188\n" \
  "v_add_u32 v191, 48000, v188\n" \
  "s_waitcnt vmcnt(0)\n" \
  "v_lshlrev_b32 v184, 16, v204\n" \
  "v_and_b32 v185, 0xffff0000, v204\n" \
  "v_lshlrev_b32 v186, 16, v205\n" \
  "v_and_b32 v187, 0xffff0000, v205\n" \
  "s_setprio 0\n" \
  "s_barrier\n" \
  "s_cmp_lg_u32 s28, 0\n" \
  "s_cbranch_scc1 LB_nb\n" \
  "s_mov_b32 exec_hi, 0\n" \
  "global_atomic_add v196, v129, s[48:49] offset:3076 sc1\n" \
  "s_mov_b64 exec, -1\n" \
  "LB_nb:\n" \
  "s_bitcmp1_b32 s30, 2\n" \
  "s_cbranch_scc1 LB_w_h\n" \
  "ds_write_b128 v128, v[64:67] offset:0\n" \
  "ds_write_b128 v128, v[68:71] offset:1024\n" \
  "ds_write_b128 v128, v[72:75] offset:2048\n" \
  "ds_write_b128 v128, v[76:79] offset:3072\n" \
  "s_add_u32 s35, s59, 64000\n" \
  "s_add_u32 s54, s54, s35\n" \
  "s_addc_u32 s55, s55, 0\n" \
  "s_add_u32 s58, s59, 0x10000\n" \
  "s_mov_b32 m0, s58\n" \
  "s_add_u32 s35, s58, 0x8000\n" \
  "global_load_lds_dwordx4 v188, s[54:55] sc1\n" \
  "global_load_lds_dwordx4 v188, s[54:55] offset:1024 sc1\n" \
  "ds_write_b128 v128, v[80:83] offset:16384\n" \
  "ds_write_b128 v128, v[84:87] offset:17408\n" \
  "ds_write_b128 v128, v[88:91] offset:18432\n" \
  "ds_write_b128 v128, v[92:95] offset:19456\n" \
  "ds_write_b128 v128, v[96:99] offset:32768\n" \
  "ds_write_b128 v128, v[100:103] offset:33792\n" \
  "ds_write_b128 v128, v[104:107] offset:34816\n" \
  "ds_write_b128 v128, v[108:111] offset:35840\n" \
  "s_add_u32 m0, s58, 0x4000\n" \
  "s_cmp_lt_u32 s35, 0x28000\n" \
  "global_load_lds_dwordx4 v189, s[54:55] sc1\n" \
  "global_load_lds_dwordx4 v189, s[54:55] offset:1024 sc1\n" \
  "s_cselect_b32 s58, s35, s59\n" \
  "s_add_u32 s54, s54, 32000\n" \
  "s_addc_u32 s55, s55, 0\n" \
  "ds_write_b128 v128, v[112:115] offset:49152\n" \
  "ds_write_b128 v128, v[116:119] offset:50176\n" \
  "ds_write_b128 v128, v[120:123] offset:51200\n" \
  "ds_write_b128 v128, v[124:127] offset:52224\n" \
  "s_branch LB_w_e\n" \
  "LB_w_h:\n" \
  "ds_write_b128 v128, v[0:3] offset:0\n" \
  "ds_write_b128 v128, v[4:7] offset:1024\n" \
  "ds_write_b128 v128, v[8:11] offset:2048\n" \
  "ds_write_b128 v128, v[12:15] offset:3072\n" \
  "s_add_u32 s35, s59, 64000\n" \
  "s_add_u32 s54, s54, s35\n" \
  "s_addc_u32 s55, s55, 0\n" \
  "s_add_u32 s58, s59, 0x10000\n" \
  "s_mov_b32 m0, s58\n" \
  "s_add_u32 s35, s58, 0x8000\n" \
  "global_load_lds_dwordx4 v188, s[54:55] sc1\n" \
  "global_load_lds_dwordx4 v188, s[54:55] offset:1024 sc1\n" \
  "ds_write_b128 v128, v[16:19] offset:16384\n" \
  "ds_write_b128 v128, v[20:23] offset:17408\n" \
  "ds_write_b128 v128, v[24:27] offset:18432\n" \
  "ds_write_b128 v128, v[28:31] offset:19456\n" \
  "ds_write_b128 v128, v[32:35] offset:32768\n" \
  "ds_write_b128 v128, v[36:39] offset:33792\n" \
  "ds_write_b128 v128, v[40:43] offset:34816\n" \
  "ds_write_b128 v128, v[44:47] offset:35840\n" \
  "s_add_u32 m0, s58, 0x4000\n" \
  "s_cmp_lt_u32 s35, 0x28000\n" \
  "global_load_lds_dwordx4 v189, s[54:55] sc1\n" \
  "global_load_lds_dwordx4 v189, s[54:55] offset:1024 sc1\n" \
  "s_cselect_b32 s58, s35, s59\n" \
  "s_add_u32 s54, s54, 32000\n" \
  "s_addc_u32 s55, s55, 0\n" \
  "ds_write_b128 v128, v[48:51] offset:49152\n" \
  "ds_write_b128 v128, v[52:55] offset:50176\n" \
  "ds_write_b128 v128, v[56:59] offset:51200\n" \
  "ds_write_b128 v128, v[60:63] offset:52224\n" \
  "LB_w_e:\n" \
  "s_waitcnt lgkmcnt(0)\n" \
  "s_mov_b32 s51, 7\n" \
  "s_setprio 0\n" \
  "s_barrier\n" \
  "LG_start:\n" \
  "v_lshlrev_b32_sdwa v64, 4, v208 dst_sel:DWORD dst_unused:UNUSED_PAD src0_sel:DWORD src1_sel:WORD_0\n" \
  "ds_read_b128 v[68:71], v64\n" \
  "v_mov_b64 v[0:1], v[184:185]\n" \
  "v_lshlrev_b32_sdwa v65, 4, v208 dst_sel:DWORD dst_unused:UNUSED_PAD src0_sel:DWORD src1_sel:WORD_1\n" \
  "ds_read_b128 v[72:75], v65\n" \
  "v_mov_b64 v[2:3], v[186:187]\n" \
  "v_lshlrev_b32_sdwa v66, 4, v209 dst_sel:DWORD dst_unused:UNUSED_PAD src0_sel:DWORD src1_sel:WORD_0\n" \
  "ds_read_b128 v[76:79], v66\n" \
  "v_mov_b64 v[4:5], v[184:185]\n" \
  "v_lshlrev_b32_sdwa v67, 4, v209 dst_sel:DWORD dst_unused:UNUSED_PAD src0_sel:DWORD src1_sel:WORD_1\n" \
  "ds_read_b128 v[80:83], v67\n" \
  "v_mov_b64 v[6:7], v[186:187]\n" \
  "s_mov_b32 m0, s58\n" \
  "s_add_u32 s35, s58, 0x8000\n" \
  "global_load_lds_dwordx4 v188, s[54:55] sc1\n" \
  "global_load_lds_dwordx4 v188, s[54:55] offset:1024 sc1\n" \
  "v_lshlrev_b32_sdwa v64, 4, v210 dst_sel:DWORD dst_unused:UNUSED_PAD src0_sel:DWORD src1_sel:WORD_0\n" \
  "ds_read_b128 v[84:87], v64\n" \
  "v_mov_b64 v[8:9], v[184:185]\n" \
  "v_lshlrev_b32_sdwa v65, 4, v210 dst_sel:DWORD dst_unused:UNUSED_PAD src0_sel:DWORD src1_sel:WORD_1\n" \
  "ds_read_b128 v[88:91], v65\n" \
  "v_mov_b64 v[10:11], v[186:187]\n" \
  "v_lshlrev_b32_sdwa v66, 4, v211 dst_sel:DWORD dst_unused:UNUSED_PAD src0_sel:DWORD src1_sel:WORD_0\n" \
  "ds_read_b128 v[92:95], v66\n" \
  "v_mov_b64 v[12:13], v[184:185]\n" \
  "v_lshlrev_b32_sdwa v67, 4, v211 dst_sel:DWORD dst_unused:UNUSED_PAD src0_sel:DWORD src1_sel:WORD_1\n" \
  "ds_read_b128 v[96:99], v67\n" \
  "v_mov_b64 v[14:15], v[186:187]\n" \
  "s_add_u32 m0, s58, 0x4000\n" \
  "s_cmp_lt_u32 s35, 0x28000\n" \
  "global_load_lds_dwordx4 v189, s[54:55] sc1\n" \
  "global_load_lds_dwordx4 v189, s[54:55] offset:1024 sc1\n" \
  "s_cselect_b32 s58, s35, s59\n" \
  "v_lshlrev_b32_sdwa v64, 4, v212 dst_sel:DWORD dst_unused:UNUSED_PAD src0_sel:DWORD src1_sel:WORD_0\n" \
  "ds_read_b128 v[100:103], v64\n" \
  "v_mov_b64 v[16:17], v[184:185]\n" \
  "v_lshlrev_b32_sdwa v65, 4, v212 dst_sel:DWORD dst_unused:UNUSED_PAD src0_sel:DWORD src1_sel:WORD_1\n" \
  "ds_read_b128 v[104:107], v65\n" \
  "v_mov_b64 v[18:19], v[186:187]\n" \
  "v_lshlrev_b32_sdwa v66, 4, v213 dst_sel:DWORD dst_unused:UNUSED_PAD src0_sel:DWORD src1_sel:WORD_0\n" \
  "ds_read_b128 v[108:111], v66\n" \
  "v_mov_b64 v[20:21], v[184:185]\n" \
  "v_lshlrev_b32_sdwa v67, 4, v213 dst_sel:DWORD dst_unused:UNUSED_PAD src0_sel:DWORD src1_sel:WORD_1\n" \
  "ds_read_b128 v[112:115], v67\n" \
  "v_mov_b64 v[22:23], v[186:187]\n" \
  "s_waitcnt lgkmcnt(10)\n" \
  "v_smfmac_f32_16x16x64_bf16 v[0:3], v[180:183], v[68:75], v192\n" \
  "v_lshlrev_b32_sdwa v64, 4, v214 dst_sel:DWORD dst_unused:UNUSED_PAD src0_sel:DWORD src1_sel:WORD_0\n" \
  "ds_read_b128 v[68:71], v64\n" \
  "v_mov_b64 v[24:25], v[184:185]\n" \
  "v_lshlrev_b32_sdwa v65, 4, v214 dst_sel:DWORD dst_unused:UNUSED_PAD src0_sel:DWORD src1_sel:WORD_1\n" \
  "ds_read_b128 v[72:75], v65\n" \
  "v_mov_b64 v[26:27], v[186:187]\n" \
  "s_mov_b32 m0, s58\n" \
  "s_add_u32 s35, s58, 0x8000\n" \
  "global_load_lds_dwordx4 v190, s[54:55] sc1\n" \
  "global_load_lds_dwordx4 v190, s[54:55] offset:1024 sc1\n" \
  "s_waitcnt lgkmcnt(10)\n" \
  "v_smfmac_f32_16x16x64_bf16 v[4:7], v[180:183], v[76:83], v192\n" \
  "v_lshlrev_b32_sdwa v66, 4, v215 dst_sel:DWORD dst_unused:UNUSED_PAD src0_sel:DWORD src1_sel:WORD_0\n" \
  "ds_read_b128 v[76:79], v66\n" \
  "v_mov_b64 v[28:29], v[184:185]\n" \
  "v_lshlrev_b32_sdwa v67, 4, v215 dst_sel:DWORD dst_unused:UNUSED_PAD src0_sel:DWORD src1_sel:WORD_1\n" \
  "ds_read_b128 v[80:83], v67\n" \
  "v_mov_b64 v[30:31], v[186:187]\n" \
  "s_waitcnt lgkmcnt(10)\n" \
  "v_smfmac_f32_16x16x64_bf16 v[8:11], v[180:183], v[84:91], v192\n" \
  "v_lshlrev_b32_sdwa v64, 4, v216 dst_sel:DWORD dst_unused:UNUSED_PAD src0_sel:DWORD src1_sel:WORD_0\n" \
  "ds_read_b128 v[84:87], v64\n" \
  "v_mov_b64 v[32:33], v[184:185]\n" \
  "v_lshlrev_b32_sdwa v65, 4, v216 dst_sel:DWORD dst_unused:UNUSED_PAD src0_sel:DWORD src1_sel:WORD_1\n" \
  "ds_read_b128 v[88:91], v65\n" \
  "v_mov_b64 v[34:35], v[186:187]\n" \
  "s_add_u32 m0, s58, 0x4000\n" \
  "s_cmp_lt_u32 s35, 0x28000\n" \
  "global_load_lds_dwordx4 v191, s[54:55] sc1\n" \
  "global_load_lds_dwordx4 v191, s[54:55] offset:1024 sc1\n" \
  "s_cselect_b32 s58, s35, s59\n" \
  "s_waitcnt lgkmcnt(10)\n" \
  "v_smfmac_f32_16x16x64_bf16 v[12:15], v[180:183], v[92:99], v192\n" \
  "v_lshlrev_b32_sdwa v66, 4, v217 dst_sel:DWORD dst_unused:UNUSED_PAD src0_sel:DWORD src1_sel:WORD_0\n" \
  "ds_read_b128 v[92:95], v66\n" \
  "v_mov_b64 v[36:37], v[184:185]\n" \
  "v_lshlrev_b32_sdwa v67, 4, v217 dst_sel:DWORD dst_unused:UNUSED_PAD src0_sel:DWORD src1_sel:WORD_1\n" \
  "ds_read_b128 v[96:99], v67\n" \
  "v_mov_b64 v[38:39], v[186:187]\n" \
  "s_waitcnt lgkmcnt(10)\n" \
  "v_smfmac_f32_16x16x64_bf16 v[16:19], v[180:183], v[100:107], v192\n" \
  "v_lshlrev_b32_sdwa v64, 4, v218 dst_sel:DWORD dst_unused:UNUSED_PAD src0_sel:DWORD src1_sel:WORD_0\n" \
  "ds_read_b128 v[100:103], v64\n" \
  "v_mov_b64 v[40:41], v[184:185]\n" \
  "v_lshlrev_b32_sdwa v65, 4, v218 dst_sel:DWORD dst_unused:UNUSED_PAD src0_sel:DWORD src1_sel:WORD_1\n" \
  "ds_read_b128 v[104:107], v65\n" \
  "v_mov_b64 v[42:43], v[186:187]\n" \
  "global_load_dwordx4 v[240:243], v188, s[56:57] sc1\n" \
  "global_load_dwordx4 v[244:247], v188, s[56:57] offset:1024 sc1\n" \
  "s_waitcnt lgkmcnt(10)\n" \
  "v_smfmac_f32_16x16x64_bf16 v[20:23], v[180:183], v[108:115], v192\n" \
  "v_lshlrev_b32_sdwa v66, 4, v219 dst_sel:DWORD dst_unused:UNUSED_PAD src0_sel:DWORD src1_sel:WORD_0\n" \
  "ds_read_b128 v[108:111], v66\n" \
  "v_mov_b64 v[44:45], v[184:185]\n" \
  "v_lshlrev_b32_sdwa v67, 4, v219 dst_sel:DWORD dst_unused:UNUSED_PAD src0_sel:DWORD src1_sel:WORD_1\n" \
  "ds_read_b128 v[112:115], v67\n" \
  "v_mov_b64 v[46:47], v[186:187]\n" \
  "s_waitcnt lgkmcnt(10)\n" \
  "v_smfmac_f32_16x16x64_bf16 v[24:27], v[180:183], v[68:75], v192\n" \
  "v_lshlrev_b32_sdwa v64, 4, v220 dst_sel:DWORD dst_unused:UNUSED_PAD src0_sel:DWORD src1_sel:WORD_0\n" \
  "ds_read_b128 v[68:71], v64\n" \
  "v_mov_b64 v[48:49], v[184:185]\n" \
  "v_lshlrev_b32_sdwa v65, 4, v220 dst_sel:DWORD dst_unused:UNUSED_PAD src0_sel:DWORD src1_sel:WORD_1\n" \
  "ds_read_b128 v[72:75], v65\n" \
  "v_mov_b64 v[50:51], v[186:187]\n" \
  "global_load_dwordx4 v[248:251], v188, s[56:57] offset:2048 sc1\n" \
  "global_load_dwordx4 v[252:255], v188, s[56:57] offset:3072 sc1\n" \
  "s_waitcnt lgkmcnt(10)\n" \
  "v_smfmac_f32_16x16x64_bf16 v[28:31], v[180:183], v[76:83], v192\n" \
  "v_lshlrev_b32_sdwa v66, 4, v221 dst_sel:DWORD dst_unused:UNUSED_PAD src0_sel:DWORD src1_sel:WORD_0\n" \
  "ds_read_b128 v[76:79], v66\n" \
  "v_mov_b64 v[52:53], v[184:185]\n" \
  "v_lshlrev_b32_sdwa v67, 4, v221 dst_sel:DWORD dst_unused:UNUSED_PAD src0_sel:DWORD src1_sel:WORD_1\n" \
  "ds_read_b128 v[80:83], v67\n" \
  "v_mov_b64 v[54:55], v[186:187]\n" \
  "s_waitcnt lgkmcnt(10)\n" \
  "v_smfmac_f32_16x16x64_bf16 v[32:35], v[180:183], v[84:91], v192\n" \
  "v_lshlrev_b32_sdwa v64, 4, v222 dst_sel:DWORD dst_unused:UNUSED_PAD src0_sel:DWORD src1_sel:WORD_0\n" \
  "ds_read_b128 v[84:87], v64\n" \
  "v_mov_b64 v[56:57], v[184:185]\n" \
  "v_lshlrev_b32_sdwa v65, 4, v222 dst_sel:DWORD dst_unused:UNUSED_PAD src0_sel:DWORD src1_sel:WORD_1\n" \
  "ds_read_b128 v[88:91], v65\n" \
  "v_mov_b64 v[58:59], v[186:187]\n" \
  "s_waitcnt lgkmcnt(10)\n" \
  "v_smfmac_f32_16x16x64_bf16 v[36:39], v[180:183], v[92:99], v192\n" \
  "v_lshlrev_b32_sdwa v66, 4, v223 dst_sel:DWORD dst_unused:UNUSED_PAD src0_sel:DWORD src1_sel:WORD_0\n" \
  "ds_read_b128 v[92:95], v66\n" \
  "v_mov_b64 v[60:61], v[184:185]\n" \
  "v_lshlrev_b32_sdwa v67, 4, v223 dst_sel:DWORD dst_unused:UNUSED_PAD src0_sel:DWORD src1_sel:WORD_1\n" \
  "ds_read_b128 v[96:99], v67\n" \
  "v_mov_b64 v[62:63], v[186:187]\n" \
  "s_add_u32 s56, s56, 32768\n" \
  "s_addc_u32 s57, s57, 0\n" \
  "s_add_u32 s54, s54, 64000\n" \
  "s_addc_u32 s55, s55, 0\n" \
  "s_waitcnt lgkmcnt(10)\n" \
  "v_smfmac_f32_16x16x64_bf16 v[40:43], v[180:183], v[100:107], v192\n" \
  "s_waitcnt lgkmcnt(8)\n" \
  "v_smfmac_f32_16x16x64_bf16 v[44:47], v[180:183], v[108:115], v192\n" \
  "s_waitcnt vmcnt(8)\n" \
  "s_barrier\n" \
  "LG_loop:\n" \
  "v_lshlrev_b32_sdwa v64, 4, v224 dst_sel:DWORD dst_unused:UNUSED_PAD src0_sel:DWORD src1_sel:WORD_0\n" \
  "ds_read_b128 v[100:103], v64\n" \
  "v_lshlrev_b32_sdwa v65, 4, v224 dst_sel:DWORD dst_unused:UNUSED_PAD src0_sel:DWORD src1_sel:WORD_1\n" \
  "ds_read_b128 v[104:107], v65\n" \
  "v_lshlrev_b32_sdwa v66, 4, v225 dst_sel:DWORD dst_unused:UNUSED_PAD src0_sel:DWORD src1_sel:WORD_0\n" \
  "ds_read_b128 v[108:111], v66\n" \
  "v_lshlrev_b32_sdwa v67, 4, v225 dst_sel:DWORD dst_unused:UNUSED_PAD src0_sel:DWORD src1_sel:WORD_1\n" \
  "ds_read_b128 v[112:115], v67\n" \
  "s_waitcnt lgkmcnt(10)\n" \
  "v_smfmac_f32_16x16x64_bf16 v[48:51], v[180:183], v[68:75], v192\n" \
  "s_waitcnt lgkmcnt(8)\n" \
  "v_smfmac_f32_16x16x64_bf16 v[52:55], v[180:183], v[76:83], v192\n" \
  "s_waitcnt lgkmcnt(6)\n" \
  "v_smfmac_f32_16x16x64_bf16 v[56:59], v[180:183], v[84:91], v192\n" \
  "s_waitcnt lgkmcnt(4)\n" \
  "v_smfmac_f32_16x16x64_bf16 v[60:63], v[180:183], v[92:99], v192\n" \
  "s_barrier\n" \
  "s_mov_b32 m0, s58\n" \
  "s_add_u32 s35, s58, 0x8000\n" \
  "global_load_lds_dwordx4 v188, s[54:55] sc1\n" \
  "global_load_lds_dwordx4 v188, s[54:55] offset:1024 sc1\n" \
  "v_lshlrev_b32_sdwa v64, 4, v226 dst_sel:DWORD dst_unused:UNUSED_PAD src0_sel:DWORD src1_sel:WORD_0\n" \
  "ds_read_b128 v[68:71], v64\n" \
  "v_lshlrev_b32_sdwa v65, 4, v226 dst_sel:DWORD dst_unused:UNUSED_PAD src0_sel:DWORD src1_sel:WORD_1\n" \
  "ds_read_b128 v[72:75], v65\n" \
  "v_lshlrev_b32_sdwa v66, 4, v227 dst_sel:DWORD dst_unused:UNUSED_PAD src0_sel:DWORD src1_sel:WORD_0\n" \
  "ds_read_b128 v[76:79], v66\n" \
  "v_lshlrev_b32_sdwa v67, 4, v227 dst_sel:DWORD dst_unused:UNUSED_PAD src0_sel:DWORD src1_sel:WORD_1\n" \
  "ds_read_b128 v[80:83], v67\n" \
  "s_add_u32 m0, s58, 0x4000\n" \
  "s_cmp_lt_u32 s35, 0x28000\n" \
  "global_load_lds_dwordx4 v189, s[54:55] sc1\n" \
  "global_load_lds_dwordx4 v189, s[54:55] offset:1024 sc1\n" \
  "s_cselect_b32 s58, s35, s59\n" \
  "v_lshlrev_b32_sdwa v64, 4, v228 dst_sel:DWORD dst_unused:UNUSED_PAD src0_sel:DWORD src1_sel:WORD_0\n" \
  "ds_read_b128 v[84:87], v64\n" \
  "v_lshlrev_b32_sdwa v65, 4, v228 dst_sel:DWORD dst_unused:UNUSED_PAD src0_sel:DWORD src1_sel:WORD_1\n" \
  "ds_read_b128 v[88:91], v65\n" \
  "v_lshlrev_b32_sdwa v66, 4, v229 dst_sel:DWORD dst_unused:UNUSED_PAD src0_sel:DWORD src1_sel:WORD_0\n" \
  "ds_read_b128 v[92:95], v66\n" \
  "v_lshlrev_b32_sdwa v67, 4, v229 dst_sel:DWORD dst_unused:UNUSED_PAD src0_sel:DWORD src1_sel:WORD_1\n" \
  "ds_read_b128 v[96:99], v67\n" \
  "s_waitcnt lgkmcnt(10)\n" \
  "v_smfmac_f32_16x16x64_bf16 v[0:3], v[180:183], v[100:107], v192\n" \
  "v_lshlrev_b32_sdwa v64, 4, v230 dst_sel:DWORD dst_unused:UNUSED_PAD src0_sel:DWORD src1_sel:WORD_0\n" \
  "ds_read_b128 v[100:103], v64\n" \
  "v_lshlrev_b32_sdwa v65, 4, v230 dst_sel:DWORD dst_unused:UNUSED_PAD src0_sel:DWORD src1_sel:WORD_1\n" \
  "ds_read_b128 v[104:107], v65\n" \
  "s_mov_b32 m0, s58\n" \
  "s_add_u32 s35, s58, 0x8000\n" \
  "global_load_lds_dwordx4 v190, s[54:55] sc1\n" \
  "global_load_lds_dwordx4 v190, s[54:55] offset:1024 sc1\n" \
  "s_waitcnt lgkmcnt(10)\n" \
  "v_smfmac_f32_16x16x64_bf16 v[4:7], v[180:183], v[108:115], v192\n" \
  "v_lshlrev_b32_sdwa v66, 4, v231 dst_sel:DWORD dst_unused:UNUSED_PAD src0_sel:DWORD src1_sel:WORD_0\n" \
  "ds_read_b128 v[108:111], v66\n" \
  "v_lshlrev_b32_sdwa v67, 4, v231 dst_sel:DWORD dst_unused:UNUSED_PAD src0_sel:DWORD src1_sel:WORD_1\n" \
  "ds_read_b128 v[112:115], v67\n" \
  "s_waitcnt lgkmcnt(10)\n" \
  "v_smfmac_f32_16x16x64_bf16 v[8:11], v[180:183], v[68:75], v192\n" \
  "v_lshlrev_b32_sdwa v64, 4, v232 dst_sel:DWORD dst_unused:UNUSED_PAD src0_sel:DWORD src1_sel:WORD_0\n" \
  "ds_read_b128 v[68:71], v64\n" \
  "v_lshlrev_b32_sdwa v65, 4, v232 dst_sel:DWORD dst_unused:UNUSED_PAD src0_sel:DWORD src1_sel:WORD_1\n" \
  "ds_read_b128 v[72:75], v65\n" \
  "s_add_u32 m0, s58, 0x4000\n" \
  "s_cmp_lt_u32 s35, 0x28000\n" \
  "global_load_lds_dwordx4 v191, s[54:55] sc1\n" \
  "global_load_lds_dwordx4 v191, s[54:55] offset:1024 sc1\n" \
  "s_cselect_b32 s58, s35, s59\n" \
  "s_waitcnt lgkmcnt(10)\n" \
  "v_smfmac_f32_16x16x64_bf16 v[12:15], v[180:183], v[76:83], v192\n" \
  "v_lshlrev_b32_sdwa v66, 4, v233 dst_sel:DWORD dst_unused:UNUSED_PAD src0_sel:DWORD src1_sel:WORD_0\n" \
  "ds_read_b128 v[76:79], v66\n" \
  "v_lshlrev_b32_sdwa v67, 4, v233 dst_sel:DWORD dst_unused:UNUSED_PAD src0_sel:DWORD src1_sel:WORD_1\n" \
  "ds_read_b128 v[80:83], v67\n" \
  "s_waitcnt lgkmcnt(10)\n" \
  "v_smfmac_f32_16x16x64_bf16 v[16:19], v[180:183], v[84:91], v192\n" \
  "v_lshlrev_b32_sdwa v64, 4, v234 dst_sel:DWORD dst_unused:UNUSED_PAD src0_sel:DWORD src1_sel:WORD_0\n" \
  "ds_read_b128 v[84:87], v64\n" \
  "v_lshlrev_b32_sdwa v65, 4, v234 dst_sel:DWORD dst_unused:UNUSED_PAD src0_sel:DWORD src1_sel:WORD_1\n" \
  "ds_read_b128 v[88:91], v65\n" \
  "global_load_dwordx4 v[208:211], v188, s[56:57] sc1\n" \
  "global_load_dwordx4 v[212:215], v188, s[56:57] offset:1024 sc1\n" \
  "s_waitcnt lgkmcnt(10)\n" \
  "v_smfmac_f32_16x16x64_bf16 v[20:23], v[180:183], v[92:99], v192\n" \
  "v_lshlrev_b32_sdwa v66, 4, v235 dst_sel:DWORD dst_unused:UNUSED_PAD src0_sel:DWORD src1_sel:WORD_0\n" \
  "ds_read_b128 v[92:95], v66\n" \
  "v_lshlrev_b32_sdwa v67, 4, v235 dst_sel:DWORD dst_unused:UNUSED_PAD src0_sel:DWORD src1_sel:WORD_1\n" \
  "ds_read_b128 v[96:99], v67\n" \
  "s_waitcnt lgkmcnt(10)\n" \
  "v_smfmac_f32_16x16x64_bf16 v[24:27], v[180:183], v[100:107], v192\n" \
  "v_lshlrev_b32_sdwa v64, 4, v236 dst_sel:DWORD dst_unused:UNUSED_PAD src0_sel:DWORD src1_sel:WORD_0\n" \
  "ds_read_b128 v[100:103], v64\n" \
  "v_lshlrev_b32_sdwa v65, 4, v236 dst_sel:DWORD dst_unused:UNUSED_PAD src0_sel:DWORD src1_sel:WORD_1\n" \
  "ds_read_b128 v[104:107], v65\n" \
  "global_load_dwordx4 v[216:219], v188, s[56:57] offset:2048 sc1\n" \
  "global_load_dwordx4 v[220:223], v188, s[56:57] offset:3072 sc1\n" \
  "s_waitcnt lgkmcnt(10)\n" \
  "v_smfmac_f32_16x16x64_bf16 v[28:31], v[180:183], v[108:115], v192\n" \
  "v_lshlrev_b32_sdwa v66, 4, v237 dst_sel:DWORD dst_unused:UNUSED_PAD src0_sel:DWORD src1_sel:WORD_0\n" \
  "ds_read_b128 v[108:111], v66\n" \
  "v_lshlrev_b32_sdwa v67, 4, v237 dst_sel:DWORD dst_unused:UNUSED_PAD src0_sel:DWORD src1_sel:WORD_1\n" \
  "ds_read_b128 v[112:115], v67\n" \
  "s_waitcnt lgkmcnt(10)\n" \
  "v_smfmac_f32_16x16x64_bf16 v[32:35], v[180:183], v[68:75], v192\n" \
  "v_lshlrev_b32_sdwa v64, 4, v238 dst_sel:DWORD dst_unused:UNUSED_PAD src0_sel:DWORD src1_sel:WORD_0\n" \
  "ds_read_b128 v[68:71], v64\n" \
  "v_lshlrev_b32_sdwa v65, 4, v238 dst_sel:DWORD dst_unused:UNUSED_PAD src0_sel:DWORD src1_sel:WORD_1\n" \
  "ds_read_b128 v[72:75], v65\n" \
  "s_waitcnt lgkmcnt(10)\n" \
  "v_smfmac_f32_16x16x64_bf16 v[36:39], v[180:183], v[76:83], v192\n" \
  "v_lshlrev_b32_sdwa v66, 4, v239 dst_sel:DWORD dst_unused:UNUSED_PAD src0_sel:DWORD src1_sel:WORD_0\n" \
  "ds_read_b128 v[76:79], v66\n" \
  "v_lshlrev_b32_sdwa v67, 4, v239 dst_sel:DWORD dst_unused:UNUSED_PAD src0_sel:DWORD src1_sel:WORD_1\n" \
  "ds_read_b128 v[80:83], v67\n" \
  "s_add_u32 s56, s56, 32768\n" \
  "s_addc_u32 s57, s57, 0\n" \
  "s_add_u32 s54, s54, 64000\n" \
  "s_addc_u32 s55, s55, 0\n" \
  "s_waitcnt lgkmcnt(10)\n" \
  "v_smfmac_f32_16x16x64_bf16 v[40:43], v[180:183], v[84:91], v192\n" \
  "s_waitcnt lgkmcnt(8)\n" \
  "v_smfmac_f32_16x16x64_bf16 v[44:47], v[180:183], v[92:99], v192\n" \
  "s_waitcnt vmcnt(8)\n" \
  "s_barrier\n" \
  "v_lshlrev_b32_sdwa v64, 4, v240 dst_sel:DWORD dst_unused:UNUSED_PAD src0_sel:DWORD src1_sel:WORD_0\n" \
  "ds_read_b128 v[84:87], v64\n" \
  "v_lshlrev_b32_sdwa v65, 4, v240 dst_sel:DWORD dst_unused:UNUSED_PAD src0_sel:DWORD src1_sel:WORD_1\n" \
  "ds_read_b128 v[88:91], v65\n" \
  "v_lshlrev_b32_sdwa v66, 4, v241 dst_sel:DWORD dst_unused:UNUSED_PAD src0_sel:DWORD src1_sel:WORD_0\n" \
  "ds_read_b128 v[92:95], v66\n" \
  "v_lshlrev_b32_sdwa v67, 4, v241 dst_sel:DWORD dst_unused:UNUSED_PAD src0_sel:DWORD src1_sel:WORD_1\n" \
  "ds_read_b128 v[96:99], v67\n" \
  "s_waitcnt lgkmcnt(10)\n" \
  "v_smfmac_f32_16x16x64_bf16 v[48:51], v[180:183], v[100:107], v192\n" \
  "s_waitcnt lgkmcnt(8)\n" \
  "v_smfmac_f32_16x16x64_bf16 v[52:55], v[180:183], v[108:115], v192\n" \
  "s_waitcnt lgkmcnt(6)\n" \
  "v_smfmac_f32_16x16x64_bf16 v[56:59], v[180:183], v[68:75], v192\n" \
  "s_waitcnt lgkmcnt(4)\n" \
  "v_smfmac_f32_16x16x64_bf16 v[60:63], v[180:183], v[76:83], v192\n" \
  "s_barrier\n" \
  "s_mov_b32 m0, s58\n" \
  "s_add_u32 s35, s58, 0x8000\n" \
  "global_load_lds_dwordx4 v188, s[54:55] sc1\n" \
  "global_load_lds_dwordx4 v188, s[54:55] offset:1024 sc1\n" \
  "v_lshlrev_b32_sdwa v64, 4, v242 dst_sel:DWORD dst_unused:UNUSED_PAD src0_sel:DWORD src1_sel:WORD_0\n" \
  "ds_read_b128 v[100:103], v64\n" \
  "v_lshlrev_b32_sdwa v65, 4, v242 dst_sel:DWORD dst_unused:UNUSED_PAD src0_sel:DWORD src1_sel:WORD_1\n" \
  "ds_read_b128 v[104:107], v65\n" \
  "v_lshlrev_b32_sdwa v66, 4, v243 dst_sel:DWORD dst_unused:UNUSED_PAD src0_sel:DWORD src1_sel:WORD_0\n" \
  "ds_read_b128 v[108:111], v66\n" \
  "v_lshlrev_b32_sdwa v67, 4, v243 dst_sel:DWORD dst_unused:UNUSED_PAD src0_sel:DWORD src1_sel:WORD_1\n" \
  "ds_read_b128 v[112:115], v67\n" \
  "s_add_u32 m0, s58, 0x4000\n" \
  "s_cmp_lt_u32 s35, 0x28000\n" \
  "global_load_lds_dwordx4 v189, s[54:55] sc1\n" \
  "global_load_lds_dwordx4 v189, s[54:55] offset:1024 sc1\n" \
  "s_cselect_b32 s58, s35, s59\n" \
  "v_lshlrev_b32_sdwa v64, 4, v244 dst_sel:DWORD dst_unused:UNUSED_PAD src0_sel:DWORD src1_sel:WORD_0\n" \
  "ds_read_b128 v[68:71], v64\n" \
  "v_lshlrev_b32_sdwa v65, 4, v244 dst_sel:DWORD dst_unused:UNUSED_PAD src0_sel:DWORD src1_sel:WORD_1\n" \
  "ds_read_b128 v[72:75], v65\n" \
  "v_lshlrev_b32_sdwa v66, 4, v245 dst_sel:DWORD dst_unused:UNUSED_PAD src0_sel:DWORD src1_sel:WORD_0\n" \
  "ds_read_b128 v[76:79], v66\n" \
  "v_lshlrev_b32_sdwa v67, 4, v245 dst_sel:DWORD dst_unused:UNUSED_PAD src0_sel:DWORD src1_sel:WORD_1\n" \
  "ds_read_b128 v[80:83], v67\n" \
  "s_waitcnt lgkmcnt(10)\n" \
  "v_smfmac_f32_16x16x64_bf16 v[0:3], v[180:183], v[84:91], v192\n" \
  "v_lshlrev_b32_sdwa v64, 4, v246 dst_sel:DWORD dst_unused:UNUSED_PAD src0_sel:DWORD src1_sel:WORD_0\n" \
  "ds_read_b128 v[84:87], v64\n" \
  "v_lshlrev_b32_sdwa v65, 4, v246 dst_sel:DWORD dst_unused:UNUSED_PAD src0_sel:DWORD src1_sel:WORD_1\n" \
  "ds_read_b128 v[88:91], v65\n" \
  "s_mov_b32 m0, s58\n" \
  "s_add_u32 s35, s58, 0x8000\n" \
  "global_load_lds_dwordx4 v190, s[54:55] sc1\n" \
  "global_load_lds_dwordx4 v190, s[54:55] offset:1024 sc1\n" \
  "s_waitcnt lgkmcnt(10)\n" \
  "v_smfmac_f32_16x16x64_bf16 v[4:7], v[180:183], v[92:99], v192\n" \
  "v_lshlrev_b32_sdwa v66, 4, v247 dst_sel:DWORD dst_unused:UNUSED_PAD src0_sel:DWORD src1_sel:WORD_0\n" \
  "ds_read_b128 v[92:95], v66\n" \
  "v_lshlrev_b32_sdwa v67, 4, v247 dst_sel:DWORD dst_unused:UNUSED_PAD src0_sel:DWORD src1_sel:WORD_1\n" \
  "ds_read_b128 v[96:99], v67\n" \
  "s_waitcnt lgkmcnt(10)\n" \
  "v_smfmac_f32_16x16x64_bf16 v[8:11], v[180:183], v[100:107], v192\n" \
  "v_lshlrev_b32_sdwa v64, 4, v248 dst_sel:DWORD dst_unused:UNUSED_PAD src0_sel:DWORD src1_sel:WORD_0\n" \
  "ds_read_b128 v[100:103], v64\n" \
  "v_lshlrev_b32_sdwa v65, 4, v248 dst_sel:DWORD dst_unused:UNUSED_PAD src0_sel:DWORD src1_sel:WORD_1\n" \
  "ds_read_b128 v[104:107], v65\n" \
  "s_add_u32 m0, s58, 0x4000\n" \
  "s_cmp_lt_u32 s35, 0x28000\n" \
  "global_load_lds_dwordx4 v191, s[54:55] sc1\n" \
  "global_load_lds_dwordx4 v191, s[54:55] offset:1024 sc1\n" \
  "s_cselect_b32 s58, s35, s59\n" \
  "s_waitcnt lgkmcnt(10)\n" \
  "v_smfmac_f32_16x16x64_bf16 v[12:15], v[180:183], v[108:115], v192\n" \
  "v_lshlrev_b32_sdwa v66, 4, v249 dst_sel:DWORD dst_unused:UNUSED_PAD src0_sel:DWORD src1_sel:WORD_0\n" \
  "ds_read_b128 v[108:111], v66\n" \
  "v_lshlrev_b32_sdwa v67, 4, v249 dst_sel:DWORD dst_unused:UNUSED_PAD src0_sel:DWORD src1_sel:WORD_1\n" \
  "ds_read_b128 v[112:115], v67\n" \
  "s_waitcnt lgkmcnt(10)\n" \
  "v_smfmac_f32_16x16x64_bf16 v[16:19], v[180:183], v[68:75], v192\n" \
  "v_lshlrev_b32_sdwa v64, 4, v250 dst_sel:DWORD dst_unused:UNUSED_PAD src0_sel:DWORD src1_sel:WORD_0\n" \
  "ds_read_b128 v[68:71], v64\n" \
  "v_lshlrev_b32_sdwa v65, 4, v250 dst_sel:DWORD dst_unused:UNUSED_PAD src0_sel:DWORD src1_sel:WORD_1\n" \
  "ds_read_b128 v[72:75], v65\n" \
  "global_load_dwordx4 v[224:227], v188, s[56:57] sc1\n" \
  "global_load_dwordx4 v[228:231], v188, s[56:57] offset:1024 sc1\n" \
  "s_waitcnt lgkmcnt(10)\n" \
  "v_smfmac_f32_16x16x64_bf16 v[20:23], v[180:183], v[76:83], v192\n" \
  "v_lshlrev_b32_sdwa v66, 4, v251 dst_sel:DWORD dst_unused:UNUSED_PAD src0_sel:DWORD src1_sel:WORD_0\n" \
  "ds_read_b128 v[76:79], v66\n" \
  "v_lshlrev_b32_sdwa v67, 4, v251 dst_sel:DWORD dst_unused:UNUSED_PAD src0_sel:DWORD src1_sel:WORD_1\n" \
  "ds_read_b128 v[80:83], v67\n" \
  "s_waitcnt lgkmcnt(10)\n" \
  "v_smfmac_f32_16x16x64_bf16 v[24:27], v[180:183], v[84:91], v192\n" \
  "v_lshlrev_b32_sdwa v64, 4, v252 dst_sel:DWORD dst_unused:UNUSED_PAD src0_sel:DWORD src1_sel:WORD_0\n" \
  "ds_read_b128 v[84:87], v64\n" \
  "v_lshlrev_b32_sdwa v65, 4, v252 dst_sel:DWORD dst_unused:UNUSED_PAD src0_sel:DWORD src1_sel:WORD_1\n" \
  "ds_read_b128 v[88:91], v65\n" \
  "global_load_dwordx4 v[232:235], v188, s[56:57] offset:2048 sc1\n" \
  "global_load_dwordx4 v[236:239], v188, s[56:57] offset:3072 sc1\n" \
  "s_waitcnt lgkmcnt(10)\n" \
  "v_smfmac_f32_16x16x64_bf16 v[28:31], v[180:183], v[92:99], v192\n" \
  "v_lshlrev_b32_sdwa v66, 4, v253 dst_sel:DWORD dst_unused:UNUSED_PAD src0_sel:DWORD src1_sel:WORD_0\n" \
  "ds_read_b128 v[92:95], v66\n" \
  "v_lshlrev_b32_sdwa v67, 4, v253 dst_sel:DWORD dst_unused:UNUSED_PAD src0_sel:DWORD src1_sel:WORD_1\n" \
  "ds_read_b128 v[96:99], v67\n" \
  "s_waitcnt lgkmcnt(10)\n" \
  "v_smfmac_f32_16x16x64_bf16 v[32:35], v[180:183], v[100:107], v192\n" \
  "v_lshlrev_b32_sdwa v64, 4, v254 dst_sel:DWORD dst_unused:UNUSED_PAD src0_sel:DWORD src1_sel:WORD_0\n" \
  "ds_read_b128 v[100:103], v64\n" \
  "v_lshlrev_b32_sdwa v65, 4, v254 dst_sel:DWORD dst_unused:UNUSED_PAD src0_sel:DWORD src1_sel:WORD_1\n" \
  "ds_read_b128 v[104:107], v65\n" \
  "s_waitcnt lgkmcnt(10)\n" \
  "v_smfmac_f32_16x16x64_bf16 v[36:39], v[180:183], v[108:115], v192\n" \
  "v_lshlrev_b32_sdwa v66, 4, v255 dst_sel:DWORD dst_unused:UNUSED_PAD src0_sel:DWORD src1_sel:WORD_0\n" \
  "ds_read_b128 v[108:111], v66\n" \
  "v_lshlrev_b32_sdwa v67, 4, v255 dst_sel:DWORD dst_unused:UNUSED_PAD src0_sel:DWORD src1_sel:WORD_1\n" \
  "ds_read_b128 v[112:115], v67\n" \
  "s_add_u32 s56, s56, 32768\n" \
  "s_addc_u32 s57, s57, 0\n" \
  "s_add_u32 s54, s54, 64000\n" \
  "s_addc_u32 s55, s55, 0\n" \
  "s_waitcnt lgkmcnt(10)\n" \
  "v_smfmac_f32_16x16x64_bf16 v[40:43], v[180:183], v[68:75], v192\n" \
  "s_waitcnt lgkmcnt(8)\n" \
  "v_smfmac_f32_16x16x64_bf16 v[44:47], v[180:183], v[76:83], v192\n" \
  "s_waitcnt vmcnt(8)\n" \
  "s_barrier\n" \
  "s_lshl3_add_u32 s36, s27, s51\n" \
  "s_cmp_eq_u32 s36, 37\n" \
  "s_cbranch_scc1 LG_pol\n" \
  "LG_rt0:\n" \
  "v_lshlrev_b32_sdwa v64, 4, v208 dst_sel:DWORD dst_unused:UNUSED_PAD src0_sel:DWORD src1_sel:WORD_0\n" \
  "ds_read_b128 v[68:71], v64\n" \
  "v_lshlrev_b32_sdwa v65, 4, v208 dst_sel:DWORD dst_unused:UNUSED_PAD src0_sel:DWORD src1_sel:WORD_1\n" \
  "ds_read_b128 v[72:75], v65\n" \
  "v_lshlrev_b32_sdwa v66, 4, v209 dst_sel:DWORD dst_unused:UNUSED_PAD src0_sel:DWORD src1_sel:WORD_0\n" \
  "ds_read_b128 v[76:79], v66\n" \
  "v_lshlrev_b32_sdwa v67, 4, v209 dst_sel:DWORD dst_unused:UNUSED_PAD src0_sel:DWORD src1_sel:WORD_1\n" \
  "ds_read_b128 v[80:83], v67\n" \
  "s_waitcnt lgkmcnt(10)\n" \
  "v_smfmac_f32_16x16x64_bf16 v[48:51], v[180:183], v[84:91], v192\n" \
  "s_waitcnt lgkmcnt(8)\n" \
  "v_smfmac_f32_16x16x64_bf16 v[52:55], v[180:183], v[92:99], v192\n" \
  "s_waitcnt lgkmcnt(6)\n" \
  "v_smfmac_f32_16x16x64_bf16 v[56:59], v[180:183], v[100:107], v192\n" \
  "s_waitcnt lgkmcnt(4)\n" \
  "v_smfmac_f32_16x16x64_bf16 v[60:63], v[180:183], v[108:115], v192\n" \
  "s_barrier\n" \
  "s_mov_b32 m0, s58\n" \
  "s_add_u32 s35, s58, 0x8000\n" \
  "global_load_lds_dwordx4 v188, s[54:55] sc1\n" \
  "global_load_lds_dwordx4 v188, s[54:55] offset:1024 sc1\n" \
  "v_lshlrev_b32_sdwa v64, 4, v210 dst_sel:DWORD dst_unused:UNUSED_PAD src0_sel:DWORD src1_sel:WORD_0\n" \
  "ds_read_b128 v[84:87], v64\n" \
  "v_lshlrev_b32_sdwa v65, 4, v210 dst_sel:DWORD dst_unused:UNUSED_PAD src0_sel:DWORD src1_sel:WORD_1\n" \
  "ds_read_b128 v[88:91], v65\n" \
  "v_lshlrev_b32_sdwa v66, 4, v211 dst_sel:DWORD dst_unused:UNUSED_PAD src0_sel:DWORD src1_sel:WORD_0\n" \
  "ds_read_b128 v[92:95], v66\n" \
  "v_lshlrev_b32_sdwa v67, 4, v211 dst_sel:DWORD dst_unused:UNUSED_PAD src0_sel:DWORD src1_sel:WORD_1\n" \
  "ds_read_b128 v[96:99], v67\n" \
  "s_add_u32 m0, s58, 0x4000\n" \
  "s_cmp_lt_u32 s35, 0x28000\n" \
  "global_load_lds_dwordx4 v189, s[54:55] sc1\n" \
  "global_load_lds_dwordx4 v189, s[54:55] offset:1024 sc1\n" \
  "s_cselect_b32 s58, s35, s59\n" \
  "v_lshlrev_b32_sdwa v64, 4, v212 dst_sel:DWORD dst_unused:UNUSED_PAD src0_sel:DWORD src1_sel:WORD_0\n" \
  "ds_read_b128 v[100:103], v64\n" \
  "v_lshlrev_b32_sdwa v65, 4, v212 dst_sel:DWORD dst_unused:UNUSED_PAD src0_sel:DWORD src1_sel:WORD_1\n" \
  "ds_read_b128 v[104:107], v65\n" \
  "v_lshlrev_b32_sdwa v66, 4, v213 dst_sel:DWORD dst_unused:UNUSED_PAD src0_sel:DWORD src1_sel:WORD_0\n" \
  "ds_read_b128 v[108:111], v66\n" \
  "v_lshlrev_b32_sdwa v67, 4, v213 dst_sel:DWORD dst_unused:UNUSED_PAD src0_sel:DWORD src1_sel:WORD_1\n" \
  "ds_read_b128 v[112:115], v67\n" \
  "s_waitcnt lgkmcnt(10)\n" \
  "v_smfmac_f32_16x16x64_bf16 v[0:3], v[180:183], v[68:75], v192\n" \
  "v_lshlrev_b32_sdwa v64, 4, v214 dst_sel:DWORD dst_unused:UNUSED_PAD src0_sel:DWORD src1_sel:WORD_0\n" \
  "ds_read_b128 v[68:71], v64\n" \
  "v_lshlrev_b32_sdwa v65, 4, v214 dst_sel:DWORD dst_unused:UNUSED_PAD src0_sel:DWORD src1_sel:WORD_1\n" \
  "ds_read_b128 v[72:75], v65\n" \
  "s_mov_b32 m0, s58\n" \
  "s_add_u32 s35, s58, 0x8000\n" \
  "global_load_lds_dwordx4 v190, s[54:55] sc1\n" \
  "global_load_lds_dwordx4 v190, s[54:55] offset:1024 sc1\n" \
  "s_waitcnt lgkmcnt(10)\n" \
  "v_smfmac_f32_16x16x64_bf16 v[4:7], v[180:183], v[76:83], v192\n" \
  "v_lshlrev_b32_sdwa v66, 4, v215 dst_sel:DWORD dst_unused:UNUSED_PAD src0_sel:DWORD src1_sel:WORD_0\n" \
  "ds_read_b128 v[76:79], v66\n" \
  "v_lshlrev_b32_sdwa v67, 4, v215 dst_sel:DWORD dst_unused:UNUSED_PAD src0_sel:DWORD src1_sel:WORD_1\n" \
  "ds_read_b128 v[80:83], v67\n" \
  "s_waitcnt lgkmcnt(10)\n" \
  "v_smfmac_f32_16x16x64_bf16 v[8:11], v[180:183], v[84:91], v192\n" \
  "v_lshlrev_b32_sdwa v64, 4, v216 dst_sel:DWORD dst_unused:UNUSED_PAD src0_sel:DWORD src1_sel:WORD_0\n" \
  "ds_read_b128 v[84:87], v64\n" \
  "v_lshlrev_b32_sdwa v65, 4, v216 dst_sel:DWORD dst_unused:UNUSED_PAD src0_sel:DWORD src1_sel:WORD_1\n" \
  "ds_read_b128 v[88:91], v65\n" \
  "s_add_u32 m0, s58, 0x4000\n" \
  "s_cmp_lt_u32 s35, 0x28000\n" \
  "global_load_lds_dwordx4 v191, s[54:55] sc1\n" \
  "global_load_lds_dwordx4 v191, s[54:55] offset:1024 sc1\n" \
  "s_cselect_b32 s58, s35, s59\n" \
  "s_waitcnt lgkmcnt(10)\n" \
  "v_smfmac_f32_16x16x64_bf16 v[12:15], v[180:183], v[92:99], v192\n" \
  "v_lshlrev_b32_sdwa v66, 4, v217 dst_sel:DWORD dst_unused:UNUSED_PAD src0_sel:DWORD src1_sel:WORD_0\n" \
  "ds_read_b128 v[92:95], v66\n" \
  "v_lshlrev_b32_sdwa v67, 4, v217 dst_sel:DWORD dst_unused:UNUSED_PAD src0_sel:DWORD src1_sel:WORD_1\n" \
  "ds_read_b128 v[96:99], v67\n" \
  "s_waitcnt lgkmcnt(10)\n" \
  "v_smfmac_f32_16x16x64_bf16 v[16:19], v[180:183], v[100:107], v192\n" \
  "v_lshlrev_b32_sdwa v64, 4, v218 dst_sel:DWORD dst_unused:UNUSED_PAD src0_sel:DWORD src1_sel:WORD_0\n" \
  "ds_read_b128 v[100:103], v64\n" \
  "v_lshlrev_b32_sdwa v65, 4, v218 dst_sel:DWORD dst_unused:UNUSED_PAD src0_sel:DWORD src1_sel:WORD_1\n" \
  "ds_read_b128 v[104:107], v65\n" \
  "global_load_dwordx4 v[240:243], v188, s[56:57] sc1\n" \
  "global_load_dwordx4 v[244:247], v188, s[56:57] offset:1024 sc1\n" \
  "s_waitcnt lgkmcnt(10)\n" \
  "v_smfmac_f32_16x16x64_bf16 v[20:23], v[180:183], v[108:115], v192\n" \
  "v_lshlrev_b32_sdwa v66, 4, v219 dst_sel:DWORD dst_unused:UNUSED_PAD src0_sel:DWORD src1_sel:WORD_0\n" \
  "ds_read_b128 v[108:111], v66\n" \
  "v_lshlrev_b32_sdwa v67, 4, v219 dst_sel:DWORD dst_unused:UNUSED_PAD src0_sel:DWORD src1_sel:WORD_1\n" \
  "ds_read_b128 v[112:115], v67\n" \
  "s_waitcnt lgkmcnt(10)\n" \
  "v_smfmac_f32_16x16x64_bf16 v[24:27], v[180:183], v[68:75], v192\n" \
  "v_lshlrev_b32_sdwa v64, 4, v220 dst_sel:DWORD dst_unused:UNUSED_PAD src0_sel:DWORD src1_sel:WORD_0\n" \
  "ds_read_b128 v[68:71], v64\n" \
  "v_lshlrev_b32_sdwa v65, 4, v220 dst_sel:DWORD dst_unused:UNUSED_PAD src0_sel:DWORD src1_sel:WORD_1\n" \
  "ds_read_b128 v[72:75], v65\n" \
  "global_load_dwordx4 v[248:251], v188, s[56:57] offset:2048 sc1\n" \
  "global_load_dwordx4 v[252:255], v188, s[56:57] offset:3072 sc1\n" \
  "s_waitcnt lgkmcnt(10)\n" \
  "v_smfmac_f32_16x16x64_bf16 v[28:31], v[180:183], v[76:83], v192\n" \
  "v_lshlrev_b32_sdwa v66, 4, v221 dst_sel:DWORD dst_unused:UNUSED_PAD src0_sel:DWORD src1_sel:WORD_0\n" \
  "ds_read_b128 v[76:79], v66\n" \
  "v_lshlrev_b32_sdwa v67, 4, v221 dst_sel:DWORD dst_unused:UNUSED_PAD src0_sel:DWORD src1_sel:WORD_1\n" \
  "ds_read_b128 v[80:83], v67\n" \
  "s_waitcnt lgkmcnt(10)\n" \
  "v_smfmac_f32_16x16x64_bf16 v[32:35], v[180:183], v[84:91], v192\n" \
  "v_lshlrev_b32_sdwa v64, 4, v222 dst_sel:DWORD dst_unused:UNUSED_PAD src0_sel:DWORD src1_sel:WORD_0\n" \
  "ds_read_b128 v[84:87], v64\n" \
  "v_lshlrev_b32_sdwa v65, 4, v222 dst_sel:DWORD dst_unused:UNUSED_PAD src0_sel:DWORD src1_sel:WORD_1\n" \
  "ds_read_b128 v[88:91], v65\n" \
  "s_waitcnt lgkmcnt(10)\n" \
  "v_smfmac_f32_16x16x64_bf16 v[36:39], v[180:183], v[92:99], v192\n" \
  "v_lshlrev_b32_sdwa v66, 4, v223 dst_sel:DWORD dst_unused:UNUSED_PAD src0_sel:DWORD src1_sel:WORD_0\n" \
  "ds_read_b128 v[92:95], v66\n" \
  "v_lshlrev_b32_sdwa v67, 4, v223 dst_sel:DWORD dst_unused:UNUSED_PAD src0_sel:DWORD src1_sel:WORD_1\n" \
  "ds_read_b128 v[96:99], v67\n" \
  "s_add_u32 s56, s56, 32768\n" \
  "s_addc_u32 s57, s57, 0\n" \
  "s_add_u32 s54, s54, 64000\n" \
  "s_addc_u32 s55, s55, 0\n" \
  "s_waitcnt lgkmcnt(10)\n" \
  "v_smfmac_f32_16x16x64_bf16 v[40:43], v[180:183], v[100:107], v192\n" \
  "s_waitcnt lgkmcnt(8)\n" \
  "v_smfmac_f32_16x16x64_bf16 v[44:47], v[180:183], v[108:115], v192\n" \
  "s_waitcnt vmcnt(8)\n" \
  "s_cmp_eq_u32 s36, 37\n" \
  "s_cbranch_scc1 LG_chk\n" \
  "LG_ret:\n" \
  "s_sub_u32 s51, s51, 1\n" \
  "s_cmp_lg_u32 s51, 0\n" \
  "s_barrier\n" \
  "s_cbranch_scc1 LG_loop\n" \
  "v_lshlrev_b32_sdwa v64, 4, v224 dst_sel:DWORD dst_unused:UNUSED_PAD src0_sel:DWORD src1_sel:WORD_0\n" \
  "ds_read_b128 v[100:103], v64\n" \
  "v_lshlrev_b32_sdwa v65, 4, v224 dst_sel:DWORD dst_unused:UNUSED_PAD src0_sel:DWORD src1_sel:WORD_1\n" \
  "ds_read_b128 v[104:107], v65\n" \
  "v_lshlrev_b32_sdwa v66, 4, v225 dst_sel:DWORD dst_unused:UNUSED_PAD src0_sel:DWORD src1_sel:WORD_0\n" \
  "ds_read_b128 v[108:111], v66\n" \
  "v_lshlrev_b32_sdwa v67, 4, v225 dst_sel:DWORD dst_unused:UNUSED_PAD src0_sel:DWORD src1_sel:WORD_1\n" \
  "ds_read_b128 v[112:115], v67\n" \
  "s_waitcnt lgkmcnt(10)\n" \
  "v_smfmac_f32_16x16x64_bf16 v[48:51], v[180:183], v[68:75], v192\n" \
  "s_waitcnt lgkmcnt(8)\n" \
  "v_smfmac_f32_16x16x64_bf16 v[52:55], v[180:183], v[76:83], v192\n" \
  "s_waitcnt lgkmcnt(6)\n" \
  "v_smfmac_f32_16x16x64_bf16 v[56:59], v[180:183], v[84:91], v192\n" \
  "s_waitcnt lgkmcnt(4)\n" \
  "v_smfmac_f32_16x16x64_bf16 v[60:63], v[180:183], v[92:99], v192\n" \
  "s_barrier\n" \
  "s_mov_b32 m0, s58\n" \
  "s_add_u32 s35, s58, 0x8000\n" \
  "global_load_lds_dwordx4 v188, s[54:55] sc1\n" \
  "global_load_lds_dwordx4 v188, s[54:55] offset:1024 sc1\n" \
  "v_lshlrev_b32_sdwa v64, 4, v226 dst_sel:DWORD dst_unused:UNUSED_PAD src0_sel:DWORD src1_sel:WORD_0\n" \
  "ds_read_b128 v[68:71], v64\n" \
  "v_lshlrev_b32_sdwa v65, 4, v226 dst_sel:DWORD dst_unused:UNUSED_PAD src0_sel:DWORD src1_sel:WORD_1\n" \
  "ds_read_b128 v[72:75], v65\n" \
  "v_lshlrev_b32_sdwa v66, 4, v227 dst_sel:DWORD dst_unused:UNUSED_PAD src0_sel:DWORD src1_sel:WORD_0\n" \
  "ds_read_b128 v[76:79], v66\n" \
  "v_lshlrev_b32_sdwa v67, 4, v227 dst_sel:DWORD dst_unused:UNUSED_PAD src0_sel:DWORD src1_sel:WORD_1\n" \
  "ds_read_b128 v[80:83], v67\n" \
  "s_add_u32 m0, s58, 0x4000\n" \
  "s_cmp_lt_u32 s35, 0x28000\n" \
  "global_load_lds_dwordx4 v189, s[54:55] sc1\n" \
  "global_load_lds_dwordx4 v189, s[54:55] offset:1024 sc1\n" \
  "s_cselect_b32 s58, s35, s59\n" \
  "v_lshlrev_b32_sdwa v64, 4, v228 dst_sel:DWORD dst_unused:UNUSED_PAD src0_sel:DWORD src1_sel:WORD_0\n" \
  "ds_read_b128 v[84:87], v64\n" \
  "v_lshlrev_b32_sdwa v65, 4, v228 dst_sel:DWORD dst_unused:UNUSED_PAD src0_sel:DWORD src1_sel:WORD_1\n" \
  "ds_read_b128 v[88:91], v65\n" \
  "v_lshlrev_b32_sdwa v66, 4, v229 dst_sel:DWORD dst_unused:UNUSED_PAD src0_sel:DWORD src1_sel:WORD_0\n" \
  "ds_read_b128 v[92:95], v66\n" \
  "v_lshlrev_b32_sdwa v67, 4, v229 dst_sel:DWORD dst_unused:UNUSED_PAD src0_sel:DWORD src1_sel:WORD_1\n" \
  "ds_read_b128 v[96:99], v67\n" \
  "s_waitcnt lgkmcnt(10)\n" \
  "v_smfmac_f32_16x16x64_bf16 v[0:3], v[180:183], v[100:107], v192\n" \
  "v_lshlrev_b32_sdwa v64, 4, v230 dst_sel:DWORD dst_unused:UNUSED_PAD src0_sel:DWORD src1_sel:WORD_0\n" \
  "ds_read_b128 v[100:103], v64\n" \
  "v_lshlrev_b32_sdwa v65, 4, v230 dst_sel:DWORD dst_unused:UNUSED_PAD src0_sel:DWORD src1_sel:WORD_1\n" \
  "ds_read_b128 v[104:107], v65\n" \
  "s_mov_b32 m0, s58\n" \
  "s_add_u32 s35, s58, 0x8000\n" \
  "global_load_lds_dwordx4 v190, s[54:55] sc1\n" \
  "global_load_lds_dwordx4 v190, s[54:55] offset:1024 sc1\n" \
  "s_waitcnt lgkmcnt(10)\n" \
  "v_smfmac_f32_16x16x64_bf16 v[4:7], v[180:183], v[108:115], v192\n" \
  "v_lshlrev_b32_sdwa v66, 4, v231 dst_sel:DWORD dst_unused:UNUSED_PAD src0_sel:DWORD src1_sel:WORD_0\n" \
  "ds_read_b128 v[108:111], v66\n" \
  "v_lshlrev_b32_sdwa v67, 4, v231 dst_sel:DWORD dst_unused:UNUSED_PAD src0_sel:DWORD src1_sel:WORD_1\n" \
  "ds_read_b128 v[112:115], v67\n" \
  "s_waitcnt lgkmcnt(10)\n" \
  "v_smfmac_f32_16x16x64_bf16 v[8:11], v[180:183], v[68:75], v192\n" \
  "v_lshlrev_b32_sdwa v64, 4, v232 dst_sel:DWORD dst_unused:UNUSED_PAD src0_sel:DWORD src1_sel:WORD_0\n" \
  "ds_read_b128 v[68:71], v64\n" \
  "v_lshlrev_b32_sdwa v65, 4, v232 dst_sel:DWORD dst_unused:UNUSED_PAD src0_sel:DWORD src1_sel:WORD_1\n" \
  "ds_read_b128 v[72:75], v65\n" \
  "s_add_u32 m0, s58, 0x4000\n" \
  "s_cmp_lt_u32 s35, 0x28000\n" \
  "global_load_lds_dwordx4 v191, s[54:55] sc1\n" \
  "global_load_lds_dwordx4 v191, s[54:55] offset:1024 sc1\n" \
  "s_cselect_b32 s58, s35, s59\n" \
  "s_waitcnt lgkmcnt(10)\n" \
  "v_smfmac_f32_16x16x64_bf16 v[12:15], v[180:183], v[76:83], v192\n" \
  "v_lshlrev_b32_sdwa v66, 4, v233 dst_sel:DWORD dst_unused:UNUSED_PAD src0_sel:DWORD src1_sel:WORD_0\n" \
  "ds_read_b128 v[76:79], v66\n" \
  "v_lshlrev_b32_sdwa v67, 4, v233 dst_sel:DWORD dst_unused:UNUSED_PAD src0_sel:DWORD src1_sel:WORD_1\n" \
  "ds_read_b128 v[80:83], v67\n" \
  "s_waitcnt lgkmcnt(10)\n" \
  "v_smfmac_f32_16x16x64_bf16 v[16:19], v[180:183], v[84:91], v192\n" \
  "v_lshlrev_b32_sdwa v64, 4, v234 dst_sel:DWORD dst_unused:UNUSED_PAD src0_sel:DWORD src1_sel:WORD_0\n" \
  "ds_read_b128 v[84:87], v64\n" \
  "v_lshlrev_b32_sdwa v65, 4, v234 dst_sel:DWORD dst_unused:UNUSED_PAD src0_sel:DWORD src1_sel:WORD_1\n" \
  "ds_read_b128 v[88:91], v65\n" \
  "global_load_dwordx4 v[208:211], v188, s[56:57] sc1\n" \
  "global_load_dwordx4 v[212:215], v188, s[56:57] offset:1024 sc1\n" \
  "s_waitcnt lgkmcnt(10)\n" \
  "v_smfmac_f32_16x16x64_bf16 v[20:23], v[180:183], v[92:99], v192\n" \
  "v_lshlrev_b32_sdwa v66, 4, v235 dst_sel:DWORD dst_unused:UNUSED_PAD src0_sel:DWORD src1_sel:WORD_0\n" \
  "ds_read_b128 v[92:95], v66\n" \
  "v_lshlrev_b32_sdwa v67, 4, v235 dst_sel:DWORD dst_unused:UNUSED_PAD src0_sel:DWORD src1_sel:WORD_1\n" \
  "ds_read_b128 v[96:99], v67\n" \
  "s_waitcnt lgkmcnt(10)\n" \
  "v_smfmac_f32_16x16x64_bf16 v[24:27], v[180:183], v[100:107], v192\n" \
  "v_lshlrev_b32_sdwa v64, 4, v236 dst_sel:DWORD dst_unused:UNUSED_PAD src0_sel:DWORD src1_sel:WORD_0\n" \
  "ds_read_b128 v[100:103], v64\n" \
  "v_lshlrev_b32_sdwa v65, 4, v236 dst_sel:DWORD dst_unused:UNUSED_PAD src0_sel:DWORD src1_sel:WORD_1\n" \
  "ds_read_b128 v[104:107], v65\n" \
  "global_load_dwordx4 v[216:219], v188, s[56:57] offset:2048 sc1\n" \
  "global_load_dwordx4 v[220:223], v188, s[56:57] offset:3072 sc1\n" \
  "s_waitcnt lgkmcnt(10)\n" \
  "v_smfmac_f32_16x16x64_bf16 v[28:31], v[180:183], v[108:115], v192\n" \
  "v_lshlrev_b32_sdwa v66, 4, v237 dst_sel:DWORD dst_unused:UNUSED_PAD src0_sel:DWORD src1_sel:WORD_0\n" \
  "ds_read_b128 v[108:111], v66\n" \
  "v_lshlrev_b32_sdwa v67, 4, v237 dst_sel:DWORD dst_unused:UNUSED_PAD src0_sel:DWORD src1_sel:WORD_1\n" \
  "ds_read_b128 v[112:115], v67\n" \
  "s_waitcnt lgkmcnt(10)\n" \
  "v_smfmac_f32_16x16x64_bf16 v[32:35], v[180:183], v[68:75], v192\n" \
  "v_lshlrev_b32_sdwa v64, 4, v238 dst_sel:DWORD dst_unused:UNUSED_PAD src0_sel:DWORD src1_sel:WORD_0\n" \
  "ds_read_b128 v[68:71], v64\n" \
  "v_lshlrev_b32_sdwa v65, 4, v238 dst_sel:DWORD dst_unused:UNUSED_PAD src0_sel:DWORD src1_sel:WORD_1\n" \
  "ds_read_b128 v[72:75], v65\n" \
  "s_waitcnt lgkmcnt(10)\n" \
  "v_smfmac_f32_16x16x64_bf16 v[36:39], v[180:183], v[76:83], v192\n" \
  "v_lshlrev_b32_sdwa v66, 4, v239 dst_sel:DWORD dst_unused:UNUSED_PAD src0_sel:DWORD src1_sel:WORD_0\n" \
  "ds_read_b128 v[76:79], v66\n" \
  "v_lshlrev_b32_sdwa v67, 4, v239 dst_sel:DWORD dst_unused:UNUSED_PAD src0_sel:DWORD src1_sel:WORD_1\n" \
  "ds_read_b128 v[80:83], v67\n" \
  "s_add_u32 s56, s56, 32768\n" \
  "s_addc_u32 s57, s57, 0\n" \
  "s_add_u32 s54, s54, 64000\n" \
  "s_addc_u32 s55, s55, 0\n" \
  "s_waitcnt lgkmcnt(10)\n" \
  "v_smfmac_f32_16x16x64_bf16 v[40:43], v[180:183], v[84:91], v192\n" \
  "s_waitcnt lgkmcnt(8)\n" \
  "v_smfmac_f32_16x16x64_bf16 v[44:47], v[180:183], v[92:99], v192\n" \
  "s_waitcnt vmcnt(8)\n" \
  "s_barrier\n" \
  "v_lshlrev_b32_sdwa v64, 4, v240 dst_sel:DWORD dst_unused:UNUSED_PAD src0_sel:DWORD src1_sel:WORD_0\n" \
  "ds_read_b128 v[84:87], v64\n" \
  "v_lshlrev_b32_sdwa v65, 4, v240 dst_sel:DWORD dst_unused:UNUSED_PAD src0_sel:DWORD src1_sel:WORD_1\n" \
  "ds_read_b128 v[88:91], v65\n" \
  "v_lshlrev_b32_sdwa v66, 4, v241 dst_sel:DWORD dst_unused:UNUSED_PAD src0_sel:DWORD src1_sel:WORD_0\n" \
  "ds_read_b128 v[92:95], v66\n" \
  "v_lshlrev_b32_sdwa v67, 4, v241 dst_sel:DWORD dst_unused:UNUSED_PAD src0_sel:DWORD src1_sel:WORD_1\n" \
  "ds_read_b128 v[96:99], v67\n" \
  "s_waitcnt lgkmcnt(10)\n" \
  "v_smfmac_f32_16x16x64_bf16 v[48:51], v[180:183], v[100:107], v192\n" \
  "s_waitcnt lgkmcnt(8)\n" \
  "v_smfmac_f32_16x16x64_bf16 v[52:55], v[180:183], v[108:115], v192\n" \
  "s_waitcnt lgkmcnt(6)\n" \
  "v_smfmac_f32_16x16x64_bf16 v[56:59], v[180:183], v[68:75], v192\n" \
  "s_waitcnt lgkmcnt(4)\n" \
  "v_smfmac_f32_16x16x64_bf16 v[60:63], v[180:183], v[76:83], v192\n" \
  "s_barrier\n" \
  "s_mov_b32 m0, s58\n" \
  "s_add_u32 s35, s58, 0x8000\n" \
  "global_load_lds_dwordx4 v188, s[54:55] sc1\n" \
  "global_load_lds_dwordx4 v188, s[54:55] offset:1024 sc1\n" \
  "s_add_u32 m0, s58, 0x4000\n" \
  "s_cmp_lt_u32 s35, 0x28000\n" \
  "global_load_lds_dwordx4 v189, s[54:55] sc1\n" \
  "global_load_lds_dwordx4 v189, s[54:55] offset:1024 sc1\n" \
  "s_cselect_b32 s58, s35, s59\n" \
  "v_lshlrev_b32 v120, 16, v206\n" \
  "v_and_b32 v121, 0xffff0000, v206\n" \
  "v_lshlrev_b32 v122, 16, v207\n" \
  "v_and_b32 v123, 0xffff0000, v207\n" \
  "v_mov_b32 v124, 0xc0135761\n" \
  "v_mov_b32 v125, 0xc0135761\n" \
  "s_mov_b32 s52, 0xbdd2d3e8\n" \
  "s_mov_b32 s53, s52\n" \
  "s_lshl2_add_u32 s35, s33, s34\n" \
  "s_lshl_b32 s35, s35, 14\n" \
  "s_add_u32 s35, s35, s59\n" \
  "s_add_u32 s35, s35, 105676800\n" \
  "s_add_u32 s40, s24, s35\n" \
  "s_addc_u32 s41, s25, 0\n" \
  "v_lshlrev_b32_sdwa v64, 4, v242 dst_sel:DWORD dst_unused:UNUSED_PAD src0_sel:DWORD src1_sel:WORD_0\n" \
  "ds_read_b128 v[100:103], v64\n" \
  "v_lshlrev_b32_sdwa v65, 4, v242 dst_sel:DWORD dst_unused:UNUSED_PAD src0_sel:DWORD src1_sel:WORD_1\n" \
  "ds_read_b128 v[104:107], v65\n" \
  "v_lshlrev_b32_sdwa v66, 4, v243 dst_sel:DWORD dst_unused:UNUSED_PAD src0_sel:DWORD src1_sel:WORD_0\n" \
  "ds_read_b128 v[108:111], v66\n" \
  "v_lshlrev_b32_sdwa v67, 4, v243 dst_sel:DWORD dst_unused:UNUSED_PAD src0_sel:DWORD src1_sel:WORD_1\n" \
  "ds_read_b128 v[112:115], v67\n" \
  "v_lshlrev_b32_sdwa v64, 4, v244 dst_sel:DWORD dst_unused:UNUSED_PAD src0_sel:DWORD src1_sel:WORD_0\n" \
  "ds_read_b128 v[68:71], v64\n" \
  "v_lshlrev_b32_sdwa v65, 4, v244 dst_sel:DWORD dst_unused:UNUSED_PAD src0_sel:DWORD src1_sel:WORD_1\n" \
  "ds_read_b128 v[72:75], v65\n" \
  "v_lshlrev_b32_sdwa v66, 4, v245 dst_sel:DWORD dst_unused:UNUSED_PAD src0_sel:DWORD src1_sel:WORD_0\n" \
  "ds_read_b128 v[76:79], v66\n" \
  "v_lshlrev_b32_sdwa v67, 4, v245 dst_sel:DWORD dst_unused:UNUSED_PAD src0_sel:DWORD src1_sel:WORD_1\n" \
  "ds_read_b128 v[80:83], v67\n" \
  "s_waitcnt lgkmcnt(10)\n" \
  "v_smfmac_f32_16x16x64_bf16 v[0:3], v[180:183], v[84:91], v192\n" \
  "v_lshlrev_b32_sdwa v64, 4, v246 dst_sel:DWORD dst_unused:UNUSED_PAD src0_sel:DWORD src1_sel:WORD_0\n" \
  "ds_read_b128 v[84:87], v64\n" \
  "v_lshlrev_b32_sdwa v65, 4, v246 dst_sel:DWORD dst_unused:UNUSED_PAD src0_sel:DWORD src1_sel:WORD_1\n" \
  "ds_read_b128 v[88:91], v65\n" \
  "s_waitcnt lgkmcnt(10)\n" \
  "v_smfmac_f32_16x16x64_bf16 v[4:7], v[180:183], v[92:99], v192\n" \
  "v_lshlrev_b32_sdwa v66, 4, v247 dst_sel:DWORD dst_unused:UNUSED_PAD src0_sel:DWORD src1_sel:WORD_0\n" \
  "ds_read_b128 v[92:95], v66\n" \
  "v_lshlrev_b32_sdwa v67, 4, v247 dst_sel:DWORD dst_unused:UNUSED_PAD src0_sel:DWORD src1_sel:WORD_1\n" \
  "ds_read_b128 v[96:99], v67\n" \
  "s_waitcnt lgkmcnt(10)\n" \
  "v_smfmac_f32_16x16x64_bf16 v[8:11], v[180:183], v[100:107], v192\n" \
  "v_lshlrev_b32_sdwa v64, 4, v248 dst_sel:DWORD dst_unused:UNUSED_PAD src0_sel:DWORD src1_sel:WORD_0\n" \
  "ds_read_b128 v[100:103], v64\n" \
  "v_lshlrev_b32_sdwa v65, 4, v248 dst_sel:DWORD dst_unused:UNUSED_PAD src0_sel:DWORD src1_sel:WORD_1\n" \
  "ds_read_b128 v[104:107], v65\n" \
  "s_waitcnt lgkmcnt(10)\n" \
  "v_smfmac_f32_16x16x64_bf16 v[12:15], v[180:183], v[108:115], v192\n" \
  "v_lshlrev_b32_sdwa v66, 4, v249 dst_sel:DWORD dst_unused:UNUSED_PAD src0_sel:DWORD src1_sel:WORD_0\n" \
  "ds_read_b128 v[108:111], v66\n" \
  "v_lshlrev_b32_sdwa v67, 4, v249 dst_sel:DWORD dst_unused:UNUSED_PAD src0_sel:DWORD src1_sel:WORD_1\n" \
  "ds_read_b128 v[112:115], v67\n" \
  "s_waitcnt vmcnt(0)\n" \
  "s_barrier\n" \
  "s_waitcnt lgkmcnt(10)\n" \
  "v_smfmac_f32_16x16x64_bf16 v[16:19], v[180:183], v[68:75], v192\n" \
  "v_lshlrev_b32_sdwa v64, 4, v208 dst_sel:DWORD dst_unused:UNUSED_PAD src0_sel:DWORD src1_sel:WORD_0\n" \
  "ds_read_b128 v[68:71], v64\n" \
  "v_lshlrev_b32_sdwa v65, 4, v208 dst_sel:DWORD dst_unused:UNUSED_PAD src0_sel:DWORD src1_sel:WORD_1\n" \
  "ds_read_b128 v[72:75], v65\n" \
  "s_waitcnt lgkmcnt(10)\n" \
  "v_smfmac_f32_16x16x64_bf16 v[20:23], v[180:183], v[76:83], v192\n" \
  "v_lshlrev_b32_sdwa v66, 4, v209 dst_sel:DWORD dst_unused:UNUSED_PAD src0_sel:DWORD src1_sel:WORD_0\n" \
  "ds_read_b128 v[76:79], v66\n" \
  "v_lshlrev_b32_sdwa v67, 4, v209 dst_sel:DWORD dst_unused:UNUSED_PAD src0_sel:DWORD src1_sel:WORD_1\n" \
  "ds_read_b128 v[80:83], v67\n" \
  "s_waitcnt lgkmcnt(10)\n" \
  "v_smfmac_f32_16x16x64_bf16 v[24:27], v[180:183], v[84:91], v192\n" \
  "v_lshlrev_b32_sdwa v64, 4, v210 dst_sel:DWORD dst_unused:UNUSED_PAD src0_sel:DWORD src1_sel:WORD_0\n" \
  "ds_read_b128 v[84:87], v64\n" \
  "v_lshlrev_b32_sdwa v65, 4, v210 dst_sel:DWORD dst_unused:UNUSED_PAD src0_sel:DWORD src1_sel:WORD_1\n" \
  "ds_read_b128 v[88:91], v65\n" \
  "s_waitcnt lgkmcnt(10)\n" \
  "v_smfmac_f32_16x16x64_bf16 v[28:31], v[180:183], v[92:99], v192\n" \
  "v_lshlrev_b32_sdwa v66, 4, v211 dst_sel:DWORD dst_unused:UNUSED_PAD src0_sel:DWORD src1_sel:WORD_0\n" \
  "ds_read_b128 v[92:95], v66\n" \
  "v_lshlrev_b32_sdwa v67, 4, v211 dst_sel:DWORD dst_unused:UNUSED_PAD src0_sel:DWORD src1_sel:WORD_1\n" \
  "ds_read_b128 v[96:99], v67\n" \
  "s_waitcnt lgkmcnt(10)\n" \
  "v_smfmac_f32_16x16x64_bf16 v[32:35], v[180:183], v[100:107], v192\n" \
  "v_lshlrev_b32_sdwa v64, 4, v212 dst_sel:DWORD dst_unused:UNUSED_PAD src0_sel:DWORD src1_sel:WORD_0\n" \
  "ds_read_b128 v[100:103], v64\n" \
  "v_lshlrev_b32_sdwa v65, 4, v212 dst_sel:DWORD dst_unused:UNUSED_PAD src0_sel:DWORD src1_sel:WORD_1\n" \
  "ds_read_b128 v[104:107], v65\n" \
  "s_waitcnt lgkmcnt(10)\n" \
  "v_smfmac_f32_16x16x64_bf16 v[36:39], v[180:183], v[108:115], v192\n" \
  "v_lshlrev_b32_sdwa v66, 4, v213 dst_sel:DWORD dst_unused:UNUSED_PAD src0_sel:DWORD src1_sel:WORD_0\n" \
  "ds_read_b128 v[108:111], v66\n" \
  "v_lshlrev_b32_sdwa v67, 4, v213 dst_sel:DWORD dst_unused:UNUSED_PAD src0_sel:DWORD src1_sel:WORD_1\n" \
  "ds_read_b128 v[112:115], v67\n" \
  "s_waitcnt lgkmcnt(10)\n" \
  "v_smfmac_f32_16x16x64_bf16 v[0:3], v[180:183], v[68:75], v192\n" \
  "v_lshlrev_b32_sdwa v64, 4, v214 dst_sel:DWORD dst_unused:UNUSED_PAD src0_sel:DWORD src1_sel:WORD_0\n" \
  "ds_read_b128 v[68:71], v64\n" \
  "v_lshlrev_b32_sdwa v65, 4, v214 dst_sel:DWORD dst_unused:UNUSED_PAD src0_sel:DWORD src1_sel:WORD_1\n" \
  "ds_read_b128 v[72:75], v65\n" \
  "s_waitcnt lgkmcnt(10)\n" \
  "v_smfmac_f32_16x16x64_bf16 v[4:7], v[180:183], v[76:83], v192\n" \
  "v_lshlrev_b32_sdwa v66, 4, v215 dst_sel:DWORD dst_unused:UNUSED_PAD src0_sel:DWORD src1_sel:WORD_0\n" \
  "ds_read_b128 v[76:79], v66\n" \
  "v_lshlrev_b32_sdwa v67, 4, v215 dst_sel:DWORD dst_unused:UNUSED_PAD src0_sel:DWORD src1_sel:WORD_1\n" \
  "ds_read_b128 v[80:83], v67\n" \
  "v_pk_mul_f32 v[128:129], v[0:1], v[0:1]\n" \
  "v_pk_mul_f32 v[130:131], v[2:3], v[2:3]\n" \
  "v_pk_fma_f32 v[128:129], v[128:129], s[52:53], v[124:125]\n" \
  "v_pk_fma_f32 v[130:131], v[130:131], s[52:53], v[124:125]\n" \
  "v_pk_mul_f32 v[128:129], v[128:129], v[0:1]\n" \
  "v_pk_mul_f32 v[130:131], v[130:131], v[2:3]\n" \
  "v_exp_f32 v128, v128\n" \
  "v_exp_f32 v129, v129\n" \
  "v_pk_mul_f32 v[0:1], v[0:1], v[120:121]\n" \
  "v_exp_f32 v130, v130\n" \
  "v_exp_f32 v131, v131\n" \
  "v_pk_mul_f32 v[2:3], v[2:3], v[122:123]\n" \
  "v_pk_add_f32 v[128:129], v[128:129], 1.0 op_sel_hi:[1,0]\n" \
  "v_pk_add_f32 v[130:131], v[130:131], 1.0 op_sel_hi:[1,0]\n" \
  "v_rcp_f32 v128, v128\n" \
  "v_rcp_f32 v129, v129\n" \
  "s_waitcnt lgkmcnt(10)\n" \
  "v_smfmac_f32_16x16x64_bf16 v[8:11], v[180:183], v[84:91], v192\n" \
  "v_lshlrev_b32_sdwa v64, 4, v216 dst_sel:DWORD dst_unused:UNUSED_PAD src0_sel:DWORD src1_sel:WORD_0\n" \
  "ds_read_b128 v[84:87], v64\n" \
  "v_lshlrev_b32_sdwa v65, 4, v216 dst_sel:DWORD dst_unused:UNUSED_PAD src0_sel:DWORD src1_sel:WORD_1\n" \
  "ds_read_b128 v[88:91], v65\n" \
  "v_rcp_f32 v130, v130\n" \
  "v_rcp_f32 v131, v131\n" \
  "v_pk_mul_f32 v[0:1], v[0:1], v[128:129]\n" \
  "v_pk_fma_f32 v[0:1], v[2:3], v[130:131], v[0:1]\n" \
  "v_add_f32 v0, v0, v1\n" \
  "v_pk_mul_f32 v[132:133], v[4:5], v[4:5]\n" \
  "v_pk_mul_f32 v[134:135], v[6:7], v[6:7]\n" \
  "v_pk_fma_f32 v[132:133], v[132:133], s[52:53], v[124:125]\n" \
  "v_pk_fma_f32 v[134:135], v[134:135], s[52:53], v[124:125]\n" \
  "v_pk_mul_f32 v[132:133], v[132:133], v[4:5]\n" \
  "v_pk_mul_f32 v[134:135], v[134:135], v[6:7]\n" \
  "v_exp_f32 v132, v132\n" \
  "v_exp_f32 v133, v133\n" \
  "v_pk_mul_f32 v[4:5], v[4:5], v[120:121]\n" \
  "v_exp_f32 v134, v134\n" \
  "v_exp_f32 v135, v135\n" \
  "s_waitcnt lgkmcnt(10)\n" \
  "v_smfmac_f32_16x16x64_bf16 v[12:15], v[180:183], v[92:99], v192\n" \
  "v_lshlrev_b32_sdwa v66, 4, v217 dst_sel:DWORD dst_unused:UNUSED_PAD src0_sel:DWORD src1_sel:WORD_0\n" \
  "ds_read_b128 v[92:95], v66\n" \
  "v_lshlrev_b32_sdwa v67, 4, v217 dst_sel:DWORD dst_unused:UNUSED_PAD src0_sel:DWORD src1_sel:WORD_1\n" \
  "ds_read_b128 v[96:99], v67\n" \
  "v_pk_mul_f32 v[6:7], v[6:7], v[122:123]\n" \
  "v_pk_add_f32 v[132:133], v[132:133], 1.0 op_sel_hi:[1,0]\n" \
  "v_pk_add_f32 v[134:135], v[134:135], 1.0 op_sel_hi:[1,0]\n" \
  "v_rcp_f32 v132, v132\n" \
  "v_rcp_f32 v133, v133\n" \
  "v_rcp_f32 v134, v134\n" \
  "v_rcp_f32 v135, v135\n" \
  "v_pk_mul_f32 v[4:5], v[4:5], v[132:133]\n" \
  "v_pk_fma_f32 v[4:5], v[6:7], v[134:135], v[4:5]\n" \
  "v_add_f32 v4, v4, v5\n" \
  "v_pk_mul_f32 v[136:137], v[8:9], v[8:9]\n" \
  "v_pk_mul_f32 v[138:139], v[10:11], v[10:11]\n" \
  "v_pk_fma_f32 v[136:137], v[136:137], s[52:53], v[124:125]\n" \
  "v_pk_fma_f32 v[138:139], v[138:139], s[52:53], v[124:125]\n" \
  "v_pk_mul_f32 v[136:137], v[136:137], v[8:9]\n" \
  "v_pk_mul_f32 v[138:139], v[138:139], v[10:11]\n" \
  "s_waitcnt lgkmcnt(10)\n" \
  "v_smfmac_f32_16x16x64_bf16 v[16:19], v[180:183], v[100:107], v192\n" \
  "v_lshlrev_b32_sdwa v64, 4, v250 dst_sel:DWORD dst_unused:UNUSED_PAD src0_sel:DWORD src1_sel:WORD_0\n" \
  "ds_read_b128 v[100:103], v64\n" \
  "v_lshlrev_b32_sdwa v65, 4, v250 dst_sel:DWORD dst_unused:UNUSED_PAD src0_sel:DWORD src1_sel:WORD_1\n" \
  "ds_read_b128 v[104:107], v65\n" \
  "v_exp_f32 v136, v136\n" \
  "v_exp_f32 v137, v137\n" \
  "v_pk_mul_f32 v[8:9], v[8:9], v[120:121]\n" \
  "v_exp_f32 v138, v138\n" \
  "v_exp_f32 v139, v139\n" \
  "v_pk_mul_f32 v[10:11], v[10:11], v[122:123]\n" \
  "v_pk_add_f32 v[136:137], v[136:137], 1.0 op_sel_hi:[1,0]\n" \
  "v_pk_add_f32 v[138:139], v[138:139], 1.0 op_sel_hi:[1,0]\n" \
  "v_rcp_f32 v136, v136\n" \
  "v_rcp_f32 v137, v137\n" \
  "v_rcp_f32 v138, v138\n" \
  "v_rcp_f32 v139, v139\n" \
  "v_pk_mul_f32 v[8:9], v[8:9], v[136:137]\n" \
  "v_pk_fma_f32 v[8:9], v[10:11], v[138:139], v[8:9]\n" \
  "v_add_f32 v8, v8, v9\n" \
  "v_pk_mul_f32 v[140:141], v[12:13], v[12:13]\n" \
  "s_waitcnt lgkmcnt(10)\n" \
  "v_smfmac_f32_16x16x64_bf16 v[20:23], v[180:183], v[108:115], v192\n" \
  "v_lshlrev_b32_sdwa v66, 4, v218 dst_sel:DWORD dst_unused:UNUSED_PAD src0_sel:DWORD src1_sel:WORD_0\n" \
  "ds_read_b128 v[108:111], v66\n" \
  "v_lshlrev_b32_sdwa v67, 4, v218 dst_sel:DWORD dst_unused:UNUSED_PAD src0_sel:DWORD src1_sel:WORD_1\n" \
  "ds_read_b128 v[112:115], v67\n" \
  "v_pk_mul_f32 v[142:143], v[14:15], v[14:15]\n" \
  "v_pk_fma_f32 v[140:141], v[140:141], s[52:53], v[124:125]\n" \
  "v_pk_fma_f32 v[142:143], v[142:143], s[52:53], v[124:125]\n" \
  "v_pk_mul_f32 v[140:141], v[140:141], v[12:13]\n" \
  "v_pk_mul_f32 v[142:143], v[142:143], v[14:15]\n" \
  "v_exp_f32 v140, v140\n" \
  "v_exp_f32 v141, v141\n" \
  "v_pk_mul_f32 v[12:13], v[12:13], v[120:121]\n" \
  "v_exp_f32 v142, v142\n" \
  "v_exp_f32 v143, v143\n" \
  "v_pk_mul_f32 v[14:15], v[14:15], v[122:123]\n" \
  "v_pk_add_f32 v[140:141], v[140:141], 1.0 op_sel_hi:[1,0]\n" \
  "v_pk_add_f32 v[142:143], v[142:143], 1.0 op_sel_hi:[1,0]\n" \
  "v_rcp_f32 v140, v140\n" \
  "v_rcp_f32 v141, v141\n" \
  "v_rcp_f32 v142, v142\n" \
  "s_waitcnt lgkmcnt(10)\n" \
  "v_smfmac_f32_16x16x64_bf16 v[24:27], v[180:183], v[68:75], v192\n" \
  "v_lshlrev_b32_sdwa v64, 4, v251 dst_sel:DWORD dst_unused:UNUSED_PAD src0_sel:DWORD src1_sel:WORD_0\n" \
  "ds_read_b128 v[68:71], v64\n" \
  "v_lshlrev_b32_sdwa v65, 4, v251 dst_sel:DWORD dst_unused:UNUSED_PAD src0_sel:DWORD src1_sel:WORD_1\n" \
  "ds_read_b128 v[72:75], v65\n" \
  "v_rcp_f32 v143, v143\n" \
  "v_pk_mul_f32 v[12:13], v[12:13], v[140:141]\n" \
  "v_pk_fma_f32 v[12:13], v[14:15], v[142:143], v[12:13]\n" \
  "v_add_f32 v12, v12, v13\n" \
  "v_pk_mul_f32 v[128:129], v[16:17], v[16:17]\n" \
  "v_pk_mul_f32 v[130:131], v[18:19], v[18:19]\n" \
  "v_pk_fma_f32 v[128:129], v[128:129], s[52:53], v[124:125]\n" \
  "v_pk_fma_f32 v[130:131], v[130:131], s[52:53], v[124:125]\n" \
  "v_pk_mul_f32 v[128:129], v[128:129], v[16:17]\n" \
  "v_pk_mul_f32 v[130:131], v[130:131], v[18:19]\n" \
  "v_exp_f32 v128, v128\n" \
  "v_exp_f32 v129, v129\n" \
  "v_pk_mul_f32 v[16:17], v[16:17], v[120:121]\n" \
  "v_exp_f32 v130, v130\n" \
  "v_exp_f32 v131, v131\n" \
  "v_pk_mul_f32 v[18:19], v[18:19], v[122:123]\n" \
  "s_waitcnt lgkmcnt(10)\n" \
  "v_smfmac_f32_16x16x64_bf16 v[28:31], v[180:183], v[76:83], v192\n" \
  "v_lshlrev_b32_sdwa v66, 4, v219 dst_sel:DWORD dst_unused:UNUSED_PAD src0_sel:DWORD src1_sel:WORD_0\n" \
  "ds_read_b128 v[76:79], v66\n" \
  "v_lshlrev_b32_sdwa v67, 4, v219 dst_sel:DWORD dst_unused:UNUSED_PAD src0_sel:DWORD src1_sel:WORD_1\n" \
  "ds_read_b128 v[80:83], v67\n" \
  "v_pk_add_f32 v[128:129], v[128:129], 1.0 op_sel_hi:[1,0]\n" \
  "v_pk_add_f32 v[130:131], v[130:131], 1.0 op_sel_hi:[1,0]\n" \
  "v_rcp_f32 v128, v128\n" \
  "v_rcp_f32 v129, v129\n" \
  "v_rcp_f32 v130, v130\n" \
  "v_rcp_f32 v131, v131\n" \
  "v_pk_mul_f32 v[16:17], v[16:17], v[128:129]\n" \
  "v_pk_fma_f32 v[16:17], v[18:19], v[130:131], v[16:17]\n" \
  "v_add_f32 v16, v16, v17\n" \
  "v_pk_mul_f32 v[132:133], v[20:21], v[20:21]\n" \
  "v_pk_mul_f32 v[134:135], v[22:23], v[22:23]\n" \
  "v_pk_fma_f32 v[132:133], v[132:133], s[52:53], v[124:125]\n" \
  "v_pk_fma_f32 v[134:135], v[134:135], s[52:53], v[124:125]\n" \
  "v_pk_mul_f32 v[132:133], v[132:133], v[20:21]\n" \
  "v_pk_mul_f32 v[134:135], v[134:135], v[22:23]\n" \
  "v_exp_f32 v132, v132\n" \
  "s_waitcnt lgkmcnt(10)\n" \
  "v_smfmac_f32_16x16x64_bf16 v[32:35], v[180:183], v[84:91], v192\n" \
  "v_lshlrev_b32_sdwa v64, 4, v252 dst_sel:DWORD dst_unused:UNUSED_PAD src0_sel:DWORD src1_sel:WORD_0\n" \
  "ds_read_b128 v[84:87], v64\n" \
  "v_lshlrev_b32_sdwa v65, 4, v252 dst_sel:DWORD dst_unused:UNUSED_PAD src0_sel:DWORD src1_sel:WORD_1\n" \
  "ds_read_b128 v[88:91], v65\n" \
  "v_exp_f32 v133, v133\n" \
  "v_pk_mul_f32 v[20:21], v[20:21], v[120:121]\n" \
  "v_exp_f32 v134, v134\n" \
  "v_exp_f32 v135, v135\n" \
  "v_pk_mul_f32 v[22:23], v[22:23], v[122:123]\n" \
  "v_pk_add_f32 v[132:133], v[132:133], 1.0 op_sel_hi:[1,0]\n" \
  "v_pk_add_f32 v[134:135], v[134:135], 1.0 op_sel_hi:[1,0]\n" \
  "v_rcp_f32 v132, v132\n" \
  "v_rcp_f32 v133, v133\n" \
  "v_rcp_f32 v134, v134\n" \
  "v_rcp_f32 v135, v135\n" \
  "v_pk_mul_f32 v[20:21], v[20:21], v[132:133]\n" \
  "v_pk_fma_f32 v[20:21], v[22:23], v[134:135], v[20:21]\n" \
  "v_add_f32 v20, v20, v21\n" \
  "v_pk_mul_f32 v[136:137], v[24:25], v[24:25]\n" \
  "v_pk_mul_f32 v[138:139], v[26:27], v[26:27]\n" \
  "s_waitcnt lgkmcnt(10)\n" \
  "v_smfmac_f32_16x16x64_bf16 v[36:39], v[180:183], v[92:99], v192\n" \
  "v_lshlrev_b32_sdwa v66, 4, v220 dst_sel:DWORD dst_unused:UNUSED_PAD src0_sel:DWORD src1_sel:WORD_0\n" \
  "ds_read_b128 v[92:95], v66\n" \
  "v_lshlrev_b32_sdwa v67, 4, v220 dst_sel:DWORD dst_unused:UNUSED_PAD src0_sel:DWORD src1_sel:WORD_1\n" \
  "ds_read_b128 v[96:99], v67\n" \
  "v_pk_fma_f32 v[136:137], v[136:137], s[52:53], v[124:125]\n" \
  "v_pk_fma_f32 v[138:139], v[138:139], s[52:53], v[124:125]\n" \
  "v_pk_mul_f32 v[136:137], v[136:137], v[24:25]\n" \
  "v_pk_mul_f32 v[138:139], v[138:139], v[26:27]\n" \
  "v_exp_f32 v136, v136\n" \
  "v_exp_f32 v137, v137\n" \
  "v_pk_mul_f32 v[24:25], v[24:25], v[120:121]\n" \
  "v_exp_f32 v138, v138\n" \
  "v_exp_f32 v139, v139\n" \
  "v_pk_mul_f32 v[26:27], v[26:27], v[122:123]\n" \
  "v_pk_add_f32 v[136:137], v[136:137], 1.0 op_sel_hi:[1,0]\n" \
  "v_pk_add_f32 v[138:139], v[138:139], 1.0 op_sel_hi:[1,0]\n" \
  "v_rcp_f32 v136, v136\n" \
  "v_rcp_f32 v137, v137\n" \
  "v_rcp_f32 v138, v138\n" \
  "v_rcp_f32 v139, v139\n" \
  "s_waitcnt lgkmcnt(10)\n" \
  "v_smfmac_f32_16x16x64_bf16 v[40:43], v[180:183], v[100:107], v192\n" \
  "v_lshlrev_b32_sdwa v64, 4, v253 dst_sel:DWORD dst_unused:UNUSED_PAD src0_sel:DWORD src1_sel:WORD_0\n" \
  "ds_read_b128 v[100:103], v64\n" \
  "v_lshlrev_b32_sdwa v65, 4, v253 dst_sel:DWORD dst_unused:UNUSED_PAD src0_sel:DWORD src1_sel:WORD_1\n" \
  "ds_read_b128 v[104:107], v65\n" \
  "v_pk_mul_f32 v[24:25], v[24:25], v[136:137]\n" \
  "v_pk_fma_f32 v[24:25], v[26:27], v[138:139], v[24:25]\n" \
  "v_add_f32 v24, v24, v25\n" \
  "v_pk_mul_f32 v[140:141], v[28:29], v[28:29]\n" \
  "v_pk_mul_f32 v[142:143], v[30:31], v[30:31]\n" \
  "v_pk_fma_f32 v[140:141], v[140:141], s[52:53], v[124:125]\n" \
  "v_pk_fma_f32 v[142:143], v[142:143], s[52:53], v[124:125]\n" \
  "v_pk_mul_f32 v[140:141], v[140:141], v[28:29]\n" \
  "v_pk_mul_f32 v[142:143], v[142:143], v[30:31]\n" \
  "v_exp_f32 v140, v140\n" \
  "v_exp_f32 v141, v141\n" \
  "v_pk_mul_f32 v[28:29], v[28:29], v[120:121]\n" \
  "v_exp_f32 v142, v142\n" \
  "v_exp_f32 v143, v143\n" \
  "v_pk_mul_f32 v[30:31], v[30:31], v[122:123]\n" \
  "v_pk_add_f32 v[140:141], v[140:141], 1.0 op_sel_hi:[1,0]\n" \
  "s_waitcnt lgkmcnt(10)\n" \
  "v_smfmac_f32_16x16x64_bf16 v[40:43], v[180:183], v[108:115], v192\n" \
  "v_lshlrev_b32_sdwa v66, 4, v221 dst_sel:DWORD dst_unused:UNUSED_PAD src0_sel:DWORD src1_sel:WORD_0\n" \
  "ds_read_b128 v[108:111], v66\n" \
  "v_lshlrev_b32_sdwa v67, 4, v221 dst_sel:DWORD dst_unused:UNUSED_PAD src0_sel:DWORD src1_sel:WORD_1\n" \
  "ds_read_b128 v[112:115], v67\n" \
  "v_pk_add_f32 v[142:143], v[142:143], 1.0 op_sel_hi:[1,0]\n" \
  "v_rcp_f32 v140, v140\n" \
  "v_rcp_f32 v141, v141\n" \
  "v_rcp_f32 v142, v142\n" \
  "v_rcp_f32 v143, v143\n" \
  "v_pk_mul_f32 v[28:29], v[28:29], v[140:141]\n" \
  "v_pk_fma_f32 v[28:29], v[30:31], v[142:143], v[28:29]\n" \
  "v_add_f32 v28, v28, v29\n" \
  "v_permlane16_swap_b32 v0, v16\n" \
  "v_permlane16_swap_b32 v4, v20\n" \
  "v_permlane16_swap_b32 v8, v24\n" \
  "v_permlane16_swap_b32 v12, v28\n" \
  "v_add_f32 v144, v0, v16\n" \
  "v_add_f32 v145, v4, v20\n" \
  "v_add_f32 v146, v8, v24\n" \
  "v_add_f32 v147, v12, v28\n" \
  "s_waitcnt lgkmcnt(10)\n" \
  "v_smfmac_f32_16x16x64_bf16 v[44:47], v[180:183], v[68:75], v192\n" \
  "v_lshlrev_b32_sdwa v64, 4, v254 dst_sel:DWORD dst_unused:UNUSED_PAD src0_sel:DWORD src1_sel:WORD_0\n" \
  "ds_read_b128 v[68:71], v64\n" \
  "v_lshlrev_b32_sdwa v65, 4, v254 dst_sel:DWORD dst_unused:UNUSED_PAD src0_sel:DWORD src1_sel:WORD_1\n" \
  "ds_read_b128 v[72:75], v65\n" \
  "v_min_u32 v144, -2, v144\n" \
  "v_min_u32 v145, -2, v145\n" \
  "v_min_u32 v146, -2, v146\n" \
  "v_min_u32 v147, -2, v147\n" \
  "global_store_dwordx4 v188, v[144:147], s[40:41] offset:0 sc1\n" \
  "v_pk_mul_f32 v[128:129], v[32:33], v[32:33]\n" \
  "v_pk_mul_f32 v[130:131], v[34:35], v[34:35]\n" \
  "v_pk_fma_f32 v[128:129], v[128:129], s[52:53], v[124:125]\n" \
  "v_pk_fma_f32 v[130:131], v[130:131], s[52:53], v[124:125]\n" \
  "v_pk_mul_f32 v[128:129], v[128:129], v[32:33]\n" \
  "v_pk_mul_f32 v[130:131], v[130:131], v[34:35]\n" \
  "v_exp_f32 v128, v128\n" \
  "v_exp_f32 v129, v129\n" \
  "v_pk_mul_f32 v[32:33], v[32:33], v[120:121]\n" \
  "v_exp_f32 v130, v130\n" \
  "v_exp_f32 v131, v131\n" \
  "s_waitcnt lgkmcnt(10)\n" \
  "v_smfmac_f32_16x16x64_bf16 v[44:47], v[180:183], v[76:83], v192\n" \
  "v_lshlrev_b32_sdwa v66, 4, v222 dst_sel:DWORD dst_unused:UNUSED_PAD src0_sel:DWORD src1_sel:WORD_0\n" \
  "ds_read_b128 v[76:79], v66\n" \
  "v_lshlrev_b32_sdwa v67, 4, v222 dst_sel:DWORD dst_unused:UNUSED_PAD src0_sel:DWORD src1_sel:WORD_1\n" \
  "ds_read_b128 v[80:83], v67\n" \
  "v_pk_mul_f32 v[34:35], v[34:35], v[122:123]\n" \
  "v_pk_add_f32 v[128:129], v[128:129], 1.0 op_sel_hi:[1,0]\n" \
  "v_pk_add_f32 v[130:131], v[130:131], 1.0 op_sel_hi:[1,0]\n" \
  "v_rcp_f32 v128, v128\n" \
  "v_rcp_f32 v129, v129\n" \
  "v_rcp_f32 v130, v130\n" \
  "v_rcp_f32 v131, v131\n" \
  "v_pk_mul_f32 v[32:33], v[32:33], v[128:129]\n" \
  "v_pk_fma_f32 v[32:33], v[34:35], v[130:131], v[32:33]\n" \
  "v_add_f32 v32, v32, v33\n" \
  "v_pk_mul_f32 v[132:133], v[36:37], v[36:37]\n" \
  "v_pk_mul_f32 v[134:135], v[38:39], v[38:39]\n" \
  "v_pk_fma_f32 v[132:133], v[132:133], s[52:53], v[124:125]\n" \
  "v_pk_fma_f32 v[134:135], v[134:135], s[52:53], v[124:125]\n" \
  "v_pk_mul_f32 v[132:133], v[132:133], v[36:37]\n" \
  "v_pk_mul_f32 v[134:135], v[134:135], v[38:39]\n" \
  "s_waitcnt lgkmcnt(10)\n" \
  "v_smfmac_f32_16x16x64_bf16 v[48:51], v[180:183], v[84:91], v192\n" \
  "v_lshlrev_b32_sdwa v64, 4, v255 dst_sel:DWORD dst_unused:UNUSED_PAD src0_sel:DWORD src1_sel:WORD_0\n" \
  "ds_read_b128 v[84:87], v64\n" \
  "v_lshlrev_b32_sdwa v65, 4, v255 dst_sel:DWORD dst_unused:UNUSED_PAD src0_sel:DWORD src1_sel:WORD_1\n" \
  "ds_read_b128 v[88:91], v65\n" \
  "v_exp_f32 v132, v132\n" \
  "v_exp_f32 v133, v133\n" \
  "v_pk_mul_f32 v[36:37], v[36:37], v[120:121]\n" \
  "v_exp_f32 v134, v134\n" \
  "v_exp_f32 v135, v135\n" \
  "v_pk_mul_f32 v[38:39], v[38:39], v[122:123]\n" \
  "v_pk_add_f32 v[132:133], v[132:133], 1.0 op_sel_hi:[1,0]\n" \
  "v_pk_add_f32 v[134:135], v[134:135], 1.0 op_sel_hi:[1,0]\n" \
  "v_rcp_f32 v132, v132\n" \
  "v_rcp_f32 v133, v133\n" \
  "v_rcp_f32 v134, v134\n" \
  "v_rcp_f32 v135, v135\n" \
  "v_pk_mul_f32 v[36:37], v[36:37], v[132:133]\n" \
  "v_pk_fma_f32 v[36:37], v[38:39], v[134:135], v[36:37]\n" \
  "v_add_f32 v36, v36, v37\n" \
  "v_pk_mul_f32 v[136:137], v[40:41], v[40:41]\n" \
  "s_waitcnt lgkmcnt(10)\n" \
  "v_smfmac_f32_16x16x64_bf16 v[48:51], v[180:183], v[92:99], v192\n" \
  "v_lshlrev_b32_sdwa v66, 4, v223 dst_sel:DWORD dst_unused:UNUSED_PAD src0_sel:DWORD src1_sel:WORD_0\n" \
  "ds_read_b128 v[92:95], v66\n" \
  "v_lshlrev_b32_sdwa v67, 4, v223 dst_sel:DWORD dst_unused:UNUSED_PAD src0_sel:DWORD src1_sel:WORD_1\n" \
  "ds_read_b128 v[96:99], v67\n" \
  "v_pk_mul_f32 v[138:139], v[42:43], v[42:43]\n" \
  "v_pk_fma_f32 v[136:137], v[136:137], s[52:53], v[124:125]\n" \
  "v_pk_fma_f32 v[138:139], v[138:139], s[52:53], v[124:125]\n" \
  "v_pk_mul_f32 v[136:137], v[136:137], v[40:41]\n" \
  "v_pk_mul_f32 v[138:139], v[138:139], v[42:43]\n" \
  "v_exp_f32 v136, v136\n" \
  "v_exp_f32 v137, v137\n" \
  "v_pk_mul_f32 v[40:41], v[40:41], v[120:121]\n" \
  "v_exp_f32 v138, v138\n" \
  "v_exp_f32 v139, v139\n" \
  "v_pk_mul_f32 v[42:43], v[42:43], v[122:123]\n" \
  "v_pk_add_f32 v[136:137], v[136:137], 1.0 op_sel_hi:[1,0]\n" \
  "v_pk_add_f32 v[138:139], v[138:139], 1.0 op_sel_hi:[1,0]\n" \
  "v_rcp_f32 v136, v136\n" \
  "v_rcp_f32 v137, v137\n" \
  "v_rcp_f32 v138, v138\n" \
  "s_waitcnt lgkmcnt(10)\n" \
  "v_smfmac_f32_16x16x64_bf16 v[52:55], v[180:183], v[100:107], v192\n" \
  "v_rcp_f32 v139, v139\n" \
  "v_pk_mul_f32 v[40:41], v[40:41], v[136:137]\n" \
  "v_pk_fma_f32 v[40:41], v[42:43], v[138:139], v[40:41]\n" \
  "v_add_f32 v40, v40, v41\n" \
  "v_pk_mul_f32 v[140:141], v[44:45], v[44:45]\n" \
  "v_pk_mul_f32 v[142:143], v[46:47], v[46:47]\n" \
  "v_pk_fma_f32 v[140:141], v[140:141], s[52:53], v[124:125]\n" \
  "v_pk_fma_f32 v[142:143], v[142:143], s[52:53], v[124:125]\n" \
  "v_pk_mul_f32 v[140:141], v[140:141], v[44:45]\n" \
  "v_pk_mul_f32 v[142:143], v[142:143], v[46:47]\n" \
  "v_exp_f32 v140, v140\n" \
  "v_exp_f32 v141, v141\n" \
  "v_pk_mul_f32 v[44:45], v[44:45], v[120:121]\n" \
  "v_exp_f32 v142, v142\n" \
  "v_exp_f32 v143, v143\n" \
  "v_pk_mul_f32 v[46:47], v[46:47], v[122:123]\n" \
  "v_pk_add_f32 v[140:141], v[140:141], 1.0 op_sel_hi:[1,0]\n" \
  "v_pk_add_f32 v[142:143], v[142:143], 1.0 op_sel_hi:[1,0]\n" \
  "v_rcp_f32 v140, v140\n" \
  "v_rcp_f32 v141, v141\n" \
  "v_rcp_f32 v142, v142\n" \
  "v_rcp_f32 v143, v143\n" \
  "v_pk_mul_f32 v[44:45], v[44:45], v[140:141]\n" \
  "v_pk_fma_f32 v[44:45], v[46:47], v[142:143], v[44:45]\n" \
  "v_add_f32 v44, v44, v45\n" \
  "v_pk_mul_f32 v[128:129], v[48:49], v[48:49]\n" \
  "v_pk_mul_f32 v[130:131], v[50:51], v[50:51]\n" \
  "v_pk_fma_f32 v[128:129], v[128:129], s[52:53], v[124:125]\n" \
  "v_pk_fma_f32 v[130:131], v[130:131], s[52:53], v[124:125]\n" \
  "v_pk_mul_f32 v[128:129], v[128:129], v[48:49]\n" \
  "v_pk_mul_f32 v[130:131], v[130:131], v[50:51]\n" \
  "v_exp_f32 v128, v128\n" \
  "s_waitcnt lgkmcnt(8)\n" \
  "v_smfmac_f32_16x16x64_bf16 v[52:55], v[180:183], v[108:115], v192\n" \
  "v_exp_f32 v129, v129\n" \
  "v_pk_mul_f32 v[48:49], v[48:49], v[120:121]\n" \
  "v_exp_f32 v130, v130\n" \
  "v_exp_f32 v131, v131\n" \
  "v_pk_mul_f32 v[50:51], v[50:51], v[122:123]\n" \
  "v_pk_add_f32 v[128:129], v[128:129], 1.0 op_sel_hi:[1,0]\n" \
  "v_pk_add_f32 v[130:131], v[130:131], 1.0 op_sel_hi:[1,0]\n" \
  "v_rcp_f32 v128, v128\n" \
  "v_rcp_f32 v129, v129\n" \
  "v_rcp_f32 v130, v130\n" \
  "v_rcp_f32 v131, v131\n" \
  "v_pk_mul_f32 v[48:49], v[48:49], v[128:129]\n" \
  "v_pk_fma_f32 v[48:49], v[50:51], v[130:131], v[48:49]\n" \
  "v_add_f32 v48, v48, v49\n" \
  "s_waitcnt lgkmcnt(6)\n" \
  "v_smfmac_f32_16x16x64_bf16 v[56:59], v[180:183], v[68:75], v192\n" \
  "v_pk_mul_f32 v[132:133], v[52:53], v[52:53]\n" \
  "v_pk_mul_f32 v[134:135], v[54:55], v[54:55]\n" \
  "v_pk_fma_f32 v[132:133], v[132:133], s[52:53], v[124:125]\n" \
  "v_pk_fma_f32 v[134:135], v[134:135], s[52:53], v[124:125]\n" \
  "v_pk_mul_f32 v[132:133], v[132:133], v[52:53]\n" \
  "v_pk_mul_f32 v[134:135], v[134:135], v[54:55]\n" \
  "v_exp_f32 v132, v132\n" \
  "v_exp_f32 v133, v133\n" \
  "v_pk_mul_f32 v[52:53], v[52:53], v[120:121]\n" \
  "v_exp_f32 v134, v134\n" \
  "v_exp_f32 v135, v135\n" \
  "v_pk_mul_f32 v[54:55], v[54:55], v[122:123]\n" \
  "v_pk_add_f32 v[132:133], v[132:133], 1.0 op_sel_hi:[1,0]\n" \
  "v_pk_add_f32 v[134:135], v[134:135], 1.0 op_sel_hi:[1,0]\n" \
  "v_rcp_f32 v132, v132\n" \
  "v_rcp_f32 v133, v133\n" \
  "v_rcp_f32 v134, v134\n" \
  "v_rcp_f32 v135, v135\n" \
  "v_pk_mul_f32 v[52:53], v[52:53], v[132:133]\n" \
  "v_pk_fma_f32 v[52:53], v[54:55], v[134:135], v[52:53]\n" \
  "v_add_f32 v52, v52, v53\n" \
  "s_waitcnt lgkmcnt(4)\n" \
  "v_smfmac_f32_16x16x64_bf16 v[56:59], v[180:183], v[76:83], v192\n" \
  "s_waitcnt lgkmcnt(2)\n" \
  "v_smfmac_f32_16x16x64_bf16 v[60:63], v[180:183], v[84:91], v192\n" \
  "s_nop 5\n" \
  "v_pk_mul_f32 v[136:137], v[56:57], v[56:57]\n" \
  "v_pk_mul_f32 v[138:139], v[58:59], v[58:59]\n" \
  "v_pk_fma_f32 v[136:137], v[136:137], s[52:53], v[124:125]\n" \
  "v_pk_fma_f32 v[138:139], v[138:139], s[52:53], v[124:125]\n" \
  "v_pk_mul_f32 v[136:137], v[136:137], v[56:57]\n" \
  "v_pk_mul_f32 v[138:139], v[138:139], v[58:59]\n" \
  "v_exp_f32 v136, v136\n" \
  "v_exp_f32 v137, v137\n" \
  "v_pk_mul_f32 v[56:57], v[56:57], v[120:121]\n" \
  "v_exp_f32 v138, v138\n" \
  "v_exp_f32 v139, v139\n" \
  "v_pk_mul_f32 v[58:59], v[58:59], v[122:123]\n" \
  "v_pk_add_f32 v[136:137], v[136:137], 1.0 op_sel_hi:[1,0]\n" \
  "v_pk_add_f32 v[138:139], v[138:139], 1.0 op_sel_hi:[1,0]\n" \
  "v_rcp_f32 v136, v136\n" \
  "v_rcp_f32 v137, v137\n" \
  "v_rcp_f32 v138, v138\n" \
  "v_rcp_f32 v139, v139\n" \
  "v_pk_mul_f32 v[56:57], v[56:57], v[136:137]\n" \
  "v_pk_fma_f32 v[56:57], v[58:59], v[138:139], v[56:57]\n" \
  "v_add_f32 v56, v56, v57\n" \
  "s_waitcnt lgkmcnt(0)\n" \
  "v_smfmac_f32_16x16x64_bf16 v[60:63], v[180:183], v[92:99], v192\n" \
  "s_nop 7\n" \
  "v_pk_mul_f32 v[140:141], v[60:61], v[60:61]\n" \
  "v_pk_mul_f32 v[142:143], v[62:63], v[62:63]\n" \
  "v_pk_fma_f32 v[140:141], v[140:141], s[52:53], v[124:125]\n" \
  "v_pk_fma_f32 v[142:143], v[142:143], s[52:53], v[124:125]\n" \
  "v_pk_mul_f32 v[140:141], v[140:141], v[60:61]\n" \
  "v_pk_mul_f32 v[142:143], v[142:143], v[62:63]\n" \
  "v_exp_f32 v140, v140\n" \
  "v_exp_f32 v141, v141\n" \
  "v_pk_mul_f32 v[60:61], v[60:61], v[120:121]\n" \
  "v_exp_f32 v142, v142\n" \
  "v_exp_f32 v143, v143\n" \
  "v_pk_mul_f32 v[62:63], v[62:63], v[122:123]\n" \
  "v_pk_add_f32 v[140:141], v[140:141], 1.0 op_sel_hi:[1,0]\n" \
  "v_pk_add_f32 v[142:143], v[142:143], 1.0 op_sel_hi:[1,0]\n" \
  "v_rcp_f32 v140, v140\n" \
  "v_rcp_f32 v141, v141\n" \
  "v_rcp_f32 v142, v142\n" \
  "v_rcp_f32 v143, v143\n" \
  "v_pk_mul_f32 v[60:61], v[60:61], v[140:141]\n" \
  "v_pk_fma_f32 v[60:61], v[62:63], v[142:143], v[60:61]\n" \
  "v_add_f32 v60, v60, v61\n" \
  "v_permlane16_swap_b32 v32, v48\n" \
  "v_permlane16_swap_b32 v36, v52\n" \
  "v_permlane16_swap_b32 v40, v56\n" \
  "v_permlane16_swap_b32 v44, v60\n" \
  "v_add_f32 v148, v32, v48\n" \
  "v_add_f32 v149, v36, v52\n" \
  "v_add_f32 v150, v40, v56\n" \
  "v_add_f32 v151, v44, v60\n" \
  "v_min_u32 v148, -2, v148\n" \
  "v_min_u32 v149, -2, v149\n" \
  "v_min_u32 v150, -2, v150\n" \
  "v_min_u32 v151, -2, v151\n" \
  "global_store_dwordx4 v188, v[148:151], s[40:41] offset:1024 sc1\n" \
  "v_lshrrev_b32 v0, 4, v188\n" \
  "v_lshrrev_b32 v1, 4, v0\n" \
  "v_and_b32 v2, 15, v0\n" \
  "v_lshlrev_b32 v2, 4, v2\n" \
  "v_lshl_add_u32 v1, v1, 17, v2\n" \
  "s_lshl_b32 s35, s27, 19\n" \
  "s_lshl_b32 s36, s26, 8\n" \
  "s_add_u32 s35, s35, s36\n" \
  "s_add_u32 s35, s35, 105676800\n" \
  "s_add_u32 s40, s24, s35\n" \
  "s_addc_u32 s41, s25, 0\n" \
  "s_add_u32 s42, s40, 65536\n" \
  "s_addc_u32 s43, s41, 0\n" \
  "s_add_u32 s44, s24, 109871104\n" \
  "s_addc_u32 s45, s25, 0\n" \
  "s_add_u32 s46, s44, 65536\n" \
  "s_addc_u32 s47, s45, 0\n" \
  "s_lshr_b32 s35, s26, 2\n" \
  "s_lshl_b32 s35, s35, 8\n" \
  "s_and_b32 s36, s26, 1\n" \
  "s_lshl_b32 s36, s36, 7\n" \
  "s_add_u32 s35, s35, s36\n" \
  "s_and_b32 s36, s26, 2\n" \
  "s_lshl3_add_u32 s35, s36, s35\n" \
  "v_and_b32 v2, 3, v0\n" \
  "v_lshrrev_b32 v3, 2, v0\n" \
  "v_lshl_add_u32 v2, v2, 5, v3\n" \
  "v_add_u32 v2, s35, v2\n" \
  "v_lshlrev_b32 v3, 2, v2\n" \
  "s_cmp_eq_u32 s27, 0\n" \
  "s_cselect_b32 s35, -1, 0\n" \
  "v_and_b32 v3, s35, v3\n" \
  "v_mov_b32 v4, s6\n" \
  "s_mov_b32 s5, 0x10000\n" \
  "s_barrier\n" \
  "s_sleep 10\n" \
  "LF_poll:\n" \
  "global_load_dwordx4 v[8:11], v1, s[40:41] sc1\n" \
  "global_load_dwordx4 v[12:15], v1, s[42:43] sc1\n" \
  "global_load_dword v16, v3, s[44:45] sc1\n" \
  "global_load_dword v17, v3, s[46:47] sc1\n" \
  "s_waitcnt vmcnt(0)\n" \
  "v_max3_u32 v5, v8, v9, v10\n" \
  "v_max3_u32 v6, v11, v12, v13\n" \
  "v_max3_u32 v5, v5, v14, v15\n" \
  "v_max3_u32 v6, v6, v16, v17\n" \
  "v_max_u32 v5, v5, v6\n" \
  "v_cmp_eq_u32 vcc, -1, v5\n" \
  "s_sub_u32 s5, s5, 1\n" \
  "s_cbranch_scc1 LF_go\n" \
  "s_nop 2\n" \
  "s_cbranch_vccz LF_go\n" \
  "s_mov_b64 exec, vcc\n" \
  "s_sleep 2\n" \
  "s_branch LF_poll\n" \
  "LF_go:\n" \
  "s_mov_b64 exec, -1\n" \
  "v_pk_add_f32 v[8:9], v[8:9], v[12:13]\n" \
  "v_pk_add_f32 v[10:11], v[10:11], v[14:15]\n" \
  "s_nop 0\n" \
  "v_permlane16_swap_b32 v8, v9\n" \
  "v_permlane16_swap_b32 v10, v11\n" \
  "v_add_f32 v8, v8, v9\n" \
  "v_add_f32 v10, v10, v11\n" \
  "s_nop 1\n" \
  "v_permlane32_swap_b32 v8, v10\n" \
  "v_add_f32 v8, v8, v10\n" \
  "v_and_b32 v5, 15, v0\n" \
  "v_lshrrev_b32 v6, 4, v0\n" \
  "v_lshl_add_u32 v5, v5, 2, v6\n" \
  "s_lshl_b32 s35, s27, 8\n" \
  "v_lshl_add_u32 v5, v5, 2, s35\n" \
  "ds_write_b32 v5, v8\n" \
  "s_waitcnt lgkmcnt(0)\n" \
  "s_barrier\n" \
  "s_cmp_lg_u32 s27, 0\n" \
  "s_cbranch_scc1 LF_end\n" \
  "v_lshlrev_b32 v5, 2, v0\n" \
  "ds_read2st64_b32 v[8:9], v5 offset0:0 offset1:1\n" \
  "ds_read2st64_b32 v[10:11], v5 offset0:2 offset1:3\n" \
  "ds_read2st64_b32 v[12:13], v5 offset0:4 offset1:5\n" \
  "ds_read2st64_b32 v[14:15], v5 offset0:6 offset1:7\n" \
  "v_add_f32 v16, v16, v17\n" \
  "v_lshlrev_b32 v4, 16, v4\n" \
  "v_cvt_pk_bf16_f32 v16, v16, v16\n" \
  "v_lshlrev_b32 v2, 1, v2\n" \
  "s_waitcnt lgkmcnt(0)\n" \
  "v_pk_add_f32 v[8:9], v[8:9], v[10:11]\n" \
  "v_pk_add_f32 v[12:13], v[12:13], v[14:15]\n" \
  "v_pk_add_f32 v[8:9], v[8:9], v[12:13]\n" \
  "v_add_f32 v8, v8, v9\n" \
  "v_cvt_pk_bf16_f32 v8, v8, v8\n" \
  "v_lshlrev_b32 v8, 16, v8\n" \
  "v_add_f32 v8, v8, v4\n" \
  "v_cvt_pk_bf16_f32 v8, v8, v8\n" \
  "v_lshlrev_b32 v8, 16, v8\n" \
  "v_lshlrev_b32 v16, 16, v16\n" \
  "v_add_f32 v8, v8, v16\n" \
  "v_cvt_pk_bf16_f32 v8, v8, v8\n" \
  "global_store_short v2, v8, s[22:23]\n" \
  "LF_end:\n" \
  "s_branch LZ_end\n" \
  "LG_pol:\n" \
  "s_mov_b64 exec, 0xff\n" \
  "global_load_dword v200, v198, s[48:49] offset:3076 sc1\n" \
  "s_mov_b64 exec, -1\n" \
  "s_branch LG_rt0\n" \
  "LG_chk:\n" \
  "s_mov_b32 s5, 0x10000\n" \
  "LG_p:\n" \
  "s_mov_b64 exec, 0xff\n" \
  "v_cmp_le_u32 vcc, 32, v200\n" \
  "s_mov_b64 exec, -1\n" \
  "s_sub_u32 s5, s5, 1\n" \
  "s_cselect_b32 s35, 0xff, vcc_lo\n" \
  "s_cmp_eq_u32 s35, 0xff\n" \
  "s_cbranch_scc1 LG_p_x\n" \
  "s_sleep 2\n" \
  "s_mov_b64 exec, 0xff\n" \
  "global_load_dword v200, v198, s[48:49] offset:3076 sc1\n" \
  "s_mov_b64 exec, -1\n" \
  "s_waitcnt vmcnt(0)\n" \
  "s_branch LG_p\n" \
  "LG_p_x:\n" \
  "s_branch LG_ret\n" \
  "LZ_end:\n" \
  "s_waitcnt vmcnt(0) lgkmcnt(0)\n"

#define R10(p, b) p #b "0", p #b "1", p #b "2", p #b "3", p #b "4", p #b "5", p #b "6", p #b "7", p #b "8", p #b "9"
__global__ __launch_bounds__(512, 2) void k1(const int* __restrict__ idx, const unsigned short* __restrict__ table,
                                             const unsigned short* __restrict__ w_p, const unsigned short* __restrict__ w1,
                                             const unsigned short* __restrict__ b1, const unsigned short* __restrict__ w2,
                                             const unsigned short* __restrict__ b2, __hip_bfloat16* __restrict__ out, char* __restrict__ ws) {
  __shared__ __attribute__((aligned(1024))) char smem[163840];
  unsigned tid = threadIdx.x;
  asm volatile(K_ASM
               : "+{v0}"(tid)
               : "{s[0:1]}"(__builtin_amdgcn_kernarg_segment_ptr()), "{s26}"(blockIdx.x), "s"((unsigned)(uintptr_t)smem)
               : "memory", "scc", "vcc", "s3", "s4", "s5", "s6", "s7", "s8", "s9", R10("s", 1), "s20", "s21", "s22", "s23", "s24", "s25", "s27", "s28", "s29", "s30", "s31", "s33", "s34", "s35", "s36", "s37", "s38", "s39", R10("s", 4), R10("s", 5), R10("s", 6), R10("s", 7), R10("s", 8), R10("s", 9),
                 "v1", "v2", "v3", "v4", "v5", "v6", "v7", "v8", "v9", R10("v", 1), R10("v", 2), R10("v", 3), R10("v", 4), R10("v", 5), R10("v", 6), R10("v", 7), R10("v", 8), R10("v", 9),
                 R10("v", 10), R10("v", 11), R10("v", 12), R10("v", 13), R10("v", 14), R10("v", 15), R10("v", 16), R10("v", 17), R10("v", 18), R10("v", 19),
                 R10("v", 20), R10("v", 21), R10("v", 22), R10("v", 23), R10("v", 24), "v250", "v251", "v252", "v253", "v254", "v255");
}

extern "C" void kernel_launch(void* const* d_in, const int* in_sizes, int n_in, void* d_out, int out_size, void* d_ws,
                              size_t ws_size, hipStream_t stream) {
  k1<<<dim3(256), dim3(512), 0, stream>>>((const int*)d_in[0], (const unsigned short*)d_in[1], (const unsigned short*)d_in[2], (const unsigned short*)d_in[3],
                                          (const unsigned short*)d_in[4], (const unsigned short*)d_in[5], (const unsigned short*)d_in[6], (__hip_bfloat16*)d_out, (char*)d_ws);
}
```

```cpp
#include <hip/hip_runtime.h>
#include <hip/hip_bf16.h>
#include <stdint.h>

#define K_ASM \
  "LZ_first:\n" \
  "s_load_dwordx16 s[8:23], s[0:1], 0x0\n" \
  "s_load_dwordx2 s[24:25], s[0:1], 0x40\n" \
  "v_readfirstlane_b32 s27, v0\n" \
  "v_and_b32 v255, 63, v0\n" \
  "s_and_b32 s29, s26, 7\n" \
  "s_lshr_b32 s30, s26, 3\n" \
  "s_lshr_b32 s27, s27, 6\n" \
  "s_and_b32 s31, s30, 1\n" \
  "s_and_b32 s28, s27, 3\n" \
  "s_lshr_b32 s35, s30, 1\n" \
  "s_lshl3_add_u32 s7, s35, s29\n" \
  "v_lshrrev_b32 v1, 3, v255\n" \
  "v_and_b32 v2, 7, v255\n" \
  "v_lshlrev_b32 v1, 8, v1\n" \
  "v_lshl_or_b32 v254, v2, 4, v1\n" \
  "s_mul_i32 s36, s7, 37\n" \
  "s_and_b32 s36, s36, 63\n" \
  "s_add_u32 s36, s36, s28\n" \
  "s_cmp_ge_u32 s27, 4\n" \
  "s_cbranch_scc1 LB_start\n" \
  "s_setprio 3\n" \
  "v_and_b32 v252, 15, v255\n" \
  "v_lshrrev_b32 v253, 4, v255\n" \
  "v_mul_u32_u24 v2, 400, v253\n" \
  "v_min_u32 v3, 8, v252\n" \
  "v_lshl_add_u32 v250, v252, 4, v2\n" \
  "v_lshl_add_u32 v251, v3, 4, v2\n" \
  "s_lshl2_add_u32 s36, s7, s28\n" \
  "s_mul_i32 s36, s36, 12800\n" \
  "s_lshl_b32 s35, s28, 10\n" \
  "v_lshl_add_u32 v1, v255, 4, s35\n" \
  "s_add_u32 m0, s35, 138240\n" \
  "s_waitcnt lgkmcnt(0)\n" \
  "s_add_u32 s44, s8, s36\n" \
  "s_addc_u32 s45, s9, 0\n" \
  "global_load_lds_dwordx4 v1, s[12:13]\n" \
  "s_add_u32 m0, m0, 0x1000\n" \
  "v_add_u32 v1, 0x1000, v1\n" \
  "global_load_lds_dwordx4 v1, s[12:13]\n" \
  "s_add_u32 m0, m0, 0x1000\n" \
  "v_add_u32 v1, 0x1000, v1\n" \
  "global_load_lds_dwordx4 v1, s[12:13]\n" \
  "s_add_u32 m0, m0, 0x1000\n" \
  "v_add_u32 v1, 0x1000, v1\n" \
  "global_load_lds_dwordx4 v1, s[12:13]\n" \
  "s_add_u32 m0, m0, 0x1000\n" \
  "v_add_u32 v1, 0x1000, v1\n" \
  "v_min_u32 v1, 19984, v1\n" \
  "global_load_lds_dwordx4 v1, s[12:13]\n" \
  "global_load_dwordx4 v[232:235], v250, s[44:45] offset:0\n" \
  "global_load_dwordx4 v[236:239], v251, s[44:45] offset:256\n" \
  "global_load_dwordx4 v[240:243], v250, s[44:45] offset:1600\n" \
  "global_load_dwordx4 v[244:247], v251, s[44:45] offset:1856\n" \
  "s_setprio 1\n" \
  "v_lshrrev_b32 v1, 2, v252\n" \
  "v_and_b32 v2, 3, v252\n" \
  "v_cmp_eq_u32 vcc, v1, v253\n" \
  "v_mov_b32 v4, 0x3f80\n" \
  "v_and_b32 v13, 1, v253\n" \
  "v_lshlrev_b32 v14, 1, v13\n" \
  "v_xor_b32 v2, v2, v14\n" \
  "v_cndmask_b32 v2, 4, v2, vcc\n" \
  "v_cmp_eq_u32 vcc, 0, v2\n" \
  "s_nop 1\n" \
  "v_cndmask_b32 v216, 0, v4, vcc\n" \
  "v_lshlrev_b32 v220, 16, v216\n" \
  "v_cmp_eq_u32 vcc, 1, v2\n" \
  "s_nop 1\n" \
  "v_cndmask_b32 v217, 0, v4, vcc\n" \
  "v_lshlrev_b32 v221, 16, v217\n" \
  "v_cmp_eq_u32 vcc, 2, v2\n" \
  "s_nop 1\n" \
  "v_cndmask_b32 v218, 0, v4, vcc\n" \
  "v_lshlrev_b32 v222, 16, v218\n" \
  "v_cmp_eq_u32 vcc, 3, v2\n" \
  "s_nop 1\n" \
  "v_cndmask_b32 v219, 0, v4, vcc\n" \
  "v_lshlrev_b32 v223, 16, v219\n" \
  "v_and_b32 v1, 2, v252\n" \
  "v_lshlrev_b32 v1, 3, v1\n" \
  "v_lshlrev_b32 v1, v1, v4\n" \
  "v_and_b32 v2, 0x31, v255\n" \
  "v_mov_b64 v[224:225], 0\n" \
  "v_cmp_eq_u32 vcc, 0, v2\n" \
  "s_nop 1\n" \
  "v_cndmask_b32 v226, 0, v1, vcc\n" \
  "v_cmp_eq_u32 vcc, 1, v2\n" \
  "s_nop 1\n" \
  "v_cndmask_b32 v227, 0, v1, vcc\n" \
  "v_mul_u32_u24 v5, 400, v252\n" \
  "v_add_u32 v5, 138240, v5\n" \
  "v_lshl_add_u32 v6, v253, 4, v5\n" \
  "v_add_u32 v7, 6400, v5\n" \
  "v_add_u32 v8, 6400, v6\n" \
  "v_add_u32 v9, 12800, v5\n" \
  "v_add_u32 v10, 12800, v6\n" \
  "v_bfe_u32 v1, v252, 1, 1\n" \
  "v_and_b32 v2, 1, v252\n" \
  "v_lshl_add_u32 v1, v2, 1, v1\n" \
  "v_mul_u32_u24 v1, 200, v1\n" \
  "v_add_u32 v11, 157440, v1\n" \
  "v_lshl_add_u32 v12, v253, 4, v11\n" \
  "v_lshlrev_b32 v13, 3, v13\n" \
  "v_sub_u32 v16, v5, v13\n" \
  "v_add_u32 v5, v5, v13\n" \
  "v_sub_u32 v17, v6, v13\n" \
  "v_add_u32 v6, v6, v13\n" \
  "v_sub_u32 v18, v7, v13\n" \
  "v_add_u32 v7, v7, v13\n" \
  "v_sub_u32 v19, v8, v13\n" \
  "v_add_u32 v8, v8, v13\n" \
  "v_sub_u32 v20, v9, v13\n" \
  "v_add_u32 v9, v9, v13\n" \
  "v_sub_u32 v21, v10, v13\n" \
  "v_add_u32 v10, v10, v13\n" \
  "v_sub_u32 v22, v11, v13\n" \
  "v_add_u32 v11, v11, v13\n" \
  "v_sub_u32 v23, v12, v13\n" \
  "v_add_u32 v12, v12, v13\n" \
  "v_lshlrev_b32 v188, 4, v253\n" \
  "v_and_b32 v96, 4, v252\n" \
  "v_lshl_add_u32 v96, v253, 5, v96\n" \
  "v_mov_b32 v191, 0x40000000\n" \
  "v_cmp_gt_u32 vcc, 8, v252\n" \
  "v_mov_b32 v193, 0x4444\n" \
  "v_mov_b32 v98, 0xeeee\n" \
  "v_cndmask_b32 v189, v191, v96, vcc\n" \
  "v_cndmask_b32 v190, v96, v191, vcc\n" \
  "v_and_b32 v99, 4, v252\n" \
  "v_cmp_ne_u32 vcc, 0, v99\n" \
  "s_lshl_b32 s35, s28, 11\n" \
  "s_add_u32 s35, s35, 130048\n" \
  "v_cndmask_b32 v193, v193, v98, vcc\n" \
  "v_lshl_add_u32 v194, v252, 4, s35\n" \
  "v_and_b32 v96, 7, v252\n" \
  "v_lshlrev_b32 v96, 5, v96\n" \
  "v_lshl_add_u32 v96, v253, 8, v96\n" \
  "v_lshrrev_b32 v97, 3, v252\n" \
  "v_lshl_add_u32 v96, v97, 2, v96\n" \
  "v_add_u32 v195, s35, v96\n" \
  "v_and_b32 v97, v97, v252\n" \
  "v_lshlrev_b32 v192, 4, v97\n" \
  "s_waitcnt vmcnt(4)\n" \
  "s_barrier\n" \
  "ds_read_b64 v[200:201], v12 offset:128\n" \
  "ds_read_b64 v[202:203], v23 offset:136\n" \
  "ds_read_b64 v[184:185], v9 offset:184\n" \
  "ds_read_b64 v[186:187], v20 offset:192\n" \
  "ds_read_b64 v[212:213], v9 offset:384\n" \
  "ds_read_b64 v[214:215], v20 offset:392\n" \
  "ds_read_b64 v[196:197], v12 offset:64\n" \
  "ds_read_b64 v[198:199], v23 offset:72\n" \
  "ds_read_b64 v[176:177], v7 offset:184\n" \
  "ds_read_b64 v[178:179], v18 offset:192\n" \
  "ds_read_b64 v[180:181], v7 offset:384\n" \
  "ds_read_b64 v[182:183], v18 offset:392\n" \
  "ds_read_b64 v[228:229], v12 offset:0\n" \
  "ds_read_b64 v[230:231], v23 offset:8\n" \
  "s_waitcnt lgkmcnt(12)\n" \
  "v_mfma_f32_16x16x32_bf16 v[44:47], v[200:203], v[216:219], 0\n" \
  "ds_read_b64 v[168:169], v5 offset:184\n" \
  "ds_read_b64 v[170:171], v16 offset:192\n" \
  "s_waitcnt lgkmcnt(12)\n" \
  "v_mfma_f32_16x16x32_bf16 v[44:47], v[224:227], v[184:187], v[44:47]\n" \
  "ds_read_b64 v[172:173], v5 offset:384\n" \
  "ds_read_b64 v[174:175], v16 offset:392\n" \
  "v_mfma_f32_16x16x32_bf16 v[208:211], v[200:203], v[220:223], 0\n" \
  "ds_read_b64 v[96:97], v6 offset:0\n" \
  "ds_read_b64 v[98:99], v17 offset:8\n" \
  "s_waitcnt lgkmcnt(14)\n" \
  "v_mfma_f32_16x16x32_bf16 v[208:211], v[224:227], v[212:215], v[208:211]\n" \
  "ds_read_b64 v[124:125], v8 offset:64\n" \
  "ds_read_b64 v[126:127], v19 offset:72\n" \
  "s_waitcnt lgkmcnt(14)\n" \
  "v_mfma_f32_16x16x32_bf16 v[60:63], v[196:199], v[216:219], 0\n" \
  "ds_read_b64 v[136:137], v8 offset:264\n" \
  "ds_read_b64 v[138:139], v19 offset:272\n" \
  "s_waitcnt lgkmcnt(14)\n" \
  "v_mfma_f32_16x16x32_bf16 v[60:63], v[224:227], v[176:179], v[60:63]\n" \
  "ds_read_b64 v[140:141], v8 offset:328\n" \
  "ds_read_b64 v[142:143], v19 offset:336\n" \
  "v_mfma_f32_16x16x32_bf16 v[184:187], v[196:199], v[220:223], 0\n" \
  "ds_read_b64 v[160:161], v10 offset:264\n" \
  "ds_read_b64 v[162:163], v21 offset:272\n" \
  "s_waitcnt lgkmcnt(15)\n" \
  "v_mfma_f32_16x16x32_bf16 v[184:187], v[224:227], v[180:183], v[184:187]\n" \
  "ds_read_b64 v[116:117], v6 offset:328\n" \
  "ds_read_b64 v[118:119], v17 offset:336\n" \
  "s_waitcnt lgkmcnt(15)\n" \
  "v_mfma_f32_16x16x32_bf16 v[68:71], v[228:231], v[216:219], 0\n" \
  "ds_read_b64 v[144:145], v10 offset:0\n" \
  "ds_read_b64 v[146:147], v21 offset:8\n" \
  "s_waitcnt lgkmcnt(15)\n" \
  "v_mfma_f32_16x16x32_bf16 v[68:71], v[224:227], v[168:171], v[68:71]\n" \
  "ds_read_b64 v[148:149], v10 offset:64\n" \
  "ds_read_b64 v[150:151], v21 offset:72\n" \
  "v_mfma_f32_16x16x32_bf16 v[212:215], v[228:231], v[220:223], 0\n" \
  "ds_read_b64 v[104:105], v6 offset:128\n" \
  "ds_read_b64 v[106:107], v17 offset:136\n" \
  "s_waitcnt lgkmcnt(15)\n" \
  "v_mfma_f32_16x16x32_bf16 v[212:215], v[224:227], v[172:175], v[212:215]\n" \
  "ds_read_b64 v[100:101], v6 offset:64\n" \
  "ds_read_b64 v[102:103], v17 offset:72\n" \
  "v_mfma_f32_16x16x32_bf16 v[92:95], v[96:99], v[216:219], 0\n" \
  "ds_read_b64 v[120:121], v8 offset:0\n" \
  "ds_read_b64 v[122:123], v19 offset:8\n" \
  "s_waitcnt lgkmcnt(15)\n" \
  "v_mfma_f32_16x16x32_bf16 v[24:27], v[124:127], v[220:223], 0\n" \
  "ds_read_b64 v[112:113], v6 offset:264\n" \
  "ds_read_b64 v[114:115], v17 offset:272\n" \
  "v_mfma_f32_16x16x32_bf16 v[24:27], v[216:219], v[136:139], v[24:27]\n" \
  "ds_read_b64 v[156:157], v10 offset:200\n" \
  "ds_read_b64 v[158:159], v21 offset:208\n" \
  "s_waitcnt lgkmcnt(15)\n" \
  "v_mfma_f32_16x16x32_bf16 v[28:31], v[140:143], v[220:223], 0\n" \
  "ds_read_b64 v[132:133], v8 offset:200\n" \
  "ds_read_b64 v[134:135], v19 offset:208\n" \
  "v_mfma_f32_16x16x32_bf16 v[28:31], v[220:223], v[160:163], v[28:31]\n" \
  "ds_read_b64 v[108:109], v6 offset:200\n" \
  "ds_read_b64 v[110:111], v17 offset:208\n" \
  "s_waitcnt lgkmcnt(15)\n" \
  "v_mfma_f32_16x16x32_bf16 v[32:35], v[116:119], v[216:219], 0\n" \
  "ds_read_b64 v[152:153], v10 offset:128\n" \
  "ds_read_b64 v[154:155], v21 offset:136\n" \
  "v_mfma_f32_16x16x32_bf16 v[32:35], v[220:223], v[144:147], v[32:35]\n" \
  "ds_read_b64 v[164:165], v10 offset:328\n" \
  "ds_read_b64 v[166:167], v21 offset:336\n" \
  "v_mfma_f32_16x16x32_bf16 v[36:39], v[140:143], v[216:219], 0\n" \
  "ds_read_b64 v[128:129], v8 offset:128\n" \
  "ds_read_b64 v[130:131], v19 offset:136\n" \
  "s_waitcnt lgkmcnt(15)\n" \
  "v_mfma_f32_16x16x32_bf16 v[36:39], v[220:223], v[148:151], v[36:39]\n" \
  "ds_read_b64 v[204:205], v11 offset:184\n" \
  "ds_read_b64 v[206:207], v22 offset:192\n" \
  "v_mfma_f32_16x16x32_bf16 v[40:43], v[104:107], v[216:219], 0\n" \
  "v_mfma_f32_16x16x32_bf16 v[40:43], v[216:219], v[144:147], v[40:43]\n" \
  "v_mfma_f32_16x16x32_bf16 v[48:51], v[100:103], v[216:219], 0\n" \
  "s_waitcnt lgkmcnt(15)\n" \
  "v_mfma_f32_16x16x32_bf16 v[48:51], v[216:219], v[120:123], v[48:51]\n" \
  "s_waitcnt lgkmcnt(14)\n" \
  "v_mfma_f32_16x16x32_bf16 v[52:55], v[112:115], v[216:219], 0\n" \
  "v_mfma_f32_16x16x32_bf16 v[52:55], v[220:223], v[120:123], v[52:55]\n" \
  "v_mfma_f32_16x16x32_bf16 v[56:59], v[116:119], v[220:223], 0\n" \
  "s_waitcnt lgkmcnt(12)\n" \
  "v_mfma_f32_16x16x32_bf16 v[56:59], v[220:223], v[156:159], v[56:59]\n" \
  "s_waitcnt vmcnt(0) lgkmcnt(0)\n" \
  "v_mfma_f32_16x16x32_bf16 v[64:67], v[104:107], v[220:223], 0\n" \
  "v_cvt_pk_u16_u32 v232, v232, v233\n" \
  "v_cvt_pk_u16_u32 v234, v234, v235\n" \
  "v_mfma_f32_16x16x32_bf16 v[64:67], v[216:219], v[156:159], v[64:67]\n" \
  "v_cvt_pk_u16_u32 v236, v236, v237\n" \
  "v_cvt_pk_u16_u32 v238, v238, v239\n" \
  "v_mfma_f32_16x16x32_bf16 v[72:75], v[136:139], v[220:223], 0\n" \
  "v_pk_lshrrev_b16 v233, 3, v232 op_sel_hi:[0,1]\n" \
  "v_pk_lshrrev_b16 v235, 3, v234 op_sel_hi:[0,1]\n" \
  "v_mfma_f32_16x16x32_bf16 v[76:79], v[112:115], v[220:223], 0\n" \
  "v_pk_lshrrev_b16 v237, 3, v236 op_sel_hi:[0,1]\n" \
  "v_pk_lshrrev_b16 v239, 3, v238 op_sel_hi:[0,1]\n" \
  "v_mfma_f32_16x16x32_bf16 v[76:79], v[220:223], v[132:135], v[76:79]\n" \
  "v_pk_mad_u16 v196, v232, 8, v233 op_sel_hi:[1,0,1]\n" \
  "v_pk_mad_u16 v197, v234, 8, v235 op_sel_hi:[1,0,1]\n" \
  "v_mfma_f32_16x16x32_bf16 v[80:83], v[100:103], v[220:223], 0\n" \
  "v_pk_mad_u16 v198, v236, 8, v237 op_sel_hi:[1,0,1]\n" \
  "v_pk_mad_u16 v199, v238, 8, v239 op_sel_hi:[1,0,1]\n" \
  "v_mfma_f32_16x16x32_bf16 v[80:83], v[216:219], v[132:135], v[80:83]\n" \
  "v_alignbit_b32 v198, v198, v198, v192\n" \
  "v_alignbit_b32 v199, v199, v199, v192\n" \
  "v_mfma_f32_16x16x32_bf16 v[84:87], v[96:99], v[220:223], 0\n" \
  "v_cvt_pk_u16_u32 v240, v240, v241\n" \
  "v_cvt_pk_u16_u32 v242, v242, v243\n" \
  "v_mfma_f32_16x16x32_bf16 v[84:87], v[216:219], v[108:111], v[84:87]\n" \
  "v_cvt_pk_u16_u32 v244, v244, v245\n" \
  "v_cvt_pk_u16_u32 v246, v246, v247\n" \
  "v_mfma_f32_16x16x32_bf16 v[88:91], v[108:111], v[220:223], 0\n" \
  "v_pk_lshrrev_b16 v241, 3, v240 op_sel_hi:[0,1]\n" \
  "v_pk_lshrrev_b16 v243, 3, v242 op_sel_hi:[0,1]\n" \
  "v_mfma_f32_16x16x32_bf16 v[0:3], v[124:127], v[216:219], 0\n" \
  "v_pk_lshrrev_b16 v245, 3, v244 op_sel_hi:[0,1]\n" \
  "v_pk_lshrrev_b16 v247, 3, v246 op_sel_hi:[0,1]\n" \
  "v_mfma_f32_16x16x32_bf16 v[4:7], v[152:155], v[216:219], 0\n" \
  "v_pk_mad_u16 v180, v240, 8, v241 op_sel_hi:[1,0,1]\n" \
  "v_pk_mad_u16 v181, v242, 8, v243 op_sel_hi:[1,0,1]\n" \
  "v_mfma_f32_16x16x32_bf16 v[8:11], v[152:155], v[220:223], 0\n" \
  "v_pk_mad_u16 v182, v244, 8, v245 op_sel_hi:[1,0,1]\n" \
  "v_pk_mad_u16 v183, v246, 8, v247 op_sel_hi:[1,0,1]\n" \
  "v_mfma_f32_16x16x32_bf16 v[8:11], v[216:219], v[164:167], v[8:11]\n" \
  "v_alignbit_b32 v182, v182, v182, v192\n" \
  "v_alignbit_b32 v183, v183, v183, v192\n" \
  "v_mfma_f32_16x16x32_bf16 v[12:15], v[164:167], v[220:223], 0\n" \
  "v_add_u32 v200, 0x400, v195\n" \
  "v_mfma_f32_16x16x32_bf16 v[16:19], v[128:131], v[220:223], 0\n" \
  "v_mfma_f32_16x16x32_bf16 v[16:19], v[216:219], v[160:163], v[16:19]\n" \
  "v_mfma_f32_16x16x32_bf16 v[20:23], v[128:131], v[216:219], 0\n" \
  "v_mfma_f32_16x16x32_bf16 v[20:23], v[216:219], v[148:151], v[20:23]\n" \
  "v_mfma_f32_16x16x32_bf16 v[168:171], v[216:219], v[96:99], 0\n" \
  "v_mfma_f32_16x16x32_bf16 v[172:175], v[204:207], v[224:227], 0\n" \
  "v_mfma_f32_16x16x32_bf16 v[176:179], v[224:227], v[204:207], 0\n" \
  "s_mov_b32 exec_lo, 0x0\n" \
  "s_mov_b32 exec_hi, 0xffffffff\n" \
  "v_mov_b64 v[44:45], v[208:209]\n" \
  "v_mov_b64 v[46:47], v[210:211]\n" \
  "v_mov_b64 v[60:61], v[184:185]\n" \
  "v_mov_b64 v[62:63], v[186:187]\n" \
  "v_mov_b64 v[68:69], v[212:213]\n" \
  "v_mov_b64 v[70:71], v[214:215]\n" \
  "s_mov_b32 exec_lo, 0xff00ff00\n" \
  "s_mov_b32 exec_hi, 0x0\n" \
  "v_add_f32 v92, v92, v168\n" \
  "v_add_f32 v93, v93, v169\n" \
  "v_add_f32 v94, v94, v170\n" \
  "v_add_f32 v95, v95, v171\n" \
  "s_mov_b32 exec_lo, 0xf0000f00\n" \
  "s_mov_b32 exec_hi, 0x0\n" \
  "v_sub_f32 v92, v92, v176\n" \
  "v_sub_f32 v93, v93, v177\n" \
  "v_sub_f32 v94, v94, v178\n" \
  "v_sub_f32 v95, v95, v179\n" \
  "s_mov_b32 exec_lo, 0x0\n" \
  "s_mov_b32 exec_hi, 0xff00ff\n" \
  "v_mov_b64 v[92:93], 0\n" \
  "v_mov_b64 v[94:95], 0\n" \
  "s_mov_b32 exec_lo, 0x0\n" \
  "s_mov_b32 exec_hi, 0xf0000f\n" \
  "v_mov_b64 v[92:93], v[172:173]\n" \
  "v_mov_b64 v[94:95], v[174:175]\n" \
  "s_mov_b64 exec, -1\n" \
  "v_add_u32 v96, 6400, v250\n" \
  "v_add_u32 v97, 6400, v251\n" \
  "v_mov_b32 v98, 163328\n" \
  "v_mov_b32 v99, 1\n" \
  "v_lshlrev_b32 v203, 4, v253\n" \
  "s_lshr_b32 s35, s30, 4\n" \
  "s_lshl1_add_u32 s35, s29, s35\n" \
  "s_lshl_b32 s35, s35, 6\n" \
  "s_bfe_u32 s37, s30, 0x20002\n" \
  "s_lshl3_add_u32 s35, s37, s35\n" \
  "v_and_b32 v203, 16, v203\n" \
  "v_lshl_add_u32 v203, v203, 1, s35\n" \
  "s_mov_b64 exec, 1\n" \
  "ds_add_u32 v98, v99 offset:16\n" \
  "s_mov_b64 exec, -1\n" \
  "ds_write2_b32 v195, v196, v197 offset0:0 offset1:4\n" \
  "ds_write2_b32 v195, v198, v199 offset0:2 offset1:6\n" \
  "ds_write2_b32 v200, v180, v181 offset0:0 offset1:4\n" \
  "ds_write2_b32 v200, v182, v183 offset0:2 offset1:6\n" \
  "global_load_dwordx2 v[204:205], v203, s[16:17]\n" \
  "global_load_dwordx2 v[206:207], v203, s[18:19]\n" \
  "v_mov_b32 v203, 0\n" \
  "global_load_ushort v203, v203, s[20:21]\n" \
  "s_add_u32 s46, s44, 4800\n" \
  "s_addc_u32 s47, s45, 0\n" \
  "global_load_dwordx4 v[208:211], v96, s[44:45] offset:-3200\n" \
  "global_load_dwordx4 v[212:215], v97, s[44:45] offset:-2944\n" \
  "global_load_dwordx4 v[216:219], v96, s[44:45] offset:-1600\n" \
  "global_load_dwordx4 v[220:223], v97, s[44:45] offset:-1344\n" \
  "global_load_dwordx4 v[224:227], v96, s[44:45] offset:0\n" \
  "global_load_dwordx4 v[228:231], v97, s[44:45] offset:256\n" \
  "global_load_dwordx4 v[232:235], v96, s[44:45] offset:1600\n" \
  "global_load_dwordx4 v[236:239], v97, s[44:45] offset:1856\n" \
  "global_load_dwordx4 v[240:243], v96, s[44:45] offset:3200\n" \
  "global_load_dwordx4 v[244:247], v97, s[44:45] offset:3456\n" \
  "global_load_dwordx4 v[248:251], v96, s[46:47] offset:0\n" \
  "global_load_dwordx4 v[252:255], v97, s[46:47] offset:256\n" \
  "ds_read_b128 v[180:183], v194\n" \
  "v_add_u32 v194, 0x100, v194\n" \
  "L1_:\n" \
  "ds_read_b32 v99, v98 offset:12\n" \
  "s_waitcnt lgkmcnt(0)\n" \
  "v_readfirstlane_b32 s35, v99\n" \
  "s_cmp_lt_u32 s35, 4\n" \
  "s_cbranch_scc1 L1_\n" \
  "s_setprio 1\n" \
  "v_mad_u32_u16 v184, v182, 16, v188\n" \
  "ds_read_b128 v[128:131], v184 offset:0\n" \
  "ds_read_b128 v[132:135], v184 offset:64\n" \
  "ds_read_b128 v[172:175], v191\n" \
  "v_mad_u32_u16 v185, v181, 16, v188\n" \
  "v_mad_u32_u16 v186, v182, 16, v188 op_sel:[1,0,0,0]\n" \
  "ds_read_b128 v[112:115], v185 offset:0\n" \
  "ds_read_b128 v[116:119], v185 offset:64\n" \
  "ds_read_b128 v[136:139], v186 offset:0\n" \
  "ds_read_b128 v[140:143], v186 offset:64\n" \
  "ds_read_b128 v[168:171], v191\n" \
  "v_mad_u32_u16 v187, v181, 16, v188 op_sel:[1,0,0,0]\n" \
  "v_mad_u32_u16 v184, v180, 16, v188 op_sel:[1,0,0,0]\n" \
  "ds_read_b128 v[120:123], v187 offset:0\n" \
  "v_mad_u32_u16 v185, v180, 16, v188\n" \
  "ds_read_b128 v[124:127], v187 offset:64\n" \
  "ds_read_b128 v[104:107], v184 offset:0\n" \
  "v_mad_u32_u16 v186, v183, 16, v189\n" \
  "ds_read_b128 v[108:111], v184 offset:64\n" \
  "ds_read_b128 v[96:99], v185 offset:0\n" \
  "s_waitcnt lgkmcnt(9)\n" \
  "s_mov_b32 s51, 3\n" \
  "s_mov_b32 s50, 8\n" \
  "s_call_b64 s[52:53], LA_loop\n" \
  "LA_ev0:\n" \
  "v_mov_b32 v196, v178\n" \
  "s_waitcnt vmcnt(10)\n" \
  "v_cvt_pk_u16_u32 v208, v208, v209\n" \
  "v_cvt_pk_u16_u32 v210, v210, v211\n" \
  "v_cvt_pk_u16_u32 v212, v212, v213\n" \
  "v_cvt_pk_u16_u32 v214, v214, v215\n" \
  "v_pk_lshrrev_b16 v209, 3, v208 op_sel_hi:[0,1]\n" \
  "v_pk_lshrrev_b16 v211, 3, v210 op_sel_hi:[0,1]\n" \
  "v_pk_lshrrev_b16 v213, 3, v212 op_sel_hi:[0,1]\n" \
  "v_pk_lshrrev_b16 v215, 3, v214 op_sel_hi:[0,1]\n" \
  "v_pk_mad_u16 v208, v208, 8, v209 op_sel_hi:[1,0,1]\n" \
  "v_pk_mad_u16 v210, v210, 8, v211 op_sel_hi:[1,0,1]\n" \
  "v_pk_mad_u16 v212, v212, 8, v213 op_sel_hi:[1,0,1]\n" \
  "v_pk_mad_u16 v214, v214, 8, v215 op_sel_hi:[1,0,1]\n" \
  "v_alignbit_b32 v212, v212, v212, v192\n" \
  "v_alignbit_b32 v214, v214, v214, v192\n" \
  "ds_write2_b32 v195, v208, v210 offset0:0 offset1:4\n" \
  "ds_write2_b32 v195, v212, v214 offset0:2 offset1:6\n" \
  "v_readfirstlane_b32 s6, v203\n" \
  "v_mov_b32 v208, 163328\n" \
  "s_mov_b32 s51, 4\n" \
  "s_mov_b32 s50, 1\n" \
  "s_mov_b32 s38, 0x800\n" \
  "s_call_b64 s[52:53], LA_loop\n" \
  "LA_ev6:\n" \
  "v_mov_b32 v202, v178\n" \
  "v_readfirstlane_b32 s35, v209\n" \
  "s_cmp_ge_u32 s35, 128\n" \
  "s_cbranch_scc1 LA_pl\n" \
  "L2_:\n" \
  "ds_read_b32 v209, v208 offset:4\n" \
  "s_waitcnt lgkmcnt(0)\n" \
  "v_readfirstlane_b32 s35, v209\n" \
  "s_cmp_lt_u32 s35, 128\n" \
  "s_cbranch_scc1 L2_\n" \
  "LA_pl:\n" \
  "s_and_b32 s4, s35, 15\n" \
  "s_sub_u32 s4, s4, 4\n" \
  "s_bfe_u32 s5, s35, 0x30004\n" \
  "s_sub_u32 s5, s5, s29\n" \
  "s_cmp_le_i32 s4, 0\n" \
  "s_cbranch_scc1 LA_nt\n" \
  "s_mul_i32 s35, s5, 51200\n" \
  "s_ashr_i32 s36, s35, 31\n" \
  "s_add_u32 s46, s46, s35\n" \
  "s_addc_u32 s47, s47, s36\n" \
  "v_lshrrev_b32 v216, 4, v188\n" \
  "v_mul_u32_u24 v216, 400, v216\n" \
  "v_add_u32 v216, 6400, v216\n" \
  "v_bfe_u32 v218, v194, 4, 4\n" \
  "v_min_u32 v217, 8, v218\n" \
  "v_lshl_add_u32 v217, v217, 4, v216\n" \
  "v_lshl_add_u32 v216, v218, 4, v216\n" \
  "global_load_dwordx4 v[208:211], v216, s[46:47] offset:0\n" \
  "global_load_dwordx4 v[212:215], v217, s[46:47] offset:256\n" \
  "LA_nt:\n" \
  "s_lshl_b32 s5, s5, 7\n" \
  "s_sub_u32 s5, s5, s4\n" \
  "s_min_i32 s51, s4, 0\n" \
  "s_add_u32 s51, s51, 4\n" \
  "s_cmp_eq_u32 s51, 0\n" \
  "s_cbranch_scc1 LA_ev7\n" \
  "s_call_b64 s[52:53], LA_loop\n" \
  "LA_ev7:\n" \
  "v_mov_b32 v203, v178\n" \
  "s_cmp_le_i32 s4, 0\n" \
  "s_cbranch_scc1 LA_ev8\n" \
  "s_waitcnt vmcnt(0)\n" \
  "v_cvt_pk_u16_u32 v208, v208, v209\n" \
  "v_cvt_pk_u16_u32 v210, v210, v211\n" \
  "v_cvt_pk_u16_u32 v212, v212, v213\n" \
  "v_cvt_pk_u16_u32 v214, v214, v215\n" \
  "v_pk_lshrrev_b16 v209, 3, v208 op_sel_hi:[0,1]\n" \
  "v_pk_lshrrev_b16 v211, 3, v210 op_sel_hi:[0,1]\n" \
  "v_pk_lshrrev_b16 v213, 3, v212 op_sel_hi:[0,1]\n" \
  "v_pk_lshrrev_b16 v215, 3, v214 op_sel_hi:[0,1]\n" \
  "v_pk_mad_u16 v208, v208, 8, v209 op_sel_hi:[1,0,1]\n" \
  "v_pk_mad_u16 v210, v210, 8, v211 op_sel_hi:[1,0,1]\n" \
  "v_pk_mad_u16 v212, v212, 8, v213 op_sel_hi:[1,0,1]\n" \
  "v_pk_mad_u16 v214, v214, 8, v215 op_sel_hi:[1,0,1]\n" \
  "v_alignbit_b32 v212, v212, v212, v192\n" \
  "v_alignbit_b32 v214, v214, v214, v192\n" \
  "ds_write2_b32 v195, v208, v210 offset0:0 offset1:4\n" \
  "ds_write2_b32 v195, v212, v214 offset0:2 offset1:6\n" \
  "s_lshl_b32 s35, s4, 8\n" \
  "s_add_u32 s35, s35, 0x400\n" \
  "v_subrev_u32 v194, s35, v194\n" \
  "s_mov_b32 s51, s4\n" \
  "s_call_b64 s[52:53], LA_loop\n" \
  "LA_ev8:\n" \
  "v_mov_b32 v179, v178\n" \
  "ds_read_b128 v[100:103], v185 offset:64\n" \
  "ds_read2_b32 v[144:145], v186 offset0:0 offset1:2\n" \
  "ds_read2_b32 v[146:147], v186 offset0:4 offset1:6\n" \
  "v_mad_u32_u16 v187, v183, 16, v190\n" \
  "ds_read2_b32 v[148:149], v187 offset0:0 offset1:2\n" \
  "ds_read2_b32 v[150:151], v187 offset0:4 offset1:6\n" \
  "ds_read_b128 v[164:167], v191\n" \
  "s_lshr_b32 s35, s30, 2\n" \
  "s_lshl3_add_u32 s33, s29, s35\n" \
  "s_and_b32 s34, s30, 3\n" \
  "s_lshl_b32 s59, s27, 11\n" \
  "s_mul_i32 s35, s33, 1600000\n" \
  "s_add_u32 s54, s24, s35\n" \
  "s_addc_u32 s55, s25, 0\n" \
  "s_mul_i32 s35, s34, 819200\n" \
  "s_lshl1_add_u32 s35, s59, s35\n" \
  "s_add_u32 s35, s35, 102400000\n" \
  "s_add_u32 s56, s24, s35\n" \
  "s_addc_u32 s57, s25, 0\n" \
  "v_mbcnt_lo_u32_b32 v189, -1, 0\n" \
  "v_mbcnt_hi_u32_b32 v189, -1, v189\n" \
  "v_lshlrev_b32 v188, 4, v189\n" \
  "s_waitcnt lgkmcnt(6)\n" \
  "v_mfma_f32_16x16x32_bf16 v[152:155], v[112:115], v[112:115], 0\n" \
  "v_fmac_f32 v176, v156, v88\n" \
  "v_fmac_f32 v176, v157, v89\n" \
  "v_mfma_f32_16x16x32_bf16 v[152:155], v[116:119], v[116:119], v[152:155]\n" \
  "v_fmac_f32 v176, v158, v90\n" \
  "v_fmac_f32 v176, v159, v91\n" \
  "v_mfma_f32_16x16x32_bf16 v[156:159], v[128:131], v[128:131], 0\n" \
  "v_fmac_f32 v176, v160, v92\n" \
  "v_fmac_f32 v176, v161, v93\n" \
  "v_mfma_f32_16x16x32_bf16 v[156:159], v[132:135], v[132:135], v[156:159]\n" \
  "v_fmac_f32 v176, v162, v94\n" \
  "v_fma_f32 v177, v163, v95, v176\n" \
  "v_mfma_f32_16x16x32_bf16 v[160:163], v[128:131], v[136:139], 0\n" \
  "v_mul_f32 v176, v152, v0\n" \
  "v_fmac_f32 v176, v153, v1\n" \
  "v_mfma_f32_16x16x32_bf16 v[160:163], v[132:135], v[140:143], v[160:163]\n" \
  "v_fmac_f32 v176, v154, v2\n" \
  "v_fmac_f32 v176, v155, v3\n" \
  "v_mfma_f32_16x16x32_bf16 v[152:155], v[136:139], v[136:139], 0\n" \
  "v_fmac_f32 v176, v156, v4\n" \
  "v_fmac_f32 v176, v157, v5\n" \
  "v_mfma_f32_16x16x32_bf16 v[152:155], v[140:143], v[140:143], v[152:155]\n" \
  "v_fmac_f32 v176, v158, v6\n" \
  "v_fmac_f32 v176, v159, v7\n" \
  "s_waitcnt lgkmcnt(0)\n" \
  "s_setprio 3\n" \
  "s_barrier\n" \
  "global_load_dwordx4 v[208:211], v188, s[56:57] offset:0 sc1\n" \
  "global_load_dwordx4 v[212:215], v188, s[56:57] offset:1024 sc1\n" \
  "global_load_dwordx4 v[216:219], v188, s[56:57] offset:2048 sc1\n" \
  "global_load_dwordx4 v[220:223], v188, s[56:57] offset:3072 sc1\n" \
  "v_mfma_f32_16x16x32_bf16 v[156:159], v[112:115], v[136:139], 0\n" \
  "v_fmac_f32 v176, v160, v8\n" \
  "v_fmac_f32 v176, v161, v9\n" \
  "v_lshrrev_b32 v180, 1, v189\n" \
  "v_mfma_f32_16x16x32_bf16 v[156:159], v[116:119], v[140:143], v[156:159]\n" \
  "v_fmac_f32 v176, v162, v10\n" \
  "v_fmac_f32 v176, v163, v11\n" \
  "v_xor_b32 v180, v180, v189\n" \
  "v_mfma_f32_16x16x32_bf16 v[160:163], v[112:115], v[128:131], 0\n" \
  "v_fmac_f32 v176, v152, v12\n" \
  "v_fmac_f32 v176, v153, v13\n" \
  "v_add_f32_dpp v177, v177, v177 row_ror:8 row_mask:0xf bank_mask:0xf\n" \
  "v_and_b32 v180, 8, v180\n" \
  "v_mfma_f32_16x16x32_bf16 v[160:163], v[116:119], v[132:135], v[160:163]\n" \
  "v_fmac_f32 v176, v154, v14\n" \
  "v_fmac_f32 v176, v155, v15\n" \
  "v_bfe_u32 v181, v189, 2, 1\n" \
  "v_mfma_f32_16x16x32_bf16 v[152:155], v[112:115], v[120:123], 0\n" \
  "v_fmac_f32 v176, v156, v16\n" \
  "v_fmac_f32 v176, v157, v17\n" \
  "v_lshlrev_b32_e64 v181, v181, 5\n" \
  "v_mfma_f32_16x16x32_bf16 v[152:155], v[116:119], v[124:127], v[152:155]\n" \
  "v_fmac_f32 v176, v158, v18\n" \
  "v_fmac_f32 v176, v159, v19\n" \
  "v_mov_b32_dpp v178, v178 row_ror:4 row_mask:0xf bank_mask:0xf\n" \
  "v_lshlrev_b32 v180, v180, v181\n" \
  "v_mfma_f32_16x16x32_bf16 v[156:159], v[120:123], v[136:139], 0\n" \
  "v_fmac_f32 v176, v160, v20\n" \
  "v_fmac_f32 v176, v161, v21\n" \
  "v_bfe_u32 v181, v189, 2, 2\n" \
  "v_mfma_f32_16x16x32_bf16 v[156:159], v[124:127], v[140:143], v[156:159]\n" \
  "v_fmac_f32 v176, v162, v22\n" \
  "v_fmac_f32 v176, v163, v23\n" \
  "v_lshlrev_b32_e64 v181, v181, 1\n" \
  "v_mfma_f32_16x16x32_bf16 v[160:163], v[104:107], v[128:131], 0\n" \
  "v_fmac_f32 v176, v152, v24\n" \
  "v_fmac_f32 v176, v153, v25\n" \
  "v_cmp_gt_u32 vcc, 32, v189\n" \
  "v_mfma_f32_16x16x32_bf16 v[160:163], v[108:111], v[132:135], v[160:163]\n" \
  "v_fmac_f32 v176, v154, v26\n" \
  "v_fmac_f32 v176, v155, v27\n" \
  "v_mov_b32 v191, 0x3f80\n" \
  "v_mfma_f32_16x16x32_bf16 v[152:155], v[120:123], v[128:131], 0\n" \
  "v_fmac_f32 v176, v156, v28\n" \
  "v_fmac_f32 v176, v157, v29\n" \
  "v_add_u32 v190, 32000, v188\n" \
  "v_mfma_f32_16x16x32_bf16 v[152:155], v[124:127], v[132:135], v[152:155]\n" \
  "v_fmac_f32 v176, v158, v30\n" \
  "v_fmac_f32 v176, v159, v31\n" \
  "v_add_f32_dpp v178, v177, v177 row_ror:4 row_mask:0xf bank_mask:0x1\n" \
  "v_cndmask_b32 v192, v181, v180, vcc\n" \
  "v_mfma_f32_16x16x32_bf16 v[156:159], v[96:99], v[128:131], 0\n" \
  "v_fmac_f32 v176, v160, v32\n" \
  "v_fmac_f32 v176, v161, v33\n" \
  "v_bfe_i32 v180, v192, 0, 1\n" \
  "v_mfma_f32_16x16x32_bf16 v[156:159], v[100:103], v[132:135], v[156:159]\n" \
  "v_fmac_f32 v176, v162, v34\n" \
  "v_fmac_f32 v176, v163, v35\n" \
  "v_and_b32 v180, v180, v191\n" \
  "v_smfmac_f32_16x16x64_bf16 v[172:175], v[144:147], v[128:135], v193\n" \
  "v_fmac_f32 v176, v152, v36\n" \
  "v_fmac_f32 v176, v153, v37\n" \
  "v_bfe_i32 v181, v192, 1, 1\n" \
  "v_smfmac_f32_16x16x64_bf16 v[172:175], v[148:151], v[136:143], v193\n" \
  "v_fmac_f32 v176, v154, v38\n" \
  "v_fmac_f32 v176, v155, v39\n" \
  "v_and_b32 v181, v181, v191\n" \
  "v_mfma_f32_16x16x32_bf16 v[160:163], v[96:99], v[112:115], 0\n" \
  "v_fmac_f32 v176, v156, v40\n" \
  "v_fmac_f32 v176, v157, v41\n" \
  "v_bfe_i32 v182, v192, 2, 1\n" \
  "v_mfma_f32_16x16x32_bf16 v[160:163], v[100:103], v[116:119], v[160:163]\n" \
  "v_fmac_f32 v176, v158, v42\n" \
  "v_fmac_f32 v176, v159, v43\n" \
  "v_and_b32 v182, v182, v191\n" \
  "v_mfma_f32_16x16x32_bf16 v[152:155], v[104:107], v[112:115], 0\n" \
  "v_fmac_f32 v176, v172, v44\n" \
  "v_fmac_f32 v176, v173, v45\n" \
  "v_bfe_i32 v183, v192, 3, 1\n" \
  "v_mfma_f32_16x16x32_bf16 v[152:155], v[108:111], v[116:119], v[152:155]\n" \
  "v_fmac_f32 v176, v174, v46\n" \
  "v_fmac_f32 v176, v175, v47\n" \
  "v_and_b32 v183, v183, v191\n" \
  "v_mfma_f32_16x16x32_bf16 v[156:159], v[104:107], v[136:139], 0\n" \
  "v_fmac_f32 v176, v160, v48\n" \
  "v_fmac_f32 v176, v161, v49\n" \
  "v_and_b32 v192, 3, v189\n" \
  "v_mfma_f32_16x16x32_bf16 v[156:159], v[108:111], v[140:143], v[156:159]\n" \
  "v_fmac_f32 v176, v162, v50\n" \
  "v_fmac_f32 v176, v163, v51\n" \
  "v_mul_u32_u24 v192, 0x5555, v192\n" \
  "v_smfmac_f32_16x16x64_bf16 v[168:171], v[144:147], v[112:119], v193\n" \
  "v_fmac_f32 v176, v152, v52\n" \
  "v_fmac_f32 v176, v153, v53\n" \
  "v_add_u32 v189, 16000, v188\n" \
  "v_smfmac_f32_16x16x64_bf16 v[168:171], v[148:151], v[120:127], v193\n" \
  "v_fmac_f32 v176, v154, v54\n" \
  "v_fmac_f32 v176, v155, v55\n" \
  "v_add_u32 v191, 48000, v188\n" \
  "v_mfma_f32_16x16x32_bf16 v[160:163], v[96:99], v[136:139], 0\n" \
  "v_fmac_f32 v176, v156, v56\n" \
  "v_fmac_f32 v176, v157, v57\n" \
  "v_mfma_f32_16x16x32_bf16 v[160:163], v[100:103], v[140:143], v[160:163]\n" \
  "v_fmac_f32 v176, v158, v58\n" \
  "v_fmac_f32 v176, v159, v59\n" \
  "s_add_u32 s35, s59, 64000\n" \
  "s_add_u32 s54, s54, s35\n" \
  "s_addc_u32 s55, s55, 0\n" \
  "s_add_u32 s58, s59, 0x10000\n" \
  "s_mov_b32 m0, s58\n" \
  "s_add_u32 s35, s58, 0x8000\n" \
  "global_load_lds_dwordx4 v188, s[54:55] sc1\n" \
  "global_load_lds_dwordx4 v188, s[54:55] offset:1024 sc1\n" \
  "v_smfmac_f32_16x16x64_bf16 v[164:167], v[148:151], v[104:111], v193\n" \
  "v_fmac_f32 v176, v168, v60\n" \
  "v_fmac_f32 v176, v169, v61\n" \
  "v_smfmac_f32_16x16x64_bf16 v[164:167], v[144:147], v[96:103], v193\n" \
  "v_fmac_f32 v176, v170, v62\n" \
  "v_fmac_f32 v176, v171, v63\n" \
  "v_mfma_f32_16x16x32_bf16 v[152:155], v[120:123], v[120:123], 0\n" \
  "v_fmac_f32 v176, v160, v64\n" \
  "v_fmac_f32 v176, v161, v65\n" \
  "v_mfma_f32_16x16x32_bf16 v[152:155], v[124:127], v[124:127], v[152:155]\n" \
  "v_fmac_f32 v176, v162, v66\n" \
  "v_fmac_f32 v176, v163, v67\n" \
  "v_mfma_f32_16x16x32_bf16 v[156:159], v[104:107], v[120:123], 0\n" \
  "v_fmac_f32 v176, v164, v68\n" \
  "v_fmac_f32 v176, v165, v69\n" \
  "v_mfma_f32_16x16x32_bf16 v[156:159], v[108:111], v[124:127], v[156:159]\n" \
  "v_fmac_f32 v176, v166, v70\n" \
  "v_fmac_f32 v176, v167, v71\n" \
  "s_add_u32 m0, s58, 0x4000\n" \
  "s_cmp_lt_u32 s35, 0x28000\n" \
  "global_load_lds_dwordx4 v189, s[54:55] sc1\n" \
  "global_load_lds_dwordx4 v189, s[54:55] offset:1024 sc1\n" \
  "s_cselect_b32 s58, s35, s59\n" \
  "s_add_u32 s54, s54, 32000\n" \
  "s_addc_u32 s55, s55, 0\n" \
  "v_mfma_f32_16x16x32_bf16 v[160:163], v[96:99], v[120:123], 0\n" \
  "v_fmac_f32 v176, v152, v72\n" \
  "v_fmac_f32 v176, v153, v73\n" \
  "v_mfma_f32_16x16x32_bf16 v[160:163], v[100:103], v[124:127], v[160:163]\n" \
  "v_fmac_f32 v176, v154, v74\n" \
  "v_fmac_f32 v176, v155, v75\n" \
  "v_mfma_f32_16x16x32_bf16 v[152:155], v[96:99], v[104:107], 0\n" \
  "v_fmac_f32 v176, v156, v76\n" \
  "v_fmac_f32 v176, v157, v77\n" \
  "v_mfma_f32_16x16x32_bf16 v[152:155], v[100:103], v[108:111], v[152:155]\n" \
  "v_fmac_f32 v176, v158, v78\n" \
  "v_fmac_f32 v176, v159, v79\n" \
  "v_mfma_f32_16x16x32_bf16 v[156:159], v[104:107], v[104:107], 0\n" \
  "v_fmac_f32 v176, v160, v80\n" \
  "v_fmac_f32 v176, v161, v81\n" \
  "v_mfma_f32_16x16x32_bf16 v[156:159], v[108:111], v[108:111], v[156:159]\n" \
  "v_fmac_f32 v176, v162, v82\n" \
  "v_fmac_f32 v176, v163, v83\n" \
  "v_mfma_f32_16x16x32_bf16 v[160:163], v[148:151], v[144:147], 0\n" \
  "v_fmac_f32 v176, v152, v84\n" \
  "v_fmac_f32 v176, v153, v85\n" \
  "v_mfma_f32_16x16x32_bf16 v[160:163], v[96:99], v[96:99], v[160:163]\n" \
  "v_fmac_f32 v176, v154, v86\n" \
  "v_fmac_f32 v176, v155, v87\n" \
  "v_mfma_f32_16x16x32_bf16 v[160:163], v[100:103], v[100:103], v[160:163]\n" \
  "s_mov_b32 s51, 7\n" \
  "v_fmac_f32 v176, v156, v88\n" \
  "v_fmac_f32 v176, v157, v89\n" \
  "v_fmac_f32 v176, v158, v90\n" \
  "v_fmac_f32 v176, v159, v91\n" \
  "s_nop 2\n" \
  "v_fmac_f32 v176, v160, v92\n" \
  "v_fmac_f32 v176, v161, v93\n" \
  "v_fmac_f32 v176, v162, v94\n" \
  "v_fma_f32 v177, v163, v95, v176\n" \
  "s_nop 1\n" \
  "v_add_f32_dpp v177, v177, v177 row_ror:8 row_mask:0xf bank_mask:0xf\n" \
  "v_mov_b32_dpp v178, v178 row_ror:4 row_mask:0xf bank_mask:0xf\n" \
  "s_nop 1\n" \
  "v_add_f32_dpp v178, v177, v177 row_ror:4 row_mask:0xf bank_mask:0x1\n" \
  "v_mov_b32 v99, v179\n" \
  "s_nop 0\n" \
  "v_mov_b32_dpp v196, v178 row_ror:8 row_mask:0xf bank_mask:0xc\n" \
  "s_nop 1\n" \
  "v_permlane16_swap_b32 v196, v197\n" \
  "v_permlane16_swap_b32 v198, v199\n" \
  "v_permlane16_swap_b32 v200, v201\n" \
  "v_permlane16_swap_b32 v202, v203\n" \
  "v_permlane16_swap_b32 v179, v99\n" \
  "v_add_f32 v196, v196, v197\n" \
  "v_add_f32 v198, v198, v199\n" \
  "v_add_f32 v200, v200, v201\n" \
  "v_add_f32 v202, v202, v203\n" \
  "v_add_f32 v179, v179, v99\n" \
  "v_lshrrev_b32 v96, 4, v188\n" \
  "v_and_b32 v97, 1, v96\n" \
  "v_mov_b32 v99, v179\n" \
  "v_lshrrev_b32 v98, 4, v96\n" \
  "v_permlane32_swap_b32 v196, v198\n" \
  "v_permlane32_swap_b32 v200, v202\n" \
  "v_permlane32_swap_b32 v179, v99\n" \
  "v_add_f32 v196, v196, v198\n" \
  "v_add_f32 v200, v200, v202\n" \
  "v_add_f32 v179, v179, v99\n" \
  "v_cmp_eq_u32 vcc, 1, v97\n" \
  "v_lshlrev_b32 v97, 2, v97\n" \
  "v_add_lshl_u32 v97, v97, v98, 2\n" \
  "v_bfe_u32 v98, v96, 2, 2\n" \
  "v_add_f32_dpp v196, v196, v196 quad_perm:[1,0,3,2] row_mask:0xf bank_mask:0xf\n" \
  "v_add_f32_dpp v200, v200, v200 quad_perm:[1,0,3,2] row_mask:0xf bank_mask:0xf\n" \
  "v_add_f32_dpp v179, v179, v179 quad_perm:[1,0,3,2] row_mask:0xf bank_mask:0xf\n" \
  "v_sub_u32 v97, v97, v98\n" \
  "v_cndmask_b32 v196, v196, v200, vcc\n" \
  "v_add_u32 v97, 1, v97\n" \
  "s_min_i32 s35, s4, 0\n" \
  "s_add_u32 s36, s4, 32\n" \
  "s_add_u32 s37, s4, 29\n" \
  "v_add_f32_dpp v179, v179, v179 quad_perm:[2,3,0,1] row_mask:0xf bank_mask:0xf\n" \
  "v_add_f32_dpp v196, v196, v196 quad_perm:[2,3,0,1] row_mask:0xf bank_mask:0xf\n" \
  "s_mov_b32 exec_lo, 0x0\n" \
  "s_mov_b32 exec_hi, 0xaaaa0000\n" \
  "v_add_u32 v97, s35, v97\n" \
  "s_mov_b32 exec_lo, 0x5500\n" \
  "s_mov_b32 exec_hi, 0x0\n" \
  "v_add_u32 v97, s36, v97\n" \
  "s_mov_b32 exec_lo, 0x4444\n" \
  "s_mov_b32 exec_hi, 0x0\n" \
  "v_sub_u32 v97, s37, v98\n" \
  "v_mov_b32 v196, v179\n" \
  "s_mov_b64 exec, -1\n" \
  "v_cmp_lt_u32 vcc, 31, v97\n" \
  "v_add_u32 v98, s5, v97\n" \
  "s_lshl2_add_u32 s35, s7, s28\n" \
  "s_lshl_b32 s35, s35, 5\n" \
  "v_cndmask_b32 v97, v97, v98, vcc\n" \
  "v_add_lshl_u32 v97, v97, s35, 2\n" \
  "s_lshl_b32 s35, s31, 16\n" \
  "s_add_u32 s35, s35, 109871104\n" \
  "s_add_u32 s40, s24, s35\n" \
  "s_addc_u32 s41, s25, 0\n" \
  "v_min_u32 v196, -2, v196\n" \
  "v_lshlrev_b32 v184, 16, v204\n" \
  "v_and_b32 v185, 0xffff0000, v204\n" \
  "v_lshlrev_b32 v186, 16, v205\n" \
  "v_and_b32 v187, 0xffff0000, v205\n" \
  "s_barrier\n" \
  "s_add_u32 s56, s56, 32768\n" \
  "s_addc_u32 s57, s57, 0\n" \
  "global_load_dwordx4 v[224:227], v188, s[56:57] sc1\n" \
  "global_load_dwordx4 v[228:231], v188, s[56:57] offset:1024 sc1\n" \
  "global_load_dwordx4 v[232:235], v188, s[56:57] offset:2048 sc1\n" \
  "global_load_dwordx4 v[236:239], v188, s[56:57] offset:3072 sc1\n" \
  "s_add_u32 s56, s56, 32768\n" \
  "s_addc_u32 s57, s57, 0\n" \
  "s_mov_b32 exec_lo, 0x33337777\n" \
  "s_mov_b32 exec_hi, 0x33333333\n" \
  "global_store_dword v97, v196, s[40:41] sc1\n" \
  "s_mov_b64 exec, -1\n" \
  "s_waitcnt vmcnt(9)\n" \
  "s_setprio 0\n" \
  "s_branch LG_start\n" \
  ".p2align 6\n" \
  "LA_loop:\n" \
  "LA_mid:\n" \
  "v_mfma_f32_16x16x32_bf16 v[152:155], v[112:115], v[112:115], 0\n" \
  "s_waitcnt lgkmcnt(8)\n" \
  "ds_read_b128 v[100:103], v185 offset:64\n" \
  "v_fmac_f32 v176, v156, v88\n" \
  "v_fmac_f32 v176, v157, v89\n" \
  "v_mfma_f32_16x16x32_bf16 v[152:155], v[116:119], v[116:119], v[152:155]\n" \
  "ds_read2_b32 v[144:145], v186 offset0:0 offset1:2\n" \
  "v_fmac_f32 v176, v158, v90\n" \
  "v_fmac_f32 v176, v159, v91\n" \
  "v_mfma_f32_16x16x32_bf16 v[156:159], v[128:131], v[128:131], 0\n" \
  "ds_read2_b32 v[146:147], v186 offset0:4 offset1:6\n" \
  "v_fmac_f32 v176, v160, v92\n" \
  "v_fmac_f32 v176, v161, v93\n" \
  "v_mad_u32_u16 v187, v183, 16, v190\n" \
  "v_mfma_f32_16x16x32_bf16 v[156:159], v[132:135], v[132:135], v[156:159]\n" \
  "s_waitcnt lgkmcnt(10)\n" \
  "ds_read2_b32 v[148:149], v187 offset0:0 offset1:2\n" \
  "v_fmac_f32 v176, v162, v94\n" \
  "v_fma_f32 v177, v163, v95, v176\n" \
  "v_mfma_f32_16x16x32_bf16 v[160:163], v[128:131], v[136:139], 0\n" \
  "s_waitcnt lgkmcnt(10)\n" \
  "ds_read2_b32 v[150:151], v187 offset0:4 offset1:6\n" \
  "v_mul_f32 v176, v152, v0\n" \
  "v_fmac_f32 v176, v153, v1\n" \
  "v_mfma_f32_16x16x32_bf16 v[160:163], v[132:135], v[140:143], v[160:163]\n" \
  "ds_read_b128 v[180:183], v194\n" \
  "v_fmac_f32 v176, v154, v2\n" \
  "v_fmac_f32 v176, v155, v3\n" \
  "v_mfma_f32_16x16x32_bf16 v[152:155], v[136:139], v[136:139], 0\n" \
  "v_fmac_f32 v176, v156, v4\n" \
  "v_fmac_f32 v176, v157, v5\n" \
  "v_mfma_f32_16x16x32_bf16 v[152:155], v[140:143], v[140:143], v[152:155]\n" \
  "ds_read_b128 v[164:167], v191\n" \
  "v_fmac_f32 v176, v158, v6\n" \
  "v_fmac_f32 v176, v159, v7\n" \
  "v_mfma_f32_16x16x32_bf16 v[156:159], v[112:115], v[136:139], 0\n" \
  "v_fmac_f32 v176, v160, v8\n" \
  "v_fmac_f32 v176, v161, v9\n" \
  "v_mfma_f32_16x16x32_bf16 v[156:159], v[116:119], v[140:143], v[156:159]\n" \
  "v_fmac_f32 v176, v162, v10\n" \
  "v_fmac_f32 v176, v163, v11\n" \
  "v_mfma_f32_16x16x32_bf16 v[160:163], v[112:115], v[128:131], 0\n" \
  "v_fmac_f32 v176, v152, v12\n" \
  "v_fmac_f32 v176, v153, v13\n" \
  "v_add_f32_dpp v177, v177, v177 row_ror:8 row_mask:0xf bank_mask:0xf\n" \
  "v_mfma_f32_16x16x32_bf16 v[160:163], v[116:119], v[132:135], v[160:163]\n" \
  "s_waitcnt lgkmcnt(11)\n" \
  "v_fmac_f32 v176, v154, v14\n" \
  "v_fmac_f32 v176, v155, v15\n" \
  "v_mfma_f32_16x16x32_bf16 v[152:155], v[112:115], v[120:123], 0\n" \
  "s_waitcnt lgkmcnt(10)\n" \
  "v_fmac_f32 v176, v156, v16\n" \
  "v_fmac_f32 v176, v157, v17\n" \
  "v_mfma_f32_16x16x32_bf16 v[152:155], v[116:119], v[124:127], v[152:155]\n" \
  "v_fmac_f32 v176, v158, v18\n" \
  "v_fmac_f32 v176, v159, v19\n" \
  "v_mov_b32_dpp v178, v178 row_ror:4 row_mask:0xf bank_mask:0xf\n" \
  "v_mfma_f32_16x16x32_bf16 v[156:159], v[120:123], v[136:139], 0\n" \
  "v_fmac_f32 v176, v160, v20\n" \
  "v_fmac_f32 v176, v161, v21\n" \
  "v_mfma_f32_16x16x32_bf16 v[156:159], v[124:127], v[140:143], v[156:159]\n" \
  "s_waitcnt lgkmcnt(9)\n" \
  "v_fmac_f32 v176, v162, v22\n" \
  "v_fmac_f32 v176, v163, v23\n" \
  "v_mfma_f32_16x16x32_bf16 v[160:163], v[104:107], v[128:131], 0\n" \
  "s_waitcnt lgkmcnt(1)\n" \
  "v_fmac_f32 v176, v152, v24\n" \
  "v_fmac_f32 v176, v153, v25\n" \
  "v_mad_u32_u16 v184, v182, 16, v188\n" \
  "v_mfma_f32_16x16x32_bf16 v[160:163], v[108:111], v[132:135], v[160:163]\n" \
  "v_fmac_f32 v176, v154, v26\n" \
  "v_fmac_f32 v176, v155, v27\n" \
  "v_mfma_f32_16x16x32_bf16 v[152:155], v[120:123], v[128:131], 0\n" \
  "v_fmac_f32 v176, v156, v28\n" \
  "v_fmac_f32 v176, v157, v29\n" \
  "v_mfma_f32_16x16x32_bf16 v[152:155], v[124:127], v[132:135], v[152:155]\n" \
  "v_fmac_f32 v176, v158, v30\n" \
  "v_fmac_f32 v176, v159, v31\n" \
  "v_add_f32_dpp v178, v177, v177 row_ror:4 row_mask:0xf bank_mask:0x1\n" \
  "v_mfma_f32_16x16x32_bf16 v[156:159], v[96:99], v[128:131], 0\n" \
  "v_fmac_f32 v176, v160, v32\n" \
  "v_fmac_f32 v176, v161, v33\n" \
  "v_mfma_f32_16x16x32_bf16 v[156:159], v[100:103], v[132:135], v[156:159]\n" \
  "v_fmac_f32 v176, v162, v34\n" \
  "v_fmac_f32 v176, v163, v35\n" \
  "v_smfmac_f32_16x16x64_bf16 v[172:175], v[144:147], v[128:135], v193\n" \
  "ds_read_b128 v[128:131], v184 offset:0\n" \
  "v_fmac_f32 v176, v152, v36\n" \
  "v_fmac_f32 v176, v153, v37\n" \
  "v_smfmac_f32_16x16x64_bf16 v[172:175], v[148:151], v[136:143], v193\n" \
  "ds_read_b128 v[132:135], v184 offset:64\n" \
  "v_fmac_f32 v176, v154, v38\n" \
  "v_fmac_f32 v176, v155, v39\n" \
  "v_mfma_f32_16x16x32_bf16 v[160:163], v[96:99], v[112:115], 0\n" \
  "v_fmac_f32 v176, v156, v40\n" \
  "v_fmac_f32 v176, v157, v41\n" \
  "v_mfma_f32_16x16x32_bf16 v[160:163], v[100:103], v[116:119], v[160:163]\n" \
  "v_fmac_f32 v176, v158, v42\n" \
  "v_fmac_f32 v176, v159, v43\n" \
  "v_add_u32 v194, 0x100, v194\n" \
  "v_mfma_f32_16x16x32_bf16 v[152:155], v[104:107], v[112:115], 0\n" \
  "v_fmac_f32 v176, v172, v44\n" \
  "v_fmac_f32 v176, v173, v45\n" \
  "v_mfma_f32_16x16x32_bf16 v[152:155], v[108:111], v[116:119], v[152:155]\n" \
  "v_fmac_f32 v176, v174, v46\n" \
  "v_fmac_f32 v176, v175, v47\n" \
  "v_mfma_f32_16x16x32_bf16 v[156:159], v[104:107], v[136:139], 0\n" \
  "ds_read_b128 v[172:175], v191\n" \
  "v_fmac_f32 v176, v160, v48\n" \
  "v_fmac_f32 v176, v161, v49\n" \
  "v_mad_u32_u16 v185, v181, 16, v188\n" \
  "v_mfma_f32_16x16x32_bf16 v[156:159], v[108:111], v[140:143], v[156:159]\n" \
  "v_fmac_f32 v176, v162, v50\n" \
  "v_fmac_f32 v176, v163, v51\n" \
  "v_mad_u32_u16 v186, v182, 16, v188 op_sel:[1,0,0,0]\n" \
  "v_smfmac_f32_16x16x64_bf16 v[168:171], v[144:147], v[112:119], v193\n" \
  "ds_read_b128 v[112:115], v185 offset:0\n" \
  "v_fmac_f32 v176, v152, v52\n" \
  "v_fmac_f32 v176, v153, v53\n" \
  "v_smfmac_f32_16x16x64_bf16 v[168:171], v[148:151], v[120:127], v193\n" \
  "ds_read_b128 v[116:119], v185 offset:64\n" \
  "v_fmac_f32 v176, v154, v54\n" \
  "v_fmac_f32 v176, v155, v55\n" \
  "v_mfma_f32_16x16x32_bf16 v[160:163], v[96:99], v[136:139], 0\n" \
  "v_fmac_f32 v176, v156, v56\n" \
  "v_fmac_f32 v176, v157, v57\n" \
  "v_mfma_f32_16x16x32_bf16 v[160:163], v[100:103], v[140:143], v[160:163]\n" \
  "s_waitcnt lgkmcnt(5)\n" \
  "v_fmac_f32 v176, v158, v58\n" \
  "v_fmac_f32 v176, v159, v59\n" \
  "v_smfmac_f32_16x16x64_bf16 v[164:167], v[148:151], v[104:111], v193\n" \
  "ds_read_b128 v[136:139], v186 offset:0\n" \
  "v_fmac_f32 v176, v168, v60\n" \
  "v_fmac_f32 v176, v169, v61\n" \
  "v_smfmac_f32_16x16x64_bf16 v[164:167], v[144:147], v[96:103], v193\n" \
  "ds_read_b128 v[140:143], v186 offset:64\n" \
  "v_fmac_f32 v176, v170, v62\n" \
  "v_fmac_f32 v176, v171, v63\n" \
  "v_mfma_f32_16x16x32_bf16 v[152:155], v[120:123], v[120:123], 0\n" \
  "v_fmac_f32 v176, v160, v64\n" \
  "v_fmac_f32 v176, v161, v65\n" \
  "v_mfma_f32_16x16x32_bf16 v[152:155], v[124:127], v[124:127], v[152:155]\n" \
  "ds_read_b128 v[168:171], v191\n" \
  "v_fmac_f32 v176, v162, v66\n" \
  "v_fmac_f32 v176, v163, v67\n" \
  "v_mad_u32_u16 v187, v181, 16, v188 op_sel:[1,0,0,0]\n" \
  "v_mfma_f32_16x16x32_bf16 v[156:159], v[104:107], v[120:123], 0\n" \
  "v_fmac_f32 v176, v164, v68\n" \
  "v_fmac_f32 v176, v165, v69\n" \
  "v_mad_u32_u16 v184, v180, 16, v188 op_sel:[1,0,0,0]\n" \
  "v_mfma_f32_16x16x32_bf16 v[156:159], v[108:111], v[124:127], v[156:159]\n" \
  "v_fmac_f32 v176, v166, v70\n" \
  "v_fmac_f32 v176, v167, v71\n" \
  "v_mfma_f32_16x16x32_bf16 v[160:163], v[96:99], v[120:123], 0\n" \
  "ds_read_b128 v[120:123], v187 offset:0\n" \
  "v_fmac_f32 v176, v152, v72\n" \
  "v_fmac_f32 v176, v153, v73\n" \
  "v_mfma_f32_16x16x32_bf16 v[160:163], v[100:103], v[124:127], v[160:163]\n" \
  "v_fmac_f32 v176, v154, v74\n" \
  "v_fmac_f32 v176, v155, v75\n" \
  "v_mad_u32_u16 v185, v180, 16, v188\n" \
  "v_mfma_f32_16x16x32_bf16 v[152:155], v[96:99], v[104:107], 0\n" \
  "ds_read_b128 v[124:127], v187 offset:64\n" \
  "v_fmac_f32 v176, v156, v76\n" \
  "v_fmac_f32 v176, v157, v77\n" \
  "v_mfma_f32_16x16x32_bf16 v[152:155], v[100:103], v[108:111], v[152:155]\n" \
  "v_fmac_f32 v176, v158, v78\n" \
  "v_fmac_f32 v176, v159, v79\n" \
  "v_mfma_f32_16x16x32_bf16 v[156:159], v[104:107], v[104:107], 0\n" \
  "ds_read_b128 v[104:107], v184 offset:0\n" \
  "v_fmac_f32 v176, v160, v80\n" \
  "v_fmac_f32 v176, v161, v81\n" \
  "v_mfma_f32_16x16x32_bf16 v[156:159], v[108:111], v[108:111], v[156:159]\n" \
  "s_sub_u32 s51, s51, 1\n" \
  "v_fmac_f32 v176, v162, v82\n" \
  "v_fmac_f32 v176, v163, v83\n" \
  "v_mad_u32_u16 v186, v183, 16, v189\n" \
  "v_mfma_f32_16x16x32_bf16 v[160:163], v[148:151], v[144:147], 0\n" \
  "s_cmp_lg_u32 s51, 0\n" \
  "ds_read_b128 v[108:111], v184 offset:64\n" \
  "v_fmac_f32 v176, v152, v84\n" \
  "v_fmac_f32 v176, v153, v85\n" \
  "v_mfma_f32_16x16x32_bf16 v[160:163], v[96:99], v[96:99], v[160:163]\n" \
  "s_waitcnt lgkmcnt(8)\n" \
  "ds_read_b128 v[96:99], v185 offset:0\n" \
  "v_fmac_f32 v176, v154, v86\n" \
  "v_fmac_f32 v176, v155, v87\n" \
  "v_mfma_f32_16x16x32_bf16 v[160:163], v[100:103], v[100:103], v[160:163]\n" \
  "s_cbranch_scc1 LA_loop\n" \
  "s_cmp_ge_u32 s50, 6\n" \
  "s_cbranch_scc1 LA_ret\n" \
  "s_set_gpr_idx_on s50, gpr_idx(DST)\n" \
  "v_mov_b32 v196, v178\n" \
  "s_lshl_b32 s35, s50, 3\n" \
  "s_waitcnt vmcnt(0)\n" \
  "s_set_gpr_idx_on s35, gpr_idx(SRC0,SRC1)\n" \
  "v_cvt_pk_u16_u32 v210, v208, v209\n" \
  "v_cvt_pk_u16_u32 v211, v210, v211\n" \
  "v_cvt_pk_u16_u32 v212, v212, v213\n" \
  "v_cvt_pk_u16_u32 v213, v214, v215\n" \
  "s_set_gpr_idx_off\n" \
  "v_pk_lshrrev_b16 v214, 3, v210 op_sel_hi:[0,1]\n" \
  "v_pk_mad_u16 v210, v210, 8, v214 op_sel_hi:[1,0,1]\n" \
  "v_pk_lshrrev_b16 v215, 3, v211 op_sel_hi:[0,1]\n" \
  "v_pk_mad_u16 v211, v211, 8, v215 op_sel_hi:[1,0,1]\n" \
  "v_pk_lshrrev_b16 v214, 3, v212 op_sel_hi:[0,1]\n" \
  "v_pk_mad_u16 v212, v212, 8, v214 op_sel_hi:[1,0,1]\n" \
  "v_pk_lshrrev_b16 v215, 3, v213 op_sel_hi:[0,1]\n" \
  "v_pk_mad_u16 v213, v213, 8, v215 op_sel_hi:[1,0,1]\n" \
  "s_lshr_b32 s35, s38, 1\n" \
  "v_add_u32 v214, s35, v195\n" \
  "v_alignbit_b32 v212, v212, v212, v192\n" \
  "v_alignbit_b32 v213, v213, v213, v192\n" \
  "ds_write2_b32 v214, v210, v211 offset0:0 offset1:4\n" \
  "ds_write2_b32 v214, v212, v213 offset0:2 offset1:6\n" \
  "ds_read_b32 v209, v208 offset:4\n" \
  "v_subrev_u32 v194, s38, v194\n" \
  "s_xor_b32 s38, s38, 0x800\n" \
  "s_add_u32 s50, s50, 1\n" \
  "s_mov_b32 s51, 4\n" \
  "s_branch LA_loop\n" \
  "LA_ret:\n" \
  "s_setpc_b64 s[52:53]\n" \
  "LB_start:\n" \
  "s_setprio 2\n" \
  "s_lshl_b32 s35, s31, 7\n" \
  "s_waitcnt lgkmcnt(0)\n" \
  "s_add_u32 s40, s10, s35\n" \
  "s_addc_u32 s41, s11, 0\n" \
  "s_add_u32 s37, s36, 32\n" \
  "L3_:\n" \
  "s_sub_u32 s35, s36, 125\n" \
  "s_min_u32 s35, s35, s36\n" \
  "s_mul_i32 m0, s35, 1040\n" \
  "s_lshl_b32 s35, s35, 11\n" \
  "s_add_u32 s42, s40, s35\n" \
  "s_addc_u32 s43, s41, 0\n" \
  "global_load_lds_dwordx4 v254, s[42:43]\n" \
  "s_add_u32 s36, s36, 4\n" \
  "s_sub_u32 s35, s36, 125\n" \
  "s_min_u32 s35, s35, s36\n" \
  "s_mul_i32 m0, s35, 1040\n" \
  "s_lshl_b32 s35, s35, 11\n" \
  "s_add_u32 s42, s40, s35\n" \
  "s_addc_u32 s43, s41, 0\n" \
  "global_load_lds_dwordx4 v254, s[42:43]\n" \
  "s_add_u32 s36, s36, 4\n" \
  "s_cmp_lg_u32 s37, s36\n" \
  "s_cbranch_scc1 L3_\n" \
  "v_mov_b32 v190, 163328\n" \
  "v_mov_b64 v[2:3], 0\n" \
  "ds_write_b64 v190, v[2:3]\n" \
  "ds_write_b64 v190, v[2:3] offset:8\n" \
  "ds_write_b64 v190, v[2:3] offset:16\n" \
  "s_waitcnt lgkmcnt(0)\n" \
  "s_barrier\n" \
  "v_lshlrev_b32 v247, 7, v255\n" \
  "s_add_u32 s37, s36, 96\n" \
  "L4_:\n" \
  "s_sub_u32 s35, s36, 125\n" \
  "s_min_u32 s35, s35, s36\n" \
  "s_mul_i32 m0, s35, 1040\n" \
  "s_lshl_b32 s35, s35, 11\n" \
  "s_add_u32 s42, s40, s35\n" \
  "s_addc_u32 s43, s41, 0\n" \
  "global_load_lds_dwordx4 v254, s[42:43]\n" \
  "s_add_u32 s36, s36, 4\n" \
  "s_sub_u32 s35, s36, 125\n" \
  "s_min_u32 s35, s35, s36\n" \
  "s_mul_i32 m0, s35, 1040\n" \
  "s_lshl_b32 s35, s35, 11\n" \
  "s_add_u32 s42, s40, s35\n" \
  "s_addc_u32 s43, s41, 0\n" \
  "global_load_lds_dwordx4 v254, s[42:43]\n" \
  "s_add_u32 s36, s36, 4\n" \
  "s_cmp_lg_u32 s37, s36\n" \
  "s_cbranch_scc1 L4_\n" \
  "s_add_u32 s48, s24, 110002176\n" \
  "s_addc_u32 s49, s25, 0\n" \
  "s_lshl_b32 s35, s26, 2\n" \
  "v_mov_b32 v193, s35\n" \
  "global_load_dword v193, v193, s[48:49] offset:1024 sc1\n" \
  "v_mov_b32 v191, 1\n" \
  "v_and_b32 v252, 15, v255\n" \
  "v_lshrrev_b32 v253, 4, v255\n" \
  "s_lshl2_add_u32 s35, s31, s28\n" \
  "s_lshl_b32 s35, s35, 1\n" \
  "v_bfe_u32 v1, v255, 2, 1\n" \
  "v_add_u32 v1, s35, v1\n" \
  "v_and_b32 v2, 3, v255\n" \
  "v_lshlrev_b32 v3, 5, v1\n" \
  "v_lshl_add_u32 v3, v2, 3, v3\n" \
  "v_lshlrev_b32 v4, 2, v2\n" \
  "v_add_u32 v4, 380, v4\n" \
  "v_cmp_gt_u32 vcc, 12, v1\n" \
  "v_lshrrev_b32 v5, 3, v255\n" \
  "v_mul_u32_u24 v5, 6400, v5\n" \
  "v_cndmask_b32 v3, v4, v3, vcc\n" \
  "v_add_u32 v5, 3200, v5\n" \
  "v_add_u32 v227, v5, v3\n" \
  "v_lshl_add_u32 v3, v1, 2, v2\n" \
  "v_add_u32 v4, 48, v3\n" \
  "v_mul_u32_u24 v5, 205, v3\n" \
  "v_lshrrev_b32 v5, 11, v5\n" \
  "v_mul_u32_u24 v5, 10, v5\n" \
  "v_sub_u32 v5, v3, v5\n" \
  "v_lshlrev_b32 v236, 26, v5\n" \
  "v_mul_u32_u24 v5, 205, v4\n" \
  "v_lshrrev_b32 v5, 11, v5\n" \
  "v_mul_u32_u24 v5, 10, v5\n" \
  "v_sub_u32 v5, v4, v5\n" \
  "v_lshlrev_b32 v237, 26, v5\n" \
  "s_lshl2_add_u32 s4, s31, s28\n" \
  "s_lshl_b32 s4, s4, 7\n" \
  "s_add_u32 s36, s4, 96\n" \
  "s_min_u32 s36, s36, 984\n" \
  "s_add_u32 s37, s4, 112\n" \
  "s_min_u32 s37, s37, 984\n" \
  "v_add_u32 v186, s4, v252\n" \
  "v_add_u32 v187, s36, v252\n" \
  "v_add_u32 v188, s37, v252\n" \
  "s_mul_i32 s38, s7, 51200\n" \
  "s_add_u32 s42, s8, s38\n" \
  "s_addc_u32 s43, s9, 0\n" \
  "s_xor_b32 s38, s31, 1\n" \
  "s_lshl_b32 s38, s38, 7\n" \
  "s_add_u32 s94, s10, s38\n" \
  "s_addc_u32 s95, s11, 0\n" \
  "s_mul_i32 s5, s28, 4224\n" \
  "s_add_u32 s5, s5, 146432\n" \
  "v_lshrrev_b32 v2, 3, v186\n" \
  "v_lshl_add_u32 v2, v186, 3, v2\n" \
  "v_add_lshl_u32 v249, v2, v253, 4\n" \
  "v_lshrrev_b32 v2, 3, v187\n" \
  "v_lshl_add_u32 v2, v187, 3, v2\n" \
  "v_add_lshl_u32 v250, v2, v253, 4\n" \
  "v_lshrrev_b32 v2, 3, v188\n" \
  "v_lshl_add_u32 v2, v188, 3, v2\n" \
  "v_add_lshl_u32 v251, v2, v253, 4\n" \
  "v_mul_u32_u24 v1, 1600000, v253\n" \
  "v_lshl_add_u32 v186, v186, 4, v1\n" \
  "v_lshl_add_u32 v187, v187, 4, v1\n" \
  "v_lshl_add_u32 v188, v188, 4, v1\n" \
  "v_lshlrev_b32 v1, 4, v253\n" \
  "v_xor_b32 v244, v1, v254\n" \
  "v_lshl_add_u32 v245, v255, 4, s5\n" \
  "v_lshrrev_b32 v1, 1, v252\n" \
  "s_waitcnt vmcnt(1)\n" \
  "s_mov_b64 exec, 1\n" \
  "ds_add_u32 v190, v191 offset:12\n" \
  "s_mov_b64 exec, -1\n" \
  "s_memtime s[88:89]\n" \
  "v_xor_b32 v1, v1, v253\n" \
  "v_lshlrev_b32 v1, 4, v1\n" \
  "v_lshl_add_u32 v1, v252, 7, v1\n" \
  "v_add_u32 v246, s5, v1\n" \
  "v_xor_b32 v1, 64, v1\n" \
  "v_add_u32 v242, s5, v1\n" \
  "v_lshlrev_b32 v1, 4, v255\n" \
  "v_xor_b32 v1, 64, v1\n" \
  "v_add_u32 v243, s5, v1\n" \
  "v_lshrrev_b32 v1, 2, v255\n" \
  "s_lshl_b32 s35, s28, 5\n" \
  "s_lshl_b32 s36, s31, 6\n" \
  "s_add_u32 s35, s35, s36\n" \
  "s_and_b32 s35, s35, 127\n" \
  "v_add_u32 v1, s35, v1\n" \
  "v_lshrrev_b32 v2, 4, v255\n" \
  "v_and_b32 v2, 2, v2\n" \
  "v_xor_b32 v2, v2, v255\n" \
  "v_and_b32 v2, 3, v2\n" \
  "v_lshlrev_b32 v2, 4, v2\n" \
  "v_lshl_add_u32 v189, v1, 10, v2\n" \
  "v_add_u32 v194, 15360, v189\n" \
  "v_lshrrev_b32 v1, 2, v252\n" \
  "v_lshlrev_b32 v1, 6, v1\n" \
  "v_lshl_add_u32 v1, v253, 9, v1\n" \
  "v_and_b32 v2, 3, v252\n" \
  "v_lshl_add_u32 v1, v2, 3, v1\n" \
  "v_and_b32 v2, 1, v253\n" \
  "v_lshl_add_u32 v1, v2, 5, v1\n" \
  "v_add_u32 v184, 138240, v1\n" \
  "v_xor_b32 v1, 32, v1\n" \
  "v_add_u32 v185, 138240, v1\n" \
  "s_bfe_u32 s35, s30, 0x10001\n" \
  "s_lshl1_add_u32 s35, s29, s35\n" \
  "s_lshr_b32 s6, s30, 2\n" \
  "s_lshl_b32 s6, s6, 18\n" \
  "s_lshl_b32 s38, s35, 6\n" \
  "s_add_u32 s92, s14, s38\n" \
  "s_addc_u32 s93, s15, 0\n" \
  "s_lshr_b32 s37, s30, 3\n" \
  "s_add_u32 s37, s37, 96\n" \
  "s_lshl_b32 s34, s37, 17\n" \
  "s_mul_i32 s35, s35, 400\n" \
  "s_add_u32 s37, s37, s35\n" \
  "s_mul_i32 s37, s37, 16000\n" \
  "s_add_u32 s46, s24, s37\n" \
  "s_addc_u32 s47, s25, 0\n" \
  "s_lshr_b32 s36, s30, 2\n" \
  "s_add_u32 s36, s36, s35\n" \
  "s_mul_i32 s36, s36, 16000\n" \
  "s_add_u32 s44, s24, s36\n" \
  "s_addc_u32 s45, s25, 0\n" \
  "s_lshl_b32 s96, s28, 11\n" \
  "s_add_u32 s96, s96, 138240\n" \
  "v_lshlrev_b32 v238, 4, v255\n" \
  "v_mov_b32 v234, 0\n" \
  "s_lshl2_add_u32 s33, s26, s28\n" \
  "s_mov_b32 s52, 0x10002\n" \
  "s_mov_b32 s53, 0xc0c0400\n" \
  "s_mov_b32 s54, 0x4000c0c\n" \
  "s_mov_b32 s55, 0x3030303\n" \
  "s_mov_b32 s56, 0x55555555\n" \
  "s_mov_b32 s38, 0x0\n" \
  "s_mov_b32 s98, 0x0\n" \
  "s_mov_b32 s99, 0x8\n" \
  "s_mov_b32 s97, 0x2000\n" \
  "v_xor_b32 v245, 64, v244\n" \
  "v_add_u32 v245, 0x400, v245\n" \
  "s_setprio 0\n" \
  "L5_:\n" \
  "ds_read_b32 v192, v190 offset:16\n" \
  "s_waitcnt lgkmcnt(0)\n" \
  "v_readfirstlane_b32 s35, v192\n" \
  "s_cmp_lt_u32 s35, 4\n" \
  "s_cbranch_scc1 L5_\n" \
  "s_add_u32 s35, s4, 0\n" \
  "s_min_u32 s35, s35, 984\n" \
  "s_lshl_b32 s35, s35, 8\n" \
  "s_add_u32 m0, s5, 0\n" \
  "s_add_u32 s58, s94, s35\n" \
  "s_addc_u32 s59, s95, 0\n" \
  "global_load_lds_dwordx4 v244, s[58:59]\n" \
  "global_load_lds_dwordx4 v245, s[58:59] offset:1024\n" \
  "s_add_u32 s35, s4, 16\n" \
  "s_min_u32 s35, s35, 984\n" \
  "s_lshl_b32 s35, s35, 8\n" \
  "s_add_u32 m0, s5, 2048\n" \
  "s_add_u32 s58, s94, s35\n" \
  "s_addc_u32 s59, s95, 0\n" \
  "global_load_lds_dwordx4 v244, s[58:59]\n" \
  "global_load_lds_dwordx4 v245, s[58:59] offset:1024\n" \
  "L6_:\n" \
  "ds_read_b32 v192, v190 offset:12\n" \
  "s_waitcnt lgkmcnt(0)\n" \
  "v_readfirstlane_b32 s35, v192\n" \
  "s_cmp_lt_u32 s35, 4\n" \
  "s_cbranch_scc1 L6_\n" \
  "s_waitcnt vmcnt(0)\n" \
  "ds_read_b128 v[8:11], v246 offset:0\n" \
  "ds_read_b128 v[12:15], v242 offset:0\n" \
  "ds_read_b128 v[24:27], v246 offset:2048\n" \
  "ds_read_b128 v[28:31], v242 offset:2048\n" \
  "ds_read_b128 v[0:3], v249 offset:0\n" \
  "ds_read_b128 v[4:7], v249 offset:64\n" \
  "ds_read_b128 v[16:19], v249 offset:2080\n" \
  "ds_read_b128 v[20:23], v249 offset:2144\n" \
  "s_waitcnt lgkmcnt(4)\n" \
  "s_add_u32 s35, s4, 32\n" \
  "s_min_u32 s35, s35, 984\n" \
  "s_lshl_b32 s35, s35, 8\n" \
  "s_add_u32 m0, s5, 0\n" \
  "s_add_u32 s58, s94, s35\n" \
  "s_addc_u32 s59, s95, 0\n" \
  "global_load_lds_dwordx4 v244, s[58:59]\n" \
  "global_load_lds_dwordx4 v245, s[58:59] offset:1024\n" \
  "s_add_u32 s35, s4, 48\n" \
  "s_min_u32 s35, s35, 984\n" \
  "s_lshl_b32 s35, s35, 8\n" \
  "s_add_u32 m0, s5, 2048\n" \
  "s_add_u32 s58, s94, s35\n" \
  "s_addc_u32 s59, s95, 0\n" \
  "global_load_lds_dwordx4 v244, s[58:59]\n" \
  "global_load_lds_dwordx4 v245, s[58:59] offset:1024\n" \
  "s_waitcnt vmcnt(0)\n" \
  "ds_read_b128 v[40:43], v246 offset:0\n" \
  "ds_read_b128 v[44:47], v242 offset:0\n" \
  "ds_read_b128 v[56:59], v246 offset:2048\n" \
  "ds_read_b128 v[60:63], v242 offset:2048\n" \
  "ds_read_b128 v[32:35], v249 offset:4160\n" \
  "ds_read_b128 v[36:39], v249 offset:4224\n" \
  "ds_read_b128 v[48:51], v249 offset:6240\n" \
  "ds_read_b128 v[52:55], v249 offset:6304\n" \
  "s_waitcnt lgkmcnt(4)\n" \
  "s_add_u32 s40, s92, s6\n" \
  "s_addc_u32 s41, s93, 0\n" \
  "s_add_u32 s6, s6, 2097152\n" \
  "s_mov_b32 m0, s96\n" \
  "s_nop 0\n" \
  "global_load_lds_dwordx4 v189, s[40:41]\n" \
  "global_load_lds_dwordx4 v194, s[40:41] offset:1024\n" \
  "global_load_dwordx2 v[196:197], v227, s[42:43] offset:-3200\n" \
  "global_load_dwordx2 v[198:199], v227, s[42:43] offset:-2800\n" \
  "global_load_dwordx2 v[200:201], v227, s[42:43] offset:-2400\n" \
  "global_load_dwordx2 v[202:203], v227, s[42:43] offset:-1600\n" \
  "global_load_dwordx2 v[204:205], v227, s[42:43] offset:-1200\n" \
  "global_load_dwordx2 v[206:207], v227, s[42:43] offset:-800\n" \
  "global_load_dwordx2 v[208:209], v227, s[42:43] offset:-400\n" \
  "global_load_dwordx2 v[210:211], v227, s[42:43] offset:0\n" \
  "global_load_dwordx2 v[212:213], v227, s[42:43] offset:400\n" \
  "global_load_dwordx2 v[214:215], v227, s[42:43] offset:800\n" \
  "global_load_dwordx2 v[216:217], v227, s[42:43] offset:1200\n" \
  "global_load_dwordx2 v[218:219], v227, s[42:43] offset:-2000\n" \
  "global_load_dwordx2 v[220:221], v227, s[42:43] offset:1600\n" \
  "global_load_dwordx2 v[222:223], v227, s[42:43] offset:2000\n" \
  "global_load_dwordx2 v[224:225], v227, s[42:43] offset:2400\n" \
  "global_load_dwordx2 v[226:227], v227, s[42:43] offset:2800\n" \
  "s_add_u32 s35, s4, 64\n" \
  "s_min_u32 s35, s35, 984\n" \
  "s_lshl_b32 s35, s35, 8\n" \
  "s_add_u32 m0, s5, 0\n" \
  "s_add_u32 s58, s94, s35\n" \
  "s_addc_u32 s59, s95, 0\n" \
  "global_load_lds_dwordx4 v244, s[58:59]\n" \
  "global_load_lds_dwordx4 v245, s[58:59] offset:1024\n" \
  "s_add_u32 s35, s4, 80\n" \
  "s_min_u32 s35, s35, 984\n" \
  "s_lshl_b32 s35, s35, 8\n" \
  "s_add_u32 m0, s5, 2048\n" \
  "s_add_u32 s58, s94, s35\n" \
  "s_addc_u32 s59, s95, 0\n" \
  "global_load_lds_dwordx4 v244, s[58:59]\n" \
  "global_load_lds_dwordx4 v245, s[58:59] offset:1024\n" \
  "s_waitcnt vmcnt(0)\n" \
  "ds_read_b128 v[72:75], v246 offset:0\n" \
  "ds_read_b128 v[76:79], v242 offset:0\n" \
  "ds_read_b128 v[88:91], v246 offset:2048\n" \
  "ds_read_b128 v[92:95], v242 offset:2048\n" \
  "ds_read_b128 v[64:67], v249 offset:8320\n" \
  "ds_read_b128 v[68:71], v249 offset:8384\n" \
  "ds_read_b128 v[80:83], v249 offset:10400\n" \
  "ds_read_b128 v[84:87], v249 offset:10464\n" \
  "s_waitcnt lgkmcnt(4)\n" \
  "s_add_u32 s35, s4, 96\n" \
  "s_min_u32 s35, s35, 984\n" \
  "s_lshl_b32 s35, s35, 8\n" \
  "s_add_u32 m0, s5, 0\n" \
  "s_add_u32 s58, s94, s35\n" \
  "s_addc_u32 s59, s95, 0\n" \
  "global_load_lds_dwordx4 v244, s[58:59]\n" \
  "global_load_lds_dwordx4 v245, s[58:59] offset:1024\n" \
  "s_add_u32 s35, s4, 112\n" \
  "s_min_u32 s35, s35, 984\n" \
  "s_lshl_b32 s35, s35, 8\n" \
  "s_add_u32 m0, s5, 2048\n" \
  "s_add_u32 s58, s94, s35\n" \
  "s_addc_u32 s59, s95, 0\n" \
  "global_load_lds_dwordx4 v244, s[58:59]\n" \
  "global_load_lds_dwordx4 v245, s[58:59] offset:1024\n" \
  "s_lshl2_add_u32 s35, s26, s28\n" \
  "s_mul_i32 s35, s35, 6400\n" \
  "s_add_u32 s35, s35, 0\n" \
  "s_add_u32 s58, s14, s35\n" \
  "s_addc_u32 s59, s15, 0\n" \
  "s_mov_b32 exec_lo, 0xffffffff\n" \
  "s_mov_b32 exec_hi, 0x3ffff\n" \
  "global_load_dword v248, v247, s[58:59]\n" \
  "s_mov_b64 exec, -1\n" \
  "s_lshl2_add_u32 s35, s26, s28\n" \
  "s_mul_i32 s35, s35, 6400\n" \
  "s_add_u32 s35, s35, 6553600\n" \
  "s_add_u32 s58, s14, s35\n" \
  "s_addc_u32 s59, s15, 0\n" \
  "s_mov_b32 exec_lo, 0xffffffff\n" \
  "s_mov_b32 exec_hi, 0x3ffff\n" \
  "global_load_dword v248, v247, s[58:59]\n" \
  "s_mov_b64 exec, -1\n" \
  "s_getpc_b64 s[58:59]\n" \
  "LB_pc:\n" \
  "s_sub_u32 s58, s58, LB_pc-LZ_first\n" \
  "s_subb_u32 s59, s59, 0\n" \
  "s_mul_i32 s35, s28, 7168\n" \
  "v_lshl_add_u32 v195, v255, 7, s35\n" \
  "v_min_u32 v195, LZ_end-LZ_first, v195\n" \
  "s_mov_b32 exec_hi, 0xffffff\n" \
  "global_load_dword v248, v195, s[58:59]\n" \
  "s_mov_b64 exec, -1\n" \
  "s_waitcnt vmcnt(3)\n" \
  "ds_read_b128 v[104:107], v246 offset:0\n" \
  "ds_read_b128 v[108:111], v242 offset:0\n" \
  "ds_read_b128 v[120:123], v246 offset:2048\n" \
  "ds_read_b128 v[124:127], v242 offset:2048\n" \
  "ds_read_b128 v[96:99], v250 offset:0\n" \
  "ds_read_b128 v[100:103], v250 offset:64\n" \
  "ds_read_b128 v[112:115], v251 offset:0\n" \
  "ds_read_b128 v[116:119], v251 offset:64\n" \
  "s_waitcnt lgkmcnt(4)\n" \
  "s_waitcnt vmcnt(0)\n" \
  "s_sleep 8\n" \
  "s_setprio 2\n" \
  "s_mul_i32 s36, s28, 2176\n" \
  "s_add_u32 s36, s36, 154624\n" \
  "v_bfe_u32 v228, v255, 3, 1\n" \
  "v_and_b32 v229, 1, v255\n" \
  "v_lshl_add_u32 v230, v228, 1, v229\n" \
  "v_lshl_add_u32 v231, v229, 1, v228\n" \
  "v_xor_b32 v229, 1, v230\n" \
  "s_mov_b32 vcc_lo, 0xcccccccc\n" \
  "s_mov_b32 vcc_hi, vcc_lo\n" \
  "v_cndmask_b32 v228, v230, v231, vcc\n" \
  "v_cndmask_b32 v229, v229, v231, vcc\n" \
  "v_and_b32 v230, 4, v255\n" \
  "v_mul_u32_u24 v230, 272, v230\n" \
  "v_lshl_add_u32 v230, v253, 2, v230\n" \
  "v_and_or_b32 v230, v255, 2, v230\n" \
  "v_add_u32 v230, s36, v230\n" \
  "s_movk_i32 s35, 272\n" \
  "v_mad_u32_u24 v195, v228, s35, v230\n" \
  "v_mad_u32_u24 v235, v229, s35, v230\n" \
  "v_lshl_add_u32 v230, v253, 4, v238\n" \
  "v_add_u32 v254, s36, v230\n" \
  "v_readfirstlane_b32 s39, v193\n" \
  "s_lshl_b32 s35, s26, 2\n" \
  "s_add_u32 s39, s39, 1\n" \
  "v_mov_b32 v193, s35\n" \
  "v_mov_b64 v[240:241], -1\n" \
  "v_mov_b64 v[242:243], -1\n" \
  "s_waitcnt lgkmcnt(0)\n" \
  "s_mov_b64 exec, 1\n" \
  "ds_add_u32 v190, v191 offset:0\n" \
  "s_mov_b64 exec, -1\n" \
  "L7_:\n" \
  "ds_read_b32 v192, v190 offset:0\n" \
  "s_waitcnt lgkmcnt(0)\n" \
  "v_readfirstlane_b32 s35, v192\n" \
  "s_cmp_lt_u32 s35, 4\n" \
  "s_cbranch_scc1 L7_\n" \
  "s_add_u32 s96, s96, 8192\n" \
  "s_add_u32 s40, s92, s6\n" \
  "s_addc_u32 s41, s93, 0\n" \
  "s_add_u32 s6, s6, 2097152\n" \
  "s_mov_b32 m0, s96\n" \
  "s_nop 0\n" \
  "global_load_lds_dwordx4 v189, s[40:41]\n" \
  "global_load_lds_dwordx4 v194, s[40:41] offset:1024\n" \
  "s_sub_u32 s96, s96, 8192\n" \
  "ds_read_b64_tr_b16 v[128:129], v184 offset:0\n" \
  "ds_read_b64_tr_b16 v[130:131], v184 offset:256\n" \
  "ds_read_b64_tr_b16 v[132:133], v184 offset:2048\n" \
  "ds_read_b64_tr_b16 v[134:135], v184 offset:2304\n" \
  "ds_read_b64_tr_b16 v[136:137], v184 offset:4096\n" \
  "ds_read_b64_tr_b16 v[138:139], v184 offset:4352\n" \
  "ds_read_b64_tr_b16 v[140:141], v184 offset:6144\n" \
  "ds_read_b64_tr_b16 v[142:143], v184 offset:6400\n" \
  "ds_read_b64_tr_b16 v[156:157], v185 offset:6144\n" \
  "ds_read_b64_tr_b16 v[158:159], v185 offset:6400\n" \
  "ds_read_b64_tr_b16 v[152:153], v185 offset:4096\n" \
  "ds_read_b64_tr_b16 v[154:155], v185 offset:4352\n" \
  "ds_read_b64_tr_b16 v[148:149], v185 offset:2048\n" \
  "ds_read_b64_tr_b16 v[150:151], v185 offset:2304\n" \
  "ds_read_b64_tr_b16 v[144:145], v185 offset:0\n" \
  "ds_read_b64_tr_b16 v[146:147], v185 offset:256\n" \
  "v_mov_b32 v192, s39\n" \
  "s_cmp_lg_u32 s28, 0\n" \
  "s_cbranch_scc1 LB_nc\n" \
  "s_mov_b64 exec, 1\n" \
  "global_store_dword v193, v192, s[48:49] offset:0 sc1\n" \
  "s_mov_b64 exec, -1\n" \
  "LB_nc:\n" \
  "s_lshl2_add_u32 s35, s7, s28\n" \
  "s_lshl_b32 s35, s35, 7\n" \
  "s_lshl_b32 s36, s31, 16\n" \
  "s_add_u32 s35, s35, s36\n" \
  "s_add_u32 s35, s35, 109871104\n" \
  "s_add_u32 s58, s24, s35\n" \
  "s_addc_u32 s59, s25, 0\n" \
  "s_mov_b64 exec, 0xff\n" \
  "global_store_dwordx4 v238, v[240:243], s[58:59] sc1\n" \
  "s_mov_b64 exec, -1\n" \
  "s_cmp_lg_u32 s33, 1021\n" \
  "s_cbranch_scc1 LB_nm\n" \
  "v_mov_b64 v[240:241], 0\n" \
  "v_mov_b64 v[242:243], 0\n" \
  "global_store_dwordx4 v238, v[240:243], s[48:49] offset:2048 sc1\n" \
  "s_nop 1\n" \
  "v_lshlrev_b32 v242, 8, v255\n" \
  "global_store_dwordx2 v242, v[240:241], s[48:49] offset:3072 sc1\n" \
  "v_add_u32 v242, 0x4000, v242\n" \
  "global_store_dwordx2 v242, v[240:241], s[48:49] offset:3072 sc1\n" \
  "v_add_u32 v242, 0x4000, v242\n" \
  "global_store_dwordx2 v242, v[240:241], s[48:49] offset:3072 sc1\n" \
  "v_add_u32 v242, 0x4000, v242\n" \
  "global_store_dwordx2 v242, v[240:241], s[48:49] offset:3072 sc1\n" \
  "v_add_u32 v242, 0x4000, v242\n" \
  "LB_nm:\n" \
  "s_mov_b32 s86, 0x78000b0\n" \
  "s_mov_b32 s87, 0x0\n" \
  "s_or_b32 s86, s86, s87\n" \
  "s_setprio 2\n" \
  "s_memtime s[90:91]\n" \
  "s_memrealtime s[42:43]\n" \
  "LT:\n" \
  "s_cmp_eq_u32 s38, 6\n" \
  "s_cselect_b32 s78, 0x33333333, -1\n" \
  "s_cmp_eq_u32 s38, 7\n" \
  "s_cselect_b32 s78, 0x33333333, s78\n" \
  "s_cmp_eq_u32 s38, 18\n" \
  "s_cselect_b32 s80, 0x33333333, -1\n" \
  "s_cmp_eq_u32 s38, 19\n" \
  "s_cselect_b32 s80, 0x33333333, s80\n" \
  "s_mov_b32 s79, s78\n" \
  "s_mov_b32 s81, s80\n" \
  "s_mov_b32 s37, s38\n" \
  "s_add_u32 s57, s38, 2\n" \
  "s_add_u32 s3, s38, 4\n" \
  "s_add_u32 s38, s38, 6\n" \
  "s_cmp_lt_u32 s57, 32\n" \
  "s_cselect_b32 s57, s57, 37\n" \
  "s_cselect_b32 s3, s3, 37\n" \
  "s_waitcnt lgkmcnt(12)\n" \
  "v_mfma_f32_16x16x32_bf16 v[160:163], v[128:131], v[0:3], 0\n" \
  "s_mov_b64 vcc, s[78:79]\n" \
  "v_cndmask_b32_dpp v234, v234, v234, vcc row_ror:8 row_mask:0xf bank_mask:0xf\n" \
  "v_mfma_f32_16x16x32_bf16 v[160:163], v[132:135], v[4:7], v[160:163]\n" \
  "s_waitcnt lgkmcnt(8)\n" \
  "v_mfma_f32_16x16x32_bf16 v[160:163], v[136:139], v[8:11], v[160:163]\n" \
  "s_set_gpr_idx_on s37, gpr_idx(SRC0)\n" \
  "v_mad_u32_u24 v232, v196, s52, v236\n" \
  "s_set_gpr_idx_off\n" \
  "v_mfma_f32_16x16x32_bf16 v[160:163], v[140:143], v[12:15], v[160:163]\n" \
  "s_waitcnt lgkmcnt(0)\n" \
  "v_lshrrev_b32 v228, v232, v234\n" \
  "v_lshrrev_b32_dpp v229, v232, v234 quad_perm:[2,3,0,1] row_mask:0xf bank_mask:0xf\n" \
  "v_mfma_f32_16x16x32_bf16 v[164:167], v[156:159], v[12:15], 0\n" \
  "v_lshrrev_b32_dpp v230, v232, v234 quad_perm:[1,2,3,0] row_mask:0xf bank_mask:0xf\n" \
  "v_lshrrev_b32_dpp v231, v232, v234 quad_perm:[3,0,1,2] row_mask:0xf bank_mask:0xf\n" \
  "v_mfma_f32_16x16x32_bf16 v[164:167], v[152:155], v[8:11], v[164:167]\n" \
  "v_perm_b32 v228, v229, v228, s53\n" \
  "v_perm_b32 v230, v231, v230, s54\n" \
  "v_mfma_f32_16x16x32_bf16 v[164:167], v[148:151], v[4:7], v[164:167]\n" \
  "v_bitop3_b32 v228, v228, v230, s55 bitop3:0xa8\n" \
  "v_lshl_or_b32 v229, v234, 1, s56\n" \
  "v_mfma_f32_16x16x32_bf16 v[164:167], v[144:147], v[0:3], v[164:167]\n" \
  "v_add_u32_dpp v228, v228, v228 quad_perm:[2,3,0,1] row_mask:0xf bank_mask:0xf\n" \
  "v_min_u32_sdwa v230, v228, v228 dst_sel:DWORD dst_unused:UNUSED_PAD src0_sel:BYTE_0 src1_sel:BYTE_1\n" \
  "v_mfma_f32_16x16x32_bf16 v[172:175], v[144:147], v[16:19], 0\n" \
  "v_min_u32_sdwa v231, v228, v228 dst_sel:DWORD dst_unused:UNUSED_PAD src0_sel:BYTE_2 src1_sel:BYTE_3\n" \
  "v_sub_u32 v230, v230, v231\n" \
  "v_mfma_f32_16x16x32_bf16 v[172:175], v[148:151], v[20:23], v[172:175]\n" \
  "v_cvt_pk_bf16_f32 v176, v160, v161\n" \
  "v_add_u32_dpp v230, v230, v230 quad_perm:[1,0,3,2] row_mask:0xf bank_mask:0xf\n" \
  "v_mfma_f32_16x16x32_bf16 v[172:175], v[152:155], v[24:27], v[172:175]\n" \
  "v_cmp_ge_i32 vcc, 0, v230\n" \
  "v_cvt_pk_bf16_f32 v177, v162, v163\n" \
  "v_mfma_f32_16x16x32_bf16 v[172:175], v[156:159], v[28:31], v[172:175]\n" \
  "v_cndmask_b32_sdwa v228, v228, v228, vcc dst_sel:DWORD dst_unused:UNUSED_PAD src0_sel:WORD_1 src1_sel:WORD_0\n" \
  "v_cvt_pk_bf16_f32 v178, v164, v165\n" \
  "v_mfma_f32_16x16x32_bf16 v[168:171], v[140:143], v[28:31], 0\n" \
  "s_mov_b64 s[50:51], vcc\n" \
  "v_cmp_le_u32_sdwa vcc, v228, v228 src0_sel:BYTE_0 src1_sel:BYTE_1\n" \
  "v_cvt_pk_bf16_f32 v179, v166, v167\n" \
  "v_mfma_f32_16x16x32_bf16 v[168:171], v[136:139], v[24:27], v[168:171]\n" \
  "v_cndmask_b32_dpp v232, v232, v232, vcc quad_perm:[2,3,0,1] row_mask:0xf bank_mask:0xf\n" \
  "v_mfma_f32_16x16x32_bf16 v[168:171], v[132:135], v[20:23], v[168:171]\n" \
  "s_mov_b64 vcc, s[50:51]\n" \
  "v_cndmask_b32_dpp v232, v232, v232, vcc quad_perm:[3,0,1,2] row_mask:0xf bank_mask:0xf\n" \
  "v_lshlrev_b32_e64 v230, v232, 3\n" \
  "v_mfma_f32_16x16x32_bf16 v[168:171], v[128:131], v[16:19], v[168:171]\n" \
  "v_and_or_b32 v234, v229, v230, v234\n" \
  "s_cmp_lg_u32 s33, 1021\n" \
  "s_cbranch_scc0 LT_e\n" \
  "LT_r:\n" \
  "s_waitcnt vmcnt(5)\n" \
  "global_store_dwordx4 v186, v[176:179], s[44:45] offset:0 sc1\n" \
  "v_mfma_f32_16x16x32_bf16 v[160:163], v[128:131], v[32:35], 0\n" \
  "s_set_gpr_idx_on s37, gpr_idx(DST)\n" \
  "v_mov_b32 v196, v232\n" \
  "s_set_gpr_idx_off\n" \
  "v_mfma_f32_16x16x32_bf16 v[160:163], v[132:135], v[36:39], v[160:163]\n" \
  "s_set_gpr_idx_on s57, gpr_idx(SRC0)\n" \
  "v_mad_u32_u24 v232, v196, s52, v236\n" \
  "s_set_gpr_idx_off\n" \
  "v_mfma_f32_16x16x32_bf16 v[160:163], v[136:139], v[40:43], v[160:163]\n" \
  "v_lshrrev_b32 v228, v232, v234\n" \
  "v_lshrrev_b32_dpp v229, v232, v234 quad_perm:[2,3,0,1] row_mask:0xf bank_mask:0xf\n" \
  "v_mfma_f32_16x16x32_bf16 v[160:163], v[140:143], v[44:47], v[160:163]\n" \
  "v_lshrrev_b32_dpp v230, v232, v234 quad_perm:[1,2,3,0] row_mask:0xf bank_mask:0xf\n" \
  "v_lshrrev_b32_dpp v231, v232, v234 quad_perm:[3,0,1,2] row_mask:0xf bank_mask:0xf\n" \
  "v_mfma_f32_16x16x32_bf16 v[164:167], v[156:159], v[44:47], 0\n" \
  "v_perm_b32 v228, v229, v228, s53\n" \
  "v_perm_b32 v230, v231, v230, s54\n" \
  "v_mfma_f32_16x16x32_bf16 v[164:167], v[152:155], v[40:43], v[164:167]\n" \
  "v_bitop3_b32 v228, v228, v230, s55 bitop3:0xa8\n" \
  "v_lshl_or_b32 v229, v234, 1, s56\n" \
  "v_mfma_f32_16x16x32_bf16 v[164:167], v[148:151], v[36:39], v[164:167]\n" \
  "v_cvt_pk_bf16_f32 v182, v172, v173\n" \
  "v_cvt_pk_bf16_f32 v183, v174, v175\n" \
  "v_mfma_f32_16x16x32_bf16 v[164:167], v[144:147], v[32:35], v[164:167]\n" \
  "v_cvt_pk_bf16_f32 v180, v168, v169\n" \
  "v_cvt_pk_bf16_f32 v181, v170, v171\n" \
  "s_waitcnt vmcnt(5)\n" \
  "global_store_dwordx4 v186, v[180:183], s[44:45] offset:256 sc1\n" \
  "v_mfma_f32_16x16x32_bf16 v[172:175], v[144:147], v[48:51], 0\n" \
  "v_add_u32_dpp v228, v228, v228 quad_perm:[2,3,0,1] row_mask:0xf bank_mask:0xf\n" \
  "v_min_u32_sdwa v230, v228, v228 dst_sel:DWORD dst_unused:UNUSED_PAD src0_sel:BYTE_0 src1_sel:BYTE_1\n" \
  "v_mfma_f32_16x16x32_bf16 v[172:175], v[148:151], v[52:55], v[172:175]\n" \
  "v_min_u32_sdwa v231, v228, v228 dst_sel:DWORD dst_unused:UNUSED_PAD src0_sel:BYTE_2 src1_sel:BYTE_3\n" \
  "v_sub_u32 v230, v230, v231\n" \
  "v_mfma_f32_16x16x32_bf16 v[172:175], v[152:155], v[56:59], v[172:175]\n" \
  "v_cvt_pk_bf16_f32 v176, v160, v161\n" \
  "v_add_u32_dpp v230, v230, v230 quad_perm:[1,0,3,2] row_mask:0xf bank_mask:0xf\n" \
  "v_mfma_f32_16x16x32_bf16 v[172:175], v[156:159], v[60:63], v[172:175]\n" \
  "v_cmp_ge_i32 vcc, 0, v230\n" \
  "v_cvt_pk_bf16_f32 v177, v162, v163\n" \
  "v_mfma_f32_16x16x32_bf16 v[168:171], v[140:143], v[60:63], 0\n" \
  "v_cndmask_b32_sdwa v228, v228, v228, vcc dst_sel:DWORD dst_unused:UNUSED_PAD src0_sel:WORD_1 src1_sel:WORD_0\n" \
  "v_cvt_pk_bf16_f32 v178, v164, v165\n" \
  "v_mfma_f32_16x16x32_bf16 v[168:171], v[136:139], v[56:59], v[168:171]\n" \
  "s_mov_b64 s[50:51], vcc\n" \
  "v_cmp_le_u32_sdwa vcc, v228, v228 src0_sel:BYTE_0 src1_sel:BYTE_1\n" \
  "v_cvt_pk_bf16_f32 v179, v166, v167\n" \
  "v_mfma_f32_16x16x32_bf16 v[168:171], v[132:135], v[52:55], v[168:171]\n" \
  "v_cndmask_b32_dpp v232, v232, v232, vcc quad_perm:[2,3,0,1] row_mask:0xf bank_mask:0xf\n" \
  "v_mfma_f32_16x16x32_bf16 v[168:171], v[128:131], v[48:51], v[168:171]\n" \
  "s_mov_b64 vcc, s[50:51]\n" \
  "v_cndmask_b32_dpp v232, v232, v232, vcc quad_perm:[3,0,1,2] row_mask:0xf bank_mask:0xf\n" \
  "v_lshlrev_b32_e64 v230, v232, 3\n" \
  "s_waitcnt vmcnt(5)\n" \
  "global_store_dwordx4 v186, v[176:179], s[44:45] offset:512 sc1\n" \
  "v_mfma_f32_16x16x32_bf16 v[160:163], v[128:131], v[64:67], 0\n" \
  "v_and_or_b32 v234, v229, v230, v234\n" \
  "v_cvt_pk_bf16_f32 v182, v172, v173\n" \
  "v_mfma_f32_16x16x32_bf16 v[160:163], v[132:135], v[68:71], v[160:163]\n" \
  "s_set_gpr_idx_on s57, gpr_idx(DST)\n" \
  "v_mov_b32 v196, v232\n" \
  "s_set_gpr_idx_off\n" \
  "v_mfma_f32_16x16x32_bf16 v[160:163], v[136:139], v[72:75], v[160:163]\n" \
  "s_mov_b64 vcc, s[80:81]\n" \
  "v_cndmask_b32_dpp v234, v234, v234, vcc row_ror:8 row_mask:0xf bank_mask:0xf\n" \
  "v_cvt_pk_bf16_f32 v183, v174, v175\n" \
  "v_mfma_f32_16x16x32_bf16 v[160:163], v[140:143], v[76:79], v[160:163]\n" \
  "s_set_gpr_idx_on s3, gpr_idx(SRC0)\n" \
  "v_mad_u32_u24 v232, v196, s52, v236\n" \
  "s_set_gpr_idx_off\n" \
  "v_mfma_f32_16x16x32_bf16 v[164:167], v[156:159], v[76:79], 0\n" \
  "v_lshrrev_b32 v228, v232, v234\n" \
  "v_lshrrev_b32_dpp v229, v232, v234 quad_perm:[2,3,0,1] row_mask:0xf bank_mask:0xf\n" \
  "v_mfma_f32_16x16x32_bf16 v[164:167], v[152:155], v[72:75], v[164:167]\n" \
  "v_lshrrev_b32_dpp v230, v232, v234 quad_perm:[1,2,3,0] row_mask:0xf bank_mask:0xf\n" \
  "v_lshrrev_b32_dpp v231, v232, v234 quad_perm:[3,0,1,2] row_mask:0xf bank_mask:0xf\n" \
  "v_mfma_f32_16x16x32_bf16 v[164:167], v[148:151], v[68:71], v[164:167]\n" \
  "v_perm_b32 v228, v229, v228, s53\n" \
  "v_perm_b32 v230, v231, v230, s54\n" \
  "v_mfma_f32_16x16x32_bf16 v[164:167], v[144:147], v[64:67], v[164:167]\n" \
  "v_cvt_pk_bf16_f32 v180, v168, v169\n" \
  "v_cvt_pk_bf16_f32 v181, v170, v171\n" \
  "s_waitcnt vmcnt(5)\n" \
  "global_store_dwordx4 v186, v[180:183], s[44:45] offset:768 sc1\n" \
  "v_mfma_f32_16x16x32_bf16 v[172:175], v[144:147], v[80:83], 0\n" \
  "s_waitcnt vmcnt(5)\n" \
  "s_mov_b64 exec, 1\n" \
  "ds_add_u32 v190, v191 offset:0\n" \
  "s_mov_b64 exec, -1\n" \
  "v_mfma_f32_16x16x32_bf16 v[172:175], v[148:151], v[84:87], v[172:175]\n" \
  "v_bitop3_b32 v228, v228, v230, s55 bitop3:0xa8\n" \
  "v_lshl_or_b32 v229, v234, 1, s56\n" \
  "v_mfma_f32_16x16x32_bf16 v[172:175], v[152:155], v[88:91], v[172:175]\n" \
  "v_add_u32_dpp v228, v228, v228 quad_perm:[2,3,0,1] row_mask:0xf bank_mask:0xf\n" \
  "v_min_u32_sdwa v230, v228, v228 dst_sel:DWORD dst_unused:UNUSED_PAD src0_sel:BYTE_0 src1_sel:BYTE_1\n" \
  "v_mfma_f32_16x16x32_bf16 v[172:175], v[156:159], v[92:95], v[172:175]\n" \
  "v_min_u32_sdwa v231, v228, v228 dst_sel:DWORD dst_unused:UNUSED_PAD src0_sel:BYTE_2 src1_sel:BYTE_3\n" \
  "v_sub_u32 v230, v230, v231\n" \
  "v_mfma_f32_16x16x32_bf16 v[168:171], v[140:143], v[92:95], 0\n" \
  "v_cvt_pk_bf16_f32 v176, v160, v161\n" \
  "v_add_u32_dpp v230, v230, v230 quad_perm:[1,0,3,2] row_mask:0xf bank_mask:0xf\n" \
  "v_mfma_f32_16x16x32_bf16 v[168:171], v[136:139], v[88:91], v[168:171]\n" \
  "v_cmp_ge_i32 vcc, 0, v230\n" \
  "v_cvt_pk_bf16_f32 v177, v162, v163\n" \
  "v_mfma_f32_16x16x32_bf16 v[168:171], v[132:135], v[84:87], v[168:171]\n" \
  "v_cndmask_b32_sdwa v228, v228, v228, vcc dst_sel:DWORD dst_unused:UNUSED_PAD src0_sel:WORD_1 src1_sel:WORD_0\n" \
  "v_cvt_pk_bf16_f32 v178, v164, v165\n" \
  "v_mfma_f32_16x16x32_bf16 v[168:171], v[128:131], v[80:83], v[168:171]\n" \
  "s_mov_b64 s[50:51], vcc\n" \
  "v_cmp_le_u32_sdwa vcc, v228, v228 src0_sel:BYTE_0 src1_sel:BYTE_1\n" \
  "v_cvt_pk_bf16_f32 v179, v166, v167\n" \
  "s_cmp_eq_u32 s98, 4\n" \
  "s_cselect_b32 s36, 12451840, 0\n" \
  "s_sub_u32 s6, s6, s36\n" \
  "s_cmp_lt_u32 s98, 10\n" \
  "s_cselect_b32 s6, s6, s34\n" \
  "s_add_u32 s40, s92, s6\n" \
  "s_addc_u32 s41, s93, 0\n" \
  "s_add_u32 s6, s6, 2097152\n" \
  "s_waitcnt vmcnt(5)\n" \
  "global_store_dwordx4 v186, v[176:179], s[44:45] offset:1024 sc1\n" \
  "v_mfma_f32_16x16x32_bf16 v[160:163], v[128:131], v[96:99], 0\n" \
  "ds_read_b32 v192, v190\n" \
  "v_add_u32 v184, s97, v184\n" \
  "v_mfma_f32_16x16x32_bf16 v[160:163], v[132:135], v[100:103], v[160:163]\n" \
  "v_add_u32 v185, s97, v185\n" \
  "v_cndmask_b32_dpp v232, v232, v232, vcc quad_perm:[2,3,0,1] row_mask:0xf bank_mask:0xf\n" \
  "v_mfma_f32_16x16x32_bf16 v[160:163], v[136:139], v[104:107], v[160:163]\n" \
  "s_mov_b64 vcc, s[50:51]\n" \
  "v_cndmask_b32_dpp v232, v232, v232, vcc quad_perm:[3,0,1,2] row_mask:0xf bank_mask:0xf\n" \
  "v_lshlrev_b32_e64 v230, v232, 3\n" \
  "v_mfma_f32_16x16x32_bf16 v[160:163], v[140:143], v[108:111], v[160:163]\n" \
  "v_and_or_b32 v234, v229, v230, v234\n" \
  "v_cvt_pk_bf16_f32 v182, v172, v173\n" \
  "v_mfma_f32_16x16x32_bf16 v[164:167], v[156:159], v[108:111], 0\n" \
  "s_set_gpr_idx_on s3, gpr_idx(DST)\n" \
  "v_mov_b32 v196, v232\n" \
  "s_set_gpr_idx_off\n" \
  "v_mfma_f32_16x16x32_bf16 v[164:167], v[152:155], v[104:107], v[164:167]\n" \
  "s_waitcnt lgkmcnt(0)\n" \
  "v_readfirstlane_b32 s35, v192\n" \
  "v_cvt_pk_bf16_f32 v183, v174, v175\n" \
  "v_mfma_f32_16x16x32_bf16 v[164:167], v[148:151], v[100:103], v[164:167]\n" \
  "v_cvt_pk_bf16_f32 v180, v168, v169\n" \
  "v_cvt_pk_bf16_f32 v181, v170, v171\n" \
  "v_mfma_f32_16x16x32_bf16 v[164:167], v[144:147], v[96:99], v[164:167]\n" \
  "s_cmp_lt_u32 s35, s99\n" \
  "s_cbranch_scc1 LT_w\n" \
  "LT_c:\n" \
  "s_mov_b32 m0, s96\n" \
  "s_nop 0\n" \
  "global_load_lds_dwordx4 v189, s[40:41]\n" \
  "global_load_lds_dwordx4 v194, s[40:41] offset:1024\n" \
  "s_add_u32 s96, s96, s97\n" \
  "s_sub_u32 s97, 0, s97\n" \
  "s_add_u32 s99, s99, 4\n" \
  "s_lshl4_add_u32 s35, s28, s98\n" \
  "s_bitcmp1_b64 s[86:87], s35\n" \
  "s_cbranch_scc1 LT_a\n" \
  "LT_s:\n" \
  "s_waitcnt vmcnt(5)\n" \
  "global_store_dwordx4 v186, v[180:183], s[44:45] offset:1280 sc1\n" \
  "v_mfma_f32_16x16x32_bf16 v[168:171], v[128:131], v[112:115], 0\n" \
  "ds_read_b64_tr_b16 v[128:129], v184 offset:0\n" \
  "ds_read_b64_tr_b16 v[130:131], v184 offset:256\n" \
  "v_mfma_f32_16x16x32_bf16 v[168:171], v[132:135], v[116:119], v[168:171]\n" \
  "ds_read_b64_tr_b16 v[132:133], v184 offset:2048\n" \
  "ds_read_b64_tr_b16 v[134:135], v184 offset:2304\n" \
  "v_mfma_f32_16x16x32_bf16 v[168:171], v[136:139], v[120:123], v[168:171]\n" \
  "ds_read_b64_tr_b16 v[136:137], v184 offset:4096\n" \
  "v_cvt_pk_bf16_f32 v176, v160, v161\n" \
  "v_mfma_f32_16x16x32_bf16 v[168:171], v[140:143], v[124:127], v[168:171]\n" \
  "ds_read_b64_tr_b16 v[138:139], v184 offset:4352\n" \
  "v_cvt_pk_bf16_f32 v177, v162, v163\n" \
  "v_mfma_f32_16x16x32_bf16 v[172:175], v[156:159], v[124:127], 0\n" \
  "ds_read_b64_tr_b16 v[140:141], v184 offset:6144\n" \
  "v_cvt_pk_bf16_f32 v178, v164, v165\n" \
  "v_mfma_f32_16x16x32_bf16 v[172:175], v[152:155], v[120:123], v[172:175]\n" \
  "ds_read_b64_tr_b16 v[142:143], v184 offset:6400\n" \
  "v_cvt_pk_bf16_f32 v179, v166, v167\n" \
  "v_mfma_f32_16x16x32_bf16 v[172:175], v[148:151], v[116:119], v[172:175]\n" \
  "ds_read_b64_tr_b16 v[156:157], v185 offset:6144\n" \
  "ds_read_b64_tr_b16 v[158:159], v185 offset:6400\n" \
  "v_mfma_f32_16x16x32_bf16 v[172:175], v[144:147], v[112:115], v[172:175]\n" \
  "ds_read_b64_tr_b16 v[152:153], v185 offset:4096\n" \
  "ds_read_b64_tr_b16 v[154:155], v185 offset:4352\n" \
  "ds_read_b64_tr_b16 v[148:149], v185 offset:2048\n" \
  "ds_read_b64_tr_b16 v[150:151], v185 offset:2304\n" \
  "ds_read_b64_tr_b16 v[144:145], v185 offset:0\n" \
  "ds_read_b64_tr_b16 v[146:147], v185 offset:256\n" \
  "s_waitcnt vmcnt(5)\n" \
  "global_store_dwordx4 v187, v[176:179], s[44:45] offset:0 sc1\n" \
  "s_add_u32 s98, s98, 1\n" \
  "v_cvt_pk_bf16_f32 v180, v168, v169\n" \
  "v_cvt_pk_bf16_f32 v181, v170, v171\n" \
  "v_cvt_pk_bf16_f32 v182, v172, v173\n" \
  "v_cvt_pk_bf16_f32 v183, v174, v175\n" \
  "s_waitcnt vmcnt(5)\n" \
  "global_store_dwordx4 v188, v[180:183], s[44:45] offset:0 sc1\n" \
  "s_add_u32 s44, s44, 128000\n" \
  "s_addc_u32 s45, s45, 0\n" \
  "s_bitcmp0_b32 4160, s98\n" \
  "s_cbranch_scc1 LT\n" \
  "s_cmp_eq_u32 s98, 6\n" \
  "s_cbranch_scc1 LT_m\n" \
  "s_lshr_b32 s35, s30, 2\n" \
  "s_lshl3_add_u32 s35, s29, s35\n" \
  "s_mul_i32 s35, s35, 1600000\n" \
  "s_lshl_b32 s36, s28, 12\n" \
  "s_add_u32 s35, s35, s36\n" \
  "s_add_u32 s40, s24, s35\n" \
  "s_addc_u32 s41, s25, 0\n" \
  "s_bitcmp1_b32 s30, 2\n" \
  "s_cbranch_scc1 LB_l_h\n" \
  "global_load_dwordx4 v[64:67], v238, s[40:41] offset:0 sc1\n" \
  "global_load_dwordx4 v[68:71], v238, s[40:41] offset:1024 sc1\n" \
  "global_load_dwordx4 v[72:75], v238, s[40:41] offset:2048 sc1\n" \
  "global_load_dwordx4 v[76:79], v238, s[40:41] offset:3072 sc1\n" \
  "s_add_u32 s40, s40, 16000\n" \
  "s_addc_u32 s41, s41, 0\n" \
  "global_load_dwordx4 v[80:83], v238, s[40:41] offset:0 sc1\n" \
  "global_load_dwordx4 v[84:87], v238, s[40:41] offset:1024 sc1\n" \
  "global_load_dwordx4 v[88:91], v238, s[40:41] offset:2048 sc1\n" \
  "global_load_dwordx4 v[92:95], v238, s[40:41] offset:3072 sc1\n" \
  "s_add_u32 s40, s40, 16000\n" \
  "s_addc_u32 s41, s41, 0\n" \
  "global_load_dwordx4 v[96:99], v238, s[40:41] offset:0 sc1\n" \
  "global_load_dwordx4 v[100:103], v238, s[40:41] offset:1024 sc1\n" \
  "global_load_dwordx4 v[104:107], v238, s[40:41] offset:2048 sc1\n" \
  "global_load_dwordx4 v[108:111], v238, s[40:41] offset:3072 sc1\n" \
  "s_add_u32 s40, s40, 16000\n" \
  "s_addc_u32 s41, s41, 0\n" \
  "global_load_dwordx4 v[112:115], v238, s[40:41] offset:0 sc1\n" \
  "global_load_dwordx4 v[116:119], v238, s[40:41] offset:1024 sc1\n" \
  "global_load_dwordx4 v[120:123], v238, s[40:41] offset:2048 sc1\n" \
  "global_load_dwordx4 v[124:127], v238, s[40:41] offset:3072 sc1\n" \
  "s_add_u32 s40, s40, 16000\n" \
  "s_addc_u32 s41, s41, 0\n" \
  "s_branch LB_l_e\n" \
  "LB_l_h:\n" \
  "global_load_dwordx4 v[0:3], v238, s[40:41] offset:0 sc1\n" \
  "global_load_dwordx4 v[4:7], v238, s[40:41] offset:1024 sc1\n" \
  "global_load_dwordx4 v[8:11], v238, s[40:41] offset:2048 sc1\n" \
  "global_load_dwordx4 v[12:15], v238, s[40:41] offset:3072 sc1\n" \
  "s_add_u32 s40, s40, 16000\n" \
  "s_addc_u32 s41, s41, 0\n" \
  "global_load_dwordx4 v[16:19], v238, s[40:41] offset:0 sc1\n" \
  "global_load_dwordx4 v[20:23], v238, s[40:41] offset:1024 sc1\n" \
  "global_load_dwordx4 v[24:27], v238, s[40:41] offset:2048 sc1\n" \
  "global_load_dwordx4 v[28:31], v238, s[40:41] offset:3072 sc1\n" \
  "s_add_u32 s40, s40, 16000\n" \
  "s_addc_u32 s41, s41, 0\n" \
  "global_load_dwordx4 v[32:35], v238, s[40:41] offset:0 sc1\n" \
  "global_load_dwordx4 v[36:39], v238, s[40:41] offset:1024 sc1\n" \
  "global_load_dwordx4 v[40:43], v238, s[40:41] offset:2048 sc1\n" \
  "global_load_dwordx4 v[44:47], v238, s[40:41] offset:3072 sc1\n" \
  "s_add_u32 s40, s40, 16000\n" \
  "s_addc_u32 s41, s41, 0\n" \
  "global_load_dwordx4 v[48:51], v238, s[40:41] offset:0 sc1\n" \
  "global_load_dwordx4 v[52:55], v238, s[40:41] offset:1024 sc1\n" \
  "global_load_dwordx4 v[56:59], v238, s[40:41] offset:2048 sc1\n" \
  "global_load_dwordx4 v[60:63], v238, s[40:41] offset:3072 sc1\n" \
  "s_add_u32 s40, s40, 16000\n" \
  "s_addc_u32 s41, s41, 0\n" \
  "LB_l_e:\n" \
  "ds_write_b16_d16_hi v195, v197 offset:0\n" \
  "ds_write_b16_d16_hi v195, v199 offset:16\n" \
  "ds_write_b16_d16_hi v195, v201 offset:32\n" \
  "ds_write_b16_d16_hi v235, v203 offset:64\n" \
  "ds_write_b16_d16_hi v235, v205 offset:80\n" \
  "ds_write_b16_d16_hi v235, v207 offset:96\n" \
  "ds_write_b16_d16_hi v235, v209 offset:112\n" \
  "ds_write_b16_d16_hi v235, v211 offset:128\n" \
  "ds_write_b16_d16_hi v235, v213 offset:144\n" \
  "ds_write_b16_d16_hi v235, v215 offset:160\n" \
  "ds_write_b16_d16_hi v235, v217 offset:176\n" \
  "ds_write_b16_d16_hi v195, v219 offset:48\n" \
  "ds_write_b16_d16_hi v195, v221 offset:192\n" \
  "ds_write_b16_d16_hi v195, v223 offset:208\n" \
  "ds_write_b16_d16_hi v195, v225 offset:224\n" \
  "ds_write_b16_d16_hi v195, v227 offset:240\n" \
  "ds_read_b128 v[244:247], v254 offset:0\n" \
  "ds_read_b128 v[248:251], v254 offset:1088\n" \
  "s_lshr_b32 s35, s7, 5\n" \
  "s_mul_i32 s35, s35, 819200\n" \
  "s_and_b32 s36, s7, 31\n" \
  "s_lshl_b32 s36, s36, 10\n" \
  "s_add_u32 s35, s35, s36\n" \
  "s_lshl_b32 s36, s4, 9\n" \
  "s_add_u32 s35, s35, s36\n" \
  "s_add_u32 s35, s35, 102793216\n" \
  "s_add_u32 s58, s24, s35\n" \
  "s_addc_u32 s59, s25, 0\n" \
  "s_waitcnt lgkmcnt(0)\n" \
  "s_cmp_ge_u32 s4, 896\n" \
  "s_cbranch_scc1 L17_\n" \
  "global_store_dwordx4 v238, v[244:247], s[58:59] sc1\n" \
  "s_cmp_ge_u32 s4, 768\n" \
  "s_cbranch_scc1 L17_\n" \
  "s_add_u32 s58, s58, 32768\n" \
  "s_addc_u32 s59, s59, 0\n" \
  "global_store_dwordx4 v238, v[248:251], s[58:59] sc1\n" \
  "L17_:\n" \
  "s_lshr_b32 s35, s30, 2\n" \
  "s_lshl3_add_u32 s33, s29, s35\n" \
  "s_and_b32 s34, s30, 3\n" \
  "s_lshl_b32 s59, s27, 11\n" \
  "s_mul_i32 s35, s33, 1600000\n" \
  "s_add_u32 s54, s24, s35\n" \
  "s_addc_u32 s55, s25, 0\n" \
  "s_mul_i32 s35, s34, 819200\n" \
  "s_lshl1_add_u32 s35, s59, s35\n" \
  "s_add_u32 s35, s35, 102400000\n" \
  "s_add_u32 s56, s24, s35\n" \
  "s_addc_u32 s57, s25, 0\n" \
  "s_lshr_b32 s35, s30, 4\n" \
  "s_lshl1_add_u32 s35, s29, s35\n" \
  "s_lshl_b32 s35, s35, 6\n" \
  "s_bfe_u32 s37, s30, 0x20002\n" \
  "s_lshl3_add_u32 s35, s37, s35\n" \
  "v_and_b32 v202, 16, v255\n" \
  "v_lshl_add_u32 v202, v202, 1, s35\n" \
  "global_load_dwordx2 v[204:205], v202, s[16:17]\n" \
  "global_load_dwordx2 v[206:207], v202, s[18:19]\n" \
  "v_mov_b32 v202, v238\n" \
  "global_load_dwordx4 v[208:211], v238, s[56:57] offset:0 sc1\n" \
  "global_load_dwordx4 v[212:215], v238, s[56:57] offset:1024 sc1\n" \
  "global_load_dwordx4 v[216:219], v238, s[56:57] offset:2048 sc1\n" \
  "global_load_dwordx4 v[220:223], v238, s[56:57] offset:3072 sc1\n" \
  "s_add_u32 s56, s56, 32768\n" \
  "s_addc_u32 s57, s57, 0\n" \
  "global_load_dwordx4 v[224:227], v238, s[56:57] sc1\n" \
  "global_load_dwordx4 v[228:231], v238, s[56:57] offset:1024 sc1\n" \
  "global_load_dwordx4 v[232:235], v238, s[56:57] offset:2048 sc1\n" \
  "global_load_dwordx4 v[236:239], v238, s[56:57] offset:3072 sc1\n" \
  "s_add_u32 s56, s56, 32768\n" \
  "s_addc_u32 s57, s57, 0\n" \
  "s_bitcmp1_b32 s30, 2\n" \
  "s_cbranch_scc1 LB_t_h\n" \
  "s_waitcnt lgkmcnt(12)\n" \
  "v_mfma_f32_16x16x32_bf16 v[160:163], v[128:131], v[0:3], 0\n" \
  "v_mfma_f32_16x16x32_bf16 v[160:163], v[132:135], v[4:7], v[160:163]\n" \
  "s_waitcnt lgkmcnt(8)\n" \
  "v_mfma_f32_16x16x32_bf16 v[160:163], v[136:139], v[8:11], v[160:163]\n" \
  "v_mfma_f32_16x16x32_bf16 v[160:163], v[140:143], v[12:15], v[160:163]\n" \
  "s_waitcnt lgkmcnt(0)\n" \
  "v_mfma_f32_16x16x32_bf16 v[164:167], v[156:159], v[12:15], 0\n" \
  "v_mfma_f32_16x16x32_bf16 v[164:167], v[152:155], v[8:11], v[164:167]\n" \
  "v_mfma_f32_16x16x32_bf16 v[164:167], v[148:151], v[4:7], v[164:167]\n" \
  "v_mfma_f32_16x16x32_bf16 v[164:167], v[144:147], v[0:3], v[164:167]\n" \
  "v_mfma_f32_16x16x32_bf16 v[172:175], v[144:147], v[16:19], 0\n" \
  "v_mfma_f32_16x16x32_bf16 v[172:175], v[148:151], v[20:23], v[172:175]\n" \
  "v_mfma_f32_16x16x32_bf16 v[172:175], v[152:155], v[24:27], v[172:175]\n" \
  "v_cvt_pk_bf16_f32 v176, v160, v161\n" \
  "v_cvt_pk_bf16_f32 v177, v162, v163\n" \
  "v_mfma_f32_16x16x32_bf16 v[172:175], v[156:159], v[28:31], v[172:175]\n" \
  "v_mfma_f32_16x16x32_bf16 v[168:171], v[140:143], v[28:31], 0\n" \
  "v_mfma_f32_16x16x32_bf16 v[168:171], v[136:139], v[24:27], v[168:171]\n" \
  "v_cvt_pk_bf16_f32 v178, v164, v165\n" \
  "v_cvt_pk_bf16_f32 v179, v166, v167\n" \
  "v_mfma_f32_16x16x32_bf16 v[168:171], v[132:135], v[20:23], v[168:171]\n" \
  "v_mfma_f32_16x16x32_bf16 v[168:171], v[128:131], v[16:19], v[168:171]\n" \
  "s_waitcnt vmcnt(63)\n" \
  "global_store_dwordx4 v186, v[176:179], s[46:47] offset:0 sc1\n" \
  "v_mfma_f32_16x16x32_bf16 v[160:163], v[128:131], v[32:35], 0\n" \
  "v_cvt_pk_bf16_f32 v182, v172, v173\n" \
  "v_cvt_pk_bf16_f32 v183, v174, v175\n" \
  "v_mfma_f32_16x16x32_bf16 v[160:163], v[132:135], v[36:39], v[160:163]\n" \
  "v_mfma_f32_16x16x32_bf16 v[160:163], v[136:139], v[40:43], v[160:163]\n" \
  "v_mfma_f32_16x16x32_bf16 v[160:163], v[140:143], v[44:47], v[160:163]\n" \
  "v_cvt_pk_bf16_f32 v180, v168, v169\n" \
  "v_cvt_pk_bf16_f32 v181, v170, v171\n" \
  "v_mfma_f32_16x16x32_bf16 v[164:167], v[156:159], v[44:47], 0\n" \
  "v_mfma_f32_16x16x32_bf16 v[164:167], v[152:155], v[40:43], v[164:167]\n" \
  "v_mfma_f32_16x16x32_bf16 v[164:167], v[148:151], v[36:39], v[164:167]\n" \
  "v_mfma_f32_16x16x32_bf16 v[164:167], v[144:147], v[32:35], v[164:167]\n" \
  "s_waitcnt vmcnt(63)\n" \
  "global_store_dwordx4 v186, v[180:183], s[46:47] offset:256 sc1\n" \
  "v_mfma_f32_16x16x32_bf16 v[172:175], v[144:147], v[48:51], 0\n" \
  "v_cvt_pk_bf16_f32 v176, v160, v161\n" \
  "v_cvt_pk_bf16_f32 v177, v162, v163\n" \
  "v_mfma_f32_16x16x32_bf16 v[172:175], v[148:151], v[52:55], v[172:175]\n" \
  "v_mfma_f32_16x16x32_bf16 v[172:175], v[152:155], v[56:59], v[172:175]\n" \
  "v_mfma_f32_16x16x32_bf16 v[172:175], v[156:159], v[60:63], v[172:175]\n" \
  "v_cvt_pk_bf16_f32 v178, v164, v165\n" \
  "v_cvt_pk_bf16_f32 v179, v166, v167\n" \
  "v_mfma_f32_16x16x32_bf16 v[168:171], v[140:143], v[60:63], 0\n" \
  "v_mfma_f32_16x16x32_bf16 v[168:171], v[136:139], v[56:59], v[168:171]\n" \
  "v_mfma_f32_16x16x32_bf16 v[168:171], v[132:135], v[52:55], v[168:171]\n" \
  "v_mfma_f32_16x16x32_bf16 v[168:171], v[128:131], v[48:51], v[168:171]\n" \
  "s_waitcnt vmcnt(63)\n" \
  "global_store_dwordx4 v186, v[176:179], s[46:47] offset:512 sc1\n" \
  "v_cvt_pk_bf16_f32 v182, v172, v173\n" \
  "v_cvt_pk_bf16_f32 v183, v174, v175\n" \
  "s_nop 3\n" \
  "v_cvt_pk_bf16_f32 v180, v168, v169\n" \
  "v_cvt_pk_bf16_f32 v181, v170, v171\n" \
  "s_waitcnt vmcnt(63)\n" \
  "global_store_dwordx4 v186, v[180:183], s[46:47] offset:768 sc1\n" \
  "s_branch LB_t_e\n" \
  "LT_w:\n" \
  "L8_:\n" \
  "s_sleep 1\n" \
  "ds_read_b32 v192, v190 offset:0\n" \
  "s_waitcnt lgkmcnt(0)\n" \
  "v_readfirstlane_b32 s35, v192\n" \
  "s_cmp_lt_u32 s35, s99\n" \
  "s_cbranch_scc1 L8_\n" \
  "s_branch LT_c\n" \
  "LT_e:\n" \
  "s_bitcmp1_b32 10, s98\n" \
  "s_cbranch_scc0 LT_r\n" \
  "global_load_dwordx4 v[240:243], v238, s[48:49] offset:0 sc1\n" \
  "s_waitcnt vmcnt(0)\n" \
  "global_store_dwordx4 v238, v[240:243], s[48:49] offset:1024 sc1\n" \
  "s_branch LT_r\n" \
  "LT_a:\n" \
  "s_cmp_lg_u32 s35, 4\n" \
  "s_cbranch_scc1 L9_\n" \
  "global_load_dword v193, v193, s[48:49] offset:1024 sc1\n" \
  "s_branch LT_s\n" \
  "L9_:\n" \
  "s_cmp_lg_u32 s35, 5\n" \
  "s_cbranch_scc1 L10_\n" \
  "s_memtime s[58:59]\n" \
  "s_memrealtime s[40:41]\n" \
  "s_mov_b32 s5, 0x10000\n" \
  "LB_ec:\n" \
  "v_readfirstlane_b32 s35, v193\n" \
  "s_cmp_eq_u32 s35, s39\n" \
  "s_cbranch_scc1 LB_ok\n" \
  "s_lshl_b32 s35, s26, 2\n" \
  "v_mov_b32 v193, s35\n" \
  "s_sleep 8\n" \
  "global_load_dword v193, v193, s[48:49] offset:1024 sc1\n" \
  "s_waitcnt vmcnt(0)\n" \
  "s_sub_u32 s5, s5, 1\n" \
  "s_cbranch_scc0 LB_ec\n" \
  "LB_ok:\n" \
  "s_lshl_b32 s35, s26, 2\n" \
  "v_mov_b32 v244, s35\n" \
  "s_waitcnt lgkmcnt(0)\n" \
  "s_sub_u32 s88, s58, s88\n" \
  "s_sub_u32 s88, 106000, s88\n" \
  "s_sub_u32 s90, s58, s90\n" \
  "s_sub_u32 s42, s40, s42\n" \
  "s_lshl_b32 s40, s40, 4\n" \
  "v_cvt_f32_u32 v245, s90\n" \
  "v_cvt_f32_u32 v246, s42\n" \
  "v_rcp_f32 v245, v245\n" \
  "v_cvt_f32_i32 v247, s88\n" \
  "v_mul_f32 v246, 0x4111eb85, v246\n" \
  "v_mul_f32 v245, v245, v246\n" \
  "v_mul_f32 v245, v245, v247\n" \
  "v_cvt_i32_f32 v245, v245\n" \
  "v_add_u32 v245, s40, v245\n" \
  "v_or_b32 v245, 1, v245\n" \
  "s_mov_b64 exec, 1\n" \
  "global_store_dword v244, v245, s[48:49] offset:2048 sc1\n" \
  "s_mov_b64 exec, -1\n" \
  "s_branch LT_s\n" \
  "L10_:\n" \
  "s_cmp_lg_u32 s35, 7\n" \
  "s_cbranch_scc1 L11_\n" \
  "s_mov_b32 exec_hi, 0\n" \
  "global_atomic_add v196, v191, s[48:49] offset:3072 sc1\n" \
  "s_mov_b64 exec, -1\n" \
  "s_branch LT_s\n" \
  "L11_:\n" \
  "s_cmp_lg_u32 s35, 23\n" \
  "s_cbranch_scc1 L12_\n" \
  "s_lshl_b32 s35, s30, 5\n" \
  "v_lshrrev_b32 v206, 3, v255\n" \
  "v_and_b32 v208, 7, v255\n" \
  "v_lshl_add_u32 v210, v206, 2, s35\n" \
  "v_lshl_add_u32 v212, v208, 2, s35\n" \
  "global_load_dword v202, v210, s[48:49] offset:2048 sc1\n" \
  "global_load_dword v204, v212, s[48:49] offset:2048 sc1\n" \
  "s_branch LT_s\n" \
  "L12_:\n" \
  "s_cmp_lg_u32 s35, 24\n" \
  "s_cbranch_scc1 L13_\n" \
  "s_mov_b32 s5, 0x10000\n" \
  "LB_z:\n" \
  "v_min_u32 v214, v202, v204\n" \
  "v_cmp_eq_u32 s[58:59], 0, v214\n" \
  "s_sub_u32 s5, s5, 1\n" \
  "s_cbranch_scc1 LB_zx\n" \
  "s_cmp_eq_u64 s[58:59], 0\n" \
  "s_cbranch_scc1 LB_zx\n" \
  "s_cmp_lg_u32 s33, 1021\n" \
  "s_cbranch_scc1 LB_n1\n" \
  "global_load_dwordx4 v[240:243], v238, s[48:49] offset:0 sc1\n" \
  "s_waitcnt vmcnt(0)\n" \
  "global_store_dwordx4 v238, v[240:243], s[48:49] offset:1024 sc1\n" \
  "LB_n1:\n" \
  "s_sleep 4\n" \
  "global_load_dword v202, v210, s[48:49] offset:2048 sc1\n" \
  "global_load_dword v204, v212, s[48:49] offset:2048 sc1\n" \
  "s_waitcnt vmcnt(0)\n" \
  "s_branch LB_z\n" \
  "LB_zx:\n" \
  "v_and_or_b32 v202, v202, -8, v206\n" \
  "v_and_or_b32 v204, v204, -8, v208\n" \
  "v_cmp_lt_u32 s[58:59], v204, v202\n" \
  "v_sub_u32 v202, v204, v202\n" \
  "s_lshl_b32 s35, s29, 3\n" \
  "s_lshr_b64 s[40:41], s[58:59], s35\n" \
  "s_and_b32 s40, s40, 0xff\n" \
  "s_bcnt1_i32_b32 s40, s40\n" \
  "s_sub_u32 s40, 7, s40\n" \
  "s_mov_b32 s36, s29\n" \
  "s_bfe_u32 s41, s58, 0x80000\n" \
  "s_bcnt1_i32_b32 s41, s41\n" \
  "s_cmp_eq_u32 s41, s40\n" \
  "s_cselect_b32 s36, 0, s36\n" \
  "s_bfe_u32 s41, s58, 0x80008\n" \
  "s_bcnt1_i32_b32 s41, s41\n" \
  "s_cmp_eq_u32 s41, s40\n" \
  "s_cselect_b32 s36, 1, s36\n" \
  "s_bfe_u32 s41, s58, 0x80010\n" \
  "s_bcnt1_i32_b32 s41, s41\n" \
  "s_cmp_eq_u32 s41, s40\n" \
  "s_cselect_b32 s36, 2, s36\n" \
  "s_bfe_u32 s41, s58, 0x80018\n" \
  "s_bcnt1_i32_b32 s41, s41\n" \
  "s_cmp_eq_u32 s41, s40\n" \
  "s_cselect_b32 s36, 3, s36\n" \
  "s_bfe_u32 s41, s59, 0x80000\n" \
  "s_bcnt1_i32_b32 s41, s41\n" \
  "s_cmp_eq_u32 s41, s40\n" \
  "s_cselect_b32 s36, 4, s36\n" \
  "s_bfe_u32 s41, s59, 0x80008\n" \
  "s_bcnt1_i32_b32 s41, s41\n" \
  "s_cmp_eq_u32 s41, s40\n" \
  "s_cselect_b32 s36, 5, s36\n" \
  "s_bfe_u32 s41, s59, 0x80010\n" \
  "s_bcnt1_i32_b32 s41, s41\n" \
  "s_cmp_eq_u32 s41, s40\n" \
  "s_cselect_b32 s36, 6, s36\n" \
  "s_bfe_u32 s41, s59, 0x80018\n" \
  "s_bcnt1_i32_b32 s41, s41\n" \
  "s_cmp_eq_u32 s41, s40\n" \
  "s_cselect_b32 s36, 7, s36\n" \
  "s_add_u32 s35, s35, s36\n" \
  "v_readlane_b32 s41, v202, s35\n" \
  "s_sub_u32 s35, 0, s41\n" \
  "s_cmp_lt_u32 s40, 4\n" \
  "s_cselect_b32 s41, s35, s41\n" \
  "s_min_u32 s41, s41, 0xffff\n" \
  "s_mul_i32 s41, s41, 39\n" \
  "s_add_u32 s41, s41, 28672\n" \
  "s_lshr_b32 s41, s41, 16\n" \
  "s_min_u32 s41, s41, 4\n" \
  "s_sub_u32 s35, 0, s41\n" \
  "s_cmp_lt_u32 s40, 4\n" \
  "s_cselect_b32 s35, s35, s41\n" \
  "s_lshl4_add_u32 s35, s36, s35\n" \
  "s_add_u32 s35, s35, 132\n" \
  "v_mov_b32 v204, s35\n" \
  "ds_write_b32 v190, v204 offset:4\n" \
  "s_branch LT_s\n" \
  "L13_:\n" \
  "s_cmp_lg_u32 s35, 25\n" \
  "s_cbranch_scc1 L14_\n" \
  "s_mov_b64 exec, 0xff\n" \
  "global_load_dword v200, v198, s[48:49] offset:3072 sc1\n" \
  "s_mov_b64 exec, -1\n" \
  "s_branch LT_s\n" \
  "L14_:\n" \
  "s_cmp_lg_u32 s35, 26\n" \
  "s_cbranch_scc1 L15_\n" \
  "s_mov_b64 s[58:59], vcc\n" \
  "s_mov_b32 s5, 0x10000\n" \
  "LB_p:\n" \
  "s_mov_b64 exec, 0xff\n" \
  "v_cmp_le_u32 vcc, 32, v200\n" \
  "s_mov_b64 exec, -1\n" \
  "s_sub_u32 s5, s5, 1\n" \
  "s_cselect_b32 s35, 0xff, vcc_lo\n" \
  "s_cmp_eq_u32 s35, 0xff\n" \
  "s_cbranch_scc1 LB_p_x\n" \
  "s_cmp_lg_u32 s33, 1021\n" \
  "s_cbranch_scc1 LB_n2\n" \
  "global_load_dwordx4 v[240:243], v238, s[48:49] offset:0 sc1\n" \
  "s_waitcnt vmcnt(0)\n" \
  "global_store_dwordx4 v238, v[240:243], s[48:49] offset:1024 sc1\n" \
  "LB_n2:\n" \
  "s_sleep 2\n" \
  "s_mov_b64 exec, 0xff\n" \
  "global_load_dword v200, v198, s[48:49] offset:3072 sc1\n" \
  "s_mov_b64 exec, -1\n" \
  "s_waitcnt vmcnt(0)\n" \
  "s_branch LB_p\n" \
  "LB_p_x:\n" \
  "s_mov_b64 vcc, s[58:59]\n" \
  "s_branch LT_s\n" \
  "L15_:\n" \
  "s_branch LT_s\n" \
  "LT_m:\n" \
  "ds_write_b16_d16_hi v195, v196 offset:0\n" \
  "ds_write_b16_d16_hi v195, v198 offset:16\n" \
  "ds_write_b16_d16_hi v195, v200 offset:32\n" \
  "ds_write_b16_d16_hi v235, v202 offset:64\n" \
  "ds_write_b16_d16_hi v235, v204 offset:80\n" \
  "ds_write_b16_d16_hi v235, v206 offset:96\n" \
  "ds_write_b16_d16_hi v235, v208 offset:112\n" \
  "ds_write_b16_d16_hi v235, v210 offset:128\n" \
  "ds_write_b16_d16_hi v235, v212 offset:144\n" \
  "ds_write_b16_d16_hi v235, v214 offset:160\n" \
  "ds_write_b16_d16_hi v235, v216 offset:176\n" \
  "ds_write_b16_d16_hi v195, v218 offset:48\n" \
  "ds_write_b16_d16_hi v195, v220 offset:192\n" \
  "ds_write_b16_d16_hi v195, v222 offset:208\n" \
  "ds_write_b16_d16_hi v195, v224 offset:224\n" \
  "ds_write_b16_d16_hi v195, v226 offset:240\n" \
  "ds_read_b128 v[244:247], v254 offset:0\n" \
  "ds_read_b128 v[248:251], v254 offset:1088\n" \
  "s_lshr_b32 s35, s7, 5\n" \
  "s_mul_i32 s35, s35, 819200\n" \
  "s_and_b32 s36, s7, 31\n" \
  "s_lshl_b32 s36, s36, 10\n" \
  "s_add_u32 s35, s35, s36\n" \
  "s_lshl_b32 s36, s4, 9\n" \
  "s_add_u32 s35, s35, s36\n" \
  "s_add_u32 s35, s35, 102400000\n" \
  "s_add_u32 s58, s24, s35\n" \
  "s_addc_u32 s59, s25, 0\n" \
  "s_waitcnt lgkmcnt(0)\n" \
  "s_cmp_ge_u32 s4, 768\n" \
  "s_cbranch_scc1 L16_\n" \
  "global_store_dwordx4 v238, v[244:247], s[58:59] sc1\n" \
  "s_cmp_ge_u32 s4, 768\n" \
  "s_cbranch_scc1 L16_\n" \
  "s_add_u32 s58, s58, 32768\n" \
  "s_addc_u32 s59, s59, 0\n" \
  "global_store_dwordx4 v238, v[248:251], s[58:59] sc1\n" \
  "L16_:\n" \
  "v_mov_b32 v234, 0\n" \
  "v_mov_b32 v236, v237\n" \
  "s_mov_b32 s38, 1\n" \
  "s_lshl_b32 s35, s29, 8\n" \
  "v_lshl_add_u32 v196, v255, 11, s35\n" \
  "s_lshl_b32 s35, s30, 11\n" \
  "v_lshl_add_u32 v198, v255, 8, s35\n" \
  "s_branch LT\n" \
  "LB_t_h:\n" \
  "s_waitcnt lgkmcnt(12)\n" \
  "v_mfma_f32_16x16x32_bf16 v[160:163], v[128:131], v[64:67], 0\n" \
  "v_mfma_f32_16x16x32_bf16 v[160:163], v[132:135], v[68:71], v[160:163]\n" \
  "s_waitcnt lgkmcnt(8)\n" \
  "v_mfma_f32_16x16x32_bf16 v[160:163], v[136:139], v[72:75], v[160:163]\n" \
  "v_mfma_f32_16x16x32_bf16 v[160:163], v[140:143], v[76:79], v[160:163]\n" \
  "s_waitcnt lgkmcnt(0)\n" \
  "v_mfma_f32_16x16x32_bf16 v[164:167], v[156:159], v[76:79], 0\n" \
  "v_mfma_f32_16x16x32_bf16 v[164:167], v[152:155], v[72:75], v[164:167]\n" \
  "v_mfma_f32_16x16x32_bf16 v[164:167], v[148:151], v[68:71], v[164:167]\n" \
  "v_mfma_f32_16x16x32_bf16 v[164:167], v[144:147], v[64:67], v[164:167]\n" \
  "v_mfma_f32_16x16x32_bf16 v[172:175], v[144:147], v[80:83], 0\n" \
  "v_mfma_f32_16x16x32_bf16 v[172:175], v[148:151], v[84:87], v[172:175]\n" \
  "v_mfma_f32_16x16x32_bf16 v[172:175], v[152:155], v[88:91], v[172:175]\n" \
  "v_cvt_pk_bf16_f32 v176, v160, v161\n" \
  "v_cvt_pk_bf16_f32 v177, v162, v163\n" \
  "v_mfma_f32_16x16x32_bf16 v[172:175], v[156:159], v[92:95], v[172:175]\n" \
  "v_mfma_f32_16x16x32_bf16 v[168:171], v[140:143], v[92:95], 0\n" \
  "v_mfma_f32_16x16x32_bf16 v[168:171], v[136:139], v[88:91], v[168:171]\n" \
  "v_cvt_pk_bf16_f32 v178, v164, v165\n" \
  "v_cvt_pk_bf16_f32 v179, v166, v167\n" \
  "v_mfma_f32_16x16x32_bf16 v[168:171], v[132:135], v[84:87], v[168:171]\n" \
  "v_mfma_f32_16x16x32_bf16 v[168:171], v[128:131], v[80:83], v[168:171]\n" \
  "s_waitcnt vmcnt(63)\n" \
  "global_store_dwordx4 v186, v[176:179], s[46:47] offset:1024 sc1\n" \
  "v_mfma_f32_16x16x32_bf16 v[160:163], v[128:131], v[96:99], 0\n" \
  "v_cvt_pk_bf16_f32 v182, v172, v173\n" \
  "v_cvt_pk_bf16_f32 v183, v174, v175\n" \
  "v_mfma_f32_16x16x32_bf16 v[160:163], v[132:135], v[100:103], v[160:163]\n" \
  "v_mfma_f32_16x16x32_bf16 v[160:163], v[136:139], v[104:107], v[160:163]\n" \
  "v_mfma_f32_16x16x32_bf16 v[160:163], v[140:143], v[108:111], v[160:163]\n" \
  "v_cvt_pk_bf16_f32 v180, v168, v169\n" \
  "v_cvt_pk_bf16_f32 v181, v170, v171\n" \
  "v_mfma_f32_16x16x32_bf16 v[164:167], v[156:159], v[108:111], 0\n" \
  "v_mfma_f32_16x16x32_bf16 v[164:167], v[152:155], v[104:107], v[164:167]\n" \
  "v_mfma_f32_16x16x32_bf16 v[164:167], v[148:151], v[100:103], v[164:167]\n" \
  "v_mfma_f32_16x16x32_bf16 v[164:167], v[144:147], v[96:99], v[164:167]\n" \
  "s_waitcnt vmcnt(63)\n" \
  "global_store_dwordx4 v186, v[180:183], s[46:47] offset:1280 sc1\n" \
  "v_mfma_f32_16x16x32_bf16 v[168:171], v[128:131], v[112:115], 0\n" \
  "v_cvt_pk_bf16_f32 v176, v160, v161\n" \
  "v_cvt_pk_bf16_f32 v177, v162, v163\n" \
  "v_mfma_f32_16x16x32_bf16 v[168:171], v[132:135], v[116:119], v[168:171]\n" \
  "v_mfma_f32_16x16x32_bf16 v[168:171], v[136:139], v[120:123], v[168:171]\n" \
  "v_mfma_f32_16x16x32_bf16 v[168:171], v[140:143], v[124:127], v[168:171]\n" \
  "v_cvt_pk_bf16_f32 v178, v164, v165\n" \
  "v_cvt_pk_bf16_f32 v179, v166, v167\n" \
  "v_mfma_f32_16x16x32_bf16 v[172:175], v[156:159], v[124:127], 0\n" \
  "v_mfma_f32_16x16x32_bf16 v[172:175], v[152:155], v[120:123], v[172:175]\n" \
  "v_mfma_f32_16x16x32_bf16 v[172:175], v[148:151], v[116:119], v[172:175]\n" \
  "v_mfma_f32_16x16x32_bf16 v[172:175], v[144:147], v[112:115], v[172:175]\n" \
  "s_waitcnt vmcnt(63)\n" \
  "global_store_dwordx4 v187, v[176:179], s[46:47] offset:0 sc1\n" \
  "v_cvt_pk_bf16_f32 v180, v168, v169\n" \
  "v_cvt_pk_bf16_f32 v181, v170, v171\n" \
  "s_nop 3\n" \
  "v_cvt_pk_bf16_f32 v182, v172, v173\n" \
  "v_cvt_pk_bf16_f32 v183, v174, v175\n" \
  "s_waitcnt vmcnt(63)\n" \
  "global_store_dwordx4 v188, v[180:183], s[46:47] offset:0 sc1\n" \
  "LB_t_e:\n" \
  "s_setprio 2\n" \
  "v_mov_b64 v[240:241], -1\n" \
  "v_mov_b64 v[242:243], -1\n" \
  "s_lshl2_add_u32 s35, s33, s34\n" \
  "s_lshl2_add_u32 s35, s35, s28\n" \
  "s_lshl_b32 s35, s35, 12\n" \
  "s_add_u32 s35, s35, 105676800\n" \
  "s_add_u32 s40, s24, s35\n" \
  "s_addc_u32 s41, s25, 0\n" \
  "v_mbcnt_lo_u32_b32 v189, -1, 0\n" \
  "v_mbcnt_hi_u32_b32 v189, -1, v189\n" \
  "v_lshlrev_b32 v188, 4, v189\n" \
  "s_lshl_b32 s35, s28, 12\n" \
  "v_add_u32 v128, s35, v188\n" \
  "v_mov_b32 v199, 1\n" \
  "v_lshrrev_b32 v180, 1, v189\n" \
  "v_xor_b32 v180, v180, v189\n" \
  "v_and_b32 v180, 8, v180\n" \
  "v_bfe_u32 v181, v189, 2, 1\n" \
  "v_lshlrev_b32_e64 v181, v181, 5\n" \
  "v_lshlrev_b32 v180, v180, v181\n" \
  "v_bfe_u32 v181, v189, 2, 2\n" \
  "v_lshlrev_b32_e64 v181, v181, 1\n" \
  "v_cmp_gt_u32 vcc, 32, v189\n" \
  "v_mov_b32 v191, 0x3f80\n" \
  "v_add_u32 v190, 32000, v188\n" \
  "v_cndmask_b32 v192, v181, v180, vcc\n" \
  "v_bfe_i32 v180, v192, 0, 1\n" \
  "v_and_b32 v180, v180, v191\n" \
  "v_bfe_i32 v181, v192, 1, 1\n" \
  "v_and_b32 v181, v181, v191\n" \
  "v_bfe_i32 v182, v192, 2, 1\n" \
  "v_and_b32 v182, v182, v191\n" \
  "v_bfe_i32 v183, v192, 3, 1\n" \
  "v_and_b32 v183, v183, v191\n" \
  "v_and_b32 v192, 3, v189\n" \
  "v_mul_u32_u24 v192, 0x5555, v192\n" \
  "v_add_u32 v189, 16000, v188\n" \
  "v_add_u32 v191, 48000, v188\n" \
  "s_waitcnt vmcnt(4)\n" \
  "v_lshlrev_b32 v184, 16, v204\n" \
  "v_and_b32 v185, 0xffff0000, v204\n" \
  "v_lshlrev_b32 v186, 16, v205\n" \
  "v_and_b32 v187, 0xffff0000, v205\n" \
  "global_store_dwordx4 v202, v[240:243], s[40:41] offset:0 sc1\n" \
  "global_store_dwordx4 v202, v[240:243], s[40:41] offset:1024 sc1\n" \
  "global_store_dwordx4 v202, v[240:243], s[40:41] offset:2048 sc1\n" \
  "global_store_dwordx4 v202, v[240:243], s[40:41] offset:3072 sc1\n" \
  "s_setprio 0\n" \
  "s_barrier\n" \
  "s_bitcmp1_b32 s30, 2\n" \
  "s_cbranch_scc1 LB_w_h\n" \
  "ds_write_b128 v128, v[64:67] offset:0\n" \
  "ds_write_b128 v128, v[68:71] offset:1024\n" \
  "ds_write_b128 v128, v[72:75] offset:2048\n" \
  "ds_write_b128 v128, v[76:79] offset:3072\n" \
  "s_add_u32 s35, s59, 64000\n" \
  "s_add_u32 s54, s54, s35\n" \
  "s_addc_u32 s55, s55, 0\n" \
  "s_add_u32 s58, s59, 0x10000\n" \
  "s_mov_b32 m0, s58\n" \
  "s_add_u32 s35, s58, 0x8000\n" \
  "global_load_lds_dwordx4 v188, s[54:55] sc1\n" \
  "global_load_lds_dwordx4 v188, s[54:55] offset:1024 sc1\n" \
  "ds_write_b128 v128, v[80:83] offset:16384\n" \
  "ds_write_b128 v128, v[84:87] offset:17408\n" \
  "ds_write_b128 v128, v[88:91] offset:18432\n" \
  "ds_write_b128 v128, v[92:95] offset:19456\n" \
  "ds_write_b128 v128, v[96:99] offset:32768\n" \
  "ds_write_b128 v128, v[100:103] offset:33792\n" \
  "ds_write_b128 v128, v[104:107] offset:34816\n" \
  "ds_write_b128 v128, v[108:111] offset:35840\n" \
  "s_add_u32 m0, s58, 0x4000\n" \
  "s_cmp_lt_u32 s35, 0x28000\n" \
  "global_load_lds_dwordx4 v189, s[54:55] sc1\n" \
  "global_load_lds_dwordx4 v189, s[54:55] offset:1024 sc1\n" \
  "s_cselect_b32 s58, s35, s59\n" \
  "s_add_u32 s54, s54, 32000\n" \
  "s_addc_u32 s55, s55, 0\n" \
  "ds_write_b128 v128, v[112:115] offset:49152\n" \
  "ds_write_b128 v128, v[116:119] offset:50176\n" \
  "ds_write_b128 v128, v[120:123] offset:51200\n" \
  "ds_write_b128 v128, v[124:127] offset:52224\n" \
  "s_branch LB_w_e\n" \
  "LB_w_h:\n" \
  "ds_write_b128 v128, v[0:3] offset:0\n" \
  "ds_write_b128 v128, v[4:7] offset:1024\n" \
  "ds_write_b128 v128, v[8:11] offset:2048\n" \
  "ds_write_b128 v128, v[12:15] offset:3072\n" \
  "s_add_u32 s35, s59, 64000\n" \
  "s_add_u32 s54, s54, s35\n" \
  "s_addc_u32 s55, s55, 0\n" \
  "s_add_u32 s58, s59, 0x10000\n" \
  "s_mov_b32 m0, s58\n" \
  "s_add_u32 s35, s58, 0x8000\n" \
  "global_load_lds_dwordx4 v188, s[54:55] sc1\n" \
  "global_load_lds_dwordx4 v188, s[54:55] offset:1024 sc1\n" \
  "ds_write_b128 v128, v[16:19] offset:16384\n" \
  "ds_write_b128 v128, v[20:23] offset:17408\n" \
  "ds_write_b128 v128, v[24:27] offset:18432\n" \
  "ds_write_b128 v128, v[28:31] offset:19456\n" \
  "ds_write_b128 v128, v[32:35] offset:32768\n" \
  "ds_write_b128 v128, v[36:39] offset:33792\n" \
  "ds_write_b128 v128, v[40:43] offset:34816\n" \
  "ds_write_b128 v128, v[44:47] offset:35840\n" \
  "s_add_u32 m0, s58, 0x4000\n" \
  "s_cmp_lt_u32 s35, 0x28000\n" \
  "global_load_lds_dwordx4 v189, s[54:55] sc1\n" \
  "global_load_lds_dwordx4 v189, s[54:55] offset:1024 sc1\n" \
  "s_cselect_b32 s58, s35, s59\n" \
  "s_add_u32 s54, s54, 32000\n" \
  "s_addc_u32 s55, s55, 0\n" \
  "ds_write_b128 v128, v[48:51] offset:49152\n" \
  "ds_write_b128 v128, v[52:55] offset:50176\n" \
  "ds_write_b128 v128, v[56:59] offset:51200\n" \
  "ds_write_b128 v128, v[60:63] offset:52224\n" \
  "LB_w_e:\n" \
  "s_waitcnt lgkmcnt(0)\n" \
  "s_mov_b32 s51, 7\n" \
  "s_setprio 0\n" \
  "s_barrier\n" \
  "LG_start:\n" \
  "v_lshlrev_b32_sdwa v64, 4, v208 dst_sel:DWORD dst_unused:UNUSED_PAD src0_sel:DWORD src1_sel:WORD_0\n" \
  "ds_read_b128 v[68:71], v64\n" \
  "v_mov_b64 v[0:1], v[184:185]\n" \
  "v_lshlrev_b32_sdwa v65, 4, v208 dst_sel:DWORD dst_unused:UNUSED_PAD src0_sel:DWORD src1_sel:WORD_1\n" \
  "ds_read_b128 v[72:75], v65\n" \
  "v_mov_b64 v[2:3], v[186:187]\n" \
  "v_lshlrev_b32_sdwa v66, 4, v209 dst_sel:DWORD dst_unused:UNUSED_PAD src0_sel:DWORD src1_sel:WORD_0\n" \
  "ds_read_b128 v[76:79], v66\n" \
  "v_mov_b64 v[4:5], v[184:185]\n" \
  "v_lshlrev_b32_sdwa v67, 4, v209 dst_sel:DWORD dst_unused:UNUSED_PAD src0_sel:DWORD src1_sel:WORD_1\n" \
  "ds_read_b128 v[80:83], v67\n" \
  "v_mov_b64 v[6:7], v[186:187]\n" \
  "s_mov_b32 m0, s58\n" \
  "s_add_u32 s35, s58, 0x8000\n" \
  "global_load_lds_dwordx4 v188, s[54:55] sc1\n" \
  "global_load_lds_dwordx4 v188, s[54:55] offset:1024 sc1\n" \
  "v_lshlrev_b32_sdwa v64, 4, v210 dst_sel:DWORD dst_unused:UNUSED_PAD src0_sel:DWORD src1_sel:WORD_0\n" \
  "ds_read_b128 v[84:87], v64\n" \
  "v_mov_b64 v[8:9], v[184:185]\n" \
  "v_lshlrev_b32_sdwa v65, 4, v210 dst_sel:DWORD dst_unused:UNUSED_PAD src0_sel:DWORD src1_sel:WORD_1\n" \
  "ds_read_b128 v[88:91], v65\n" \
  "v_mov_b64 v[10:11], v[186:187]\n" \
  "v_lshlrev_b32_sdwa v66, 4, v211 dst_sel:DWORD dst_unused:UNUSED_PAD src0_sel:DWORD src1_sel:WORD_0\n" \
  "ds_read_b128 v[92:95], v66\n" \
  "v_mov_b64 v[12:13], v[184:185]\n" \
  "v_lshlrev_b32_sdwa v67, 4, v211 dst_sel:DWORD dst_unused:UNUSED_PAD src0_sel:DWORD src1_sel:WORD_1\n" \
  "ds_read_b128 v[96:99], v67\n" \
  "v_mov_b64 v[14:15], v[186:187]\n" \
  "s_add_u32 m0, s58, 0x4000\n" \
  "s_cmp_lt_u32 s35, 0x28000\n" \
  "global_load_lds_dwordx4 v189, s[54:55] sc1\n" \
  "global_load_lds_dwordx4 v189, s[54:55] offset:1024 sc1\n" \
  "s_cselect_b32 s58, s35, s59\n" \
  "v_lshlrev_b32_sdwa v64, 4, v212 dst_sel:DWORD dst_unused:UNUSED_PAD src0_sel:DWORD src1_sel:WORD_0\n" \
  "ds_read_b128 v[100:103], v64\n" \
  "v_mov_b64 v[16:17], v[184:185]\n" \
  "v_lshlrev_b32_sdwa v65, 4, v212 dst_sel:DWORD dst_unused:UNUSED_PAD src0_sel:DWORD src1_sel:WORD_1\n" \
  "ds_read_b128 v[104:107], v65\n" \
  "v_mov_b64 v[18:19], v[186:187]\n" \
  "v_lshlrev_b32_sdwa v66, 4, v213 dst_sel:DWORD dst_unused:UNUSED_PAD src0_sel:DWORD src1_sel:WORD_0\n" \
  "ds_read_b128 v[108:111], v66\n" \
  "v_mov_b64 v[20:21], v[184:185]\n" \
  "v_lshlrev_b32_sdwa v67, 4, v213 dst_sel:DWORD dst_unused:UNUSED_PAD src0_sel:DWORD src1_sel:WORD_1\n" \
  "ds_read_b128 v[112:115], v67\n" \
  "v_mov_b64 v[22:23], v[186:187]\n" \
  "s_waitcnt lgkmcnt(10)\n" \
  "v_smfmac_f32_16x16x64_bf16 v[0:3], v[180:183], v[68:75], v192\n" \
  "v_lshlrev_b32_sdwa v64, 4, v214 dst_sel:DWORD dst_unused:UNUSED_PAD src0_sel:DWORD src1_sel:WORD_0\n" \
  "ds_read_b128 v[68:71], v64\n" \
  "v_mov_b64 v[24:25], v[184:185]\n" \
  "v_lshlrev_b32_sdwa v65, 4, v214 dst_sel:DWORD dst_unused:UNUSED_PAD src0_sel:DWORD src1_sel:WORD_1\n" \
  "ds_read_b128 v[72:75], v65\n" \
  "v_mov_b64 v[26:27], v[186:187]\n" \
  "s_mov_b32 m0, s58\n" \
  "s_add_u32 s35, s58, 0x8000\n" \
  "global_load_lds_dwordx4 v190, s[54:55] sc1\n" \
  "global_load_lds_dwordx4 v190, s[54:55] offset:1024 sc1\n" \
  "s_waitcnt lgkmcnt(10)\n" \
  "v_smfmac_f32_16x16x64_bf16 v[4:7], v[180:183], v[76:83], v192\n" \
  "v_lshlrev_b32_sdwa v66, 4, v215 dst_sel:DWORD dst_unused:UNUSED_PAD src0_sel:DWORD src1_sel:WORD_0\n" \
  "ds_read_b128 v[76:79], v66\n" \
  "v_mov_b64 v[28:29], v[184:185]\n" \
  "v_lshlrev_b32_sdwa v67, 4, v215 dst_sel:DWORD dst_unused:UNUSED_PAD src0_sel:DWORD src1_sel:WORD_1\n" \
  "ds_read_b128 v[80:83], v67\n" \
  "v_mov_b64 v[30:31], v[186:187]\n" \
  "s_waitcnt lgkmcnt(10)\n" \
  "v_smfmac_f32_16x16x64_bf16 v[8:11], v[180:183], v[84:91], v192\n" \
  "v_lshlrev_b32_sdwa v64, 4, v216 dst_sel:DWORD dst_unused:UNUSED_PAD src0_sel:DWORD src1_sel:WORD_0\n" \
  "ds_read_b128 v[84:87], v64\n" \
  "v_mov_b64 v[32:33], v[184:185]\n" \
  "v_lshlrev_b32_sdwa v65, 4, v216 dst_sel:DWORD dst_unused:UNUSED_PAD src0_sel:DWORD src1_sel:WORD_1\n" \
  "ds_read_b128 v[88:91], v65\n" \
  "v_mov_b64 v[34:35], v[186:187]\n" \
  "s_add_u32 m0, s58, 0x4000\n" \
  "s_cmp_lt_u32 s35, 0x28000\n" \
  "global_load_lds_dwordx4 v191, s[54:55] sc1\n" \
  "global_load_lds_dwordx4 v191, s[54:55] offset:1024 sc1\n" \
  "s_cselect_b32 s58, s35, s59\n" \
  "s_waitcnt lgkmcnt(10)\n" \
  "v_smfmac_f32_16x16x64_bf16 v[12:15], v[180:183], v[92:99], v192\n" \
  "v_lshlrev_b32_sdwa v66, 4, v217 dst_sel:DWORD dst_unused:UNUSED_PAD src0_sel:DWORD src1_sel:WORD_0\n" \
  "ds_read_b128 v[92:95], v66\n" \
  "v_mov_b64 v[36:37], v[184:185]\n" \
  "v_lshlrev_b32_sdwa v67, 4, v217 dst_sel:DWORD dst_unused:UNUSED_PAD src0_sel:DWORD src1_sel:WORD_1\n" \
  "ds_read_b128 v[96:99], v67\n" \
  "v_mov_b64 v[38:39], v[186:187]\n" \
  "s_waitcnt lgkmcnt(10)\n" \
  "v_smfmac_f32_16x16x64_bf16 v[16:19], v[180:183], v[100:107], v192\n" \
  "v_lshlrev_b32_sdwa v64, 4, v218 dst_sel:DWORD dst_unused:UNUSED_PAD src0_sel:DWORD src1_sel:WORD_0\n" \
  "ds_read_b128 v[100:103], v64\n" \
  "v_mov_b64 v[40:41], v[184:185]\n" \
  "v_lshlrev_b32_sdwa v65, 4, v218 dst_sel:DWORD dst_unused:UNUSED_PAD src0_sel:DWORD src1_sel:WORD_1\n" \
  "ds_read_b128 v[104:107], v65\n" \
  "v_mov_b64 v[42:43], v[186:187]\n" \
  "global_load_dwordx4 v[240:243], v188, s[56:57] sc1\n" \
  "global_load_dwordx4 v[244:247], v188, s[56:57] offset:1024 sc1\n" \
  "s_waitcnt lgkmcnt(10)\n" \
  "v_smfmac_f32_16x16x64_bf16 v[20:23], v[180:183], v[108:115], v192\n" \
  "v_lshlrev_b32_sdwa v66, 4, v219 dst_sel:DWORD dst_unused:UNUSED_PAD src0_sel:DWORD src1_sel:WORD_0\n" \
  "ds_read_b128 v[108:111], v66\n" \
  "v_mov_b64 v[44:45], v[184:185]\n" \
  "v_lshlrev_b32_sdwa v67, 4, v219 dst_sel:DWORD dst_unused:UNUSED_PAD src0_sel:DWORD src1_sel:WORD_1\n" \
  "ds_read_b128 v[112:115], v67\n" \
  "v_mov_b64 v[46:47], v[186:187]\n" \
  "s_waitcnt lgkmcnt(10)\n" \
  "v_smfmac_f32_16x16x64_bf16 v[24:27], v[180:183], v[68:75], v192\n" \
  "v_lshlrev_b32_sdwa v64, 4, v220 dst_sel:DWORD dst_unused:UNUSED_PAD src0_sel:DWORD src1_sel:WORD_0\n" \
  "ds_read_b128 v[68:71], v64\n" \
  "v_mov_b64 v[48:49], v[184:185]\n" \
  "v_lshlrev_b32_sdwa v65, 4, v220 dst_sel:DWORD dst_unused:UNUSED_PAD src0_sel:DWORD src1_sel:WORD_1\n" \
  "ds_read_b128 v[72:75], v65\n" \
  "v_mov_b64 v[50:51], v[186:187]\n" \
  "global_load_dwordx4 v[248:251], v188, s[56:57] offset:2048 sc1\n" \
  "global_load_dwordx4 v[252:255], v188, s[56:57] offset:3072 sc1\n" \
  "s_waitcnt lgkmcnt(10)\n" \
  "v_smfmac_f32_16x16x64_bf16 v[28:31], v[180:183], v[76:83], v192\n" \
  "v_lshlrev_b32_sdwa v66, 4, v221 dst_sel:DWORD dst_unused:UNUSED_PAD src0_sel:DWORD src1_sel:WORD_0\n" \
  "ds_read_b128 v[76:79], v66\n" \
  "v_mov_b64 v[52:53], v[184:185]\n" \
  "v_lshlrev_b32_sdwa v67, 4, v221 dst_sel:DWORD dst_unused:UNUSED_PAD src0_sel:DWORD src1_sel:WORD_1\n" \
  "ds_read_b128 v[80:83], v67\n" \
  "v_mov_b64 v[54:55], v[186:187]\n" \
  "s_waitcnt lgkmcnt(10)\n" \
  "v_smfmac_f32_16x16x64_bf16 v[32:35], v[180:183], v[84:91], v192\n" \
  "v_lshlrev_b32_sdwa v64, 4, v222 dst_sel:DWORD dst_unused:UNUSED_PAD src0_sel:DWORD src1_sel:WORD_0\n" \
  "ds_read_b128 v[84:87], v64\n" \
  "v_mov_b64 v[56:57], v[184:185]\n" \
  "v_lshlrev_b32_sdwa v65, 4, v222 dst_sel:DWORD dst_unused:UNUSED_PAD src0_sel:DWORD src1_sel:WORD_1\n" \
  "ds_read_b128 v[88:91], v65\n" \
  "v_mov_b64 v[58:59], v[186:187]\n" \
  "s_waitcnt lgkmcnt(10)\n" \
  "v_smfmac_f32_16x16x64_bf16 v[36:39], v[180:183], v[92:99], v192\n" \
  "v_lshlrev_b32_sdwa v66, 4, v223 dst_sel:DWORD dst_unused:UNUSED_PAD src0_sel:DWORD src1_sel:WORD_0\n" \
  "ds_read_b128 v[92:95], v66\n" \
  "v_mov_b64 v[60:61], v[184:185]\n" \
  "v_lshlrev_b32_sdwa v67, 4, v223 dst_sel:DWORD dst_unused:UNUSED_PAD src0_sel:DWORD src1_sel:WORD_1\n" \
  "ds_read_b128 v[96:99], v67\n" \
  "v_mov_b64 v[62:63], v[186:187]\n" \
  "s_add_u32 s56, s56, 32768\n" \
  "s_addc_u32 s57, s57, 0\n" \
  "s_add_u32 s54, s54, 64000\n" \
  "s_addc_u32 s55, s55, 0\n" \
  "s_cmp_eq_u32 s27, 4\n" \
  "s_cselect_b32 s37, 0x90, 0\n" \
  ".p2align 6\n" \
  "LG_loop:\n" \
  "s_bitcmp1_b32 s37, s51\n" \
  "s_waitcnt lgkmcnt(10)\n" \
  "v_smfmac_f32_16x16x64_bf16 v[40:43], v[180:183], v[100:107], v192\n" \
  "s_waitcnt lgkmcnt(8)\n" \
  "v_smfmac_f32_16x16x64_bf16 v[44:47], v[180:183], v[108:115], v192\n" \
  "s_waitcnt vmcnt(8)\n" \
  "s_cbranch_scc1 LG_h\n" \
  "LG_ret:\n" \
  "s_barrier\n" \
  "LG_aft:\n" \
  "v_lshlrev_b32_sdwa v64, 4, v224 dst_sel:DWORD dst_unused:UNUSED_PAD src0_sel:DWORD src1_sel:WORD_0\n" \
  "ds_read_b128 v[100:103], v64\n" \
  "v_lshlrev_b32_sdwa v65, 4, v224 dst_sel:DWORD dst_unused:UNUSED_PAD src0_sel:DWORD src1_sel:WORD_1\n" \
  "ds_read_b128 v[104:107], v65\n" \
  "v_lshlrev_b32_sdwa v66, 4, v225 dst_sel:DWORD dst_unused:UNUSED_PAD src0_sel:DWORD src1_sel:WORD_0\n" \
  "ds_read_b128 v[108:111], v66\n" \
  "v_lshlrev_b32_sdwa v67, 4, v225 dst_sel:DWORD dst_unused:UNUSED_PAD src0_sel:DWORD src1_sel:WORD_1\n" \
  "ds_read_b128 v[112:115], v67\n" \
  "s_waitcnt lgkmcnt(10)\n" \
  "v_smfmac_f32_16x16x64_bf16 v[48:51], v[180:183], v[68:75], v192\n" \
  "s_waitcnt lgkmcnt(8)\n" \
  "v_smfmac_f32_16x16x64_bf16 v[52:55], v[180:183], v[76:83], v192\n" \
  "s_waitcnt lgkmcnt(6)\n" \
  "v_smfmac_f32_16x16x64_bf16 v[56:59], v[180:183], v[84:91], v192\n" \
  "s_waitcnt lgkmcnt(4)\n" \
  "v_smfmac_f32_16x16x64_bf16 v[60:63], v[180:183], v[92:99], v192\n" \
  "s_barrier\n" \
  "s_mov_b32 m0, s58\n" \
  "s_add_u32 s35, s58, 0x8000\n" \
  "global_load_lds_dwordx4 v188, s[54:55] sc1\n" \
  "global_load_lds_dwordx4 v188, s[54:55] offset:1024 sc1\n" \
  "v_lshlrev_b32_sdwa v64, 4, v226 dst_sel:DWORD dst_unused:UNUSED_PAD src0_sel:DWORD src1_sel:WORD_0\n" \
  "ds_read_b128 v[68:71], v64\n" \
  "v_lshlrev_b32_sdwa v65, 4, v226 dst_sel:DWORD dst_unused:UNUSED_PAD src0_sel:DWORD src1_sel:WORD_1\n" \
  "ds_read_b128 v[72:75], v65\n" \
  "v_lshlrev_b32_sdwa v66, 4, v227 dst_sel:DWORD dst_unused:UNUSED_PAD src0_sel:DWORD src1_sel:WORD_0\n" \
  "ds_read_b128 v[76:79], v66\n" \
  "v_lshlrev_b32_sdwa v67, 4, v227 dst_sel:DWORD dst_unused:UNUSED_PAD src0_sel:DWORD src1_sel:WORD_1\n" \
  "ds_read_b128 v[80:83], v67\n" \
  "s_add_u32 m0, s58, 0x4000\n" \
  "s_cmp_lt_u32 s35, 0x28000\n" \
  "global_load_lds_dwordx4 v189, s[54:55] sc1\n" \
  "global_load_lds_dwordx4 v189, s[54:55] offset:1024 sc1\n" \
  "s_cselect_b32 s58, s35, s59\n" \
  "v_lshlrev_b32_sdwa v64, 4, v228 dst_sel:DWORD dst_unused:UNUSED_PAD src0_sel:DWORD src1_sel:WORD_0\n" \
  "ds_read_b128 v[84:87], v64\n" \
  "v_lshlrev_b32_sdwa v65, 4, v228 dst_sel:DWORD dst_unused:UNUSED_PAD src0_sel:DWORD src1_sel:WORD_1\n" \
  "ds_read_b128 v[88:91], v65\n" \
  "v_lshlrev_b32_sdwa v66, 4, v229 dst_sel:DWORD dst_unused:UNUSED_PAD src0_sel:DWORD src1_sel:WORD_0\n" \
  "ds_read_b128 v[92:95], v66\n" \
  "v_lshlrev_b32_sdwa v67, 4, v229 dst_sel:DWORD dst_unused:UNUSED_PAD src0_sel:DWORD src1_sel:WORD_1\n" \
  "ds_read_b128 v[96:99], v67\n" \
  "s_waitcnt lgkmcnt(10)\n" \
  "v_smfmac_f32_16x16x64_bf16 v[0:3], v[180:183], v[100:107], v192\n" \
  "v_lshlrev_b32_sdwa v64, 4, v230 dst_sel:DWORD dst_unused:UNUSED_PAD src0_sel:DWORD src1_sel:WORD_0\n" \
  "ds_read_b128 v[100:103], v64\n" \
  "v_lshlrev_b32_sdwa v65, 4, v230 dst_sel:DWORD dst_unused:UNUSED_PAD src0_sel:DWORD src1_sel:WORD_1\n" \
  "ds_read_b128 v[104:107], v65\n" \
  "s_mov_b32 m0, s58\n" \
  "s_add_u32 s35, s58, 0x8000\n" \
  "global_load_lds_dwordx4 v190, s[54:55] sc1\n" \
  "global_load_lds_dwordx4 v190, s[54:55] offset:1024 sc1\n" \
  "s_waitcnt lgkmcnt(10)\n" \
  "v_smfmac_f32_16x16x64_bf16 v[4:7], v[180:183], v[108:115], v192\n" \
  "v_lshlrev_b32_sdwa v66, 4, v231 dst_sel:DWORD dst_unused:UNUSED_PAD src0_sel:DWORD src1_sel:WORD_0\n" \
  "ds_read_b128 v[108:111], v66\n" \
  "v_lshlrev_b32_sdwa v67, 4, v231 dst_sel:DWORD dst_unused:UNUSED_PAD src0_sel:DWORD src1_sel:WORD_1\n" \
  "ds_read_b128 v[112:115], v67\n" \
  "s_waitcnt lgkmcnt(10)\n" \
  "v_smfmac_f32_16x16x64_bf16 v[8:11], v[180:183], v[68:75], v192\n" \
  "v_lshlrev_b32_sdwa v64, 4, v232 dst_sel:DWORD dst_unused:UNUSED_PAD src0_sel:DWORD src1_sel:WORD_0\n" \
  "ds_read_b128 v[68:71], v64\n" \
  "v_lshlrev_b32_sdwa v65, 4, v232 dst_sel:DWORD dst_unused:UNUSED_PAD src0_sel:DWORD src1_sel:WORD_1\n" \
  "ds_read_b128 v[72:75], v65\n" \
  "s_add_u32 m0, s58, 0x4000\n" \
  "s_cmp_lt_u32 s35, 0x28000\n" \
  "global_load_lds_dwordx4 v191, s[54:55] sc1\n" \
  "global_load_lds_dwordx4 v191, s[54:55] offset:1024 sc1\n" \
  "s_cselect_b32 s58, s35, s59\n" \
  "s_waitcnt lgkmcnt(10)\n" \
  "v_smfmac_f32_16x16x64_bf16 v[12:15], v[180:183], v[76:83], v192\n" \
  "v_lshlrev_b32_sdwa v66, 4, v233 dst_sel:DWORD dst_unused:UNUSED_PAD src0_sel:DWORD src1_sel:WORD_0\n" \
  "ds_read_b128 v[76:79], v66\n" \
  "v_lshlrev_b32_sdwa v67, 4, v233 dst_sel:DWORD dst_unused:UNUSED_PAD src0_sel:DWORD src1_sel:WORD_1\n" \
  "ds_read_b128 v[80:83], v67\n" \
  "s_waitcnt lgkmcnt(10)\n" \
  "v_smfmac_f32_16x16x64_bf16 v[16:19], v[180:183], v[84:91], v192\n" \
  "v_lshlrev_b32_sdwa v64, 4, v234 dst_sel:DWORD dst_unused:UNUSED_PAD src0_sel:DWORD src1_sel:WORD_0\n" \
  "ds_read_b128 v[84:87], v64\n" \
  "v_lshlrev_b32_sdwa v65, 4, v234 dst_sel:DWORD dst_unused:UNUSED_PAD src0_sel:DWORD src1_sel:WORD_1\n" \
  "ds_read_b128 v[88:91], v65\n" \
  "global_load_dwordx4 v[208:211], v188, s[56:57] sc1\n" \
  "global_load_dwordx4 v[212:215], v188, s[56:57] offset:1024 sc1\n" \
  "s_waitcnt lgkmcnt(10)\n" \
  "v_smfmac_f32_16x16x64_bf16 v[20:23], v[180:183], v[92:99], v192\n" \
  "v_lshlrev_b32_sdwa v66, 4, v235 dst_sel:DWORD dst_unused:UNUSED_PAD src0_sel:DWORD src1_sel:WORD_0\n" \
  "ds_read_b128 v[92:95], v66\n" \
  "v_lshlrev_b32_sdwa v67, 4, v235 dst_sel:DWORD dst_unused:UNUSED_PAD src0_sel:DWORD src1_sel:WORD_1\n" \
  "ds_read_b128 v[96:99], v67\n" \
  "s_waitcnt lgkmcnt(10)\n" \
  "v_smfmac_f32_16x16x64_bf16 v[24:27], v[180:183], v[100:107], v192\n" \
  "v_lshlrev_b32_sdwa v64, 4, v236 dst_sel:DWORD dst_unused:UNUSED_PAD src0_sel:DWORD src1_sel:WORD_0\n" \
  "ds_read_b128 v[100:103], v64\n" \
  "v_lshlrev_b32_sdwa v65, 4, v236 dst_sel:DWORD dst_unused:UNUSED_PAD src0_sel:DWORD src1_sel:WORD_1\n" \
  "ds_read_b128 v[104:107], v65\n" \
  "global_load_dwordx4 v[216:219], v188, s[56:57] offset:2048 sc1\n" \
  "global_load_dwordx4 v[220:223], v188, s[56:57] offset:3072 sc1\n" \
  "s_waitcnt lgkmcnt(10)\n" \
  "v_smfmac_f32_16x16x64_bf16 v[28:31], v[180:183], v[108:115], v192\n" \
  "v_lshlrev_b32_sdwa v66, 4, v237 dst_sel:DWORD dst_unused:UNUSED_PAD src0_sel:DWORD src1_sel:WORD_0\n" \
  "ds_read_b128 v[108:111], v66\n" \
  "v_lshlrev_b32_sdwa v67, 4, v237 dst_sel:DWORD dst_unused:UNUSED_PAD src0_sel:DWORD src1_sel:WORD_1\n" \
  "ds_read_b128 v[112:115], v67\n" \
  "s_waitcnt lgkmcnt(10)\n" \
  "v_smfmac_f32_16x16x64_bf16 v[32:35], v[180:183], v[68:75], v192\n" \
  "v_lshlrev_b32_sdwa v64, 4, v238 dst_sel:DWORD dst_unused:UNUSED_PAD src0_sel:DWORD src1_sel:WORD_0\n" \
  "ds_read_b128 v[68:71], v64\n" \
  "v_lshlrev_b32_sdwa v65, 4, v238 dst_sel:DWORD dst_unused:UNUSED_PAD src0_sel:DWORD src1_sel:WORD_1\n" \
  "ds_read_b128 v[72:75], v65\n" \
  "s_waitcnt lgkmcnt(10)\n" \
  "v_smfmac_f32_16x16x64_bf16 v[36:39], v[180:183], v[76:83], v192\n" \
  "v_lshlrev_b32_sdwa v66, 4, v239 dst_sel:DWORD dst_unused:UNUSED_PAD src0_sel:DWORD src1_sel:WORD_0\n" \
  "ds_read_b128 v[76:79], v66\n" \
  "v_lshlrev_b32_sdwa v67, 4, v239 dst_sel:DWORD dst_unused:UNUSED_PAD src0_sel:DWORD src1_sel:WORD_1\n" \
  "ds_read_b128 v[80:83], v67\n" \
  "s_add_u32 s56, s56, 32768\n" \
  "s_addc_u32 s57, s57, 0\n" \
  "s_add_u32 s54, s54, 64000\n" \
  "s_addc_u32 s55, s55, 0\n" \
  "s_waitcnt lgkmcnt(10)\n" \
  "v_smfmac_f32_16x16x64_bf16 v[40:43], v[180:183], v[84:91], v192\n" \
  "s_waitcnt lgkmcnt(8)\n" \
  "v_smfmac_f32_16x16x64_bf16 v[44:47], v[180:183], v[92:99], v192\n" \
  "s_waitcnt vmcnt(8)\n" \
  "s_barrier\n" \
  "v_lshlrev_b32_sdwa v64, 4, v240 dst_sel:DWORD dst_unused:UNUSED_PAD src0_sel:DWORD src1_sel:WORD_0\n" \
  "ds_read_b128 v[84:87], v64\n" \
  "v_lshlrev_b32_sdwa v65, 4, v240 dst_sel:DWORD dst_unused:UNUSED_PAD src0_sel:DWORD src1_sel:WORD_1\n" \
  "ds_read_b128 v[88:91], v65\n" \
  "v_lshlrev_b32_sdwa v66, 4, v241 dst_sel:DWORD dst_unused:UNUSED_PAD src0_sel:DWORD src1_sel:WORD_0\n" \
  "ds_read_b128 v[92:95], v66\n" \
  "v_lshlrev_b32_sdwa v67, 4, v241 dst_sel:DWORD dst_unused:UNUSED_PAD src0_sel:DWORD src1_sel:WORD_1\n" \
  "ds_read_b128 v[96:99], v67\n" \
  "s_waitcnt lgkmcnt(10)\n" \
  "v_smfmac_f32_16x16x64_bf16 v[48:51], v[180:183], v[100:107], v192\n" \
  "s_waitcnt lgkmcnt(8)\n" \
  "v_smfmac_f32_16x16x64_bf16 v[52:55], v[180:183], v[108:115], v192\n" \
  "s_waitcnt lgkmcnt(6)\n" \
  "v_smfmac_f32_16x16x64_bf16 v[56:59], v[180:183], v[68:75], v192\n" \
  "s_waitcnt lgkmcnt(4)\n" \
  "v_smfmac_f32_16x16x64_bf16 v[60:63], v[180:183], v[76:83], v192\n" \
  "s_barrier\n" \
  "s_mov_b32 m0, s58\n" \
  "s_add_u32 s35, s58, 0x8000\n" \
  "global_load_lds_dwordx4 v188, s[54:55] sc1\n" \
  "global_load_lds_dwordx4 v188, s[54:55] offset:1024 sc1\n" \
  "v_lshlrev_b32_sdwa v64, 4, v242 dst_sel:DWORD dst_unused:UNUSED_PAD src0_sel:DWORD src1_sel:WORD_0\n" \
  "ds_read_b128 v[100:103], v64\n" \
  "v_lshlrev_b32_sdwa v65, 4, v242 dst_sel:DWORD dst_unused:UNUSED_PAD src0_sel:DWORD src1_sel:WORD_1\n" \
  "ds_read_b128 v[104:107], v65\n" \
  "v_lshlrev_b32_sdwa v66, 4, v243 dst_sel:DWORD dst_unused:UNUSED_PAD src0_sel:DWORD src1_sel:WORD_0\n" \
  "ds_read_b128 v[108:111], v66\n" \
  "v_lshlrev_b32_sdwa v67, 4, v243 dst_sel:DWORD dst_unused:UNUSED_PAD src0_sel:DWORD src1_sel:WORD_1\n" \
  "ds_read_b128 v[112:115], v67\n" \
  "s_add_u32 m0, s58, 0x4000\n" \
  "s_cmp_lt_u32 s35, 0x28000\n" \
  "global_load_lds_dwordx4 v189, s[54:55] sc1\n" \
  "global_load_lds_dwordx4 v189, s[54:55] offset:1024 sc1\n" \
  "s_cselect_b32 s58, s35, s59\n" \
  "v_lshlrev_b32_sdwa v64, 4, v244 dst_sel:DWORD dst_unused:UNUSED_PAD src0_sel:DWORD src1_sel:WORD_0\n" \
  "ds_read_b128 v[68:71], v64\n" \
  "v_lshlrev_b32_sdwa v65, 4, v244 dst_sel:DWORD dst_unused:UNUSED_PAD src0_sel:DWORD src1_sel:WORD_1\n" \
  "ds_read_b128 v[72:75], v65\n" \
  "v_lshlrev_b32_sdwa v66, 4, v245 dst_sel:DWORD dst_unused:UNUSED_PAD src0_sel:DWORD src1_sel:WORD_0\n" \
  "ds_read_b128 v[76:79], v66\n" \
  "v_lshlrev_b32_sdwa v67, 4, v245 dst_sel:DWORD dst_unused:UNUSED_PAD src0_sel:DWORD src1_sel:WORD_1\n" \
  "ds_read_b128 v[80:83], v67\n" \
  "s_waitcnt lgkmcnt(10)\n" \
  "v_smfmac_f32_16x16x64_bf16 v[0:3], v[180:183], v[84:91], v192\n" \
  "v_lshlrev_b32_sdwa v64, 4, v246 dst_sel:DWORD dst_unused:UNUSED_PAD src0_sel:DWORD src1_sel:WORD_0\n" \
  "ds_read_b128 v[84:87], v64\n" \
  "v_lshlrev_b32_sdwa v65, 4, v246 dst_sel:DWORD dst_unused:UNUSED_PAD src0_sel:DWORD src1_sel:WORD_1\n" \
  "ds_read_b128 v[88:91], v65\n" \
  "s_mov_b32 m0, s58\n" \
  "s_add_u32 s35, s58, 0x8000\n" \
  "global_load_lds_dwordx4 v190, s[54:55] sc1\n" \
  "global_load_lds_dwordx4 v190, s[54:55] offset:1024 sc1\n" \
  "s_waitcnt lgkmcnt(10)\n" \
  "v_smfmac_f32_16x16x64_bf16 v[4:7], v[180:183], v[92:99], v192\n" \
  "v_lshlrev_b32_sdwa v66, 4, v247 dst_sel:DWORD dst_unused:UNUSED_PAD src0_sel:DWORD src1_sel:WORD_0\n" \
  "ds_read_b128 v[92:95], v66\n" \
  "v_lshlrev_b32_sdwa v67, 4, v247 dst_sel:DWORD dst_unused:UNUSED_PAD src0_sel:DWORD src1_sel:WORD_1\n" \
  "ds_read_b128 v[96:99], v67\n" \
  "s_waitcnt lgkmcnt(10)\n" \
  "v_smfmac_f32_16x16x64_bf16 v[8:11], v[180:183], v[100:107], v192\n" \
  "v_lshlrev_b32_sdwa v64, 4, v248 dst_sel:DWORD dst_unused:UNUSED_PAD src0_sel:DWORD src1_sel:WORD_0\n" \
  "ds_read_b128 v[100:103], v64\n" \
  "v_lshlrev_b32_sdwa v65, 4, v248 dst_sel:DWORD dst_unused:UNUSED_PAD src0_sel:DWORD src1_sel:WORD_1\n" \
  "ds_read_b128 v[104:107], v65\n" \
  "s_add_u32 m0, s58, 0x4000\n" \
  "s_cmp_lt_u32 s35, 0x28000\n" \
  "global_load_lds_dwordx4 v191, s[54:55] sc1\n" \
  "global_load_lds_dwordx4 v191, s[54:55] offset:1024 sc1\n" \
  "s_cselect_b32 s58, s35, s59\n" \
  "s_waitcnt lgkmcnt(10)\n" \
  "v_smfmac_f32_16x16x64_bf16 v[12:15], v[180:183], v[108:115], v192\n" \
  "v_lshlrev_b32_sdwa v66, 4, v249 dst_sel:DWORD dst_unused:UNUSED_PAD src0_sel:DWORD src1_sel:WORD_0\n" \
  "ds_read_b128 v[108:111], v66\n" \
  "v_lshlrev_b32_sdwa v67, 4, v249 dst_sel:DWORD dst_unused:UNUSED_PAD src0_sel:DWORD src1_sel:WORD_1\n" \
  "ds_read_b128 v[112:115], v67\n" \
  "s_waitcnt lgkmcnt(10)\n" \
  "v_smfmac_f32_16x16x64_bf16 v[16:19], v[180:183], v[68:75], v192\n" \
  "v_lshlrev_b32_sdwa v64, 4, v250 dst_sel:DWORD dst_unused:UNUSED_PAD src0_sel:DWORD src1_sel:WORD_0\n" \
  "ds_read_b128 v[68:71], v64\n" \
  "v_lshlrev_b32_sdwa v65, 4, v250 dst_sel:DWORD dst_unused:UNUSED_PAD src0_sel:DWORD src1_sel:WORD_1\n" \
  "ds_read_b128 v[72:75], v65\n" \
  "global_load_dwordx4 v[224:227], v188, s[56:57] sc1\n" \
  "global_load_dwordx4 v[228:231], v188, s[56:57] offset:1024 sc1\n" \
  "s_waitcnt lgkmcnt(10)\n" \
  "v_smfmac_f32_16x16x64_bf16 v[20:23], v[180:183], v[76:83], v192\n" \
  "v_lshlrev_b32_sdwa v66, 4, v251 dst_sel:DWORD dst_unused:UNUSED_PAD src0_sel:DWORD src1_sel:WORD_0\n" \
  "ds_read_b128 v[76:79], v66\n" \
  "v_lshlrev_b32_sdwa v67, 4, v251 dst_sel:DWORD dst_unused:UNUSED_PAD src0_sel:DWORD src1_sel:WORD_1\n" \
  "ds_read_b128 v[80:83], v67\n" \
  "s_waitcnt lgkmcnt(10)\n" \
  "v_smfmac_f32_16x16x64_bf16 v[24:27], v[180:183], v[84:91], v192\n" \
  "v_lshlrev_b32_sdwa v64, 4, v252 dst_sel:DWORD dst_unused:UNUSED_PAD src0_sel:DWORD src1_sel:WORD_0\n" \
  "ds_read_b128 v[84:87], v64\n" \
  "v_lshlrev_b32_sdwa v65, 4, v252 dst_sel:DWORD dst_unused:UNUSED_PAD src0_sel:DWORD src1_sel:WORD_1\n" \
  "ds_read_b128 v[88:91], v65\n" \
  "global_load_dwordx4 v[232:235], v188, s[56:57] offset:2048 sc1\n" \
  "global_load_dwordx4 v[236:239], v188, s[56:57] offset:3072 sc1\n" \
  "s_waitcnt lgkmcnt(10)\n" \
  "v_smfmac_f32_16x16x64_bf16 v[28:31], v[180:183], v[92:99], v192\n" \
  "v_lshlrev_b32_sdwa v66, 4, v253 dst_sel:DWORD dst_unused:UNUSED_PAD src0_sel:DWORD src1_sel:WORD_0\n" \
  "ds_read_b128 v[92:95], v66\n" \
  "v_lshlrev_b32_sdwa v67, 4, v253 dst_sel:DWORD dst_unused:UNUSED_PAD src0_sel:DWORD src1_sel:WORD_1\n" \
  "ds_read_b128 v[96:99], v67\n" \
  "s_waitcnt lgkmcnt(10)\n" \
  "v_smfmac_f32_16x16x64_bf16 v[32:35], v[180:183], v[100:107], v192\n" \
  "v_lshlrev_b32_sdwa v64, 4, v254 dst_sel:DWORD dst_unused:UNUSED_PAD src0_sel:DWORD src1_sel:WORD_0\n" \
  "ds_read_b128 v[100:103], v64\n" \
  "v_lshlrev_b32_sdwa v65, 4, v254 dst_sel:DWORD dst_unused:UNUSED_PAD src0_sel:DWORD src1_sel:WORD_1\n" \
  "ds_read_b128 v[104:107], v65\n" \
  "s_waitcnt lgkmcnt(10)\n" \
  "v_smfmac_f32_16x16x64_bf16 v[36:39], v[180:183], v[108:115], v192\n" \
  "v_lshlrev_b32_sdwa v66, 4, v255 dst_sel:DWORD dst_unused:UNUSED_PAD src0_sel:DWORD src1_sel:WORD_0\n" \
  "ds_read_b128 v[108:111], v66\n" \
  "v_lshlrev_b32_sdwa v67, 4, v255 dst_sel:DWORD dst_unused:UNUSED_PAD src0_sel:DWORD src1_sel:WORD_1\n" \
  "ds_read_b128 v[112:115], v67\n" \
  "s_add_u32 s56, s56, 32768\n" \
  "s_addc_u32 s57, s57, 0\n" \
  "s_add_u32 s54, s54, 64000\n" \
  "s_addc_u32 s55, s55, 0\n" \
  "s_lshl3_add_u32 s36, s27, s51\n" \
  "s_cmp_eq_u32 s36, 37\n" \
  "s_waitcnt lgkmcnt(10)\n" \
  "v_smfmac_f32_16x16x64_bf16 v[40:43], v[180:183], v[68:75], v192\n" \
  "s_waitcnt lgkmcnt(8)\n" \
  "v_smfmac_f32_16x16x64_bf16 v[44:47], v[180:183], v[76:83], v192\n" \
  "s_waitcnt vmcnt(8)\n" \
  "s_cbranch_scc1 LG_pol\n" \
  "LG_rt2:\n" \
  "s_barrier\n" \
  "v_lshlrev_b32_sdwa v64, 4, v208 dst_sel:DWORD dst_unused:UNUSED_PAD src0_sel:DWORD src1_sel:WORD_0\n" \
  "ds_read_b128 v[68:71], v64\n" \
  "v_lshlrev_b32_sdwa v65, 4, v208 dst_sel:DWORD dst_unused:UNUSED_PAD src0_sel:DWORD src1_sel:WORD_1\n" \
  "ds_read_b128 v[72:75], v65\n" \
  "v_lshlrev_b32_sdwa v66, 4, v209 dst_sel:DWORD dst_unused:UNUSED_PAD src0_sel:DWORD src1_sel:WORD_0\n" \
  "ds_read_b128 v[76:79], v66\n" \
  "v_lshlrev_b32_sdwa v67, 4, v209 dst_sel:DWORD dst_unused:UNUSED_PAD src0_sel:DWORD src1_sel:WORD_1\n" \
  "ds_read_b128 v[80:83], v67\n" \
  "s_waitcnt lgkmcnt(10)\n" \
  "v_smfmac_f32_16x16x64_bf16 v[48:51], v[180:183], v[84:91], v192\n" \
  "s_waitcnt lgkmcnt(8)\n" \
  "v_smfmac_f32_16x16x64_bf16 v[52:55], v[180:183], v[92:99], v192\n" \
  "s_waitcnt lgkmcnt(6)\n" \
  "v_smfmac_f32_16x16x64_bf16 v[56:59], v[180:183], v[100:107], v192\n" \
  "s_waitcnt lgkmcnt(4)\n" \
  "v_smfmac_f32_16x16x64_bf16 v[60:63], v[180:183], v[108:115], v192\n" \
  "s_barrier\n" \
  "s_mov_b32 m0, s58\n" \
  "s_add_u32 s35, s58, 0x8000\n" \
  "global_load_lds_dwordx4 v188, s[54:55] sc1\n" \
  "global_load_lds_dwordx4 v188, s[54:55] offset:1024 sc1\n" \
  "v_lshlrev_b32_sdwa v64, 4, v210 dst_sel:DWORD dst_unused:UNUSED_PAD src0_sel:DWORD src1_sel:WORD_0\n" \
  "ds_read_b128 v[84:87], v64\n" \
  "v_lshlrev_b32_sdwa v65, 4, v210 dst_sel:DWORD dst_unused:UNUSED_PAD src0_sel:DWORD src1_sel:WORD_1\n" \
  "ds_read_b128 v[88:91], v65\n" \
  "v_lshlrev_b32_sdwa v66, 4, v211 dst_sel:DWORD dst_unused:UNUSED_PAD src0_sel:DWORD src1_sel:WORD_0\n" \
  "ds_read_b128 v[92:95], v66\n" \
  "v_lshlrev_b32_sdwa v67, 4, v211 dst_sel:DWORD dst_unused:UNUSED_PAD src0_sel:DWORD src1_sel:WORD_1\n" \
  "ds_read_b128 v[96:99], v67\n" \
  "s_add_u32 m0, s58, 0x4000\n" \
  "s_cmp_lt_u32 s35, 0x28000\n" \
  "global_load_lds_dwordx4 v189, s[54:55] sc1\n" \
  "global_load_lds_dwordx4 v189, s[54:55] offset:1024 sc1\n" \
  "s_cselect_b32 s58, s35, s59\n" \
  "v_lshlrev_b32_sdwa v64, 4, v212 dst_sel:DWORD dst_unused:UNUSED_PAD src0_sel:DWORD src1_sel:WORD_0\n" \
  "ds_read_b128 v[100:103], v64\n" \
  "v_lshlrev_b32_sdwa v65, 4, v212 dst_sel:DWORD dst_unused:UNUSED_PAD src0_sel:DWORD src1_sel:WORD_1\n" \
  "ds_read_b128 v[104:107], v65\n" \
  "v_lshlrev_b32_sdwa v66, 4, v213 dst_sel:DWORD dst_unused:UNUSED_PAD src0_sel:DWORD src1_sel:WORD_0\n" \
  "ds_read_b128 v[108:111], v66\n" \
  "v_lshlrev_b32_sdwa v67, 4, v213 dst_sel:DWORD dst_unused:UNUSED_PAD src0_sel:DWORD src1_sel:WORD_1\n" \
  "ds_read_b128 v[112:115], v67\n" \
  "s_waitcnt lgkmcnt(10)\n" \
  "v_smfmac_f32_16x16x64_bf16 v[0:3], v[180:183], v[68:75], v192\n" \
  "v_lshlrev_b32_sdwa v64, 4, v214 dst_sel:DWORD dst_unused:UNUSED_PAD src0_sel:DWORD src1_sel:WORD_0\n" \
  "ds_read_b128 v[68:71], v64\n" \
  "v_lshlrev_b32_sdwa v65, 4, v214 dst_sel:DWORD dst_unused:UNUSED_PAD src0_sel:DWORD src1_sel:WORD_1\n" \
  "ds_read_b128 v[72:75], v65\n" \
  "s_mov_b32 m0, s58\n" \
  "s_add_u32 s35, s58, 0x8000\n" \
  "global_load_lds_dwordx4 v190, s[54:55] sc1\n" \
  "global_load_lds_dwordx4 v190, s[54:55] offset:1024 sc1\n" \
  "s_waitcnt lgkmcnt(10)\n" \
  "v_smfmac_f32_16x16x64_bf16 v[4:7], v[180:183], v[76:83], v192\n" \
  "v_lshlrev_b32_sdwa v66, 4, v215 dst_sel:DWORD dst_unused:UNUSED_PAD src0_sel:DWORD src1_sel:WORD_0\n" \
  "ds_read_b128 v[76:79], v66\n" \
  "v_lshlrev_b32_sdwa v67, 4, v215 dst_sel:DWORD dst_unused:UNUSED_PAD src0_sel:DWORD src1_sel:WORD_1\n" \
  "ds_read_b128 v[80:83], v67\n" \
  "s_waitcnt lgkmcnt(10)\n" \
  "v_smfmac_f32_16x16x64_bf16 v[8:11], v[180:183], v[84:91], v192\n" \
  "v_lshlrev_b32_sdwa v64, 4, v216 dst_sel:DWORD dst_unused:UNUSED_PAD src0_sel:DWORD src1_sel:WORD_0\n" \
  "ds_read_b128 v[84:87], v64\n" \
  "v_lshlrev_b32_sdwa v65, 4, v216 dst_sel:DWORD dst_unused:UNUSED_PAD src0_sel:DWORD src1_sel:WORD_1\n" \
  "ds_read_b128 v[88:91], v65\n" \
  "s_add_u32 m0, s58, 0x4000\n" \
  "s_cmp_lt_u32 s35, 0x28000\n" \
  "global_load_lds_dwordx4 v191, s[54:55] sc1\n" \
  "global_load_lds_dwordx4 v191, s[54:55] offset:1024 sc1\n" \
  "s_cselect_b32 s58, s35, s59\n" \
  "s_waitcnt lgkmcnt(10)\n" \
  "v_smfmac_f32_16x16x64_bf16 v[12:15], v[180:183], v[92:99], v192\n" \
  "v_lshlrev_b32_sdwa v66, 4, v217 dst_sel:DWORD dst_unused:UNUSED_PAD src0_sel:DWORD src1_sel:WORD_0\n" \
  "ds_read_b128 v[92:95], v66\n" \
  "v_lshlrev_b32_sdwa v67, 4, v217 dst_sel:DWORD dst_unused:UNUSED_PAD src0_sel:DWORD src1_sel:WORD_1\n" \
  "ds_read_b128 v[96:99], v67\n" \
  "s_waitcnt lgkmcnt(10)\n" \
  "v_smfmac_f32_16x16x64_bf16 v[16:19], v[180:183], v[100:107], v192\n" \
  "v_lshlrev_b32_sdwa v64, 4, v218 dst_sel:DWORD dst_unused:UNUSED_PAD src0_sel:DWORD src1_sel:WORD_0\n" \
  "ds_read_b128 v[100:103], v64\n" \
  "v_lshlrev_b32_sdwa v65, 4, v218 dst_sel:DWORD dst_unused:UNUSED_PAD src0_sel:DWORD src1_sel:WORD_1\n" \
  "ds_read_b128 v[104:107], v65\n" \
  "global_load_dwordx4 v[240:243], v188, s[56:57] sc1\n" \
  "global_load_dwordx4 v[244:247], v188, s[56:57] offset:1024 sc1\n" \
  "s_waitcnt lgkmcnt(10)\n" \
  "v_smfmac_f32_16x16x64_bf16 v[20:23], v[180:183], v[108:115], v192\n" \
  "v_lshlrev_b32_sdwa v66, 4, v219 dst_sel:DWORD dst_unused:UNUSED_PAD src0_sel:DWORD src1_sel:WORD_0\n" \
  "ds_read_b128 v[108:111], v66\n" \
  "v_lshlrev_b32_sdwa v67, 4, v219 dst_sel:DWORD dst_unused:UNUSED_PAD src0_sel:DWORD src1_sel:WORD_1\n" \
  "ds_read_b128 v[112:115], v67\n" \
  "s_waitcnt lgkmcnt(10)\n" \
  "v_smfmac_f32_16x16x64_bf16 v[24:27], v[180:183], v[68:75], v192\n" \
  "v_lshlrev_b32_sdwa v64, 4, v220 dst_sel:DWORD dst_unused:UNUSED_PAD src0_sel:DWORD src1_sel:WORD_0\n" \
  "ds_read_b128 v[68:71], v64\n" \
  "v_lshlrev_b32_sdwa v65, 4, v220 dst_sel:DWORD dst_unused:UNUSED_PAD src0_sel:DWORD src1_sel:WORD_1\n" \
  "ds_read_b128 v[72:75], v65\n" \
  "global_load_dwordx4 v[248:251], v188, s[56:57] offset:2048 sc1\n" \
  "global_load_dwordx4 v[252:255], v188, s[56:57] offset:3072 sc1\n" \
  "s_waitcnt lgkmcnt(10)\n" \
  "v_smfmac_f32_16x16x64_bf16 v[28:31], v[180:183], v[76:83], v192\n" \
  "v_lshlrev_b32_sdwa v66, 4, v221 dst_sel:DWORD dst_unused:UNUSED_PAD src0_sel:DWORD src1_sel:WORD_0\n" \
  "ds_read_b128 v[76:79], v66\n" \
  "v_lshlrev_b32_sdwa v67, 4, v221 dst_sel:DWORD dst_unused:UNUSED_PAD src0_sel:DWORD src1_sel:WORD_1\n" \
  "ds_read_b128 v[80:83], v67\n" \
  "s_waitcnt lgkmcnt(10)\n" \
  "v_smfmac_f32_16x16x64_bf16 v[32:35], v[180:183], v[84:91], v192\n" \
  "v_lshlrev_b32_sdwa v64, 4, v222 dst_sel:DWORD dst_unused:UNUSED_PAD src0_sel:DWORD src1_sel:WORD_0\n" \
  "ds_read_b128 v[84:87], v64\n" \
  "v_lshlrev_b32_sdwa v65, 4, v222 dst_sel:DWORD dst_unused:UNUSED_PAD src0_sel:DWORD src1_sel:WORD_1\n" \
  "ds_read_b128 v[88:91], v65\n" \
  "s_waitcnt lgkmcnt(10)\n" \
  "v_smfmac_f32_16x16x64_bf16 v[36:39], v[180:183], v[92:99], v192\n" \
  "v_lshlrev_b32_sdwa v66, 4, v223 dst_sel:DWORD dst_unused:UNUSED_PAD src0_sel:DWORD src1_sel:WORD_0\n" \
  "ds_read_b128 v[92:95], v66\n" \
  "v_lshlrev_b32_sdwa v67, 4, v223 dst_sel:DWORD dst_unused:UNUSED_PAD src0_sel:DWORD src1_sel:WORD_1\n" \
  "ds_read_b128 v[96:99], v67\n" \
  "s_add_u32 s56, s56, 32768\n" \
  "s_addc_u32 s57, s57, 0\n" \
  "s_add_u32 s54, s54, 64000\n" \
  "s_addc_u32 s55, s55, 0\n" \
  "s_sub_u32 s51, s51, 1\n" \
  "s_cmp_lg_u32 s51, 0\n" \
  "s_cbranch_scc1 LG_loop\n" \
  "s_waitcnt lgkmcnt(10)\n" \
  "v_smfmac_f32_16x16x64_bf16 v[40:43], v[180:183], v[100:107], v192\n" \
  "s_waitcnt lgkmcnt(8)\n" \
  "v_smfmac_f32_16x16x64_bf16 v[44:47], v[180:183], v[108:115], v192\n" \
  "s_waitcnt vmcnt(8)\n" \
  "s_barrier\n" \
  "v_lshlrev_b32_sdwa v64, 4, v224 dst_sel:DWORD dst_unused:UNUSED_PAD src0_sel:DWORD src1_sel:WORD_0\n" \
  "ds_read_b128 v[100:103], v64\n" \
  "v_lshlrev_b32_sdwa v65, 4, v224 dst_sel:DWORD dst_unused:UNUSED_PAD src0_sel:DWORD src1_sel:WORD_1\n" \
  "ds_read_b128 v[104:107], v65\n" \
  "v_lshlrev_b32_sdwa v66, 4, v225 dst_sel:DWORD dst_unused:UNUSED_PAD src0_sel:DWORD src1_sel:WORD_0\n" \
  "ds_read_b128 v[108:111], v66\n" \
  "v_lshlrev_b32_sdwa v67, 4, v225 dst_sel:DWORD dst_unused:UNUSED_PAD src0_sel:DWORD src1_sel:WORD_1\n" \
  "ds_read_b128 v[112:115], v67\n" \
  "s_waitcnt lgkmcnt(10)\n" \
  "v_smfmac_f32_16x16x64_bf16 v[48:51], v[180:183], v[68:75], v192\n" \
  "s_waitcnt lgkmcnt(8)\n" \
  "v_smfmac_f32_16x16x64_bf16 v[52:55], v[180:183], v[76:83], v192\n" \
  "s_waitcnt lgkmcnt(6)\n" \
  "v_smfmac_f32_16x16x64_bf16 v[56:59], v[180:183], v[84:91], v192\n" \
  "s_waitcnt lgkmcnt(4)\n" \
  "v_smfmac_f32_16x16x64_bf16 v[60:63], v[180:183], v[92:99], v192\n" \
  "s_barrier\n" \
  "s_mov_b32 m0, s58\n" \
  "s_add_u32 s35, s58, 0x8000\n" \
  "global_load_lds_dwordx4 v188, s[54:55] sc1\n" \
  "global_load_lds_dwordx4 v188, s[54:55] offset:1024 sc1\n" \
  "v_lshlrev_b32_sdwa v64, 4, v226 dst_sel:DWORD dst_unused:UNUSED_PAD src0_sel:DWORD src1_sel:WORD_0\n" \
  "ds_read_b128 v[68:71], v64\n" \
  "v_lshlrev_b32_sdwa v65, 4, v226 dst_sel:DWORD dst_unused:UNUSED_PAD src0_sel:DWORD src1_sel:WORD_1\n" \
  "ds_read_b128 v[72:75], v65\n" \
  "v_lshlrev_b32_sdwa v66, 4, v227 dst_sel:DWORD dst_unused:UNUSED_PAD src0_sel:DWORD src1_sel:WORD_0\n" \
  "ds_read_b128 v[76:79], v66\n" \
  "v_lshlrev_b32_sdwa v67, 4, v227 dst_sel:DWORD dst_unused:UNUSED_PAD src0_sel:DWORD src1_sel:WORD_1\n" \
  "ds_read_b128 v[80:83], v67\n" \
  "s_add_u32 m0, s58, 0x4000\n" \
  "s_cmp_lt_u32 s35, 0x28000\n" \
  "global_load_lds_dwordx4 v189, s[54:55] sc1\n" \
  "global_load_lds_dwordx4 v189, s[54:55] offset:1024 sc1\n" \
  "s_cselect_b32 s58, s35, s59\n" \
  "v_lshlrev_b32_sdwa v64, 4, v228 dst_sel:DWORD dst_unused:UNUSED_PAD src0_sel:DWORD src1_sel:WORD_0\n" \
  "ds_read_b128 v[84:87], v64\n" \
  "v_lshlrev_b32_sdwa v65, 4, v228 dst_sel:DWORD dst_unused:UNUSED_PAD src0_sel:DWORD src1_sel:WORD_1\n" \
  "ds_read_b128 v[88:91], v65\n" \
  "v_lshlrev_b32_sdwa v66, 4, v229 dst_sel:DWORD dst_unused:UNUSED_PAD src0_sel:DWORD src1_sel:WORD_0\n" \
  "ds_read_b128 v[92:95], v66\n" \
  "v_lshlrev_b32_sdwa v67, 4, v229 dst_sel:DWORD dst_unused:UNUSED_PAD src0_sel:DWORD src1_sel:WORD_1\n" \
  "ds_read_b128 v[96:99], v67\n" \
  "s_waitcnt lgkmcnt(10)\n" \
  "v_smfmac_f32_16x16x64_bf16 v[0:3], v[180:183], v[100:107], v192\n" \
  "v_lshlrev_b32_sdwa v64, 4, v230 dst_sel:DWORD dst_unused:UNUSED_PAD src0_sel:DWORD src1_sel:WORD_0\n" \
  "ds_read_b128 v[100:103], v64\n" \
  "v_lshlrev_b32_sdwa v65, 4, v230 dst_sel:DWORD dst_unused:UNUSED_PAD src0_sel:DWORD src1_sel:WORD_1\n" \
  "ds_read_b128 v[104:107], v65\n" \
  "s_mov_b32 m0, s58\n" \
  "s_add_u32 s35, s58, 0x8000\n" \
  "global_load_lds_dwordx4 v190, s[54:55] sc1\n" \
  "global_load_lds_dwordx4 v190, s[54:55] offset:1024 sc1\n" \
  "s_waitcnt lgkmcnt(10)\n" \
  "v_smfmac_f32_16x16x64_bf16 v[4:7], v[180:183], v[108:115], v192\n" \
  "v_lshlrev_b32_sdwa v66, 4, v231 dst_sel:DWORD dst_unused:UNUSED_PAD src0_sel:DWORD src1_sel:WORD_0\n" \
  "ds_read_b128 v[108:111], v66\n" \
  "v_lshlrev_b32_sdwa v67, 4, v231 dst_sel:DWORD dst_unused:UNUSED_PAD src0_sel:DWORD src1_sel:WORD_1\n" \
  "ds_read_b128 v[112:115], v67\n" \
  "s_waitcnt lgkmcnt(10)\n" \
  "v_smfmac_f32_16x16x64_bf16 v[8:11], v[180:183], v[68:75], v192\n" \
  "v_lshlrev_b32_sdwa v64, 4, v232 dst_sel:DWORD dst_unused:UNUSED_PAD src0_sel:DWORD src1_sel:WORD_0\n" \
  "ds_read_b128 v[68:71], v64\n" \
  "v_lshlrev_b32_sdwa v65, 4, v232 dst_sel:DWORD dst_unused:UNUSED_PAD src0_sel:DWORD src1_sel:WORD_1\n" \
  "ds_read_b128 v[72:75], v65\n" \
  "s_add_u32 m0, s58, 0x4000\n" \
  "s_cmp_lt_u32 s35, 0x28000\n" \
  "global_load_lds_dwordx4 v191, s[54:55] sc1\n" \
  "global_load_lds_dwordx4 v191, s[54:55] offset:1024 sc1\n" \
  "s_cselect_b32 s58, s35, s59\n" \
  "s_waitcnt lgkmcnt(10)\n" \
  "v_smfmac_f32_16x16x64_bf16 v[12:15], v[180:183], v[76:83], v192\n" \
  "v_lshlrev_b32_sdwa v66, 4, v233 dst_sel:DWORD dst_unused:UNUSED_PAD src0_sel:DWORD src1_sel:WORD_0\n" \
  "ds_read_b128 v[76:79], v66\n" \
  "v_lshlrev_b32_sdwa v67, 4, v233 dst_sel:DWORD dst_unused:UNUSED_PAD src0_sel:DWORD src1_sel:WORD_1\n" \
  "ds_read_b128 v[80:83], v67\n" \
  "s_waitcnt lgkmcnt(10)\n" \
  "v_smfmac_f32_16x16x64_bf16 v[16:19], v[180:183], v[84:91], v192\n" \
  "v_lshlrev_b32_sdwa v64, 4, v234 dst_sel:DWORD dst_unused:UNUSED_PAD src0_sel:DWORD src1_sel:WORD_0\n" \
  "ds_read_b128 v[84:87], v64\n" \
  "v_lshlrev_b32_sdwa v65, 4, v234 dst_sel:DWORD dst_unused:UNUSED_PAD src0_sel:DWORD src1_sel:WORD_1\n" \
  "ds_read_b128 v[88:91], v65\n" \
  "global_load_dwordx4 v[208:211], v188, s[56:57] sc1\n" \
  "global_load_dwordx4 v[212:215], v188, s[56:57] offset:1024 sc1\n" \
  "s_waitcnt lgkmcnt(10)\n" \
  "v_smfmac_f32_16x16x64_bf16 v[20:23], v[180:183], v[92:99], v192\n" \
  "v_lshlrev_b32_sdwa v66, 4, v235 dst_sel:DWORD dst_unused:UNUSED_PAD src0_sel:DWORD src1_sel:WORD_0\n" \
  "ds_read_b128 v[92:95], v66\n" \
  "v_lshlrev_b32_sdwa v67, 4, v235 dst_sel:DWORD dst_unused:UNUSED_PAD src0_sel:DWORD src1_sel:WORD_1\n" \
  "ds_read_b128 v[96:99], v67\n" \
  "s_waitcnt lgkmcnt(10)\n" \
  "v_smfmac_f32_16x16x64_bf16 v[24:27], v[180:183], v[100:107], v192\n" \
  "v_lshlrev_b32_sdwa v64, 4, v236 dst_sel:DWORD dst_unused:UNUSED_PAD src0_sel:DWORD src1_sel:WORD_0\n" \
  "ds_read_b128 v[100:103], v64\n" \
  "v_lshlrev_b32_sdwa v65, 4, v236 dst_sel:DWORD dst_unused:UNUSED_PAD src0_sel:DWORD src1_sel:WORD_1\n" \
  "ds_read_b128 v[104:107], v65\n" \
  "global_load_dwordx4 v[216:219], v188, s[56:57] offset:2048 sc1\n" \
  "global_load_dwordx4 v[220:223], v188, s[56:57] offset:3072 sc1\n" \
  "s_waitcnt lgkmcnt(10)\n" \
  "v_smfmac_f32_16x16x64_bf16 v[28:31], v[180:183], v[108:115], v192\n" \
  "v_lshlrev_b32_sdwa v66, 4, v237 dst_sel:DWORD dst_unused:UNUSED_PAD src0_sel:DWORD src1_sel:WORD_0\n" \
  "ds_read_b128 v[108:111], v66\n" \
  "v_lshlrev_b32_sdwa v67, 4, v237 dst_sel:DWORD dst_unused:UNUSED_PAD src0_sel:DWORD src1_sel:WORD_1\n" \
  "ds_read_b128 v[112:115], v67\n" \
  "s_waitcnt lgkmcnt(10)\n" \
  "v_smfmac_f32_16x16x64_bf16 v[32:35], v[180:183], v[68:75], v192\n" \
  "v_lshlrev_b32_sdwa v64, 4, v238 dst_sel:DWORD dst_unused:UNUSED_PAD src0_sel:DWORD src1_sel:WORD_0\n" \
  "ds_read_b128 v[68:71], v64\n" \
  "v_lshlrev_b32_sdwa v65, 4, v238 dst_sel:DWORD dst_unused:UNUSED_PAD src0_sel:DWORD src1_sel:WORD_1\n" \
  "ds_read_b128 v[72:75], v65\n" \
  "s_waitcnt lgkmcnt(10)\n" \
  "v_smfmac_f32_16x16x64_bf16 v[36:39], v[180:183], v[76:83], v192\n" \
  "v_lshlrev_b32_sdwa v66, 4, v239 dst_sel:DWORD dst_unused:UNUSED_PAD src0_sel:DWORD src1_sel:WORD_0\n" \
  "ds_read_b128 v[76:79], v66\n" \
  "v_lshlrev_b32_sdwa v67, 4, v239 dst_sel:DWORD dst_unused:UNUSED_PAD src0_sel:DWORD src1_sel:WORD_1\n" \
  "ds_read_b128 v[80:83], v67\n" \
  "s_add_u32 s56, s56, 32768\n" \
  "s_addc_u32 s57, s57, 0\n" \
  "s_add_u32 s54, s54, 64000\n" \
  "s_addc_u32 s55, s55, 0\n" \
  "s_waitcnt lgkmcnt(10)\n" \
  "v_smfmac_f32_16x16x64_bf16 v[40:43], v[180:183], v[84:91], v192\n" \
  "s_waitcnt lgkmcnt(8)\n" \
  "v_smfmac_f32_16x16x64_bf16 v[44:47], v[180:183], v[92:99], v192\n" \
  "s_waitcnt vmcnt(8)\n" \
  "s_barrier\n" \
  "v_lshlrev_b32_sdwa v64, 4, v240 dst_sel:DWORD dst_unused:UNUSED_PAD src0_sel:DWORD src1_sel:WORD_0\n" \
  "ds_read_b128 v[84:87], v64\n" \
  "v_lshlrev_b32_sdwa v65, 4, v240 dst_sel:DWORD dst_unused:UNUSED_PAD src0_sel:DWORD src1_sel:WORD_1\n" \
  "ds_read_b128 v[88:91], v65\n" \
  "v_lshlrev_b32_sdwa v66, 4, v241 dst_sel:DWORD dst_unused:UNUSED_PAD src0_sel:DWORD src1_sel:WORD_0\n" \
  "ds_read_b128 v[92:95], v66\n" \
  "v_lshlrev_b32_sdwa v67, 4, v241 dst_sel:DWORD dst_unused:UNUSED_PAD src0_sel:DWORD src1_sel:WORD_1\n" \
  "ds_read_b128 v[96:99], v67\n" \
  "s_waitcnt lgkmcnt(10)\n" \
  "v_smfmac_f32_16x16x64_bf16 v[48:51], v[180:183], v[100:107], v192\n" \
  "s_waitcnt lgkmcnt(8)\n" \
  "v_smfmac_f32_16x16x64_bf16 v[52:55], v[180:183], v[108:115], v192\n" \
  "s_waitcnt lgkmcnt(6)\n" \
  "v_smfmac_f32_16x16x64_bf16 v[56:59], v[180:183], v[68:75], v192\n" \
  "s_waitcnt lgkmcnt(4)\n" \
  "v_smfmac_f32_16x16x64_bf16 v[60:63], v[180:183], v[76:83], v192\n" \
  "s_barrier\n" \
  "s_mov_b32 m0, s58\n" \
  "s_add_u32 s35, s58, 0x8000\n" \
  "global_load_lds_dwordx4 v188, s[54:55] sc1\n" \
  "global_load_lds_dwordx4 v188, s[54:55] offset:1024 sc1\n" \
  "s_add_u32 m0, s58, 0x4000\n" \
  "s_cmp_lt_u32 s35, 0x28000\n" \
  "global_load_lds_dwordx4 v189, s[54:55] sc1\n" \
  "global_load_lds_dwordx4 v189, s[54:55] offset:1024 sc1\n" \
  "s_cselect_b32 s58, s35, s59\n" \
  "v_lshlrev_b32 v120, 16, v206\n" \
  "v_and_b32 v121, 0xffff0000, v206\n" \
  "v_lshlrev_b32 v122, 16, v207\n" \
  "v_and_b32 v123, 0xffff0000, v207\n" \
  "v_mov_b32 v124, 0xc0135761\n" \
  "v_mov_b32 v125, 0xc0135761\n" \
  "s_mov_b32 s52, 0xbdd2d3e8\n" \
  "s_mov_b32 s53, s52\n" \
  "s_lshl2_add_u32 s35, s33, s34\n" \
  "s_lshl_b32 s35, s35, 14\n" \
  "s_add_u32 s35, s35, s59\n" \
  "s_add_u32 s35, s35, 105676800\n" \
  "s_add_u32 s40, s24, s35\n" \
  "s_addc_u32 s41, s25, 0\n" \
  "v_lshlrev_b32_sdwa v64, 4, v242 dst_sel:DWORD dst_unused:UNUSED_PAD src0_sel:DWORD src1_sel:WORD_0\n" \
  "ds_read_b128 v[100:103], v64\n" \
  "v_lshlrev_b32_sdwa v65, 4, v242 dst_sel:DWORD dst_unused:UNUSED_PAD src0_sel:DWORD src1_sel:WORD_1\n" \
  "ds_read_b128 v[104:107], v65\n" \
  "v_lshlrev_b32_sdwa v66, 4, v243 dst_sel:DWORD dst_unused:UNUSED_PAD src0_sel:DWORD src1_sel:WORD_0\n" \
  "ds_read_b128 v[108:111], v66\n" \
  "v_lshlrev_b32_sdwa v67, 4, v243 dst_sel:DWORD dst_unused:UNUSED_PAD src0_sel:DWORD src1_sel:WORD_1\n" \
  "ds_read_b128 v[112:115], v67\n" \
  "v_lshlrev_b32_sdwa v64, 4, v244 dst_sel:DWORD dst_unused:UNUSED_PAD src0_sel:DWORD src1_sel:WORD_0\n" \
  "ds_read_b128 v[68:71], v64\n" \
  "v_lshlrev_b32_sdwa v65, 4, v244 dst_sel:DWORD dst_unused:UNUSED_PAD src0_sel:DWORD src1_sel:WORD_1\n" \
  "ds_read_b128 v[72:75], v65\n" \
  "v_lshlrev_b32_sdwa v66, 4, v245 dst_sel:DWORD dst_unused:UNUSED_PAD src0_sel:DWORD src1_sel:WORD_0\n" \
  "ds_read_b128 v[76:79], v66\n" \
  "v_lshlrev_b32_sdwa v67, 4, v245 dst_sel:DWORD dst_unused:UNUSED_PAD src0_sel:DWORD src1_sel:WORD_1\n" \
  "ds_read_b128 v[80:83], v67\n" \
  "s_waitcnt lgkmcnt(10)\n" \
  "v_smfmac_f32_16x16x64_bf16 v[0:3], v[180:183], v[84:91], v192\n" \
  "v_lshlrev_b32_sdwa v64, 4, v246 dst_sel:DWORD dst_unused:UNUSED_PAD src0_sel:DWORD src1_sel:WORD_0\n" \
  "ds_read_b128 v[84:87], v64\n" \
  "v_lshlrev_b32_sdwa v65, 4, v246 dst_sel:DWORD dst_unused:UNUSED_PAD src0_sel:DWORD src1_sel:WORD_1\n" \
  "ds_read_b128 v[88:91], v65\n" \
  "s_waitcnt lgkmcnt(10)\n" \
  "v_smfmac_f32_16x16x64_bf16 v[4:7], v[180:183], v[92:99], v192\n" \
  "v_lshlrev_b32_sdwa v66, 4, v247 dst_sel:DWORD dst_unused:UNUSED_PAD src0_sel:DWORD src1_sel:WORD_0\n" \
  "ds_read_b128 v[92:95], v66\n" \
  "v_lshlrev_b32_sdwa v67, 4, v247 dst_sel:DWORD dst_unused:UNUSED_PAD src0_sel:DWORD src1_sel:WORD_1\n" \
  "ds_read_b128 v[96:99], v67\n" \
  "s_waitcnt lgkmcnt(10)\n" \
  "v_smfmac_f32_16x16x64_bf16 v[8:11], v[180:183], v[100:107], v192\n" \
  "v_lshlrev_b32_sdwa v64, 4, v248 dst_sel:DWORD dst_unused:UNUSED_PAD src0_sel:DWORD src1_sel:WORD_0\n" \
  "ds_read_b128 v[100:103], v64\n" \
  "v_lshlrev_b32_sdwa v65, 4, v248 dst_sel:DWORD dst_unused:UNUSED_PAD src0_sel:DWORD src1_sel:WORD_1\n" \
  "ds_read_b128 v[104:107], v65\n" \
  "s_waitcnt lgkmcnt(10)\n" \
  "v_smfmac_f32_16x16x64_bf16 v[12:15], v[180:183], v[108:115], v192\n" \
  "v_lshlrev_b32_sdwa v66, 4, v249 dst_sel:DWORD dst_unused:UNUSED_PAD src0_sel:DWORD src1_sel:WORD_0\n" \
  "ds_read_b128 v[108:111], v66\n" \
  "v_lshlrev_b32_sdwa v67, 4, v249 dst_sel:DWORD dst_unused:UNUSED_PAD src0_sel:DWORD src1_sel:WORD_1\n" \
  "ds_read_b128 v[112:115], v67\n" \
  "s_waitcnt vmcnt(0)\n" \
  "s_barrier\n" \
  "s_waitcnt lgkmcnt(10)\n" \
  "v_smfmac_f32_16x16x64_bf16 v[16:19], v[180:183], v[68:75], v192\n" \
  "s_waitcnt lgkmcnt(8)\n" \
  "v_smfmac_f32_16x16x64_bf16 v[20:23], v[180:183], v[76:83], v192\n" \
  "s_waitcnt lgkmcnt(6)\n" \
  "v_smfmac_f32_16x16x64_bf16 v[24:27], v[180:183], v[84:91], v192\n" \
  "s_waitcnt lgkmcnt(4)\n" \
  "v_smfmac_f32_16x16x64_bf16 v[28:31], v[180:183], v[92:99], v192\n" \
  "v_lshlrev_b32_sdwa v64, 4, v208 dst_sel:DWORD dst_unused:UNUSED_PAD src0_sel:DWORD src1_sel:WORD_0\n" \
  "ds_read_b128 v[68:71], v64\n" \
  "v_lshlrev_b32_sdwa v65, 4, v208 dst_sel:DWORD dst_unused:UNUSED_PAD src0_sel:DWORD src1_sel:WORD_1\n" \
  "ds_read_b128 v[72:75], v65\n" \
  "s_waitcnt lgkmcnt(4)\n" \
  "v_smfmac_f32_16x16x64_bf16 v[32:35], v[180:183], v[100:107], v192\n" \
  "v_lshlrev_b32_sdwa v66, 4, v209 dst_sel:DWORD dst_unused:UNUSED_PAD src0_sel:DWORD src1_sel:WORD_0\n" \
  "ds_read_b128 v[76:79], v66\n" \
  "v_lshlrev_b32_sdwa v67, 4, v209 dst_sel:DWORD dst_unused:UNUSED_PAD src0_sel:DWORD src1_sel:WORD_1\n" \
  "ds_read_b128 v[80:83], v67\n" \
  "s_waitcnt lgkmcnt(4)\n" \
  "v_smfmac_f32_16x16x64_bf16 v[36:39], v[180:183], v[108:115], v192\n" \
  "v_lshlrev_b32_sdwa v64, 4, v210 dst_sel:DWORD dst_unused:UNUSED_PAD src0_sel:DWORD src1_sel:WORD_0\n" \
  "ds_read_b128 v[84:87], v64\n" \
  "v_lshlrev_b32_sdwa v65, 4, v210 dst_sel:DWORD dst_unused:UNUSED_PAD src0_sel:DWORD src1_sel:WORD_1\n" \
  "ds_read_b128 v[88:91], v65\n" \
  "s_waitcnt lgkmcnt(4)\n" \
  "v_smfmac_f32_16x16x64_bf16 v[0:3], v[180:183], v[68:75], v192\n" \
  "v_lshlrev_b32_sdwa v66, 4, v211 dst_sel:DWORD dst_unused:UNUSED_PAD src0_sel:DWORD src1_sel:WORD_0\n" \
  "ds_read_b128 v[92:95], v66\n" \
  "v_lshlrev_b32_sdwa v67, 4, v211 dst_sel:DWORD dst_unused:UNUSED_PAD src0_sel:DWORD src1_sel:WORD_1\n" \
  "ds_read_b128 v[96:99], v67\n" \
  "s_waitcnt lgkmcnt(4)\n" \
  "v_smfmac_f32_16x16x64_bf16 v[4:7], v[180:183], v[76:83], v192\n" \
  "v_lshlrev_b32_sdwa v64, 4, v212 dst_sel:DWORD dst_unused:UNUSED_PAD src0_sel:DWORD src1_sel:WORD_0\n" \
  "ds_read_b128 v[100:103], v64\n" \
  "v_lshlrev_b32_sdwa v65, 4, v212 dst_sel:DWORD dst_unused:UNUSED_PAD src0_sel:DWORD src1_sel:WORD_1\n" \
  "ds_read_b128 v[104:107], v65\n" \
  "v_pk_mul_f32 v[128:129], v[0:1], v[0:1]\n" \
  "v_pk_mul_f32 v[130:131], v[2:3], v[2:3]\n" \
  "v_pk_fma_f32 v[128:129], v[128:129], s[52:53], v[124:125]\n" \
  "v_pk_fma_f32 v[130:131], v[130:131], s[52:53], v[124:125]\n" \
  "v_pk_mul_f32 v[128:129], v[128:129], v[0:1]\n" \
  "v_pk_mul_f32 v[130:131], v[130:131], v[2:3]\n" \
  "v_exp_f32 v128, v128\n" \
  "v_exp_f32 v129, v129\n" \
  "v_pk_mul_f32 v[0:1], v[0:1], v[120:121]\n" \
  "v_exp_f32 v130, v130\n" \
  "v_exp_f32 v131, v131\n" \
  "v_pk_mul_f32 v[2:3], v[2:3], v[122:123]\n" \
  "v_pk_add_f32 v[128:129], v[128:129], 1.0 op_sel_hi:[1,0]\n" \
  "v_pk_add_f32 v[130:131], v[130:131], 1.0 op_sel_hi:[1,0]\n" \
  "v_rcp_f32 v128, v128\n" \
  "v_rcp_f32 v129, v129\n" \
  "s_waitcnt lgkmcnt(4)\n" \
  "v_smfmac_f32_16x16x64_bf16 v[8:11], v[180:183], v[84:91], v192\n" \
  "v_lshlrev_b32_sdwa v66, 4, v213 dst_sel:DWORD dst_unused:UNUSED_PAD src0_sel:DWORD src1_sel:WORD_0\n" \
  "ds_read_b128 v[108:111], v66\n" \
  "v_lshlrev_b32_sdwa v67, 4, v213 dst_sel:DWORD dst_unused:UNUSED_PAD src0_sel:DWORD src1_sel:WORD_1\n" \
  "ds_read_b128 v[112:115], v67\n" \
  "v_rcp_f32 v130, v130\n" \
  "v_rcp_f32 v131, v131\n" \
  "v_pk_mul_f32 v[0:1], v[0:1], v[128:129]\n" \
  "v_pk_fma_f32 v[0:1], v[2:3], v[130:131], v[0:1]\n" \
  "v_add_f32 v0, v0, v1\n" \
  "v_pk_mul_f32 v[132:133], v[4:5], v[4:5]\n" \
  "v_pk_mul_f32 v[134:135], v[6:7], v[6:7]\n" \
  "v_pk_fma_f32 v[132:133], v[132:133], s[52:53], v[124:125]\n" \
  "v_pk_fma_f32 v[134:135], v[134:135], s[52:53], v[124:125]\n" \
  "v_pk_mul_f32 v[132:133], v[132:133], v[4:5]\n" \
  "v_pk_mul_f32 v[134:135], v[134:135], v[6:7]\n" \
  "v_exp_f32 v132, v132\n" \
  "v_exp_f32 v133, v133\n" \
  "v_pk_mul_f32 v[4:5], v[4:5], v[120:121]\n" \
  "v_exp_f32 v134, v134\n" \
  "v_exp_f32 v135, v135\n" \
  "s_waitcnt lgkmcnt(4)\n" \
  "v_smfmac_f32_16x16x64_bf16 v[12:15], v[180:183], v[92:99], v192\n" \
  "v_lshlrev_b32_sdwa v64, 4, v214 dst_sel:DWORD dst_unused:UNUSED_PAD src0_sel:DWORD src1_sel:WORD_0\n" \
  "ds_read_b128 v[68:71], v64\n" \
  "v_lshlrev_b32_sdwa v65, 4, v214 dst_sel:DWORD dst_unused:UNUSED_PAD src0_sel:DWORD src1_sel:WORD_1\n" \
  "ds_read_b128 v[72:75], v65\n" \
  "v_pk_mul_f32 v[6:7], v[6:7], v[122:123]\n" \
  "v_pk_add_f32 v[132:133], v[132:133], 1.0 op_sel_hi:[1,0]\n" \
  "v_pk_add_f32 v[134:135], v[134:135], 1.0 op_sel_hi:[1,0]\n" \
  "v_rcp_f32 v132, v132\n" \
  "v_rcp_f32 v133, v133\n" \
  "v_rcp_f32 v134, v134\n" \
  "v_rcp_f32 v135, v135\n" \
  "v_pk_mul_f32 v[4:5], v[4:5], v[132:133]\n" \
  "v_pk_fma_f32 v[4:5], v[6:7], v[134:135], v[4:5]\n" \
  "v_add_f32 v4, v4, v5\n" \
  "v_pk_mul_f32 v[136:137], v[8:9], v[8:9]\n" \
  "v_pk_mul_f32 v[138:139], v[10:11], v[10:11]\n" \
  "v_pk_fma_f32 v[136:137], v[136:137], s[52:53], v[124:125]\n" \
  "v_pk_fma_f32 v[138:139], v[138:139], s[52:53], v[124:125]\n" \
  "v_pk_mul_f32 v[136:137], v[136:137], v[8:9]\n" \
  "v_pk_mul_f32 v[138:139], v[138:139], v[10:11]\n" \
  "s_waitcnt lgkmcnt(4)\n" \
  "v_smfmac_f32_16x16x64_bf16 v[16:19], v[180:183], v[100:107], v192\n" \
  "v_lshlrev_b32_sdwa v66, 4, v215 dst_sel:DWORD dst_unused:UNUSED_PAD src0_sel:DWORD src1_sel:WORD_0\n" \
  "ds_read_b128 v[76:79], v66\n" \
  "v_lshlrev_b32_sdwa v67, 4, v215 dst_sel:DWORD dst_unused:UNUSED_PAD src0_sel:DWORD src1_sel:WORD_1\n" \
  "ds_read_b128 v[80:83], v67\n" \
  "v_exp_f32 v136, v136\n" \
  "v_exp_f32 v137, v137\n" \
  "v_pk_mul_f32 v[8:9], v[8:9], v[120:121]\n" \
  "v_exp_f32 v138, v138\n" \
  "v_exp_f32 v139, v139\n" \
  "v_pk_mul_f32 v[10:11], v[10:11], v[122:123]\n" \
  "v_pk_add_f32 v[136:137], v[136:137], 1.0 op_sel_hi:[1,0]\n" \
  "v_pk_add_f32 v[138:139], v[138:139], 1.0 op_sel_hi:[1,0]\n" \
  "v_rcp_f32 v136, v136\n" \
  "v_rcp_f32 v137, v137\n" \
  "v_rcp_f32 v138, v138\n" \
  "v_rcp_f32 v139, v139\n" \
  "v_pk_mul_f32 v[8:9], v[8:9], v[136:137]\n" \
  "v_pk_fma_f32 v[8:9], v[10:11], v[138:139], v[8:9]\n" \
  "v_add_f32 v8, v8, v9\n" \
  "v_pk_mul_f32 v[140:141], v[12:13], v[12:13]\n" \
  "s_waitcnt lgkmcnt(4)\n" \
  "v_smfmac_f32_16x16x64_bf16 v[20:23], v[180:183], v[108:115], v192\n" \
  "v_lshlrev_b32_sdwa v64, 4, v216 dst_sel:DWORD dst_unused:UNUSED_PAD src0_sel:DWORD src1_sel:WORD_0\n" \
  "ds_read_b128 v[84:87], v64\n" \
  "v_lshlrev_b32_sdwa v65, 4, v216 dst_sel:DWORD dst_unused:UNUSED_PAD src0_sel:DWORD src1_sel:WORD_1\n" \
  "ds_read_b128 v[88:91], v65\n" \
  "v_pk_mul_f32 v[142:143], v[14:15], v[14:15]\n" \
  "v_pk_fma_f32 v[140:141], v[140:141], s[52:53], v[124:125]\n" \
  "v_pk_fma_f32 v[142:143], v[142:143], s[52:53], v[124:125]\n" \
  "v_pk_mul_f32 v[140:141], v[140:141], v[12:13]\n" \
  "v_pk_mul_f32 v[142:143], v[142:143], v[14:15]\n" \
  "v_exp_f32 v140, v140\n" \
  "v_exp_f32 v141, v141\n" \
  "v_pk_mul_f32 v[12:13], v[12:13], v[120:121]\n" \
  "v_exp_f32 v142, v142\n" \
  "v_exp_f32 v143, v143\n" \
  "v_pk_mul_f32 v[14:15], v[14:15], v[122:123]\n" \
  "v_pk_add_f32 v[140:141], v[140:141], 1.0 op_sel_hi:[1,0]\n" \
  "v_pk_add_f32 v[142:143], v[142:143], 1.0 op_sel_hi:[1,0]\n" \
  "v_rcp_f32 v140, v140\n" \
  "v_rcp_f32 v141, v141\n" \
  "v_rcp_f32 v142, v142\n" \
  "s_waitcnt lgkmcnt(4)\n" \
  "v_smfmac_f32_16x16x64_bf16 v[24:27], v[180:183], v[68:75], v192\n" \
  "v_lshlrev_b32_sdwa v66, 4, v217 dst_sel:DWORD dst_unused:UNUSED_PAD src0_sel:DWORD src1_sel:WORD_0\n" \
  "ds_read_b128 v[92:95], v66\n" \
  "v_lshlrev_b32_sdwa v67, 4, v217 dst_sel:DWORD dst_unused:UNUSED_PAD src0_sel:DWORD src1_sel:WORD_1\n" \
  "ds_read_b128 v[96:99], v67\n" \
  "v_rcp_f32 v143, v143\n" \
  "v_pk_mul_f32 v[12:13], v[12:13], v[140:141]\n" \
  "v_pk_fma_f32 v[12:13], v[14:15], v[142:143], v[12:13]\n" \
  "v_add_f32 v12, v12, v13\n" \
  "v_pk_mul_f32 v[128:129], v[16:17], v[16:17]\n" \
  "v_pk_mul_f32 v[130:131], v[18:19], v[18:19]\n" \
  "v_pk_fma_f32 v[128:129], v[128:129], s[52:53], v[124:125]\n" \
  "v_pk_fma_f32 v[130:131], v[130:131], s[52:53], v[124:125]\n" \
  "v_pk_mul_f32 v[128:129], v[128:129], v[16:17]\n" \
  "v_pk_mul_f32 v[130:131], v[130:131], v[18:19]\n" \
  "v_exp_f32 v128, v128\n" \
  "v_exp_f32 v129, v129\n" \
  "v_pk_mul_f32 v[16:17], v[16:17], v[120:121]\n" \
  "v_exp_f32 v130, v130\n" \
  "v_exp_f32 v131, v131\n" \
  "v_pk_mul_f32 v[18:19], v[18:19], v[122:123]\n" \
  "s_waitcnt lgkmcnt(4)\n" \
  "v_smfmac_f32_16x16x64_bf16 v[28:31], v[180:183], v[76:83], v192\n" \
  "v_lshlrev_b32_sdwa v64, 4, v250 dst_sel:DWORD dst_unused:UNUSED_PAD src0_sel:DWORD src1_sel:WORD_0\n" \
  "ds_read_b128 v[100:103], v64\n" \
  "v_lshlrev_b32_sdwa v65, 4, v250 dst_sel:DWORD dst_unused:UNUSED_PAD src0_sel:DWORD src1_sel:WORD_1\n" \
  "ds_read_b128 v[104:107], v65\n" \
  "v_pk_add_f32 v[128:129], v[128:129], 1.0 op_sel_hi:[1,0]\n" \
  "v_pk_add_f32 v[130:131], v[130:131], 1.0 op_sel_hi:[1,0]\n" \
  "v_rcp_f32 v128, v128\n" \
  "v_rcp_f32 v129, v129\n" \
  "v_rcp_f32 v130, v130\n" \
  "v_rcp_f32 v131, v131\n" \
  "v_pk_mul_f32 v[16:17], v[16:17], v[128:129]\n" \
  "v_pk_fma_f32 v[16:17], v[18:19], v[130:131], v[16:17]\n" \
  "v_add_f32 v16, v16, v17\n" \
  "v_pk_mul_f32 v[132:133], v[20:21], v[20:21]\n" \
  "v_pk_mul_f32 v[134:135], v[22:23], v[22:23]\n" \
  "v_pk_fma_f32 v[132:133], v[132:133], s[52:53], v[124:125]\n" \
  "v_pk_fma_f32 v[134:135], v[134:135], s[52:53], v[124:125]\n" \
  "v_pk_mul_f32 v[132:133], v[132:133], v[20:21]\n" \
  "v_pk_mul_f32 v[134:135], v[134:135], v[22:23]\n" \
  "v_exp_f32 v132, v132\n" \
  "s_waitcnt lgkmcnt(4)\n" \
  "v_smfmac_f32_16x16x64_bf16 v[32:35], v[180:183], v[84:91], v192\n" \
  "v_lshlrev_b32_sdwa v66, 4, v218 dst_sel:DWORD dst_unused:UNUSED_PAD src0_sel:DWORD src1_sel:WORD_0\n" \
  "ds_read_b128 v[108:111], v66\n" \
  "v_lshlrev_b32_sdwa v67, 4, v218 dst_sel:DWORD dst_unused:UNUSED_PAD src0_sel:DWORD src1_sel:WORD_1\n" \
  "ds_read_b128 v[112:115], v67\n" \
  "v_exp_f32 v133, v133\n" \
  "v_pk_mul_f32 v[20:21], v[20:21], v[120:121]\n" \
  "v_exp_f32 v134, v134\n" \
  "v_exp_f32 v135, v135\n" \
  "v_pk_mul_f32 v[22:23], v[22:23], v[122:123]\n" \
  "v_pk_add_f32 v[132:133], v[132:133], 1.0 op_sel_hi:[1,0]\n" \
  "v_pk_add_f32 v[134:135], v[134:135], 1.0 op_sel_hi:[1,0]\n" \
  "v_rcp_f32 v132, v132\n" \
  "v_rcp_f32 v133, v133\n" \
  "v_rcp_f32 v134, v134\n" \
  "v_rcp_f32 v135, v135\n" \
  "v_pk_mul_f32 v[20:21], v[20:21], v[132:133]\n" \
  "v_pk_fma_f32 v[20:21], v[22:23], v[134:135], v[20:21]\n" \
  "v_add_f32 v20, v20, v21\n" \
  "v_pk_mul_f32 v[136:137], v[24:25], v[24:25]\n" \
  "v_pk_mul_f32 v[138:139], v[26:27], v[26:27]\n" \
  "s_waitcnt lgkmcnt(4)\n" \
  "v_smfmac_f32_16x16x64_bf16 v[36:39], v[180:183], v[92:99], v192\n" \
  "v_lshlrev_b32_sdwa v64, 4, v251 dst_sel:DWORD dst_unused:UNUSED_PAD src0_sel:DWORD src1_sel:WORD_0\n" \
  "ds_read_b128 v[68:71], v64\n" \
  "v_lshlrev_b32_sdwa v65, 4, v251 dst_sel:DWORD dst_unused:UNUSED_PAD src0_sel:DWORD src1_sel:WORD_1\n" \
  "ds_read_b128 v[72:75], v65\n" \
  "v_pk_fma_f32 v[136:137], v[136:137], s[52:53], v[124:125]\n" \
  "v_pk_fma_f32 v[138:139], v[138:139], s[52:53], v[124:125]\n" \
  "v_pk_mul_f32 v[136:137], v[136:137], v[24:25]\n" \
  "v_pk_mul_f32 v[138:139], v[138:139], v[26:27]\n" \
  "v_exp_f32 v136, v136\n" \
  "v_exp_f32 v137, v137\n" \
  "v_pk_mul_f32 v[24:25], v[24:25], v[120:121]\n" \
  "v_exp_f32 v138, v138\n" \
  "v_exp_f32 v139, v139\n" \
  "v_pk_mul_f32 v[26:27], v[26:27], v[122:123]\n" \
  "v_pk_add_f32 v[136:137], v[136:137], 1.0 op_sel_hi:[1,0]\n" \
  "v_pk_add_f32 v[138:139], v[138:139], 1.0 op_sel_hi:[1,0]\n" \
  "v_rcp_f32 v136, v136\n" \
  "v_rcp_f32 v137, v137\n" \
  "v_rcp_f32 v138, v138\n" \
  "v_rcp_f32 v139, v139\n" \
  "s_waitcnt lgkmcnt(4)\n" \
  "v_smfmac_f32_16x16x64_bf16 v[40:43], v[180:183], v[100:107], v192\n" \
  "v_lshlrev_b32_sdwa v66, 4, v219 dst_sel:DWORD dst_unused:UNUSED_PAD src0_sel:DWORD src1_sel:WORD_0\n" \
  "ds_read_b128 v[76:79], v66\n" \
  "v_lshlrev_b32_sdwa v67, 4, v219 dst_sel:DWORD dst_unused:UNUSED_PAD src0_sel:DWORD src1_sel:WORD_1\n" \
  "ds_read_b128 v[80:83], v67\n" \
  "v_pk_mul_f32 v[24:25], v[24:25], v[136:137]\n" \
  "v_pk_fma_f32 v[24:25], v[26:27], v[138:139], v[24:25]\n" \
  "v_add_f32 v24, v24, v25\n" \
  "v_pk_mul_f32 v[140:141], v[28:29], v[28:29]\n" \
  "v_pk_mul_f32 v[142:143], v[30:31], v[30:31]\n" \
  "v_pk_fma_f32 v[140:141], v[140:141], s[52:53], v[124:125]\n" \
  "v_pk_fma_f32 v[142:143], v[142:143], s[52:53], v[124:125]\n" \
  "v_pk_mul_f32 v[140:141], v[140:141], v[28:29]\n" \
  "v_pk_mul_f32 v[142:143], v[142:143], v[30:31]\n" \
  "v_exp_f32 v140, v140\n" \
  "v_exp_f32 v141, v141\n" \
  "v_pk_mul_f32 v[28:29], v[28:29], v[120:121]\n" \
  "v_exp_f32 v142, v142\n" \
  "v_exp_f32 v143, v143\n" \
  "v_pk_mul_f32 v[30:31], v[30:31], v[122:123]\n" \
  "v_pk_add_f32 v[140:141], v[140:141], 1.0 op_sel_hi:[1,0]\n" \
  "s_waitcnt lgkmcnt(4)\n" \
  "v_smfmac_f32_16x16x64_bf16 v[40:43], v[180:183], v[108:115], v192\n" \
  "v_lshlrev_b32_sdwa v64, 4, v252 dst_sel:DWORD dst_unused:UNUSED_PAD src0_sel:DWORD src1_sel:WORD_0\n" \
  "ds_read_b128 v[84:87], v64\n" \
  "v_lshlrev_b32_sdwa v65, 4, v252 dst_sel:DWORD dst_unused:UNUSED_PAD src0_sel:DWORD src1_sel:WORD_1\n" \
  "ds_read_b128 v[88:91], v65\n" \
  "v_pk_add_f32 v[142:143], v[142:143], 1.0 op_sel_hi:[1,0]\n" \
  "v_rcp_f32 v140, v140\n" \
  "v_rcp_f32 v141, v141\n" \
  "v_rcp_f32 v142, v142\n" \
  "v_rcp_f32 v143, v143\n" \
  "v_pk_mul_f32 v[28:29], v[28:29], v[140:141]\n" \
  "v_pk_fma_f32 v[28:29], v[30:31], v[142:143], v[28:29]\n" \
  "v_add_f32 v28, v28, v29\n" \
  "v_permlane16_swap_b32 v0, v16\n" \
  "v_permlane16_swap_b32 v4, v20\n" \
  "v_permlane16_swap_b32 v8, v24\n" \
  "v_permlane16_swap_b32 v12, v28\n" \
  "v_add_f32 v144, v0, v16\n" \
  "v_add_f32 v145, v4, v20\n" \
  "v_add_f32 v146, v8, v24\n" \
  "v_add_f32 v147, v12, v28\n" \
  "s_waitcnt lgkmcnt(4)\n" \
  "v_smfmac_f32_16x16x64_bf16 v[44:47], v[180:183], v[68:75], v192\n" \
  "v_lshlrev_b32_sdwa v66, 4, v220 dst_sel:DWORD dst_unused:UNUSED_PAD src0_sel:DWORD src1_sel:WORD_0\n" \
  "ds_read_b128 v[92:95], v66\n" \
  "v_lshlrev_b32_sdwa v67, 4, v220 dst_sel:DWORD dst_unused:UNUSED_PAD src0_sel:DWORD src1_sel:WORD_1\n" \
  "ds_read_b128 v[96:99], v67\n" \
  "v_min_u32 v144, -2, v144\n" \
  "v_min_u32 v145, -2, v145\n" \
  "v_min_u32 v146, -2, v146\n" \
  "v_min_u32 v147, -2, v147\n" \
  "global_store_dwordx4 v188, v[144:147], s[40:41] offset:0 sc1\n" \
  "v_pk_mul_f32 v[128:129], v[32:33], v[32:33]\n" \
  "v_pk_mul_f32 v[130:131], v[34:35], v[34:35]\n" \
  "v_pk_fma_f32 v[128:129], v[128:129], s[52:53], v[124:125]\n" \
  "v_pk_fma_f32 v[130:131], v[130:131], s[52:53], v[124:125]\n" \
  "v_pk_mul_f32 v[128:129], v[128:129], v[32:33]\n" \
  "v_pk_mul_f32 v[130:131], v[130:131], v[34:35]\n" \
  "v_exp_f32 v128, v128\n" \
  "v_exp_f32 v129, v129\n" \
  "v_pk_mul_f32 v[32:33], v[32:33], v[120:121]\n" \
  "v_exp_f32 v130, v130\n" \
  "v_exp_f32 v131, v131\n" \
  "s_waitcnt lgkmcnt(4)\n" \
  "v_smfmac_f32_16x16x64_bf16 v[44:47], v[180:183], v[76:83], v192\n" \
  "v_lshlrev_b32_sdwa v64, 4, v253 dst_sel:DWORD dst_unused:UNUSED_PAD src0_sel:DWORD src1_sel:WORD_0\n" \
  "ds_read_b128 v[100:103], v64\n" \
  "v_lshlrev_b32_sdwa v65, 4, v253 dst_sel:DWORD dst_unused:UNUSED_PAD src0_sel:DWORD src1_sel:WORD_1\n" \
  "ds_read_b128 v[104:107], v65\n" \
  "v_pk_mul_f32 v[34:35], v[34:35], v[122:123]\n" \
  "v_pk_add_f32 v[128:129], v[128:129], 1.0 op_sel_hi:[1,0]\n" \
  "v_pk_add_f32 v[130:131], v[130:131], 1.0 op_sel_hi:[1,0]\n" \
  "v_rcp_f32 v128, v128\n" \
  "v_rcp_f32 v129, v129\n" \
  "v_rcp_f32 v130, v130\n" \
  "v_rcp_f32 v131, v131\n" \
  "v_pk_mul_f32 v[32:33], v[32:33], v[128:129]\n" \
  "v_pk_fma_f32 v[32:33], v[34:35], v[130:131], v[32:33]\n" \
  "v_add_f32 v32, v32, v33\n" \
  "v_pk_mul_f32 v[132:133], v[36:37], v[36:37]\n" \
  "v_pk_mul_f32 v[134:135], v[38:39], v[38:39]\n" \
  "v_pk_fma_f32 v[132:133], v[132:133], s[52:53], v[124:125]\n" \
  "v_pk_fma_f32 v[134:135], v[134:135], s[52:53], v[124:125]\n" \
  "v_pk_mul_f32 v[132:133], v[132:133], v[36:37]\n" \
  "v_pk_mul_f32 v[134:135], v[134:135], v[38:39]\n" \
  "s_waitcnt lgkmcnt(4)\n" \
  "v_smfmac_f32_16x16x64_bf16 v[48:51], v[180:183], v[84:91], v192\n" \
  "v_lshlrev_b32_sdwa v66, 4, v221 dst_sel:DWORD dst_unused:UNUSED_PAD src0_sel:DWORD src1_sel:WORD_0\n" \
  "ds_read_b128 v[108:111], v66\n" \
  "v_lshlrev_b32_sdwa v67, 4, v221 dst_sel:DWORD dst_unused:UNUSED_PAD src0_sel:DWORD src1_sel:WORD_1\n" \
  "ds_read_b128 v[112:115], v67\n" \
  "v_exp_f32 v132, v132\n" \
  "v_exp_f32 v133, v133\n" \
  "v_pk_mul_f32 v[36:37], v[36:37], v[120:121]\n" \
  "v_exp_f32 v134, v134\n" \
  "v_exp_f32 v135, v135\n" \
  "v_pk_mul_f32 v[38:39], v[38:39], v[122:123]\n" \
  "v_pk_add_f32 v[132:133], v[132:133], 1.0 op_sel_hi:[1,0]\n" \
  "v_pk_add_f32 v[134:135], v[134:135], 1.0 op_sel_hi:[1,0]\n" \
  "v_rcp_f32 v132, v132\n" \
  "v_rcp_f32 v133, v133\n" \
  "v_rcp_f32 v134, v134\n" \
  "v_rcp_f32 v135, v135\n" \
  "v_pk_mul_f32 v[36:37], v[36:37], v[132:133]\n" \
  "v_pk_fma_f32 v[36:37], v[38:39], v[134:135], v[36:37]\n" \
  "v_add_f32 v36, v36, v37\n" \
  "v_pk_mul_f32 v[136:137], v[40:41], v[40:41]\n" \
  "s_waitcnt lgkmcnt(4)\n" \
  "v_smfmac_f32_16x16x64_bf16 v[48:51], v[180:183], v[92:99], v192\n" \
  "v_lshlrev_b32_sdwa v64, 4, v254 dst_sel:DWORD dst_unused:UNUSED_PAD src0_sel:DWORD src1_sel:WORD_0\n" \
  "ds_read_b128 v[68:71], v64\n" \
  "v_lshlrev_b32_sdwa v65, 4, v254 dst_sel:DWORD dst_unused:UNUSED_PAD src0_sel:DWORD src1_sel:WORD_1\n" \
  "ds_read_b128 v[72:75], v65\n" \
  "v_pk_mul_f32 v[138:139], v[42:43], v[42:43]\n" \
  "v_pk_fma_f32 v[136:137], v[136:137], s[52:53], v[124:125]\n" \
  "v_pk_fma_f32 v[138:139], v[138:139], s[52:53], v[124:125]\n" \
  "v_pk_mul_f32 v[136:137], v[136:137], v[40:41]\n" \
  "v_pk_mul_f32 v[138:139], v[138:139], v[42:43]\n" \
  "v_exp_f32 v136, v136\n" \
  "v_exp_f32 v137, v137\n" \
  "v_pk_mul_f32 v[40:41], v[40:41], v[120:121]\n" \
  "v_exp_f32 v138, v138\n" \
  "v_exp_f32 v139, v139\n" \
  "v_pk_mul_f32 v[42:43], v[42:43], v[122:123]\n" \
  "v_pk_add_f32 v[136:137], v[136:137], 1.0 op_sel_hi:[1,0]\n" \
  "v_pk_add_f32 v[138:139], v[138:139], 1.0 op_sel_hi:[1,0]\n" \
  "v_rcp_f32 v136, v136\n" \
  "v_rcp_f32 v137, v137\n" \
  "v_rcp_f32 v138, v138\n" \
  "s_waitcnt lgkmcnt(4)\n" \
  "v_smfmac_f32_16x16x64_bf16 v[52:55], v[180:183], v[100:107], v192\n" \
  "v_lshlrev_b32_sdwa v66, 4, v222 dst_sel:DWORD dst_unused:UNUSED_PAD src0_sel:DWORD src1_sel:WORD_0\n" \
  "ds_read_b128 v[76:79], v66\n" \
  "v_lshlrev_b32_sdwa v67, 4, v222 dst_sel:DWORD dst_unused:UNUSED_PAD src0_sel:DWORD src1_sel:WORD_1\n" \
  "ds_read_b128 v[80:83], v67\n" \
  "v_rcp_f32 v139, v139\n" \
  "v_pk_mul_f32 v[40:41], v[40:41], v[136:137]\n" \
  "v_pk_fma_f32 v[40:41], v[42:43], v[138:139], v[40:41]\n" \
  "v_add_f32 v40, v40, v41\n" \
  "v_pk_mul_f32 v[140:141], v[44:45], v[44:45]\n" \
  "v_pk_mul_f32 v[142:143], v[46:47], v[46:47]\n" \
  "v_pk_fma_f32 v[140:141], v[140:141], s[52:53], v[124:125]\n" \
  "v_pk_fma_f32 v[142:143], v[142:143], s[52:53], v[124:125]\n" \
  "v_pk_mul_f32 v[140:141], v[140:141], v[44:45]\n" \
  "v_pk_mul_f32 v[142:143], v[142:143], v[46:47]\n" \
  "v_exp_f32 v140, v140\n" \
  "v_exp_f32 v141, v141\n" \
  "v_pk_mul_f32 v[44:45], v[44:45], v[120:121]\n" \
  "v_exp_f32 v142, v142\n" \
  "v_exp_f32 v143, v143\n" \
  "v_pk_mul_f32 v[46:47], v[46:47], v[122:123]\n" \
  "s_waitcnt lgkmcnt(4)\n" \
  "v_smfmac_f32_16x16x64_bf16 v[52:55], v[180:183], v[108:115], v192\n" \
  "v_lshlrev_b32_sdwa v64, 4, v255 dst_sel:DWORD dst_unused:UNUSED_PAD src0_sel:DWORD src1_sel:WORD_0\n" \
  "ds_read_b128 v[84:87], v64\n" \
  "v_lshlrev_b32_sdwa v65, 4, v255 dst_sel:DWORD dst_unused:UNUSED_PAD src0_sel:DWORD src1_sel:WORD_1\n" \
  "ds_read_b128 v[88:91], v65\n" \
  "v_pk_add_f32 v[140:141], v[140:141], 1.0 op_sel_hi:[1,0]\n" \
  "v_pk_add_f32 v[142:143], v[142:143], 1.0 op_sel_hi:[1,0]\n" \
  "v_rcp_f32 v140, v140\n" \
  "v_rcp_f32 v141, v141\n" \
  "v_rcp_f32 v142, v142\n" \
  "v_rcp_f32 v143, v143\n" \
  "v_pk_mul_f32 v[44:45], v[44:45], v[140:141]\n" \
  "v_pk_fma_f32 v[44:45], v[46:47], v[142:143], v[44:45]\n" \
  "v_add_f32 v44, v44, v45\n" \
  "v_pk_mul_f32 v[128:129], v[48:49], v[48:49]\n" \
  "v_pk_mul_f32 v[130:131], v[50:51], v[50:51]\n" \
  "v_pk_fma_f32 v[128:129], v[128:129], s[52:53], v[124:125]\n" \
  "v_pk_fma_f32 v[130:131], v[130:131], s[52:53], v[124:125]\n" \
  "v_pk_mul_f32 v[128:129], v[128:129], v[48:49]\n" \
  "v_pk_mul_f32 v[130:131], v[130:131], v[50:51]\n" \
  "v_exp_f32 v128, v128\n" \
  "s_waitcnt lgkmcnt(4)\n" \
  "v_smfmac_f32_16x16x64_bf16 v[56:59], v[180:183], v[68:75], v192\n" \
  "v_lshlrev_b32_sdwa v66, 4, v223 dst_sel:DWORD dst_unused:UNUSED_PAD src0_sel:DWORD src1_sel:WORD_0\n" \
  "ds_read_b128 v[92:95], v66\n" \
  "v_lshlrev_b32_sdwa v67, 4, v223 dst_sel:DWORD dst_unused:UNUSED_PAD src0_sel:DWORD src1_sel:WORD_1\n" \
  "ds_read_b128 v[96:99], v67\n" \
  "v_exp_f32 v129, v129\n" \
  "v_pk_mul_f32 v[48:49], v[48:49], v[120:121]\n" \
  "v_exp_f32 v130, v130\n" \
  "v_exp_f32 v131, v131\n" \
  "v_pk_mul_f32 v[50:51], v[50:51], v[122:123]\n" \
  "v_pk_add_f32 v[128:129], v[128:129], 1.0 op_sel_hi:[1,0]\n" \
  "v_pk_add_f32 v[130:131], v[130:131], 1.0 op_sel_hi:[1,0]\n" \
  "v_rcp_f32 v128, v128\n" \
  "v_rcp_f32 v129, v129\n" \
  "v_rcp_f32 v130, v130\n" \
  "v_rcp_f32 v131, v131\n" \
  "v_pk_mul_f32 v[48:49], v[48:49], v[128:129]\n" \
  "v_pk_fma_f32 v[48:49], v[50:51], v[130:131], v[48:49]\n" \
  "v_add_f32 v48, v48, v49\n" \
  "v_pk_mul_f32 v[132:133], v[52:53], v[52:53]\n" \
  "v_pk_mul_f32 v[134:135], v[54:55], v[54:55]\n" \
  "s_waitcnt lgkmcnt(4)\n" \
  "v_smfmac_f32_16x16x64_bf16 v[56:59], v[180:183], v[76:83], v192\n" \
  "v_pk_fma_f32 v[132:133], v[132:133], s[52:53], v[124:125]\n" \
  "v_pk_fma_f32 v[134:135], v[134:135], s[52:53], v[124:125]\n" \
  "v_pk_mul_f32 v[132:133], v[132:133], v[52:53]\n" \
  "v_pk_mul_f32 v[134:135], v[134:135], v[54:55]\n" \
  "v_exp_f32 v132, v132\n" \
  "v_exp_f32 v133, v133\n" \
  "v_pk_mul_f32 v[52:53], v[52:53], v[120:121]\n" \
  "v_exp_f32 v134, v134\n" \
  "v_exp_f32 v135, v135\n" \
  "v_pk_mul_f32 v[54:55], v[54:55], v[122:123]\n" \
  "v_pk_add_f32 v[132:133], v[132:133], 1.0 op_sel_hi:[1,0]\n" \
  "v_pk_add_f32 v[134:135], v[134:135], 1.0 op_sel_hi:[1,0]\n" \
  "v_rcp_f32 v132, v132\n" \
  "v_rcp_f32 v133, v133\n" \
  "v_rcp_f32 v134, v134\n" \
  "v_rcp_f32 v135, v135\n" \
  "v_pk_mul_f32 v[52:53], v[52:53], v[132:133]\n" \
  "v_pk_fma_f32 v[52:53], v[54:55], v[134:135], v[52:53]\n" \
  "v_add_f32 v52, v52, v53\n" \
  "s_waitcnt lgkmcnt(2)\n" \
  "v_smfmac_f32_16x16x64_bf16 v[60:63], v[180:183], v[84:91], v192\n" \
  "v_pk_mul_f32 v[136:137], v[56:57], v[56:57]\n" \
  "v_pk_mul_f32 v[138:139], v[58:59], v[58:59]\n" \
  "v_pk_fma_f32 v[136:137], v[136:137], s[52:53], v[124:125]\n" \
  "v_pk_fma_f32 v[138:139], v[138:139], s[52:53], v[124:125]\n" \
  "v_pk_mul_f32 v[136:137], v[136:137], v[56:57]\n" \
  "v_pk_mul_f32 v[138:139], v[138:139], v[58:59]\n" \
  "v_exp_f32 v136, v136\n" \
  "v_exp_f32 v137, v137\n" \
  "v_pk_mul_f32 v[56:57], v[56:57], v[120:121]\n" \
  "v_exp_f32 v138, v138\n" \
  "v_exp_f32 v139, v139\n" \
  "v_pk_mul_f32 v[58:59], v[58:59], v[122:123]\n" \
  "v_pk_add_f32 v[136:137], v[136:137], 1.0 op_sel_hi:[1,0]\n" \
  "v_pk_add_f32 v[138:139], v[138:139], 1.0 op_sel_hi:[1,0]\n" \
  "v_rcp_f32 v136, v136\n" \
  "v_rcp_f32 v137, v137\n" \
  "v_rcp_f32 v138, v138\n" \
  "v_rcp_f32 v139, v139\n" \
  "v_pk_mul_f32 v[56:57], v[56:57], v[136:137]\n" \
  "v_pk_fma_f32 v[56:57], v[58:59], v[138:139], v[56:57]\n" \
  "v_add_f32 v56, v56, v57\n" \
  "s_waitcnt lgkmcnt(0)\n" \
  "v_smfmac_f32_16x16x64_bf16 v[60:63], v[180:183], v[92:99], v192\n" \
  "s_nop 7\n" \
  "v_pk_mul_f32 v[140:141], v[60:61], v[60:61]\n" \
  "v_pk_mul_f32 v[142:143], v[62:63], v[62:63]\n" \
  "v_pk_fma_f32 v[140:141], v[140:141], s[52:53], v[124:125]\n" \
  "v_pk_fma_f32 v[142:143], v[142:143], s[52:53], v[124:125]\n" \
  "v_pk_mul_f32 v[140:141], v[140:141], v[60:61]\n" \
  "v_pk_mul_f32 v[142:143], v[142:143], v[62:63]\n" \
  "v_exp_f32 v140, v140\n" \
  "v_exp_f32 v141, v141\n" \
  "v_pk_mul_f32 v[60:61], v[60:61], v[120:121]\n" \
  "v_exp_f32 v142, v142\n" \
  "v_exp_f32 v143, v143\n" \
  "v_pk_mul_f32 v[62:63], v[62:63], v[122:123]\n" \
  "v_pk_add_f32 v[140:141], v[140:141], 1.0 op_sel_hi:[1,0]\n" \
  "v_pk_add_f32 v[142:143], v[142:143], 1.0 op_sel_hi:[1,0]\n" \
  "v_rcp_f32 v140, v140\n" \
  "v_rcp_f32 v141, v141\n" \
  "v_rcp_f32 v142, v142\n" \
  "v_rcp_f32 v143, v143\n" \
  "v_pk_mul_f32 v[60:61], v[60:61], v[140:141]\n" \
  "v_pk_fma_f32 v[60:61], v[62:63], v[142:143], v[60:61]\n" \
  "v_add_f32 v60, v60, v61\n" \
  "v_permlane16_swap_b32 v32, v48\n" \
  "v_permlane16_swap_b32 v36, v52\n" \
  "v_permlane16_swap_b32 v40, v56\n" \
  "v_permlane16_swap_b32 v44, v60\n" \
  "v_add_f32 v148, v32, v48\n" \
  "v_add_f32 v149, v36, v52\n" \
  "v_add_f32 v150, v40, v56\n" \
  "v_add_f32 v151, v44, v60\n" \
  "v_min_u32 v148, -2, v148\n" \
  "v_min_u32 v149, -2, v149\n" \
  "v_min_u32 v150, -2, v150\n" \
  "v_min_u32 v151, -2, v151\n" \
  "global_store_dwordx4 v188, v[148:151], s[40:41] offset:1024 sc1\n" \
  "v_lshrrev_b32 v0, 4, v188\n" \
  "v_lshrrev_b32 v1, 4, v0\n" \
  "v_and_b32 v2, 15, v0\n" \
  "v_lshlrev_b32 v2, 4, v2\n" \
  "v_lshl_add_u32 v1, v1, 17, v2\n" \
  "s_lshl_b32 s35, s27, 19\n" \
  "s_lshl_b32 s36, s26, 8\n" \
  "s_add_u32 s35, s35, s36\n" \
  "s_add_u32 s35, s35, 105676800\n" \
  "s_add_u32 s40, s24, s35\n" \
  "s_addc_u32 s41, s25, 0\n" \
  "s_add_u32 s42, s40, 65536\n" \
  "s_addc_u32 s43, s41, 0\n" \
  "s_add_u32 s44, s24, 109871104\n" \
  "s_addc_u32 s45, s25, 0\n" \
  "s_add_u32 s46, s44, 65536\n" \
  "s_addc_u32 s47, s45, 0\n" \
  "s_lshr_b32 s35, s26, 2\n" \
  "s_lshl_b32 s35, s35, 8\n" \
  "s_and_b32 s36, s26, 1\n" \
  "s_lshl_b32 s36, s36, 7\n" \
  "s_add_u32 s35, s35, s36\n" \
  "s_and_b32 s36, s26, 2\n" \
  "s_lshl3_add_u32 s35, s36, s35\n" \
  "v_and_b32 v2, 3, v0\n" \
  "v_lshrrev_b32 v3, 2, v0\n" \
  "v_lshl_add_u32 v2, v2, 5, v3\n" \
  "v_add_u32 v2, s35, v2\n" \
  "v_lshlrev_b32 v3, 2, v2\n" \
  "s_cmp_eq_u32 s27, 0\n" \
  "s_cselect_b32 s35, -1, 0\n" \
  "v_and_b32 v3, s35, v3\n" \
  "v_mov_b32 v4, s6\n" \
  "s_mov_b32 s5, 0x10000\n" \
  "s_barrier\n" \
  "s_sleep 10\n" \
  "LF_poll:\n" \
  "global_load_dwordx4 v[8:11], v1, s[40:41] sc1\n" \
  "global_load_dwordx4 v[12:15], v1, s[42:43] sc1\n" \
  "global_load_dword v16, v3, s[44:45] sc1\n" \
  "global_load_dword v17, v3, s[46:47] sc1\n" \
  "s_waitcnt vmcnt(0)\n" \
  "v_max3_u32 v5, v8, v9, v10\n" \
  "v_max3_u32 v6, v11, v12, v13\n" \
  "v_max3_u32 v5, v5, v14, v15\n" \
  "v_max3_u32 v6, v6, v16, v17\n" \
  "v_max_u32 v5, v5, v6\n" \
  "v_cmp_eq_u32 vcc, -1, v5\n" \
  "s_sub_u32 s5, s5, 1\n" \
  "s_cbranch_scc1 LF_go\n" \
  "s_nop 2\n" \
  "s_cbranch_vccz LF_go\n" \
  "s_mov_b64 exec, vcc\n" \
  "s_sleep 2\n" \
  "s_branch LF_poll\n" \
  "LF_go:\n" \
  "s_mov_b64 exec, -1\n" \
  "v_pk_add_f32 v[8:9], v[8:9], v[12:13]\n" \
  "v_pk_add_f32 v[10:11], v[10:11], v[14:15]\n" \
  "s_nop 0\n" \
  "v_permlane16_swap_b32 v8, v9\n" \
  "v_permlane16_swap_b32 v10, v11\n" \
  "v_add_f32 v8, v8, v9\n" \
  "v_add_f32 v10, v10, v11\n" \
  "s_nop 1\n" \
  "v_permlane32_swap_b32 v8, v10\n" \
  "v_add_f32 v8, v8, v10\n" \
  "v_and_b32 v5, 15, v0\n" \
  "v_lshrrev_b32 v6, 4, v0\n" \
  "v_lshl_add_u32 v5, v5, 2, v6\n" \
  "s_lshl_b32 s35, s27, 8\n" \
  "v_lshl_add_u32 v5, v5, 2, s35\n" \
  "ds_write_b32 v5, v8\n" \
  "s_waitcnt lgkmcnt(0)\n" \
  "s_barrier\n" \
  "s_cmp_lg_u32 s27, 0\n" \
  "s_cbranch_scc1 LF_end\n" \
  "v_lshlrev_b32 v5, 2, v0\n" \
  "ds_read2st64_b32 v[8:9], v5 offset0:0 offset1:1\n" \
  "ds_read2st64_b32 v[10:11], v5 offset0:2 offset1:3\n" \
  "ds_read2st64_b32 v[12:13], v5 offset0:4 offset1:5\n" \
  "ds_read2st64_b32 v[14:15], v5 offset0:6 offset1:7\n" \
  "v_add_f32 v16, v16, v17\n" \
  "v_lshlrev_b32 v4, 16, v4\n" \
  "v_cvt_pk_bf16_f32 v16, v16, v16\n" \
  "v_lshlrev_b32 v2, 1, v2\n" \
  "s_waitcnt lgkmcnt(0)\n" \
  "v_pk_add_f32 v[8:9], v[8:9], v[10:11]\n" \
  "v_pk_add_f32 v[12:13], v[12:13], v[14:15]\n" \
  "v_pk_add_f32 v[8:9], v[8:9], v[12:13]\n" \
  "v_add_f32 v8, v8, v9\n" \
  "v_cvt_pk_bf16_f32 v8, v8, v8\n" \
  "v_lshlrev_b32 v8, 16, v8\n" \
  "v_add_f32 v8, v8, v4\n" \
  "v_cvt_pk_bf16_f32 v8, v8, v8\n" \
  "v_lshlrev_b32 v8, 16, v8\n" \
  "v_lshlrev_b32 v16, 16, v16\n" \
  "v_add_f32 v8, v8, v16\n" \
  "v_cvt_pk_bf16_f32 v8, v8, v8\n" \
  "global_store_short v2, v8, s[22:23]\n" \
  "LF_end:\n" \
  "s_branch LZ_end\n" \
  "LG_h:\n" \
  "s_cmp_eq_u32 s51, 4\n" \
  "s_cbranch_scc1 LG_chk\n" \
  "s_cmp_lg_u32 s51, 7\n" \
  "s_cbranch_scc1 LG_ret\n" \
  "s_barrier\n" \
  "s_mov_b32 exec_hi, 0\n" \
  "global_atomic_add v196, v199, s[48:49] offset:3076 sc1\n" \
  "s_mov_b64 exec, -1\n" \
  "s_branch LG_aft\n" \
  "LG_pol:\n" \
  "s_mov_b64 exec, 0xff\n" \
  "global_load_dword v200, v198, s[48:49] offset:3076 sc1\n" \
  "s_mov_b64 exec, -1\n" \
  "s_branch LG_rt2\n" \
  "LG_chk:\n" \
  "s_mov_b32 s5, 0x10000\n" \
  "LG_p:\n" \
  "s_mov_b64 exec, 0xff\n" \
  "v_cmp_le_u32 vcc, 32, v200\n" \
  "s_mov_b64 exec, -1\n" \
  "s_sub_u32 s5, s5, 1\n" \
  "s_cselect_b32 s35, 0xff, vcc_lo\n" \
  "s_cmp_eq_u32 s35, 0xff\n" \
  "s_cbranch_scc1 LG_p_x\n" \
  "s_sleep 2\n" \
  "s_mov_b64 exec, 0xff\n" \
  "global_load_dword v200, v198, s[48:49] offset:3076 sc1\n" \
  "s_mov_b64 exec, -1\n" \
  "s_waitcnt vmcnt(0)\n" \
  "s_branch LG_p\n" \
  "LG_p_x:\n" \
  "s_branch LG_ret\n" \
  "LZ_end:\n" \
  "s_waitcnt vmcnt(0) lgkmcnt(0)\n"

#define R10(p, b) p #b "0", p #b "1", p #b "2", p #b "3", p #b "4", p #b "5", p #b "6", p #b "7", p #b "8", p #b "9"
__global__ __launch_bounds__(512, 2) void k1(const int* __restrict__ idx, const unsigned short* __restrict__ table,
                                             const unsigned short* __restrict__ w_p, const unsigned short* __restrict__ w1,
                                             const unsigned short* __restrict__ b1, const unsigned short* __restrict__ w2,
                                             const unsigned short* __restrict__ b2, __hip_bfloat16* __restrict__ out, char* __restrict__ ws) {
  __shared__ __attribute__((aligned(1024))) char smem[163840];
  unsigned tid = threadIdx.x;
  asm volatile(K_ASM
               : "+{v0}"(tid)
               : "{s[0:1]}"(__builtin_amdgcn_kernarg_segment_ptr()), "{s26}"(blockIdx.x), "s"((unsigned)(uintptr_t)smem)
               : "memory", "scc", "vcc", "s3", "s4", "s5", "s6", "s7", "s8", "s9", R10("s", 1), "s20", "s21", "s22", "s23", "s24", "s25", "s27", "s28", "s29", "s30", "s31", "s33", "s34", "s35", "s36", "s37", "s38", "s39", R10("s", 4), R10("s", 5), R10("s", 6), R10("s", 7), R10("s", 8), R10("s", 9),
                 "v1", "v2", "v3", "v4", "v5", "v6", "v7", "v8", "v9", R10("v", 1), R10("v", 2), R10("v", 3), R10("v", 4), R10("v", 5), R10("v", 6), R10("v", 7), R10("v", 8), R10("v", 9),
                 R10("v", 10), R10("v", 11), R10("v", 12), R10("v", 13), R10("v", 14), R10("v", 15), R10("v", 16), R10("v", 17), R10("v", 18), R10("v", 19),
                 R10("v", 20), R10("v", 21), R10("v", 22), R10("v", 23), R10("v", 24), "v250", "v251", "v252", "v253", "v254", "v255");
}

extern "C" void kernel_launch(void* const* d_in, const int* in_sizes, int n_in, void* d_out, int out_size, void* d_ws,
                              size_t ws_size, hipStream_t stream) {
  k1<<<dim3(256), dim3(512), 0, stream>>>((const int*)d_in[0], (const unsigned short*)d_in[1], (const unsigned short*)d_in[2], (const unsigned short*)d_in[3],
                                          (const unsigned short*)d_in[4], (const unsigned short*)d_in[5], (const unsigned short*)d_in[6], (__hip_bfloat16*)d_out, (char*)d_ws);
}
```

```cpp
#include <hip/hip_runtime.h>
#include <hip/hip_bf16.h>
#include <stdint.h>

#define K_ASM \
  "LZ_first:\n" \
  "s_load_dwordx16 s[8:23], s[0:1], 0x0\n" \
  "s_load_dwordx2 s[24:25], s[0:1], 0x40\n" \
  "v_readfirstlane_b32 s27, v0\n" \
  "v_and_b32 v255, 63, v0\n" \
  "s_and_b32 s29, s26, 7\n" \
  "s_lshr_b32 s30, s26, 3\n" \
  "s_lshr_b32 s27, s27, 6\n" \
  "s_and_b32 s31, s30, 1\n" \
  "s_and_b32 s28, s27, 3\n" \
  "s_lshr_b32 s35, s30, 1\n" \
  "s_lshl3_add_u32 s7, s35, s29\n" \
  "v_lshrrev_b32 v1, 3, v255\n" \
  "v_and_b32 v2, 7, v255\n" \
  "v_lshlrev_b32 v1, 8, v1\n" \
  "v_lshl_or_b32 v254, v2, 4, v1\n" \
  "s_mul_i32 s36, s7, 37\n" \
  "s_and_b32 s36, s36, 63\n" \
  "s_add_u32 s36, s36, s28\n" \
  "s_cmp_ge_u32 s27, 4\n" \
  "s_cbranch_scc1 LB_start\n" \
  "s_setprio 3\n" \
  "v_and_b32 v252, 15, v255\n" \
  "v_lshrrev_b32 v253, 4, v255\n" \
  "v_mul_u32_u24 v2, 400, v253\n" \
  "v_min_u32 v3, 8, v252\n" \
  "v_lshl_add_u32 v250, v252, 4, v2\n" \
  "v_lshl_add_u32 v251, v3, 4, v2\n" \
  "s_lshl2_add_u32 s36, s7, s28\n" \
  "s_mul_i32 s36, s36, 12800\n" \
  "s_lshl_b32 s35, s28, 10\n" \
  "v_lshl_add_u32 v1, v255, 4, s35\n" \
  "s_add_u32 m0, s35, 138240\n" \
  "s_waitcnt lgkmcnt(0)\n" \
  "s_add_u32 s44, s8, s36\n" \
  "s_addc_u32 s45, s9, 0\n" \
  "global_load_lds_dwordx4 v1, s[12:13]\n" \
  "s_add_u32 m0, m0, 0x1000\n" \
  "v_add_u32 v1, 0x1000, v1\n" \
  "global_load_lds_dwordx4 v1, s[12:13]\n" \
  "s_add_u32 m0, m0, 0x1000\n" \
  "v_add_u32 v1, 0x1000, v1\n" \
  "global_load_lds_dwordx4 v1, s[12:13]\n" \
  "s_add_u32 m0, m0, 0x1000\n" \
  "v_add_u32 v1, 0x1000, v1\n" \
  "global_load_lds_dwordx4 v1, s[12:13]\n" \
  "s_add_u32 m0, m0, 0x1000\n" \
  "v_add_u32 v1, 0x1000, v1\n" \
  "v_min_u32 v1, 19984, v1\n" \
  "global_load_lds_dwordx4 v1, s[12:13]\n" \
  "global_load_dwordx4 v[232:235], v250, s[44:45] offset:0\n" \
  "global_load_dwordx4 v[236:239], v251, s[44:45] offset:256\n" \
  "global_load_dwordx4 v[240:243], v250, s[44:45] offset:1600\n" \
  "global_load_dwordx4 v[244:247], v251, s[44:45] offset:1856\n" \
  "s_setprio 1\n" \
  "v_lshrrev_b32 v1, 2, v252\n" \
  "v_and_b32 v2, 3, v252\n" \
  "v_cmp_eq_u32 vcc, v1, v253\n" \
  "v_mov_b32 v4, 0x3f80\n" \
  "v_and_b32 v13, 1, v253\n" \
  "v_lshlrev_b32 v14, 1, v13\n" \
  "v_xor_b32 v2, v2, v14\n" \
  "v_cndmask_b32 v2, 4, v2, vcc\n" \
  "v_cmp_eq_u32 vcc, 0, v2\n" \
  "s_nop 1\n" \
  "v_cndmask_b32 v216, 0, v4, vcc\n" \
  "v_lshlrev_b32 v220, 16, v216\n" \
  "v_cmp_eq_u32 vcc, 1, v2\n" \
  "s_nop 1\n" \
  "v_cndmask_b32 v217, 0, v4, vcc\n" \
  "v_lshlrev_b32 v221, 16, v217\n" \
  "v_cmp_eq_u32 vcc, 2, v2\n" \
  "s_nop 1\n" \
  "v_cndmask_b32 v218, 0, v4, vcc\n" \
  "v_lshlrev_b32 v222, 16, v218\n" \
  "v_cmp_eq_u32 vcc, 3, v2\n" \
  "s_nop 1\n" \
  "v_cndmask_b32 v219, 0, v4, vcc\n" \
  "v_lshlrev_b32 v223, 16, v219\n" \
  "v_and_b32 v1, 2, v252\n" \
  "v_lshlrev_b32 v1, 3, v1\n" \
  "v_lshlrev_b32 v1, v1, v4\n" \
  "v_and_b32 v2, 0x31, v255\n" \
  "v_mov_b64 v[224:225], 0\n" \
  "v_cmp_eq_u32 vcc, 0, v2\n" \
  "s_nop 1\n" \
  "v_cndmask_b32 v226, 0, v1, vcc\n" \
  "v_cmp_eq_u32 vcc, 1, v2\n" \
  "s_nop 1\n" \
  "v_cndmask_b32 v227, 0, v1, vcc\n" \
  "v_mul_u32_u24 v5, 400, v252\n" \
  "v_add_u32 v5, 138240, v5\n" \
  "v_lshl_add_u32 v6, v253, 4, v5\n" \
  "v_add_u32 v7, 6400, v5\n" \
  "v_add_u32 v8, 6400, v6\n" \
  "v_add_u32 v9, 12800, v5\n" \
  "v_add_u32 v10, 12800, v6\n" \
  "v_bfe_u32 v1, v252, 1, 1\n" \
  "v_and_b32 v2, 1, v252\n" \
  "v_lshl_add_u32 v1, v2, 1, v1\n" \
  "v_mul_u32_u24 v1, 200, v1\n" \
  "v_add_u32 v11, 157440, v1\n" \
  "v_lshl_add_u32 v12, v253, 4, v11\n" \
  "v_lshlrev_b32 v13, 3, v13\n" \
  "v_sub_u32 v16, v5, v13\n" \
  "v_add_u32 v5, v5, v13\n" \
  "v_sub_u32 v17, v6, v13\n" \
  "v_add_u32 v6, v6, v13\n" \
  "v_sub_u32 v18, v7, v13\n" \
  "v_add_u32 v7, v7, v13\n" \
  "v_sub_u32 v19, v8, v13\n" \
  "v_add_u32 v8, v8, v13\n" \
  "v_sub_u32 v20, v9, v13\n" \
  "v_add_u32 v9, v9, v13\n" \
  "v_sub_u32 v21, v10, v13\n" \
  "v_add_u32 v10, v10, v13\n" \
  "v_sub_u32 v22, v11, v13\n" \
  "v_add_u32 v11, v11, v13\n" \
  "v_sub_u32 v23, v12, v13\n" \
  "v_add_u32 v12, v12, v13\n" \
  "v_lshlrev_b32 v188, 4, v253\n" \
  "v_and_b32 v96, 4, v252\n" \
  "v_lshl_add_u32 v96, v253, 5, v96\n" \
  "v_mov_b32 v191, 0x40000000\n" \
  "v_cmp_gt_u32 vcc, 8, v252\n" \
  "v_mov_b32 v193, 0x4444\n" \
  "v_mov_b32 v98, 0xeeee\n" \
  "v_cndmask_b32 v189, v191, v96, vcc\n" \
  "v_cndmask_b32 v190, v96, v191, vcc\n" \
  "v_and_b32 v99, 4, v252\n" \
  "v_cmp_ne_u32 vcc, 0, v99\n" \
  "s_lshl_b32 s35, s28, 11\n" \
  "s_add_u32 s35, s35, 130048\n" \
  "v_cndmask_b32 v193, v193, v98, vcc\n" \
  "v_lshl_add_u32 v194, v252, 4, s35\n" \
  "v_and_b32 v96, 7, v252\n" \
  "v_lshlrev_b32 v96, 5, v96\n" \
  "v_lshl_add_u32 v96, v253, 8, v96\n" \
  "v_lshrrev_b32 v97, 3, v252\n" \
  "v_lshl_add_u32 v96, v97, 2, v96\n" \
  "v_add_u32 v195, s35, v96\n" \
  "v_and_b32 v97, v97, v252\n" \
  "v_lshlrev_b32 v192, 4, v97\n" \
  "s_waitcnt vmcnt(4)\n" \
  "s_barrier\n" \
  "ds_read_b64 v[200:201], v12 offset:128\n" \
  "ds_read_b64 v[202:203], v23 offset:136\n" \
  "ds_read_b64 v[184:185], v9 offset:184\n" \
  "ds_read_b64 v[186:187], v20 offset:192\n" \
  "ds_read_b64 v[212:213], v9 offset:384\n" \
  "ds_read_b64 v[214:215], v20 offset:392\n" \
  "ds_read_b64 v[196:197], v12 offset:64\n" \
  "ds_read_b64 v[198:199], v23 offset:72\n" \
  "ds_read_b64 v[176:177], v7 offset:184\n" \
  "ds_read_b64 v[178:179], v18 offset:192\n" \
  "ds_read_b64 v[180:181], v7 offset:384\n" \
  "ds_read_b64 v[182:183], v18 offset:392\n" \
  "ds_read_b64 v[228:229], v12 offset:0\n" \
  "ds_read_b64 v[230:231], v23 offset:8\n" \
  "s_waitcnt lgkmcnt(12)\n" \
  "v_mfma_f32_16x16x32_bf16 v[44:47], v[200:203], v[216:219], 0\n" \
  "ds_read_b64 v[168:169], v5 offset:184\n" \
  "ds_read_b64 v[170:171], v16 offset:192\n" \
  "s_waitcnt lgkmcnt(12)\n" \
  "v_mfma_f32_16x16x32_bf16 v[44:47], v[224:227], v[184:187], v[44:47]\n" \
  "ds_read_b64 v[172:173], v5 offset:384\n" \
  "ds_read_b64 v[174:175], v16 offset:392\n" \
  "v_mfma_f32_16x16x32_bf16 v[208:211], v[200:203], v[220:223], 0\n" \
  "ds_read_b64 v[96:97], v6 offset:0\n" \
  "ds_read_b64 v[98:99], v17 offset:8\n" \
  "s_waitcnt lgkmcnt(14)\n" \
  "v_mfma_f32_16x16x32_bf16 v[208:211], v[224:227], v[212:215], v[208:211]\n" \
  "ds_read_b64 v[124:125], v8 offset:64\n" \
  "ds_read_b64 v[126:127], v19 offset:72\n" \
  "s_waitcnt lgkmcnt(14)\n" \
  "v_mfma_f32_16x16x32_bf16 v[60:63], v[196:199], v[216:219], 0\n" \
  "ds_read_b64 v[136:137], v8 offset:264\n" \
  "ds_read_b64 v[138:139], v19 offset:272\n" \
  "s_waitcnt lgkmcnt(14)\n" \
  "v_mfma_f32_16x16x32_bf16 v[60:63], v[224:227], v[176:179], v[60:63]\n" \
  "ds_read_b64 v[140:141], v8 offset:328\n" \
  "ds_read_b64 v[142:143], v19 offset:336\n" \
  "v_mfma_f32_16x16x32_bf16 v[184:187], v[196:199], v[220:223], 0\n" \
  "ds_read_b64 v[160:161], v10 offset:264\n" \
  "ds_read_b64 v[162:163], v21 offset:272\n" \
  "s_waitcnt lgkmcnt(15)\n" \
  "v_mfma_f32_16x16x32_bf16 v[184:187], v[224:227], v[180:183], v[184:187]\n" \
  "ds_read_b64 v[116:117], v6 offset:328\n" \
  "ds_read_b64 v[118:119], v17 offset:336\n" \
  "s_waitcnt lgkmcnt(15)\n" \
  "v_mfma_f32_16x16x32_bf16 v[68:71], v[228:231], v[216:219], 0\n" \
  "ds_read_b64 v[144:145], v10 offset:0\n" \
  "ds_read_b64 v[146:147], v21 offset:8\n" \
  "s_waitcnt lgkmcnt(15)\n" \
  "v_mfma_f32_16x16x32_bf16 v[68:71], v[224:227], v[168:171], v[68:71]\n" \
  "ds_read_b64 v[148:149], v10 offset:64\n" \
  "ds_read_b64 v[150:151], v21 offset:72\n" \
  "v_mfma_f32_16x16x32_bf16 v[212:215], v[228:231], v[220:223], 0\n" \
  "ds_read_b64 v[104:105], v6 offset:128\n" \
  "ds_read_b64 v[106:107], v17 offset:136\n" \
  "s_waitcnt lgkmcnt(15)\n" \
  "v_mfma_f32_16x16x32_bf16 v[212:215], v[224:227], v[172:175], v[212:215]\n" \
  "ds_read_b64 v[100:101], v6 offset:64\n" \
  "ds_read_b64 v[102:103], v17 offset:72\n" \
  "v_mfma_f32_16x16x32_bf16 v[92:95], v[96:99], v[216:219], 0\n" \
  "ds_read_b64 v[120:121], v8 offset:0\n" \
  "ds_read_b64 v[122:123], v19 offset:8\n" \
  "s_waitcnt lgkmcnt(15)\n" \
  "v_mfma_f32_16x16x32_bf16 v[24:27], v[124:127], v[220:223], 0\n" \
  "ds_read_b64 v[112:113], v6 offset:264\n" \
  "ds_read_b64 v[114:115], v17 offset:272\n" \
  "v_mfma_f32_16x16x32_bf16 v[24:27], v[216:219], v[136:139], v[24:27]\n" \
  "ds_read_b64 v[156:157], v10 offset:200\n" \
  "ds_read_b64 v[158:159], v21 offset:208\n" \
  "s_waitcnt lgkmcnt(15)\n" \
  "v_mfma_f32_16x16x32_bf16 v[28:31], v[140:143], v[220:223], 0\n" \
  "ds_read_b64 v[132:133], v8 offset:200\n" \
  "ds_read_b64 v[134:135], v19 offset:208\n" \
  "v_mfma_f32_16x16x32_bf16 v[28:31], v[220:223], v[160:163], v[28:31]\n" \
  "ds_read_b64 v[108:109], v6 offset:200\n" \
  "ds_read_b64 v[110:111], v17 offset:208\n" \
  "s_waitcnt lgkmcnt(15)\n" \
  "v_mfma_f32_16x16x32_bf16 v[32:35], v[116:119], v[216:219], 0\n" \
  "ds_read_b64 v[152:153], v10 offset:128\n" \
  "ds_read_b64 v[154:155], v21 offset:136\n" \
  "v_mfma_f32_16x16x32_bf16 v[32:35], v[220:223], v[144:147], v[32:35]\n" \
  "ds_read_b64 v[164:165], v10 offset:328\n" \
  "ds_read_b64 v[166:167], v21 offset:336\n" \
  "v_mfma_f32_16x16x32_bf16 v[36:39], v[140:143], v[216:219], 0\n" \
  "ds_read_b64 v[128:129], v8 offset:128\n" \
  "ds_read_b64 v[130:131], v19 offset:136\n" \
  "s_waitcnt lgkmcnt(15)\n" \
  "v_mfma_f32_16x16x32_bf16 v[36:39], v[220:223], v[148:151], v[36:39]\n" \
  "ds_read_b64 v[204:205], v11 offset:184\n" \
  "ds_read_b64 v[206:207], v22 offset:192\n" \
  "v_mfma_f32_16x16x32_bf16 v[40:43], v[104:107], v[216:219], 0\n" \
  "v_mfma_f32_16x16x32_bf16 v[40:43], v[216:219], v[144:147], v[40:43]\n" \
  "v_mfma_f32_16x16x32_bf16 v[48:51], v[100:103], v[216:219], 0\n" \
  "s_waitcnt lgkmcnt(15)\n" \
  "v_mfma_f32_16x16x32_bf16 v[48:51], v[216:219], v[120:123], v[48:51]\n" \
  "s_waitcnt lgkmcnt(14)\n" \
  "v_mfma_f32_16x16x32_bf16 v[52:55], v[112:115], v[216:219], 0\n" \
  "v_mfma_f32_16x16x32_bf16 v[52:55], v[220:223], v[120:123], v[52:55]\n" \
  "v_mfma_f32_16x16x32_bf16 v[56:59], v[116:119], v[220:223], 0\n" \
  "s_waitcnt lgkmcnt(12)\n" \
  "v_mfma_f32_16x16x32_bf16 v[56:59], v[220:223], v[156:159], v[56:59]\n" \
  "s_waitcnt vmcnt(0) lgkmcnt(0)\n" \
  "v_mfma_f32_16x16x32_bf16 v[64:67], v[104:107], v[220:223], 0\n" \
  "v_cvt_pk_u16_u32 v232, v232, v233\n" \
  "v_cvt_pk_u16_u32 v234, v234, v235\n" \
  "v_mfma_f32_16x16x32_bf16 v[64:67], v[216:219], v[156:159], v[64:67]\n" \
  "v_cvt_pk_u16_u32 v236, v236, v237\n" \
  "v_cvt_pk_u16_u32 v238, v238, v239\n" \
  "v_mfma_f32_16x16x32_bf16 v[72:75], v[136:139], v[220:223], 0\n" \
  "v_pk_lshrrev_b16 v233, 3, v232 op_sel_hi:[0,1]\n" \
  "v_pk_lshrrev_b16 v235, 3, v234 op_sel_hi:[0,1]\n" \
  "v_mfma_f32_16x16x32_bf16 v[76:79], v[112:115], v[220:223], 0\n" \
  "v_pk_lshrrev_b16 v237, 3, v236 op_sel_hi:[0,1]\n" \
  "v_pk_lshrrev_b16 v239, 3, v238 op_sel_hi:[0,1]\n" \
  "v_mfma_f32_16x16x32_bf16 v[76:79], v[220:223], v[132:135], v[76:79]\n" \
  "v_pk_mad_u16 v180, v232, 8, v233 op_sel_hi:[1,0,1]\n" \
  "v_pk_mad_u16 v181, v234, 8, v235 op_sel_hi:[1,0,1]\n" \
  "v_mfma_f32_16x16x32_bf16 v[80:83], v[100:103], v[220:223], 0\n" \
  "v_pk_mad_u16 v182, v236, 8, v237 op_sel_hi:[1,0,1]\n" \
  "v_pk_mad_u16 v183, v238, 8, v239 op_sel_hi:[1,0,1]\n" \
  "v_mfma_f32_16x16x32_bf16 v[80:83], v[216:219], v[132:135], v[80:83]\n" \
  "v_alignbit_b32 v182, v182, v182, v192\n" \
  "v_alignbit_b32 v183, v183, v183, v192\n" \
  "v_mfma_f32_16x16x32_bf16 v[84:87], v[96:99], v[220:223], 0\n" \
  "v_cvt_pk_u16_u32 v240, v240, v241\n" \
  "v_cvt_pk_u16_u32 v242, v242, v243\n" \
  "v_mfma_f32_16x16x32_bf16 v[84:87], v[216:219], v[108:111], v[84:87]\n" \
  "v_cvt_pk_u16_u32 v244, v244, v245\n" \
  "v_cvt_pk_u16_u32 v246, v246, v247\n" \
  "v_mfma_f32_16x16x32_bf16 v[88:91], v[108:111], v[220:223], 0\n" \
  "v_pk_lshrrev_b16 v241, 3, v240 op_sel_hi:[0,1]\n" \
  "v_pk_lshrrev_b16 v243, 3, v242 op_sel_hi:[0,1]\n" \
  "v_mfma_f32_16x16x32_bf16 v[0:3], v[124:127], v[216:219], 0\n" \
  "v_pk_lshrrev_b16 v245, 3, v244 op_sel_hi:[0,1]\n" \
  "v_pk_lshrrev_b16 v247, 3, v246 op_sel_hi:[0,1]\n" \
  "v_mfma_f32_16x16x32_bf16 v[4:7], v[152:155], v[216:219], 0\n" \
  "v_pk_mad_u16 v196, v240, 8, v241 op_sel_hi:[1,0,1]\n" \
  "v_pk_mad_u16 v197, v242, 8, v243 op_sel_hi:[1,0,1]\n" \
  "v_mfma_f32_16x16x32_bf16 v[8:11], v[152:155], v[220:223], 0\n" \
  "v_pk_mad_u16 v198, v244, 8, v245 op_sel_hi:[1,0,1]\n" \
  "v_pk_mad_u16 v199, v246, 8, v247 op_sel_hi:[1,0,1]\n" \
  "v_mfma_f32_16x16x32_bf16 v[8:11], v[216:219], v[164:167], v[8:11]\n" \
  "v_alignbit_b32 v198, v198, v198, v192\n" \
  "v_alignbit_b32 v199, v199, v199, v192\n" \
  "v_mfma_f32_16x16x32_bf16 v[12:15], v[164:167], v[220:223], 0\n" \
  "v_add_u32 v200, 0x400, v195\n" \
  "v_mfma_f32_16x16x32_bf16 v[16:19], v[128:131], v[220:223], 0\n" \
  "v_mfma_f32_16x16x32_bf16 v[16:19], v[216:219], v[160:163], v[16:19]\n" \
  "v_mfma_f32_16x16x32_bf16 v[20:23], v[128:131], v[216:219], 0\n" \
  "v_mfma_f32_16x16x32_bf16 v[20:23], v[216:219], v[148:151], v[20:23]\n" \
  "v_mfma_f32_16x16x32_bf16 v[168:171], v[216:219], v[96:99], 0\n" \
  "v_mfma_f32_16x16x32_bf16 v[172:175], v[204:207], v[224:227], 0\n" \
  "v_mfma_f32_16x16x32_bf16 v[176:179], v[224:227], v[204:207], 0\n" \
  "s_mov_b32 exec_lo, 0x0\n" \
  "s_mov_b32 exec_hi, 0xffffffff\n" \
  "v_mov_b64 v[44:45], v[208:209]\n" \
  "v_mov_b64 v[46:47], v[210:211]\n" \
  "v_mov_b64 v[60:61], v[184:185]\n" \
  "v_mov_b64 v[62:63], v[186:187]\n" \
  "v_mov_b64 v[68:69], v[212:213]\n" \
  "v_mov_b64 v[70:71], v[214:215]\n" \
  "s_mov_b32 exec_lo, 0xff00ff00\n" \
  "s_mov_b32 exec_hi, 0x0\n" \
  "v_add_f32 v92, v92, v168\n" \
  "v_add_f32 v93, v93, v169\n" \
  "v_add_f32 v94, v94, v170\n" \
  "v_add_f32 v95, v95, v171\n" \
  "s_mov_b32 exec_lo, 0xf0000f00\n" \
  "s_mov_b32 exec_hi, 0x0\n" \
  "v_sub_f32 v92, v92, v176\n" \
  "v_sub_f32 v93, v93, v177\n" \
  "v_sub_f32 v94, v94, v178\n" \
  "v_sub_f32 v95, v95, v179\n" \
  "s_mov_b32 exec_lo, 0x0\n" \
  "s_mov_b32 exec_hi, 0xff00ff\n" \
  "v_mov_b64 v[92:93], 0\n" \
  "v_mov_b64 v[94:95], 0\n" \
  "s_mov_b32 exec_lo, 0x0\n" \
  "s_mov_b32 exec_hi, 0xf0000f\n" \
  "v_mov_b64 v[92:93], v[172:173]\n" \
  "v_mov_b64 v[94:95], v[174:175]\n" \
  "s_mov_b64 exec, -1\n" \
  "v_add_u32 v96, 6400, v250\n" \
  "v_add_u32 v97, 6400, v251\n" \
  "v_mov_b32 v98, 163328\n" \
  "v_mov_b32 v99, 1\n" \
  "v_lshlrev_b32 v179, 4, v253\n" \
  "s_lshr_b32 s35, s30, 4\n" \
  "s_lshl1_add_u32 s35, s29, s35\n" \
  "s_lshl_b32 s35, s35, 6\n" \
  "s_bfe_u32 s37, s30, 0x20002\n" \
  "s_lshl3_add_u32 s35, s37, s35\n" \
  "v_and_b32 v179, 16, v179\n" \
  "v_lshl_add_u32 v179, v179, 1, s35\n" \
  "s_mov_b64 exec, 1\n" \
  "ds_add_u32 v98, v99 offset:16\n" \
  "s_mov_b64 exec, -1\n" \
  "ds_write2_b32 v195, v180, v181 offset0:0 offset1:4\n" \
  "ds_write2_b32 v195, v182, v183 offset0:2 offset1:6\n" \
  "ds_write2_b32 v200, v196, v197 offset0:0 offset1:4\n" \
  "ds_write2_b32 v200, v198, v199 offset0:2 offset1:6\n" \
  "global_load_dwordx2 v[204:205], v179, s[16:17]\n" \
  "global_load_dwordx2 v[206:207], v179, s[18:19]\n" \
  "v_mov_b32 v179, 0\n" \
  "global_load_ushort v179, v179, s[20:21]\n" \
  "s_add_u32 s46, s44, 4800\n" \
  "s_addc_u32 s47, s45, 0\n" \
  "global_load_dwordx4 v[208:211], v96, s[44:45] offset:-3200\n" \
  "global_load_dwordx4 v[212:215], v97, s[44:45] offset:-2944\n" \
  "global_load_dwordx4 v[216:219], v96, s[44:45] offset:-1600\n" \
  "global_load_dwordx4 v[220:223], v97, s[44:45] offset:-1344\n" \
  "global_load_dwordx4 v[224:227], v96, s[44:45] offset:0\n" \
  "global_load_dwordx4 v[228:231], v97, s[44:45] offset:256\n" \
  "global_load_dwordx4 v[232:235], v96, s[44:45] offset:1600\n" \
  "global_load_dwordx4 v[236:239], v97, s[44:45] offset:1856\n" \
  "global_load_dwordx4 v[240:243], v96, s[44:45] offset:3200\n" \
  "global_load_dwordx4 v[244:247], v97, s[44:45] offset:3456\n" \
  "global_load_dwordx4 v[248:251], v96, s[46:47] offset:0\n" \
  "global_load_dwordx4 v[252:255], v97, s[46:47] offset:256\n" \
  "ds_read_b128 v[180:183], v194\n" \
  "v_add_u32 v194, 0x100, v194\n" \
  "L1_:\n" \
  "ds_read_b32 v99, v98 offset:12\n" \
  "s_waitcnt lgkmcnt(0)\n" \
  "v_readfirstlane_b32 s35, v99\n" \
  "s_cmp_lt_u32 s35, 4\n" \
  "s_cbranch_scc1 L1_\n" \
  "s_setprio 1\n" \
  "v_mad_u32_u16 v184, v182, 16, v188\n" \
  "ds_read_b128 v[128:131], v184 offset:0\n" \
  "ds_read_b128 v[132:135], v184 offset:64\n" \
  "ds_read_b128 v[172:175], v191\n" \
  "v_mad_u32_u16 v185, v181, 16, v188\n" \
  "v_mad_u32_u16 v186, v182, 16, v188 op_sel:[1,0,0,0]\n" \
  "ds_read_b128 v[112:115], v185 offset:0\n" \
  "ds_read_b128 v[116:119], v185 offset:64\n" \
  "ds_read_b128 v[136:139], v186 offset:0\n" \
  "ds_read_b128 v[140:143], v186 offset:64\n" \
  "ds_read_b128 v[168:171], v191\n" \
  "v_mad_u32_u16 v187, v181, 16, v188 op_sel:[1,0,0,0]\n" \
  "v_mad_u32_u16 v184, v180, 16, v188 op_sel:[1,0,0,0]\n" \
  "ds_read_b128 v[120:123], v187 offset:0\n" \
  "v_mad_u32_u16 v185, v180, 16, v188\n" \
  "ds_read_b128 v[124:127], v187 offset:64\n" \
  "ds_read_b128 v[104:107], v184 offset:0\n" \
  "v_mad_u32_u16 v186, v183, 16, v189\n" \
  "ds_read_b128 v[108:111], v184 offset:64\n" \
  "ds_read_b128 v[96:99], v185 offset:0\n" \
  "s_waitcnt lgkmcnt(9)\n" \
  "s_mov_b32 s51, 3\n" \
  "s_mov_b32 s50, 8\n" \
  "s_call_b64 s[52:53], LA_loop\n" \
  "LA_ev0:\n" \
  "s_waitcnt vmcnt(10)\n" \
  "v_cvt_pk_u16_u32 v208, v208, v209\n" \
  "v_cvt_pk_u16_u32 v210, v210, v211\n" \
  "v_cvt_pk_u16_u32 v212, v212, v213\n" \
  "v_cvt_pk_u16_u32 v214, v214, v215\n" \
  "v_pk_lshrrev_b16 v209, 3, v208 op_sel_hi:[0,1]\n" \
  "v_pk_lshrrev_b16 v211, 3, v210 op_sel_hi:[0,1]\n" \
  "v_pk_lshrrev_b16 v213, 3, v212 op_sel_hi:[0,1]\n" \
  "v_pk_lshrrev_b16 v215, 3, v214 op_sel_hi:[0,1]\n" \
  "v_pk_mad_u16 v208, v208, 8, v209 op_sel_hi:[1,0,1]\n" \
  "v_pk_mad_u16 v210, v210, 8, v211 op_sel_hi:[1,0,1]\n" \
  "v_pk_mad_u16 v212, v212, 8, v213 op_sel_hi:[1,0,1]\n" \
  "v_pk_mad_u16 v214, v214, 8, v215 op_sel_hi:[1,0,1]\n" \
  "v_alignbit_b32 v212, v212, v212, v192\n" \
  "v_alignbit_b32 v214, v214, v214, v192\n" \
  "ds_write2_b32 v195, v208, v210 offset0:0 offset1:4\n" \
  "ds_write2_b32 v195, v212, v214 offset0:2 offset1:6\n" \
  "v_mov_b32 v196, v178\n" \
  "v_readfirstlane_b32 s6, v179\n" \
  "v_mov_b32 v208, 163328\n" \
  "s_mov_b32 s51, 4\n" \
  "s_mov_b32 s50, 1\n" \
  "s_mov_b32 s38, 0x800\n" \
  "s_call_b64 s[52:53], LA_loop\n" \
  "LA_ev6:\n" \
  "v_mov_b32 v202, v178\n" \
  "v_readfirstlane_b32 s35, v209\n" \
  "s_cmp_ge_u32 s35, 128\n" \
  "s_cbranch_scc1 LA_pl\n" \
  "L2_:\n" \
  "ds_read_b32 v209, v208 offset:4\n" \
  "s_waitcnt lgkmcnt(0)\n" \
  "v_readfirstlane_b32 s35, v209\n" \
  "s_cmp_lt_u32 s35, 128\n" \
  "s_cbranch_scc1 L2_\n" \
  "LA_pl:\n" \
  "s_and_b32 s4, s35, 15\n" \
  "s_sub_u32 s4, s4, 4\n" \
  "s_bfe_u32 s5, s35, 0x30004\n" \
  "s_sub_u32 s5, s5, s29\n" \
  "s_cmp_le_i32 s4, 0\n" \
  "s_cbranch_scc1 LA_nt\n" \
  "s_mul_i32 s35, s5, 51200\n" \
  "s_ashr_i32 s36, s35, 31\n" \
  "s_add_u32 s46, s46, s35\n" \
  "s_addc_u32 s47, s47, s36\n" \
  "v_lshrrev_b32 v216, 4, v188\n" \
  "v_mul_u32_u24 v216, 400, v216\n" \
  "v_add_u32 v216, 6400, v216\n" \
  "v_bfe_u32 v218, v194, 4, 4\n" \
  "v_min_u32 v217, 8, v218\n" \
  "v_lshl_add_u32 v217, v217, 4, v216\n" \
  "v_lshl_add_u32 v216, v218, 4, v216\n" \
  "global_load_dwordx4 v[208:211], v216, s[46:47] offset:0\n" \
  "global_load_dwordx4 v[212:215], v217, s[46:47] offset:256\n" \
  "LA_nt:\n" \
  "s_lshl_b32 s5, s5, 7\n" \
  "s_sub_u32 s5, s5, s4\n" \
  "s_min_i32 s51, s4, 0\n" \
  "s_add_u32 s51, s51, 4\n" \
  "s_cmp_eq_u32 s51, 0\n" \
  "s_cbranch_scc1 LA_ev7\n" \
  "s_call_b64 s[52:53], LA_loop\n" \
  "LA_ev7:\n" \
  "v_mov_b32 v203, v178\n" \
  "s_cmp_le_i32 s4, 0\n" \
  "s_cbranch_scc1 LA_ev8\n" \
  "s_waitcnt vmcnt(0)\n" \
  "v_cvt_pk_u16_u32 v208, v208, v209\n" \
  "v_cvt_pk_u16_u32 v210, v210, v211\n" \
  "v_cvt_pk_u16_u32 v212, v212, v213\n" \
  "v_cvt_pk_u16_u32 v214, v214, v215\n" \
  "v_pk_lshrrev_b16 v209, 3, v208 op_sel_hi:[0,1]\n" \
  "v_pk_lshrrev_b16 v211, 3, v210 op_sel_hi:[0,1]\n" \
  "v_pk_lshrrev_b16 v213, 3, v212 op_sel_hi:[0,1]\n" \
  "v_pk_lshrrev_b16 v215, 3, v214 op_sel_hi:[0,1]\n" \
  "v_pk_mad_u16 v208, v208, 8, v209 op_sel_hi:[1,0,1]\n" \
  "v_pk_mad_u16 v210, v210, 8, v211 op_sel_hi:[1,0,1]\n" \
  "v_pk_mad_u16 v212, v212, 8, v213 op_sel_hi:[1,0,1]\n" \
  "v_pk_mad_u16 v214, v214, 8, v215 op_sel_hi:[1,0,1]\n" \
  "v_alignbit_b32 v212, v212, v212, v192\n" \
  "v_alignbit_b32 v214, v214, v214, v192\n" \
  "ds_write2_b32 v195, v208, v210 offset0:0 offset1:4\n" \
  "ds_write2_b32 v195, v212, v214 offset0:2 offset1:6\n" \
  "s_lshl_b32 s35, s4, 8\n" \
  "s_add_u32 s35, s35, 0x400\n" \
  "v_subrev_u32 v194, s35, v194\n" \
  "s_mov_b32 s51, s4\n" \
  "s_call_b64 s[52:53], LA_loop\n" \
  "LA_ev8:\n" \
  "v_mov_b32 v179, v178\n" \
  "ds_read_b128 v[100:103], v185 offset:64\n" \
  "ds_read2_b32 v[144:145], v186 offset0:0 offset1:2\n" \
  "ds_read2_b32 v[146:147], v186 offset0:4 offset1:6\n" \
  "v_mad_u32_u16 v187, v183, 16, v190\n" \
  "ds_read2_b32 v[148:149], v187 offset0:0 offset1:2\n" \
  "ds_read2_b32 v[150:151], v187 offset0:4 offset1:6\n" \
  "ds_read_b128 v[164:167], v191\n" \
  "s_lshr_b32 s35, s30, 2\n" \
  "s_lshl3_add_u32 s33, s29, s35\n" \
  "s_and_b32 s34, s30, 3\n" \
  "s_lshl_b32 s59, s27, 11\n" \
  "s_mul_i32 s35, s33, 1600000\n" \
  "s_add_u32 s54, s24, s35\n" \
  "s_addc_u32 s55, s25, 0\n" \
  "s_mul_i32 s35, s34, 819200\n" \
  "s_lshl1_add_u32 s35, s59, s35\n" \
  "s_add_u32 s35, s35, 102400000\n" \
  "s_add_u32 s56, s24, s35\n" \
  "s_addc_u32 s57, s25, 0\n" \
  "v_mbcnt_lo_u32_b32 v189, -1, 0\n" \
  "v_mbcnt_hi_u32_b32 v189, -1, v189\n" \
  "v_lshlrev_b32 v188, 4, v189\n" \
  "s_waitcnt lgkmcnt(6)\n" \
  "v_mfma_f32_16x16x32_bf16 v[152:155], v[112:115], v[112:115], 0\n" \
  "v_fmac_f32 v176, v156, v88\n" \
  "v_fmac_f32 v176, v157, v89\n" \
  "v_mfma_f32_16x16x32_bf16 v[152:155], v[116:119], v[116:119], v[152:155]\n" \
  "v_fmac_f32 v176, v158, v90\n" \
  "v_fmac_f32 v176, v159, v91\n" \
  "v_mfma_f32_16x16x32_bf16 v[156:159], v[128:131], v[128:131], 0\n" \
  "v_fmac_f32 v176, v160, v92\n" \
  "v_fmac_f32 v176, v161, v93\n" \
  "v_mfma_f32_16x16x32_bf16 v[156:159], v[132:135], v[132:135], v[156:159]\n" \
  "v_fmac_f32 v176, v162, v94\n" \
  "v_fma_f32 v177, v163, v95, v176\n" \
  "v_mfma_f32_16x16x32_bf16 v[160:163], v[128:131], v[136:139], 0\n" \
  "v_mul_f32 v176, v152, v0\n" \
  "v_fmac_f32 v176, v153, v1\n" \
  "v_mfma_f32_16x16x32_bf16 v[160:163], v[132:135], v[140:143], v[160:163]\n" \
  "v_fmac_f32 v176, v154, v2\n" \
  "v_fmac_f32 v176, v155, v3\n" \
  "v_mfma_f32_16x16x32_bf16 v[152:155], v[136:139], v[136:139], 0\n" \
  "v_fmac_f32 v176, v156, v4\n" \
  "v_fmac_f32 v176, v157, v5\n" \
  "v_mfma_f32_16x16x32_bf16 v[152:155], v[140:143], v[140:143], v[152:155]\n" \
  "v_fmac_f32 v176, v158, v6\n" \
  "v_fmac_f32 v176, v159, v7\n" \
  "s_waitcnt lgkmcnt(0)\n" \
  "s_setprio 3\n" \
  "s_barrier\n" \
  "global_load_dwordx4 v[208:211], v188, s[56:57] offset:0 sc1\n" \
  "global_load_dwordx4 v[212:215], v188, s[56:57] offset:1024 sc1\n" \
  "global_load_dwordx4 v[216:219], v188, s[56:57] offset:2048 sc1\n" \
  "global_load_dwordx4 v[220:223], v188, s[56:57] offset:3072 sc1\n" \
  "v_mfma_f32_16x16x32_bf16 v[156:159], v[112:115], v[136:139], 0\n" \
  "v_fmac_f32 v176, v160, v8\n" \
  "v_fmac_f32 v176, v161, v9\n" \
  "v_lshrrev_b32 v180, 1, v189\n" \
  "v_mfma_f32_16x16x32_bf16 v[156:159], v[116:119], v[140:143], v[156:159]\n" \
  "v_fmac_f32 v176, v162, v10\n" \
  "v_fmac_f32 v176, v163, v11\n" \
  "v_xor_b32 v180, v180, v189\n" \
  "v_mfma_f32_16x16x32_bf16 v[160:163], v[112:115], v[128:131], 0\n" \
  "v_fmac_f32 v176, v152, v12\n" \
  "v_fmac_f32 v176, v153, v13\n" \
  "v_add_f32_dpp v177, v177, v177 row_ror:8 row_mask:0xf bank_mask:0xf\n" \
  "v_and_b32 v180, 8, v180\n" \
  "v_mfma_f32_16x16x32_bf16 v[160:163], v[116:119], v[132:135], v[160:163]\n" \
  "v_fmac_f32 v176, v154, v14\n" \
  "v_fmac_f32 v176, v155, v15\n" \
  "v_bfe_u32 v181, v189, 2, 1\n" \
  "v_mfma_f32_16x16x32_bf16 v[152:155], v[112:115], v[120:123], 0\n" \
  "v_fmac_f32 v176, v156, v16\n" \
  "v_fmac_f32 v176, v157, v17\n" \
  "v_lshlrev_b32_e64 v181, v181, 5\n" \
  "v_mfma_f32_16x16x32_bf16 v[152:155], v[116:119], v[124:127], v[152:155]\n" \
  "v_fmac_f32 v176, v158, v18\n" \
  "v_fmac_f32 v176, v159, v19\n" \
  "v_mov_b32_dpp v178, v178 row_ror:4 row_mask:0xf bank_mask:0xf\n" \
  "v_lshlrev_b32 v180, v180, v181\n" \
  "v_mfma_f32_16x16x32_bf16 v[156:159], v[120:123], v[136:139], 0\n" \
  "v_fmac_f32 v176, v160, v20\n" \
  "v_fmac_f32 v176, v161, v21\n" \
  "v_bfe_u32 v181, v189, 2, 2\n" \
  "v_mfma_f32_16x16x32_bf16 v[156:159], v[124:127], v[140:143], v[156:159]\n" \
  "v_fmac_f32 v176, v162, v22\n" \
  "v_fmac_f32 v176, v163, v23\n" \
  "v_lshlrev_b32_e64 v181, v181, 1\n" \
  "v_mfma_f32_16x16x32_bf16 v[160:163], v[104:107], v[128:131], 0\n" \
  "v_fmac_f32 v176, v152, v24\n" \
  "v_fmac_f32 v176, v153, v25\n" \
  "v_cmp_gt_u32 vcc, 32, v189\n" \
  "v_mfma_f32_16x16x32_bf16 v[160:163], v[108:111], v[132:135], v[160:163]\n" \
  "v_fmac_f32 v176, v154, v26\n" \
  "v_fmac_f32 v176, v155, v27\n" \
  "v_mov_b32 v191, 0x3f80\n" \
  "v_mfma_f32_16x16x32_bf16 v[152:155], v[120:123], v[128:131], 0\n" \
  "v_fmac_f32 v176, v156, v28\n" \
  "v_fmac_f32 v176, v157, v29\n" \
  "v_add_u32 v190, 32000, v188\n" \
  "v_mfma_f32_16x16x32_bf16 v[152:155], v[124:127], v[132:135], v[152:155]\n" \
  "v_fmac_f32 v176, v158, v30\n" \
  "v_fmac_f32 v176, v159, v31\n" \
  "v_add_f32_dpp v178, v177, v177 row_ror:4 row_mask:0xf bank_mask:0x1\n" \
  "v_cndmask_b32 v192, v181, v180, vcc\n" \
  "v_mfma_f32_16x16x32_bf16 v[156:159], v[96:99], v[128:131], 0\n" \
  "v_fmac_f32 v176, v160, v32\n" \
  "v_fmac_f32 v176, v161, v33\n" \
  "v_bfe_i32 v180, v192, 0, 1\n" \
  "v_mfma_f32_16x16x32_bf16 v[156:159], v[100:103], v[132:135], v[156:159]\n" \
  "v_fmac_f32 v176, v162, v34\n" \
  "v_fmac_f32 v176, v163, v35\n" \
  "v_and_b32 v180, v180, v191\n" \
  "v_smfmac_f32_16x16x64_bf16 v[172:175], v[144:147], v[128:135], v193\n" \
  "v_fmac_f32 v176, v152, v36\n" \
  "v_fmac_f32 v176, v153, v37\n" \
  "v_bfe_i32 v181, v192, 1, 1\n" \
  "v_smfmac_f32_16x16x64_bf16 v[172:175], v[148:151], v[136:143], v193\n" \
  "v_fmac_f32 v176, v154, v38\n" \
  "v_fmac_f32 v176, v155, v39\n" \
  "v_and_b32 v181, v181, v191\n" \
  "v_mfma_f32_16x16x32_bf16 v[160:163], v[96:99], v[112:115], 0\n" \
  "v_fmac_f32 v176, v156, v40\n" \
  "v_fmac_f32 v176, v157, v41\n" \
  "v_bfe_i32 v182, v192, 2, 1\n" \
  "v_mfma_f32_16x16x32_bf16 v[160:163], v[100:103], v[116:119], v[160:163]\n" \
  "v_fmac_f32 v176, v158, v42\n" \
  "v_fmac_f32 v176, v159, v43\n" \
  "v_and_b32 v182, v182, v191\n" \
  "v_mfma_f32_16x16x32_bf16 v[152:155], v[104:107], v[112:115], 0\n" \
  "v_fmac_f32 v176, v172, v44\n" \
  "v_fmac_f32 v176, v173, v45\n" \
  "v_bfe_i32 v183, v192, 3, 1\n" \
  "v_mfma_f32_16x16x32_bf16 v[152:155], v[108:111], v[116:119], v[152:155]\n" \
  "v_fmac_f32 v176, v174, v46\n" \
  "v_fmac_f32 v176, v175, v47\n" \
  "v_and_b32 v183, v183, v191\n" \
  "v_mfma_f32_16x16x32_bf16 v[156:159], v[104:107], v[136:139], 0\n" \
  "v_fmac_f32 v176, v160, v48\n" \
  "v_fmac_f32 v176, v161, v49\n" \
  "v_and_b32 v192, 3, v189\n" \
  "v_mfma_f32_16x16x32_bf16 v[156:159], v[108:111], v[140:143], v[156:159]\n" \
  "v_fmac_f32 v176, v162, v50\n" \
  "v_fmac_f32 v176, v163, v51\n" \
  "v_mul_u32_u24 v192, 0x5555, v192\n" \
  "v_smfmac_f32_16x16x64_bf16 v[168:171], v[144:147], v[112:119], v193\n" \
  "v_fmac_f32 v176, v152, v52\n" \
  "v_fmac_f32 v176, v153, v53\n" \
  "v_add_u32 v189, 16000, v188\n" \
  "v_smfmac_f32_16x16x64_bf16 v[168:171], v[148:151], v[120:127], v193\n" \
  "v_fmac_f32 v176, v154, v54\n" \
  "v_fmac_f32 v176, v155, v55\n" \
  "v_add_u32 v191, 48000, v188\n" \
  "v_mfma_f32_16x16x32_bf16 v[160:163], v[96:99], v[136:139], 0\n" \
  "v_fmac_f32 v176, v156, v56\n" \
  "v_fmac_f32 v176, v157, v57\n" \
  "v_mfma_f32_16x16x32_bf16 v[160:163], v[100:103], v[140:143], v[160:163]\n" \
  "v_fmac_f32 v176, v158, v58\n" \
  "v_fmac_f32 v176, v159, v59\n" \
  "s_add_u32 s35, s59, 64000\n" \
  "s_add_u32 s54, s54, s35\n" \
  "s_addc_u32 s55, s55, 0\n" \
  "s_add_u32 s58, s59, 0x10000\n" \
  "s_mov_b32 m0, s58\n" \
  "s_add_u32 s35, s58, 0x8000\n" \
  "global_load_lds_dwordx4 v188, s[54:55] sc1\n" \
  "global_load_lds_dwordx4 v188, s[54:55] offset:1024 sc1\n" \
  "v_smfmac_f32_16x16x64_bf16 v[164:167], v[148:151], v[104:111], v193\n" \
  "v_fmac_f32 v176, v168, v60\n" \
  "v_fmac_f32 v176, v169, v61\n" \
  "v_smfmac_f32_16x16x64_bf16 v[164:167], v[144:147], v[96:103], v193\n" \
  "v_fmac_f32 v176, v170, v62\n" \
  "v_fmac_f32 v176, v171, v63\n" \
  "v_mfma_f32_16x16x32_bf16 v[152:155], v[120:123], v[120:123], 0\n" \
  "v_fmac_f32 v176, v160, v64\n" \
  "v_fmac_f32 v176, v161, v65\n" \
  "v_mfma_f32_16x16x32_bf16 v[152:155], v[124:127], v[124:127], v[152:155]\n" \
  "v_fmac_f32 v176, v162, v66\n" \
  "v_fmac_f32 v176, v163, v67\n" \
  "v_mfma_f32_16x16x32_bf16 v[156:159], v[104:107], v[120:123], 0\n" \
  "v_fmac_f32 v176, v164, v68\n" \
  "v_fmac_f32 v176, v165, v69\n" \
  "v_mfma_f32_16x16x32_bf16 v[156:159], v[108:111], v[124:127], v[156:159]\n" \
  "v_fmac_f32 v176, v166, v70\n" \
  "v_fmac_f32 v176, v167, v71\n" \
  "s_add_u32 m0, s58, 0x4000\n" \
  "s_cmp_lt_u32 s35, 0x28000\n" \
  "global_load_lds_dwordx4 v189, s[54:55] sc1\n" \
  "global_load_lds_dwordx4 v189, s[54:55] offset:1024 sc1\n" \
  "s_cselect_b32 s58, s35, s59\n" \
  "s_add_u32 s54, s54, 32000\n" \
  "s_addc_u32 s55, s55, 0\n" \
  "v_mfma_f32_16x16x32_bf16 v[160:163], v[96:99], v[120:123], 0\n" \
  "v_fmac_f32 v176, v152, v72\n" \
  "v_fmac_f32 v176, v153, v73\n" \
  "v_mfma_f32_16x16x32_bf16 v[160:163], v[100:103], v[124:127], v[160:163]\n" \
  "v_fmac_f32 v176, v154, v74\n" \
  "v_fmac_f32 v176, v155, v75\n" \
  "v_mfma_f32_16x16x32_bf16 v[152:155], v[96:99], v[104:107], 0\n" \
  "v_fmac_f32 v176, v156, v76\n" \
  "v_fmac_f32 v176, v157, v77\n" \
  "v_mfma_f32_16x16x32_bf16 v[152:155], v[100:103], v[108:111], v[152:155]\n" \
  "v_fmac_f32 v176, v158, v78\n" \
  "v_fmac_f32 v176, v159, v79\n" \
  "v_mfma_f32_16x16x32_bf16 v[156:159], v[104:107], v[104:107], 0\n" \
  "v_fmac_f32 v176, v160, v80\n" \
  "v_fmac_f32 v176, v161, v81\n" \
  "v_mfma_f32_16x16x32_bf16 v[156:159], v[108:111], v[108:111], v[156:159]\n" \
  "v_fmac_f32 v176, v162, v82\n" \
  "v_fmac_f32 v176, v163, v83\n" \
  "v_mfma_f32_16x16x32_bf16 v[160:163], v[148:151], v[144:147], 0\n" \
  "v_fmac_f32 v176, v152, v84\n" \
  "v_fmac_f32 v176, v153, v85\n" \
  "v_mfma_f32_16x16x32_bf16 v[160:163], v[96:99], v[96:99], v[160:163]\n" \
  "v_fmac_f32 v176, v154, v86\n" \
  "v_fmac_f32 v176, v155, v87\n" \
  "v_mfma_f32_16x16x32_bf16 v[160:163], v[100:103], v[100:103], v[160:163]\n" \
  "s_mov_b32 s51, 7\n" \
  "v_fmac_f32 v176, v156, v88\n" \
  "v_fmac_f32 v176, v157, v89\n" \
  "v_fmac_f32 v176, v158, v90\n" \
  "v_fmac_f32 v176, v159, v91\n" \
  "s_nop 2\n" \
  "v_fmac_f32 v176, v160, v92\n" \
  "v_fmac_f32 v176, v161, v93\n" \
  "v_fmac_f32 v176, v162, v94\n" \
  "v_fma_f32 v177, v163, v95, v176\n" \
  "s_nop 1\n" \
  "v_add_f32_dpp v177, v177, v177 row_ror:8 row_mask:0xf bank_mask:0xf\n" \
  "v_mov_b32_dpp v178, v178 row_ror:4 row_mask:0xf bank_mask:0xf\n" \
  "s_nop 1\n" \
  "v_add_f32_dpp v178, v177, v177 row_ror:4 row_mask:0xf bank_mask:0x1\n" \
  "v_mov_b32 v99, v179\n" \
  "s_nop 0\n" \
  "v_mov_b32_dpp v196, v178 row_ror:8 row_mask:0xf bank_mask:0xc\n" \
  "s_nop 1\n" \
  "v_permlane16_swap_b32 v196, v197\n" \
  "v_permlane16_swap_b32 v198, v199\n" \
  "v_permlane16_swap_b32 v200, v201\n" \
  "v_permlane16_swap_b32 v202, v203\n" \
  "v_permlane16_swap_b32 v179, v99\n" \
  "v_add_f32 v196, v196, v197\n" \
  "v_add_f32 v198, v198, v199\n" \
  "v_add_f32 v200, v200, v201\n" \
  "v_add_f32 v202, v202, v203\n" \
  "v_add_f32 v179, v179, v99\n" \
  "v_lshrrev_b32 v96, 4, v188\n" \
  "v_and_b32 v97, 1, v96\n" \
  "v_mov_b32 v99, v179\n" \
  "v_lshrrev_b32 v98, 4, v96\n" \
  "v_permlane32_swap_b32 v196, v198\n" \
  "v_permlane32_swap_b32 v200, v202\n" \
  "v_permlane32_swap_b32 v179, v99\n" \
  "v_add_f32 v196, v196, v198\n" \
  "v_add_f32 v200, v200, v202\n" \
  "v_add_f32 v179, v179, v99\n" \
  "v_cmp_eq_u32 vcc, 1, v97\n" \
  "v_lshlrev_b32 v97, 2, v97\n" \
  "v_add_lshl_u32 v97, v97, v98, 2\n" \
  "v_bfe_u32 v98, v96, 2, 2\n" \
  "v_add_f32_dpp v196, v196, v196 quad_perm:[1,0,3,2] row_mask:0xf bank_mask:0xf\n" \
  "v_add_f32_dpp v200, v200, v200 quad_perm:[1,0,3,2] row_mask:0xf bank_mask:0xf\n" \
  "v_add_f32_dpp v179, v179, v179 quad_perm:[1,0,3,2] row_mask:0xf bank_mask:0xf\n" \
  "v_sub_u32 v97, v97, v98\n" \
  "v_cndmask_b32 v196, v196, v200, vcc\n" \
  "v_add_u32 v97, 1, v97\n" \
  "s_min_i32 s35, s4, 0\n" \
  "s_add_u32 s36, s4, 32\n" \
  "s_add_u32 s37, s4, 29\n" \
  "v_add_f32_dpp v179, v179, v179 quad_perm:[2,3,0,1] row_mask:0xf bank_mask:0xf\n" \
  "v_add_f32_dpp v196, v196, v196 quad_perm:[2,3,0,1] row_mask:0xf bank_mask:0xf\n" \
  "s_mov_b32 exec_lo, 0x0\n" \
  "s_mov_b32 exec_hi, 0xaaaa0000\n" \
  "v_add_u32 v97, s35, v97\n" \
  "s_mov_b32 exec_lo, 0x5500\n" \
  "s_mov_b32 exec_hi, 0x0\n" \
  "v_add_u32 v97, s36, v97\n" \
  "s_mov_b32 exec_lo, 0x4444\n" \
  "s_mov_b32 exec_hi, 0x0\n" \
  "v_sub_u32 v97, s37, v98\n" \
  "v_mov_b32 v196, v179\n" \
  "s_mov_b64 exec, -1\n" \
  "v_cmp_lt_u32 vcc, 31, v97\n" \
  "v_add_u32 v98, s5, v97\n" \
  "s_lshl2_add_u32 s35, s7, s28\n" \
  "s_lshl_b32 s35, s35, 5\n" \
  "v_cndmask_b32 v97, v97, v98, vcc\n" \
  "v_add_lshl_u32 v97, v97, s35, 2\n" \
  "s_lshl_b32 s35, s31, 16\n" \
  "s_add_u32 s35, s35, 109871104\n" \
  "s_add_u32 s40, s24, s35\n" \
  "s_addc_u32 s41, s25, 0\n" \
  "v_min_u32 v196, -2, v196\n" \
  "v_lshlrev_b32 v184, 16, v204\n" \
  "v_and_b32 v185, 0xffff0000, v204\n" \
  "v_lshlrev_b32 v186, 16, v205\n" \
  "v_and_b32 v187, 0xffff0000, v205\n" \
  "s_barrier\n" \
  "s_add_u32 s56, s56, 32768\n" \
  "s_addc_u32 s57, s57, 0\n" \
  "global_load_dwordx4 v[224:227], v188, s[56:57] sc1\n" \
  "global_load_dwordx4 v[228:231], v188, s[56:57] offset:1024 sc1\n" \
  "global_load_dwordx4 v[232:235], v188, s[56:57] offset:2048 sc1\n" \
  "global_load_dwordx4 v[236:239], v188, s[56:57] offset:3072 sc1\n" \
  "s_add_u32 s56, s56, 32768\n" \
  "s_addc_u32 s57, s57, 0\n" \
  "s_mov_b32 exec_lo, 0x33337777\n" \
  "s_mov_b32 exec_hi, 0x33333333\n" \
  "global_store_dword v97, v196, s[40:41] sc1\n" \
  "s_mov_b64 exec, -1\n" \
  "s_waitcnt vmcnt(9)\n" \
  "s_setprio 0\n" \
  "s_branch LG_start\n" \
  ".p2align 6\n" \
  "LA_loop:\n" \
  "LA_mid:\n" \
  "v_mfma_f32_16x16x32_bf16 v[152:155], v[112:115], v[112:115], 0\n" \
  "s_waitcnt lgkmcnt(8)\n" \
  "ds_read_b128 v[100:103], v185 offset:64\n" \
  "v_fmac_f32 v176, v156, v88\n" \
  "v_fmac_f32 v176, v157, v89\n" \
  "v_mfma_f32_16x16x32_bf16 v[152:155], v[116:119], v[116:119], v[152:155]\n" \
  "ds_read2_b32 v[144:145], v186 offset0:0 offset1:2\n" \
  "v_fmac_f32 v176, v158, v90\n" \
  "v_fmac_f32 v176, v159, v91\n" \
  "v_mfma_f32_16x16x32_bf16 v[156:159], v[128:131], v[128:131], 0\n" \
  "ds_read2_b32 v[146:147], v186 offset0:4 offset1:6\n" \
  "v_fmac_f32 v176, v160, v92\n" \
  "v_fmac_f32 v176, v161, v93\n" \
  "v_mad_u32_u16 v187, v183, 16, v190\n" \
  "v_mfma_f32_16x16x32_bf16 v[156:159], v[132:135], v[132:135], v[156:159]\n" \
  "s_waitcnt lgkmcnt(10)\n" \
  "ds_read2_b32 v[148:149], v187 offset0:0 offset1:2\n" \
  "v_fmac_f32 v176, v162, v94\n" \
  "v_fma_f32 v177, v163, v95, v176\n" \
  "v_mfma_f32_16x16x32_bf16 v[160:163], v[128:131], v[136:139], 0\n" \
  "s_waitcnt lgkmcnt(10)\n" \
  "ds_read2_b32 v[150:151], v187 offset0:4 offset1:6\n" \
  "v_mul_f32 v176, v152, v0\n" \
  "v_fmac_f32 v176, v153, v1\n" \
  "v_mfma_f32_16x16x32_bf16 v[160:163], v[132:135], v[140:143], v[160:163]\n" \
  "ds_read_b128 v[180:183], v194\n" \
  "v_fmac_f32 v176, v154, v2\n" \
  "v_fmac_f32 v176, v155, v3\n" \
  "v_mfma_f32_16x16x32_bf16 v[152:155], v[136:139], v[136:139], 0\n" \
  "v_fmac_f32 v176, v156, v4\n" \
  "v_fmac_f32 v176, v157, v5\n" \
  "v_mfma_f32_16x16x32_bf16 v[152:155], v[140:143], v[140:143], v[152:155]\n" \
  "ds_read_b128 v[164:167], v191\n" \
  "v_fmac_f32 v176, v158, v6\n" \
  "v_fmac_f32 v176, v159, v7\n" \
  "v_mfma_f32_16x16x32_bf16 v[156:159], v[112:115], v[136:139], 0\n" \
  "v_fmac_f32 v176, v160, v8\n" \
  "v_fmac_f32 v176, v161, v9\n" \
  "v_mfma_f32_16x16x32_bf16 v[156:159], v[116:119], v[140:143], v[156:159]\n" \
  "v_fmac_f32 v176, v162, v10\n" \
  "v_fmac_f32 v176, v163, v11\n" \
  "v_mfma_f32_16x16x32_bf16 v[160:163], v[112:115], v[128:131], 0\n" \
  "v_fmac_f32 v176, v152, v12\n" \
  "v_fmac_f32 v176, v153, v13\n" \
  "v_add_f32_dpp v177, v177, v177 row_ror:8 row_mask:0xf bank_mask:0xf\n" \
  "v_mfma_f32_16x16x32_bf16 v[160:163], v[116:119], v[132:135], v[160:163]\n" \
  "s_waitcnt lgkmcnt(11)\n" \
  "v_fmac_f32 v176, v154, v14\n" \
  "v_fmac_f32 v176, v155, v15\n" \
  "v_mfma_f32_16x16x32_bf16 v[152:155], v[112:115], v[120:123], 0\n" \
  "s_waitcnt lgkmcnt(10)\n" \
  "v_fmac_f32 v176, v156, v16\n" \
  "v_fmac_f32 v176, v157, v17\n" \
  "v_mfma_f32_16x16x32_bf16 v[152:155], v[116:119], v[124:127], v[152:155]\n" \
  "v_fmac_f32 v176, v158, v18\n" \
  "v_fmac_f32 v176, v159, v19\n" \
  "v_mov_b32_dpp v178, v178 row_ror:4 row_mask:0xf bank_mask:0xf\n" \
  "v_mfma_f32_16x16x32_bf16 v[156:159], v[120:123], v[136:139], 0\n" \
  "v_fmac_f32 v176, v160, v20\n" \
  "v_fmac_f32 v176, v161, v21\n" \
  "v_mfma_f32_16x16x32_bf16 v[156:159], v[124:127], v[140:143], v[156:159]\n" \
  "s_waitcnt lgkmcnt(9)\n" \
  "v_fmac_f32 v176, v162, v22\n" \
  "v_fmac_f32 v176, v163, v23\n" \
  "v_mfma_f32_16x16x32_bf16 v[160:163], v[104:107], v[128:131], 0\n" \
  "s_waitcnt lgkmcnt(1)\n" \
  "v_fmac_f32 v176, v152, v24\n" \
  "v_fmac_f32 v176, v153, v25\n" \
  "v_mad_u32_u16 v184, v182, 16, v188\n" \
  "v_mfma_f32_16x16x32_bf16 v[160:163], v[108:111], v[132:135], v[160:163]\n" \
  "v_fmac_f32 v176, v154, v26\n" \
  "v_fmac_f32 v176, v155, v27\n" \
  "v_mfma_f32_16x16x32_bf16 v[152:155], v[120:123], v[128:131], 0\n" \
  "v_fmac_f32 v176, v156, v28\n" \
  "v_fmac_f32 v176, v157, v29\n" \
  "v_mfma_f32_16x16x32_bf16 v[152:155], v[124:127], v[132:135], v[152:155]\n" \
  "v_fmac_f32 v176, v158, v30\n" \
  "v_fmac_f32 v176, v159, v31\n" \
  "v_add_f32_dpp v178, v177, v177 row_ror:4 row_mask:0xf bank_mask:0x1\n" \
  "v_mfma_f32_16x16x32_bf16 v[156:159], v[96:99], v[128:131], 0\n" \
  "v_fmac_f32 v176, v160, v32\n" \
  "v_fmac_f32 v176, v161, v33\n" \
  "v_mfma_f32_16x16x32_bf16 v[156:159], v[100:103], v[132:135], v[156:159]\n" \
  "v_fmac_f32 v176, v162, v34\n" \
  "v_fmac_f32 v176, v163, v35\n" \
  "v_smfmac_f32_16x16x64_bf16 v[172:175], v[144:147], v[128:135], v193\n" \
  "ds_read_b128 v[128:131], v184 offset:0\n" \
  "v_fmac_f32 v176, v152, v36\n" \
  "v_fmac_f32 v176, v153, v37\n" \
  "v_smfmac_f32_16x16x64_bf16 v[172:175], v[148:151], v[136:143], v193\n" \
  "ds_read_b128 v[132:135], v184 offset:64\n" \
  "v_fmac_f32 v176, v154, v38\n" \
  "v_fmac_f32 v176, v155, v39\n" \
  "v_mfma_f32_16x16x32_bf16 v[160:163], v[96:99], v[112:115], 0\n" \
  "v_fmac_f32 v176, v156, v40\n" \
  "v_fmac_f32 v176, v157, v41\n" \
  "v_mfma_f32_16x16x32_bf16 v[160:163], v[100:103], v[116:119], v[160:163]\n" \
  "v_fmac_f32 v176, v158, v42\n" \
  "v_fmac_f32 v176, v159, v43\n" \
  "v_add_u32 v194, 0x100, v194\n" \
  "v_mfma_f32_16x16x32_bf16 v[152:155], v[104:107], v[112:115], 0\n" \
  "v_fmac_f32 v176, v172, v44\n" \
  "v_fmac_f32 v176, v173, v45\n" \
  "v_mfma_f32_16x16x32_bf16 v[152:155], v[108:111], v[116:119], v[152:155]\n" \
  "v_fmac_f32 v176, v174, v46\n" \
  "v_fmac_f32 v176, v175, v47\n" \
  "v_mfma_f32_16x16x32_bf16 v[156:159], v[104:107], v[136:139], 0\n" \
  "ds_read_b128 v[172:175], v191\n" \
  "v_fmac_f32 v176, v160, v48\n" \
  "v_fmac_f32 v176, v161, v49\n" \
  "v_mad_u32_u16 v185, v181, 16, v188\n" \
  "v_mfma_f32_16x16x32_bf16 v[156:159], v[108:111], v[140:143], v[156:159]\n" \
  "v_fmac_f32 v176, v162, v50\n" \
  "v_fmac_f32 v176, v163, v51\n" \
  "v_mad_u32_u16 v186, v182, 16, v188 op_sel:[1,0,0,0]\n" \
  "v_smfmac_f32_16x16x64_bf16 v[168:171], v[144:147], v[112:119], v193\n" \
  "ds_read_b128 v[112:115], v185 offset:0\n" \
  "v_fmac_f32 v176, v152, v52\n" \
  "v_fmac_f32 v176, v153, v53\n" \
  "v_smfmac_f32_16x16x64_bf16 v[168:171], v[148:151], v[120:127], v193\n" \
  "ds_read_b128 v[116:119], v185 offset:64\n" \
  "v_fmac_f32 v176, v154, v54\n" \
  "v_fmac_f32 v176, v155, v55\n" \
  "v_mfma_f32_16x16x32_bf16 v[160:163], v[96:99], v[136:139], 0\n" \
  "v_fmac_f32 v176, v156, v56\n" \
  "v_fmac_f32 v176, v157, v57\n" \
  "v_mfma_f32_16x16x32_bf16 v[160:163], v[100:103], v[140:143], v[160:163]\n" \
  "s_waitcnt lgkmcnt(5)\n" \
  "v_fmac_f32 v176, v158, v58\n" \
  "v_fmac_f32 v176, v159, v59\n" \
  "v_smfmac_f32_16x16x64_bf16 v[164:167], v[148:151], v[104:111], v193\n" \
  "ds_read_b128 v[136:139], v186 offset:0\n" \
  "v_fmac_f32 v176, v168, v60\n" \
  "v_fmac_f32 v176, v169, v61\n" \
  "v_smfmac_f32_16x16x64_bf16 v[164:167], v[144:147], v[96:103], v193\n" \
  "ds_read_b128 v[140:143], v186 offset:64\n" \
  "v_fmac_f32 v176, v170, v62\n" \
  "v_fmac_f32 v176, v171, v63\n" \
  "v_mfma_f32_16x16x32_bf16 v[152:155], v[120:123], v[120:123], 0\n" \
  "v_fmac_f32 v176, v160, v64\n" \
  "v_fmac_f32 v176, v161, v65\n" \
  "v_mfma_f32_16x16x32_bf16 v[152:155], v[124:127], v[124:127], v[152:155]\n" \
  "ds_read_b128 v[168:171], v191\n" \
  "v_fmac_f32 v176, v162, v66\n" \
  "v_fmac_f32 v176, v163, v67\n" \
  "v_mad_u32_u16 v187, v181, 16, v188 op_sel:[1,0,0,0]\n" \
  "v_mfma_f32_16x16x32_bf16 v[156:159], v[104:107], v[120:123], 0\n" \
  "v_fmac_f32 v176, v164, v68\n" \
  "v_fmac_f32 v176, v165, v69\n" \
  "v_mad_u32_u16 v184, v180, 16, v188 op_sel:[1,0,0,0]\n" \
  "v_mfma_f32_16x16x32_bf16 v[156:159], v[108:111], v[124:127], v[156:159]\n" \
  "v_fmac_f32 v176, v166, v70\n" \
  "v_fmac_f32 v176, v167, v71\n" \
  "v_mfma_f32_16x16x32_bf16 v[160:163], v[96:99], v[120:123], 0\n" \
  "ds_read_b128 v[120:123], v187 offset:0\n" \
  "v_fmac_f32 v176, v152, v72\n" \
  "v_fmac_f32 v176, v153, v73\n" \
  "v_mfma_f32_16x16x32_bf16 v[160:163], v[100:103], v[124:127], v[160:163]\n" \
  "v_fmac_f32 v176, v154, v74\n" \
  "v_fmac_f32 v176, v155, v75\n" \
  "v_mad_u32_u16 v185, v180, 16, v188\n" \
  "v_mfma_f32_16x16x32_bf16 v[152:155], v[96:99], v[104:107], 0\n" \
  "ds_read_b128 v[124:127], v187 offset:64\n" \
  "v_fmac_f32 v176, v156, v76\n" \
  "v_fmac_f32 v176, v157, v77\n" \
  "v_mfma_f32_16x16x32_bf16 v[152:155], v[100:103], v[108:111], v[152:155]\n" \
  "v_fmac_f32 v176, v158, v78\n" \
  "v_fmac_f32 v176, v159, v79\n" \
  "v_mfma_f32_16x16x32_bf16 v[156:159], v[104:107], v[104:107], 0\n" \
  "ds_read_b128 v[104:107], v184 offset:0\n" \
  "v_fmac_f32 v176, v160, v80\n" \
  "v_fmac_f32 v176, v161, v81\n" \
  "v_mfma_f32_16x16x32_bf16 v[156:159], v[108:111], v[108:111], v[156:159]\n" \
  "s_sub_u32 s51, s51, 1\n" \
  "v_fmac_f32 v176, v162, v82\n" \
  "v_fmac_f32 v176, v163, v83\n" \
  "v_mad_u32_u16 v186, v183, 16, v189\n" \
  "v_mfma_f32_16x16x32_bf16 v[160:163], v[148:151], v[144:147], 0\n" \
  "s_cmp_lg_u32 s51, 0\n" \
  "ds_read_b128 v[108:111], v184 offset:64\n" \
  "v_fmac_f32 v176, v152, v84\n" \
  "v_fmac_f32 v176, v153, v85\n" \
  "v_mfma_f32_16x16x32_bf16 v[160:163], v[96:99], v[96:99], v[160:163]\n" \
  "s_waitcnt lgkmcnt(8)\n" \
  "ds_read_b128 v[96:99], v185 offset:0\n" \
  "v_fmac_f32 v176, v154, v86\n" \
  "v_fmac_f32 v176, v155, v87\n" \
  "v_mfma_f32_16x16x32_bf16 v[160:163], v[100:103], v[100:103], v[160:163]\n" \
  "s_cbranch_scc1 LA_loop\n" \
  "s_cmp_ge_u32 s50, 6\n" \
  "s_cbranch_scc1 LA_ret\n" \
  "s_set_gpr_idx_on s50, gpr_idx(DST)\n" \
  "v_mov_b32 v196, v178\n" \
  "s_lshl_b32 s35, s50, 3\n" \
  "s_waitcnt vmcnt(0)\n" \
  "s_set_gpr_idx_on s35, gpr_idx(SRC0,SRC1)\n" \
  "v_cvt_pk_u16_u32 v210, v208, v209\n" \
  "v_cvt_pk_u16_u32 v211, v210, v211\n" \
  "v_cvt_pk_u16_u32 v212, v212, v213\n" \
  "v_cvt_pk_u16_u32 v213, v214, v215\n" \
  "s_set_gpr_idx_off\n" \
  "v_pk_lshrrev_b16 v214, 3, v210 op_sel_hi:[0,1]\n" \
  "v_pk_mad_u16 v210, v210, 8, v214 op_sel_hi:[1,0,1]\n" \
  "v_pk_lshrrev_b16 v215, 3, v211 op_sel_hi:[0,1]\n" \
  "v_pk_mad_u16 v211, v211, 8, v215 op_sel_hi:[1,0,1]\n" \
  "v_pk_lshrrev_b16 v214, 3, v212 op_sel_hi:[0,1]\n" \
  "v_pk_mad_u16 v212, v212, 8, v214 op_sel_hi:[1,0,1]\n" \
  "v_pk_lshrrev_b16 v215, 3, v213 op_sel_hi:[0,1]\n" \
  "v_pk_mad_u16 v213, v213, 8, v215 op_sel_hi:[1,0,1]\n" \
  "s_lshr_b32 s35, s38, 1\n" \
  "v_add_u32 v214, s35, v195\n" \
  "v_alignbit_b32 v212, v212, v212, v192\n" \
  "v_alignbit_b32 v213, v213, v213, v192\n" \
  "ds_write2_b32 v214, v210, v211 offset0:0 offset1:4\n" \
  "ds_write2_b32 v214, v212, v213 offset0:2 offset1:6\n" \
  "ds_read_b32 v209, v208 offset:4\n" \
  "v_subrev_u32 v194, s38, v194\n" \
  "s_xor_b32 s38, s38, 0x800\n" \
  "s_add_u32 s50, s50, 1\n" \
  "s_mov_b32 s51, 4\n" \
  "s_branch LA_loop\n" \
  "LA_ret:\n" \
  "s_setpc_b64 s[52:53]\n" \
  "LB_start:\n" \
  "s_setprio 2\n" \
  "s_lshl_b32 s35, s31, 7\n" \
  "s_waitcnt lgkmcnt(0)\n" \
  "s_add_u32 s40, s10, s35\n" \
  "s_addc_u32 s41, s11, 0\n" \
  "s_add_u32 s37, s36, 32\n" \
  "L3_:\n" \
  "s_sub_u32 s35, s36, 125\n" \
  "s_min_u32 s35, s35, s36\n" \
  "s_mul_i32 m0, s35, 1040\n" \
  "s_lshl_b32 s35, s35, 11\n" \
  "s_add_u32 s42, s40, s35\n" \
  "s_addc_u32 s43, s41, 0\n" \
  "global_load_lds_dwordx4 v254, s[42:43]\n" \
  "s_add_u32 s36, s36, 4\n" \
  "s_sub_u32 s35, s36, 125\n" \
  "s_min_u32 s35, s35, s36\n" \
  "s_mul_i32 m0, s35, 1040\n" \
  "s_lshl_b32 s35, s35, 11\n" \
  "s_add_u32 s42, s40, s35\n" \
  "s_addc_u32 s43, s41, 0\n" \
  "global_load_lds_dwordx4 v254, s[42:43]\n" \
  "s_add_u32 s36, s36, 4\n" \
  "s_cmp_lg_u32 s37, s36\n" \
  "s_cbranch_scc1 L3_\n" \
  "v_mov_b32 v190, 163328\n" \
  "v_mov_b64 v[2:3], 0\n" \
  "ds_write_b64 v190, v[2:3]\n" \
  "ds_write_b64 v190, v[2:3] offset:8\n" \
  "ds_write_b64 v190, v[2:3] offset:16\n" \
  "s_waitcnt lgkmcnt(0)\n" \
  "s_barrier\n" \
  "v_lshlrev_b32 v247, 7, v255\n" \
  "s_add_u32 s37, s36, 96\n" \
  "L4_:\n" \
  "s_sub_u32 s35, s36, 125\n" \
  "s_min_u32 s35, s35, s36\n" \
  "s_mul_i32 m0, s35, 1040\n" \
  "s_lshl_b32 s35, s35, 11\n" \
  "s_add_u32 s42, s40, s35\n" \
  "s_addc_u32 s43, s41, 0\n" \
  "global_load_lds_dwordx4 v254, s[42:43]\n" \
  "s_add_u32 s36, s36, 4\n" \
  "s_sub_u32 s35, s36, 125\n" \
  "s_min_u32 s35, s35, s36\n" \
  "s_mul_i32 m0, s35, 1040\n" \
  "s_lshl_b32 s35, s35, 11\n" \
  "s_add_u32 s42, s40, s35\n" \
  "s_addc_u32 s43, s41, 0\n" \
  "global_load_lds_dwordx4 v254, s[42:43]\n" \
  "s_add_u32 s36, s36, 4\n" \
  "s_cmp_lg_u32 s37, s36\n" \
  "s_cbranch_scc1 L4_\n" \
  "s_add_u32 s48, s24, 110002176\n" \
  "s_addc_u32 s49, s25, 0\n" \
  "s_lshl_b32 s35, s26, 2\n" \
  "v_mov_b32 v193, s35\n" \
  "global_load_dword v193, v193, s[48:49] offset:1024 sc1\n" \
  "v_mov_b32 v191, 1\n" \
  "v_and_b32 v252, 15, v255\n" \
  "v_lshrrev_b32 v253, 4, v255\n" \
  "s_lshl2_add_u32 s35, s31, s28\n" \
  "s_lshl_b32 s35, s35, 1\n" \
  "v_bfe_u32 v1, v255, 2, 1\n" \
  "v_add_u32 v1, s35, v1\n" \
  "v_and_b32 v2, 3, v255\n" \
  "v_lshlrev_b32 v3, 5, v1\n" \
  "v_lshl_add_u32 v3, v2, 3, v3\n" \
  "v_lshlrev_b32 v4, 2, v2\n" \
  "v_add_u32 v4, 380, v4\n" \
  "v_cmp_gt_u32 vcc, 12, v1\n" \
  "v_lshrrev_b32 v5, 3, v255\n" \
  "v_mul_u32_u24 v5, 6400, v5\n" \
  "v_cndmask_b32 v3, v4, v3, vcc\n" \
  "v_add_u32 v5, 3200, v5\n" \
  "v_add_u32 v227, v5, v3\n" \
  "v_lshl_add_u32 v3, v1, 2, v2\n" \
  "v_add_u32 v4, 48, v3\n" \
  "v_mul_u32_u24 v5, 205, v3\n" \
  "v_lshrrev_b32 v5, 11, v5\n" \
  "v_mul_u32_u24 v5, 10, v5\n" \
  "v_sub_u32 v5, v3, v5\n" \
  "v_lshlrev_b32 v236, 26, v5\n" \
  "v_mul_u32_u24 v5, 205, v4\n" \
  "v_lshrrev_b32 v5, 11, v5\n" \
  "v_mul_u32_u24 v5, 10, v5\n" \
  "v_sub_u32 v5, v4, v5\n" \
  "v_lshlrev_b32 v237, 26, v5\n" \
  "s_lshl2_add_u32 s4, s31, s28\n" \
  "s_lshl_b32 s4, s4, 7\n" \
  "s_add_u32 s36, s4, 96\n" \
  "s_min_u32 s36, s36, 984\n" \
  "s_add_u32 s37, s4, 112\n" \
  "s_min_u32 s37, s37, 984\n" \
  "v_add_u32 v186, s4, v252\n" \
  "v_add_u32 v187, s36, v252\n" \
  "v_add_u32 v188, s37, v252\n" \
  "s_mul_i32 s38, s7, 51200\n" \
  "s_add_u32 s42, s8, s38\n" \
  "s_addc_u32 s43, s9, 0\n" \
  "s_xor_b32 s38, s31, 1\n" \
  "s_lshl_b32 s38, s38, 7\n" \
  "s_add_u32 s94, s10, s38\n" \
  "s_addc_u32 s95, s11, 0\n" \
  "s_mul_i32 s5, s28, 4224\n" \
  "s_add_u32 s5, s5, 146432\n" \
  "v_lshrrev_b32 v2, 3, v186\n" \
  "v_lshl_add_u32 v2, v186, 3, v2\n" \
  "v_add_lshl_u32 v249, v2, v253, 4\n" \
  "v_lshrrev_b32 v2, 3, v187\n" \
  "v_lshl_add_u32 v2, v187, 3, v2\n" \
  "v_add_lshl_u32 v250, v2, v253, 4\n" \
  "v_lshrrev_b32 v2, 3, v188\n" \
  "v_lshl_add_u32 v2, v188, 3, v2\n" \
  "v_add_lshl_u32 v251, v2, v253, 4\n" \
  "v_mul_u32_u24 v1, 1600000, v253\n" \
  "v_lshl_add_u32 v186, v186, 4, v1\n" \
  "v_lshl_add_u32 v187, v187, 4, v1\n" \
  "v_lshl_add_u32 v188, v188, 4, v1\n" \
  "v_lshlrev_b32 v1, 4, v253\n" \
  "v_xor_b32 v244, v1, v254\n" \
  "v_lshl_add_u32 v245, v255, 4, s5\n" \
  "v_lshrrev_b32 v1, 1, v252\n" \
  "s_waitcnt vmcnt(1)\n" \
  "s_mov_b64 exec, 1\n" \
  "ds_add_u32 v190, v191 offset:12\n" \
  "s_mov_b64 exec, -1\n" \
  "s_memtime s[88:89]\n" \
  "v_xor_b32 v1, v1, v253\n" \
  "v_lshlrev_b32 v1, 4, v1\n" \
  "v_lshl_add_u32 v1, v252, 7, v1\n" \
  "v_add_u32 v246, s5, v1\n" \
  "v_xor_b32 v1, 64, v1\n" \
  "v_add_u32 v242, s5, v1\n" \
  "v_lshlrev_b32 v1, 4, v255\n" \
  "v_xor_b32 v1, 64, v1\n" \
  "v_add_u32 v243, s5, v1\n" \
  "v_lshrrev_b32 v1, 2, v255\n" \
  "s_lshl_b32 s35, s28, 5\n" \
  "s_lshl_b32 s36, s31, 6\n" \
  "s_add_u32 s35, s35, s36\n" \
  "s_and_b32 s35, s35, 127\n" \
  "v_add_u32 v1, s35, v1\n" \
  "v_lshrrev_b32 v2, 4, v255\n" \
  "v_and_b32 v2, 2, v2\n" \
  "v_xor_b32 v2, v2, v255\n" \
  "v_and_b32 v2, 3, v2\n" \
  "v_lshlrev_b32 v2, 4, v2\n" \
  "v_lshl_add_u32 v189, v1, 10, v2\n" \
  "v_add_u32 v194, 15360, v189\n" \
  "v_lshrrev_b32 v1, 2, v252\n" \
  "v_lshlrev_b32 v1, 6, v1\n" \
  "v_lshl_add_u32 v1, v253, 9, v1\n" \
  "v_and_b32 v2, 3, v252\n" \
  "v_lshl_add_u32 v1, v2, 3, v1\n" \
  "v_and_b32 v2, 1, v253\n" \
  "v_lshl_add_u32 v1, v2, 5, v1\n" \
  "v_add_u32 v184, 138240, v1\n" \
  "v_xor_b32 v1, 32, v1\n" \
  "v_add_u32 v185, 138240, v1\n" \
  "s_bfe_u32 s35, s30, 0x10001\n" \
  "s_lshl1_add_u32 s35, s29, s35\n" \
  "s_lshr_b32 s6, s30, 2\n" \
  "s_lshl_b32 s6, s6, 18\n" \
  "s_lshl_b32 s38, s35, 6\n" \
  "s_add_u32 s92, s14, s38\n" \
  "s_addc_u32 s93, s15, 0\n" \
  "s_lshr_b32 s37, s30, 3\n" \
  "s_add_u32 s37, s37, 96\n" \
  "s_lshl_b32 s34, s37, 17\n" \
  "s_mul_i32 s35, s35, 400\n" \
  "s_add_u32 s37, s37, s35\n" \
  "s_mul_i32 s37, s37, 16000\n" \
  "s_add_u32 s46, s24, s37\n" \
  "s_addc_u32 s47, s25, 0\n" \
  "s_lshr_b32 s36, s30, 2\n" \
  "s_add_u32 s36, s36, s35\n" \
  "s_mul_i32 s36, s36, 16000\n" \
  "s_add_u32 s44, s24, s36\n" \
  "s_addc_u32 s45, s25, 0\n" \
  "s_lshl_b32 s96, s28, 11\n" \
  "s_add_u32 s96, s96, 138240\n" \
  "v_lshlrev_b32 v238, 4, v255\n" \
  "v_mov_b32 v234, 0\n" \
  "s_lshl2_add_u32 s33, s26, s28\n" \
  "s_mov_b32 s52, 0x10002\n" \
  "s_mov_b32 s53, 0xc0c0400\n" \
  "s_mov_b32 s54, 0x4000c0c\n" \
  "s_mov_b32 s55, 0x3030303\n" \
  "s_mov_b32 s56, 0x55555555\n" \
  "s_mov_b32 s38, 0x0\n" \
  "s_mov_b32 s98, 0x0\n" \
  "s_mov_b32 s99, 0x8\n" \
  "s_mov_b32 s97, 0x2000\n" \
  "v_xor_b32 v245, 64, v244\n" \
  "v_add_u32 v245, 0x400, v245\n" \
  "s_setprio 0\n" \
  "L5_:\n" \
  "ds_read_b32 v192, v190 offset:16\n" \
  "s_waitcnt lgkmcnt(0)\n" \
  "v_readfirstlane_b32 s35, v192\n" \
  "s_cmp_lt_u32 s35, 4\n" \
  "s_cbranch_scc1 L5_\n" \
  "s_add_u32 s35, s4, 0\n" \
  "s_min_u32 s35, s35, 984\n" \
  "s_lshl_b32 s35, s35, 8\n" \
  "s_add_u32 m0, s5, 0\n" \
  "s_add_u32 s58, s94, s35\n" \
  "s_addc_u32 s59, s95, 0\n" \
  "global_load_lds_dwordx4 v244, s[58:59]\n" \
  "global_load_lds_dwordx4 v245, s[58:59] offset:1024\n" \
  "s_add_u32 s35, s4, 16\n" \
  "s_min_u32 s35, s35, 984\n" \
  "s_lshl_b32 s35, s35, 8\n" \
  "s_add_u32 m0, s5, 2048\n" \
  "s_add_u32 s58, s94, s35\n" \
  "s_addc_u32 s59, s95, 0\n" \
  "global_load_lds_dwordx4 v244, s[58:59]\n" \
  "global_load_lds_dwordx4 v245, s[58:59] offset:1024\n" \
  "L6_:\n" \
  "ds_read_b32 v192, v190 offset:12\n" \
  "s_waitcnt lgkmcnt(0)\n" \
  "v_readfirstlane_b32 s35, v192\n" \
  "s_cmp_lt_u32 s35, 4\n" \
  "s_cbranch_scc1 L6_\n" \
  "s_waitcnt vmcnt(0)\n" \
  "ds_read_b128 v[8:11], v246 offset:0\n" \
  "ds_read_b128 v[12:15], v242 offset:0\n" \
  "ds_read_b128 v[24:27], v246 offset:2048\n" \
  "ds_read_b128 v[28:31], v242 offset:2048\n" \
  "ds_read_b128 v[0:3], v249 offset:0\n" \
  "ds_read_b128 v[4:7], v249 offset:64\n" \
  "ds_read_b128 v[16:19], v249 offset:2080\n" \
  "ds_read_b128 v[20:23], v249 offset:2144\n" \
  "s_waitcnt lgkmcnt(4)\n" \
  "s_add_u32 s35, s4, 32\n" \
  "s_min_u32 s35, s35, 984\n" \
  "s_lshl_b32 s35, s35, 8\n" \
  "s_add_u32 m0, s5, 0\n" \
  "s_add_u32 s58, s94, s35\n" \
  "s_addc_u32 s59, s95, 0\n" \
  "global_load_lds_dwordx4 v244, s[58:59]\n" \
  "global_load_lds_dwordx4 v245, s[58:59] offset:1024\n" \
  "s_add_u32 s35, s4, 48\n" \
  "s_min_u32 s35, s35, 984\n" \
  "s_lshl_b32 s35, s35, 8\n" \
  "s_add_u32 m0, s5, 2048\n" \
  "s_add_u32 s58, s94, s35\n" \
  "s_addc_u32 s59, s95, 0\n" \
  "global_load_lds_dwordx4 v244, s[58:59]\n" \
  "global_load_lds_dwordx4 v245, s[58:59] offset:1024\n" \
  "s_waitcnt vmcnt(0)\n" \
  "ds_read_b128 v[40:43], v246 offset:0\n" \
  "ds_read_b128 v[44:47], v242 offset:0\n" \
  "ds_read_b128 v[56:59], v246 offset:2048\n" \
  "ds_read_b128 v[60:63], v242 offset:2048\n" \
  "ds_read_b128 v[32:35], v249 offset:4160\n" \
  "ds_read_b128 v[36:39], v249 offset:4224\n" \
  "ds_read_b128 v[48:51], v249 offset:6240\n" \
  "ds_read_b128 v[52:55], v249 offset:6304\n" \
  "s_waitcnt lgkmcnt(4)\n" \
  "s_add_u32 s40, s92, s6\n" \
  "s_addc_u32 s41, s93, 0\n" \
  "s_add_u32 s6, s6, 2097152\n" \
  "s_mov_b32 m0, s96\n" \
  "s_nop 0\n" \
  "global_load_lds_dwordx4 v189, s[40:41]\n" \
  "global_load_lds_dwordx4 v194, s[40:41] offset:1024\n" \
  "global_load_dwordx2 v[196:197], v227, s[42:43] offset:-3200\n" \
  "global_load_dwordx2 v[198:199], v227, s[42:43] offset:-2800\n" \
  "global_load_dwordx2 v[200:201], v227, s[42:43] offset:-2400\n" \
  "global_load_dwordx2 v[202:203], v227, s[42:43] offset:-1600\n" \
  "global_load_dwordx2 v[204:205], v227, s[42:43] offset:-1200\n" \
  "global_load_dwordx2 v[206:207], v227, s[42:43] offset:-800\n" \
  "global_load_dwordx2 v[208:209], v227, s[42:43] offset:-400\n" \
  "global_load_dwordx2 v[210:211], v227, s[42:43] offset:0\n" \
  "global_load_dwordx2 v[212:213], v227, s[42:43] offset:400\n" \
  "global_load_dwordx2 v[214:215], v227, s[42:43] offset:800\n" \
  "global_load_dwordx2 v[216:217], v227, s[42:43] offset:1200\n" \
  "global_load_dwordx2 v[218:219], v227, s[42:43] offset:-2000\n" \
  "global_load_dwordx2 v[220:221], v227, s[42:43] offset:1600\n" \
  "global_load_dwordx2 v[222:223], v227, s[42:43] offset:2000\n" \
  "global_load_dwordx2 v[224:225], v227, s[42:43] offset:2400\n" \
  "global_load_dwordx2 v[226:227], v227, s[42:43] offset:2800\n" \
  "s_add_u32 s35, s4, 64\n" \
  "s_min_u32 s35, s35, 984\n" \
  "s_lshl_b32 s35, s35, 8\n" \
  "s_add_u32 m0, s5, 0\n" \
  "s_add_u32 s58, s94, s35\n" \
  "s_addc_u32 s59, s95, 0\n" \
  "global_load_lds_dwordx4 v244, s[58:59]\n" \
  "global_load_lds_dwordx4 v245, s[58:59] offset:1024\n" \
  "s_add_u32 s35, s4, 80\n" \
  "s_min_u32 s35, s35, 984\n" \
  "s_lshl_b32 s35, s35, 8\n" \
  "s_add_u32 m0, s5, 2048\n" \
  "s_add_u32 s58, s94, s35\n" \
  "s_addc_u32 s59, s95, 0\n" \
  "global_load_lds_dwordx4 v244, s[58:59]\n" \
  "global_load_lds_dwordx4 v245, s[58:59] offset:1024\n" \
  "s_waitcnt vmcnt(0)\n" \
  "ds_read_b128 v[72:75], v246 offset:0\n" \
  "ds_read_b128 v[76:79], v242 offset:0\n" \
  "ds_read_b128 v[88:91], v246 offset:2048\n" \
  "ds_read_b128 v[92:95], v242 offset:2048\n" \
  "ds_read_b128 v[64:67], v249 offset:8320\n" \
  "ds_read_b128 v[68:71], v249 offset:8384\n" \
  "ds_read_b128 v[80:83], v249 offset:10400\n" \
  "ds_read_b128 v[84:87], v249 offset:10464\n" \
  "s_waitcnt lgkmcnt(4)\n" \
  "s_add_u32 s35, s4, 96\n" \
  "s_min_u32 s35, s35, 984\n" \
  "s_lshl_b32 s35, s35, 8\n" \
  "s_add_u32 m0, s5, 0\n" \
  "s_add_u32 s58, s94, s35\n" \
  "s_addc_u32 s59, s95, 0\n" \
  "global_load_lds_dwordx4 v244, s[58:59]\n" \
  "global_load_lds_dwordx4 v245, s[58:59] offset:1024\n" \
  "s_add_u32 s35, s4, 112\n" \
  "s_min_u32 s35, s35, 984\n" \
  "s_lshl_b32 s35, s35, 8\n" \
  "s_add_u32 m0, s5, 2048\n" \
  "s_add_u32 s58, s94, s35\n" \
  "s_addc_u32 s59, s95, 0\n" \
  "global_load_lds_dwordx4 v244, s[58:59]\n" \
  "global_load_lds_dwordx4 v245, s[58:59] offset:1024\n" \
  "s_lshl2_add_u32 s35, s26, s28\n" \
  "s_mul_i32 s35, s35, 6400\n" \
  "s_add_u32 s35, s35, 0\n" \
  "s_add_u32 s58, s14, s35\n" \
  "s_addc_u32 s59, s15, 0\n" \
  "s_mov_b32 exec_lo, 0xffffffff\n" \
  "s_mov_b32 exec_hi, 0x3ffff\n" \
  "global_load_dword v248, v247, s[58:59]\n" \
  "s_mov_b64 exec, -1\n" \
  "s_lshl2_add_u32 s35, s26, s28\n" \
  "s_mul_i32 s35, s35, 6400\n" \
  "s_add_u32 s35, s35, 6553600\n" \
  "s_add_u32 s58, s14, s35\n" \
  "s_addc_u32 s59, s15, 0\n" \
  "s_mov_b32 exec_lo, 0xffffffff\n" \
  "s_mov_b32 exec_hi, 0x3ffff\n" \
  "global_load_dword v248, v247, s[58:59]\n" \
  "s_mov_b64 exec, -1\n" \
  "s_getpc_b64 s[58:59]\n" \
  "LB_pc:\n" \
  "s_sub_u32 s58, s58, LB_pc-LZ_first\n" \
  "s_subb_u32 s59, s59, 0\n" \
  "s_mul_i32 s35, s28, 7168\n" \
  "v_lshl_add_u32 v195, v255, 7, s35\n" \
  "v_min_u32 v195, LZ_end-LZ_first, v195\n" \
  "s_mov_b32 exec_hi, 0xffffff\n" \
  "global_load_dword v248, v195, s[58:59]\n" \
  "s_mov_b64 exec, -1\n" \
  "s_waitcnt vmcnt(3)\n" \
  "ds_read_b128 v[104:107], v246 offset:0\n" \
  "ds_read_b128 v[108:111], v242 offset:0\n" \
  "ds_read_b128 v[120:123], v246 offset:2048\n" \
  "ds_read_b128 v[124:127], v242 offset:2048\n" \
  "ds_read_b128 v[96:99], v250 offset:0\n" \
  "ds_read_b128 v[100:103], v250 offset:64\n" \
  "ds_read_b128 v[112:115], v251 offset:0\n" \
  "ds_read_b128 v[116:119], v251 offset:64\n" \
  "s_waitcnt lgkmcnt(4)\n" \
  "s_waitcnt vmcnt(0)\n" \
  "s_sleep 8\n" \
  "s_setprio 2\n" \
  "s_mul_i32 s36, s28, 2176\n" \
  "s_add_u32 s36, s36, 154624\n" \
  "v_bfe_u32 v228, v255, 3, 1\n" \
  "v_and_b32 v229, 1, v255\n" \
  "v_lshl_add_u32 v230, v228, 1, v229\n" \
  "v_lshl_add_u32 v231, v229, 1, v228\n" \
  "v_xor_b32 v229, 1, v230\n" \
  "s_mov_b32 vcc_lo, 0xcccccccc\n" \
  "s_mov_b32 vcc_hi, vcc_lo\n" \
  "v_cndmask_b32 v228, v230, v231, vcc\n" \
  "v_cndmask_b32 v229, v229, v231, vcc\n" \
  "v_and_b32 v230, 4, v255\n" \
  "v_mul_u32_u24 v230, 272, v230\n" \
  "v_lshl_add_u32 v230, v253, 2, v230\n" \
  "v_and_or_b32 v230, v255, 2, v230\n" \
  "v_add_u32 v230, s36, v230\n" \
  "s_movk_i32 s35, 272\n" \
  "v_mad_u32_u24 v195, v228, s35, v230\n" \
  "v_mad_u32_u24 v235, v229, s35, v230\n" \
  "v_lshl_add_u32 v230, v253, 4, v238\n" \
  "v_add_u32 v254, s36, v230\n" \
  "v_readfirstlane_b32 s39, v193\n" \
  "s_lshl_b32 s35, s26, 2\n" \
  "s_add_u32 s39, s39, 1\n" \
  "v_mov_b32 v193, s35\n" \
  "v_mov_b64 v[240:241], -1\n" \
  "v_mov_b64 v[242:243], -1\n" \
  "s_waitcnt lgkmcnt(0)\n" \
  "s_mov_b64 exec, 1\n" \
  "ds_add_u32 v190, v191 offset:0\n" \
  "s_mov_b64 exec, -1\n" \
  "L7_:\n" \
  "ds_read_b32 v192, v190 offset:0\n" \
  "s_waitcnt lgkmcnt(0)\n" \
  "v_readfirstlane_b32 s35, v192\n" \
  "s_cmp_lt_u32 s35, 4\n" \
  "s_cbranch_scc1 L7_\n" \
  "s_add_u32 s96, s96, 8192\n" \
  "s_add_u32 s40, s92, s6\n" \
  "s_addc_u32 s41, s93, 0\n" \
  "s_add_u32 s6, s6, 2097152\n" \
  "s_mov_b32 m0, s96\n" \
  "s_nop 0\n" \
  "global_load_lds_dwordx4 v189, s[40:41]\n" \
  "global_load_lds_dwordx4 v194, s[40:41] offset:1024\n" \
  "s_sub_u32 s96, s96, 8192\n" \
  "ds_read_b64_tr_b16 v[128:129], v184 offset:0\n" \
  "ds_read_b64_tr_b16 v[130:131], v184 offset:256\n" \
  "ds_read_b64_tr_b16 v[132:133], v184 offset:2048\n" \
  "ds_read_b64_tr_b16 v[134:135], v184 offset:2304\n" \
  "ds_read_b64_tr_b16 v[136:137], v184 offset:4096\n" \
  "ds_read_b64_tr_b16 v[138:139], v184 offset:4352\n" \
  "ds_read_b64_tr_b16 v[140:141], v184 offset:6144\n" \
  "ds_read_b64_tr_b16 v[142:143], v184 offset:6400\n" \
  "ds_read_b64_tr_b16 v[156:157], v185 offset:6144\n" \
  "ds_read_b64_tr_b16 v[158:159], v185 offset:6400\n" \
  "ds_read_b64_tr_b16 v[152:153], v185 offset:4096\n" \
  "ds_read_b64_tr_b16 v[154:155], v185 offset:4352\n" \
  "ds_read_b64_tr_b16 v[148:149], v185 offset:2048\n" \
  "ds_read_b64_tr_b16 v[150:151], v185 offset:2304\n" \
  "ds_read_b64_tr_b16 v[144:145], v185 offset:0\n" \
  "ds_read_b64_tr_b16 v[146:147], v185 offset:256\n" \
  "v_mov_b32 v192, s39\n" \
  "s_cmp_lg_u32 s28, 0\n" \
  "s_cbranch_scc1 LB_nc\n" \
  "s_mov_b64 exec, 1\n" \
  "global_store_dword v193, v192, s[48:49] offset:0 sc1\n" \
  "s_mov_b64 exec, -1\n" \
  "LB_nc:\n" \
  "s_lshl2_add_u32 s35, s7, s28\n" \
  "s_lshl_b32 s35, s35, 7\n" \
  "s_lshl_b32 s36, s31, 16\n" \
  "s_add_u32 s35, s35, s36\n" \
  "s_add_u32 s35, s35, 109871104\n" \
  "s_add_u32 s58, s24, s35\n" \
  "s_addc_u32 s59, s25, 0\n" \
  "s_mov_b64 exec, 0xff\n" \
  "global_store_dwordx4 v238, v[240:243], s[58:59] sc1\n" \
  "s_mov_b64 exec, -1\n" \
  "s_cmp_lg_u32 s33, 1021\n" \
  "s_cbranch_scc1 LB_nm\n" \
  "v_mov_b64 v[240:241], 0\n" \
  "v_mov_b64 v[242:243], 0\n" \
  "global_store_dwordx4 v238, v[240:243], s[48:49] offset:2048 sc1\n" \
  "s_nop 1\n" \
  "v_lshlrev_b32 v242, 8, v255\n" \
  "global_store_dwordx2 v242, v[240:241], s[48:49] offset:3072 sc1\n" \
  "v_add_u32 v242, 0x4000, v242\n" \
  "global_store_dwordx2 v242, v[240:241], s[48:49] offset:3072 sc1\n" \
  "v_add_u32 v242, 0x4000, v242\n" \
  "global_store_dwordx2 v242, v[240:241], s[48:49] offset:3072 sc1\n" \
  "v_add_u32 v242, 0x4000, v242\n" \
  "global_store_dwordx2 v242, v[240:241], s[48:49] offset:3072 sc1\n" \
  "v_add_u32 v242, 0x4000, v242\n" \
  "LB_nm:\n" \
  "s_mov_b32 s86, 0x78000b0\n" \
  "s_mov_b32 s87, 0x0\n" \
  "s_or_b32 s86, s86, s87\n" \
  "s_setprio 2\n" \
  "s_memtime s[90:91]\n" \
  "s_memrealtime s[42:43]\n" \
  "LT:\n" \
  "s_cmp_eq_u32 s38, 6\n" \
  "s_cselect_b32 s78, 0x33333333, -1\n" \
  "s_cmp_eq_u32 s38, 7\n" \
  "s_cselect_b32 s78, 0x33333333, s78\n" \
  "s_cmp_eq_u32 s38, 18\n" \
  "s_cselect_b32 s80, 0x33333333, -1\n" \
  "s_cmp_eq_u32 s38, 19\n" \
  "s_cselect_b32 s80, 0x33333333, s80\n" \
  "s_mov_b32 s79, s78\n" \
  "s_mov_b32 s81, s80\n" \
  "s_mov_b32 s37, s38\n" \
  "s_add_u32 s57, s38, 2\n" \
  "s_add_u32 s3, s38, 4\n" \
  "s_add_u32 s38, s38, 6\n" \
  "s_cmp_lt_u32 s57, 32\n" \
  "s_cselect_b32 s57, s57, 37\n" \
  "s_cselect_b32 s3, s3, 37\n" \
  "s_waitcnt lgkmcnt(12)\n" \
  "v_mfma_f32_16x16x32_bf16 v[160:163], v[128:131], v[0:3], 0\n" \
  "s_mov_b64 vcc, s[78:79]\n" \
  "v_cndmask_b32_dpp v234, v234, v234, vcc row_ror:8 row_mask:0xf bank_mask:0xf\n" \
  "v_mfma_f32_16x16x32_bf16 v[160:163], v[132:135], v[4:7], v[160:163]\n" \
  "s_waitcnt lgkmcnt(8)\n" \
  "v_mfma_f32_16x16x32_bf16 v[160:163], v[136:139], v[8:11], v[160:163]\n" \
  "s_set_gpr_idx_on s37, gpr_idx(SRC0)\n" \
  "v_mad_u32_u24 v232, v196, s52, v236\n" \
  "s_set_gpr_idx_off\n" \
  "v_mfma_f32_16x16x32_bf16 v[160:163], v[140:143], v[12:15], v[160:163]\n" \
  "s_waitcnt lgkmcnt(0)\n" \
  "v_lshrrev_b32 v228, v232, v234\n" \
  "v_lshrrev_b32_dpp v229, v232, v234 quad_perm:[2,3,0,1] row_mask:0xf bank_mask:0xf\n" \
  "v_mfma_f32_16x16x32_bf16 v[164:167], v[156:159], v[12:15], 0\n" \
  "v_lshrrev_b32_dpp v230, v232, v234 quad_perm:[1,2,3,0] row_mask:0xf bank_mask:0xf\n" \
  "v_lshrrev_b32_dpp v231, v232, v234 quad_perm:[3,0,1,2] row_mask:0xf bank_mask:0xf\n" \
  "v_mfma_f32_16x16x32_bf16 v[164:167], v[152:155], v[8:11], v[164:167]\n" \
  "v_perm_b32 v228, v229, v228, s53\n" \
  "v_perm_b32 v230, v231, v230, s54\n" \
  "v_mfma_f32_16x16x32_bf16 v[164:167], v[148:151], v[4:7], v[164:167]\n" \
  "v_bitop3_b32 v228, v228, v230, s55 bitop3:0xa8\n" \
  "v_lshl_or_b32 v229, v234, 1, s56\n" \
  "v_mfma_f32_16x16x32_bf16 v[164:167], v[144:147], v[0:3], v[164:167]\n" \
  "v_add_u32_dpp v228, v228, v228 quad_perm:[2,3,0,1] row_mask:0xf bank_mask:0xf\n" \
  "v_min_u32_sdwa v230, v228, v228 dst_sel:DWORD dst_unused:UNUSED_PAD src0_sel:BYTE_0 src1_sel:BYTE_1\n" \
  "v_mfma_f32_16x16x32_bf16 v[172:175], v[144:147], v[16:19], 0\n" \
  "v_min_u32_sdwa v231, v228, v228 dst_sel:DWORD dst_unused:UNUSED_PAD src0_sel:BYTE_2 src1_sel:BYTE_3\n" \
  "v_sub_u32 v230, v230, v231\n" \
  "v_mfma_f32_16x16x32_bf16 v[172:175], v[148:151], v[20:23], v[172:175]\n" \
  "v_cvt_pk_bf16_f32 v176, v160, v161\n" \
  "v_add_u32_dpp v230, v230, v230 quad_perm:[1,0,3,2] row_mask:0xf bank_mask:0xf\n" \
  "v_mfma_f32_16x16x32_bf16 v[172:175], v[152:155], v[24:27], v[172:175]\n" \
  "v_cmp_ge_i32 vcc, 0, v230\n" \
  "v_cvt_pk_bf16_f32 v177, v162, v163\n" \
  "v_mfma_f32_16x16x32_bf16 v[172:175], v[156:159], v[28:31], v[172:175]\n" \
  "v_cndmask_b32_sdwa v228, v228, v228, vcc dst_sel:DWORD dst_unused:UNUSED_PAD src0_sel:WORD_1 src1_sel:WORD_0\n" \
  "v_cvt_pk_bf16_f32 v178, v164, v165\n" \
  "v_mfma_f32_16x16x32_bf16 v[168:171], v[140:143], v[28:31], 0\n" \
  "s_mov_b64 s[50:51], vcc\n" \
  "v_cmp_le_u32_sdwa vcc, v228, v228 src0_sel:BYTE_0 src1_sel:BYTE_1\n" \
  "v_cvt_pk_bf16_f32 v179, v166, v167\n" \
  "v_mfma_f32_16x16x32_bf16 v[168:171], v[136:139], v[24:27], v[168:171]\n" \
  "v_cndmask_b32_dpp v232, v232, v232, vcc quad_perm:[2,3,0,1] row_mask:0xf bank_mask:0xf\n" \
  "v_mfma_f32_16x16x32_bf16 v[168:171], v[132:135], v[20:23], v[168:171]\n" \
  "s_mov_b64 vcc, s[50:51]\n" \
  "v_cndmask_b32_dpp v232, v232, v232, vcc quad_perm:[3,0,1,2] row_mask:0xf bank_mask:0xf\n" \
  "v_lshlrev_b32_e64 v230, v232, 3\n" \
  "v_mfma_f32_16x16x32_bf16 v[168:171], v[128:131], v[16:19], v[168:171]\n" \
  "v_and_or_b32 v234, v229, v230, v234\n" \
  "s_cmp_lg_u32 s33, 1021\n" \
  "s_cbranch_scc0 LT_e\n" \
  "LT_r:\n" \
  "s_waitcnt vmcnt(5)\n" \
  "global_store_dwordx4 v186, v[176:179], s[44:45] offset:0 sc1\n" \
  "v_mfma_f32_16x16x32_bf16 v[160:163], v[128:131], v[32:35], 0\n" \
  "s_set_gpr_idx_on s37, gpr_idx(DST)\n" \
  "v_mov_b32 v196, v232\n" \
  "s_set_gpr_idx_off\n" \
  "v_mfma_f32_16x16x32_bf16 v[160:163], v[132:135], v[36:39], v[160:163]\n" \
  "s_set_gpr_idx_on s57, gpr_idx(SRC0)\n" \
  "v_mad_u32_u24 v232, v196, s52, v236\n" \
  "s_set_gpr_idx_off\n" \
  "v_mfma_f32_16x16x32_bf16 v[160:163], v[136:139], v[40:43], v[160:163]\n" \
  "v_lshrrev_b32 v228, v232, v234\n" \
  "v_lshrrev_b32_dpp v229, v232, v234 quad_perm:[2,3,0,1] row_mask:0xf bank_mask:0xf\n" \
  "v_mfma_f32_16x16x32_bf16 v[160:163], v[140:143], v[44:47], v[160:163]\n" \
  "v_lshrrev_b32_dpp v230, v232, v234 quad_perm:[1,2,3,0] row_mask:0xf bank_mask:0xf\n" \
  "v_lshrrev_b32_dpp v231, v232, v234 quad_perm:[3,0,1,2] row_mask:0xf bank_mask:0xf\n" \
  "v_mfma_f32_16x16x32_bf16 v[164:167], v[156:159], v[44:47], 0\n" \
  "v_perm_b32 v228, v229, v228, s53\n" \
  "v_perm_b32 v230, v231, v230, s54\n" \
  "v_mfma_f32_16x16x32_bf16 v[164:167], v[152:155], v[40:43], v[164:167]\n" \
  "v_bitop3_b32 v228, v228, v230, s55 bitop3:0xa8\n" \
  "v_lshl_or_b32 v229, v234, 1, s56\n" \
  "v_mfma_f32_16x16x32_bf16 v[164:167], v[148:151], v[36:39], v[164:167]\n" \
  "v_cvt_pk_bf16_f32 v182, v172, v173\n" \
  "v_cvt_pk_bf16_f32 v183, v174, v175\n" \
  "v_mfma_f32_16x16x32_bf16 v[164:167], v[144:147], v[32:35], v[164:167]\n" \
  "v_cvt_pk_bf16_f32 v180, v168, v169\n" \
  "v_cvt_pk_bf16_f32 v181, v170, v171\n" \
  "s_waitcnt vmcnt(5)\n" \
  "global_store_dwordx4 v186, v[180:183], s[44:45] offset:256 sc1\n" \
  "v_mfma_f32_16x16x32_bf16 v[172:175], v[144:147], v[48:51], 0\n" \
  "v_add_u32_dpp v228, v228, v228 quad_perm:[2,3,0,1] row_mask:0xf bank_mask:0xf\n" \
  "v_min_u32_sdwa v230, v228, v228 dst_sel:DWORD dst_unused:UNUSED_PAD src0_sel:BYTE_0 src1_sel:BYTE_1\n" \
  "v_mfma_f32_16x16x32_bf16 v[172:175], v[148:151], v[52:55], v[172:175]\n" \
  "v_min_u32_sdwa v231, v228, v228 dst_sel:DWORD dst_unused:UNUSED_PAD src0_sel:BYTE_2 src1_sel:BYTE_3\n" \
  "v_sub_u32 v230, v230, v231\n" \
  "v_mfma_f32_16x16x32_bf16 v[172:175], v[152:155], v[56:59], v[172:175]\n" \
  "v_cvt_pk_bf16_f32 v176, v160, v161\n" \
  "v_add_u32_dpp v230, v230, v230 quad_perm:[1,0,3,2] row_mask:0xf bank_mask:0xf\n" \
  "v_mfma_f32_16x16x32_bf16 v[172:175], v[156:159], v[60:63], v[172:175]\n" \
  "v_cmp_ge_i32 vcc, 0, v230\n" \
  "v_cvt_pk_bf16_f32 v177, v162, v163\n" \
  "v_mfma_f32_16x16x32_bf16 v[168:171], v[140:143], v[60:63], 0\n" \
  "v_cndmask_b32_sdwa v228, v228, v228, vcc dst_sel:DWORD dst_unused:UNUSED_PAD src0_sel:WORD_1 src1_sel:WORD_0\n" \
  "v_cvt_pk_bf16_f32 v178, v164, v165\n" \
  "v_mfma_f32_16x16x32_bf16 v[168:171], v[136:139], v[56:59], v[168:171]\n" \
  "s_mov_b64 s[50:51], vcc\n" \
  "v_cmp_le_u32_sdwa vcc, v228, v228 src0_sel:BYTE_0 src1_sel:BYTE_1\n" \
  "v_cvt_pk_bf16_f32 v179, v166, v167\n" \
  "v_mfma_f32_16x16x32_bf16 v[168:171], v[132:135], v[52:55], v[168:171]\n" \
  "v_cndmask_b32_dpp v232, v232, v232, vcc quad_perm:[2,3,0,1] row_mask:0xf bank_mask:0xf\n" \
  "v_mfma_f32_16x16x32_bf16 v[168:171], v[128:131], v[48:51], v[168:171]\n" \
  "s_mov_b64 vcc, s[50:51]\n" \
  "v_cndmask_b32_dpp v232, v232, v232, vcc quad_perm:[3,0,1,2] row_mask:0xf bank_mask:0xf\n" \
  "v_lshlrev_b32_e64 v230, v232, 3\n" \
  "s_waitcnt vmcnt(5)\n" \
  "global_store_dwordx4 v186, v[176:179], s[44:45] offset:512 sc1\n" \
  "v_mfma_f32_16x16x32_bf16 v[160:163], v[128:131], v[64:67], 0\n" \
  "v_and_or_b32 v234, v229, v230, v234\n" \
  "v_cvt_pk_bf16_f32 v182, v172, v173\n" \
  "v_mfma_f32_16x16x32_bf16 v[160:163], v[132:135], v[68:71], v[160:163]\n" \
  "s_set_gpr_idx_on s57, gpr_idx(DST)\n" \
  "v_mov_b32 v196, v232\n" \
  "s_set_gpr_idx_off\n" \
  "v_mfma_f32_16x16x32_bf16 v[160:163], v[136:139], v[72:75], v[160:163]\n" \
  "s_mov_b64 vcc, s[80:81]\n" \
  "v_cndmask_b32_dpp v234, v234, v234, vcc row_ror:8 row_mask:0xf bank_mask:0xf\n" \
  "v_cvt_pk_bf16_f32 v183, v174, v175\n" \
  "v_mfma_f32_16x16x32_bf16 v[160:163], v[140:143], v[76:79], v[160:163]\n" \
  "s_set_gpr_idx_on s3, gpr_idx(SRC0)\n" \
  "v_mad_u32_u24 v232, v196, s52, v236\n" \
  "s_set_gpr_idx_off\n" \
  "v_mfma_f32_16x16x32_bf16 v[164:167], v[156:159], v[76:79], 0\n" \
  "v_lshrrev_b32 v228, v232, v234\n" \
  "v_lshrrev_b32_dpp v229, v232, v234 quad_perm:[2,3,0,1] row_mask:0xf bank_mask:0xf\n" \
  "v_mfma_f32_16x16x32_bf16 v[164:167], v[152:155], v[72:75], v[164:167]\n" \
  "v_lshrrev_b32_dpp v230, v232, v234 quad_perm:[1,2,3,0] row_mask:0xf bank_mask:0xf\n" \
  "v_lshrrev_b32_dpp v231, v232, v234 quad_perm:[3,0,1,2] row_mask:0xf bank_mask:0xf\n" \
  "v_mfma_f32_16x16x32_bf16 v[164:167], v[148:151], v[68:71], v[164:167]\n" \
  "v_perm_b32 v228, v229, v228, s53\n" \
  "v_perm_b32 v230, v231, v230, s54\n" \
  "v_mfma_f32_16x16x32_bf16 v[164:167], v[144:147], v[64:67], v[164:167]\n" \
  "v_cvt_pk_bf16_f32 v180, v168, v169\n" \
  "v_cvt_pk_bf16_f32 v181, v170, v171\n" \
  "s_waitcnt vmcnt(5)\n" \
  "global_store_dwordx4 v186, v[180:183], s[44:45] offset:768 sc1\n" \
  "v_mfma_f32_16x16x32_bf16 v[172:175], v[144:147], v[80:83], 0\n" \
  "s_waitcnt vmcnt(5)\n" \
  "s_mov_b64 exec, 1\n" \
  "ds_add_u32 v190, v191 offset:0\n" \
  "s_mov_b64 exec, -1\n" \
  "v_mfma_f32_16x16x32_bf16 v[172:175], v[148:151], v[84:87], v[172:175]\n" \
  "v_bitop3_b32 v228, v228, v230, s55 bitop3:0xa8\n" \
  "v_lshl_or_b32 v229, v234, 1, s56\n" \
  "v_mfma_f32_16x16x32_bf16 v[172:175], v[152:155], v[88:91], v[172:175]\n" \
  "v_add_u32_dpp v228, v228, v228 quad_perm:[2,3,0,1] row_mask:0xf bank_mask:0xf\n" \
  "v_min_u32_sdwa v230, v228, v228 dst_sel:DWORD dst_unused:UNUSED_PAD src0_sel:BYTE_0 src1_sel:BYTE_1\n" \
  "v_mfma_f32_16x16x32_bf16 v[172:175], v[156:159], v[92:95], v[172:175]\n" \
  "v_min_u32_sdwa v231, v228, v228 dst_sel:DWORD dst_unused:UNUSED_PAD src0_sel:BYTE_2 src1_sel:BYTE_3\n" \
  "v_sub_u32 v230, v230, v231\n" \
  "v_mfma_f32_16x16x32_bf16 v[168:171], v[140:143], v[92:95], 0\n" \
  "v_cvt_pk_bf16_f32 v176, v160, v161\n" \
  "v_add_u32_dpp v230, v230, v230 quad_perm:[1,0,3,2] row_mask:0xf bank_mask:0xf\n" \
  "v_mfma_f32_16x16x32_bf16 v[168:171], v[136:139], v[88:91], v[168:171]\n" \
  "v_cmp_ge_i32 vcc, 0, v230\n" \
  "v_cvt_pk_bf16_f32 v177, v162, v163\n" \
  "v_mfma_f32_16x16x32_bf16 v[168:171], v[132:135], v[84:87], v[168:171]\n" \
  "v_cndmask_b32_sdwa v228, v228, v228, vcc dst_sel:DWORD dst_unused:UNUSED_PAD src0_sel:WORD_1 src1_sel:WORD_0\n" \
  "v_cvt_pk_bf16_f32 v178, v164, v165\n" \
  "v_mfma_f32_16x16x32_bf16 v[168:171], v[128:131], v[80:83], v[168:171]\n" \
  "s_mov_b64 s[50:51], vcc\n" \
  "v_cmp_le_u32_sdwa vcc, v228, v228 src0_sel:BYTE_0 src1_sel:BYTE_1\n" \
  "v_cvt_pk_bf16_f32 v179, v166, v167\n" \
  "s_cmp_eq_u32 s98, 4\n" \
  "s_cselect_b32 s36, 12451840, 0\n" \
  "s_sub_u32 s6, s6, s36\n" \
  "s_cmp_lt_u32 s98, 10\n" \
  "s_cselect_b32 s6, s6, s34\n" \
  "s_add_u32 s40, s92, s6\n" \
  "s_addc_u32 s41, s93, 0\n" \
  "s_add_u32 s6, s6, 2097152\n" \
  "s_waitcnt vmcnt(5)\n" \
  "global_store_dwordx4 v186, v[176:179], s[44:45] offset:1024 sc1\n" \
  "v_mfma_f32_16x16x32_bf16 v[160:163], v[128:131], v[96:99], 0\n" \
  "ds_read_b32 v192, v190\n" \
  "v_add_u32 v184, s97, v184\n" \
  "v_mfma_f32_16x16x32_bf16 v[160:163], v[132:135], v[100:103], v[160:163]\n" \
  "v_add_u32 v185, s97, v185\n" \
  "v_cndmask_b32_dpp v232, v232, v232, vcc quad_perm:[2,3,0,1] row_mask:0xf bank_mask:0xf\n" \
  "v_mfma_f32_16x16x32_bf16 v[160:163], v[136:139], v[104:107], v[160:163]\n" \
  "s_mov_b64 vcc, s[50:51]\n" \
  "v_cndmask_b32_dpp v232, v232, v232, vcc quad_perm:[3,0,1,2] row_mask:0xf bank_mask:0xf\n" \
  "v_lshlrev_b32_e64 v230, v232, 3\n" \
  "v_mfma_f32_16x16x32_bf16 v[160:163], v[140:143], v[108:111], v[160:163]\n" \
  "v_and_or_b32 v234, v229, v230, v234\n" \
  "v_cvt_pk_bf16_f32 v182, v172, v173\n" \
  "v_mfma_f32_16x16x32_bf16 v[164:167], v[156:159], v[108:111], 0\n" \
  "s_set_gpr_idx_on s3, gpr_idx(DST)\n" \
  "v_mov_b32 v196, v232\n" \
  "s_set_gpr_idx_off\n" \
  "v_mfma_f32_16x16x32_bf16 v[164:167], v[152:155], v[104:107], v[164:167]\n" \
  "s_waitcnt lgkmcnt(0)\n" \
  "v_readfirstlane_b32 s35, v192\n" \
  "v_cvt_pk_bf16_f32 v183, v174, v175\n" \
  "v_mfma_f32_16x16x32_bf16 v[164:167], v[148:151], v[100:103], v[164:167]\n" \
  "v_cvt_pk_bf16_f32 v180, v168, v169\n" \
  "v_cvt_pk_bf16_f32 v181, v170, v171\n" \
  "v_mfma_f32_16x16x32_bf16 v[164:167], v[144:147], v[96:99], v[164:167]\n" \
  "s_cmp_lt_u32 s35, s99\n" \
  "s_cbranch_scc1 LT_w\n" \
  "LT_c:\n" \
  "s_mov_b32 m0, s96\n" \
  "s_nop 0\n" \
  "global_load_lds_dwordx4 v189, s[40:41]\n" \
  "global_load_lds_dwordx4 v194, s[40:41] offset:1024\n" \
  "s_add_u32 s96, s96, s97\n" \
  "s_sub_u32 s97, 0, s97\n" \
  "s_add_u32 s99, s99, 4\n" \
  "s_lshl4_add_u32 s35, s28, s98\n" \
  "s_bitcmp1_b64 s[86:87], s35\n" \
  "s_cbranch_scc1 LT_a\n" \
  "LT_s:\n" \
  "s_waitcnt vmcnt(5)\n" \
  "global_store_dwordx4 v186, v[180:183], s[44:45] offset:1280 sc1\n" \
  "v_mfma_f32_16x16x32_bf16 v[168:171], v[128:131], v[112:115], 0\n" \
  "ds_read_b64_tr_b16 v[128:129], v184 offset:0\n" \
  "ds_read_b64_tr_b16 v[130:131], v184 offset:256\n" \
  "v_mfma_f32_16x16x32_bf16 v[168:171], v[132:135], v[116:119], v[168:171]\n" \
  "ds_read_b64_tr_b16 v[132:133], v184 offset:2048\n" \
  "ds_read_b64_tr_b16 v[134:135], v184 offset:2304\n" \
  "v_mfma_f32_16x16x32_bf16 v[168:171], v[136:139], v[120:123], v[168:171]\n" \
  "ds_read_b64_tr_b16 v[136:137], v184 offset:4096\n" \
  "v_cvt_pk_bf16_f32 v176, v160, v161\n" \
  "v_mfma_f32_16x16x32_bf16 v[168:171], v[140:143], v[124:127], v[168:171]\n" \
  "ds_read_b64_tr_b16 v[138:139], v184 offset:4352\n" \
  "v_cvt_pk_bf16_f32 v177, v162, v163\n" \
  "v_mfma_f32_16x16x32_bf16 v[172:175], v[156:159], v[124:127], 0\n" \
  "ds_read_b64_tr_b16 v[140:141], v184 offset:6144\n" \
  "v_cvt_pk_bf16_f32 v178, v164, v165\n" \
  "v_mfma_f32_16x16x32_bf16 v[172:175], v[152:155], v[120:123], v[172:175]\n" \
  "ds_read_b64_tr_b16 v[142:143], v184 offset:6400\n" \
  "v_cvt_pk_bf16_f32 v179, v166, v167\n" \
  "v_mfma_f32_16x16x32_bf16 v[172:175], v[148:151], v[116:119], v[172:175]\n" \
  "ds_read_b64_tr_b16 v[156:157], v185 offset:6144\n" \
  "ds_read_b64_tr_b16 v[158:159], v185 offset:6400\n" \
  "v_mfma_f32_16x16x32_bf16 v[172:175], v[144:147], v[112:115], v[172:175]\n" \
  "ds_read_b64_tr_b16 v[152:153], v185 offset:4096\n" \
  "ds_read_b64_tr_b16 v[154:155], v185 offset:4352\n" \
  "ds_read_b64_tr_b16 v[148:149], v185 offset:2048\n" \
  "ds_read_b64_tr_b16 v[150:151], v185 offset:2304\n" \
  "ds_read_b64_tr_b16 v[144:145], v185 offset:0\n" \
  "ds_read_b64_tr_b16 v[146:147], v185 offset:256\n" \
  "s_waitcnt vmcnt(5)\n" \
  "global_store_dwordx4 v187, v[176:179], s[44:45] offset:0 sc1\n" \
  "s_add_u32 s98, s98, 1\n" \
  "v_cvt_pk_bf16_f32 v180, v168, v169\n" \
  "v_cvt_pk_bf16_f32 v181, v170, v171\n" \
  "v_cvt_pk_bf16_f32 v182, v172, v173\n" \
  "v_cvt_pk_bf16_f32 v183, v174, v175\n" \
  "s_waitcnt vmcnt(5)\n" \
  "global_store_dwordx4 v188, v[180:183], s[44:45] offset:0 sc1\n" \
  "s_add_u32 s44, s44, 128000\n" \
  "s_addc_u32 s45, s45, 0\n" \
  "s_bitcmp0_b32 4160, s98\n" \
  "s_cbranch_scc1 LT\n" \
  "s_cmp_eq_u32 s98, 6\n" \
  "s_cbranch_scc1 LT_m\n" \
  "s_lshr_b32 s35, s30, 2\n" \
  "s_lshl3_add_u32 s35, s29, s35\n" \
  "s_mul_i32 s35, s35, 1600000\n" \
  "s_lshl_b32 s36, s28, 12\n" \
  "s_add_u32 s35, s35, s36\n" \
  "s_add_u32 s40, s24, s35\n" \
  "s_addc_u32 s41, s25, 0\n" \
  "s_bitcmp1_b32 s30, 2\n" \
  "s_cbranch_scc1 LB_l_h\n" \
  "global_load_dwordx4 v[64:67], v238, s[40:41] offset:0 sc1\n" \
  "global_load_dwordx4 v[68:71], v238, s[40:41] offset:1024 sc1\n" \
  "global_load_dwordx4 v[72:75], v238, s[40:41] offset:2048 sc1\n" \
  "global_load_dwordx4 v[76:79], v238, s[40:41] offset:3072 sc1\n" \
  "s_add_u32 s40, s40, 16000\n" \
  "s_addc_u32 s41, s41, 0\n" \
  "global_load_dwordx4 v[80:83], v238, s[40:41] offset:0 sc1\n" \
  "global_load_dwordx4 v[84:87], v238, s[40:41] offset:1024 sc1\n" \
  "global_load_dwordx4 v[88:91], v238, s[40:41] offset:2048 sc1\n" \
  "global_load_dwordx4 v[92:95], v238, s[40:41] offset:3072 sc1\n" \
  "s_add_u32 s40, s40, 16000\n" \
  "s_addc_u32 s41, s41, 0\n" \
  "global_load_dwordx4 v[96:99], v238, s[40:41] offset:0 sc1\n" \
  "global_load_dwordx4 v[100:103], v238, s[40:41] offset:1024 sc1\n" \
  "global_load_dwordx4 v[104:107], v238, s[40:41] offset:2048 sc1\n" \
  "global_load_dwordx4 v[108:111], v238, s[40:41] offset:3072 sc1\n" \
  "s_add_u32 s40, s40, 16000\n" \
  "s_addc_u32 s41, s41, 0\n" \
  "global_load_dwordx4 v[112:115], v238, s[40:41] offset:0 sc1\n" \
  "global_load_dwordx4 v[116:119], v238, s[40:41] offset:1024 sc1\n" \
  "global_load_dwordx4 v[120:123], v238, s[40:41] offset:2048 sc1\n" \
  "global_load_dwordx4 v[124:127], v238, s[40:41] offset:3072 sc1\n" \
  "s_add_u32 s40, s40, 16000\n" \
  "s_addc_u32 s41, s41, 0\n" \
  "s_branch LB_l_e\n" \
  "LB_l_h:\n" \
  "global_load_dwordx4 v[0:3], v238, s[40:41] offset:0 sc1\n" \
  "global_load_dwordx4 v[4:7], v238, s[40:41] offset:1024 sc1\n" \
  "global_load_dwordx4 v[8:11], v238, s[40:41] offset:2048 sc1\n" \
  "global_load_dwordx4 v[12:15], v238, s[40:41] offset:3072 sc1\n" \
  "s_add_u32 s40, s40, 16000\n" \
  "s_addc_u32 s41, s41, 0\n" \
  "global_load_dwordx4 v[16:19], v238, s[40:41] offset:0 sc1\n" \
  "global_load_dwordx4 v[20:23], v238, s[40:41] offset:1024 sc1\n" \
  "global_load_dwordx4 v[24:27], v238, s[40:41] offset:2048 sc1\n" \
  "global_load_dwordx4 v[28:31], v238, s[40:41] offset:3072 sc1\n" \
  "s_add_u32 s40, s40, 16000\n" \
  "s_addc_u32 s41, s41, 0\n" \
  "global_load_dwordx4 v[32:35], v238, s[40:41] offset:0 sc1\n" \
  "global_load_dwordx4 v[36:39], v238, s[40:41] offset:1024 sc1\n" \
  "global_load_dwordx4 v[40:43], v238, s[40:41] offset:2048 sc1\n" \
  "global_load_dwordx4 v[44:47], v238, s[40:41] offset:3072 sc1\n" \
  "s_add_u32 s40, s40, 16000\n" \
  "s_addc_u32 s41, s41, 0\n" \
  "global_load_dwordx4 v[48:51], v238, s[40:41] offset:0 sc1\n" \
  "global_load_dwordx4 v[52:55], v238, s[40:41] offset:1024 sc1\n" \
  "global_load_dwordx4 v[56:59], v238, s[40:41] offset:2048 sc1\n" \
  "global_load_dwordx4 v[60:63], v238, s[40:41] offset:3072 sc1\n" \
  "s_add_u32 s40, s40, 16000\n" \
  "s_addc_u32 s41, s41, 0\n" \
  "LB_l_e:\n" \
  "ds_write_b16_d16_hi v195, v197 offset:0\n" \
  "ds_write_b16_d16_hi v195, v199 offset:16\n" \
  "ds_write_b16_d16_hi v195, v201 offset:32\n" \
  "ds_write_b16_d16_hi v235, v203 offset:64\n" \
  "ds_write_b16_d16_hi v235, v205 offset:80\n" \
  "ds_write_b16_d16_hi v235, v207 offset:96\n" \
  "ds_write_b16_d16_hi v235, v209 offset:112\n" \
  "ds_write_b16_d16_hi v235, v211 offset:128\n" \
  "ds_write_b16_d16_hi v235, v213 offset:144\n" \
  "ds_write_b16_d16_hi v235, v215 offset:160\n" \
  "ds_write_b16_d16_hi v235, v217 offset:176\n" \
  "ds_write_b16_d16_hi v195, v219 offset:48\n" \
  "ds_write_b16_d16_hi v195, v221 offset:192\n" \
  "ds_write_b16_d16_hi v195, v223 offset:208\n" \
  "ds_write_b16_d16_hi v195, v225 offset:224\n" \
  "ds_write_b16_d16_hi v195, v227 offset:240\n" \
  "ds_read_b128 v[244:247], v254 offset:0\n" \
  "ds_read_b128 v[248:251], v254 offset:1088\n" \
  "s_lshr_b32 s35, s7, 5\n" \
  "s_mul_i32 s35, s35, 819200\n" \
  "s_and_b32 s36, s7, 31\n" \
  "s_lshl_b32 s36, s36, 10\n" \
  "s_add_u32 s35, s35, s36\n" \
  "s_lshl_b32 s36, s4, 9\n" \
  "s_add_u32 s35, s35, s36\n" \
  "s_add_u32 s35, s35, 102793216\n" \
  "s_add_u32 s58, s24, s35\n" \
  "s_addc_u32 s59, s25, 0\n" \
  "s_waitcnt lgkmcnt(0)\n" \
  "s_cmp_ge_u32 s4, 896\n" \
  "s_cbranch_scc1 L17_\n" \
  "global_store_dwordx4 v238, v[244:247], s[58:59] sc1\n" \
  "s_cmp_ge_u32 s4, 768\n" \
  "s_cbranch_scc1 L17_\n" \
  "s_add_u32 s58, s58, 32768\n" \
  "s_addc_u32 s59, s59, 0\n" \
  "global_store_dwordx4 v238, v[248:251], s[58:59] sc1\n" \
  "L17_:\n" \
  "s_lshr_b32 s35, s30, 2\n" \
  "s_lshl3_add_u32 s33, s29, s35\n" \
  "s_and_b32 s34, s30, 3\n" \
  "s_lshl_b32 s59, s27, 11\n" \
  "s_mul_i32 s35, s33, 1600000\n" \
  "s_add_u32 s54, s24, s35\n" \
  "s_addc_u32 s55, s25, 0\n" \
  "s_mul_i32 s35, s34, 819200\n" \
  "s_lshl1_add_u32 s35, s59, s35\n" \
  "s_add_u32 s35, s35, 102400000\n" \
  "s_add_u32 s56, s24, s35\n" \
  "s_addc_u32 s57, s25, 0\n" \
  "s_lshr_b32 s35, s30, 4\n" \
  "s_lshl1_add_u32 s35, s29, s35\n" \
  "s_lshl_b32 s35, s35, 6\n" \
  "s_bfe_u32 s37, s30, 0x20002\n" \
  "s_lshl3_add_u32 s35, s37, s35\n" \
  "v_and_b32 v202, 16, v255\n" \
  "v_lshl_add_u32 v202, v202, 1, s35\n" \
  "global_load_dwordx2 v[204:205], v202, s[16:17]\n" \
  "global_load_dwordx2 v[206:207], v202, s[18:19]\n" \
  "v_mov_b32 v202, v238\n" \
  "global_load_dwordx4 v[208:211], v238, s[56:57] offset:0 sc1\n" \
  "global_load_dwordx4 v[212:215], v238, s[56:57] offset:1024 sc1\n" \
  "global_load_dwordx4 v[216:219], v238, s[56:57] offset:2048 sc1\n" \
  "global_load_dwordx4 v[220:223], v238, s[56:57] offset:3072 sc1\n" \
  "s_add_u32 s56, s56, 32768\n" \
  "s_addc_u32 s57, s57, 0\n" \
  "global_load_dwordx4 v[224:227], v238, s[56:57] sc1\n" \
  "global_load_dwordx4 v[228:231], v238, s[56:57] offset:1024 sc1\n" \
  "global_load_dwordx4 v[232:235], v238, s[56:57] offset:2048 sc1\n" \
  "global_load_dwordx4 v[236:239], v238, s[56:57] offset:3072 sc1\n" \
  "s_add_u32 s56, s56, 32768\n" \
  "s_addc_u32 s57, s57, 0\n" \
  "s_bitcmp1_b32 s30, 2\n" \
  "s_cbranch_scc1 LB_t_h\n" \
  "s_waitcnt lgkmcnt(12)\n" \
  "v_mfma_f32_16x16x32_bf16 v[160:163], v[128:131], v[0:3], 0\n" \
  "v_mfma_f32_16x16x32_bf16 v[160:163], v[132:135], v[4:7], v[160:163]\n" \
  "s_waitcnt lgkmcnt(8)\n" \
  "v_mfma_f32_16x16x32_bf16 v[160:163], v[136:139], v[8:11], v[160:163]\n" \
  "v_mfma_f32_16x16x32_bf16 v[160:163], v[140:143], v[12:15], v[160:163]\n" \
  "s_waitcnt lgkmcnt(0)\n" \
  "v_mfma_f32_16x16x32_bf16 v[164:167], v[156:159], v[12:15], 0\n" \
  "v_mfma_f32_16x16x32_bf16 v[164:167], v[152:155], v[8:11], v[164:167]\n" \
  "v_mfma_f32_16x16x32_bf16 v[164:167], v[148:151], v[4:7], v[164:167]\n" \
  "v_mfma_f32_16x16x32_bf16 v[164:167], v[144:147], v[0:3], v[164:167]\n" \
  "v_mfma_f32_16x16x32_bf16 v[172:175], v[144:147], v[16:19], 0\n" \
  "v_mfma_f32_16x16x32_bf16 v[172:175], v[148:151], v[20:23], v[172:175]\n" \
  "v_mfma_f32_16x16x32_bf16 v[172:175], v[152:155], v[24:27], v[172:175]\n" \
  "v_cvt_pk_bf16_f32 v176, v160, v161\n" \
  "v_cvt_pk_bf16_f32 v177, v162, v163\n" \
  "v_mfma_f32_16x16x32_bf16 v[172:175], v[156:159], v[28:31], v[172:175]\n" \
  "v_mfma_f32_16x16x32_bf16 v[168:171], v[140:143], v[28:31], 0\n" \
  "v_mfma_f32_16x16x32_bf16 v[168:171], v[136:139], v[24:27], v[168:171]\n" \
  "v_cvt_pk_bf16_f32 v178, v164, v165\n" \
  "v_cvt_pk_bf16_f32 v179, v166, v167\n" \
  "v_mfma_f32_16x16x32_bf16 v[168:171], v[132:135], v[20:23], v[168:171]\n" \
  "v_mfma_f32_16x16x32_bf16 v[168:171], v[128:131], v[16:19], v[168:171]\n" \
  "s_waitcnt vmcnt(63)\n" \
  "global_store_dwordx4 v186, v[176:179], s[46:47] offset:0 sc1\n" \
  "v_mfma_f32_16x16x32_bf16 v[160:163], v[128:131], v[32:35], 0\n" \
  "v_cvt_pk_bf16_f32 v182, v172, v173\n" \
  "v_cvt_pk_bf16_f32 v183, v174, v175\n" \
  "v_mfma_f32_16x16x32_bf16 v[160:163], v[132:135], v[36:39], v[160:163]\n" \
  "v_mfma_f32_16x16x32_bf16 v[160:163], v[136:139], v[40:43], v[160:163]\n" \
  "v_mfma_f32_16x16x32_bf16 v[160:163], v[140:143], v[44:47], v[160:163]\n" \
  "v_cvt_pk_bf16_f32 v180, v168, v169\n" \
  "v_cvt_pk_bf16_f32 v181, v170, v171\n" \
  "v_mfma_f32_16x16x32_bf16 v[164:167], v[156:159], v[44:47], 0\n" \
  "v_mfma_f32_16x16x32_bf16 v[164:167], v[152:155], v[40:43], v[164:167]\n" \
  "v_mfma_f32_16x16x32_bf16 v[164:167], v[148:151], v[36:39], v[164:167]\n" \
  "v_mfma_f32_16x16x32_bf16 v[164:167], v[144:147], v[32:35], v[164:167]\n" \
  "s_waitcnt vmcnt(63)\n" \
  "global_store_dwordx4 v186, v[180:183], s[46:47] offset:256 sc1\n" \
  "v_mfma_f32_16x16x32_bf16 v[172:175], v[144:147], v[48:51], 0\n" \
  "v_cvt_pk_bf16_f32 v176, v160, v161\n" \
  "v_cvt_pk_bf16_f32 v177, v162, v163\n" \
  "v_mfma_f32_16x16x32_bf16 v[172:175], v[148:151], v[52:55], v[172:175]\n" \
  "v_mfma_f32_16x16x32_bf16 v[172:175], v[152:155], v[56:59], v[172:175]\n" \
  "v_mfma_f32_16x16x32_bf16 v[172:175], v[156:159], v[60:63], v[172:175]\n" \
  "v_cvt_pk_bf16_f32 v178, v164, v165\n" \
  "v_cvt_pk_bf16_f32 v179, v166, v167\n" \
  "v_mfma_f32_16x16x32_bf16 v[168:171], v[140:143], v[60:63], 0\n" \
  "v_mfma_f32_16x16x32_bf16 v[168:171], v[136:139], v[56:59], v[168:171]\n" \
  "v_mfma_f32_16x16x32_bf16 v[168:171], v[132:135], v[52:55], v[168:171]\n" \
  "v_mfma_f32_16x16x32_bf16 v[168:171], v[128:131], v[48:51], v[168:171]\n" \
  "s_waitcnt vmcnt(63)\n" \
  "global_store_dwordx4 v186, v[176:179], s[46:47] offset:512 sc1\n" \
  "v_cvt_pk_bf16_f32 v182, v172, v173\n" \
  "v_cvt_pk_bf16_f32 v183, v174, v175\n" \
  "s_nop 3\n" \
  "v_cvt_pk_bf16_f32 v180, v168, v169\n" \
  "v_cvt_pk_bf16_f32 v181, v170, v171\n" \
  "s_waitcnt vmcnt(63)\n" \
  "global_store_dwordx4 v186, v[180:183], s[46:47] offset:768 sc1\n" \
  "s_branch LB_t_e\n" \
  "LT_w:\n" \
  "L8_:\n" \
  "s_sleep 1\n" \
  "ds_read_b32 v192, v190 offset:0\n" \
  "s_waitcnt lgkmcnt(0)\n" \
  "v_readfirstlane_b32 s35, v192\n" \
  "s_cmp_lt_u32 s35, s99\n" \
  "s_cbranch_scc1 L8_\n" \
  "s_branch LT_c\n" \
  "LT_e:\n" \
  "s_bitcmp1_b32 10, s98\n" \
  "s_cbranch_scc0 LT_r\n" \
  "global_load_dwordx4 v[240:243], v238, s[48:49] offset:0 sc1\n" \
  "s_waitcnt vmcnt(0)\n" \
  "global_store_dwordx4 v238, v[240:243], s[48:49] offset:1024 sc1\n" \
  "s_branch LT_r\n" \
  "LT_a:\n" \
  "s_cmp_lg_u32 s35, 4\n" \
  "s_cbranch_scc1 L9_\n" \
  "global_load_dword v193, v193, s[48:49] offset:1024 sc1\n" \
  "s_branch LT_s\n" \
  "L9_:\n" \
  "s_cmp_lg_u32 s35, 5\n" \
  "s_cbranch_scc1 L10_\n" \
  "s_memtime s[58:59]\n" \
  "s_memrealtime s[40:41]\n" \
  "s_mov_b32 s5, 0x10000\n" \
  "LB_ec:\n" \
  "v_readfirstlane_b32 s35, v193\n" \
  "s_cmp_eq_u32 s35, s39\n" \
  "s_cbranch_scc1 LB_ok\n" \
  "s_lshl_b32 s35, s26, 2\n" \
  "v_mov_b32 v193, s35\n" \
  "s_sleep 8\n" \
  "global_load_dword v193, v193, s[48:49] offset:1024 sc1\n" \
  "s_waitcnt vmcnt(0)\n" \
  "s_sub_u32 s5, s5, 1\n" \
  "s_cbranch_scc0 LB_ec\n" \
  "LB_ok:\n" \
  "s_lshl_b32 s35, s26, 2\n" \
  "v_mov_b32 v244, s35\n" \
  "s_waitcnt lgkmcnt(0)\n" \
  "s_sub_u32 s88, s58, s88\n" \
  "s_sub_u32 s88, 106000, s88\n" \
  "s_sub_u32 s90, s58, s90\n" \
  "s_sub_u32 s42, s40, s42\n" \
  "s_lshl_b32 s40, s40, 4\n" \
  "v_cvt_f32_u32 v245, s90\n" \
  "v_cvt_f32_u32 v246, s42\n" \
  "v_rcp_f32 v245, v245\n" \
  "v_cvt_f32_i32 v247, s88\n" \
  "v_mul_f32 v246, 0x4111eb85, v246\n" \
  "v_mul_f32 v245, v245, v246\n" \
  "v_mul_f32 v245, v245, v247\n" \
  "v_cvt_i32_f32 v245, v245\n" \
  "v_add_u32 v245, s40, v245\n" \
  "v_or_b32 v245, 1, v245\n" \
  "s_mov_b64 exec, 1\n" \
  "global_store_dword v244, v245, s[48:49] offset:2048 sc1\n" \
  "s_mov_b64 exec, -1\n" \
  "s_branch LT_s\n" \
  "L10_:\n" \
  "s_cmp_lg_u32 s35, 7\n" \
  "s_cbranch_scc1 L11_\n" \
  "s_mov_b32 exec_hi, 0\n" \
  "global_atomic_add v196, v191, s[48:49] offset:3072 sc1\n" \
  "s_mov_b64 exec, -1\n" \
  "s_branch LT_s\n" \
  "L11_:\n" \
  "s_cmp_lg_u32 s35, 23\n" \
  "s_cbranch_scc1 L12_\n" \
  "s_lshl_b32 s35, s30, 5\n" \
  "v_lshrrev_b32 v206, 3, v255\n" \
  "v_and_b32 v208, 7, v255\n" \
  "v_lshl_add_u32 v210, v206, 2, s35\n" \
  "v_lshl_add_u32 v212, v208, 2, s35\n" \
  "global_load_dword v202, v210, s[48:49] offset:2048 sc1\n" \
  "global_load_dword v204, v212, s[48:49] offset:2048 sc1\n" \
  "s_branch LT_s\n" \
  "L12_:\n" \
  "s_cmp_lg_u32 s35, 24\n" \
  "s_cbranch_scc1 L13_\n" \
  "s_mov_b32 s5, 0x10000\n" \
  "LB_z:\n" \
  "v_min_u32 v214, v202, v204\n" \
  "v_cmp_eq_u32 s[58:59], 0, v214\n" \
  "s_sub_u32 s5, s5, 1\n" \
  "s_cbranch_scc1 LB_zx\n" \
  "s_cmp_eq_u64 s[58:59], 0\n" \
  "s_cbranch_scc1 LB_zx\n" \
  "s_cmp_lg_u32 s33, 1021\n" \
  "s_cbranch_scc1 LB_n1\n" \
  "global_load_dwordx4 v[240:243], v238, s[48:49] offset:0 sc1\n" \
  "s_waitcnt vmcnt(0)\n" \
  "global_store_dwordx4 v238, v[240:243], s[48:49] offset:1024 sc1\n" \
  "LB_n1:\n" \
  "s_sleep 4\n" \
  "global_load_dword v202, v210, s[48:49] offset:2048 sc1\n" \
  "global_load_dword v204, v212, s[48:49] offset:2048 sc1\n" \
  "s_waitcnt vmcnt(0)\n" \
  "s_branch LB_z\n" \
  "LB_zx:\n" \
  "v_and_or_b32 v202, v202, -8, v206\n" \
  "v_and_or_b32 v204, v204, -8, v208\n" \
  "v_cmp_lt_u32 s[58:59], v204, v202\n" \
  "v_sub_u32 v202, v204, v202\n" \
  "s_lshl_b32 s35, s29, 3\n" \
  "s_lshr_b64 s[40:41], s[58:59], s35\n" \
  "s_and_b32 s40, s40, 0xff\n" \
  "s_bcnt1_i32_b32 s40, s40\n" \
  "s_sub_u32 s40, 7, s40\n" \
  "s_mov_b32 s36, s29\n" \
  "s_bfe_u32 s41, s58, 0x80000\n" \
  "s_bcnt1_i32_b32 s41, s41\n" \
  "s_cmp_eq_u32 s41, s40\n" \
  "s_cselect_b32 s36, 0, s36\n" \
  "s_bfe_u32 s41, s58, 0x80008\n" \
  "s_bcnt1_i32_b32 s41, s41\n" \
  "s_cmp_eq_u32 s41, s40\n" \
  "s_cselect_b32 s36, 1, s36\n" \
  "s_bfe_u32 s41, s58, 0x80010\n" \
  "s_bcnt1_i32_b32 s41, s41\n" \
  "s_cmp_eq_u32 s41, s40\n" \
  "s_cselect_b32 s36, 2, s36\n" \
  "s_bfe_u32 s41, s58, 0x80018\n" \
  "s_bcnt1_i32_b32 s41, s41\n" \
  "s_cmp_eq_u32 s41, s40\n" \
  "s_cselect_b32 s36, 3, s36\n" \
  "s_bfe_u32 s41, s59, 0x80000\n" \
  "s_bcnt1_i32_b32 s41, s41\n" \
  "s_cmp_eq_u32 s41, s40\n" \
  "s_cselect_b32 s36, 4, s36\n" \
  "s_bfe_u32 s41, s59, 0x80008\n" \
  "s_bcnt1_i32_b32 s41, s41\n" \
  "s_cmp_eq_u32 s41, s40\n" \
  "s_cselect_b32 s36, 5, s36\n" \
  "s_bfe_u32 s41, s59, 0x80010\n" \
  "s_bcnt1_i32_b32 s41, s41\n" \
  "s_cmp_eq_u32 s41, s40\n" \
  "s_cselect_b32 s36, 6, s36\n" \
  "s_bfe_u32 s41, s59, 0x80018\n" \
  "s_bcnt1_i32_b32 s41, s41\n" \
  "s_cmp_eq_u32 s41, s40\n" \
  "s_cselect_b32 s36, 7, s36\n" \
  "s_add_u32 s35, s35, s36\n" \
  "v_readlane_b32 s41, v202, s35\n" \
  "s_sub_u32 s35, 0, s41\n" \
  "s_cmp_lt_u32 s40, 4\n" \
  "s_cselect_b32 s41, s35, s41\n" \
  "s_min_u32 s41, s41, 0xffff\n" \
  "s_mul_i32 s41, s41, 39\n" \
  "s_add_u32 s41, s41, 28672\n" \
  "s_lshr_b32 s41, s41, 16\n" \
  "s_min_u32 s41, s41, 4\n" \
  "s_sub_u32 s35, 0, s41\n" \
  "s_cmp_lt_u32 s40, 4\n" \
  "s_cselect_b32 s35, s35, s41\n" \
  "s_lshl4_add_u32 s35, s36, s35\n" \
  "s_add_u32 s35, s35, 132\n" \
  "v_mov_b32 v204, s35\n" \
  "ds_write_b32 v190, v204 offset:4\n" \
  "s_branch LT_s\n" \
  "L13_:\n" \
  "s_cmp_lg_u32 s35, 25\n" \
  "s_cbranch_scc1 L14_\n" \
  "s_mov_b64 exec, 0xff\n" \
  "global_load_dword v200, v198, s[48:49] offset:3072 sc1\n" \
  "s_mov_b64 exec, -1\n" \
  "s_branch LT_s\n" \
  "L14_:\n" \
  "s_cmp_lg_u32 s35, 26\n" \
  "s_cbranch_scc1 L15_\n" \
  "s_mov_b64 s[58:59], vcc\n" \
  "s_mov_b32 s5, 0x10000\n" \
  "LB_p:\n" \
  "s_mov_b64 exec, 0xff\n" \
  "v_cmp_le_u32 vcc, 32, v200\n" \
  "s_mov_b64 exec, -1\n" \
  "s_sub_u32 s5, s5, 1\n" \
  "s_cselect_b32 s35, 0xff, vcc_lo\n" \
  "s_cmp_eq_u32 s35, 0xff\n" \
  "s_cbranch_scc1 LB_p_x\n" \
  "s_cmp_lg_u32 s33, 1021\n" \
  "s_cbranch_scc1 LB_n2\n" \
  "global_load_dwordx4 v[240:243], v238, s[48:49] offset:0 sc1\n" \
  "s_waitcnt vmcnt(0)\n" \
  "global_store_dwordx4 v238, v[240:243], s[48:49] offset:1024 sc1\n" \
  "LB_n2:\n" \
  "s_sleep 2\n" \
  "s_mov_b64 exec, 0xff\n" \
  "global_load_dword v200, v198, s[48:49] offset:3072 sc1\n" \
  "s_mov_b64 exec, -1\n" \
  "s_waitcnt vmcnt(0)\n" \
  "s_branch LB_p\n" \
  "LB_p_x:\n" \
  "s_mov_b64 vcc, s[58:59]\n" \
  "s_branch LT_s\n" \
  "L15_:\n" \
  "s_branch LT_s\n" \
  "LT_m:\n" \
  "ds_write_b16_d16_hi v195, v196 offset:0\n" \
  "ds_write_b16_d16_hi v195, v198 offset:16\n" \
  "ds_write_b16_d16_hi v195, v200 offset:32\n" \
  "ds_write_b16_d16_hi v235, v202 offset:64\n" \
  "ds_write_b16_d16_hi v235, v204 offset:80\n" \
  "ds_write_b16_d16_hi v235, v206 offset:96\n" \
  "ds_write_b16_d16_hi v235, v208 offset:112\n" \
  "ds_write_b16_d16_hi v235, v210 offset:128\n" \
  "ds_write_b16_d16_hi v235, v212 offset:144\n" \
  "ds_write_b16_d16_hi v235, v214 offset:160\n" \
  "ds_write_b16_d16_hi v235, v216 offset:176\n" \
  "ds_write_b16_d16_hi v195, v218 offset:48\n" \
  "ds_write_b16_d16_hi v195, v220 offset:192\n" \
  "ds_write_b16_d16_hi v195, v222 offset:208\n" \
  "ds_write_b16_d16_hi v195, v224 offset:224\n" \
  "ds_write_b16_d16_hi v195, v226 offset:240\n" \
  "ds_read_b128 v[244:247], v254 offset:0\n" \
  "ds_read_b128 v[248:251], v254 offset:1088\n" \
  "s_lshr_b32 s35, s7, 5\n" \
  "s_mul_i32 s35, s35, 819200\n" \
  "s_and_b32 s36, s7, 31\n" \
  "s_lshl_b32 s36, s36, 10\n" \
  "s_add_u32 s35, s35, s36\n" \
  "s_lshl_b32 s36, s4, 9\n" \
  "s_add_u32 s35, s35, s36\n" \
  "s_add_u32 s35, s35, 102400000\n" \
  "s_add_u32 s58, s24, s35\n" \
  "s_addc_u32 s59, s25, 0\n" \
  "s_waitcnt lgkmcnt(0)\n" \
  "s_cmp_ge_u32 s4, 768\n" \
  "s_cbranch_scc1 L16_\n" \
  "global_store_dwordx4 v238, v[244:247], s[58:59] sc1\n" \
  "s_cmp_ge_u32 s4, 768\n" \
  "s_cbranch_scc1 L16_\n" \
  "s_add_u32 s58, s58, 32768\n" \
  "s_addc_u32 s59, s59, 0\n" \
  "global_store_dwordx4 v238, v[248:251], s[58:59] sc1\n" \
  "L16_:\n" \
  "v_mov_b32 v234, 0\n" \
  "v_mov_b32 v236, v237\n" \
  "s_mov_b32 s38, 1\n" \
  "s_lshl_b32 s35, s29, 8\n" \
  "v_lshl_add_u32 v196, v255, 11, s35\n" \
  "s_lshl_b32 s35, s30, 11\n" \
  "v_lshl_add_u32 v198, v255, 8, s35\n" \
  "s_branch LT\n" \
  "LB_t_h:\n" \
  "s_waitcnt lgkmcnt(12)\n" \
  "v_mfma_f32_16x16x32_bf16 v[160:163], v[128:131], v[64:67], 0\n" \
  "v_mfma_f32_16x16x32_bf16 v[160:163], v[132:135], v[68:71], v[160:163]\n" \
  "s_waitcnt lgkmcnt(8)\n" \
  "v_mfma_f32_16x16x32_bf16 v[160:163], v[136:139], v[72:75], v[160:163]\n" \
  "v_mfma_f32_16x16x32_bf16 v[160:163], v[140:143], v[76:79], v[160:163]\n" \
  "s_waitcnt lgkmcnt(0)\n" \
  "v_mfma_f32_16x16x32_bf16 v[164:167], v[156:159], v[76:79], 0\n" \
  "v_mfma_f32_16x16x32_bf16 v[164:167], v[152:155], v[72:75], v[164:167]\n" \
  "v_mfma_f32_16x16x32_bf16 v[164:167], v[148:151], v[68:71], v[164:167]\n" \
  "v_mfma_f32_16x16x32_bf16 v[164:167], v[144:147], v[64:67], v[164:167]\n" \
  "v_mfma_f32_16x16x32_bf16 v[172:175], v[144:147], v[80:83], 0\n" \
  "v_mfma_f32_16x16x32_bf16 v[172:175], v[148:151], v[84:87], v[172:175]\n" \
  "v_mfma_f32_16x16x32_bf16 v[172:175], v[152:155], v[88:91], v[172:175]\n" \
  "v_cvt_pk_bf16_f32 v176, v160, v161\n" \
  "v_cvt_pk_bf16_f32 v177, v162, v163\n" \
  "v_mfma_f32_16x16x32_bf16 v[172:175], v[156:159], v[92:95], v[172:175]\n" \
  "v_mfma_f32_16x16x32_bf16 v[168:171], v[140:143], v[92:95], 0\n" \
  "v_mfma_f32_16x16x32_bf16 v[168:171], v[136:139], v[88:91], v[168:171]\n" \
  "v_cvt_pk_bf16_f32 v178, v164, v165\n" \
  "v_cvt_pk_bf16_f32 v179, v166, v167\n" \
  "v_mfma_f32_16x16x32_bf16 v[168:171], v[132:135], v[84:87], v[168:171]\n" \
  "v_mfma_f32_16x16x32_bf16 v[168:171], v[128:131], v[80:83], v[168:171]\n" \
  "s_waitcnt vmcnt(63)\n" \
  "global_store_dwordx4 v186, v[176:179], s[46:47] offset:1024 sc1\n" \
  "v_mfma_f32_16x16x32_bf16 v[160:163], v[128:131], v[96:99], 0\n" \
  "v_cvt_pk_bf16_f32 v182, v172, v173\n" \
  "v_cvt_pk_bf16_f32 v183, v174, v175\n" \
  "v_mfma_f32_16x16x32_bf16 v[160:163], v[132:135], v[100:103], v[160:163]\n" \
  "v_mfma_f32_16x16x32_bf16 v[160:163], v[136:139], v[104:107], v[160:163]\n" \
  "v_mfma_f32_16x16x32_bf16 v[160:163], v[140:143], v[108:111], v[160:163]\n" \
  "v_cvt_pk_bf16_f32 v180, v168, v169\n" \
  "v_cvt_pk_bf16_f32 v181, v170, v171\n" \
  "v_mfma_f32_16x16x32_bf16 v[164:167], v[156:159], v[108:111], 0\n" \
  "v_mfma_f32_16x16x32_bf16 v[164:167], v[152:155], v[104:107], v[164:167]\n" \
  "v_mfma_f32_16x16x32_bf16 v[164:167], v[148:151], v[100:103], v[164:167]\n" \
  "v_mfma_f32_16x16x32_bf16 v[164:167], v[144:147], v[96:99], v[164:167]\n" \
  "s_waitcnt vmcnt(63)\n" \
  "global_store_dwordx4 v186, v[180:183], s[46:47] offset:1280 sc1\n" \
  "v_mfma_f32_16x16x32_bf16 v[168:171], v[128:131], v[112:115], 0\n" \
  "v_cvt_pk_bf16_f32 v176, v160, v161\n" \
  "v_cvt_pk_bf16_f32 v177, v162, v163\n" \
  "v_mfma_f32_16x16x32_bf16 v[168:171], v[132:135], v[116:119], v[168:171]\n" \
  "v_mfma_f32_16x16x32_bf16 v[168:171], v[136:139], v[120:123], v[168:171]\n" \
  "v_mfma_f32_16x16x32_bf16 v[168:171], v[140:143], v[124:127], v[168:171]\n" \
  "v_cvt_pk_bf16_f32 v178, v164, v165\n" \
  "v_cvt_pk_bf16_f32 v179, v166, v167\n" \
  "v_mfma_f32_16x16x32_bf16 v[172:175], v[156:159], v[124:127], 0\n" \
  "v_mfma_f32_16x16x32_bf16 v[172:175], v[152:155], v[120:123], v[172:175]\n" \
  "v_mfma_f32_16x16x32_bf16 v[172:175], v[148:151], v[116:119], v[172:175]\n" \
  "v_mfma_f32_16x16x32_bf16 v[172:175], v[144:147], v[112:115], v[172:175]\n" \
  "s_waitcnt vmcnt(63)\n" \
  "global_store_dwordx4 v187, v[176:179], s[46:47] offset:0 sc1\n" \
  "v_cvt_pk_bf16_f32 v180, v168, v169\n" \
  "v_cvt_pk_bf16_f32 v181, v170, v171\n" \
  "s_nop 3\n" \
  "v_cvt_pk_bf16_f32 v182, v172, v173\n" \
  "v_cvt_pk_bf16_f32 v183, v174, v175\n" \
  "s_waitcnt vmcnt(63)\n" \
  "global_store_dwordx4 v188, v[180:183], s[46:47] offset:0 sc1\n" \
  "LB_t_e:\n" \
  "s_setprio 2\n" \
  "v_mov_b64 v[240:241], -1\n" \
  "v_mov_b64 v[242:243], -1\n" \
  "s_lshl2_add_u32 s35, s33, s34\n" \
  "s_lshl2_add_u32 s35, s35, s28\n" \
  "s_lshl_b32 s35, s35, 12\n" \
  "s_add_u32 s35, s35, 105676800\n" \
  "s_add_u32 s40, s24, s35\n" \
  "s_addc_u32 s41, s25, 0\n" \
  "v_mbcnt_lo_u32_b32 v189, -1, 0\n" \
  "v_mbcnt_hi_u32_b32 v189, -1, v189\n" \
  "v_lshlrev_b32 v188, 4, v189\n" \
  "s_lshl_b32 s35, s28, 12\n" \
  "v_add_u32 v128, s35, v188\n" \
  "v_mov_b32 v199, 1\n" \
  "v_lshrrev_b32 v180, 1, v189\n" \
  "v_xor_b32 v180, v180, v189\n" \
  "v_and_b32 v180, 8, v180\n" \
  "v_bfe_u32 v181, v189, 2, 1\n" \
  "v_lshlrev_b32_e64 v181, v181, 5\n" \
  "v_lshlrev_b32 v180, v180, v181\n" \
  "v_bfe_u32 v181, v189, 2, 2\n" \
  "v_lshlrev_b32_e64 v181, v181, 1\n" \
  "v_cmp_gt_u32 vcc, 32, v189\n" \
  "v_mov_b32 v191, 0x3f80\n" \
  "v_add_u32 v190, 32000, v188\n" \
  "v_cndmask_b32 v192, v181, v180, vcc\n" \
  "v_bfe_i32 v180, v192, 0, 1\n" \
  "v_and_b32 v180, v180, v191\n" \
  "v_bfe_i32 v181, v192, 1, 1\n" \
  "v_and_b32 v181, v181, v191\n" \
  "v_bfe_i32 v182, v192, 2, 1\n" \
  "v_and_b32 v182, v182, v191\n" \
  "v_bfe_i32 v183, v192, 3, 1\n" \
  "v_and_b32 v183, v183, v191\n" \
  "v_and_b32 v192, 3, v189\n" \
  "v_mul_u32_u24 v192, 0x5555, v192\n" \
  "v_add_u32 v189, 16000, v188\n" \
  "v_add_u32 v191, 48000, v188\n" \
  "s_waitcnt vmcnt(4)\n" \
  "v_lshlrev_b32 v184, 16, v204\n" \
  "v_and_b32 v185, 0xffff0000, v204\n" \
  "v_lshlrev_b32 v186, 16, v205\n" \
  "v_and_b32 v187, 0xffff0000, v205\n" \
  "global_store_dwordx4 v202, v[240:243], s[40:41] offset:0 sc1\n" \
  "global_store_dwordx4 v202, v[240:243], s[40:41] offset:1024 sc1\n" \
  "global_store_dwordx4 v202, v[240:243], s[40:41] offset:2048 sc1\n" \
  "global_store_dwordx4 v202, v[240:243], s[40:41] offset:3072 sc1\n" \
  "s_setprio 0\n" \
  "s_barrier\n" \
  "s_bitcmp1_b32 s30, 2\n" \
  "s_cbranch_scc1 LB_w_h\n" \
  "ds_write_b128 v128, v[64:67] offset:0\n" \
  "ds_write_b128 v128, v[68:71] offset:1024\n" \
  "ds_write_b128 v128, v[72:75] offset:2048\n" \
  "ds_write_b128 v128, v[76:79] offset:3072\n" \
  "s_add_u32 s35, s59, 64000\n" \
  "s_add_u32 s54, s54, s35\n" \
  "s_addc_u32 s55, s55, 0\n" \
  "s_add_u32 s58, s59, 0x10000\n" \
  "s_mov_b32 m0, s58\n" \
  "s_add_u32 s35, s58, 0x8000\n" \
  "global_load_lds_dwordx4 v188, s[54:55] sc1\n" \
  "global_load_lds_dwordx4 v188, s[54:55] offset:1024 sc1\n" \
  "ds_write_b128 v128, v[80:83] offset:16384\n" \
  "ds_write_b128 v128, v[84:87] offset:17408\n" \
  "ds_write_b128 v128, v[88:91] offset:18432\n" \
  "ds_write_b128 v128, v[92:95] offset:19456\n" \
  "ds_write_b128 v128, v[96:99] offset:32768\n" \
  "ds_write_b128 v128, v[100:103] offset:33792\n" \
  "ds_write_b128 v128, v[104:107] offset:34816\n" \
  "ds_write_b128 v128, v[108:111] offset:35840\n" \
  "s_add_u32 m0, s58, 0x4000\n" \
  "s_cmp_lt_u32 s35, 0x28000\n" \
  "global_load_lds_dwordx4 v189, s[54:55] sc1\n" \
  "global_load_lds_dwordx4 v189, s[54:55] offset:1024 sc1\n" \
  "s_cselect_b32 s58, s35, s59\n" \
  "s_add_u32 s54, s54, 32000\n" \
  "s_addc_u32 s55, s55, 0\n" \
  "ds_write_b128 v128, v[112:115] offset:49152\n" \
  "ds_write_b128 v128, v[116:119] offset:50176\n" \
  "ds_write_b128 v128, v[120:123] offset:51200\n" \
  "ds_write_b128 v128, v[124:127] offset:52224\n" \
  "s_branch LB_w_e\n" \
  "LB_w_h:\n" \
  "ds_write_b128 v128, v[0:3] offset:0\n" \
  "ds_write_b128 v128, v[4:7] offset:1024\n" \
  "ds_write_b128 v128, v[8:11] offset:2048\n" \
  "ds_write_b128 v128, v[12:15] offset:3072\n" \
  "s_add_u32 s35, s59, 64000\n" \
  "s_add_u32 s54, s54, s35\n" \
  "s_addc_u32 s55, s55, 0\n" \
  "s_add_u32 s58, s59, 0x10000\n" \
  "s_mov_b32 m0, s58\n" \
  "s_add_u32 s35, s58, 0x8000\n" \
  "global_load_lds_dwordx4 v188, s[54:55] sc1\n" \
  "global_load_lds_dwordx4 v188, s[54:55] offset:1024 sc1\n" \
  "ds_write_b128 v128, v[16:19] offset:16384\n" \
  "ds_write_b128 v128, v[20:23] offset:17408\n" \
  "ds_write_b128 v128, v[24:27] offset:18432\n" \
  "ds_write_b128 v128, v[28:31] offset:19456\n" \
  "ds_write_b128 v128, v[32:35] offset:32768\n" \
  "ds_write_b128 v128, v[36:39] offset:33792\n" \
  "ds_write_b128 v128, v[40:43] offset:34816\n" \
  "ds_write_b128 v128, v[44:47] offset:35840\n" \
  "s_add_u32 m0, s58, 0x4000\n" \
  "s_cmp_lt_u32 s35, 0x28000\n" \
  "global_load_lds_dwordx4 v189, s[54:55] sc1\n" \
  "global_load_lds_dwordx4 v189, s[54:55] offset:1024 sc1\n" \
  "s_cselect_b32 s58, s35, s59\n" \
  "s_add_u32 s54, s54, 32000\n" \
  "s_addc_u32 s55, s55, 0\n" \
  "ds_write_b128 v128, v[48:51] offset:49152\n" \
  "ds_write_b128 v128, v[52:55] offset:50176\n" \
  "ds_write_b128 v128, v[56:59] offset:51200\n" \
  "ds_write_b128 v128, v[60:63] offset:52224\n" \
  "LB_w_e:\n" \
  "s_waitcnt lgkmcnt(0)\n" \
  "s_mov_b32 s51, 7\n" \
  "s_setprio 0\n" \
  "s_barrier\n" \
  "LG_start:\n" \
  "v_lshlrev_b32_sdwa v64, 4, v208 dst_sel:DWORD dst_unused:UNUSED_PAD src0_sel:DWORD src1_sel:WORD_0\n" \
  "ds_read_b128 v[68:71], v64\n" \
  "v_mov_b64 v[0:1], v[184:185]\n" \
  "v_lshlrev_b32_sdwa v65, 4, v208 dst_sel:DWORD dst_unused:UNUSED_PAD src0_sel:DWORD src1_sel:WORD_1\n" \
  "ds_read_b128 v[72:75], v65\n" \
  "v_mov_b64 v[2:3], v[186:187]\n" \
  "v_lshlrev_b32_sdwa v66, 4, v209 dst_sel:DWORD dst_unused:UNUSED_PAD src0_sel:DWORD src1_sel:WORD_0\n" \
  "ds_read_b128 v[76:79], v66\n" \
  "v_mov_b64 v[4:5], v[184:185]\n" \
  "v_lshlrev_b32_sdwa v67, 4, v209 dst_sel:DWORD dst_unused:UNUSED_PAD src0_sel:DWORD src1_sel:WORD_1\n" \
  "ds_read_b128 v[80:83], v67\n" \
  "v_mov_b64 v[6:7], v[186:187]\n" \
  "s_mov_b32 m0, s58\n" \
  "s_add_u32 s35, s58, 0x8000\n" \
  "global_load_lds_dwordx4 v188, s[54:55] sc1\n" \
  "global_load_lds_dwordx4 v188, s[54:55] offset:1024 sc1\n" \
  "v_lshlrev_b32_sdwa v64, 4, v210 dst_sel:DWORD dst_unused:UNUSED_PAD src0_sel:DWORD src1_sel:WORD_0\n" \
  "ds_read_b128 v[84:87], v64\n" \
  "v_mov_b64 v[8:9], v[184:185]\n" \
  "v_lshlrev_b32_sdwa v65, 4, v210 dst_sel:DWORD dst_unused:UNUSED_PAD src0_sel:DWORD src1_sel:WORD_1\n" \
  "ds_read_b128 v[88:91], v65\n" \
  "v_mov_b64 v[10:11], v[186:187]\n" \
  "v_lshlrev_b32_sdwa v66, 4, v211 dst_sel:DWORD dst_unused:UNUSED_PAD src0_sel:DWORD src1_sel:WORD_0\n" \
  "ds_read_b128 v[92:95], v66\n" \
  "v_mov_b64 v[12:13], v[184:185]\n" \
  "v_lshlrev_b32_sdwa v67, 4, v211 dst_sel:DWORD dst_unused:UNUSED_PAD src0_sel:DWORD src1_sel:WORD_1\n" \
  "ds_read_b128 v[96:99], v67\n" \
  "v_mov_b64 v[14:15], v[186:187]\n" \
  "s_add_u32 m0, s58, 0x4000\n" \
  "s_cmp_lt_u32 s35, 0x28000\n" \
  "global_load_lds_dwordx4 v189, s[54:55] sc1\n" \
  "global_load_lds_dwordx4 v189, s[54:55] offset:1024 sc1\n" \
  "s_cselect_b32 s58, s35, s59\n" \
  "v_lshlrev_b32_sdwa v64, 4, v212 dst_sel:DWORD dst_unused:UNUSED_PAD src0_sel:DWORD src1_sel:WORD_0\n" \
  "ds_read_b128 v[100:103], v64\n" \
  "v_mov_b64 v[16:17], v[184:185]\n" \
  "v_lshlrev_b32_sdwa v65, 4, v212 dst_sel:DWORD dst_unused:UNUSED_PAD src0_sel:DWORD src1_sel:WORD_1\n" \
  "ds_read_b128 v[104:107], v65\n" \
  "v_mov_b64 v[18:19], v[186:187]\n" \
  "v_lshlrev_b32_sdwa v66, 4, v213 dst_sel:DWORD dst_unused:UNUSED_PAD src0_sel:DWORD src1_sel:WORD_0\n" \
  "ds_read_b128 v[108:111], v66\n" \
  "v_mov_b64 v[20:21], v[184:185]\n" \
  "v_lshlrev_b32_sdwa v67, 4, v213 dst_sel:DWORD dst_unused:UNUSED_PAD src0_sel:DWORD src1_sel:WORD_1\n" \
  "ds_read_b128 v[112:115], v67\n" \
  "v_mov_b64 v[22:23], v[186:187]\n" \
  "s_waitcnt lgkmcnt(10)\n" \
  "v_smfmac_f32_16x16x64_bf16 v[0:3], v[180:183], v[68:75], v192\n" \
  "v_lshlrev_b32_sdwa v64, 4, v214 dst_sel:DWORD dst_unused:UNUSED_PAD src0_sel:DWORD src1_sel:WORD_0\n" \
  "ds_read_b128 v[68:71], v64\n" \
  "v_mov_b64 v[24:25], v[184:185]\n" \
  "v_lshlrev_b32_sdwa v65, 4, v214 dst_sel:DWORD dst_unused:UNUSED_PAD src0_sel:DWORD src1_sel:WORD_1\n" \
  "ds_read_b128 v[72:75], v65\n" \
  "v_mov_b64 v[26:27], v[186:187]\n" \
  "s_mov_b32 m0, s58\n" \
  "s_add_u32 s35, s58, 0x8000\n" \
  "global_load_lds_dwordx4 v190, s[54:55] sc1\n" \
  "global_load_lds_dwordx4 v190, s[54:55] offset:1024 sc1\n" \
  "s_waitcnt lgkmcnt(10)\n" \
  "v_smfmac_f32_16x16x64_bf16 v[4:7], v[180:183], v[76:83], v192\n" \
  "v_lshlrev_b32_sdwa v66, 4, v215 dst_sel:DWORD dst_unused:UNUSED_PAD src0_sel:DWORD src1_sel:WORD_0\n" \
  "ds_read_b128 v[76:79], v66\n" \
  "v_mov_b64 v[28:29], v[184:185]\n" \
  "v_lshlrev_b32_sdwa v67, 4, v215 dst_sel:DWORD dst_unused:UNUSED_PAD src0_sel:DWORD src1_sel:WORD_1\n" \
  "ds_read_b128 v[80:83], v67\n" \
  "v_mov_b64 v[30:31], v[186:187]\n" \
  "s_waitcnt lgkmcnt(10)\n" \
  "v_smfmac_f32_16x16x64_bf16 v[8:11], v[180:183], v[84:91], v192\n" \
  "v_lshlrev_b32_sdwa v64, 4, v216 dst_sel:DWORD dst_unused:UNUSED_PAD src0_sel:DWORD src1_sel:WORD_0\n" \
  "ds_read_b128 v[84:87], v64\n" \
  "v_mov_b64 v[32:33], v[184:185]\n" \
  "v_lshlrev_b32_sdwa v65, 4, v216 dst_sel:DWORD dst_unused:UNUSED_PAD src0_sel:DWORD src1_sel:WORD_1\n" \
  "ds_read_b128 v[88:91], v65\n" \
  "v_mov_b64 v[34:35], v[186:187]\n" \
  "s_add_u32 m0, s58, 0x4000\n" \
  "s_cmp_lt_u32 s35, 0x28000\n" \
  "global_load_lds_dwordx4 v191, s[54:55] sc1\n" \
  "global_load_lds_dwordx4 v191, s[54:55] offset:1024 sc1\n" \
  "s_cselect_b32 s58, s35, s59\n" \
  "s_waitcnt lgkmcnt(10)\n" \
  "v_smfmac_f32_16x16x64_bf16 v[12:15], v[180:183], v[92:99], v192\n" \
  "v_lshlrev_b32_sdwa v66, 4, v217 dst_sel:DWORD dst_unused:UNUSED_PAD src0_sel:DWORD src1_sel:WORD_0\n" \
  "ds_read_b128 v[92:95], v66\n" \
  "v_mov_b64 v[36:37], v[184:185]\n" \
  "v_lshlrev_b32_sdwa v67, 4, v217 dst_sel:DWORD dst_unused:UNUSED_PAD src0_sel:DWORD src1_sel:WORD_1\n" \
  "ds_read_b128 v[96:99], v67\n" \
  "v_mov_b64 v[38:39], v[186:187]\n" \
  "s_waitcnt lgkmcnt(10)\n" \
  "v_smfmac_f32_16x16x64_bf16 v[16:19], v[180:183], v[100:107], v192\n" \
  "v_lshlrev_b32_sdwa v64, 4, v218 dst_sel:DWORD dst_unused:UNUSED_PAD src0_sel:DWORD src1_sel:WORD_0\n" \
  "ds_read_b128 v[100:103], v64\n" \
  "v_mov_b64 v[40:41], v[184:185]\n" \
  "v_lshlrev_b32_sdwa v65, 4, v218 dst_sel:DWORD dst_unused:UNUSED_PAD src0_sel:DWORD src1_sel:WORD_1\n" \
  "ds_read_b128 v[104:107], v65\n" \
  "v_mov_b64 v[42:43], v[186:187]\n" \
  "global_load_dwordx4 v[240:243], v188, s[56:57] sc1\n" \
  "global_load_dwordx4 v[244:247], v188, s[56:57] offset:1024 sc1\n" \
  "s_waitcnt lgkmcnt(10)\n" \
  "v_smfmac_f32_16x16x64_bf16 v[20:23], v[180:183], v[108:115], v192\n" \
  "v_lshlrev_b32_sdwa v66, 4, v219 dst_sel:DWORD dst_unused:UNUSED_PAD src0_sel:DWORD src1_sel:WORD_0\n" \
  "ds_read_b128 v[108:111], v66\n" \
  "v_mov_b64 v[44:45], v[184:185]\n" \
  "v_lshlrev_b32_sdwa v67, 4, v219 dst_sel:DWORD dst_unused:UNUSED_PAD src0_sel:DWORD src1_sel:WORD_1\n" \
  "ds_read_b128 v[112:115], v67\n" \
  "v_mov_b64 v[46:47], v[186:187]\n" \
  "s_waitcnt lgkmcnt(10)\n" \
  "v_smfmac_f32_16x16x64_bf16 v[24:27], v[180:183], v[68:75], v192\n" \
  "v_lshlrev_b32_sdwa v64, 4, v220 dst_sel:DWORD dst_unused:UNUSED_PAD src0_sel:DWORD src1_sel:WORD_0\n" \
  "ds_read_b128 v[68:71], v64\n" \
  "v_mov_b64 v[48:49], v[184:185]\n" \
  "v_lshlrev_b32_sdwa v65, 4, v220 dst_sel:DWORD dst_unused:UNUSED_PAD src0_sel:DWORD src1_sel:WORD_1\n" \
  "ds_read_b128 v[72:75], v65\n" \
  "v_mov_b64 v[50:51], v[186:187]\n" \
  "global_load_dwordx4 v[248:251], v188, s[56:57] offset:2048 sc1\n" \
  "global_load_dwordx4 v[252:255], v188, s[56:57] offset:3072 sc1\n" \
  "s_waitcnt lgkmcnt(10)\n" \
  "v_smfmac_f32_16x16x64_bf16 v[28:31], v[180:183], v[76:83], v192\n" \
  "v_lshlrev_b32_sdwa v66, 4, v221 dst_sel:DWORD dst_unused:UNUSED_PAD src0_sel:DWORD src1_sel:WORD_0\n" \
  "ds_read_b128 v[76:79], v66\n" \
  "v_mov_b64 v[52:53], v[184:185]\n" \
  "v_lshlrev_b32_sdwa v67, 4, v221 dst_sel:DWORD dst_unused:UNUSED_PAD src0_sel:DWORD src1_sel:WORD_1\n" \
  "ds_read_b128 v[80:83], v67\n" \
  "v_mov_b64 v[54:55], v[186:187]\n" \
  "s_waitcnt lgkmcnt(10)\n" \
  "v_smfmac_f32_16x16x64_bf16 v[32:35], v[180:183], v[84:91], v192\n" \
  "v_lshlrev_b32_sdwa v64, 4, v222 dst_sel:DWORD dst_unused:UNUSED_PAD src0_sel:DWORD src1_sel:WORD_0\n" \
  "ds_read_b128 v[84:87], v64\n" \
  "v_mov_b64 v[56:57], v[184:185]\n" \
  "v_lshlrev_b32_sdwa v65, 4, v222 dst_sel:DWORD dst_unused:UNUSED_PAD src0_sel:DWORD src1_sel:WORD_1\n" \
  "ds_read_b128 v[88:91], v65\n" \
  "v_mov_b64 v[58:59], v[186:187]\n" \
  "s_waitcnt lgkmcnt(10)\n" \
  "v_smfmac_f32_16x16x64_bf16 v[36:39], v[180:183], v[92:99], v192\n" \
  "v_lshlrev_b32_sdwa v66, 4, v223 dst_sel:DWORD dst_unused:UNUSED_PAD src0_sel:DWORD src1_sel:WORD_0\n" \
  "ds_read_b128 v[92:95], v66\n" \
  "v_mov_b64 v[60:61], v[184:185]\n" \
  "v_lshlrev_b32_sdwa v67, 4, v223 dst_sel:DWORD dst_unused:UNUSED_PAD src0_sel:DWORD src1_sel:WORD_1\n" \
  "ds_read_b128 v[96:99], v67\n" \
  "v_mov_b64 v[62:63], v[186:187]\n" \
  "s_add_u32 s56, s56, 32768\n" \
  "s_addc_u32 s57, s57, 0\n" \
  "s_add_u32 s54, s54, 64000\n" \
  "s_addc_u32 s55, s55, 0\n" \
  "s_cmp_eq_u32 s27, 4\n" \
  "s_cselect_b32 s37, 0x90, 0\n" \
  ".p2align 6\n" \
  "LG_loop:\n" \
  "s_bitcmp1_b32 s37, s51\n" \
  "s_waitcnt lgkmcnt(10)\n" \
  "v_smfmac_f32_16x16x64_bf16 v[40:43], v[180:183], v[100:107], v192\n" \
  "s_waitcnt lgkmcnt(8)\n" \
  "v_smfmac_f32_16x16x64_bf16 v[44:47], v[180:183], v[108:115], v192\n" \
  "s_waitcnt vmcnt(8)\n" \
  "s_cbranch_scc1 LG_h\n" \
  "LG_ret:\n" \
  "s_barrier\n" \
  "LG_aft:\n" \
  "v_lshlrev_b32_sdwa v64, 4, v224 dst_sel:DWORD dst_unused:UNUSED_PAD src0_sel:DWORD src1_sel:WORD_0\n" \
  "ds_read_b128 v[100:103], v64\n" \
  "v_lshlrev_b32_sdwa v65, 4, v224 dst_sel:DWORD dst_unused:UNUSED_PAD src0_sel:DWORD src1_sel:WORD_1\n" \
  "ds_read_b128 v[104:107], v65\n" \
  "v_lshlrev_b32_sdwa v66, 4, v225 dst_sel:DWORD dst_unused:UNUSED_PAD src0_sel:DWORD src1_sel:WORD_0\n" \
  "ds_read_b128 v[108:111], v66\n" \
  "v_lshlrev_b32_sdwa v67, 4, v225 dst_sel:DWORD dst_unused:UNUSED_PAD src0_sel:DWORD src1_sel:WORD_1\n" \
  "ds_read_b128 v[112:115], v67\n" \
  "s_waitcnt lgkmcnt(10)\n" \
  "v_smfmac_f32_16x16x64_bf16 v[48:51], v[180:183], v[68:75], v192\n" \
  "s_waitcnt lgkmcnt(8)\n" \
  "v_smfmac_f32_16x16x64_bf16 v[52:55], v[180:183], v[76:83], v192\n" \
  "s_waitcnt lgkmcnt(6)\n" \
  "v_smfmac_f32_16x16x64_bf16 v[56:59], v[180:183], v[84:91], v192\n" \
  "s_waitcnt lgkmcnt(4)\n" \
  "v_smfmac_f32_16x16x64_bf16 v[60:63], v[180:183], v[92:99], v192\n" \
  "s_barrier\n" \
  "s_mov_b32 m0, s58\n" \
  "s_add_u32 s35, s58, 0x8000\n" \
  "global_load_lds_dwordx4 v188, s[54:55] sc1\n" \
  "global_load_lds_dwordx4 v188, s[54:55] offset:1024 sc1\n" \
  "v_lshlrev_b32_sdwa v64, 4, v226 dst_sel:DWORD dst_unused:UNUSED_PAD src0_sel:DWORD src1_sel:WORD_0\n" \
  "ds_read_b128 v[68:71], v64\n" \
  "v_lshlrev_b32_sdwa v65, 4, v226 dst_sel:DWORD dst_unused:UNUSED_PAD src0_sel:DWORD src1_sel:WORD_1\n" \
  "ds_read_b128 v[72:75], v65\n" \
  "v_lshlrev_b32_sdwa v66, 4, v227 dst_sel:DWORD dst_unused:UNUSED_PAD src0_sel:DWORD src1_sel:WORD_0\n" \
  "ds_read_b128 v[76:79], v66\n" \
  "v_lshlrev_b32_sdwa v67, 4, v227 dst_sel:DWORD dst_unused:UNUSED_PAD src0_sel:DWORD src1_sel:WORD_1\n" \
  "ds_read_b128 v[80:83], v67\n" \
  "s_add_u32 m0, s58, 0x4000\n" \
  "s_cmp_lt_u32 s35, 0x28000\n" \
  "global_load_lds_dwordx4 v189, s[54:55] sc1\n" \
  "global_load_lds_dwordx4 v189, s[54:55] offset:1024 sc1\n" \
  "s_cselect_b32 s58, s35, s59\n" \
  "v_lshlrev_b32_sdwa v64, 4, v228 dst_sel:DWORD dst_unused:UNUSED_PAD src0_sel:DWORD src1_sel:WORD_0\n" \
  "ds_read_b128 v[84:87], v64\n" \
  "v_lshlrev_b32_sdwa v65, 4, v228 dst_sel:DWORD dst_unused:UNUSED_PAD src0_sel:DWORD src1_sel:WORD_1\n" \
  "ds_read_b128 v[88:91], v65\n" \
  "v_lshlrev_b32_sdwa v66, 4, v229 dst_sel:DWORD dst_unused:UNUSED_PAD src0_sel:DWORD src1_sel:WORD_0\n" \
  "ds_read_b128 v[92:95], v66\n" \
  "v_lshlrev_b32_sdwa v67, 4, v229 dst_sel:DWORD dst_unused:UNUSED_PAD src0_sel:DWORD src1_sel:WORD_1\n" \
  "ds_read_b128 v[96:99], v67\n" \
  "s_waitcnt lgkmcnt(10)\n" \
  "v_smfmac_f32_16x16x64_bf16 v[0:3], v[180:183], v[100:107], v192\n" \
  "v_lshlrev_b32_sdwa v64, 4, v230 dst_sel:DWORD dst_unused:UNUSED_PAD src0_sel:DWORD src1_sel:WORD_0\n" \
  "ds_read_b128 v[100:103], v64\n" \
  "v_lshlrev_b32_sdwa v65, 4, v230 dst_sel:DWORD dst_unused:UNUSED_PAD src0_sel:DWORD src1_sel:WORD_1\n" \
  "ds_read_b128 v[104:107], v65\n" \
  "s_mov_b32 m0, s58\n" \
  "s_add_u32 s35, s58, 0x8000\n" \
  "global_load_lds_dwordx4 v190, s[54:55] sc1\n" \
  "global_load_lds_dwordx4 v190, s[54:55] offset:1024 sc1\n" \
  "s_waitcnt lgkmcnt(10)\n" \
  "v_smfmac_f32_16x16x64_bf16 v[4:7], v[180:183], v[108:115], v192\n" \
  "v_lshlrev_b32_sdwa v66, 4, v231 dst_sel:DWORD dst_unused:UNUSED_PAD src0_sel:DWORD src1_sel:WORD_0\n" \
  "ds_read_b128 v[108:111], v66\n" \
  "v_lshlrev_b32_sdwa v67, 4, v231 dst_sel:DWORD dst_unused:UNUSED_PAD src0_sel:DWORD src1_sel:WORD_1\n" \
  "ds_read_b128 v[112:115], v67\n" \
  "s_waitcnt lgkmcnt(10)\n" \
  "v_smfmac_f32_16x16x64_bf16 v[8:11], v[180:183], v[68:75], v192\n" \
  "v_lshlrev_b32_sdwa v64, 4, v232 dst_sel:DWORD dst_unused:UNUSED_PAD src0_sel:DWORD src1_sel:WORD_0\n" \
  "ds_read_b128 v[68:71], v64\n" \
  "v_lshlrev_b32_sdwa v65, 4, v232 dst_sel:DWORD dst_unused:UNUSED_PAD src0_sel:DWORD src1_sel:WORD_1\n" \
  "ds_read_b128 v[72:75], v65\n" \
  "s_add_u32 m0, s58, 0x4000\n" \
  "s_cmp_lt_u32 s35, 0x28000\n" \
  "global_load_lds_dwordx4 v191, s[54:55] sc1\n" \
  "global_load_lds_dwordx4 v191, s[54:55] offset:1024 sc1\n" \
  "s_cselect_b32 s58, s35, s59\n" \
  "s_waitcnt lgkmcnt(10)\n" \
  "v_smfmac_f32_16x16x64_bf16 v[12:15], v[180:183], v[76:83], v192\n" \
  "v_lshlrev_b32_sdwa v66, 4, v233 dst_sel:DWORD dst_unused:UNUSED_PAD src0_sel:DWORD src1_sel:WORD_0\n" \
  "ds_read_b128 v[76:79], v66\n" \
  "v_lshlrev_b32_sdwa v67, 4, v233 dst_sel:DWORD dst_unused:UNUSED_PAD src0_sel:DWORD src1_sel:WORD_1\n" \
  "ds_read_b128 v[80:83], v67\n" \
  "s_waitcnt lgkmcnt(10)\n" \
  "v_smfmac_f32_16x16x64_bf16 v[16:19], v[180:183], v[84:91], v192\n" \
  "v_lshlrev_b32_sdwa v64, 4, v234 dst_sel:DWORD dst_unused:UNUSED_PAD src0_sel:DWORD src1_sel:WORD_0\n" \
  "ds_read_b128 v[84:87], v64\n" \
  "v_lshlrev_b32_sdwa v65, 4, v234 dst_sel:DWORD dst_unused:UNUSED_PAD src0_sel:DWORD src1_sel:WORD_1\n" \
  "ds_read_b128 v[88:91], v65\n" \
  "global_load_dwordx4 v[208:211], v188, s[56:57] sc1\n" \
  "global_load_dwordx4 v[212:215], v188, s[56:57] offset:1024 sc1\n" \
  "s_waitcnt lgkmcnt(10)\n" \
  "v_smfmac_f32_16x16x64_bf16 v[20:23], v[180:183], v[92:99], v192\n" \
  "v_lshlrev_b32_sdwa v66, 4, v235 dst_sel:DWORD dst_unused:UNUSED_PAD src0_sel:DWORD src1_sel:WORD_0\n" \
  "ds_read_b128 v[92:95], v66\n" \
  "v_lshlrev_b32_sdwa v67, 4, v235 dst_sel:DWORD dst_unused:UNUSED_PAD src0_sel:DWORD src1_sel:WORD_1\n" \
  "ds_read_b128 v[96:99], v67\n" \
  "s_waitcnt lgkmcnt(10)\n" \
  "v_smfmac_f32_16x16x64_bf16 v[24:27], v[180:183], v[100:107], v192\n" \
  "v_lshlrev_b32_sdwa v64, 4, v236 dst_sel:DWORD dst_unused:UNUSED_PAD src0_sel:DWORD src1_sel:WORD_0\n" \
  "ds_read_b128 v[100:103], v64\n" \
  "v_lshlrev_b32_sdwa v65, 4, v236 dst_sel:DWORD dst_unused:UNUSED_PAD src0_sel:DWORD src1_sel:WORD_1\n" \
  "ds_read_b128 v[104:107], v65\n" \
  "global_load_dwordx4 v[216:219], v188, s[56:57] offset:2048 sc1\n" \
  "global_load_dwordx4 v[220:223], v188, s[56:57] offset:3072 sc1\n" \
  "s_waitcnt lgkmcnt(10)\n" \
  "v_smfmac_f32_16x16x64_bf16 v[28:31], v[180:183], v[108:115], v192\n" \
  "v_lshlrev_b32_sdwa v66, 4, v237 dst_sel:DWORD dst_unused:UNUSED_PAD src0_sel:DWORD src1_sel:WORD_0\n" \
  "ds_read_b128 v[108:111], v66\n" \
  "v_lshlrev_b32_sdwa v67, 4, v237 dst_sel:DWORD dst_unused:UNUSED_PAD src0_sel:DWORD src1_sel:WORD_1\n" \
  "ds_read_b128 v[112:115], v67\n" \
  "s_waitcnt lgkmcnt(10)\n" \
  "v_smfmac_f32_16x16x64_bf16 v[32:35], v[180:183], v[68:75], v192\n" \
  "v_lshlrev_b32_sdwa v64, 4, v238 dst_sel:DWORD dst_unused:UNUSED_PAD src0_sel:DWORD src1_sel:WORD_0\n" \
  "ds_read_b128 v[68:71], v64\n" \
  "v_lshlrev_b32_sdwa v65, 4, v238 dst_sel:DWORD dst_unused:UNUSED_PAD src0_sel:DWORD src1_sel:WORD_1\n" \
  "ds_read_b128 v[72:75], v65\n" \
  "s_waitcnt lgkmcnt(10)\n" \
  "v_smfmac_f32_16x16x64_bf16 v[36:39], v[180:183], v[76:83], v192\n" \
  "v_lshlrev_b32_sdwa v66, 4, v239 dst_sel:DWORD dst_unused:UNUSED_PAD src0_sel:DWORD src1_sel:WORD_0\n" \
  "ds_read_b128 v[76:79], v66\n" \
  "v_lshlrev_b32_sdwa v67, 4, v239 dst_sel:DWORD dst_unused:UNUSED_PAD src0_sel:DWORD src1_sel:WORD_1\n" \
  "ds_read_b128 v[80:83], v67\n" \
  "s_add_u32 s56, s56, 32768\n" \
  "s_addc_u32 s57, s57, 0\n" \
  "s_add_u32 s54, s54, 64000\n" \
  "s_addc_u32 s55, s55, 0\n" \
  "s_waitcnt lgkmcnt(10)\n" \
  "v_smfmac_f32_16x16x64_bf16 v[40:43], v[180:183], v[84:91], v192\n" \
  "s_waitcnt lgkmcnt(8)\n" \
  "v_smfmac_f32_16x16x64_bf16 v[44:47], v[180:183], v[92:99], v192\n" \
  "s_waitcnt vmcnt(8)\n" \
  "s_barrier\n" \
  "v_lshlrev_b32_sdwa v64, 4, v240 dst_sel:DWORD dst_unused:UNUSED_PAD src0_sel:DWORD src1_sel:WORD_0\n" \
  "ds_read_b128 v[84:87], v64\n" \
  "v_lshlrev_b32_sdwa v65, 4, v240 dst_sel:DWORD dst_unused:UNUSED_PAD src0_sel:DWORD src1_sel:WORD_1\n" \
  "ds_read_b128 v[88:91], v65\n" \
  "v_lshlrev_b32_sdwa v66, 4, v241 dst_sel:DWORD dst_unused:UNUSED_PAD src0_sel:DWORD src1_sel:WORD_0\n" \
  "ds_read_b128 v[92:95], v66\n" \
  "v_lshlrev_b32_sdwa v67, 4, v241 dst_sel:DWORD dst_unused:UNUSED_PAD src0_sel:DWORD src1_sel:WORD_1\n" \
  "ds_read_b128 v[96:99], v67\n" \
  "s_waitcnt lgkmcnt(10)\n" \
  "v_smfmac_f32_16x16x64_bf16 v[48:51], v[180:183], v[100:107], v192\n" \
  "s_waitcnt lgkmcnt(8)\n" \
  "v_smfmac_f32_16x16x64_bf16 v[52:55], v[180:183], v[108:115], v192\n" \
  "s_waitcnt lgkmcnt(6)\n" \
  "v_smfmac_f32_16x16x64_bf16 v[56:59], v[180:183], v[68:75], v192\n" \
  "s_waitcnt lgkmcnt(4)\n" \
  "v_smfmac_f32_16x16x64_bf16 v[60:63], v[180:183], v[76:83], v192\n" \
  "s_barrier\n" \
  "s_mov_b32 m0, s58\n" \
  "s_add_u32 s35, s58, 0x8000\n" \
  "global_load_lds_dwordx4 v188, s[54:55] sc1\n" \
  "global_load_lds_dwordx4 v188, s[54:55] offset:1024 sc1\n" \
  "v_lshlrev_b32_sdwa v64, 4, v242 dst_sel:DWORD dst_unused:UNUSED_PAD src0_sel:DWORD src1_sel:WORD_0\n" \
  "ds_read_b128 v[100:103], v64\n" \
  "v_lshlrev_b32_sdwa v65, 4, v242 dst_sel:DWORD dst_unused:UNUSED_PAD src0_sel:DWORD src1_sel:WORD_1\n" \
  "ds_read_b128 v[104:107], v65\n" \
  "v_lshlrev_b32_sdwa v66, 4, v243 dst_sel:DWORD dst_unused:UNUSED_PAD src0_sel:DWORD src1_sel:WORD_0\n" \
  "ds_read_b128 v[108:111], v66\n" \
  "v_lshlrev_b32_sdwa v67, 4, v243 dst_sel:DWORD dst_unused:UNUSED_PAD src0_sel:DWORD src1_sel:WORD_1\n" \
  "ds_read_b128 v[112:115], v67\n" \
  "s_add_u32 m0, s58, 0x4000\n" \
  "s_cmp_lt_u32 s35, 0x28000\n" \
  "global_load_lds_dwordx4 v189, s[54:55] sc1\n" \
  "global_load_lds_dwordx4 v189, s[54:55] offset:1024 sc1\n" \
  "s_cselect_b32 s58, s35, s59\n" \
  "v_lshlrev_b32_sdwa v64, 4, v244 dst_sel:DWORD dst_unused:UNUSED_PAD src0_sel:DWORD src1_sel:WORD_0\n" \
  "ds_read_b128 v[68:71], v64\n" \
  "v_lshlrev_b32_sdwa v65, 4, v244 dst_sel:DWORD dst_unused:UNUSED_PAD src0_sel:DWORD src1_sel:WORD_1\n" \
  "ds_read_b128 v[72:75], v65\n" \
  "v_lshlrev_b32_sdwa v66, 4, v245 dst_sel:DWORD dst_unused:UNUSED_PAD src0_sel:DWORD src1_sel:WORD_0\n" \
  "ds_read_b128 v[76:79], v66\n" \
  "v_lshlrev_b32_sdwa v67, 4, v245 dst_sel:DWORD dst_unused:UNUSED_PAD src0_sel:DWORD src1_sel:WORD_1\n" \
  "ds_read_b128 v[80:83], v67\n" \
  "s_waitcnt lgkmcnt(10)\n" \
  "v_smfmac_f32_16x16x64_bf16 v[0:3], v[180:183], v[84:91], v192\n" \
  "v_lshlrev_b32_sdwa v64, 4, v246 dst_sel:DWORD dst_unused:UNUSED_PAD src0_sel:DWORD src1_sel:WORD_0\n" \
  "ds_read_b128 v[84:87], v64\n" \
  "v_lshlrev_b32_sdwa v65, 4, v246 dst_sel:DWORD dst_unused:UNUSED_PAD src0_sel:DWORD src1_sel:WORD_1\n" \
  "ds_read_b128 v[88:91], v65\n" \
  "s_mov_b32 m0, s58\n" \
  "s_add_u32 s35, s58, 0x8000\n" \
  "global_load_lds_dwordx4 v190, s[54:55] sc1\n" \
  "global_load_lds_dwordx4 v190, s[54:55] offset:1024 sc1\n" \
  "s_waitcnt lgkmcnt(10)\n" \
  "v_smfmac_f32_16x16x64_bf16 v[4:7], v[180:183], v[92:99], v192\n" \
  "v_lshlrev_b32_sdwa v66, 4, v247 dst_sel:DWORD dst_unused:UNUSED_PAD src0_sel:DWORD src1_sel:WORD_0\n" \
  "ds_read_b128 v[92:95], v66\n" \
  "v_lshlrev_b32_sdwa v67, 4, v247 dst_sel:DWORD dst_unused:UNUSED_PAD src0_sel:DWORD src1_sel:WORD_1\n" \
  "ds_read_b128 v[96:99], v67\n" \
  "s_waitcnt lgkmcnt(10)\n" \
  "v_smfmac_f32_16x16x64_bf16 v[8:11], v[180:183], v[100:107], v192\n" \
  "v_lshlrev_b32_sdwa v64, 4, v248 dst_sel:DWORD dst_unused:UNUSED_PAD src0_sel:DWORD src1_sel:WORD_0\n" \
  "ds_read_b128 v[100:103], v64\n" \
  "v_lshlrev_b32_sdwa v65, 4, v248 dst_sel:DWORD dst_unused:UNUSED_PAD src0_sel:DWORD src1_sel:WORD_1\n" \
  "ds_read_b128 v[104:107], v65\n" \
  "s_add_u32 m0, s58, 0x4000\n" \
  "s_cmp_lt_u32 s35, 0x28000\n" \
  "global_load_lds_dwordx4 v191, s[54:55] sc1\n" \
  "global_load_lds_dwordx4 v191, s[54:55] offset:1024 sc1\n" \
  "s_cselect_b32 s58, s35, s59\n" \
  "s_waitcnt lgkmcnt(10)\n" \
  "v_smfmac_f32_16x16x64_bf16 v[12:15], v[180:183], v[108:115], v192\n" \
  "v_lshlrev_b32_sdwa v66, 4, v249 dst_sel:DWORD dst_unused:UNUSED_PAD src0_sel:DWORD src1_sel:WORD_0\n" \
  "ds_read_b128 v[108:111], v66\n" \
  "v_lshlrev_b32_sdwa v67, 4, v249 dst_sel:DWORD dst_unused:UNUSED_PAD src0_sel:DWORD src1_sel:WORD_1\n" \
  "ds_read_b128 v[112:115], v67\n" \
  "s_waitcnt lgkmcnt(10)\n" \
  "v_smfmac_f32_16x16x64_bf16 v[16:19], v[180:183], v[68:75], v192\n" \
  "v_lshlrev_b32_sdwa v64, 4, v250 dst_sel:DWORD dst_unused:UNUSED_PAD src0_sel:DWORD src1_sel:WORD_0\n" \
  "ds_read_b128 v[68:71], v64\n" \
  "v_lshlrev_b32_sdwa v65, 4, v250 dst_sel:DWORD dst_unused:UNUSED_PAD src0_sel:DWORD src1_sel:WORD_1\n" \
  "ds_read_b128 v[72:75], v65\n" \
  "global_load_dwordx4 v[224:227], v188, s[56:57] sc1\n" \
  "global_load_dwordx4 v[228:231], v188, s[56:57] offset:1024 sc1\n" \
  "s_waitcnt lgkmcnt(10)\n" \
  "v_smfmac_f32_16x16x64_bf16 v[20:23], v[180:183], v[76:83], v192\n" \
  "v_lshlrev_b32_sdwa v66, 4, v251 dst_sel:DWORD dst_unused:UNUSED_PAD src0_sel:DWORD src1_sel:WORD_0\n" \
  "ds_read_b128 v[76:79], v66\n" \
  "v_lshlrev_b32_sdwa v67, 4, v251 dst_sel:DWORD dst_unused:UNUSED_PAD src0_sel:DWORD src1_sel:WORD_1\n" \
  "ds_read_b128 v[80:83], v67\n" \
  "s_waitcnt lgkmcnt(10)\n" \
  "v_smfmac_f32_16x16x64_bf16 v[24:27], v[180:183], v[84:91], v192\n" \
  "v_lshlrev_b32_sdwa v64, 4, v252 dst_sel:DWORD dst_unused:UNUSED_PAD src0_sel:DWORD src1_sel:WORD_0\n" \
  "ds_read_b128 v[84:87], v64\n" \
  "v_lshlrev_b32_sdwa v65, 4, v252 dst_sel:DWORD dst_unused:UNUSED_PAD src0_sel:DWORD src1_sel:WORD_1\n" \
  "ds_read_b128 v[88:91], v65\n" \
  "global_load_dwordx4 v[232:235], v188, s[56:57] offset:2048 sc1\n" \
  "global_load_dwordx4 v[236:239], v188, s[56:57] offset:3072 sc1\n" \
  "s_waitcnt lgkmcnt(10)\n" \
  "v_smfmac_f32_16x16x64_bf16 v[28:31], v[180:183], v[92:99], v192\n" \
  "v_lshlrev_b32_sdwa v66, 4, v253 dst_sel:DWORD dst_unused:UNUSED_PAD src0_sel:DWORD src1_sel:WORD_0\n" \
  "ds_read_b128 v[92:95], v66\n" \
  "v_lshlrev_b32_sdwa v67, 4, v253 dst_sel:DWORD dst_unused:UNUSED_PAD src0_sel:DWORD src1_sel:WORD_1\n" \
  "ds_read_b128 v[96:99], v67\n" \
  "s_waitcnt lgkmcnt(10)\n" \
  "v_smfmac_f32_16x16x64_bf16 v[32:35], v[180:183], v[100:107], v192\n" \
  "v_lshlrev_b32_sdwa v64, 4, v254 dst_sel:DWORD dst_unused:UNUSED_PAD src0_sel:DWORD src1_sel:WORD_0\n" \
  "ds_read_b128 v[100:103], v64\n" \
  "v_lshlrev_b32_sdwa v65, 4, v254 dst_sel:DWORD dst_unused:UNUSED_PAD src0_sel:DWORD src1_sel:WORD_1\n" \
  "ds_read_b128 v[104:107], v65\n" \
  "s_waitcnt lgkmcnt(10)\n" \
  "v_smfmac_f32_16x16x64_bf16 v[36:39], v[180:183], v[108:115], v192\n" \
  "v_lshlrev_b32_sdwa v66, 4, v255 dst_sel:DWORD dst_unused:UNUSED_PAD src0_sel:DWORD src1_sel:WORD_0\n" \
  "ds_read_b128 v[108:111], v66\n" \
  "v_lshlrev_b32_sdwa v67, 4, v255 dst_sel:DWORD dst_unused:UNUSED_PAD src0_sel:DWORD src1_sel:WORD_1\n" \
  "ds_read_b128 v[112:115], v67\n" \
  "s_add_u32 s56, s56, 32768\n" \
  "s_addc_u32 s57, s57, 0\n" \
  "s_add_u32 s54, s54, 64000\n" \
  "s_addc_u32 s55, s55, 0\n" \
  "s_lshl3_add_u32 s36, s27, s51\n" \
  "s_cmp_eq_u32 s36, 37\n" \
  "s_waitcnt lgkmcnt(10)\n" \
  "v_smfmac_f32_16x16x64_bf16 v[40:43], v[180:183], v[68:75], v192\n" \
  "s_waitcnt lgkmcnt(8)\n" \
  "v_smfmac_f32_16x16x64_bf16 v[44:47], v[180:183], v[76:83], v192\n" \
  "s_waitcnt vmcnt(8)\n" \
  "s_cbranch_scc1 LG_pol\n" \
  "LG_rt2:\n" \
  "s_barrier\n" \
  "v_lshlrev_b32_sdwa v64, 4, v208 dst_sel:DWORD dst_unused:UNUSED_PAD src0_sel:DWORD src1_sel:WORD_0\n" \
  "ds_read_b128 v[68:71], v64\n" \
  "v_lshlrev_b32_sdwa v65, 4, v208 dst_sel:DWORD dst_unused:UNUSED_PAD src0_sel:DWORD src1_sel:WORD_1\n" \
  "ds_read_b128 v[72:75], v65\n" \
  "v_lshlrev_b32_sdwa v66, 4, v209 dst_sel:DWORD dst_unused:UNUSED_PAD src0_sel:DWORD src1_sel:WORD_0\n" \
  "ds_read_b128 v[76:79], v66\n" \
  "v_lshlrev_b32_sdwa v67, 4, v209 dst_sel:DWORD dst_unused:UNUSED_PAD src0_sel:DWORD src1_sel:WORD_1\n" \
  "ds_read_b128 v[80:83], v67\n" \
  "s_waitcnt lgkmcnt(10)\n" \
  "v_smfmac_f32_16x16x64_bf16 v[48:51], v[180:183], v[84:91], v192\n" \
  "s_waitcnt lgkmcnt(8)\n" \
  "v_smfmac_f32_16x16x64_bf16 v[52:55], v[180:183], v[92:99], v192\n" \
  "s_waitcnt lgkmcnt(6)\n" \
  "v_smfmac_f32_16x16x64_bf16 v[56:59], v[180:183], v[100:107], v192\n" \
  "s_waitcnt lgkmcnt(4)\n" \
  "v_smfmac_f32_16x16x64_bf16 v[60:63], v[180:183], v[108:115], v192\n" \
  "s_barrier\n" \
  "s_mov_b32 m0, s58\n" \
  "s_add_u32 s35, s58, 0x8000\n" \
  "global_load_lds_dwordx4 v188, s[54:55] sc1\n" \
  "global_load_lds_dwordx4 v188, s[54:55] offset:1024 sc1\n" \
  "v_lshlrev_b32_sdwa v64, 4, v210 dst_sel:DWORD dst_unused:UNUSED_PAD src0_sel:DWORD src1_sel:WORD_0\n" \
  "ds_read_b128 v[84:87], v64\n" \
  "v_lshlrev_b32_sdwa v65, 4, v210 dst_sel:DWORD dst_unused:UNUSED_PAD src0_sel:DWORD src1_sel:WORD_1\n" \
  "ds_read_b128 v[88:91], v65\n" \
  "v_lshlrev_b32_sdwa v66, 4, v211 dst_sel:DWORD dst_unused:UNUSED_PAD src0_sel:DWORD src1_sel:WORD_0\n" \
  "ds_read_b128 v[92:95], v66\n" \
  "v_lshlrev_b32_sdwa v67, 4, v211 dst_sel:DWORD dst_unused:UNUSED_PAD src0_sel:DWORD src1_sel:WORD_1\n" \
  "ds_read_b128 v[96:99], v67\n" \
  "s_add_u32 m0, s58, 0x4000\n" \
  "s_cmp_lt_u32 s35, 0x28000\n" \
  "global_load_lds_dwordx4 v189, s[54:55] sc1\n" \
  "global_load_lds_dwordx4 v189, s[54:55] offset:1024 sc1\n" \
  "s_cselect_b32 s58, s35, s59\n" \
  "v_lshlrev_b32_sdwa v64, 4, v212 dst_sel:DWORD dst_unused:UNUSED_PAD src0_sel:DWORD src1_sel:WORD_0\n" \
  "ds_read_b128 v[100:103], v64\n" \
  "v_lshlrev_b32_sdwa v65, 4, v212 dst_sel:DWORD dst_unused:UNUSED_PAD src0_sel:DWORD src1_sel:WORD_1\n" \
  "ds_read_b128 v[104:107], v65\n" \
  "v_lshlrev_b32_sdwa v66, 4, v213 dst_sel:DWORD dst_unused:UNUSED_PAD src0_sel:DWORD src1_sel:WORD_0\n" \
  "ds_read_b128 v[108:111], v66\n" \
  "v_lshlrev_b32_sdwa v67, 4, v213 dst_sel:DWORD dst_unused:UNUSED_PAD src0_sel:DWORD src1_sel:WORD_1\n" \
  "ds_read_b128 v[112:115], v67\n" \
  "s_waitcnt lgkmcnt(10)\n" \
  "v_smfmac_f32_16x16x64_bf16 v[0:3], v[180:183], v[68:75], v192\n" \
  "v_lshlrev_b32_sdwa v64, 4, v214 dst_sel:DWORD dst_unused:UNUSED_PAD src0_sel:DWORD src1_sel:WORD_0\n" \
  "ds_read_b128 v[68:71], v64\n" \
  "v_lshlrev_b32_sdwa v65, 4, v214 dst_sel:DWORD dst_unused:UNUSED_PAD src0_sel:DWORD src1_sel:WORD_1\n" \
  "ds_read_b128 v[72:75], v65\n" \
  "s_mov_b32 m0, s58\n" \
  "s_add_u32 s35, s58, 0x8000\n" \
  "global_load_lds_dwordx4 v190, s[54:55] sc1\n" \
  "global_load_lds_dwordx4 v190, s[54:55] offset:1024 sc1\n" \
  "s_waitcnt lgkmcnt(10)\n" \
  "v_smfmac_f32_16x16x64_bf16 v[4:7], v[180:183], v[76:83], v192\n" \
  "v_lshlrev_b32_sdwa v66, 4, v215 dst_sel:DWORD dst_unused:UNUSED_PAD src0_sel:DWORD src1_sel:WORD_0\n" \
  "ds_read_b128 v[76:79], v66\n" \
  "v_lshlrev_b32_sdwa v67, 4, v215 dst_sel:DWORD dst_unused:UNUSED_PAD src0_sel:DWORD src1_sel:WORD_1\n" \
  "ds_read_b128 v[80:83], v67\n" \
  "s_waitcnt lgkmcnt(10)\n" \
  "v_smfmac_f32_16x16x64_bf16 v[8:11], v[180:183], v[84:91], v192\n" \
  "v_lshlrev_b32_sdwa v64, 4, v216 dst_sel:DWORD dst_unused:UNUSED_PAD src0_sel:DWORD src1_sel:WORD_0\n" \
  "ds_read_b128 v[84:87], v64\n" \
  "v_lshlrev_b32_sdwa v65, 4, v216 dst_sel:DWORD dst_unused:UNUSED_PAD src0_sel:DWORD src1_sel:WORD_1\n" \
  "ds_read_b128 v[88:91], v65\n" \
  "s_add_u32 m0, s58, 0x4000\n" \
  "s_cmp_lt_u32 s35, 0x28000\n" \
  "global_load_lds_dwordx4 v191, s[54:55] sc1\n" \
  "global_load_lds_dwordx4 v191, s[54:55] offset:1024 sc1\n" \
  "s_cselect_b32 s58, s35, s59\n" \
  "s_waitcnt lgkmcnt(10)\n" \
  "v_smfmac_f32_16x16x64_bf16 v[12:15], v[180:183], v[92:99], v192\n" \
  "v_lshlrev_b32_sdwa v66, 4, v217 dst_sel:DWORD dst_unused:UNUSED_PAD src0_sel:DWORD src1_sel:WORD_0\n" \
  "ds_read_b128 v[92:95], v66\n" \
  "v_lshlrev_b32_sdwa v67, 4, v217 dst_sel:DWORD dst_unused:UNUSED_PAD src0_sel:DWORD src1_sel:WORD_1\n" \
  "ds_read_b128 v[96:99], v67\n" \
  "s_waitcnt lgkmcnt(10)\n" \
  "v_smfmac_f32_16x16x64_bf16 v[16:19], v[180:183], v[100:107], v192\n" \
  "v_lshlrev_b32_sdwa v64, 4, v218 dst_sel:DWORD dst_unused:UNUSED_PAD src0_sel:DWORD src1_sel:WORD_0\n" \
  "ds_read_b128 v[100:103], v64\n" \
  "v_lshlrev_b32_sdwa v65, 4, v218 dst_sel:DWORD dst_unused:UNUSED_PAD src0_sel:DWORD src1_sel:WORD_1\n" \
  "ds_read_b128 v[104:107], v65\n" \
  "global_load_dwordx4 v[240:243], v188, s[56:57] sc1\n" \
  "global_load_dwordx4 v[244:247], v188, s[56:57] offset:1024 sc1\n" \
  "s_waitcnt lgkmcnt(10)\n" \
  "v_smfmac_f32_16x16x64_bf16 v[20:23], v[180:183], v[108:115], v192\n" \
  "v_lshlrev_b32_sdwa v66, 4, v219 dst_sel:DWORD dst_unused:UNUSED_PAD src0_sel:DWORD src1_sel:WORD_0\n" \
  "ds_read_b128 v[108:111], v66\n" \
  "v_lshlrev_b32_sdwa v67, 4, v219 dst_sel:DWORD dst_unused:UNUSED_PAD src0_sel:DWORD src1_sel:WORD_1\n" \
  "ds_read_b128 v[112:115], v67\n" \
  "s_waitcnt lgkmcnt(10)\n" \
  "v_smfmac_f32_16x16x64_bf16 v[24:27], v[180:183], v[68:75], v192\n" \
  "v_lshlrev_b32_sdwa v64, 4, v220 dst_sel:DWORD dst_unused:UNUSED_PAD src0_sel:DWORD src1_sel:WORD_0\n" \
  "ds_read_b128 v[68:71], v64\n" \
  "v_lshlrev_b32_sdwa v65, 4, v220 dst_sel:DWORD dst_unused:UNUSED_PAD src0_sel:DWORD src1_sel:WORD_1\n" \
  "ds_read_b128 v[72:75], v65\n" \
  "global_load_dwordx4 v[248:251], v188, s[56:57] offset:2048 sc1\n" \
  "global_load_dwordx4 v[252:255], v188, s[56:57] offset:3072 sc1\n" \
  "s_waitcnt lgkmcnt(10)\n" \
  "v_smfmac_f32_16x16x64_bf16 v[28:31], v[180:183], v[76:83], v192\n" \
  "v_lshlrev_b32_sdwa v66, 4, v221 dst_sel:DWORD dst_unused:UNUSED_PAD src0_sel:DWORD src1_sel:WORD_0\n" \
  "ds_read_b128 v[76:79], v66\n" \
  "v_lshlrev_b32_sdwa v67, 4, v221 dst_sel:DWORD dst_unused:UNUSED_PAD src0_sel:DWORD src1_sel:WORD_1\n" \
  "ds_read_b128 v[80:83], v67\n" \
  "s_waitcnt lgkmcnt(10)\n" \
  "v_smfmac_f32_16x16x64_bf16 v[32:35], v[180:183], v[84:91], v192\n" \
  "v_lshlrev_b32_sdwa v64, 4, v222 dst_sel:DWORD dst_unused:UNUSED_PAD src0_sel:DWORD src1_sel:WORD_0\n" \
  "ds_read_b128 v[84:87], v64\n" \
  "v_lshlrev_b32_sdwa v65, 4, v222 dst_sel:DWORD dst_unused:UNUSED_PAD src0_sel:DWORD src1_sel:WORD_1\n" \
  "ds_read_b128 v[88:91], v65\n" \
  "s_waitcnt lgkmcnt(10)\n" \
  "v_smfmac_f32_16x16x64_bf16 v[36:39], v[180:183], v[92:99], v192\n" \
  "v_lshlrev_b32_sdwa v66, 4, v223 dst_sel:DWORD dst_unused:UNUSED_PAD src0_sel:DWORD src1_sel:WORD_0\n" \
  "ds_read_b128 v[92:95], v66\n" \
  "v_lshlrev_b32_sdwa v67, 4, v223 dst_sel:DWORD dst_unused:UNUSED_PAD src0_sel:DWORD src1_sel:WORD_1\n" \
  "ds_read_b128 v[96:99], v67\n" \
  "s_add_u32 s56, s56, 32768\n" \
  "s_addc_u32 s57, s57, 0\n" \
  "s_add_u32 s54, s54, 64000\n" \
  "s_addc_u32 s55, s55, 0\n" \
  "s_sub_u32 s51, s51, 1\n" \
  "s_cmp_lg_u32 s51, 0\n" \
  "s_cbranch_scc1 LG_loop\n" \
  "s_waitcnt lgkmcnt(10)\n" \
  "v_smfmac_f32_16x16x64_bf16 v[40:43], v[180:183], v[100:107], v192\n" \
  "s_waitcnt lgkmcnt(8)\n" \
  "v_smfmac_f32_16x16x64_bf16 v[44:47], v[180:183], v[108:115], v192\n" \
  "s_waitcnt vmcnt(8)\n" \
  "s_barrier\n" \
  "v_lshlrev_b32_sdwa v64, 4, v224 dst_sel:DWORD dst_unused:UNUSED_PAD src0_sel:DWORD src1_sel:WORD_0\n" \
  "ds_read_b128 v[100:103], v64\n" \
  "v_lshlrev_b32_sdwa v65, 4, v224 dst_sel:DWORD dst_unused:UNUSED_PAD src0_sel:DWORD src1_sel:WORD_1\n" \
  "ds_read_b128 v[104:107], v65\n" \
  "v_lshlrev_b32_sdwa v66, 4, v225 dst_sel:DWORD dst_unused:UNUSED_PAD src0_sel:DWORD src1_sel:WORD_0\n" \
  "ds_read_b128 v[108:111], v66\n" \
  "v_lshlrev_b32_sdwa v67, 4, v225 dst_sel:DWORD dst_unused:UNUSED_PAD src0_sel:DWORD src1_sel:WORD_1\n" \
  "ds_read_b128 v[112:115], v67\n" \
  "s_waitcnt lgkmcnt(10)\n" \
  "v_smfmac_f32_16x16x64_bf16 v[48:51], v[180:183], v[68:75], v192\n" \
  "s_waitcnt lgkmcnt(8)\n" \
  "v_smfmac_f32_16x16x64_bf16 v[52:55], v[180:183], v[76:83], v192\n" \
  "s_waitcnt lgkmcnt(6)\n" \
  "v_smfmac_f32_16x16x64_bf16 v[56:59], v[180:183], v[84:91], v192\n" \
  "s_waitcnt lgkmcnt(4)\n" \
  "v_smfmac_f32_16x16x64_bf16 v[60:63], v[180:183], v[92:99], v192\n" \
  "s_barrier\n" \
  "s_mov_b32 m0, s58\n" \
  "s_add_u32 s35, s58, 0x8000\n" \
  "global_load_lds_dwordx4 v188, s[54:55] sc1\n" \
  "global_load_lds_dwordx4 v188, s[54:55] offset:1024 sc1\n" \
  "v_lshlrev_b32_sdwa v64, 4, v226 dst_sel:DWORD dst_unused:UNUSED_PAD src0_sel:DWORD src1_sel:WORD_0\n" \
  "ds_read_b128 v[68:71], v64\n" \
  "v_lshlrev_b32_sdwa v65, 4, v226 dst_sel:DWORD dst_unused:UNUSED_PAD src0_sel:DWORD src1_sel:WORD_1\n" \
  "ds_read_b128 v[72:75], v65\n" \
  "v_lshlrev_b32_sdwa v66, 4, v227 dst_sel:DWORD dst_unused:UNUSED_PAD src0_sel:DWORD src1_sel:WORD_0\n" \
  "ds_read_b128 v[76:79], v66\n" \
  "v_lshlrev_b32_sdwa v67, 4, v227 dst_sel:DWORD dst_unused:UNUSED_PAD src0_sel:DWORD src1_sel:WORD_1\n" \
  "ds_read_b128 v[80:83], v67\n" \
  "s_add_u32 m0, s58, 0x4000\n" \
  "s_cmp_lt_u32 s35, 0x28000\n" \
  "global_load_lds_dwordx4 v189, s[54:55] sc1\n" \
  "global_load_lds_dwordx4 v189, s[54:55] offset:1024 sc1\n" \
  "s_cselect_b32 s58, s35, s59\n" \
  "v_lshlrev_b32_sdwa v64, 4, v228 dst_sel:DWORD dst_unused:UNUSED_PAD src0_sel:DWORD src1_sel:WORD_0\n" \
  "ds_read_b128 v[84:87], v64\n" \
  "v_lshlrev_b32_sdwa v65, 4, v228 dst_sel:DWORD dst_unused:UNUSED_PAD src0_sel:DWORD src1_sel:WORD_1\n" \
  "ds_read_b128 v[88:91], v65\n" \
  "v_lshlrev_b32_sdwa v66, 4, v229 dst_sel:DWORD dst_unused:UNUSED_PAD src0_sel:DWORD src1_sel:WORD_0\n" \
  "ds_read_b128 v[92:95], v66\n" \
  "v_lshlrev_b32_sdwa v67, 4, v229 dst_sel:DWORD dst_unused:UNUSED_PAD src0_sel:DWORD src1_sel:WORD_1\n" \
  "ds_read_b128 v[96:99], v67\n" \
  "s_waitcnt lgkmcnt(10)\n" \
  "v_smfmac_f32_16x16x64_bf16 v[0:3], v[180:183], v[100:107], v192\n" \
  "v_lshlrev_b32_sdwa v64, 4, v230 dst_sel:DWORD dst_unused:UNUSED_PAD src0_sel:DWORD src1_sel:WORD_0\n" \
  "ds_read_b128 v[100:103], v64\n" \
  "v_lshlrev_b32_sdwa v65, 4, v230 dst_sel:DWORD dst_unused:UNUSED_PAD src0_sel:DWORD src1_sel:WORD_1\n" \
  "ds_read_b128 v[104:107], v65\n" \
  "s_mov_b32 m0, s58\n" \
  "s_add_u32 s35, s58, 0x8000\n" \
  "global_load_lds_dwordx4 v190, s[54:55] sc1\n" \
  "global_load_lds_dwordx4 v190, s[54:55] offset:1024 sc1\n" \
  "s_waitcnt lgkmcnt(10)\n" \
  "v_smfmac_f32_16x16x64_bf16 v[4:7], v[180:183], v[108:115], v192\n" \
  "v_lshlrev_b32_sdwa v66, 4, v231 dst_sel:DWORD dst_unused:UNUSED_PAD src0_sel:DWORD src1_sel:WORD_0\n" \
  "ds_read_b128 v[108:111], v66\n" \
  "v_lshlrev_b32_sdwa v67, 4, v231 dst_sel:DWORD dst_unused:UNUSED_PAD src0_sel:DWORD src1_sel:WORD_1\n" \
  "ds_read_b128 v[112:115], v67\n" \
  "s_waitcnt lgkmcnt(10)\n" \
  "v_smfmac_f32_16x16x64_bf16 v[8:11], v[180:183], v[68:75], v192\n" \
  "v_lshlrev_b32_sdwa v64, 4, v232 dst_sel:DWORD dst_unused:UNUSED_PAD src0_sel:DWORD src1_sel:WORD_0\n" \
  "ds_read_b128 v[68:71], v64\n" \
  "v_lshlrev_b32_sdwa v65, 4, v232 dst_sel:DWORD dst_unused:UNUSED_PAD src0_sel:DWORD src1_sel:WORD_1\n" \
  "ds_read_b128 v[72:75], v65\n" \
  "s_add_u32 m0, s58, 0x4000\n" \
  "s_cmp_lt_u32 s35, 0x28000\n" \
  "global_load_lds_dwordx4 v191, s[54:55] sc1\n" \
  "global_load_lds_dwordx4 v191, s[54:55] offset:1024 sc1\n" \
  "s_cselect_b32 s58, s35, s59\n" \
  "s_waitcnt lgkmcnt(10)\n" \
  "v_smfmac_f32_16x16x64_bf16 v[12:15], v[180:183], v[76:83], v192\n" \
  "v_lshlrev_b32_sdwa v66, 4, v233 dst_sel:DWORD dst_unused:UNUSED_PAD src0_sel:DWORD src1_sel:WORD_0\n" \
  "ds_read_b128 v[76:79], v66\n" \
  "v_lshlrev_b32_sdwa v67, 4, v233 dst_sel:DWORD dst_unused:UNUSED_PAD src0_sel:DWORD src1_sel:WORD_1\n" \
  "ds_read_b128 v[80:83], v67\n" \
  "s_waitcnt lgkmcnt(10)\n" \
  "v_smfmac_f32_16x16x64_bf16 v[16:19], v[180:183], v[84:91], v192\n" \
  "v_lshlrev_b32_sdwa v64, 4, v234 dst_sel:DWORD dst_unused:UNUSED_PAD src0_sel:DWORD src1_sel:WORD_0\n" \
  "ds_read_b128 v[84:87], v64\n" \
  "v_lshlrev_b32_sdwa v65, 4, v234 dst_sel:DWORD dst_unused:UNUSED_PAD src0_sel:DWORD src1_sel:WORD_1\n" \
  "ds_read_b128 v[88:91], v65\n" \
  "global_load_dwordx4 v[208:211], v188, s[56:57] sc1\n" \
  "global_load_dwordx4 v[212:215], v188, s[56:57] offset:1024 sc1\n" \
  "s_waitcnt lgkmcnt(10)\n" \
  "v_smfmac_f32_16x16x64_bf16 v[20:23], v[180:183], v[92:99], v192\n" \
  "v_lshlrev_b32_sdwa v66, 4, v235 dst_sel:DWORD dst_unused:UNUSED_PAD src0_sel:DWORD src1_sel:WORD_0\n" \
  "ds_read_b128 v[92:95], v66\n" \
  "v_lshlrev_b32_sdwa v67, 4, v235 dst_sel:DWORD dst_unused:UNUSED_PAD src0_sel:DWORD src1_sel:WORD_1\n" \
  "ds_read_b128 v[96:99], v67\n" \
  "s_waitcnt lgkmcnt(10)\n" \
  "v_smfmac_f32_16x16x64_bf16 v[24:27], v[180:183], v[100:107], v192\n" \
  "v_lshlrev_b32_sdwa v64, 4, v236 dst_sel:DWORD dst_unused:UNUSED_PAD src0_sel:DWORD src1_sel:WORD_0\n" \
  "ds_read_b128 v[100:103], v64\n" \
  "v_lshlrev_b32_sdwa v65, 4, v236 dst_sel:DWORD dst_unused:UNUSED_PAD src0_sel:DWORD src1_sel:WORD_1\n" \
  "ds_read_b128 v[104:107], v65\n" \
  "global_load_dwordx4 v[216:219], v188, s[56:57] offset:2048 sc1\n" \
  "global_load_dwordx4 v[220:223], v188, s[56:57] offset:3072 sc1\n" \
  "s_waitcnt lgkmcnt(10)\n" \
  "v_smfmac_f32_16x16x64_bf16 v[28:31], v[180:183], v[108:115], v192\n" \
  "v_lshlrev_b32_sdwa v66, 4, v237 dst_sel:DWORD dst_unused:UNUSED_PAD src0_sel:DWORD src1_sel:WORD_0\n" \
  "ds_read_b128 v[108:111], v66\n" \
  "v_lshlrev_b32_sdwa v67, 4, v237 dst_sel:DWORD dst_unused:UNUSED_PAD src0_sel:DWORD src1_sel:WORD_1\n" \
  "ds_read_b128 v[112:115], v67\n" \
  "s_waitcnt lgkmcnt(10)\n" \
  "v_smfmac_f32_16x16x64_bf16 v[32:35], v[180:183], v[68:75], v192\n" \
  "v_lshlrev_b32_sdwa v64, 4, v238 dst_sel:DWORD dst_unused:UNUSED_PAD src0_sel:DWORD src1_sel:WORD_0\n" \
  "ds_read_b128 v[68:71], v64\n" \
  "v_lshlrev_b32_sdwa v65, 4, v238 dst_sel:DWORD dst_unused:UNUSED_PAD src0_sel:DWORD src1_sel:WORD_1\n" \
  "ds_read_b128 v[72:75], v65\n" \
  "s_waitcnt lgkmcnt(10)\n" \
  "v_smfmac_f32_16x16x64_bf16 v[36:39], v[180:183], v[76:83], v192\n" \
  "v_lshlrev_b32_sdwa v66, 4, v239 dst_sel:DWORD dst_unused:UNUSED_PAD src0_sel:DWORD src1_sel:WORD_0\n" \
  "ds_read_b128 v[76:79], v66\n" \
  "v_lshlrev_b32_sdwa v67, 4, v239 dst_sel:DWORD dst_unused:UNUSED_PAD src0_sel:DWORD src1_sel:WORD_1\n" \
  "ds_read_b128 v[80:83], v67\n" \
  "s_add_u32 s56, s56, 32768\n" \
  "s_addc_u32 s57, s57, 0\n" \
  "s_add_u32 s54, s54, 64000\n" \
  "s_addc_u32 s55, s55, 0\n" \
  "s_waitcnt lgkmcnt(10)\n" \
  "v_smfmac_f32_16x16x64_bf16 v[40:43], v[180:183], v[84:91], v192\n" \
  "s_waitcnt lgkmcnt(8)\n" \
  "v_smfmac_f32_16x16x64_bf16 v[44:47], v[180:183], v[92:99], v192\n" \
  "s_waitcnt vmcnt(8)\n" \
  "s_barrier\n" \
  "v_lshlrev_b32_sdwa v64, 4, v240 dst_sel:DWORD dst_unused:UNUSED_PAD src0_sel:DWORD src1_sel:WORD_0\n" \
  "ds_read_b128 v[84:87], v64\n" \
  "v_lshlrev_b32_sdwa v65, 4, v240 dst_sel:DWORD dst_unused:UNUSED_PAD src0_sel:DWORD src1_sel:WORD_1\n" \
  "ds_read_b128 v[88:91], v65\n" \
  "v_lshlrev_b32_sdwa v66, 4, v241 dst_sel:DWORD dst_unused:UNUSED_PAD src0_sel:DWORD src1_sel:WORD_0\n" \
  "ds_read_b128 v[92:95], v66\n" \
  "v_lshlrev_b32_sdwa v67, 4, v241 dst_sel:DWORD dst_unused:UNUSED_PAD src0_sel:DWORD src1_sel:WORD_1\n" \
  "ds_read_b128 v[96:99], v67\n" \
  "s_waitcnt lgkmcnt(10)\n" \
  "v_smfmac_f32_16x16x64_bf16 v[48:51], v[180:183], v[100:107], v192\n" \
  "s_waitcnt lgkmcnt(8)\n" \
  "v_smfmac_f32_16x16x64_bf16 v[52:55], v[180:183], v[108:115], v192\n" \
  "s_waitcnt lgkmcnt(6)\n" \
  "v_smfmac_f32_16x16x64_bf16 v[56:59], v[180:183], v[68:75], v192\n" \
  "s_waitcnt lgkmcnt(4)\n" \
  "v_smfmac_f32_16x16x64_bf16 v[60:63], v[180:183], v[76:83], v192\n" \
  "s_barrier\n" \
  "s_mov_b32 m0, s58\n" \
  "s_add_u32 s35, s58, 0x8000\n" \
  "global_load_lds_dwordx4 v188, s[54:55] sc1\n" \
  "global_load_lds_dwordx4 v188, s[54:55] offset:1024 sc1\n" \
  "s_add_u32 m0, s58, 0x4000\n" \
  "s_cmp_lt_u32 s35, 0x28000\n" \
  "global_load_lds_dwordx4 v189, s[54:55] sc1\n" \
  "global_load_lds_dwordx4 v189, s[54:55] offset:1024 sc1\n" \
  "s_cselect_b32 s58, s35, s59\n" \
  "v_lshlrev_b32 v120, 16, v206\n" \
  "v_and_b32 v121, 0xffff0000, v206\n" \
  "v_lshlrev_b32 v122, 16, v207\n" \
  "v_and_b32 v123, 0xffff0000, v207\n" \
  "v_mov_b32 v124, 0xc0135761\n" \
  "v_mov_b32 v125, 0xc0135761\n" \
  "s_mov_b32 s52, 0xbdd2d3e8\n" \
  "s_mov_b32 s53, s52\n" \
  "s_lshl2_add_u32 s35, s33, s34\n" \
  "s_lshl_b32 s35, s35, 14\n" \
  "s_add_u32 s35, s35, s59\n" \
  "s_add_u32 s35, s35, 105676800\n" \
  "s_add_u32 s40, s24, s35\n" \
  "s_addc_u32 s41, s25, 0\n" \
  "v_lshlrev_b32_sdwa v64, 4, v242 dst_sel:DWORD dst_unused:UNUSED_PAD src0_sel:DWORD src1_sel:WORD_0\n" \
  "ds_read_b128 v[100:103], v64\n" \
  "v_lshlrev_b32_sdwa v65, 4, v242 dst_sel:DWORD dst_unused:UNUSED_PAD src0_sel:DWORD src1_sel:WORD_1\n" \
  "ds_read_b128 v[104:107], v65\n" \
  "v_lshlrev_b32_sdwa v66, 4, v243 dst_sel:DWORD dst_unused:UNUSED_PAD src0_sel:DWORD src1_sel:WORD_0\n" \
  "ds_read_b128 v[108:111], v66\n" \
  "v_lshlrev_b32_sdwa v67, 4, v243 dst_sel:DWORD dst_unused:UNUSED_PAD src0_sel:DWORD src1_sel:WORD_1\n" \
  "ds_read_b128 v[112:115], v67\n" \
  "v_lshlrev_b32_sdwa v64, 4, v244 dst_sel:DWORD dst_unused:UNUSED_PAD src0_sel:DWORD src1_sel:WORD_0\n" \
  "ds_read_b128 v[68:71], v64\n" \
  "v_lshlrev_b32_sdwa v65, 4, v244 dst_sel:DWORD dst_unused:UNUSED_PAD src0_sel:DWORD src1_sel:WORD_1\n" \
  "ds_read_b128 v[72:75], v65\n" \
  "v_lshlrev_b32_sdwa v66, 4, v245 dst_sel:DWORD dst_unused:UNUSED_PAD src0_sel:DWORD src1_sel:WORD_0\n" \
  "ds_read_b128 v[76:79], v66\n" \
  "v_lshlrev_b32_sdwa v67, 4, v245 dst_sel:DWORD dst_unused:UNUSED_PAD src0_sel:DWORD src1_sel:WORD_1\n" \
  "ds_read_b128 v[80:83], v67\n" \
  "s_waitcnt lgkmcnt(10)\n" \
  "v_smfmac_f32_16x16x64_bf16 v[0:3], v[180:183], v[84:91], v192\n" \
  "v_lshlrev_b32_sdwa v64, 4, v246 dst_sel:DWORD dst_unused:UNUSED_PAD src0_sel:DWORD src1_sel:WORD_0\n" \
  "ds_read_b128 v[84:87], v64\n" \
  "v_lshlrev_b32_sdwa v65, 4, v246 dst_sel:DWORD dst_unused:UNUSED_PAD src0_sel:DWORD src1_sel:WORD_1\n" \
  "ds_read_b128 v[88:91], v65\n" \
  "s_waitcnt lgkmcnt(10)\n" \
  "v_smfmac_f32_16x16x64_bf16 v[4:7], v[180:183], v[92:99], v192\n" \
  "v_lshlrev_b32_sdwa v66, 4, v247 dst_sel:DWORD dst_unused:UNUSED_PAD src0_sel:DWORD src1_sel:WORD_0\n" \
  "ds_read_b128 v[92:95], v66\n" \
  "v_lshlrev_b32_sdwa v67, 4, v247 dst_sel:DWORD dst_unused:UNUSED_PAD src0_sel:DWORD src1_sel:WORD_1\n" \
  "ds_read_b128 v[96:99], v67\n" \
  "s_waitcnt lgkmcnt(10)\n" \
  "v_smfmac_f32_16x16x64_bf16 v[8:11], v[180:183], v[100:107], v192\n" \
  "v_lshlrev_b32_sdwa v64, 4, v248 dst_sel:DWORD dst_unused:UNUSED_PAD src0_sel:DWORD src1_sel:WORD_0\n" \
  "ds_read_b128 v[100:103], v64\n" \
  "v_lshlrev_b32_sdwa v65, 4, v248 dst_sel:DWORD dst_unused:UNUSED_PAD src0_sel:DWORD src1_sel:WORD_1\n" \
  "ds_read_b128 v[104:107], v65\n" \
  "s_waitcnt lgkmcnt(10)\n" \
  "v_smfmac_f32_16x16x64_bf16 v[12:15], v[180:183], v[108:115], v192\n" \
  "v_lshlrev_b32_sdwa v66, 4, v249 dst_sel:DWORD dst_unused:UNUSED_PAD src0_sel:DWORD src1_sel:WORD_0\n" \
  "ds_read_b128 v[108:111], v66\n" \
  "v_lshlrev_b32_sdwa v67, 4, v249 dst_sel:DWORD dst_unused:UNUSED_PAD src0_sel:DWORD src1_sel:WORD_1\n" \
  "ds_read_b128 v[112:115], v67\n" \
  "s_waitcnt vmcnt(0)\n" \
  "s_barrier\n" \
  "s_waitcnt lgkmcnt(10)\n" \
  "v_smfmac_f32_16x16x64_bf16 v[16:19], v[180:183], v[68:75], v192\n" \
  "s_waitcnt lgkmcnt(8)\n" \
  "v_smfmac_f32_16x16x64_bf16 v[20:23], v[180:183], v[76:83], v192\n" \
  "s_waitcnt lgkmcnt(6)\n" \
  "v_smfmac_f32_16x16x64_bf16 v[24:27], v[180:183], v[84:91], v192\n" \
  "s_waitcnt lgkmcnt(4)\n" \
  "v_smfmac_f32_16x16x64_bf16 v[28:31], v[180:183], v[92:99], v192\n" \
  "v_lshlrev_b32_sdwa v64, 4, v208 dst_sel:DWORD dst_unused:UNUSED_PAD src0_sel:DWORD src1_sel:WORD_0\n" \
  "ds_read_b128 v[68:71], v64\n" \
  "v_lshlrev_b32_sdwa v65, 4, v208 dst_sel:DWORD dst_unused:UNUSED_PAD src0_sel:DWORD src1_sel:WORD_1\n" \
  "ds_read_b128 v[72:75], v65\n" \
  "s_waitcnt lgkmcnt(4)\n" \
  "v_smfmac_f32_16x16x64_bf16 v[32:35], v[180:183], v[100:107], v192\n" \
  "v_lshlrev_b32_sdwa v66, 4, v209 dst_sel:DWORD dst_unused:UNUSED_PAD src0_sel:DWORD src1_sel:WORD_0\n" \
  "ds_read_b128 v[76:79], v66\n" \
  "v_lshlrev_b32_sdwa v67, 4, v209 dst_sel:DWORD dst_unused:UNUSED_PAD src0_sel:DWORD src1_sel:WORD_1\n" \
  "ds_read_b128 v[80:83], v67\n" \
  "s_waitcnt lgkmcnt(4)\n" \
  "v_smfmac_f32_16x16x64_bf16 v[36:39], v[180:183], v[108:115], v192\n" \
  "v_lshlrev_b32_sdwa v64, 4, v210 dst_sel:DWORD dst_unused:UNUSED_PAD src0_sel:DWORD src1_sel:WORD_0\n" \
  "ds_read_b128 v[84:87], v64\n" \
  "v_lshlrev_b32_sdwa v65, 4, v210 dst_sel:DWORD dst_unused:UNUSED_PAD src0_sel:DWORD src1_sel:WORD_1\n" \
  "ds_read_b128 v[88:91], v65\n" \
  "s_waitcnt lgkmcnt(4)\n" \
  "v_smfmac_f32_16x16x64_bf16 v[0:3], v[180:183], v[68:75], v192\n" \
  "v_lshlrev_b32_sdwa v66, 4, v211 dst_sel:DWORD dst_unused:UNUSED_PAD src0_sel:DWORD src1_sel:WORD_0\n" \
  "ds_read_b128 v[92:95], v66\n" \
  "v_lshlrev_b32_sdwa v67, 4, v211 dst_sel:DWORD dst_unused:UNUSED_PAD src0_sel:DWORD src1_sel:WORD_1\n" \
  "ds_read_b128 v[96:99], v67\n" \
  "s_waitcnt lgkmcnt(4)\n" \
  "v_smfmac_f32_16x16x64_bf16 v[4:7], v[180:183], v[76:83], v192\n" \
  "v_lshlrev_b32_sdwa v64, 4, v212 dst_sel:DWORD dst_unused:UNUSED_PAD src0_sel:DWORD src1_sel:WORD_0\n" \
  "ds_read_b128 v[100:103], v64\n" \
  "v_lshlrev_b32_sdwa v65, 4, v212 dst_sel:DWORD dst_unused:UNUSED_PAD src0_sel:DWORD src1_sel:WORD_1\n" \
  "ds_read_b128 v[104:107], v65\n" \
  "v_pk_mul_f32 v[128:129], v[0:1], v[0:1]\n" \
  "v_pk_mul_f32 v[130:131], v[2:3], v[2:3]\n" \
  "v_pk_fma_f32 v[128:129], v[128:129], s[52:53], v[124:125]\n" \
  "v_pk_fma_f32 v[130:131], v[130:131], s[52:53], v[124:125]\n" \
  "v_pk_mul_f32 v[128:129], v[128:129], v[0:1]\n" \
  "v_pk_mul_f32 v[130:131], v[130:131], v[2:3]\n" \
  "v_exp_f32 v128, v128\n" \
  "v_exp_f32 v129, v129\n" \
  "v_pk_mul_f32 v[0:1], v[0:1], v[120:121]\n" \
  "v_exp_f32 v130, v130\n" \
  "v_exp_f32 v131, v131\n" \
  "v_pk_mul_f32 v[2:3], v[2:3], v[122:123]\n" \
  "v_pk_add_f32 v[128:129], v[128:129], 1.0 op_sel_hi:[1,0]\n" \
  "v_pk_add_f32 v[130:131], v[130:131], 1.0 op_sel_hi:[1,0]\n" \
  "v_rcp_f32 v128, v128\n" \
  "v_rcp_f32 v129, v129\n" \
  "s_waitcnt lgkmcnt(4)\n" \
  "v_smfmac_f32_16x16x64_bf16 v[8:11], v[180:183], v[84:91], v192\n" \
  "v_lshlrev_b32_sdwa v66, 4, v213 dst_sel:DWORD dst_unused:UNUSED_PAD src0_sel:DWORD src1_sel:WORD_0\n" \
  "ds_read_b128 v[108:111], v66\n" \
  "v_lshlrev_b32_sdwa v67, 4, v213 dst_sel:DWORD dst_unused:UNUSED_PAD src0_sel:DWORD src1_sel:WORD_1\n" \
  "ds_read_b128 v[112:115], v67\n" \
  "v_rcp_f32 v130, v130\n" \
  "v_rcp_f32 v131, v131\n" \
  "v_pk_mul_f32 v[0:1], v[0:1], v[128:129]\n" \
  "v_pk_fma_f32 v[0:1], v[2:3], v[130:131], v[0:1]\n" \
  "v_add_f32 v0, v0, v1\n" \
  "v_pk_mul_f32 v[132:133], v[4:5], v[4:5]\n" \
  "v_pk_mul_f32 v[134:135], v[6:7], v[6:7]\n" \
  "v_pk_fma_f32 v[132:133], v[132:133], s[52:53], v[124:125]\n" \
  "v_pk_fma_f32 v[134:135], v[134:135], s[52:53], v[124:125]\n" \
  "v_pk_mul_f32 v[132:133], v[132:133], v[4:5]\n" \
  "v_pk_mul_f32 v[134:135], v[134:135], v[6:7]\n" \
  "v_exp_f32 v132, v132\n" \
  "v_exp_f32 v133, v133\n" \
  "v_pk_mul_f32 v[4:5], v[4:5], v[120:121]\n" \
  "v_exp_f32 v134, v134\n" \
  "v_exp_f32 v135, v135\n" \
  "s_waitcnt lgkmcnt(4)\n" \
  "v_smfmac_f32_16x16x64_bf16 v[12:15], v[180:183], v[92:99], v192\n" \
  "v_lshlrev_b32_sdwa v64, 4, v214 dst_sel:DWORD dst_unused:UNUSED_PAD src0_sel:DWORD src1_sel:WORD_0\n" \
  "ds_read_b128 v[68:71], v64\n" \
  "v_lshlrev_b32_sdwa v65, 4, v214 dst_sel:DWORD dst_unused:UNUSED_PAD src0_sel:DWORD src1_sel:WORD_1\n" \
  "ds_read_b128 v[72:75], v65\n" \
  "v_pk_mul_f32 v[6:7], v[6:7], v[122:123]\n" \
  "v_pk_add_f32 v[132:133], v[132:133], 1.0 op_sel_hi:[1,0]\n" \
  "v_pk_add_f32 v[134:135], v[134:135], 1.0 op_sel_hi:[1,0]\n" \
  "v_rcp_f32 v132, v132\n" \
  "v_rcp_f32 v133, v133\n" \
  "v_rcp_f32 v134, v134\n" \
  "v_rcp_f32 v135, v135\n" \
  "v_pk_mul_f32 v[4:5], v[4:5], v[132:133]\n" \
  "v_pk_fma_f32 v[4:5], v[6:7], v[134:135], v[4:5]\n" \
  "v_add_f32 v4, v4, v5\n" \
  "v_pk_mul_f32 v[136:137], v[8:9], v[8:9]\n" \
  "v_pk_mul_f32 v[138:139], v[10:11], v[10:11]\n" \
  "v_pk_fma_f32 v[136:137], v[136:137], s[52:53], v[124:125]\n" \
  "v_pk_fma_f32 v[138:139], v[138:139], s[52:53], v[124:125]\n" \
  "v_pk_mul_f32 v[136:137], v[136:137], v[8:9]\n" \
  "v_pk_mul_f32 v[138:139], v[138:139], v[10:11]\n" \
  "s_waitcnt lgkmcnt(4)\n" \
  "v_smfmac_f32_16x16x64_bf16 v[16:19], v[180:183], v[100:107], v192\n" \
  "v_lshlrev_b32_sdwa v66, 4, v215 dst_sel:DWORD dst_unused:UNUSED_PAD src0_sel:DWORD src1_sel:WORD_0\n" \
  "ds_read_b128 v[76:79], v66\n" \
  "v_lshlrev_b32_sdwa v67, 4, v215 dst_sel:DWORD dst_unused:UNUSED_PAD src0_sel:DWORD src1_sel:WORD_1\n" \
  "ds_read_b128 v[80:83], v67\n" \
  "v_exp_f32 v136, v136\n" \
  "v_exp_f32 v137, v137\n" \
  "v_pk_mul_f32 v[8:9], v[8:9], v[120:121]\n" \
  "v_exp_f32 v138, v138\n" \
  "v_exp_f32 v139, v139\n" \
  "v_pk_mul_f32 v[10:11], v[10:11], v[122:123]\n" \
  "v_pk_add_f32 v[136:137], v[136:137], 1.0 op_sel_hi:[1,0]\n" \
  "v_pk_add_f32 v[138:139], v[138:139], 1.0 op_sel_hi:[1,0]\n" \
  "v_rcp_f32 v136, v136\n" \
  "v_rcp_f32 v137, v137\n" \
  "v_rcp_f32 v138, v138\n" \
  "v_rcp_f32 v139, v139\n" \
  "v_pk_mul_f32 v[8:9], v[8:9], v[136:137]\n" \
  "v_pk_fma_f32 v[8:9], v[10:11], v[138:139], v[8:9]\n" \
  "v_add_f32 v8, v8, v9\n" \
  "v_pk_mul_f32 v[140:141], v[12:13], v[12:13]\n" \
  "s_waitcnt lgkmcnt(4)\n" \
  "v_smfmac_f32_16x16x64_bf16 v[20:23], v[180:183], v[108:115], v192\n" \
  "v_lshlrev_b32_sdwa v64, 4, v216 dst_sel:DWORD dst_unused:UNUSED_PAD src0_sel:DWORD src1_sel:WORD_0\n" \
  "ds_read_b128 v[84:87], v64\n" \
  "v_lshlrev_b32_sdwa v65, 4, v216 dst_sel:DWORD dst_unused:UNUSED_PAD src0_sel:DWORD src1_sel:WORD_1\n" \
  "ds_read_b128 v[88:91], v65\n" \
  "v_pk_mul_f32 v[142:143], v[14:15], v[14:15]\n" \
  "v_pk_fma_f32 v[140:141], v[140:141], s[52:53], v[124:125]\n" \
  "v_pk_fma_f32 v[142:143], v[142:143], s[52:53], v[124:125]\n" \
  "v_pk_mul_f32 v[140:141], v[140:141], v[12:13]\n" \
  "v_pk_mul_f32 v[142:143], v[142:143], v[14:15]\n" \
  "v_exp_f32 v140, v140\n" \
  "v_exp_f32 v141, v141\n" \
  "v_pk_mul_f32 v[12:13], v[12:13], v[120:121]\n" \
  "v_exp_f32 v142, v142\n" \
  "v_exp_f32 v143, v143\n" \
  "v_pk_mul_f32 v[14:15], v[14:15], v[122:123]\n" \
  "v_pk_add_f32 v[140:141], v[140:141], 1.0 op_sel_hi:[1,0]\n" \
  "v_pk_add_f32 v[142:143], v[142:143], 1.0 op_sel_hi:[1,0]\n" \
  "v_rcp_f32 v140, v140\n" \
  "v_rcp_f32 v141, v141\n" \
  "v_rcp_f32 v142, v142\n" \
  "s_waitcnt lgkmcnt(4)\n" \
  "v_smfmac_f32_16x16x64_bf16 v[24:27], v[180:183], v[68:75], v192\n" \
  "v_lshlrev_b32_sdwa v66, 4, v217 dst_sel:DWORD dst_unused:UNUSED_PAD src0_sel:DWORD src1_sel:WORD_0\n" \
  "ds_read_b128 v[92:95], v66\n" \
  "v_lshlrev_b32_sdwa v67, 4, v217 dst_sel:DWORD dst_unused:UNUSED_PAD src0_sel:DWORD src1_sel:WORD_1\n" \
  "ds_read_b128 v[96:99], v67\n" \
  "v_rcp_f32 v143, v143\n" \
  "v_pk_mul_f32 v[12:13], v[12:13], v[140:141]\n" \
  "v_pk_fma_f32 v[12:13], v[14:15], v[142:143], v[12:13]\n" \
  "v_add_f32 v12, v12, v13\n" \
  "v_pk_mul_f32 v[128:129], v[16:17], v[16:17]\n" \
  "v_pk_mul_f32 v[130:131], v[18:19], v[18:19]\n" \
  "v_pk_fma_f32 v[128:129], v[128:129], s[52:53], v[124:125]\n" \
  "v_pk_fma_f32 v[130:131], v[130:131], s[52:53], v[124:125]\n" \
  "v_pk_mul_f32 v[128:129], v[128:129], v[16:17]\n" \
  "v_pk_mul_f32 v[130:131], v[130:131], v[18:19]\n" \
  "v_exp_f32 v128, v128\n" \
  "v_exp_f32 v129, v129\n" \
  "v_pk_mul_f32 v[16:17], v[16:17], v[120:121]\n" \
  "v_exp_f32 v130, v130\n" \
  "v_exp_f32 v131, v131\n" \
  "v_pk_mul_f32 v[18:19], v[18:19], v[122:123]\n" \
  "s_waitcnt lgkmcnt(4)\n" \
  "v_smfmac_f32_16x16x64_bf16 v[28:31], v[180:183], v[76:83], v192\n" \
  "v_lshlrev_b32_sdwa v64, 4, v250 dst_sel:DWORD dst_unused:UNUSED_PAD src0_sel:DWORD src1_sel:WORD_0\n" \
  "ds_read_b128 v[100:103], v64\n" \
  "v_lshlrev_b32_sdwa v65, 4, v250 dst_sel:DWORD dst_unused:UNUSED_PAD src0_sel:DWORD src1_sel:WORD_1\n" \
  "ds_read_b128 v[104:107], v65\n" \
  "v_pk_add_f32 v[128:129], v[128:129], 1.0 op_sel_hi:[1,0]\n" \
  "v_pk_add_f32 v[130:131], v[130:131], 1.0 op_sel_hi:[1,0]\n" \
  "v_rcp_f32 v128, v128\n" \
  "v_rcp_f32 v129, v129\n" \
  "v_rcp_f32 v130, v130\n" \
  "v_rcp_f32 v131, v131\n" \
  "v_pk_mul_f32 v[16:17], v[16:17], v[128:129]\n" \
  "v_pk_fma_f32 v[16:17], v[18:19], v[130:131], v[16:17]\n" \
  "v_add_f32 v16, v16, v17\n" \
  "v_pk_mul_f32 v[132:133], v[20:21], v[20:21]\n" \
  "v_pk_mul_f32 v[134:135], v[22:23], v[22:23]\n" \
  "v_pk_fma_f32 v[132:133], v[132:133], s[52:53], v[124:125]\n" \
  "v_pk_fma_f32 v[134:135], v[134:135], s[52:53], v[124:125]\n" \
  "v_pk_mul_f32 v[132:133], v[132:133], v[20:21]\n" \
  "v_pk_mul_f32 v[134:135], v[134:135], v[22:23]\n" \
  "v_exp_f32 v132, v132\n" \
  "s_waitcnt lgkmcnt(4)\n" \
  "v_smfmac_f32_16x16x64_bf16 v[32:35], v[180:183], v[84:91], v192\n" \
  "v_lshlrev_b32_sdwa v66, 4, v218 dst_sel:DWORD dst_unused:UNUSED_PAD src0_sel:DWORD src1_sel:WORD_0\n" \
  "ds_read_b128 v[108:111], v66\n" \
  "v_lshlrev_b32_sdwa v67, 4, v218 dst_sel:DWORD dst_unused:UNUSED_PAD src0_sel:DWORD src1_sel:WORD_1\n" \
  "ds_read_b128 v[112:115], v67\n" \
  "v_exp_f32 v133, v133\n" \
  "v_pk_mul_f32 v[20:21], v[20:21], v[120:121]\n" \
  "v_exp_f32 v134, v134\n" \
  "v_exp_f32 v135, v135\n" \
  "v_pk_mul_f32 v[22:23], v[22:23], v[122:123]\n" \
  "v_pk_add_f32 v[132:133], v[132:133], 1.0 op_sel_hi:[1,0]\n" \
  "v_pk_add_f32 v[134:135], v[134:135], 1.0 op_sel_hi:[1,0]\n" \
  "v_rcp_f32 v132, v132\n" \
  "v_rcp_f32 v133, v133\n" \
  "v_rcp_f32 v134, v134\n" \
  "v_rcp_f32 v135, v135\n" \
  "v_pk_mul_f32 v[20:21], v[20:21], v[132:133]\n" \
  "v_pk_fma_f32 v[20:21], v[22:23], v[134:135], v[20:21]\n" \
  "v_add_f32 v20, v20, v21\n" \
  "v_pk_mul_f32 v[136:137], v[24:25], v[24:25]\n" \
  "v_pk_mul_f32 v[138:139], v[26:27], v[26:27]\n" \
  "s_waitcnt lgkmcnt(4)\n" \
  "v_smfmac_f32_16x16x64_bf16 v[36:39], v[180:183], v[92:99], v192\n" \
  "v_lshlrev_b32_sdwa v64, 4, v251 dst_sel:DWORD dst_unused:UNUSED_PAD src0_sel:DWORD src1_sel:WORD_0\n" \
  "ds_read_b128 v[68:71], v64\n" \
  "v_lshlrev_b32_sdwa v65, 4, v251 dst_sel:DWORD dst_unused:UNUSED_PAD src0_sel:DWORD src1_sel:WORD_1\n" \
  "ds_read_b128 v[72:75], v65\n" \
  "v_pk_fma_f32 v[136:137], v[136:137], s[52:53], v[124:125]\n" \
  "v_pk_fma_f32 v[138:139], v[138:139], s[52:53], v[124:125]\n" \
  "v_pk_mul_f32 v[136:137], v[136:137], v[24:25]\n" \
  "v_pk_mul_f32 v[138:139], v[138:139], v[26:27]\n" \
  "v_exp_f32 v136, v136\n" \
  "v_exp_f32 v137, v137\n" \
  "v_pk_mul_f32 v[24:25], v[24:25], v[120:121]\n" \
  "v_exp_f32 v138, v138\n" \
  "v_exp_f32 v139, v139\n" \
  "v_pk_mul_f32 v[26:27], v[26:27], v[122:123]\n" \
  "v_pk_add_f32 v[136:137], v[136:137], 1.0 op_sel_hi:[1,0]\n" \
  "v_pk_add_f32 v[138:139], v[138:139], 1.0 op_sel_hi:[1,0]\n" \
  "v_rcp_f32 v136, v136\n" \
  "v_rcp_f32 v137, v137\n" \
  "v_rcp_f32 v138, v138\n" \
  "v_rcp_f32 v139, v139\n" \
  "s_waitcnt lgkmcnt(4)\n" \
  "v_smfmac_f32_16x16x64_bf16 v[40:43], v[180:183], v[100:107], v192\n" \
  "v_lshlrev_b32_sdwa v66, 4, v219 dst_sel:DWORD dst_unused:UNUSED_PAD src0_sel:DWORD src1_sel:WORD_0\n" \
  "ds_read_b128 v[76:79], v66\n" \
  "v_lshlrev_b32_sdwa v67, 4, v219 dst_sel:DWORD dst_unused:UNUSED_PAD src0_sel:DWORD src1_sel:WORD_1\n" \
  "ds_read_b128 v[80:83], v67\n" \
  "v_pk_mul_f32 v[24:25], v[24:25], v[136:137]\n" \
  "v_pk_fma_f32 v[24:25], v[26:27], v[138:139], v[24:25]\n" \
  "v_add_f32 v24, v24, v25\n" \
  "v_pk_mul_f32 v[140:141], v[28:29], v[28:29]\n" \
  "v_pk_mul_f32 v[142:143], v[30:31], v[30:31]\n" \
  "v_pk_fma_f32 v[140:141], v[140:141], s[52:53], v[124:125]\n" \
  "v_pk_fma_f32 v[142:143], v[142:143], s[52:53], v[124:125]\n" \
  "v_pk_mul_f32 v[140:141], v[140:141], v[28:29]\n" \
  "v_pk_mul_f32 v[142:143], v[142:143], v[30:31]\n" \
  "v_exp_f32 v140, v140\n" \
  "v_exp_f32 v141, v141\n" \
  "v_pk_mul_f32 v[28:29], v[28:29], v[120:121]\n" \
  "v_exp_f32 v142, v142\n" \
  "v_exp_f32 v143, v143\n" \
  "v_pk_mul_f32 v[30:31], v[30:31], v[122:123]\n" \
  "v_pk_add_f32 v[140:141], v[140:141], 1.0 op_sel_hi:[1,0]\n" \
  "s_waitcnt lgkmcnt(4)\n" \
  "v_smfmac_f32_16x16x64_bf16 v[40:43], v[180:183], v[108:115], v192\n" \
  "v_lshlrev_b32_sdwa v64, 4, v252 dst_sel:DWORD dst_unused:UNUSED_PAD src0_sel:DWORD src1_sel:WORD_0\n" \
  "ds_read_b128 v[84:87], v64\n" \
  "v_lshlrev_b32_sdwa v65, 4, v252 dst_sel:DWORD dst_unused:UNUSED_PAD src0_sel:DWORD src1_sel:WORD_1\n" \
  "ds_read_b128 v[88:91], v65\n" \
  "v_pk_add_f32 v[142:143], v[142:143], 1.0 op_sel_hi:[1,0]\n" \
  "v_rcp_f32 v140, v140\n" \
  "v_rcp_f32 v141, v141\n" \
  "v_rcp_f32 v142, v142\n" \
  "v_rcp_f32 v143, v143\n" \
  "v_pk_mul_f32 v[28:29], v[28:29], v[140:141]\n" \
  "v_pk_fma_f32 v[28:29], v[30:31], v[142:143], v[28:29]\n" \
  "v_add_f32 v28, v28, v29\n" \
  "v_permlane16_swap_b32 v0, v16\n" \
  "v_permlane16_swap_b32 v4, v20\n" \
  "v_permlane16_swap_b32 v8, v24\n" \
  "v_permlane16_swap_b32 v12, v28\n" \
  "v_add_f32 v144, v0, v16\n" \
  "v_add_f32 v145, v4, v20\n" \
  "v_add_f32 v146, v8, v24\n" \
  "v_add_f32 v147, v12, v28\n" \
  "s_waitcnt lgkmcnt(4)\n" \
  "v_smfmac_f32_16x16x64_bf16 v[44:47], v[180:183], v[68:75], v192\n" \
  "v_lshlrev_b32_sdwa v66, 4, v220 dst_sel:DWORD dst_unused:UNUSED_PAD src0_sel:DWORD src1_sel:WORD_0\n" \
  "ds_read_b128 v[92:95], v66\n" \
  "v_lshlrev_b32_sdwa v67, 4, v220 dst_sel:DWORD dst_unused:UNUSED_PAD src0_sel:DWORD src1_sel:WORD_1\n" \
  "ds_read_b128 v[96:99], v67\n" \
  "v_min_u32 v144, -2, v144\n" \
  "v_min_u32 v145, -2, v145\n" \
  "v_min_u32 v146, -2, v146\n" \
  "v_min_u32 v147, -2, v147\n" \
  "global_store_dwordx4 v188, v[144:147], s[40:41] offset:0 sc1\n" \
  "v_pk_mul_f32 v[128:129], v[32:33], v[32:33]\n" \
  "v_pk_mul_f32 v[130:131], v[34:35], v[34:35]\n" \
  "v_pk_fma_f32 v[128:129], v[128:129], s[52:53], v[124:125]\n" \
  "v_pk_fma_f32 v[130:131], v[130:131], s[52:53], v[124:125]\n" \
  "v_pk_mul_f32 v[128:129], v[128:129], v[32:33]\n" \
  "v_pk_mul_f32 v[130:131], v[130:131], v[34:35]\n" \
  "v_exp_f32 v128, v128\n" \
  "v_exp_f32 v129, v129\n" \
  "v_pk_mul_f32 v[32:33], v[32:33], v[120:121]\n" \
  "v_exp_f32 v130, v130\n" \
  "v_exp_f32 v131, v131\n" \
  "s_waitcnt lgkmcnt(4)\n" \
  "v_smfmac_f32_16x16x64_bf16 v[44:47], v[180:183], v[76:83], v192\n" \
  "v_lshlrev_b32_sdwa v64, 4, v253 dst_sel:DWORD dst_unused:UNUSED_PAD src0_sel:DWORD src1_sel:WORD_0\n" \
  "ds_read_b128 v[100:103], v64\n" \
  "v_lshlrev_b32_sdwa v65, 4, v253 dst_sel:DWORD dst_unused:UNUSED_PAD src0_sel:DWORD src1_sel:WORD_1\n" \
  "ds_read_b128 v[104:107], v65\n" \
  "v_pk_mul_f32 v[34:35], v[34:35], v[122:123]\n" \
  "v_pk_add_f32 v[128:129], v[128:129], 1.0 op_sel_hi:[1,0]\n" \
  "v_pk_add_f32 v[130:131], v[130:131], 1.0 op_sel_hi:[1,0]\n" \
  "v_rcp_f32 v128, v128\n" \
  "v_rcp_f32 v129, v129\n" \
  "v_rcp_f32 v130, v130\n" \
  "v_rcp_f32 v131, v131\n" \
  "v_pk_mul_f32 v[32:33], v[32:33], v[128:129]\n" \
  "v_pk_fma_f32 v[32:33], v[34:35], v[130:131], v[32:33]\n" \
  "v_add_f32 v32, v32, v33\n" \
  "v_pk_mul_f32 v[132:133], v[36:37], v[36:37]\n" \
  "v_pk_mul_f32 v[134:135], v[38:39], v[38:39]\n" \
  "v_pk_fma_f32 v[132:133], v[132:133], s[52:53], v[124:125]\n" \
  "v_pk_fma_f32 v[134:135], v[134:135], s[52:53], v[124:125]\n" \
  "v_pk_mul_f32 v[132:133], v[132:133], v[36:37]\n" \
  "v_pk_mul_f32 v[134:135], v[134:135], v[38:39]\n" \
  "s_waitcnt lgkmcnt(4)\n" \
  "v_smfmac_f32_16x16x64_bf16 v[48:51], v[180:183], v[84:91], v192\n" \
  "v_lshlrev_b32_sdwa v66, 4, v221 dst_sel:DWORD dst_unused:UNUSED_PAD src0_sel:DWORD src1_sel:WORD_0\n" \
  "ds_read_b128 v[108:111], v66\n" \
  "v_lshlrev_b32_sdwa v67, 4, v221 dst_sel:DWORD dst_unused:UNUSED_PAD src0_sel:DWORD src1_sel:WORD_1\n" \
  "ds_read_b128 v[112:115], v67\n" \
  "v_exp_f32 v132, v132\n" \
  "v_exp_f32 v133, v133\n" \
  "v_pk_mul_f32 v[36:37], v[36:37], v[120:121]\n" \
  "v_exp_f32 v134, v134\n" \
  "v_exp_f32 v135, v135\n" \
  "v_pk_mul_f32 v[38:39], v[38:39], v[122:123]\n" \
  "v_pk_add_f32 v[132:133], v[132:133], 1.0 op_sel_hi:[1,0]\n" \
  "v_pk_add_f32 v[134:135], v[134:135], 1.0 op_sel_hi:[1,0]\n" \
  "v_rcp_f32 v132, v132\n" \
  "v_rcp_f32 v133, v133\n" \
  "v_rcp_f32 v134, v134\n" \
  "v_rcp_f32 v135, v135\n" \
  "v_pk_mul_f32 v[36:37], v[36:37], v[132:133]\n" \
  "v_pk_fma_f32 v[36:37], v[38:39], v[134:135], v[36:37]\n" \
  "v_add_f32 v36, v36, v37\n" \
  "v_pk_mul_f32 v[136:137], v[40:41], v[40:41]\n" \
  "s_waitcnt lgkmcnt(4)\n" \
  "v_smfmac_f32_16x16x64_bf16 v[48:51], v[180:183], v[92:99], v192\n" \
  "v_lshlrev_b32_sdwa v64, 4, v254 dst_sel:DWORD dst_unused:UNUSED_PAD src0_sel:DWORD src1_sel:WORD_0\n" \
  "ds_read_b128 v[68:71], v64\n" \
  "v_lshlrev_b32_sdwa v65, 4, v254 dst_sel:DWORD dst_unused:UNUSED_PAD src0_sel:DWORD src1_sel:WORD_1\n" \
  "ds_read_b128 v[72:75], v65\n" \
  "v_pk_mul_f32 v[138:139], v[42:43], v[42:43]\n" \
  "v_pk_fma_f32 v[136:137], v[136:137], s[52:53], v[124:125]\n" \
  "v_pk_fma_f32 v[138:139], v[138:139], s[52:53], v[124:125]\n" \
  "v_pk_mul_f32 v[136:137], v[136:137], v[40:41]\n" \
  "v_pk_mul_f32 v[138:139], v[138:139], v[42:43]\n" \
  "v_exp_f32 v136, v136\n" \
  "v_exp_f32 v137, v137\n" \
  "v_pk_mul_f32 v[40:41], v[40:41], v[120:121]\n" \
  "v_exp_f32 v138, v138\n" \
  "v_exp_f32 v139, v139\n" \
  "v_pk_mul_f32 v[42:43], v[42:43], v[122:123]\n" \
  "v_pk_add_f32 v[136:137], v[136:137], 1.0 op_sel_hi:[1,0]\n" \
  "v_pk_add_f32 v[138:139], v[138:139], 1.0 op_sel_hi:[1,0]\n" \
  "v_rcp_f32 v136, v136\n" \
  "v_rcp_f32 v137, v137\n" \
  "v_rcp_f32 v138, v138\n" \
  "s_waitcnt lgkmcnt(4)\n" \
  "v_smfmac_f32_16x16x64_bf16 v[52:55], v[180:183], v[100:107], v192\n" \
  "v_lshlrev_b32_sdwa v66, 4, v222 dst_sel:DWORD dst_unused:UNUSED_PAD src0_sel:DWORD src1_sel:WORD_0\n" \
  "ds_read_b128 v[76:79], v66\n" \
  "v_lshlrev_b32_sdwa v67, 4, v222 dst_sel:DWORD dst_unused:UNUSED_PAD src0_sel:DWORD src1_sel:WORD_1\n" \
  "ds_read_b128 v[80:83], v67\n" \
  "v_rcp_f32 v139, v139\n" \
  "v_pk_mul_f32 v[40:41], v[40:41], v[136:137]\n" \
  "v_pk_fma_f32 v[40:41], v[42:43], v[138:139], v[40:41]\n" \
  "v_add_f32 v40, v40, v41\n" \
  "v_pk_mul_f32 v[140:141], v[44:45], v[44:45]\n" \
  "v_pk_mul_f32 v[142:143], v[46:47], v[46:47]\n" \
  "v_pk_fma_f32 v[140:141], v[140:141], s[52:53], v[124:125]\n" \
  "v_pk_fma_f32 v[142:143], v[142:143], s[52:53], v[124:125]\n" \
  "v_pk_mul_f32 v[140:141], v[140:141], v[44:45]\n" \
  "v_pk_mul_f32 v[142:143], v[142:143], v[46:47]\n" \
  "v_exp_f32 v140, v140\n" \
  "v_exp_f32 v141, v141\n" \
  "v_pk_mul_f32 v[44:45], v[44:45], v[120:121]\n" \
  "v_exp_f32 v142, v142\n" \
  "v_exp_f32 v143, v143\n" \
  "v_pk_mul_f32 v[46:47], v[46:47], v[122:123]\n" \
  "s_waitcnt lgkmcnt(4)\n" \
  "v_smfmac_f32_16x16x64_bf16 v[52:55], v[180:183], v[108:115], v192\n" \
  "v_lshlrev_b32_sdwa v64, 4, v255 dst_sel:DWORD dst_unused:UNUSED_PAD src0_sel:DWORD src1_sel:WORD_0\n" \
  "ds_read_b128 v[84:87], v64\n" \
  "v_lshlrev_b32_sdwa v65, 4, v255 dst_sel:DWORD dst_unused:UNUSED_PAD src0_sel:DWORD src1_sel:WORD_1\n" \
  "ds_read_b128 v[88:91], v65\n" \
  "v_pk_add_f32 v[140:141], v[140:141], 1.0 op_sel_hi:[1,0]\n" \
  "v_pk_add_f32 v[142:143], v[142:143], 1.0 op_sel_hi:[1,0]\n" \
  "v_rcp_f32 v140, v140\n" \
  "v_rcp_f32 v141, v141\n" \
  "v_rcp_f32 v142, v142\n" \
  "v_rcp_f32 v143, v143\n" \
  "v_pk_mul_f32 v[44:45], v[44:45], v[140:141]\n" \
  "v_pk_fma_f32 v[44:45], v[46:47], v[142:143], v[44:45]\n" \
  "v_add_f32 v44, v44, v45\n" \
  "v_pk_mul_f32 v[128:129], v[48:49], v[48:49]\n" \
  "v_pk_mul_f32 v[130:131], v[50:51], v[50:51]\n" \
  "v_pk_fma_f32 v[128:129], v[128:129], s[52:53], v[124:125]\n" \
  "v_pk_fma_f32 v[130:131], v[130:131], s[52:53], v[124:125]\n" \
  "v_pk_mul_f32 v[128:129], v[128:129], v[48:49]\n" \
  "v_pk_mul_f32 v[130:131], v[130:131], v[50:51]\n" \
  "v_exp_f32 v128, v128\n" \
  "s_waitcnt lgkmcnt(4)\n" \
  "v_smfmac_f32_16x16x64_bf16 v[56:59], v[180:183], v[68:75], v192\n" \
  "v_lshlrev_b32_sdwa v66, 4, v223 dst_sel:DWORD dst_unused:UNUSED_PAD src0_sel:DWORD src1_sel:WORD_0\n" \
  "ds_read_b128 v[92:95], v66\n" \
  "v_lshlrev_b32_sdwa v67, 4, v223 dst_sel:DWORD dst_unused:UNUSED_PAD src0_sel:DWORD src1_sel:WORD_1\n" \
  "ds_read_b128 v[96:99], v67\n" \
  "v_exp_f32 v129, v129\n" \
  "v_pk_mul_f32 v[48:49], v[48:49], v[120:121]\n" \
  "v_exp_f32 v130, v130\n" \
  "v_exp_f32 v131, v131\n" \
  "v_pk_mul_f32 v[50:51], v[50:51], v[122:123]\n" \
  "v_pk_add_f32 v[128:129], v[128:129], 1.0 op_sel_hi:[1,0]\n" \
  "v_pk_add_f32 v[130:131], v[130:131], 1.0 op_sel_hi:[1,0]\n" \
  "v_rcp_f32 v128, v128\n" \
  "v_rcp_f32 v129, v129\n" \
  "v_rcp_f32 v130, v130\n" \
  "v_rcp_f32 v131, v131\n" \
  "v_pk_mul_f32 v[48:49], v[48:49], v[128:129]\n" \
  "v_pk_fma_f32 v[48:49], v[50:51], v[130:131], v[48:49]\n" \
  "v_add_f32 v48, v48, v49\n" \
  "v_pk_mul_f32 v[132:133], v[52:53], v[52:53]\n" \
  "v_pk_mul_f32 v[134:135], v[54:55], v[54:55]\n" \
  "s_waitcnt lgkmcnt(4)\n" \
  "v_smfmac_f32_16x16x64_bf16 v[56:59], v[180:183], v[76:83], v192\n" \
  "v_pk_fma_f32 v[132:133], v[132:133], s[52:53], v[124:125]\n" \
  "v_pk_fma_f32 v[134:135], v[134:135], s[52:53], v[124:125]\n" \
  "v_pk_mul_f32 v[132:133], v[132:133], v[52:53]\n" \
  "v_pk_mul_f32 v[134:135], v[134:135], v[54:55]\n" \
  "v_exp_f32 v132, v132\n" \
  "v_exp_f32 v133, v133\n" \
  "v_pk_mul_f32 v[52:53], v[52:53], v[120:121]\n" \
  "v_exp_f32 v134, v134\n" \
  "v_exp_f32 v135, v135\n" \
  "v_pk_mul_f32 v[54:55], v[54:55], v[122:123]\n" \
  "v_pk_add_f32 v[132:133], v[132:133], 1.0 op_sel_hi:[1,0]\n" \
  "v_pk_add_f32 v[134:135], v[134:135], 1.0 op_sel_hi:[1,0]\n" \
  "v_rcp_f32 v132, v132\n" \
  "v_rcp_f32 v133, v133\n" \
  "v_rcp_f32 v134, v134\n" \
  "v_rcp_f32 v135, v135\n" \
  "v_pk_mul_f32 v[52:53], v[52:53], v[132:133]\n" \
  "v_pk_fma_f32 v[52:53], v[54:55], v[134:135], v[52:53]\n" \
  "v_add_f32 v52, v52, v53\n" \
  "s_waitcnt lgkmcnt(2)\n" \
  "v_smfmac_f32_16x16x64_bf16 v[60:63], v[180:183], v[84:91], v192\n" \
  "v_pk_mul_f32 v[136:137], v[56:57], v[56:57]\n" \
  "v_pk_mul_f32 v[138:139], v[58:59], v[58:59]\n" \
  "v_pk_fma_f32 v[136:137], v[136:137], s[52:53], v[124:125]\n" \
  "v_pk_fma_f32 v[138:139], v[138:139], s[52:53], v[124:125]\n" \
  "v_pk_mul_f32 v[136:137], v[136:137], v[56:57]\n" \
  "v_pk_mul_f32 v[138:139], v[138:139], v[58:59]\n" \
  "v_exp_f32 v136, v136\n" \
  "v_exp_f32 v137, v137\n" \
  "v_pk_mul_f32 v[56:57], v[56:57], v[120:121]\n" \
  "v_exp_f32 v138, v138\n" \
  "v_exp_f32 v139, v139\n" \
  "v_pk_mul_f32 v[58:59], v[58:59], v[122:123]\n" \
  "v_pk_add_f32 v[136:137], v[136:137], 1.0 op_sel_hi:[1,0]\n" \
  "v_pk_add_f32 v[138:139], v[138:139], 1.0 op_sel_hi:[1,0]\n" \
  "v_rcp_f32 v136, v136\n" \
  "v_rcp_f32 v137, v137\n" \
  "v_rcp_f32 v138, v138\n" \
  "v_rcp_f32 v139, v139\n" \
  "v_pk_mul_f32 v[56:57], v[56:57], v[136:137]\n" \
  "v_pk_fma_f32 v[56:57], v[58:59], v[138:139], v[56:57]\n" \
  "v_add_f32 v56, v56, v57\n" \
  "s_waitcnt lgkmcnt(0)\n" \
  "v_smfmac_f32_16x16x64_bf16 v[60:63], v[180:183], v[92:99], v192\n" \
  "s_nop 7\n" \
  "v_pk_mul_f32 v[140:141], v[60:61], v[60:61]\n" \
  "v_pk_mul_f32 v[142:143], v[62:63], v[62:63]\n" \
  "v_pk_fma_f32 v[140:141], v[140:141], s[52:53], v[124:125]\n" \
  "v_pk_fma_f32 v[142:143], v[142:143], s[52:53], v[124:125]\n" \
  "v_pk_mul_f32 v[140:141], v[140:141], v[60:61]\n" \
  "v_pk_mul_f32 v[142:143], v[142:143], v[62:63]\n" \
  "v_exp_f32 v140, v140\n" \
  "v_exp_f32 v141, v141\n" \
  "v_pk_mul_f32 v[60:61], v[60:61], v[120:121]\n" \
  "v_exp_f32 v142, v142\n" \
  "v_exp_f32 v143, v143\n" \
  "v_pk_mul_f32 v[62:63], v[62:63], v[122:123]\n" \
  "v_pk_add_f32 v[140:141], v[140:141], 1.0 op_sel_hi:[1,0]\n" \
  "v_pk_add_f32 v[142:143], v[142:143], 1.0 op_sel_hi:[1,0]\n" \
  "v_rcp_f32 v140, v140\n" \
  "v_rcp_f32 v141, v141\n" \
  "v_rcp_f32 v142, v142\n" \
  "v_rcp_f32 v143, v143\n" \
  "v_pk_mul_f32 v[60:61], v[60:61], v[140:141]\n" \
  "v_pk_fma_f32 v[60:61], v[62:63], v[142:143], v[60:61]\n" \
  "v_add_f32 v60, v60, v61\n" \
  "v_permlane16_swap_b32 v32, v48\n" \
  "v_permlane16_swap_b32 v36, v52\n" \
  "v_permlane16_swap_b32 v40, v56\n" \
  "v_permlane16_swap_b32 v44, v60\n" \
  "v_add_f32 v148, v32, v48\n" \
  "v_add_f32 v149, v36, v52\n" \
  "v_add_f32 v150, v40, v56\n" \
  "v_add_f32 v151, v44, v60\n" \
  "v_min_u32 v148, -2, v148\n" \
  "v_min_u32 v149, -2, v149\n" \
  "v_min_u32 v150, -2, v150\n" \
  "v_min_u32 v151, -2, v151\n" \
  "global_store_dwordx4 v188, v[148:151], s[40:41] offset:1024 sc1\n" \
  "v_lshrrev_b32 v0, 4, v188\n" \
  "v_lshrrev_b32 v1, 4, v0\n" \
  "v_and_b32 v2, 15, v0\n" \
  "v_lshlrev_b32 v2, 4, v2\n" \
  "v_lshl_add_u32 v1, v1, 17, v2\n" \
  "s_lshl_b32 s35, s27, 19\n" \
  "s_lshl_b32 s36, s26, 8\n" \
  "s_add_u32 s35, s35, s36\n" \
  "s_add_u32 s35, s35, 105676800\n" \
  "s_add_u32 s40, s24, s35\n" \
  "s_addc_u32 s41, s25, 0\n" \
  "s_add_u32 s42, s40, 65536\n" \
  "s_addc_u32 s43, s41, 0\n" \
  "s_add_u32 s44, s24, 109871104\n" \
  "s_addc_u32 s45, s25, 0\n" \
  "s_add_u32 s46, s44, 65536\n" \
  "s_addc_u32 s47, s45, 0\n" \
  "s_lshr_b32 s35, s26, 2\n" \
  "s_lshl_b32 s35, s35, 8\n" \
  "s_and_b32 s36, s26, 1\n" \
  "s_lshl_b32 s36, s36, 7\n" \
  "s_add_u32 s35, s35, s36\n" \
  "s_and_b32 s36, s26, 2\n" \
  "s_lshl3_add_u32 s35, s36, s35\n" \
  "v_and_b32 v2, 3, v0\n" \
  "v_lshrrev_b32 v3, 2, v0\n" \
  "v_lshl_add_u32 v2, v2, 5, v3\n" \
  "v_add_u32 v2, s35, v2\n" \
  "v_lshlrev_b32 v3, 2, v2\n" \
  "s_cmp_eq_u32 s27, 0\n" \
  "s_cselect_b32 s35, -1, 0\n" \
  "v_and_b32 v3, s35, v3\n" \
  "v_mov_b32 v4, s6\n" \
  "s_mov_b32 s5, 0x10000\n" \
  "s_barrier\n" \
  "s_sleep 10\n" \
  "LF_poll:\n" \
  "global_load_dwordx4 v[8:11], v1, s[40:41] sc1\n" \
  "global_load_dwordx4 v[12:15], v1, s[42:43] sc1\n" \
  "global_load_dword v16, v3, s[44:45] sc1\n" \
  "global_load_dword v17, v3, s[46:47] sc1\n" \
  "s_waitcnt vmcnt(0)\n" \
  "v_max3_u32 v5, v8, v9, v10\n" \
  "v_max3_u32 v6, v11, v12, v13\n" \
  "v_max3_u32 v5, v5, v14, v15\n" \
  "v_max3_u32 v6, v6, v16, v17\n" \
  "v_max_u32 v5, v5, v6\n" \
  "v_cmp_eq_u32 vcc, -1, v5\n" \
  "s_sub_u32 s5, s5, 1\n" \
  "s_cbranch_scc1 LF_go\n" \
  "s_nop 2\n" \
  "s_cbranch_vccz LF_go\n" \
  "s_mov_b64 exec, vcc\n" \
  "s_sleep 2\n" \
  "s_branch LF_poll\n" \
  "LF_go:\n" \
  "s_mov_b64 exec, -1\n" \
  "v_pk_add_f32 v[8:9], v[8:9], v[12:13]\n" \
  "v_pk_add_f32 v[10:11], v[10:11], v[14:15]\n" \
  "s_nop 0\n" \
  "v_permlane16_swap_b32 v8, v9\n" \
  "v_permlane16_swap_b32 v10, v11\n" \
  "v_add_f32 v8, v8, v9\n" \
  "v_add_f32 v10, v10, v11\n" \
  "s_nop 1\n" \
  "v_permlane32_swap_b32 v8, v10\n" \
  "v_add_f32 v8, v8, v10\n" \
  "v_and_b32 v5, 15, v0\n" \
  "v_lshrrev_b32 v6, 4, v0\n" \
  "v_lshl_add_u32 v5, v5, 2, v6\n" \
  "s_lshl_b32 s35, s27, 8\n" \
  "v_lshl_add_u32 v5, v5, 2, s35\n" \
  "ds_write_b32 v5, v8\n" \
  "s_waitcnt lgkmcnt(0)\n" \
  "s_barrier\n" \
  "s_cmp_lg_u32 s27, 0\n" \
  "s_cbranch_scc1 LF_end\n" \
  "v_lshlrev_b32 v5, 2, v0\n" \
  "ds_read2st64_b32 v[8:9], v5 offset0:0 offset1:1\n" \
  "ds_read2st64_b32 v[10:11], v5 offset0:2 offset1:3\n" \
  "ds_read2st64_b32 v[12:13], v5 offset0:4 offset1:5\n" \
  "ds_read2st64_b32 v[14:15], v5 offset0:6 offset1:7\n" \
  "v_add_f32 v16, v16, v17\n" \
  "v_lshlrev_b32 v4, 16, v4\n" \
  "v_cvt_pk_bf16_f32 v16, v16, v16\n" \
  "v_lshlrev_b32 v2, 1, v2\n" \
  "s_waitcnt lgkmcnt(0)\n" \
  "v_pk_add_f32 v[8:9], v[8:9], v[10:11]\n" \
  "v_pk_add_f32 v[12:13], v[12:13], v[14:15]\n" \
  "v_pk_add_f32 v[8:9], v[8:9], v[12:13]\n" \
  "v_add_f32 v8, v8, v9\n" \
  "v_cvt_pk_bf16_f32 v8, v8, v8\n" \
  "v_lshlrev_b32 v8, 16, v8\n" \
  "v_add_f32 v8, v8, v4\n" \
  "v_cvt_pk_bf16_f32 v8, v8, v8\n" \
  "v_lshlrev_b32 v8, 16, v8\n" \
  "v_lshlrev_b32 v16, 16, v16\n" \
  "v_add_f32 v8, v8, v16\n" \
  "v_cvt_pk_bf16_f32 v8, v8, v8\n" \
  "global_store_short v2, v8, s[22:23]\n" \
  "LF_end:\n" \
  "s_branch LZ_end\n" \
  "LG_h:\n" \
  "s_cmp_eq_u32 s51, 4\n" \
  "s_cbranch_scc1 LG_chk\n" \
  "s_cmp_lg_u32 s51, 7\n" \
  "s_cbranch_scc1 LG_ret\n" \
  "s_barrier\n" \
  "s_mov_b32 exec_hi, 0\n" \
  "global_atomic_add v196, v199, s[48:49] offset:3076 sc1\n" \
  "s_mov_b64 exec, -1\n" \
  "s_branch LG_aft\n" \
  "LG_pol:\n" \
  "s_mov_b64 exec, 0xff\n" \
  "global_load_dword v200, v198, s[48:49] offset:3076 sc1\n" \
  "s_mov_b64 exec, -1\n" \
  "s_branch LG_rt2\n" \
  "LG_chk:\n" \
  "s_mov_b32 s5, 0x10000\n" \
  "LG_p:\n" \
  "s_mov_b64 exec, 0xff\n" \
  "v_cmp_le_u32 vcc, 32, v200\n" \
  "s_mov_b64 exec, -1\n" \
  "s_sub_u32 s5, s5, 1\n" \
  "s_cselect_b32 s35, 0xff, vcc_lo\n" \
  "s_cmp_eq_u32 s35, 0xff\n" \
  "s_cbranch_scc1 LG_p_x\n" \
  "s_sleep 2\n" \
  "s_mov_b64 exec, 0xff\n" \
  "global_load_dword v200, v198, s[48:49] offset:3076 sc1\n" \
  "s_mov_b64 exec, -1\n" \
  "s_waitcnt vmcnt(0)\n" \
  "s_branch LG_p\n" \
  "LG_p_x:\n" \
  "s_branch LG_ret\n" \
  "LZ_end:\n" \
  "s_waitcnt vmcnt(0) lgkmcnt(0)\n"

#define R10(p, b) p #b "0", p #b "1", p #b "2", p #b "3", p #b "4", p #b "5", p #b "6", p #b "7", p #b "8", p #b "9"
__global__ __launch_bounds__(512, 2) void k1(const int* __restrict__ idx, const unsigned short* __restrict__ table,
                                             const unsigned short* __restrict__ w_p, const unsigned short* __restrict__ w1,
                                             const unsigned short* __restrict__ b1, const unsigned short* __restrict__ w2,
                                             const unsigned short* __restrict__ b2, __hip_bfloat16* __restrict__ out, char* __restrict__ ws) {
  __shared__ __attribute__((aligned(1024))) char smem[163840];
  unsigned tid = threadIdx.x;
  asm volatile(K_ASM
               : "+{v0}"(tid)
               : "{s[0:1]}"(__builtin_amdgcn_kernarg_segment_ptr()), "{s26}"(blockIdx.x), "s"((unsigned)(uintptr_t)smem)
               : "memory", "scc", "vcc", "s3", "s4", "s5", "s6", "s7", "s8", "s9", R10("s", 1), "s20", "s21", "s22", "s23", "s24", "s25", "s27", "s28", "s29", "s30", "s31", "s33", "s34", "s35", "s36", "s37", "s38", "s39", R10("s", 4), R10("s", 5), R10("s", 6), R10("s", 7), R10("s", 8), R10("s", 9),
                 "v1", "v2", "v3", "v4", "v5", "v6", "v7", "v8", "v9", R10("v", 1), R10("v", 2), R10("v", 3), R10("v", 4), R10("v", 5), R10("v", 6), R10("v", 7), R10("v", 8), R10("v", 9),
                 R10("v", 10), R10("v", 11), R10("v", 12), R10("v", 13), R10("v", 14), R10("v", 15), R10("v", 16), R10("v", 17), R10("v", 18), R10("v", 19),
                 R10("v", 20), R10("v", 21), R10("v", 22), R10("v", 23), R10("v", 24), "v250", "v251", "v252", "v253", "v254", "v255");
}

extern "C" void kernel_launch(void* const* d_in, const int* in_sizes, int n_in, void* d_out, int out_size, void* d_ws,
                              size_t ws_size, hipStream_t stream) {
  k1<<<dim3(256), dim3(512), 0, stream>>>((const int*)d_in[0], (const unsigned short*)d_in[1], (const unsigned short*)d_in[2], (const unsigned short*)d_in[3],
                                          (const unsigned short*)d_in[4], (const unsigned short*)d_in[5], (const unsigned short*)d_in[6], (__hip_bfloat16*)d_out, (char*)d_ws);
}
```

```cpp
#include <hip/hip_runtime.h>
#include <hip/hip_bf16.h>
#include <stdint.h>

#define K_ASM \
  "LZ_first:\n" \
  "s_load_dwordx16 s[8:23], s[0:1], 0x0\n" \
  "s_load_dwordx2 s[24:25], s[0:1], 0x40\n" \
  "v_readfirstlane_b32 s27, v0\n" \
  "v_and_b32 v255, 63, v0\n" \
  "s_and_b32 s29, s26, 7\n" \
  "s_lshr_b32 s30, s26, 3\n" \
  "s_lshr_b32 s27, s27, 6\n" \
  "s_and_b32 s31, s30, 1\n" \
  "s_and_b32 s28, s27, 3\n" \
  "s_lshr_b32 s35, s30, 1\n" \
  "s_lshl3_add_u32 s7, s35, s29\n" \
  "v_lshrrev_b32 v1, 3, v255\n" \
  "v_and_b32 v2, 7, v255\n" \
  "v_lshlrev_b32 v1, 8, v1\n" \
  "v_lshl_or_b32 v254, v2, 4, v1\n" \
  "s_mul_i32 s36, s7, 37\n" \
  "s_and_b32 s36, s36, 63\n" \
  "s_add_u32 s36, s36, s28\n" \
  "s_cmp_ge_u32 s27, 4\n" \
  "s_cbranch_scc1 LB_start\n" \
  "s_setprio 3\n" \
  "v_and_b32 v252, 15, v255\n" \
  "v_lshrrev_b32 v253, 4, v255\n" \
  "v_mul_u32_u24 v2, 400, v253\n" \
  "v_min_u32 v3, 8, v252\n" \
  "v_lshl_add_u32 v250, v252, 4, v2\n" \
  "v_lshl_add_u32 v251, v3, 4, v2\n" \
  "s_lshl2_add_u32 s36, s7, s28\n" \
  "s_mul_i32 s36, s36, 12800\n" \
  "s_lshl_b32 s35, s28, 10\n" \
  "v_lshl_add_u32 v1, v255, 4, s35\n" \
  "s_add_u32 m0, s35, 138240\n" \
  "s_waitcnt lgkmcnt(0)\n" \
  "s_add_u32 s44, s8, s36\n" \
  "s_addc_u32 s45, s9, 0\n" \
  "global_load_lds_dwordx4 v1, s[12:13]\n" \
  "s_add_u32 m0, m0, 0x1000\n" \
  "v_add_u32 v1, 0x1000, v1\n" \
  "global_load_lds_dwordx4 v1, s[12:13]\n" \
  "s_add_u32 m0, m0, 0x1000\n" \
  "v_add_u32 v1, 0x1000, v1\n" \
  "global_load_lds_dwordx4 v1, s[12:13]\n" \
  "s_add_u32 m0, m0, 0x1000\n" \
  "v_add_u32 v1, 0x1000, v1\n" \
  "global_load_lds_dwordx4 v1, s[12:13]\n" \
  "s_add_u32 m0, m0, 0x1000\n" \
  "v_add_u32 v1, 0x1000, v1\n" \
  "v_min_u32 v1, 19984, v1\n" \
  "global_load_lds_dwordx4 v1, s[12:13]\n" \
  "global_load_dwordx4 v[232:235], v250, s[44:45] offset:0\n" \
  "global_load_dwordx4 v[236:239], v251, s[44:45] offset:256\n" \
  "global_load_dwordx4 v[240:243], v250, s[44:45] offset:1600\n" \
  "global_load_dwordx4 v[244:247], v251, s[44:45] offset:1856\n" \
  "s_setprio 1\n" \
  "v_lshrrev_b32 v1, 2, v252\n" \
  "v_and_b32 v2, 3, v252\n" \
  "v_cmp_eq_u32 vcc, v1, v253\n" \
  "v_mov_b32 v4, 0x3f80\n" \
  "v_and_b32 v13, 1, v253\n" \
  "v_lshlrev_b32 v14, 1, v13\n" \
  "v_xor_b32 v2, v2, v14\n" \
  "v_cndmask_b32 v2, 4, v2, vcc\n" \
  "v_cmp_eq_u32 vcc, 0, v2\n" \
  "s_nop 1\n" \
  "v_cndmask_b32 v216, 0, v4, vcc\n" \
  "v_lshlrev_b32 v220, 16, v216\n" \
  "v_cmp_eq_u32 vcc, 1, v2\n" \
  "s_nop 1\n" \
  "v_cndmask_b32 v217, 0, v4, vcc\n" \
  "v_lshlrev_b32 v221, 16, v217\n" \
  "v_cmp_eq_u32 vcc, 2, v2\n" \
  "s_nop 1\n" \
  "v_cndmask_b32 v218, 0, v4, vcc\n" \
  "v_lshlrev_b32 v222, 16, v218\n" \
  "v_cmp_eq_u32 vcc, 3, v2\n" \
  "s_nop 1\n" \
  "v_cndmask_b32 v219, 0, v4, vcc\n" \
  "v_lshlrev_b32 v223, 16, v219\n" \
  "v_and_b32 v1, 2, v252\n" \
  "v_lshlrev_b32 v1, 3, v1\n" \
  "v_lshlrev_b32 v1, v1, v4\n" \
  "v_and_b32 v2, 0x31, v255\n" \
  "v_mov_b64 v[224:225], 0\n" \
  "v_cmp_eq_u32 vcc, 0, v2\n" \
  "s_nop 1\n" \
  "v_cndmask_b32 v226, 0, v1, vcc\n" \
  "v_cmp_eq_u32 vcc, 1, v2\n" \
  "s_nop 1\n" \
  "v_cndmask_b32 v227, 0, v1, vcc\n" \
  "v_mul_u32_u24 v5, 400, v252\n" \
  "v_add_u32 v5, 138240, v5\n" \
  "v_lshl_add_u32 v6, v253, 4, v5\n" \
  "v_add_u32 v7, 6400, v5\n" \
  "v_add_u32 v8, 6400, v6\n" \
  "v_add_u32 v9, 12800, v5\n" \
  "v_add_u32 v10, 12800, v6\n" \
  "v_bfe_u32 v1, v252, 1, 1\n" \
  "v_and_b32 v2, 1, v252\n" \
  "v_lshl_add_u32 v1, v2, 1, v1\n" \
  "v_mul_u32_u24 v1, 200, v1\n" \
  "v_add_u32 v11, 157440, v1\n" \
  "v_lshl_add_u32 v12, v253, 4, v11\n" \
  "v_lshlrev_b32 v13, 3, v13\n" \
  "v_sub_u32 v16, v5, v13\n" \
  "v_add_u32 v5, v5, v13\n" \
  "v_sub_u32 v17, v6, v13\n" \
  "v_add_u32 v6, v6, v13\n" \
  "v_sub_u32 v18, v7, v13\n" \
  "v_add_u32 v7, v7, v13\n" \
  "v_sub_u32 v19, v8, v13\n" \
  "v_add_u32 v8, v8, v13\n" \
  "v_sub_u32 v20, v9, v13\n" \
  "v_add_u32 v9, v9, v13\n" \
  "v_sub_u32 v21, v10, v13\n" \
  "v_add_u32 v10, v10, v13\n" \
  "v_sub_u32 v22, v11, v13\n" \
  "v_add_u32 v11, v11, v13\n" \
  "v_sub_u32 v23, v12, v13\n" \
  "v_add_u32 v12, v12, v13\n" \
  "v_lshlrev_b32 v188, 4, v253\n" \
  "v_and_b32 v96, 4, v252\n" \
  "v_lshl_add_u32 v96, v253, 5, v96\n" \
  "v_mov_b32 v191, 0x40000000\n" \
  "v_cmp_gt_u32 vcc, 8, v252\n" \
  "v_mov_b32 v193, 0x4444\n" \
  "v_mov_b32 v98, 0xeeee\n" \
  "v_cndmask_b32 v189, v191, v96, vcc\n" \
  "v_cndmask_b32 v190, v96, v191, vcc\n" \
  "v_and_b32 v99, 4, v252\n" \
  "v_cmp_ne_u32 vcc, 0, v99\n" \
  "s_lshl_b32 s35, s28, 11\n" \
  "s_add_u32 s35, s35, 130048\n" \
  "v_cndmask_b32 v193, v193, v98, vcc\n" \
  "v_lshl_add_u32 v194, v252, 4, s35\n" \
  "v_and_b32 v96, 7, v252\n" \
  "v_lshlrev_b32 v96, 5, v96\n" \
  "v_lshl_add_u32 v96, v253, 8, v96\n" \
  "v_lshrrev_b32 v97, 3, v252\n" \
  "v_lshl_add_u32 v96, v97, 2, v96\n" \
  "v_add_u32 v195, s35, v96\n" \
  "v_and_b32 v97, v97, v252\n" \
  "v_lshlrev_b32 v192, 4, v97\n" \
  "s_waitcnt vmcnt(4)\n" \
  "s_barrier\n" \
  "ds_read_b64 v[200:201], v12 offset:128\n" \
  "ds_read_b64 v[202:203], v23 offset:136\n" \
  "ds_read_b64 v[184:185], v9 offset:184\n" \
  "ds_read_b64 v[186:187], v20 offset:192\n" \
  "ds_read_b64 v[212:213], v9 offset:384\n" \
  "ds_read_b64 v[214:215], v20 offset:392\n" \
  "ds_read_b64 v[196:197], v12 offset:64\n" \
  "ds_read_b64 v[198:199], v23 offset:72\n" \
  "ds_read_b64 v[176:177], v7 offset:184\n" \
  "ds_read_b64 v[178:179], v18 offset:192\n" \
  "ds_read_b64 v[180:181], v7 offset:384\n" \
  "ds_read_b64 v[182:183], v18 offset:392\n" \
  "ds_read_b64 v[228:229], v12 offset:0\n" \
  "ds_read_b64 v[230:231], v23 offset:8\n" \
  "s_waitcnt lgkmcnt(12)\n" \
  "v_mfma_f32_16x16x32_bf16 v[44:47], v[200:203], v[216:219], 0\n" \
  "ds_read_b64 v[168:169], v5 offset:184\n" \
  "ds_read_b64 v[170:171], v16 offset:192\n" \
  "s_waitcnt lgkmcnt(12)\n" \
  "v_mfma_f32_16x16x32_bf16 v[44:47], v[224:227], v[184:187], v[44:47]\n" \
  "ds_read_b64 v[172:173], v5 offset:384\n" \
  "ds_read_b64 v[174:175], v16 offset:392\n" \
  "v_mfma_f32_16x16x32_bf16 v[208:211], v[200:203], v[220:223], 0\n" \
  "ds_read_b64 v[96:97], v6 offset:0\n" \
  "ds_read_b64 v[98:99], v17 offset:8\n" \
  "s_waitcnt lgkmcnt(14)\n" \
  "v_mfma_f32_16x16x32_bf16 v[208:211], v[224:227], v[212:215], v[208:211]\n" \
  "ds_read_b64 v[124:125], v8 offset:64\n" \
  "ds_read_b64 v[126:127], v19 offset:72\n" \
  "s_waitcnt lgkmcnt(14)\n" \
  "v_mfma_f32_16x16x32_bf16 v[60:63], v[196:199], v[216:219], 0\n" \
  "ds_read_b64 v[136:137], v8 offset:264\n" \
  "ds_read_b64 v[138:139], v19 offset:272\n" \
  "s_waitcnt lgkmcnt(14)\n" \
  "v_mfma_f32_16x16x32_bf16 v[60:63], v[224:227], v[176:179], v[60:63]\n" \
  "ds_read_b64 v[140:141], v8 offset:328\n" \
  "ds_read_b64 v[142:143], v19 offset:336\n" \
  "v_mfma_f32_16x16x32_bf16 v[184:187], v[196:199], v[220:223], 0\n" \
  "ds_read_b64 v[160:161], v10 offset:264\n" \
  "ds_read_b64 v[162:163], v21 offset:272\n" \
  "s_waitcnt lgkmcnt(15)\n" \
  "v_mfma_f32_16x16x32_bf16 v[184:187], v[224:227], v[180:183], v[184:187]\n" \
  "ds_read_b64 v[116:117], v6 offset:328\n" \
  "ds_read_b64 v[118:119], v17 offset:336\n" \
  "s_waitcnt lgkmcnt(15)\n" \
  "v_mfma_f32_16x16x32_bf16 v[68:71], v[228:231], v[216:219], 0\n" \
  "ds_read_b64 v[144:145], v10 offset:0\n" \
  "ds_read_b64 v[146:147], v21 offset:8\n" \
  "s_waitcnt lgkmcnt(15)\n" \
  "v_mfma_f32_16x16x32_bf16 v[68:71], v[224:227], v[168:171], v[68:71]\n" \
  "ds_read_b64 v[148:149], v10 offset:64\n" \
  "ds_read_b64 v[150:151], v21 offset:72\n" \
  "v_mfma_f32_16x16x32_bf16 v[212:215], v[228:231], v[220:223], 0\n" \
  "ds_read_b64 v[104:105], v6 offset:128\n" \
  "ds_read_b64 v[106:107], v17 offset:136\n" \
  "s_waitcnt lgkmcnt(15)\n" \
  "v_mfma_f32_16x16x32_bf16 v[212:215], v[224:227], v[172:175], v[212:215]\n" \
  "ds_read_b64 v[100:101], v6 offset:64\n" \
  "ds_read_b64 v[102:103], v17 offset:72\n" \
  "v_mfma_f32_16x16x32_bf16 v[92:95], v[96:99], v[216:219], 0\n" \
  "ds_read_b64 v[120:121], v8 offset:0\n" \
  "ds_read_b64 v[122:123], v19 offset:8\n" \
  "s_waitcnt lgkmcnt(15)\n" \
  "v_mfma_f32_16x16x32_bf16 v[24:27], v[124:127], v[220:223], 0\n" \
  "ds_read_b64 v[112:113], v6 offset:264\n" \
  "ds_read_b64 v[114:115], v17 offset:272\n" \
  "v_mfma_f32_16x16x32_bf16 v[24:27], v[216:219], v[136:139], v[24:27]\n" \
  "ds_read_b64 v[156:157], v10 offset:200\n" \
  "ds_read_b64 v[158:159], v21 offset:208\n" \
  "s_waitcnt lgkmcnt(15)\n" \
  "v_mfma_f32_16x16x32_bf16 v[28:31], v[140:143], v[220:223], 0\n" \
  "ds_read_b64 v[132:133], v8 offset:200\n" \
  "ds_read_b64 v[134:135], v19 offset:208\n" \
  "v_mfma_f32_16x16x32_bf16 v[28:31], v[220:223], v[160:163], v[28:31]\n" \
  "ds_read_b64 v[108:109], v6 offset:200\n" \
  "ds_read_b64 v[110:111], v17 offset:208\n" \
  "s_waitcnt lgkmcnt(15)\n" \
  "v_mfma_f32_16x16x32_bf16 v[32:35], v[116:119], v[216:219], 0\n" \
  "ds_read_b64 v[152:153], v10 offset:128\n" \
  "ds_read_b64 v[154:155], v21 offset:136\n" \
  "v_mfma_f32_16x16x32_bf16 v[32:35], v[220:223], v[144:147], v[32:35]\n" \
  "ds_read_b64 v[164:165], v10 offset:328\n" \
  "ds_read_b64 v[166:167], v21 offset:336\n" \
  "v_mfma_f32_16x16x32_bf16 v[36:39], v[140:143], v[216:219], 0\n" \
  "ds_read_b64 v[128:129], v8 offset:128\n" \
  "ds_read_b64 v[130:131], v19 offset:136\n" \
  "s_waitcnt lgkmcnt(15)\n" \
  "v_mfma_f32_16x16x32_bf16 v[36:39], v[220:223], v[148:151], v[36:39]\n" \
  "ds_read_b64 v[204:205], v11 offset:184\n" \
  "ds_read_b64 v[206:207], v22 offset:192\n" \
  "v_mfma_f32_16x16x32_bf16 v[40:43], v[104:107], v[216:219], 0\n" \
  "v_mfma_f32_16x16x32_bf16 v[40:43], v[216:219], v[144:147], v[40:43]\n" \
  "v_mfma_f32_16x16x32_bf16 v[48:51], v[100:103], v[216:219], 0\n" \
  "s_waitcnt lgkmcnt(15)\n" \
  "v_mfma_f32_16x16x32_bf16 v[48:51], v[216:219], v[120:123], v[48:51]\n" \
  "s_waitcnt lgkmcnt(14)\n" \
  "v_mfma_f32_16x16x32_bf16 v[52:55], v[112:115], v[216:219], 0\n" \
  "v_mfma_f32_16x16x32_bf16 v[52:55], v[220:223], v[120:123], v[52:55]\n" \
  "v_mfma_f32_16x16x32_bf16 v[56:59], v[116:119], v[220:223], 0\n" \
  "s_waitcnt lgkmcnt(12)\n" \
  "v_mfma_f32_16x16x32_bf16 v[56:59], v[220:223], v[156:159], v[56:59]\n" \
  "s_waitcnt vmcnt(0) lgkmcnt(0)\n" \
  "v_mfma_f32_16x16x32_bf16 v[64:67], v[104:107], v[220:223], 0\n" \
  "v_cvt_pk_u16_u32 v232, v232, v233\n" \
  "v_cvt_pk_u16_u32 v234, v234, v235\n" \
  "v_mfma_f32_16x16x32_bf16 v[64:67], v[216:219], v[156:159], v[64:67]\n" \
  "v_cvt_pk_u16_u32 v236, v236, v237\n" \
  "v_cvt_pk_u16_u32 v238, v238, v239\n" \
  "v_mfma_f32_16x16x32_bf16 v[72:75], v[136:139], v[220:223], 0\n" \
  "v_pk_lshrrev_b16 v233, 3, v232 op_sel_hi:[0,1]\n" \
  "v_pk_lshrrev_b16 v235, 3, v234 op_sel_hi:[0,1]\n" \
  "v_mfma_f32_16x16x32_bf16 v[76:79], v[112:115], v[220:223], 0\n" \
  "v_pk_lshrrev_b16 v237, 3, v236 op_sel_hi:[0,1]\n" \
  "v_pk_lshrrev_b16 v239, 3, v238 op_sel_hi:[0,1]\n" \
  "v_mfma_f32_16x16x32_bf16 v[76:79], v[220:223], v[132:135], v[76:79]\n" \
  "v_pk_mad_u16 v180, v232, 8, v233 op_sel_hi:[1,0,1]\n" \
  "v_pk_mad_u16 v181, v234, 8, v235 op_sel_hi:[1,0,1]\n" \
  "v_mfma_f32_16x16x32_bf16 v[80:83], v[100:103], v[220:223], 0\n" \
  "v_pk_mad_u16 v182, v236, 8, v237 op_sel_hi:[1,0,1]\n" \
  "v_pk_mad_u16 v183, v238, 8, v239 op_sel_hi:[1,0,1]\n" \
  "v_mfma_f32_16x16x32_bf16 v[80:83], v[216:219], v[132:135], v[80:83]\n" \
  "v_alignbit_b32 v182, v182, v182, v192\n" \
  "v_alignbit_b32 v183, v183, v183, v192\n" \
  "v_mfma_f32_16x16x32_bf16 v[84:87], v[96:99], v[220:223], 0\n" \
  "v_cvt_pk_u16_u32 v240, v240, v241\n" \
  "v_cvt_pk_u16_u32 v242, v242, v243\n" \
  "v_mfma_f32_16x16x32_bf16 v[84:87], v[216:219], v[108:111], v[84:87]\n" \
  "v_cvt_pk_u16_u32 v244, v244, v245\n" \
  "v_cvt_pk_u16_u32 v246, v246, v247\n" \
  "v_mfma_f32_16x16x32_bf16 v[88:91], v[108:111], v[220:223], 0\n" \
  "v_pk_lshrrev_b16 v241, 3, v240 op_sel_hi:[0,1]\n" \
  "v_pk_lshrrev_b16 v243, 3, v242 op_sel_hi:[0,1]\n" \
  "v_mfma_f32_16x16x32_bf16 v[0:3], v[124:127], v[216:219], 0\n" \
  "v_pk_lshrrev_b16 v245, 3, v244 op_sel_hi:[0,1]\n" \
  "v_pk_lshrrev_b16 v247, 3, v246 op_sel_hi:[0,1]\n" \
  "v_mfma_f32_16x16x32_bf16 v[4:7], v[152:155], v[216:219], 0\n" \
  "v_pk_mad_u16 v196, v240, 8, v241 op_sel_hi:[1,0,1]\n" \
  "v_pk_mad_u16 v197, v242, 8, v243 op_sel_hi:[1,0,1]\n" \
  "v_mfma_f32_16x16x32_bf16 v[8:11], v[152:155], v[220:223], 0\n" \
  "v_pk_mad_u16 v198, v244, 8, v245 op_sel_hi:[1,0,1]\n" \
  "v_pk_mad_u16 v199, v246, 8, v247 op_sel_hi:[1,0,1]\n" \
  "v_mfma_f32_16x16x32_bf16 v[8:11], v[216:219], v[164:167], v[8:11]\n" \
  "v_alignbit_b32 v198, v198, v198, v192\n" \
  "v_alignbit_b32 v199, v199, v199, v192\n" \
  "v_mfma_f32_16x16x32_bf16 v[12:15], v[164:167], v[220:223], 0\n" \
  "v_add_u32 v200, 0x400, v195\n" \
  "v_mfma_f32_16x16x32_bf16 v[16:19], v[128:131], v[220:223], 0\n" \
  "v_mfma_f32_16x16x32_bf16 v[16:19], v[216:219], v[160:163], v[16:19]\n" \
  "v_mfma_f32_16x16x32_bf16 v[20:23], v[128:131], v[216:219], 0\n" \
  "v_mfma_f32_16x16x32_bf16 v[20:23], v[216:219], v[148:151], v[20:23]\n" \
  "v_mfma_f32_16x16x32_bf16 v[168:171], v[216:219], v[96:99], 0\n" \
  "v_mfma_f32_16x16x32_bf16 v[172:175], v[204:207], v[224:227], 0\n" \
  "v_mfma_f32_16x16x32_bf16 v[176:179], v[224:227], v[204:207], 0\n" \
  "s_mov_b32 exec_lo, 0x0\n" \
  "s_mov_b32 exec_hi, 0xffffffff\n" \
  "v_mov_b64 v[44:45], v[208:209]\n" \
  "v_mov_b64 v[46:47], v[210:211]\n" \
  "v_mov_b64 v[60:61], v[184:185]\n" \
  "v_mov_b64 v[62:63], v[186:187]\n" \
  "v_mov_b64 v[68:69], v[212:213]\n" \
  "v_mov_b64 v[70:71], v[214:215]\n" \
  "s_mov_b32 exec_lo, 0xff00ff00\n" \
  "s_mov_b32 exec_hi, 0x0\n" \
  "v_add_f32 v92, v92, v168\n" \
  "v_add_f32 v93, v93, v169\n" \
  "v_add_f32 v94, v94, v170\n" \
  "v_add_f32 v95, v95, v171\n" \
  "s_mov_b32 exec_lo, 0xf0000f00\n" \
  "s_mov_b32 exec_hi, 0x0\n" \
  "v_sub_f32 v92, v92, v176\n" \
  "v_sub_f32 v93, v93, v177\n" \
  "v_sub_f32 v94, v94, v178\n" \
  "v_sub_f32 v95, v95, v179\n" \
  "s_mov_b32 exec_lo, 0x0\n" \
  "s_mov_b32 exec_hi, 0xff00ff\n" \
  "v_mov_b64 v[92:93], 0\n" \
  "v_mov_b64 v[94:95], 0\n" \
  "s_mov_b32 exec_lo, 0x0\n" \
  "s_mov_b32 exec_hi, 0xf0000f\n" \
  "v_mov_b64 v[92:93], v[172:173]\n" \
  "v_mov_b64 v[94:95], v[174:175]\n" \
  "s_mov_b64 exec, -1\n" \
  "v_add_u32 v96, 6400, v250\n" \
  "v_add_u32 v97, 6400, v251\n" \
  "v_mov_b32 v98, 163328\n" \
  "v_mov_b32 v99, 1\n" \
  "v_lshlrev_b32 v179, 4, v253\n" \
  "s_lshr_b32 s35, s30, 4\n" \
  "s_lshl1_add_u32 s35, s29, s35\n" \
  "s_lshl_b32 s35, s35, 6\n" \
  "s_bfe_u32 s37, s30, 0x20002\n" \
  "s_lshl3_add_u32 s35, s37, s35\n" \
  "v_and_b32 v179, 16, v179\n" \
  "v_lshl_add_u32 v179, v179, 1, s35\n" \
  "s_mov_b64 exec, 1\n" \
  "ds_add_u32 v98, v99 offset:16\n" \
  "s_mov_b64 exec, -1\n" \
  "ds_write2_b32 v195, v180, v181 offset0:0 offset1:4\n" \
  "ds_write2_b32 v195, v182, v183 offset0:2 offset1:6\n" \
  "ds_write2_b32 v200, v196, v197 offset0:0 offset1:4\n" \
  "ds_write2_b32 v200, v198, v199 offset0:2 offset1:6\n" \
  "global_load_dwordx2 v[204:205], v179, s[16:17]\n" \
  "global_load_dwordx2 v[206:207], v179, s[18:19]\n" \
  "v_mov_b32 v179, 0\n" \
  "global_load_ushort v179, v179, s[20:21]\n" \
  "s_add_u32 s46, s44, 4800\n" \
  "s_addc_u32 s47, s45, 0\n" \
  "global_load_dwordx4 v[208:211], v96, s[44:45] offset:-3200\n" \
  "global_load_dwordx4 v[212:215], v97, s[44:45] offset:-2944\n" \
  "global_load_dwordx4 v[216:219], v96, s[44:45] offset:-1600\n" \
  "global_load_dwordx4 v[220:223], v97, s[44:45] offset:-1344\n" \
  "global_load_dwordx4 v[224:227], v96, s[44:45] offset:0\n" \
  "global_load_dwordx4 v[228:231], v97, s[44:45] offset:256\n" \
  "global_load_dwordx4 v[232:235], v96, s[44:45] offset:1600\n" \
  "global_load_dwordx4 v[236:239], v97, s[44:45] offset:1856\n" \
  "global_load_dwordx4 v[240:243], v96, s[44:45] offset:3200\n" \
  "global_load_dwordx4 v[244:247], v97, s[44:45] offset:3456\n" \
  "global_load_dwordx4 v[248:251], v96, s[46:47] offset:0\n" \
  "global_load_dwordx4 v[252:255], v97, s[46:47] offset:256\n" \
  "ds_read_b128 v[180:183], v194\n" \
  "v_add_u32 v194, 0x100, v194\n" \
  "L1_:\n" \
  "ds_read_b32 v99, v98 offset:12\n" \
  "s_waitcnt lgkmcnt(0)\n" \
  "v_readfirstlane_b32 s35, v99\n" \
  "s_cmp_lt_u32 s35, 4\n" \
  "s_cbranch_scc1 L1_\n" \
  "s_setprio 1\n" \
  "v_mad_u32_u16 v184, v182, 16, v188\n" \
  "ds_read_b128 v[128:131], v184 offset:0\n" \
  "ds_read_b128 v[132:135], v184 offset:64\n" \
  "ds_read_b128 v[172:175], v191\n" \
  "v_mad_u32_u16 v185, v181, 16, v188\n" \
  "v_mad_u32_u16 v186, v182, 16, v188 op_sel:[1,0,0,0]\n" \
  "ds_read_b128 v[112:115], v185 offset:0\n" \
  "ds_read_b128 v[116:119], v185 offset:64\n" \
  "ds_read_b128 v[136:139], v186 offset:0\n" \
  "ds_read_b128 v[140:143], v186 offset:64\n" \
  "ds_read_b128 v[168:171], v191\n" \
  "v_mad_u32_u16 v187, v181, 16, v188 op_sel:[1,0,0,0]\n" \
  "v_mad_u32_u16 v184, v180, 16, v188 op_sel:[1,0,0,0]\n" \
  "ds_read_b128 v[120:123], v187 offset:0\n" \
  "v_mad_u32_u16 v185, v180, 16, v188\n" \
  "ds_read_b128 v[124:127], v187 offset:64\n" \
  "ds_read_b128 v[104:107], v184 offset:0\n" \
  "v_mad_u32_u16 v186, v183, 16, v189\n" \
  "ds_read_b128 v[108:111], v184 offset:64\n" \
  "ds_read_b128 v[96:99], v185 offset:0\n" \
  "s_waitcnt lgkmcnt(9)\n" \
  "s_mov_b32 s51, 3\n" \
  "s_mov_b32 s50, 8\n" \
  "s_call_b64 s[52:53], LA_loop\n" \
  "LA_ev0:\n" \
  "s_waitcnt vmcnt(10)\n" \
  "v_cvt_pk_u16_u32 v208, v208, v209\n" \
  "v_cvt_pk_u16_u32 v210, v210, v211\n" \
  "v_cvt_pk_u16_u32 v212, v212, v213\n" \
  "v_cvt_pk_u16_u32 v214, v214, v215\n" \
  "v_pk_lshrrev_b16 v209, 3, v208 op_sel_hi:[0,1]\n" \
  "v_pk_lshrrev_b16 v211, 3, v210 op_sel_hi:[0,1]\n" \
  "v_pk_lshrrev_b16 v213, 3, v212 op_sel_hi:[0,1]\n" \
  "v_pk_lshrrev_b16 v215, 3, v214 op_sel_hi:[0,1]\n" \
  "v_pk_mad_u16 v208, v208, 8, v209 op_sel_hi:[1,0,1]\n" \
  "v_pk_mad_u16 v210, v210, 8, v211 op_sel_hi:[1,0,1]\n" \
  "v_pk_mad_u16 v212, v212, 8, v213 op_sel_hi:[1,0,1]\n" \
  "v_pk_mad_u16 v214, v214, 8, v215 op_sel_hi:[1,0,1]\n" \
  "v_alignbit_b32 v212, v212, v212, v192\n" \
  "v_alignbit_b32 v214, v214, v214, v192\n" \
  "ds_write2_b32 v195, v208, v210 offset0:0 offset1:4\n" \
  "ds_write2_b32 v195, v212, v214 offset0:2 offset1:6\n" \
  "v_mov_b32 v196, v178\n" \
  "v_readfirstlane_b32 s6, v179\n" \
  "v_mov_b32 v208, 163328\n" \
  "s_mov_b32 s51, 4\n" \
  "s_mov_b32 s50, 1\n" \
  "s_mov_b32 s38, 0x800\n" \
  "s_call_b64 s[52:53], LA_loop\n" \
  "LA_ev6:\n" \
  "v_mov_b32 v202, v178\n" \
  "v_readfirstlane_b32 s35, v209\n" \
  "s_cmp_ge_u32 s35, 128\n" \
  "s_cbranch_scc1 LA_pl\n" \
  "L2_:\n" \
  "ds_read_b32 v209, v208 offset:4\n" \
  "s_waitcnt lgkmcnt(0)\n" \
  "v_readfirstlane_b32 s35, v209\n" \
  "s_cmp_lt_u32 s35, 128\n" \
  "s_cbranch_scc1 L2_\n" \
  "LA_pl:\n" \
  "s_and_b32 s4, s35, 15\n" \
  "s_sub_u32 s4, s4, 4\n" \
  "s_bfe_u32 s5, s35, 0x30004\n" \
  "s_sub_u32 s5, s5, s29\n" \
  "s_cmp_le_i32 s4, 0\n" \
  "s_cbranch_scc1 LA_nt\n" \
  "s_mul_i32 s35, s5, 51200\n" \
  "s_ashr_i32 s36, s35, 31\n" \
  "s_add_u32 s46, s46, s35\n" \
  "s_addc_u32 s47, s47, s36\n" \
  "v_lshrrev_b32 v216, 4, v188\n" \
  "v_mul_u32_u24 v216, 400, v216\n" \
  "v_add_u32 v216, 6400, v216\n" \
  "v_bfe_u32 v218, v194, 4, 4\n" \
  "v_min_u32 v217, 8, v218\n" \
  "v_lshl_add_u32 v217, v217, 4, v216\n" \
  "v_lshl_add_u32 v216, v218, 4, v216\n" \
  "global_load_dwordx4 v[208:211], v216, s[46:47] offset:0\n" \
  "global_load_dwordx4 v[212:215], v217, s[46:47] offset:256\n" \
  "LA_nt:\n" \
  "s_lshl_b32 s5, s5, 7\n" \
  "s_sub_u32 s5, s5, s4\n" \
  "s_min_i32 s51, s4, 0\n" \
  "s_add_u32 s51, s51, 4\n" \
  "s_cmp_eq_u32 s51, 0\n" \
  "s_cbranch_scc1 LA_ev7\n" \
  "s_call_b64 s[52:53], LA_loop\n" \
  "LA_ev7:\n" \
  "v_mov_b32 v203, v178\n" \
  "s_cmp_le_i32 s4, 0\n" \
  "s_cbranch_scc1 LA_ev8\n" \
  "s_waitcnt vmcnt(0)\n" \
  "v_cvt_pk_u16_u32 v208, v208, v209\n" \
  "v_cvt_pk_u16_u32 v210, v210, v211\n" \
  "v_cvt_pk_u16_u32 v212, v212, v213\n" \
  "v_cvt_pk_u16_u32 v214, v214, v215\n" \
  "v_pk_lshrrev_b16 v209, 3, v208 op_sel_hi:[0,1]\n" \
  "v_pk_lshrrev_b16 v211, 3, v210 op_sel_hi:[0,1]\n" \
  "v_pk_lshrrev_b16 v213, 3, v212 op_sel_hi:[0,1]\n" \
  "v_pk_lshrrev_b16 v215, 3, v214 op_sel_hi:[0,1]\n" \
  "v_pk_mad_u16 v208, v208, 8, v209 op_sel_hi:[1,0,1]\n" \
  "v_pk_mad_u16 v210, v210, 8, v211 op_sel_hi:[1,0,1]\n" \
  "v_pk_mad_u16 v212, v212, 8, v213 op_sel_hi:[1,0,1]\n" \
  "v_pk_mad_u16 v214, v214, 8, v215 op_sel_hi:[1,0,1]\n" \
  "v_alignbit_b32 v212, v212, v212, v192\n" \
  "v_alignbit_b32 v214, v214, v214, v192\n" \
  "ds_write2_b32 v195, v208, v210 offset0:0 offset1:4\n" \
  "ds_write2_b32 v195, v212, v214 offset0:2 offset1:6\n" \
  "s_lshl_b32 s35, s4, 8\n" \
  "s_add_u32 s35, s35, 0x400\n" \
  "v_subrev_u32 v194, s35, v194\n" \
  "s_mov_b32 s51, s4\n" \
  "s_call_b64 s[52:53], LA_loop\n" \
  "LA_ev8:\n" \
  "v_mov_b32 v179, v178\n" \
  "ds_read_b128 v[100:103], v185 offset:64\n" \
  "ds_read2_b32 v[144:145], v186 offset0:0 offset1:2\n" \
  "ds_read2_b32 v[146:147], v186 offset0:4 offset1:6\n" \
  "v_mad_u32_u16 v187, v183, 16, v190\n" \
  "ds_read2_b32 v[148:149], v187 offset0:0 offset1:2\n" \
  "ds_read2_b32 v[150:151], v187 offset0:4 offset1:6\n" \
  "ds_read_b128 v[164:167], v191\n" \
  "s_lshr_b32 s35, s30, 2\n" \
  "s_lshl3_add_u32 s33, s29, s35\n" \
  "s_and_b32 s34, s30, 3\n" \
  "s_lshl_b32 s59, s27, 11\n" \
  "s_mul_i32 s35, s33, 1600000\n" \
  "s_add_u32 s54, s24, s35\n" \
  "s_addc_u32 s55, s25, 0\n" \
  "s_mul_i32 s35, s34, 819200\n" \
  "s_lshl1_add_u32 s35, s59, s35\n" \
  "s_add_u32 s35, s35, 102400000\n" \
  "s_add_u32 s56, s24, s35\n" \
  "s_addc_u32 s57, s25, 0\n" \
  "v_mbcnt_lo_u32_b32 v189, -1, 0\n" \
  "v_mbcnt_hi_u32_b32 v189, -1, v189\n" \
  "v_lshlrev_b32 v188, 4, v189\n" \
  "s_waitcnt lgkmcnt(6)\n" \
  "v_mfma_f32_16x16x32_bf16 v[152:155], v[112:115], v[112:115], 0\n" \
  "v_fmac_f32 v176, v156, v88\n" \
  "v_fmac_f32 v176, v157, v89\n" \
  "v_mfma_f32_16x16x32_bf16 v[152:155], v[116:119], v[116:119], v[152:155]\n" \
  "v_fmac_f32 v176, v158, v90\n" \
  "v_fmac_f32 v176, v159, v91\n" \
  "v_mfma_f32_16x16x32_bf16 v[156:159], v[128:131], v[128:131], 0\n" \
  "v_fmac_f32 v176, v160, v92\n" \
  "v_fmac_f32 v176, v161, v93\n" \
  "v_mfma_f32_16x16x32_bf16 v[156:159], v[132:135], v[132:135], v[156:159]\n" \
  "v_fmac_f32 v176, v162, v94\n" \
  "v_fma_f32 v177, v163, v95, v176\n" \
  "v_mfma_f32_16x16x32_bf16 v[160:163], v[128:131], v[136:139], 0\n" \
  "v_mul_f32 v176, v152, v0\n" \
  "v_fmac_f32 v176, v153, v1\n" \
  "v_mfma_f32_16x16x32_bf16 v[160:163], v[132:135], v[140:143], v[160:163]\n" \
  "v_fmac_f32 v176, v154, v2\n" \
  "v_fmac_f32 v176, v155, v3\n" \
  "v_mfma_f32_16x16x32_bf16 v[152:155], v[136:139], v[136:139], 0\n" \
  "v_fmac_f32 v176, v156, v4\n" \
  "v_fmac_f32 v176, v157, v5\n" \
  "v_mfma_f32_16x16x32_bf16 v[152:155], v[140:143], v[140:143], v[152:155]\n" \
  "v_fmac_f32 v176, v158, v6\n" \
  "v_fmac_f32 v176, v159, v7\n" \
  "s_waitcnt lgkmcnt(0)\n" \
  "s_setprio 3\n" \
  "s_barrier\n" \
  "global_load_dwordx4 v[208:211], v188, s[56:57] offset:0 sc1\n" \
  "global_load_dwordx4 v[212:215], v188, s[56:57] offset:1024 sc1\n" \
  "global_load_dwordx4 v[216:219], v188, s[56:57] offset:2048 sc1\n" \
  "global_load_dwordx4 v[220:223], v188, s[56:57] offset:3072 sc1\n" \
  "v_mfma_f32_16x16x32_bf16 v[156:159], v[112:115], v[136:139], 0\n" \
  "v_fmac_f32 v176, v160, v8\n" \
  "v_fmac_f32 v176, v161, v9\n" \
  "v_lshrrev_b32 v180, 1, v189\n" \
  "v_mfma_f32_16x16x32_bf16 v[156:159], v[116:119], v[140:143], v[156:159]\n" \
  "v_fmac_f32 v176, v162, v10\n" \
  "v_fmac_f32 v176, v163, v11\n" \
  "v_xor_b32 v180, v180, v189\n" \
  "v_mfma_f32_16x16x32_bf16 v[160:163], v[112:115], v[128:131], 0\n" \
  "v_fmac_f32 v176, v152, v12\n" \
  "v_fmac_f32 v176, v153, v13\n" \
  "v_add_f32_dpp v177, v177, v177 row_ror:8 row_mask:0xf bank_mask:0xf\n" \
  "v_and_b32 v180, 8, v180\n" \
  "v_mfma_f32_16x16x32_bf16 v[160:163], v[116:119], v[132:135], v[160:163]\n" \
  "v_fmac_f32 v176, v154, v14\n" \
  "v_fmac_f32 v176, v155, v15\n" \
  "v_bfe_u32 v181, v189, 2, 1\n" \
  "v_mfma_f32_16x16x32_bf16 v[152:155], v[112:115], v[120:123], 0\n" \
  "v_fmac_f32 v176, v156, v16\n" \
  "v_fmac_f32 v176, v157, v17\n" \
  "v_lshlrev_b32_e64 v181, v181, 5\n" \
  "v_mfma_f32_16x16x32_bf16 v[152:155], v[116:119], v[124:127], v[152:155]\n" \
  "v_fmac_f32 v176, v158, v18\n" \
  "v_fmac_f32 v176, v159, v19\n" \
  "v_mov_b32_dpp v178, v178 row_ror:4 row_mask:0xf bank_mask:0xf\n" \
  "v_lshlrev_b32 v180, v180, v181\n" \
  "v_mfma_f32_16x16x32_bf16 v[156:159], v[120:123], v[136:139], 0\n" \
  "v_fmac_f32 v176, v160, v20\n" \
  "v_fmac_f32 v176, v161, v21\n" \
  "v_bfe_u32 v181, v189, 2, 2\n" \
  "v_mfma_f32_16x16x32_bf16 v[156:159], v[124:127], v[140:143], v[156:159]\n" \
  "v_fmac_f32 v176, v162, v22\n" \
  "v_fmac_f32 v176, v163, v23\n" \
  "v_lshlrev_b32_e64 v181, v181, 1\n" \
  "v_mfma_f32_16x16x32_bf16 v[160:163], v[104:107], v[128:131], 0\n" \
  "v_fmac_f32 v176, v152, v24\n" \
  "v_fmac_f32 v176, v153, v25\n" \
  "v_cmp_gt_u32 vcc, 32, v189\n" \
  "v_mfma_f32_16x16x32_bf16 v[160:163], v[108:111], v[132:135], v[160:163]\n" \
  "v_fmac_f32 v176, v154, v26\n" \
  "v_fmac_f32 v176, v155, v27\n" \
  "v_mov_b32 v191, 0x3f80\n" \
  "v_mfma_f32_16x16x32_bf16 v[152:155], v[120:123], v[128:131], 0\n" \
  "v_fmac_f32 v176, v156, v28\n" \
  "v_fmac_f32 v176, v157, v29\n" \
  "v_add_u32 v190, 32000, v188\n" \
  "v_mfma_f32_16x16x32_bf16 v[152:155], v[124:127], v[132:135], v[152:155]\n" \
  "v_fmac_f32 v176, v158, v30\n" \
  "v_fmac_f32 v176, v159, v31\n" \
  "v_add_f32_dpp v178, v177, v177 row_ror:4 row_mask:0xf bank_mask:0x1\n" \
  "v_cndmask_b32 v192, v181, v180, vcc\n" \
  "v_mfma_f32_16x16x32_bf16 v[156:159], v[96:99], v[128:131], 0\n" \
  "v_fmac_f32 v176, v160, v32\n" \
  "v_fmac_f32 v176, v161, v33\n" \
  "v_bfe_i32 v180, v192, 0, 1\n" \
  "v_mfma_f32_16x16x32_bf16 v[156:159], v[100:103], v[132:135], v[156:159]\n" \
  "v_fmac_f32 v176, v162, v34\n" \
  "v_fmac_f32 v176, v163, v35\n" \
  "v_and_b32 v180, v180, v191\n" \
  "v_smfmac_f32_16x16x64_bf16 v[172:175], v[144:147], v[128:135], v193\n" \
  "v_fmac_f32 v176, v152, v36\n" \
  "v_fmac_f32 v176, v153, v37\n" \
  "v_bfe_i32 v181, v192, 1, 1\n" \
  "v_smfmac_f32_16x16x64_bf16 v[172:175], v[148:151], v[136:143], v193\n" \
  "v_fmac_f32 v176, v154, v38\n" \
  "v_fmac_f32 v176, v155, v39\n" \
  "v_and_b32 v181, v181, v191\n" \
  "v_mfma_f32_16x16x32_bf16 v[160:163], v[96:99], v[112:115], 0\n" \
  "v_fmac_f32 v176, v156, v40\n" \
  "v_fmac_f32 v176, v157, v41\n" \
  "v_bfe_i32 v182, v192, 2, 1\n" \
  "v_mfma_f32_16x16x32_bf16 v[160:163], v[100:103], v[116:119], v[160:163]\n" \
  "v_fmac_f32 v176, v158, v42\n" \
  "v_fmac_f32 v176, v159, v43\n" \
  "v_and_b32 v182, v182, v191\n" \
  "v_mfma_f32_16x16x32_bf16 v[152:155], v[104:107], v[112:115], 0\n" \
  "v_fmac_f32 v176, v172, v44\n" \
  "v_fmac_f32 v176, v173, v45\n" \
  "v_bfe_i32 v183, v192, 3, 1\n" \
  "v_mfma_f32_16x16x32_bf16 v[152:155], v[108:111], v[116:119], v[152:155]\n" \
  "v_fmac_f32 v176, v174, v46\n" \
  "v_fmac_f32 v176, v175, v47\n" \
  "v_and_b32 v183, v183, v191\n" \
  "v_mfma_f32_16x16x32_bf16 v[156:159], v[104:107], v[136:139], 0\n" \
  "v_fmac_f32 v176, v160, v48\n" \
  "v_fmac_f32 v176, v161, v49\n" \
  "v_and_b32 v192, 3, v189\n" \
  "v_mfma_f32_16x16x32_bf16 v[156:159], v[108:111], v[140:143], v[156:159]\n" \
  "v_fmac_f32 v176, v162, v50\n" \
  "v_fmac_f32 v176, v163, v51\n" \
  "v_mul_u32_u24 v192, 0x5555, v192\n" \
  "v_smfmac_f32_16x16x64_bf16 v[168:171], v[144:147], v[112:119], v193\n" \
  "v_fmac_f32 v176, v152, v52\n" \
  "v_fmac_f32 v176, v153, v53\n" \
  "v_add_u32 v189, 16000, v188\n" \
  "v_smfmac_f32_16x16x64_bf16 v[168:171], v[148:151], v[120:127], v193\n" \
  "v_fmac_f32 v176, v154, v54\n" \
  "v_fmac_f32 v176, v155, v55\n" \
  "v_add_u32 v191, 48000, v188\n" \
  "v_mfma_f32_16x16x32_bf16 v[160:163], v[96:99], v[136:139], 0\n" \
  "v_fmac_f32 v176, v156, v56\n" \
  "v_fmac_f32 v176, v157, v57\n" \
  "v_mfma_f32_16x16x32_bf16 v[160:163], v[100:103], v[140:143], v[160:163]\n" \
  "v_fmac_f32 v176, v158, v58\n" \
  "v_fmac_f32 v176, v159, v59\n" \
  "s_add_u32 s35, s59, 64000\n" \
  "s_add_u32 s54, s54, s35\n" \
  "s_addc_u32 s55, s55, 0\n" \
  "s_add_u32 s58, s59, 0x10000\n" \
  "s_mov_b32 m0, s58\n" \
  "s_add_u32 s35, s58, 0x8000\n" \
  "global_load_lds_dwordx4 v188, s[54:55] sc1\n" \
  "global_load_lds_dwordx4 v188, s[54:55] offset:1024 sc1\n" \
  "v_smfmac_f32_16x16x64_bf16 v[164:167], v[148:151], v[104:111], v193\n" \
  "v_fmac_f32 v176, v168, v60\n" \
  "v_fmac_f32 v176, v169, v61\n" \
  "v_smfmac_f32_16x16x64_bf16 v[164:167], v[144:147], v[96:103], v193\n" \
  "v_fmac_f32 v176, v170, v62\n" \
  "v_fmac_f32 v176, v171, v63\n" \
  "v_mfma_f32_16x16x32_bf16 v[152:155], v[120:123], v[120:123], 0\n" \
  "v_fmac_f32 v176, v160, v64\n" \
  "v_fmac_f32 v176, v161, v65\n" \
  "v_mfma_f32_16x16x32_bf16 v[152:155], v[124:127], v[124:127], v[152:155]\n" \
  "v_fmac_f32 v176, v162, v66\n" \
  "v_fmac_f32 v176, v163, v67\n" \
  "v_mfma_f32_16x16x32_bf16 v[156:159], v[104:107], v[120:123], 0\n" \
  "v_fmac_f32 v176, v164, v68\n" \
  "v_fmac_f32 v176, v165, v69\n" \
  "v_mfma_f32_16x16x32_bf16 v[156:159], v[108:111], v[124:127], v[156:159]\n" \
  "v_fmac_f32 v176, v166, v70\n" \
  "v_fmac_f32 v176, v167, v71\n" \
  "s_add_u32 m0, s58, 0x4000\n" \
  "s_cmp_lt_u32 s35, 0x28000\n" \
  "global_load_lds_dwordx4 v189, s[54:55] sc1\n" \
  "global_load_lds_dwordx4 v189, s[54:55] offset:1024 sc1\n" \
  "s_cselect_b32 s58, s35, s59\n" \
  "s_add_u32 s54, s54, 32000\n" \
  "s_addc_u32 s55, s55, 0\n" \
  "v_mfma_f32_16x16x32_bf16 v[160:163], v[96:99], v[120:123], 0\n" \
  "v_fmac_f32 v176, v152, v72\n" \
  "v_fmac_f32 v176, v153, v73\n" \
  "v_mfma_f32_16x16x32_bf16 v[160:163], v[100:103], v[124:127], v[160:163]\n" \
  "v_fmac_f32 v176, v154, v74\n" \
  "v_fmac_f32 v176, v155, v75\n" \
  "v_mfma_f32_16x16x32_bf16 v[152:155], v[96:99], v[104:107], 0\n" \
  "v_fmac_f32 v176, v156, v76\n" \
  "v_fmac_f32 v176, v157, v77\n" \
  "v_mfma_f32_16x16x32_bf16 v[152:155], v[100:103], v[108:111], v[152:155]\n" \
  "v_fmac_f32 v176, v158, v78\n" \
  "v_fmac_f32 v176, v159, v79\n" \
  "v_mfma_f32_16x16x32_bf16 v[156:159], v[104:107], v[104:107], 0\n" \
  "v_fmac_f32 v176, v160, v80\n" \
  "v_fmac_f32 v176, v161, v81\n" \
  "v_mfma_f32_16x16x32_bf16 v[156:159], v[108:111], v[108:111], v[156:159]\n" \
  "v_fmac_f32 v176, v162, v82\n" \
  "v_fmac_f32 v176, v163, v83\n" \
  "v_mfma_f32_16x16x32_bf16 v[160:163], v[148:151], v[144:147], 0\n" \
  "v_fmac_f32 v176, v152, v84\n" \
  "v_fmac_f32 v176, v153, v85\n" \
  "v_mfma_f32_16x16x32_bf16 v[160:163], v[96:99], v[96:99], v[160:163]\n" \
  "v_fmac_f32 v176, v154, v86\n" \
  "v_fmac_f32 v176, v155, v87\n" \
  "v_mfma_f32_16x16x32_bf16 v[160:163], v[100:103], v[100:103], v[160:163]\n" \
  "s_mov_b32 s51, 7\n" \
  "v_fmac_f32 v176, v156, v88\n" \
  "v_fmac_f32 v176, v157, v89\n" \
  "v_fmac_f32 v176, v158, v90\n" \
  "v_fmac_f32 v176, v159, v91\n" \
  "s_nop 2\n" \
  "v_fmac_f32 v176, v160, v92\n" \
  "v_fmac_f32 v176, v161, v93\n" \
  "v_fmac_f32 v176, v162, v94\n" \
  "v_fma_f32 v177, v163, v95, v176\n" \
  "s_nop 1\n" \
  "v_add_f32_dpp v177, v177, v177 row_ror:8 row_mask:0xf bank_mask:0xf\n" \
  "v_mov_b32_dpp v178, v178 row_ror:4 row_mask:0xf bank_mask:0xf\n" \
  "s_nop 1\n" \
  "v_add_f32_dpp v178, v177, v177 row_ror:4 row_mask:0xf bank_mask:0x1\n" \
  "v_mov_b32 v99, v179\n" \
  "s_nop 0\n" \
  "v_mov_b32_dpp v196, v178 row_ror:8 row_mask:0xf bank_mask:0xc\n" \
  "s_nop 1\n" \
  "v_permlane16_swap_b32 v196, v197\n" \
  "v_permlane16_swap_b32 v198, v199\n" \
  "v_permlane16_swap_b32 v200, v201\n" \
  "v_permlane16_swap_b32 v202, v203\n" \
  "v_permlane16_swap_b32 v179, v99\n" \
  "v_add_f32 v196, v196, v197\n" \
  "v_add_f32 v198, v198, v199\n" \
  "v_add_f32 v200, v200, v201\n" \
  "v_add_f32 v202, v202, v203\n" \
  "v_add_f32 v179, v179, v99\n" \
  "v_lshrrev_b32 v96, 4, v188\n" \
  "v_and_b32 v97, 1, v96\n" \
  "v_mov_b32 v99, v179\n" \
  "v_lshrrev_b32 v98, 4, v96\n" \
  "v_permlane32_swap_b32 v196, v198\n" \
  "v_permlane32_swap_b32 v200, v202\n" \
  "v_permlane32_swap_b32 v179, v99\n" \
  "v_add_f32 v196, v196, v198\n" \
  "v_add_f32 v200, v200, v202\n" \
  "v_add_f32 v179, v179, v99\n" \
  "v_cmp_eq_u32 vcc, 1, v97\n" \
  "v_lshlrev_b32 v97, 2, v97\n" \
  "v_add_lshl_u32 v97, v97, v98, 2\n" \
  "v_bfe_u32 v98, v96, 2, 2\n" \
  "v_add_f32_dpp v196, v196, v196 quad_perm:[1,0,3,2] row_mask:0xf bank_mask:0xf\n" \
  "v_add_f32_dpp v200, v200, v200 quad_perm:[1,0,3,2] row_mask:0xf bank_mask:0xf\n" \
  "v_add_f32_dpp v179, v179, v179 quad_perm:[1,0,3,2] row_mask:0xf bank_mask:0xf\n" \
  "v_sub_u32 v97, v97, v98\n" \
  "v_cndmask_b32 v196, v196, v200, vcc\n" \
  "v_add_u32 v97, 1, v97\n" \
  "s_min_i32 s35, s4, 0\n" \
  "s_add_u32 s36, s4, 32\n" \
  "s_add_u32 s37, s4, 29\n" \
  "v_add_f32_dpp v179, v179, v179 quad_perm:[2,3,0,1] row_mask:0xf bank_mask:0xf\n" \
  "v_add_f32_dpp v196, v196, v196 quad_perm:[2,3,0,1] row_mask:0xf bank_mask:0xf\n" \
  "s_mov_b32 exec_lo, 0x0\n" \
  "s_mov_b32 exec_hi, 0xaaaa0000\n" \
  "v_add_u32 v97, s35, v97\n" \
  "s_mov_b32 exec_lo, 0x5500\n" \
  "s_mov_b32 exec_hi, 0x0\n" \
  "v_add_u32 v97, s36, v97\n" \
  "s_mov_b32 exec_lo, 0x4444\n" \
  "s_mov_b32 exec_hi, 0x0\n" \
  "v_sub_u32 v97, s37, v98\n" \
  "v_mov_b32 v196, v179\n" \
  "s_mov_b64 exec, -1\n" \
  "v_cmp_lt_u32 vcc, 31, v97\n" \
  "v_add_u32 v98, s5, v97\n" \
  "s_lshl2_add_u32 s35, s7, s28\n" \
  "s_lshl_b32 s35, s35, 5\n" \
  "v_cndmask_b32 v97, v97, v98, vcc\n" \
  "v_add_lshl_u32 v97, v97, s35, 2\n" \
  "s_lshl_b32 s35, s31, 16\n" \
  "s_add_u32 s35, s35, 109871104\n" \
  "s_add_u32 s40, s24, s35\n" \
  "s_addc_u32 s41, s25, 0\n" \
  "v_min_u32 v196, -2, v196\n" \
  "v_lshlrev_b32 v184, 16, v204\n" \
  "v_and_b32 v185, 0xffff0000, v204\n" \
  "v_lshlrev_b32 v186, 16, v205\n" \
  "v_and_b32 v187, 0xffff0000, v205\n" \
  "s_barrier\n" \
  "s_add_u32 s56, s56, 32768\n" \
  "s_addc_u32 s57, s57, 0\n" \
  "global_load_dwordx4 v[224:227], v188, s[56:57] sc1\n" \
  "global_load_dwordx4 v[228:231], v188, s[56:57] offset:1024 sc1\n" \
  "global_load_dwordx4 v[232:235], v188, s[56:57] offset:2048 sc1\n" \
  "global_load_dwordx4 v[236:239], v188, s[56:57] offset:3072 sc1\n" \
  "s_add_u32 s56, s56, 32768\n" \
  "s_addc_u32 s57, s57, 0\n" \
  "s_mov_b32 exec_lo, 0x33337777\n" \
  "s_mov_b32 exec_hi, 0x33333333\n" \
  "global_store_dword v97, v196, s[40:41] sc1\n" \
  "s_mov_b64 exec, -1\n" \
  "s_waitcnt vmcnt(9)\n" \
  "s_setprio 0\n" \
  "s_branch LG_start\n" \
  ".p2align 6\n" \
  "LA_loop:\n" \
  "LA_mid:\n" \
  "v_mfma_f32_16x16x32_bf16 v[152:155], v[112:115], v[112:115], 0\n" \
  "s_waitcnt lgkmcnt(8)\n" \
  "ds_read_b128 v[100:103], v185 offset:64\n" \
  "v_fmac_f32 v176, v156, v88\n" \
  "v_fmac_f32 v176, v157, v89\n" \
  "v_mfma_f32_16x16x32_bf16 v[152:155], v[116:119], v[116:119], v[152:155]\n" \
  "ds_read2_b32 v[144:145], v186 offset0:0 offset1:2\n" \
  "v_fmac_f32 v176, v158, v90\n" \
  "v_fmac_f32 v176, v159, v91\n" \
  "v_mfma_f32_16x16x32_bf16 v[156:159], v[128:131], v[128:131], 0\n" \
  "ds_read2_b32 v[146:147], v186 offset0:4 offset1:6\n" \
  "v_fmac_f32 v176, v160, v92\n" \
  "v_fmac_f32 v176, v161, v93\n" \
  "v_mad_u32_u16 v187, v183, 16, v190\n" \
  "v_mfma_f32_16x16x32_bf16 v[156:159], v[132:135], v[132:135], v[156:159]\n" \
  "s_waitcnt lgkmcnt(10)\n" \
  "ds_read2_b32 v[148:149], v187 offset0:0 offset1:2\n" \
  "v_fmac_f32 v176, v162, v94\n" \
  "v_fma_f32 v177, v163, v95, v176\n" \
  "v_mfma_f32_16x16x32_bf16 v[160:163], v[128:131], v[136:139], 0\n" \
  "s_waitcnt lgkmcnt(10)\n" \
  "ds_read2_b32 v[150:151], v187 offset0:4 offset1:6\n" \
  "v_mul_f32 v176, v152, v0\n" \
  "v_fmac_f32 v176, v153, v1\n" \
  "v_mfma_f32_16x16x32_bf16 v[160:163], v[132:135], v[140:143], v[160:163]\n" \
  "ds_read_b128 v[180:183], v194\n" \
  "v_fmac_f32 v176, v154, v2\n" \
  "v_fmac_f32 v176, v155, v3\n" \
  "v_mfma_f32_16x16x32_bf16 v[152:155], v[136:139], v[136:139], 0\n" \
  "v_fmac_f32 v176, v156, v4\n" \
  "v_fmac_f32 v176, v157, v5\n" \
  "v_mfma_f32_16x16x32_bf16 v[152:155], v[140:143], v[140:143], v[152:155]\n" \
  "ds_read_b128 v[164:167], v191\n" \
  "v_fmac_f32 v176, v158, v6\n" \
  "v_fmac_f32 v176, v159, v7\n" \
  "v_mfma_f32_16x16x32_bf16 v[156:159], v[112:115], v[136:139], 0\n" \
  "v_fmac_f32 v176, v160, v8\n" \
  "v_fmac_f32 v176, v161, v9\n" \
  "v_mfma_f32_16x16x32_bf16 v[156:159], v[116:119], v[140:143], v[156:159]\n" \
  "v_fmac_f32 v176, v162, v10\n" \
  "v_fmac_f32 v176, v163, v11\n" \
  "v_mfma_f32_16x16x32_bf16 v[160:163], v[112:115], v[128:131], 0\n" \
  "v_fmac_f32 v176, v152, v12\n" \
  "v_fmac_f32 v176, v153, v13\n" \
  "v_add_f32_dpp v177, v177, v177 row_ror:8 row_mask:0xf bank_mask:0xf\n" \
  "v_mfma_f32_16x16x32_bf16 v[160:163], v[116:119], v[132:135], v[160:163]\n" \
  "s_waitcnt lgkmcnt(11)\n" \
  "v_fmac_f32 v176, v154, v14\n" \
  "v_fmac_f32 v176, v155, v15\n" \
  "v_mfma_f32_16x16x32_bf16 v[152:155], v[112:115], v[120:123], 0\n" \
  "s_waitcnt lgkmcnt(10)\n" \
  "v_fmac_f32 v176, v156, v16\n" \
  "v_fmac_f32 v176, v157, v17\n" \
  "v_mfma_f32_16x16x32_bf16 v[152:155], v[116:119], v[124:127], v[152:155]\n" \
  "v_fmac_f32 v176, v158, v18\n" \
  "v_fmac_f32 v176, v159, v19\n" \
  "v_mov_b32_dpp v178, v178 row_ror:4 row_mask:0xf bank_mask:0xf\n" \
  "v_mfma_f32_16x16x32_bf16 v[156:159], v[120:123], v[136:139], 0\n" \
  "v_fmac_f32 v176, v160, v20\n" \
  "v_fmac_f32 v176, v161, v21\n" \
  "v_mfma_f32_16x16x32_bf16 v[156:159], v[124:127], v[140:143], v[156:159]\n" \
  "s_waitcnt lgkmcnt(9)\n" \
  "v_fmac_f32 v176, v162, v22\n" \
  "v_fmac_f32 v176, v163, v23\n" \
  "v_mfma_f32_16x16x32_bf16 v[160:163], v[104:107], v[128:131], 0\n" \
  "s_waitcnt lgkmcnt(1)\n" \
  "v_fmac_f32 v176, v152, v24\n" \
  "v_fmac_f32 v176, v153, v25\n" \
  "v_mad_u32_u16 v184, v182, 16, v188\n" \
  "v_mfma_f32_16x16x32_bf16 v[160:163], v[108:111], v[132:135], v[160:163]\n" \
  "v_fmac_f32 v176, v154, v26\n" \
  "v_fmac_f32 v176, v155, v27\n" \
  "v_mfma_f32_16x16x32_bf16 v[152:155], v[120:123], v[128:131], 0\n" \
  "v_fmac_f32 v176, v156, v28\n" \
  "v_fmac_f32 v176, v157, v29\n" \
  "v_mfma_f32_16x16x32_bf16 v[152:155], v[124:127], v[132:135], v[152:155]\n" \
  "v_fmac_f32 v176, v158, v30\n" \
  "v_fmac_f32 v176, v159, v31\n" \
  "v_add_f32_dpp v178, v177, v177 row_ror:4 row_mask:0xf bank_mask:0x1\n" \
  "v_mfma_f32_16x16x32_bf16 v[156:159], v[96:99], v[128:131], 0\n" \
  "v_fmac_f32 v176, v160, v32\n" \
  "v_fmac_f32 v176, v161, v33\n" \
  "v_mfma_f32_16x16x32_bf16 v[156:159], v[100:103], v[132:135], v[156:159]\n" \
  "v_fmac_f32 v176, v162, v34\n" \
  "v_fmac_f32 v176, v163, v35\n" \
  "v_smfmac_f32_16x16x64_bf16 v[172:175], v[144:147], v[128:135], v193\n" \
  "ds_read_b128 v[128:131], v184 offset:0\n" \
  "v_fmac_f32 v176, v152, v36\n" \
  "v_fmac_f32 v176, v153, v37\n" \
  "v_smfmac_f32_16x16x64_bf16 v[172:175], v[148:151], v[136:143], v193\n" \
  "ds_read_b128 v[132:135], v184 offset:64\n" \
  "v_fmac_f32 v176, v154, v38\n" \
  "v_fmac_f32 v176, v155, v39\n" \
  "v_mfma_f32_16x16x32_bf16 v[160:163], v[96:99], v[112:115], 0\n" \
  "v_fmac_f32 v176, v156, v40\n" \
  "v_fmac_f32 v176, v157, v41\n" \
  "v_mfma_f32_16x16x32_bf16 v[160:163], v[100:103], v[116:119], v[160:163]\n" \
  "v_fmac_f32 v176, v158, v42\n" \
  "v_fmac_f32 v176, v159, v43\n" \
  "v_add_u32 v194, 0x100, v194\n" \
  "v_mfma_f32_16x16x32_bf16 v[152:155], v[104:107], v[112:115], 0\n" \
  "v_fmac_f32 v176, v172, v44\n" \
  "v_fmac_f32 v176, v173, v45\n" \
  "v_mfma_f32_16x16x32_bf16 v[152:155], v[108:111], v[116:119], v[152:155]\n" \
  "v_fmac_f32 v176, v174, v46\n" \
  "v_fmac_f32 v176, v175, v47\n" \
  "v_mfma_f32_16x16x32_bf16 v[156:159], v[104:107], v[136:139], 0\n" \
  "ds_read_b128 v[172:175], v191\n" \
  "v_fmac_f32 v176, v160, v48\n" \
  "v_fmac_f32 v176, v161, v49\n" \
  "v_mad_u32_u16 v185, v181, 16, v188\n" \
  "v_mfma_f32_16x16x32_bf16 v[156:159], v[108:111], v[140:143], v[156:159]\n" \
  "v_fmac_f32 v176, v162, v50\n" \
  "v_fmac_f32 v176, v163, v51\n" \
  "v_mad_u32_u16 v186, v182, 16, v188 op_sel:[1,0,0,0]\n" \
  "v_smfmac_f32_16x16x64_bf16 v[168:171], v[144:147], v[112:119], v193\n" \
  "ds_read_b128 v[112:115], v185 offset:0\n" \
  "v_fmac_f32 v176, v152, v52\n" \
  "v_fmac_f32 v176, v153, v53\n" \
  "v_smfmac_f32_16x16x64_bf16 v[168:171], v[148:151], v[120:127], v193\n" \
  "ds_read_b128 v[116:119], v185 offset:64\n" \
  "v_fmac_f32 v176, v154, v54\n" \
  "v_fmac_f32 v176, v155, v55\n" \
  "v_mfma_f32_16x16x32_bf16 v[160:163], v[96:99], v[136:139], 0\n" \
  "v_fmac_f32 v176, v156, v56\n" \
  "v_fmac_f32 v176, v157, v57\n" \
  "v_mfma_f32_16x16x32_bf16 v[160:163], v[100:103], v[140:143], v[160:163]\n" \
  "s_waitcnt lgkmcnt(5)\n" \
  "v_fmac_f32 v176, v158, v58\n" \
  "v_fmac_f32 v176, v159, v59\n" \
  "v_smfmac_f32_16x16x64_bf16 v[164:167], v[148:151], v[104:111], v193\n" \
  "ds_read_b128 v[136:139], v186 offset:0\n" \
  "v_fmac_f32 v176, v168, v60\n" \
  "v_fmac_f32 v176, v169, v61\n" \
  "v_smfmac_f32_16x16x64_bf16 v[164:167], v[144:147], v[96:103], v193\n" \
  "ds_read_b128 v[140:143], v186 offset:64\n" \
  "v_fmac_f32 v176, v170, v62\n" \
  "v_fmac_f32 v176, v171, v63\n" \
  "v_mfma_f32_16x16x32_bf16 v[152:155], v[120:123], v[120:123], 0\n" \
  "v_fmac_f32 v176, v160, v64\n" \
  "v_fmac_f32 v176, v161, v65\n" \
  "v_mfma_f32_16x16x32_bf16 v[152:155], v[124:127], v[124:127], v[152:155]\n" \
  "ds_read_b128 v[168:171], v191\n" \
  "v_fmac_f32 v176, v162, v66\n" \
  "v_fmac_f32 v176, v163, v67\n" \
  "v_mad_u32_u16 v187, v181, 16, v188 op_sel:[1,0,0,0]\n" \
  "v_mfma_f32_16x16x32_bf16 v[156:159], v[104:107], v[120:123], 0\n" \
  "v_fmac_f32 v176, v164, v68\n" \
  "v_fmac_f32 v176, v165, v69\n" \
  "v_mad_u32_u16 v184, v180, 16, v188 op_sel:[1,0,0,0]\n" \
  "v_mfma_f32_16x16x32_bf16 v[156:159], v[108:111], v[124:127], v[156:159]\n" \
  "v_fmac_f32 v176, v166, v70\n" \
  "v_fmac_f32 v176, v167, v71\n" \
  "v_mfma_f32_16x16x32_bf16 v[160:163], v[96:99], v[120:123], 0\n" \
  "ds_read_b128 v[120:123], v187 offset:0\n" \
  "v_fmac_f32 v176, v152, v72\n" \
  "v_fmac_f32 v176, v153, v73\n" \
  "v_mfma_f32_16x16x32_bf16 v[160:163], v[100:103], v[124:127], v[160:163]\n" \
  "v_fmac_f32 v176, v154, v74\n" \
  "v_fmac_f32 v176, v155, v75\n" \
  "v_mad_u32_u16 v185, v180, 16, v188\n" \
  "v_mfma_f32_16x16x32_bf16 v[152:155], v[96:99], v[104:107], 0\n" \
  "ds_read_b128 v[124:127], v187 offset:64\n" \
  "v_fmac_f32 v176, v156, v76\n" \
  "v_fmac_f32 v176, v157, v77\n" \
  "v_mfma_f32_16x16x32_bf16 v[152:155], v[100:103], v[108:111], v[152:155]\n" \
  "v_fmac_f32 v176, v158, v78\n" \
  "v_fmac_f32 v176, v159, v79\n" \
  "v_mfma_f32_16x16x32_bf16 v[156:159], v[104:107], v[104:107], 0\n" \
  "ds_read_b128 v[104:107], v184 offset:0\n" \
  "v_fmac_f32 v176, v160, v80\n" \
  "v_fmac_f32 v176, v161, v81\n" \
  "v_mfma_f32_16x16x32_bf16 v[156:159], v[108:111], v[108:111], v[156:159]\n" \
  "s_sub_u32 s51, s51, 1\n" \
  "v_fmac_f32 v176, v162, v82\n" \
  "v_fmac_f32 v176, v163, v83\n" \
  "v_mad_u32_u16 v186, v183, 16, v189\n" \
  "v_mfma_f32_16x16x32_bf16 v[160:163], v[148:151], v[144:147], 0\n" \
  "s_cmp_lg_u32 s51, 0\n" \
  "ds_read_b128 v[108:111], v184 offset:64\n" \
  "v_fmac_f32 v176, v152, v84\n" \
  "v_fmac_f32 v176, v153, v85\n" \
  "v_mfma_f32_16x16x32_bf16 v[160:163], v[96:99], v[96:99], v[160:163]\n" \
  "s_waitcnt lgkmcnt(8)\n" \
  "ds_read_b128 v[96:99], v185 offset:0\n" \
  "v_fmac_f32 v176, v154, v86\n" \
  "v_fmac_f32 v176, v155, v87\n" \
  "v_mfma_f32_16x16x32_bf16 v[160:163], v[100:103], v[100:103], v[160:163]\n" \
  "s_cbranch_scc1 LA_loop\n" \
  "s_cmp_ge_u32 s50, 6\n" \
  "s_cbranch_scc1 LA_ret\n" \
  "s_set_gpr_idx_on s50, gpr_idx(DST)\n" \
  "v_mov_b32 v196, v178\n" \
  "s_lshl_b32 s35, s50, 3\n" \
  "s_waitcnt vmcnt(0)\n" \
  "s_set_gpr_idx_on s35, gpr_idx(SRC0,SRC1)\n" \
  "v_cvt_pk_u16_u32 v210, v208, v209\n" \
  "v_cvt_pk_u16_u32 v211, v210, v211\n" \
  "v_cvt_pk_u16_u32 v212, v212, v213\n" \
  "v_cvt_pk_u16_u32 v213, v214, v215\n" \
  "s_set_gpr_idx_off\n" \
  "v_pk_lshrrev_b16 v214, 3, v210 op_sel_hi:[0,1]\n" \
  "v_pk_mad_u16 v210, v210, 8, v214 op_sel_hi:[1,0,1]\n" \
  "v_pk_lshrrev_b16 v215, 3, v211 op_sel_hi:[0,1]\n" \
  "v_pk_mad_u16 v211, v211, 8, v215 op_sel_hi:[1,0,1]\n" \
  "v_pk_lshrrev_b16 v214, 3, v212 op_sel_hi:[0,1]\n" \
  "v_pk_mad_u16 v212, v212, 8, v214 op_sel_hi:[1,0,1]\n" \
  "v_pk_lshrrev_b16 v215, 3, v213 op_sel_hi:[0,1]\n" \
  "v_pk_mad_u16 v213, v213, 8, v215 op_sel_hi:[1,0,1]\n" \
  "s_lshr_b32 s35, s38, 1\n" \
  "v_add_u32 v214, s35, v195\n" \
  "v_alignbit_b32 v212, v212, v212, v192\n" \
  "v_alignbit_b32 v213, v213, v213, v192\n" \
  "ds_write2_b32 v214, v210, v211 offset0:0 offset1:4\n" \
  "ds_write2_b32 v214, v212, v213 offset0:2 offset1:6\n" \
  "ds_read_b32 v209, v208 offset:4\n" \
  "v_subrev_u32 v194, s38, v194\n" \
  "s_xor_b32 s38, s38, 0x800\n" \
  "s_add_u32 s50, s50, 1\n" \
  "s_mov_b32 s51, 4\n" \
  "s_branch LA_loop\n" \
  "LA_ret:\n" \
  "s_setpc_b64 s[52:53]\n" \
  "LB_start:\n" \
  "s_setprio 2\n" \
  "s_lshl_b32 s35, s31, 7\n" \
  "s_waitcnt lgkmcnt(0)\n" \
  "s_add_u32 s40, s10, s35\n" \
  "s_addc_u32 s41, s11, 0\n" \
  "s_add_u32 s37, s36, 32\n" \
  "L3_:\n" \
  "s_sub_u32 s35, s36, 125\n" \
  "s_min_u32 s35, s35, s36\n" \
  "s_mul_i32 m0, s35, 1040\n" \
  "s_lshl_b32 s35, s35, 11\n" \
  "s_add_u32 s42, s40, s35\n" \
  "s_addc_u32 s43, s41, 0\n" \
  "global_load_lds_dwordx4 v254, s[42:43]\n" \
  "s_add_u32 s36, s36, 4\n" \
  "s_sub_u32 s35, s36, 125\n" \
  "s_min_u32 s35, s35, s36\n" \
  "s_mul_i32 m0, s35, 1040\n" \
  "s_lshl_b32 s35, s35, 11\n" \
  "s_add_u32 s42, s40, s35\n" \
  "s_addc_u32 s43, s41, 0\n" \
  "global_load_lds_dwordx4 v254, s[42:43]\n" \
  "s_add_u32 s36, s36, 4\n" \
  "s_cmp_lg_u32 s37, s36\n" \
  "s_cbranch_scc1 L3_\n" \
  "v_mov_b32 v190, 163328\n" \
  "v_mov_b64 v[2:3], 0\n" \
  "ds_write_b64 v190, v[2:3]\n" \
  "ds_write_b64 v190, v[2:3] offset:8\n" \
  "ds_write_b64 v190, v[2:3] offset:16\n" \
  "s_waitcnt lgkmcnt(0)\n" \
  "s_barrier\n" \
  "v_lshlrev_b32 v247, 7, v255\n" \
  "s_add_u32 s37, s36, 96\n" \
  "L4_:\n" \
  "s_sub_u32 s35, s36, 125\n" \
  "s_min_u32 s35, s35, s36\n" \
  "s_mul_i32 m0, s35, 1040\n" \
  "s_lshl_b32 s35, s35, 11\n" \
  "s_add_u32 s42, s40, s35\n" \
  "s_addc_u32 s43, s41, 0\n" \
  "global_load_lds_dwordx4 v254, s[42:43]\n" \
  "s_add_u32 s36, s36, 4\n" \
  "s_sub_u32 s35, s36, 125\n" \
  "s_min_u32 s35, s35, s36\n" \
  "s_mul_i32 m0, s35, 1040\n" \
  "s_lshl_b32 s35, s35, 11\n" \
  "s_add_u32 s42, s40, s35\n" \
  "s_addc_u32 s43, s41, 0\n" \
  "global_load_lds_dwordx4 v254, s[42:43]\n" \
  "s_add_u32 s36, s36, 4\n" \
  "s_cmp_lg_u32 s37, s36\n" \
  "s_cbranch_scc1 L4_\n" \
  "s_add_u32 s48, s24, 110002176\n" \
  "s_addc_u32 s49, s25, 0\n" \
  "s_lshl_b32 s35, s26, 2\n" \
  "v_mov_b32 v193, s35\n" \
  "global_load_dword v193, v193, s[48:49] offset:1024 sc1\n" \
  "v_mov_b32 v191, 1\n" \
  "v_and_b32 v252, 15, v255\n" \
  "v_lshrrev_b32 v253, 4, v255\n" \
  "s_lshl2_add_u32 s35, s31, s28\n" \
  "s_lshl_b32 s35, s35, 1\n" \
  "v_bfe_u32 v1, v255, 2, 1\n" \
  "v_add_u32 v1, s35, v1\n" \
  "v_and_b32 v2, 3, v255\n" \
  "v_lshlrev_b32 v3, 5, v1\n" \
  "v_lshl_add_u32 v3, v2, 3, v3\n" \
  "v_lshlrev_b32 v4, 2, v2\n" \
  "v_add_u32 v4, 380, v4\n" \
  "v_cmp_gt_u32 vcc, 12, v1\n" \
  "v_lshrrev_b32 v5, 3, v255\n" \
  "v_mul_u32_u24 v5, 6400, v5\n" \
  "v_cndmask_b32 v3, v4, v3, vcc\n" \
  "v_add_u32 v5, 3200, v5\n" \
  "v_add_u32 v227, v5, v3\n" \
  "v_lshl_add_u32 v3, v1, 2, v2\n" \
  "v_add_u32 v4, 48, v3\n" \
  "v_mul_u32_u24 v5, 205, v3\n" \
  "v_lshrrev_b32 v5, 11, v5\n" \
  "v_mul_u32_u24 v5, 10, v5\n" \
  "v_sub_u32 v5, v3, v5\n" \
  "v_lshlrev_b32 v236, 26, v5\n" \
  "v_mul_u32_u24 v5, 205, v4\n" \
  "v_lshrrev_b32 v5, 11, v5\n" \
  "v_mul_u32_u24 v5, 10, v5\n" \
  "v_sub_u32 v5, v4, v5\n" \
  "v_lshlrev_b32 v237, 26, v5\n" \
  "s_lshl2_add_u32 s4, s31, s28\n" \
  "s_lshl_b32 s4, s4, 7\n" \
  "s_add_u32 s36, s4, 96\n" \
  "s_min_u32 s36, s36, 984\n" \
  "s_add_u32 s37, s4, 112\n" \
  "s_min_u32 s37, s37, 984\n" \
  "v_add_u32 v186, s4, v252\n" \
  "v_add_u32 v187, s36, v252\n" \
  "v_add_u32 v188, s37, v252\n" \
  "s_mul_i32 s38, s7, 51200\n" \
  "s_add_u32 s42, s8, s38\n" \
  "s_addc_u32 s43, s9, 0\n" \
  "s_xor_b32 s38, s31, 1\n" \
  "s_lshl_b32 s38, s38, 7\n" \
  "s_add_u32 s94, s10, s38\n" \
  "s_addc_u32 s95, s11, 0\n" \
  "s_mul_i32 s5, s28, 4224\n" \
  "s_add_u32 s5, s5, 146432\n" \
  "v_lshrrev_b32 v2, 3, v186\n" \
  "v_lshl_add_u32 v2, v186, 3, v2\n" \
  "v_add_lshl_u32 v249, v2, v253, 4\n" \
  "v_lshrrev_b32 v2, 3, v187\n" \
  "v_lshl_add_u32 v2, v187, 3, v2\n" \
  "v_add_lshl_u32 v250, v2, v253, 4\n" \
  "v_lshrrev_b32 v2, 3, v188\n" \
  "v_lshl_add_u32 v2, v188, 3, v2\n" \
  "v_add_lshl_u32 v251, v2, v253, 4\n" \
  "v_mul_u32_u24 v1, 1600000, v253\n" \
  "v_lshl_add_u32 v186, v186, 4, v1\n" \
  "v_lshl_add_u32 v187, v187, 4, v1\n" \
  "v_lshl_add_u32 v188, v188, 4, v1\n" \
  "v_lshlrev_b32 v1, 4, v253\n" \
  "v_xor_b32 v244, v1, v254\n" \
  "v_lshl_add_u32 v245, v255, 4, s5\n" \
  "v_lshrrev_b32 v1, 1, v252\n" \
  "s_waitcnt vmcnt(1)\n" \
  "s_mov_b64 exec, 1\n" \
  "ds_add_u32 v190, v191 offset:12\n" \
  "s_mov_b64 exec, -1\n" \
  "s_memtime s[88:89]\n" \
  "v_xor_b32 v1, v1, v253\n" \
  "v_lshlrev_b32 v1, 4, v1\n" \
  "v_lshl_add_u32 v1, v252, 7, v1\n" \
  "v_add_u32 v246, s5, v1\n" \
  "v_xor_b32 v1, 64, v1\n" \
  "v_add_u32 v242, s5, v1\n" \
  "v_lshlrev_b32 v1, 4, v255\n" \
  "v_xor_b32 v1, 64, v1\n" \
  "v_add_u32 v243, s5, v1\n" \
  "v_lshrrev_b32 v1, 2, v255\n" \
  "s_lshl_b32 s35, s28, 5\n" \
  "s_lshl_b32 s36, s31, 6\n" \
  "s_add_u32 s35, s35, s36\n" \
  "s_and_b32 s35, s35, 127\n" \
  "v_add_u32 v1, s35, v1\n" \
  "v_lshrrev_b32 v2, 4, v255\n" \
  "v_and_b32 v2, 2, v2\n" \
  "v_xor_b32 v2, v2, v255\n" \
  "v_and_b32 v2, 3, v2\n" \
  "v_lshlrev_b32 v2, 4, v2\n" \
  "v_lshl_add_u32 v189, v1, 10, v2\n" \
  "v_add_u32 v194, 15360, v189\n" \
  "v_lshrrev_b32 v1, 2, v252\n" \
  "v_lshlrev_b32 v1, 6, v1\n" \
  "v_lshl_add_u32 v1, v253, 9, v1\n" \
  "v_and_b32 v2, 3, v252\n" \
  "v_lshl_add_u32 v1, v2, 3, v1\n" \
  "v_and_b32 v2, 1, v253\n" \
  "v_lshl_add_u32 v1, v2, 5, v1\n" \
  "v_add_u32 v184, 138240, v1\n" \
  "v_xor_b32 v1, 32, v1\n" \
  "v_add_u32 v185, 138240, v1\n" \
  "s_bfe_u32 s35, s30, 0x10001\n" \
  "s_lshl1_add_u32 s35, s29, s35\n" \
  "s_lshr_b32 s6, s30, 2\n" \
  "s_lshl_b32 s6, s6, 18\n" \
  "s_lshl_b32 s38, s35, 6\n" \
  "s_add_u32 s92, s14, s38\n" \
  "s_addc_u32 s93, s15, 0\n" \
  "s_lshr_b32 s37, s30, 3\n" \
  "s_add_u32 s37, s37, 96\n" \
  "s_lshl_b32 s34, s37, 17\n" \
  "s_mul_i32 s35, s35, 400\n" \
  "s_add_u32 s37, s37, s35\n" \
  "s_mul_i32 s37, s37, 16000\n" \
  "s_add_u32 s46, s24, s37\n" \
  "s_addc_u32 s47, s25, 0\n" \
  "s_lshr_b32 s36, s30, 2\n" \
  "s_add_u32 s36, s36, s35\n" \
  "s_mul_i32 s36, s36, 16000\n" \
  "s_add_u32 s44, s24, s36\n" \
  "s_addc_u32 s45, s25, 0\n" \
  "s_lshl_b32 s96, s28, 11\n" \
  "s_add_u32 s96, s96, 138240\n" \
  "v_lshlrev_b32 v238, 4, v255\n" \
  "v_mov_b32 v234, 0\n" \
  "s_lshl2_add_u32 s33, s26, s28\n" \
  "s_mov_b32 s52, 0x10002\n" \
  "s_mov_b32 s53, 0xc0c0400\n" \
  "s_mov_b32 s54, 0x4000c0c\n" \
  "s_mov_b32 s55, 0x3030303\n" \
  "s_mov_b32 s56, 0x55555555\n" \
  "s_mov_b32 s38, 0x0\n" \
  "s_mov_b32 s98, 0x0\n" \
  "s_mov_b32 s99, 0x8\n" \
  "s_mov_b32 s97, 0x2000\n" \
  "v_xor_b32 v245, 64, v244\n" \
  "v_add_u32 v245, 0x400, v245\n" \
  "s_setprio 0\n" \
  "L5_:\n" \
  "ds_read_b32 v192, v190 offset:16\n" \
  "s_waitcnt lgkmcnt(0)\n" \
  "v_readfirstlane_b32 s35, v192\n" \
  "s_cmp_lt_u32 s35, 4\n" \
  "s_cbranch_scc1 L5_\n" \
  "s_add_u32 s35, s4, 0\n" \
  "s_min_u32 s35, s35, 984\n" \
  "s_lshl_b32 s35, s35, 8\n" \
  "s_add_u32 m0, s5, 0\n" \
  "s_add_u32 s58, s94, s35\n" \
  "s_addc_u32 s59, s95, 0\n" \
  "global_load_lds_dwordx4 v244, s[58:59]\n" \
  "global_load_lds_dwordx4 v245, s[58:59] offset:1024\n" \
  "s_add_u32 s35, s4, 16\n" \
  "s_min_u32 s35, s35, 984\n" \
  "s_lshl_b32 s35, s35, 8\n" \
  "s_add_u32 m0, s5, 2048\n" \
  "s_add_u32 s58, s94, s35\n" \
  "s_addc_u32 s59, s95, 0\n" \
  "global_load_lds_dwordx4 v244, s[58:59]\n" \
  "global_load_lds_dwordx4 v245, s[58:59] offset:1024\n" \
  "L6_:\n" \
  "ds_read_b32 v192, v190 offset:12\n" \
  "s_waitcnt lgkmcnt(0)\n" \
  "v_readfirstlane_b32 s35, v192\n" \
  "s_cmp_lt_u32 s35, 4\n" \
  "s_cbranch_scc1 L6_\n" \
  "s_waitcnt vmcnt(0)\n" \
  "ds_read_b128 v[8:11], v246 offset:0\n" \
  "ds_read_b128 v[12:15], v242 offset:0\n" \
  "ds_read_b128 v[24:27], v246 offset:2048\n" \
  "ds_read_b128 v[28:31], v242 offset:2048\n" \
  "ds_read_b128 v[0:3], v249 offset:0\n" \
  "ds_read_b128 v[4:7], v249 offset:64\n" \
  "ds_read_b128 v[16:19], v249 offset:2080\n" \
  "ds_read_b128 v[20:23], v249 offset:2144\n" \
  "s_waitcnt lgkmcnt(4)\n" \
  "s_add_u32 s35, s4, 32\n" \
  "s_min_u32 s35, s35, 984\n" \
  "s_lshl_b32 s35, s35, 8\n" \
  "s_add_u32 m0, s5, 0\n" \
  "s_add_u32 s58, s94, s35\n" \
  "s_addc_u32 s59, s95, 0\n" \
  "global_load_lds_dwordx4 v244, s[58:59]\n" \
  "global_load_lds_dwordx4 v245, s[58:59] offset:1024\n" \
  "s_add_u32 s35, s4, 48\n" \
  "s_min_u32 s35, s35, 984\n" \
  "s_lshl_b32 s35, s35, 8\n" \
  "s_add_u32 m0, s5, 2048\n" \
  "s_add_u32 s58, s94, s35\n" \
  "s_addc_u32 s59, s95, 0\n" \
  "global_load_lds_dwordx4 v244, s[58:59]\n" \
  "global_load_lds_dwordx4 v245, s[58:59] offset:1024\n" \
  "s_waitcnt vmcnt(0)\n" \
  "ds_read_b128 v[40:43], v246 offset:0\n" \
  "ds_read_b128 v[44:47], v242 offset:0\n" \
  "ds_read_b128 v[56:59], v246 offset:2048\n" \
  "ds_read_b128 v[60:63], v242 offset:2048\n" \
  "ds_read_b128 v[32:35], v249 offset:4160\n" \
  "ds_read_b128 v[36:39], v249 offset:4224\n" \
  "ds_read_b128 v[48:51], v249 offset:6240\n" \
  "ds_read_b128 v[52:55], v249 offset:6304\n" \
  "s_waitcnt lgkmcnt(4)\n" \
  "s_add_u32 s40, s92, s6\n" \
  "s_addc_u32 s41, s93, 0\n" \
  "s_add_u32 s6, s6, 2097152\n" \
  "s_mov_b32 m0, s96\n" \
  "s_nop 0\n" \
  "global_load_lds_dwordx4 v189, s[40:41]\n" \
  "global_load_lds_dwordx4 v194, s[40:41] offset:1024\n" \
  "global_load_dwordx2 v[196:197], v227, s[42:43] offset:-3200\n" \
  "global_load_dwordx2 v[198:199], v227, s[42:43] offset:-2800\n" \
  "global_load_dwordx2 v[200:201], v227, s[42:43] offset:-2400\n" \
  "global_load_dwordx2 v[202:203], v227, s[42:43] offset:-1600\n" \
  "global_load_dwordx2 v[204:205], v227, s[42:43] offset:-1200\n" \
  "global_load_dwordx2 v[206:207], v227, s[42:43] offset:-800\n" \
  "global_load_dwordx2 v[208:209], v227, s[42:43] offset:-400\n" \
  "global_load_dwordx2 v[210:211], v227, s[42:43] offset:0\n" \
  "global_load_dwordx2 v[212:213], v227, s[42:43] offset:400\n" \
  "global_load_dwordx2 v[214:215], v227, s[42:43] offset:800\n" \
  "global_load_dwordx2 v[216:217], v227, s[42:43] offset:1200\n" \
  "global_load_dwordx2 v[218:219], v227, s[42:43] offset:-2000\n" \
  "global_load_dwordx2 v[220:221], v227, s[42:43] offset:1600\n" \
  "global_load_dwordx2 v[222:223], v227, s[42:43] offset:2000\n" \
  "global_load_dwordx2 v[224:225], v227, s[42:43] offset:2400\n" \
  "global_load_dwordx2 v[226:227], v227, s[42:43] offset:2800\n" \
  "s_add_u32 s35, s4, 64\n" \
  "s_min_u32 s35, s35, 984\n" \
  "s_lshl_b32 s35, s35, 8\n" \
  "s_add_u32 m0, s5, 0\n" \
  "s_add_u32 s58, s94, s35\n" \
  "s_addc_u32 s59, s95, 0\n" \
  "global_load_lds_dwordx4 v244, s[58:59]\n" \
  "global_load_lds_dwordx4 v245, s[58:59] offset:1024\n" \
  "s_add_u32 s35, s4, 80\n" \
  "s_min_u32 s35, s35, 984\n" \
  "s_lshl_b32 s35, s35, 8\n" \
  "s_add_u32 m0, s5, 2048\n" \
  "s_add_u32 s58, s94, s35\n" \
  "s_addc_u32 s59, s95, 0\n" \
  "global_load_lds_dwordx4 v244, s[58:59]\n" \
  "global_load_lds_dwordx4 v245, s[58:59] offset:1024\n" \
  "s_waitcnt vmcnt(0)\n" \
  "ds_read_b128 v[72:75], v246 offset:0\n" \
  "ds_read_b128 v[76:79], v242 offset:0\n" \
  "ds_read_b128 v[88:91], v246 offset:2048\n" \
  "ds_read_b128 v[92:95], v242 offset:2048\n" \
  "ds_read_b128 v[64:67], v249 offset:8320\n" \
  "ds_read_b128 v[68:71], v249 offset:8384\n" \
  "ds_read_b128 v[80:83], v249 offset:10400\n" \
  "ds_read_b128 v[84:87], v249 offset:10464\n" \
  "s_waitcnt lgkmcnt(4)\n" \
  "s_add_u32 s35, s4, 96\n" \
  "s_min_u32 s35, s35, 984\n" \
  "s_lshl_b32 s35, s35, 8\n" \
  "s_add_u32 m0, s5, 0\n" \
  "s_add_u32 s58, s94, s35\n" \
  "s_addc_u32 s59, s95, 0\n" \
  "global_load_lds_dwordx4 v244, s[58:59]\n" \
  "global_load_lds_dwordx4 v245, s[58:59] offset:1024\n" \
  "s_add_u32 s35, s4, 112\n" \
  "s_min_u32 s35, s35, 984\n" \
  "s_lshl_b32 s35, s35, 8\n" \
  "s_add_u32 m0, s5, 2048\n" \
  "s_add_u32 s58, s94, s35\n" \
  "s_addc_u32 s59, s95, 0\n" \
  "global_load_lds_dwordx4 v244, s[58:59]\n" \
  "global_load_lds_dwordx4 v245, s[58:59] offset:1024\n" \
  "s_lshl2_add_u32 s35, s26, s28\n" \
  "s_mul_i32 s35, s35, 6400\n" \
  "s_add_u32 s35, s35, 0\n" \
  "s_add_u32 s58, s14, s35\n" \
  "s_addc_u32 s59, s15, 0\n" \
  "s_mov_b32 exec_lo, 0xffffffff\n" \
  "s_mov_b32 exec_hi, 0x3ffff\n" \
  "global_load_dword v248, v247, s[58:59]\n" \
  "s_mov_b64 exec, -1\n" \
  "s_lshl2_add_u32 s35, s26, s28\n" \
  "s_mul_i32 s35, s35, 6400\n" \
  "s_add_u32 s35, s35, 6553600\n" \
  "s_add_u32 s58, s14, s35\n" \
  "s_addc_u32 s59, s15, 0\n" \
  "s_mov_b32 exec_lo, 0xffffffff\n" \
  "s_mov_b32 exec_hi, 0x3ffff\n" \
  "global_load_dword v248, v247, s[58:59]\n" \
  "s_mov_b64 exec, -1\n" \
  "s_getpc_b64 s[58:59]\n" \
  "LB_pc:\n" \
  "s_sub_u32 s58, s58, LB_pc-LZ_first\n" \
  "s_subb_u32 s59, s59, 0\n" \
  "s_mul_i32 s35, s28, 7168\n" \
  "v_lshl_add_u32 v195, v255, 7, s35\n" \
  "v_min_u32 v195, LZ_end-LZ_first, v195\n" \
  "s_mov_b32 exec_hi, 0xffffff\n" \
  "global_load_dword v248, v195, s[58:59]\n" \
  "s_mov_b64 exec, -1\n" \
  "s_waitcnt vmcnt(3)\n" \
  "ds_read_b128 v[104:107], v246 offset:0\n" \
  "ds_read_b128 v[108:111], v242 offset:0\n" \
  "ds_read_b128 v[120:123], v246 offset:2048\n" \
  "ds_read_b128 v[124:127], v242 offset:2048\n" \
  "ds_read_b128 v[96:99], v250 offset:0\n" \
  "ds_read_b128 v[100:103], v250 offset:64\n" \
  "ds_read_b128 v[112:115], v251 offset:0\n" \
  "ds_read_b128 v[116:119], v251 offset:64\n" \
  "s_waitcnt lgkmcnt(4)\n" \
  "s_waitcnt vmcnt(0)\n" \
  "s_sleep 8\n" \
  "s_setprio 2\n" \
  "s_mul_i32 s36, s28, 2176\n" \
  "s_add_u32 s36, s36, 154624\n" \
  "v_bfe_u32 v228, v255, 3, 1\n" \
  "v_and_b32 v229, 1, v255\n" \
  "v_lshl_add_u32 v230, v228, 1, v229\n" \
  "v_lshl_add_u32 v231, v229, 1, v228\n" \
  "v_xor_b32 v229, 1, v230\n" \
  "s_mov_b32 vcc_lo, 0xcccccccc\n" \
  "s_mov_b32 vcc_hi, vcc_lo\n" \
  "v_cndmask_b32 v228, v230, v231, vcc\n" \
  "v_cndmask_b32 v229, v229, v231, vcc\n" \
  "v_and_b32 v230, 4, v255\n" \
  "v_mul_u32_u24 v230, 272, v230\n" \
  "v_lshl_add_u32 v230, v253, 2, v230\n" \
  "v_and_or_b32 v230, v255, 2, v230\n" \
  "v_add_u32 v230, s36, v230\n" \
  "s_movk_i32 s35, 272\n" \
  "v_mad_u32_u24 v195, v228, s35, v230\n" \
  "v_mad_u32_u24 v235, v229, s35, v230\n" \
  "v_lshl_add_u32 v230, v253, 4, v238\n" \
  "v_add_u32 v254, s36, v230\n" \
  "v_readfirstlane_b32 s39, v193\n" \
  "s_lshl_b32 s35, s26, 2\n" \
  "s_add_u32 s39, s39, 1\n" \
  "v_mov_b32 v193, s35\n" \
  "v_mov_b64 v[240:241], -1\n" \
  "v_mov_b64 v[242:243], -1\n" \
  "s_waitcnt lgkmcnt(0)\n" \
  "s_mov_b64 exec, 1\n" \
  "ds_add_u32 v190, v191 offset:0\n" \
  "s_mov_b64 exec, -1\n" \
  "L7_:\n" \
  "ds_read_b32 v192, v190 offset:0\n" \
  "s_waitcnt lgkmcnt(0)\n" \
  "v_readfirstlane_b32 s35, v192\n" \
  "s_cmp_lt_u32 s35, 4\n" \
  "s_cbranch_scc1 L7_\n" \
  "s_add_u32 s96, s96, 8192\n" \
  "s_add_u32 s40, s92, s6\n" \
  "s_addc_u32 s41, s93, 0\n" \
  "s_add_u32 s6, s6, 2097152\n" \
  "s_mov_b32 m0, s96\n" \
  "s_nop 0\n" \
  "global_load_lds_dwordx4 v189, s[40:41]\n" \
  "global_load_lds_dwordx4 v194, s[40:41] offset:1024\n" \
  "s_sub_u32 s96, s96, 8192\n" \
  "ds_read_b64_tr_b16 v[128:129], v184 offset:0\n" \
  "ds_read_b64_tr_b16 v[130:131], v184 offset:256\n" \
  "ds_read_b64_tr_b16 v[132:133], v184 offset:2048\n" \
  "ds_read_b64_tr_b16 v[134:135], v184 offset:2304\n" \
  "ds_read_b64_tr_b16 v[136:137], v184 offset:4096\n" \
  "ds_read_b64_tr_b16 v[138:139], v184 offset:4352\n" \
  "ds_read_b64_tr_b16 v[140:141], v184 offset:6144\n" \
  "ds_read_b64_tr_b16 v[142:143], v184 offset:6400\n" \
  "ds_read_b64_tr_b16 v[156:157], v185 offset:6144\n" \
  "ds_read_b64_tr_b16 v[158:159], v185 offset:6400\n" \
  "ds_read_b64_tr_b16 v[152:153], v185 offset:4096\n" \
  "ds_read_b64_tr_b16 v[154:155], v185 offset:4352\n" \
  "ds_read_b64_tr_b16 v[148:149], v185 offset:2048\n" \
  "ds_read_b64_tr_b16 v[150:151], v185 offset:2304\n" \
  "ds_read_b64_tr_b16 v[144:145], v185 offset:0\n" \
  "ds_read_b64_tr_b16 v[146:147], v185 offset:256\n" \
  "v_mov_b32 v192, s39\n" \
  "s_cmp_lg_u32 s28, 0\n" \
  "s_cbranch_scc1 LB_nc\n" \
  "s_mov_b64 exec, 1\n" \
  "global_store_dword v193, v192, s[48:49] offset:0 sc1\n" \
  "s_mov_b64 exec, -1\n" \
  "LB_nc:\n" \
  "s_lshl2_add_u32 s35, s7, s28\n" \
  "s_lshl_b32 s35, s35, 7\n" \
  "s_lshl_b32 s36, s31, 16\n" \
  "s_add_u32 s35, s35, s36\n" \
  "s_add_u32 s35, s35, 109871104\n" \
  "s_add_u32 s58, s24, s35\n" \
  "s_addc_u32 s59, s25, 0\n" \
  "s_mov_b64 exec, 0xff\n" \
  "global_store_dwordx4 v238, v[240:243], s[58:59] sc1\n" \
  "s_mov_b64 exec, -1\n" \
  "s_cmp_lg_u32 s33, 1021\n" \
  "s_cbranch_scc1 LB_nm\n" \
  "v_mov_b64 v[240:241], 0\n" \
  "v_mov_b64 v[242:243], 0\n" \
  "global_store_dwordx4 v238, v[240:243], s[48:49] offset:2048 sc1\n" \
  "s_nop 1\n" \
  "v_lshlrev_b32 v242, 8, v255\n" \
  "global_store_dwordx2 v242, v[240:241], s[48:49] offset:3072 sc1\n" \
  "v_add_u32 v242, 0x4000, v242\n" \
  "global_store_dwordx2 v242, v[240:241], s[48:49] offset:3072 sc1\n" \
  "v_add_u32 v242, 0x4000, v242\n" \
  "global_store_dwordx2 v242, v[240:241], s[48:49] offset:3072 sc1\n" \
  "v_add_u32 v242, 0x4000, v242\n" \
  "global_store_dwordx2 v242, v[240:241], s[48:49] offset:3072 sc1\n" \
  "v_add_u32 v242, 0x4000, v242\n" \
  "LB_nm:\n" \
  "s_mov_b32 s86, 0x78000b0\n" \
  "s_mov_b32 s87, 0x0\n" \
  "s_or_b32 s86, s86, s87\n" \
  "s_setprio 2\n" \
  "s_memtime s[90:91]\n" \
  "s_memrealtime s[42:43]\n" \
  "LT:\n" \
  "s_cmp_eq_u32 s38, 6\n" \
  "s_cselect_b32 s78, 0x33333333, -1\n" \
  "s_cmp_eq_u32 s38, 7\n" \
  "s_cselect_b32 s78, 0x33333333, s78\n" \
  "s_cmp_eq_u32 s38, 18\n" \
  "s_cselect_b32 s80, 0x33333333, -1\n" \
  "s_cmp_eq_u32 s38, 19\n" \
  "s_cselect_b32 s80, 0x33333333, s80\n" \
  "s_mov_b32 s79, s78\n" \
  "s_mov_b32 s81, s80\n" \
  "s_mov_b32 s37, s38\n" \
  "s_add_u32 s57, s38, 2\n" \
  "s_add_u32 s3, s38, 4\n" \
  "s_add_u32 s38, s38, 6\n" \
  "s_cmp_lt_u32 s57, 32\n" \
  "s_cselect_b32 s57, s57, 37\n" \
  "s_cselect_b32 s3, s3, 37\n" \
  "s_waitcnt lgkmcnt(12)\n" \
  "v_mfma_f32_16x16x32_bf16 v[160:163], v[128:131], v[0:3], 0\n" \
  "s_mov_b64 vcc, s[78:79]\n" \
  "v_cndmask_b32_dpp v234, v234, v234, vcc row_ror:8 row_mask:0xf bank_mask:0xf\n" \
  "v_mfma_f32_16x16x32_bf16 v[160:163], v[132:135], v[4:7], v[160:163]\n" \
  "s_waitcnt lgkmcnt(8)\n" \
  "v_mfma_f32_16x16x32_bf16 v[160:163], v[136:139], v[8:11], v[160:163]\n" \
  "s_set_gpr_idx_on s37, gpr_idx(SRC0)\n" \
  "v_mad_u32_u24 v232, v196, s52, v236\n" \
  "s_set_gpr_idx_off\n" \
  "v_mfma_f32_16x16x32_bf16 v[160:163], v[140:143], v[12:15], v[160:163]\n" \
  "s_waitcnt lgkmcnt(0)\n" \
  "v_lshrrev_b32 v228, v232, v234\n" \
  "v_lshrrev_b32_dpp v229, v232, v234 quad_perm:[2,3,0,1] row_mask:0xf bank_mask:0xf\n" \
  "v_mfma_f32_16x16x32_bf16 v[164:167], v[156:159], v[12:15], 0\n" \
  "v_lshrrev_b32_dpp v230, v232, v234 quad_perm:[1,2,3,0] row_mask:0xf bank_mask:0xf\n" \
  "v_lshrrev_b32_dpp v231, v232, v234 quad_perm:[3,0,1,2] row_mask:0xf bank_mask:0xf\n" \
  "v_mfma_f32_16x16x32_bf16 v[164:167], v[152:155], v[8:11], v[164:167]\n" \
  "v_perm_b32 v228, v229, v228, s53\n" \
  "v_perm_b32 v230, v231, v230, s54\n" \
  "v_mfma_f32_16x16x32_bf16 v[164:167], v[148:151], v[4:7], v[164:167]\n" \
  "v_bitop3_b32 v228, v228, v230, s55 bitop3:0xa8\n" \
  "v_lshl_or_b32 v229, v234, 1, s56\n" \
  "v_mfma_f32_16x16x32_bf16 v[164:167], v[144:147], v[0:3], v[164:167]\n" \
  "v_add_u32_dpp v228, v228, v228 quad_perm:[2,3,0,1] row_mask:0xf bank_mask:0xf\n" \
  "v_min_u32_sdwa v230, v228, v228 dst_sel:DWORD dst_unused:UNUSED_PAD src0_sel:BYTE_0 src1_sel:BYTE_1\n" \
  "v_mfma_f32_16x16x32_bf16 v[172:175], v[144:147], v[16:19], 0\n" \
  "v_min_u32_sdwa v231, v228, v228 dst_sel:DWORD dst_unused:UNUSED_PAD src0_sel:BYTE_2 src1_sel:BYTE_3\n" \
  "v_sub_u32 v230, v230, v231\n" \
  "v_mfma_f32_16x16x32_bf16 v[172:175], v[148:151], v[20:23], v[172:175]\n" \
  "v_cvt_pk_bf16_f32 v176, v160, v161\n" \
  "v_add_u32_dpp v230, v230, v230 quad_perm:[1,0,3,2] row_mask:0xf bank_mask:0xf\n" \
  "v_mfma_f32_16x16x32_bf16 v[172:175], v[152:155], v[24:27], v[172:175]\n" \
  "v_cmp_ge_i32 vcc, 0, v230\n" \
  "v_cvt_pk_bf16_f32 v177, v162, v163\n" \
  "v_mfma_f32_16x16x32_bf16 v[172:175], v[156:159], v[28:31], v[172:175]\n" \
  "v_cndmask_b32_sdwa v228, v228, v228, vcc dst_sel:DWORD dst_unused:UNUSED_PAD src0_sel:WORD_1 src1_sel:WORD_0\n" \
  "v_cvt_pk_bf16_f32 v178, v164, v165\n" \
  "v_mfma_f32_16x16x32_bf16 v[168:171], v[140:143], v[28:31], 0\n" \
  "s_mov_b64 s[50:51], vcc\n" \
  "v_cmp_le_u32_sdwa vcc, v228, v228 src0_sel:BYTE_0 src1_sel:BYTE_1\n" \
  "v_cvt_pk_bf16_f32 v179, v166, v167\n" \
  "v_mfma_f32_16x16x32_bf16 v[168:171], v[136:139], v[24:27], v[168:171]\n" \
  "v_cndmask_b32_dpp v232, v232, v232, vcc quad_perm:[2,3,0,1] row_mask:0xf bank_mask:0xf\n" \
  "v_mfma_f32_16x16x32_bf16 v[168:171], v[132:135], v[20:23], v[168:171]\n" \
  "s_mov_b64 vcc, s[50:51]\n" \
  "v_cndmask_b32_dpp v232, v232, v232, vcc quad_perm:[3,0,1,2] row_mask:0xf bank_mask:0xf\n" \
  "v_lshlrev_b32_e64 v230, v232, 3\n" \
  "v_mfma_f32_16x16x32_bf16 v[168:171], v[128:131], v[16:19], v[168:171]\n" \
  "v_and_or_b32 v234, v229, v230, v234\n" \
  "s_cmp_lg_u32 s33, 1021\n" \
  "s_cbranch_scc0 LT_e\n" \
  "LT_r:\n" \
  "s_waitcnt vmcnt(5)\n" \
  "global_store_dwordx4 v186, v[176:179], s[44:45] offset:0 sc1\n" \
  "v_mfma_f32_16x16x32_bf16 v[160:163], v[128:131], v[32:35], 0\n" \
  "s_set_gpr_idx_on s37, gpr_idx(DST)\n" \
  "v_mov_b32 v196, v232\n" \
  "s_set_gpr_idx_off\n" \
  "v_mfma_f32_16x16x32_bf16 v[160:163], v[132:135], v[36:39], v[160:163]\n" \
  "s_set_gpr_idx_on s57, gpr_idx(SRC0)\n" \
  "v_mad_u32_u24 v232, v196, s52, v236\n" \
  "s_set_gpr_idx_off\n" \
  "v_mfma_f32_16x16x32_bf16 v[160:163], v[136:139], v[40:43], v[160:163]\n" \
  "v_lshrrev_b32 v228, v232, v234\n" \
  "v_lshrrev_b32_dpp v229, v232, v234 quad_perm:[2,3,0,1] row_mask:0xf bank_mask:0xf\n" \
  "v_mfma_f32_16x16x32_bf16 v[160:163], v[140:143], v[44:47], v[160:163]\n" \
  "v_lshrrev_b32_dpp v230, v232, v234 quad_perm:[1,2,3,0] row_mask:0xf bank_mask:0xf\n" \
  "v_lshrrev_b32_dpp v231, v232, v234 quad_perm:[3,0,1,2] row_mask:0xf bank_mask:0xf\n" \
  "v_mfma_f32_16x16x32_bf16 v[164:167], v[156:159], v[44:47], 0\n" \
  "v_perm_b32 v228, v229, v228, s53\n" \
  "v_perm_b32 v230, v231, v230, s54\n" \
  "v_mfma_f32_16x16x32_bf16 v[164:167], v[152:155], v[40:43], v[164:167]\n" \
  "v_bitop3_b32 v228, v228, v230, s55 bitop3:0xa8\n" \
  "v_lshl_or_b32 v229, v234, 1, s56\n" \
  "v_mfma_f32_16x16x32_bf16 v[164:167], v[148:151], v[36:39], v[164:167]\n" \
  "v_cvt_pk_bf16_f32 v182, v172, v173\n" \
  "v_cvt_pk_bf16_f32 v183, v174, v175\n" \
  "v_mfma_f32_16x16x32_bf16 v[164:167], v[144:147], v[32:35], v[164:167]\n" \
  "v_cvt_pk_bf16_f32 v180, v168, v169\n" \
  "v_cvt_pk_bf16_f32 v181, v170, v171\n" \
  "s_waitcnt vmcnt(5)\n" \
  "global_store_dwordx4 v186, v[180:183], s[44:45] offset:256 sc1\n" \
  "v_mfma_f32_16x16x32_bf16 v[172:175], v[144:147], v[48:51], 0\n" \
  "v_add_u32_dpp v228, v228, v228 quad_perm:[2,3,0,1] row_mask:0xf bank_mask:0xf\n" \
  "v_min_u32_sdwa v230, v228, v228 dst_sel:DWORD dst_unused:UNUSED_PAD src0_sel:BYTE_0 src1_sel:BYTE_1\n" \
  "v_mfma_f32_16x16x32_bf16 v[172:175], v[148:151], v[52:55], v[172:175]\n" \
  "v_min_u32_sdwa v231, v228, v228 dst_sel:DWORD dst_unused:UNUSED_PAD src0_sel:BYTE_2 src1_sel:BYTE_3\n" \
  "v_sub_u32 v230, v230, v231\n" \
  "v_mfma_f32_16x16x32_bf16 v[172:175], v[152:155], v[56:59], v[172:175]\n" \
  "v_cvt_pk_bf16_f32 v176, v160, v161\n" \
  "v_add_u32_dpp v230, v230, v230 quad_perm:[1,0,3,2] row_mask:0xf bank_mask:0xf\n" \
  "v_mfma_f32_16x16x32_bf16 v[172:175], v[156:159], v[60:63], v[172:175]\n" \
  "v_cmp_ge_i32 vcc, 0, v230\n" \
  "v_cvt_pk_bf16_f32 v177, v162, v163\n" \
  "v_mfma_f32_16x16x32_bf16 v[168:171], v[140:143], v[60:63], 0\n" \
  "v_cndmask_b32_sdwa v228, v228, v228, vcc dst_sel:DWORD dst_unused:UNUSED_PAD src0_sel:WORD_1 src1_sel:WORD_0\n" \
  "v_cvt_pk_bf16_f32 v178, v164, v165\n" \
  "v_mfma_f32_16x16x32_bf16 v[168:171], v[136:139], v[56:59], v[168:171]\n" \
  "s_mov_b64 s[50:51], vcc\n" \
  "v_cmp_le_u32_sdwa vcc, v228, v228 src0_sel:BYTE_0 src1_sel:BYTE_1\n" \
  "v_cvt_pk_bf16_f32 v179, v166, v167\n" \
  "v_mfma_f32_16x16x32_bf16 v[168:171], v[132:135], v[52:55], v[168:171]\n" \
  "v_cndmask_b32_dpp v232, v232, v232, vcc quad_perm:[2,3,0,1] row_mask:0xf bank_mask:0xf\n" \
  "v_mfma_f32_16x16x32_bf16 v[168:171], v[128:131], v[48:51], v[168:171]\n" \
  "s_mov_b64 vcc, s[50:51]\n" \
  "v_cndmask_b32_dpp v232, v232, v232, vcc quad_perm:[3,0,1,2] row_mask:0xf bank_mask:0xf\n" \
  "v_lshlrev_b32_e64 v230, v232, 3\n" \
  "s_waitcnt vmcnt(5)\n" \
  "global_store_dwordx4 v186, v[176:179], s[44:45] offset:512 sc1\n" \
  "v_mfma_f32_16x16x32_bf16 v[160:163], v[128:131], v[64:67], 0\n" \
  "v_and_or_b32 v234, v229, v230, v234\n" \
  "v_cvt_pk_bf16_f32 v182, v172, v173\n" \
  "v_mfma_f32_16x16x32_bf16 v[160:163], v[132:135], v[68:71], v[160:163]\n" \
  "s_set_gpr_idx_on s57, gpr_idx(DST)\n" \
  "v_mov_b32 v196, v232\n" \
  "s_set_gpr_idx_off\n" \
  "v_mfma_f32_16x16x32_bf16 v[160:163], v[136:139], v[72:75], v[160:163]\n" \
  "s_mov_b64 vcc, s[80:81]\n" \
  "v_cndmask_b32_dpp v234, v234, v234, vcc row_ror:8 row_mask:0xf bank_mask:0xf\n" \
  "v_cvt_pk_bf16_f32 v183, v174, v175\n" \
  "v_mfma_f32_16x16x32_bf16 v[160:163], v[140:143], v[76:79], v[160:163]\n" \
  "s_set_gpr_idx_on s3, gpr_idx(SRC0)\n" \
  "v_mad_u32_u24 v232, v196, s52, v236\n" \
  "s_set_gpr_idx_off\n" \
  "v_mfma_f32_16x16x32_bf16 v[164:167], v[156:159], v[76:79], 0\n" \
  "v_lshrrev_b32 v228, v232, v234\n" \
  "v_lshrrev_b32_dpp v229, v232, v234 quad_perm:[2,3,0,1] row_mask:0xf bank_mask:0xf\n" \
  "v_mfma_f32_16x16x32_bf16 v[164:167], v[152:155], v[72:75], v[164:167]\n" \
  "v_lshrrev_b32_dpp v230, v232, v234 quad_perm:[1,2,3,0] row_mask:0xf bank_mask:0xf\n" \
  "v_lshrrev_b32_dpp v231, v232, v234 quad_perm:[3,0,1,2] row_mask:0xf bank_mask:0xf\n" \
  "v_mfma_f32_16x16x32_bf16 v[164:167], v[148:151], v[68:71], v[164:167]\n" \
  "v_perm_b32 v228, v229, v228, s53\n" \
  "v_perm_b32 v230, v231, v230, s54\n" \
  "v_mfma_f32_16x16x32_bf16 v[164:167], v[144:147], v[64:67], v[164:167]\n" \
  "v_cvt_pk_bf16_f32 v180, v168, v169\n" \
  "v_cvt_pk_bf16_f32 v181, v170, v171\n" \
  "s_waitcnt vmcnt(5)\n" \
  "global_store_dwordx4 v186, v[180:183], s[44:45] offset:768 sc1\n" \
  "v_mfma_f32_16x16x32_bf16 v[172:175], v[144:147], v[80:83], 0\n" \
  "s_waitcnt vmcnt(5)\n" \
  "s_mov_b64 exec, 1\n" \
  "ds_add_u32 v190, v191 offset:0\n" \
  "s_mov_b64 exec, -1\n" \
  "v_mfma_f32_16x16x32_bf16 v[172:175], v[148:151], v[84:87], v[172:175]\n" \
  "v_bitop3_b32 v228, v228, v230, s55 bitop3:0xa8\n" \
  "v_lshl_or_b32 v229, v234, 1, s56\n" \
  "v_mfma_f32_16x16x32_bf16 v[172:175], v[152:155], v[88:91], v[172:175]\n" \
  "v_add_u32_dpp v228, v228, v228 quad_perm:[2,3,0,1] row_mask:0xf bank_mask:0xf\n" \
  "v_min_u32_sdwa v230, v228, v228 dst_sel:DWORD dst_unused:UNUSED_PAD src0_sel:BYTE_0 src1_sel:BYTE_1\n" \
  "v_mfma_f32_16x16x32_bf16 v[172:175], v[156:159], v[92:95], v[172:175]\n" \
  "v_min_u32_sdwa v231, v228, v228 dst_sel:DWORD dst_unused:UNUSED_PAD src0_sel:BYTE_2 src1_sel:BYTE_3\n" \
  "v_sub_u32 v230, v230, v231\n" \
  "v_mfma_f32_16x16x32_bf16 v[168:171], v[140:143], v[92:95], 0\n" \
  "v_cvt_pk_bf16_f32 v176, v160, v161\n" \
  "v_add_u32_dpp v230, v230, v230 quad_perm:[1,0,3,2] row_mask:0xf bank_mask:0xf\n" \
  "v_mfma_f32_16x16x32_bf16 v[168:171], v[136:139], v[88:91], v[168:171]\n" \
  "v_cmp_ge_i32 vcc, 0, v230\n" \
  "v_cvt_pk_bf16_f32 v177, v162, v163\n" \
  "v_mfma_f32_16x16x32_bf16 v[168:171], v[132:135], v[84:87], v[168:171]\n" \
  "v_cndmask_b32_sdwa v228, v228, v228, vcc dst_sel:DWORD dst_unused:UNUSED_PAD src0_sel:WORD_1 src1_sel:WORD_0\n" \
  "v_cvt_pk_bf16_f32 v178, v164, v165\n" \
  "v_mfma_f32_16x16x32_bf16 v[168:171], v[128:131], v[80:83], v[168:171]\n" \
  "s_mov_b64 s[50:51], vcc\n" \
  "v_cmp_le_u32_sdwa vcc, v228, v228 src0_sel:BYTE_0 src1_sel:BYTE_1\n" \
  "v_cvt_pk_bf16_f32 v179, v166, v167\n" \
  "s_cmp_eq_u32 s98, 4\n" \
  "s_cselect_b32 s36, 12451840, 0\n" \
  "s_sub_u32 s6, s6, s36\n" \
  "s_cmp_lt_u32 s98, 10\n" \
  "s_cselect_b32 s6, s6, s34\n" \
  "s_add_u32 s40, s92, s6\n" \
  "s_addc_u32 s41, s93, 0\n" \
  "s_add_u32 s6, s6, 2097152\n" \
  "s_waitcnt vmcnt(5)\n" \
  "global_store_dwordx4 v186, v[176:179], s[44:45] offset:1024 sc1\n" \
  "v_mfma_f32_16x16x32_bf16 v[160:163], v[128:131], v[96:99], 0\n" \
  "ds_read_b32 v192, v190\n" \
  "v_add_u32 v184, s97, v184\n" \
  "v_mfma_f32_16x16x32_bf16 v[160:163], v[132:135], v[100:103], v[160:163]\n" \
  "v_add_u32 v185, s97, v185\n" \
  "v_cndmask_b32_dpp v232, v232, v232, vcc quad_perm:[2,3,0,1] row_mask:0xf bank_mask:0xf\n" \
  "v_mfma_f32_16x16x32_bf16 v[160:163], v[136:139], v[104:107], v[160:163]\n" \
  "s_mov_b64 vcc, s[50:51]\n" \
  "v_cndmask_b32_dpp v232, v232, v232, vcc quad_perm:[3,0,1,2] row_mask:0xf bank_mask:0xf\n" \
  "v_lshlrev_b32_e64 v230, v232, 3\n" \
  "v_mfma_f32_16x16x32_bf16 v[160:163], v[140:143], v[108:111], v[160:163]\n" \
  "v_and_or_b32 v234, v229, v230, v234\n" \
  "v_cvt_pk_bf16_f32 v182, v172, v173\n" \
  "v_mfma_f32_16x16x32_bf16 v[164:167], v[156:159], v[108:111], 0\n" \
  "s_set_gpr_idx_on s3, gpr_idx(DST)\n" \
  "v_mov_b32 v196, v232\n" \
  "s_set_gpr_idx_off\n" \
  "v_mfma_f32_16x16x32_bf16 v[164:167], v[152:155], v[104:107], v[164:167]\n" \
  "s_waitcnt lgkmcnt(0)\n" \
  "v_readfirstlane_b32 s35, v192\n" \
  "v_cvt_pk_bf16_f32 v183, v174, v175\n" \
  "v_mfma_f32_16x16x32_bf16 v[164:167], v[148:151], v[100:103], v[164:167]\n" \
  "v_cvt_pk_bf16_f32 v180, v168, v169\n" \
  "v_cvt_pk_bf16_f32 v181, v170, v171\n" \
  "v_mfma_f32_16x16x32_bf16 v[164:167], v[144:147], v[96:99], v[164:167]\n" \
  "s_cmp_lt_u32 s35, s99\n" \
  "s_cbranch_scc1 LT_w\n" \
  "LT_c:\n" \
  "s_mov_b32 m0, s96\n" \
  "s_nop 0\n" \
  "global_load_lds_dwordx4 v189, s[40:41]\n" \
  "global_load_lds_dwordx4 v194, s[40:41] offset:1024\n" \
  "s_add_u32 s96, s96, s97\n" \
  "s_sub_u32 s97, 0, s97\n" \
  "s_add_u32 s99, s99, 4\n" \
  "s_lshl4_add_u32 s35, s28, s98\n" \
  "s_bitcmp1_b64 s[86:87], s35\n" \
  "s_cbranch_scc1 LT_a\n" \
  "LT_s:\n" \
  "s_waitcnt vmcnt(5)\n" \
  "global_store_dwordx4 v186, v[180:183], s[44:45] offset:1280 sc1\n" \
  "v_mfma_f32_16x16x32_bf16 v[168:171], v[128:131], v[112:115], 0\n" \
  "ds_read_b64_tr_b16 v[128:129], v184 offset:0\n" \
  "ds_read_b64_tr_b16 v[130:131], v184 offset:256\n" \
  "v_mfma_f32_16x16x32_bf16 v[168:171], v[132:135], v[116:119], v[168:171]\n" \
  "ds_read_b64_tr_b16 v[132:133], v184 offset:2048\n" \
  "ds_read_b64_tr_b16 v[134:135], v184 offset:2304\n" \
  "v_mfma_f32_16x16x32_bf16 v[168:171], v[136:139], v[120:123], v[168:171]\n" \
  "ds_read_b64_tr_b16 v[136:137], v184 offset:4096\n" \
  "v_cvt_pk_bf16_f32 v176, v160, v161\n" \
  "v_mfma_f32_16x16x32_bf16 v[168:171], v[140:143], v[124:127], v[168:171]\n" \
  "ds_read_b64_tr_b16 v[138:139], v184 offset:4352\n" \
  "v_cvt_pk_bf16_f32 v177, v162, v163\n" \
  "v_mfma_f32_16x16x32_bf16 v[172:175], v[156:159], v[124:127], 0\n" \
  "ds_read_b64_tr_b16 v[140:141], v184 offset:6144\n" \
  "v_cvt_pk_bf16_f32 v178, v164, v165\n" \
  "v_mfma_f32_16x16x32_bf16 v[172:175], v[152:155], v[120:123], v[172:175]\n" \
  "ds_read_b64_tr_b16 v[142:143], v184 offset:6400\n" \
  "v_cvt_pk_bf16_f32 v179, v166, v167\n" \
  "v_mfma_f32_16x16x32_bf16 v[172:175], v[148:151], v[116:119], v[172:175]\n" \
  "ds_read_b64_tr_b16 v[156:157], v185 offset:6144\n" \
  "ds_read_b64_tr_b16 v[158:159], v185 offset:6400\n" \
  "v_mfma_f32_16x16x32_bf16 v[172:175], v[144:147], v[112:115], v[172:175]\n" \
  "ds_read_b64_tr_b16 v[152:153], v185 offset:4096\n" \
  "ds_read_b64_tr_b16 v[154:155], v185 offset:4352\n" \
  "ds_read_b64_tr_b16 v[148:149], v185 offset:2048\n" \
  "ds_read_b64_tr_b16 v[150:151], v185 offset:2304\n" \
  "ds_read_b64_tr_b16 v[144:145], v185 offset:0\n" \
  "ds_read_b64_tr_b16 v[146:147], v185 offset:256\n" \
  "s_waitcnt vmcnt(5)\n" \
  "global_store_dwordx4 v187, v[176:179], s[44:45] offset:0 sc1\n" \
  "s_add_u32 s98, s98, 1\n" \
  "v_cvt_pk_bf16_f32 v180, v168, v169\n" \
  "v_cvt_pk_bf16_f32 v181, v170, v171\n" \
  "v_cvt_pk_bf16_f32 v182, v172, v173\n" \
  "v_cvt_pk_bf16_f32 v183, v174, v175\n" \
  "s_waitcnt vmcnt(5)\n" \
  "global_store_dwordx4 v188, v[180:183], s[44:45] offset:0 sc1\n" \
  "s_add_u32 s44, s44, 128000\n" \
  "s_addc_u32 s45, s45, 0\n" \
  "s_bitcmp0_b32 4160, s98\n" \
  "s_cbranch_scc1 LT\n" \
  "s_cmp_eq_u32 s98, 6\n" \
  "s_cbranch_scc1 LT_m\n" \
  "s_lshr_b32 s35, s30, 2\n" \
  "s_lshl3_add_u32 s35, s29, s35\n" \
  "s_mul_i32 s35, s35, 1600000\n" \
  "s_lshl_b32 s36, s28, 12\n" \
  "s_add_u32 s35, s35, s36\n" \
  "s_add_u32 s40, s24, s35\n" \
  "s_addc_u32 s41, s25, 0\n" \
  "s_bitcmp1_b32 s30, 2\n" \
  "s_cbranch_scc1 LB_l_h\n" \
  "global_load_dwordx4 v[64:67], v238, s[40:41] offset:0 sc1\n" \
  "global_load_dwordx4 v[68:71], v238, s[40:41] offset:1024 sc1\n" \
  "global_load_dwordx4 v[72:75], v238, s[40:41] offset:2048 sc1\n" \
  "global_load_dwordx4 v[76:79], v238, s[40:41] offset:3072 sc1\n" \
  "s_add_u32 s40, s40, 16000\n" \
  "s_addc_u32 s41, s41, 0\n" \
  "global_load_dwordx4 v[80:83], v238, s[40:41] offset:0 sc1\n" \
  "global_load_dwordx4 v[84:87], v238, s[40:41] offset:1024 sc1\n" \
  "global_load_dwordx4 v[88:91], v238, s[40:41] offset:2048 sc1\n" \
  "global_load_dwordx4 v[92:95], v238, s[40:41] offset:3072 sc1\n" \
  "s_add_u32 s40, s40, 16000\n" \
  "s_addc_u32 s41, s41, 0\n" \
  "global_load_dwordx4 v[96:99], v238, s[40:41] offset:0 sc1\n" \
  "global_load_dwordx4 v[100:103], v238, s[40:41] offset:1024 sc1\n" \
  "global_load_dwordx4 v[104:107], v238, s[40:41] offset:2048 sc1\n" \
  "global_load_dwordx4 v[108:111], v238, s[40:41] offset:3072 sc1\n" \
  "s_add_u32 s40, s40, 16000\n" \
  "s_addc_u32 s41, s41, 0\n" \
  "global_load_dwordx4 v[112:115], v238, s[40:41] offset:0 sc1\n" \
  "global_load_dwordx4 v[116:119], v238, s[40:41] offset:1024 sc1\n" \
  "global_load_dwordx4 v[120:123], v238, s[40:41] offset:2048 sc1\n" \
  "global_load_dwordx4 v[124:127], v238, s[40:41] offset:3072 sc1\n" \
  "s_add_u32 s40, s40, 16000\n" \
  "s_addc_u32 s41, s41, 0\n" \
  "s_branch LB_l_e\n" \
  "LB_l_h:\n" \
  "global_load_dwordx4 v[0:3], v238, s[40:41] offset:0 sc1\n" \
  "global_load_dwordx4 v[4:7], v238, s[40:41] offset:1024 sc1\n" \
  "global_load_dwordx4 v[8:11], v238, s[40:41] offset:2048 sc1\n" \
  "global_load_dwordx4 v[12:15], v238, s[40:41] offset:3072 sc1\n" \
  "s_add_u32 s40, s40, 16000\n" \
  "s_addc_u32 s41, s41, 0\n" \
  "global_load_dwordx4 v[16:19], v238, s[40:41] offset:0 sc1\n" \
  "global_load_dwordx4 v[20:23], v238, s[40:41] offset:1024 sc1\n" \
  "global_load_dwordx4 v[24:27], v238, s[40:41] offset:2048 sc1\n" \
  "global_load_dwordx4 v[28:31], v238, s[40:41] offset:3072 sc1\n" \
  "s_add_u32 s40, s40, 16000\n" \
  "s_addc_u32 s41, s41, 0\n" \
  "global_load_dwordx4 v[32:35], v238, s[40:41] offset:0 sc1\n" \
  "global_load_dwordx4 v[36:39], v238, s[40:41] offset:1024 sc1\n" \
  "global_load_dwordx4 v[40:43], v238, s[40:41] offset:2048 sc1\n" \
  "global_load_dwordx4 v[44:47], v238, s[40:41] offset:3072 sc1\n" \
  "s_add_u32 s40, s40, 16000\n" \
  "s_addc_u32 s41, s41, 0\n" \
  "global_load_dwordx4 v[48:51], v238, s[40:41] offset:0 sc1\n" \
  "global_load_dwordx4 v[52:55], v238, s[40:41] offset:1024 sc1\n" \
  "global_load_dwordx4 v[56:59], v238, s[40:41] offset:2048 sc1\n" \
  "global_load_dwordx4 v[60:63], v238, s[40:41] offset:3072 sc1\n" \
  "s_add_u32 s40, s40, 16000\n" \
  "s_addc_u32 s41, s41, 0\n" \
  "LB_l_e:\n" \
  "ds_write_b16_d16_hi v195, v197 offset:0\n" \
  "ds_write_b16_d16_hi v195, v199 offset:16\n" \
  "ds_write_b16_d16_hi v195, v201 offset:32\n" \
  "ds_write_b16_d16_hi v235, v203 offset:64\n" \
  "ds_write_b16_d16_hi v235, v205 offset:80\n" \
  "ds_write_b16_d16_hi v235, v207 offset:96\n" \
  "ds_write_b16_d16_hi v235, v209 offset:112\n" \
  "ds_write_b16_d16_hi v235, v211 offset:128\n" \
  "ds_write_b16_d16_hi v235, v213 offset:144\n" \
  "ds_write_b16_d16_hi v235, v215 offset:160\n" \
  "ds_write_b16_d16_hi v235, v217 offset:176\n" \
  "ds_write_b16_d16_hi v195, v219 offset:48\n" \
  "ds_write_b16_d16_hi v195, v221 offset:192\n" \
  "ds_write_b16_d16_hi v195, v223 offset:208\n" \
  "ds_write_b16_d16_hi v195, v225 offset:224\n" \
  "ds_write_b16_d16_hi v195, v227 offset:240\n" \
  "ds_read_b128 v[244:247], v254 offset:0\n" \
  "ds_read_b128 v[248:251], v254 offset:1088\n" \
  "s_lshr_b32 s35, s7, 5\n" \
  "s_mul_i32 s35, s35, 819200\n" \
  "s_and_b32 s36, s7, 31\n" \
  "s_lshl_b32 s36, s36, 10\n" \
  "s_add_u32 s35, s35, s36\n" \
  "s_lshl_b32 s36, s4, 9\n" \
  "s_add_u32 s35, s35, s36\n" \
  "s_add_u32 s35, s35, 102793216\n" \
  "s_add_u32 s58, s24, s35\n" \
  "s_addc_u32 s59, s25, 0\n" \
  "s_waitcnt lgkmcnt(0)\n" \
  "s_cmp_ge_u32 s4, 896\n" \
  "s_cbranch_scc1 L17_\n" \
  "global_store_dwordx4 v238, v[244:247], s[58:59] sc1\n" \
  "s_cmp_ge_u32 s4, 768\n" \
  "s_cbranch_scc1 L17_\n" \
  "s_add_u32 s58, s58, 32768\n" \
  "s_addc_u32 s59, s59, 0\n" \
  "global_store_dwordx4 v238, v[248:251], s[58:59] sc1\n" \
  "L17_:\n" \
  "s_lshr_b32 s35, s30, 2\n" \
  "s_lshl3_add_u32 s33, s29, s35\n" \
  "s_and_b32 s34, s30, 3\n" \
  "s_lshl_b32 s59, s27, 11\n" \
  "s_mul_i32 s35, s33, 1600000\n" \
  "s_add_u32 s54, s24, s35\n" \
  "s_addc_u32 s55, s25, 0\n" \
  "s_mul_i32 s35, s34, 819200\n" \
  "s_lshl1_add_u32 s35, s59, s35\n" \
  "s_add_u32 s35, s35, 102400000\n" \
  "s_add_u32 s56, s24, s35\n" \
  "s_addc_u32 s57, s25, 0\n" \
  "s_lshr_b32 s35, s30, 4\n" \
  "s_lshl1_add_u32 s35, s29, s35\n" \
  "s_lshl_b32 s35, s35, 6\n" \
  "s_bfe_u32 s37, s30, 0x20002\n" \
  "s_lshl3_add_u32 s35, s37, s35\n" \
  "v_and_b32 v202, 16, v255\n" \
  "v_lshl_add_u32 v202, v202, 1, s35\n" \
  "global_load_dwordx2 v[204:205], v202, s[16:17]\n" \
  "global_load_dwordx2 v[206:207], v202, s[18:19]\n" \
  "v_mov_b32 v202, v238\n" \
  "global_load_dwordx4 v[208:211], v238, s[56:57] offset:0 sc1\n" \
  "global_load_dwordx4 v[212:215], v238, s[56:57] offset:1024 sc1\n" \
  "global_load_dwordx4 v[216:219], v238, s[56:57] offset:2048 sc1\n" \
  "global_load_dwordx4 v[220:223], v238, s[56:57] offset:3072 sc1\n" \
  "s_add_u32 s56, s56, 32768\n" \
  "s_addc_u32 s57, s57, 0\n" \
  "global_load_dwordx4 v[224:227], v238, s[56:57] sc1\n" \
  "global_load_dwordx4 v[228:231], v238, s[56:57] offset:1024 sc1\n" \
  "global_load_dwordx4 v[232:235], v238, s[56:57] offset:2048 sc1\n" \
  "global_load_dwordx4 v[236:239], v238, s[56:57] offset:3072 sc1\n" \
  "s_add_u32 s56, s56, 32768\n" \
  "s_addc_u32 s57, s57, 0\n" \
  "s_bitcmp1_b32 s30, 2\n" \
  "s_cbranch_scc1 LB_t_h\n" \
  "s_waitcnt lgkmcnt(12)\n" \
  "v_mfma_f32_16x16x32_bf16 v[160:163], v[128:131], v[0:3], 0\n" \
  "v_mfma_f32_16x16x32_bf16 v[160:163], v[132:135], v[4:7], v[160:163]\n" \
  "s_waitcnt lgkmcnt(8)\n" \
  "v_mfma_f32_16x16x32_bf16 v[160:163], v[136:139], v[8:11], v[160:163]\n" \
  "v_mfma_f32_16x16x32_bf16 v[160:163], v[140:143], v[12:15], v[160:163]\n" \
  "s_waitcnt lgkmcnt(0)\n" \
  "v_mfma_f32_16x16x32_bf16 v[164:167], v[156:159], v[12:15], 0\n" \
  "v_mfma_f32_16x16x32_bf16 v[164:167], v[152:155], v[8:11], v[164:167]\n" \
  "v_mfma_f32_16x16x32_bf16 v[164:167], v[148:151], v[4:7], v[164:167]\n" \
  "v_mfma_f32_16x16x32_bf16 v[164:167], v[144:147], v[0:3], v[164:167]\n" \
  "v_mfma_f32_16x16x32_bf16 v[172:175], v[144:147], v[16:19], 0\n" \
  "v_mfma_f32_16x16x32_bf16 v[172:175], v[148:151], v[20:23], v[172:175]\n" \
  "v_mfma_f32_16x16x32_bf16 v[172:175], v[152:155], v[24:27], v[172:175]\n" \
  "v_cvt_pk_bf16_f32 v176, v160, v161\n" \
  "v_cvt_pk_bf16_f32 v177, v162, v163\n" \
  "v_mfma_f32_16x16x32_bf16 v[172:175], v[156:159], v[28:31], v[172:175]\n" \
  "v_mfma_f32_16x16x32_bf16 v[168:171], v[140:143], v[28:31], 0\n" \
  "v_mfma_f32_16x16x32_bf16 v[168:171], v[136:139], v[24:27], v[168:171]\n" \
  "v_cvt_pk_bf16_f32 v178, v164, v165\n" \
  "v_cvt_pk_bf16_f32 v179, v166, v167\n" \
  "v_mfma_f32_16x16x32_bf16 v[168:171], v[132:135], v[20:23], v[168:171]\n" \
  "v_mfma_f32_16x16x32_bf16 v[168:171], v[128:131], v[16:19], v[168:171]\n" \
  "s_waitcnt vmcnt(63)\n" \
  "global_store_dwordx4 v186, v[176:179], s[46:47] offset:0 sc1\n" \
  "v_mfma_f32_16x16x32_bf16 v[160:163], v[128:131], v[32:35], 0\n" \
  "v_cvt_pk_bf16_f32 v182, v172, v173\n" \
  "v_cvt_pk_bf16_f32 v183, v174, v175\n" \
  "v_mfma_f32_16x16x32_bf16 v[160:163], v[132:135], v[36:39], v[160:163]\n" \
  "v_mfma_f32_16x16x32_bf16 v[160:163], v[136:139], v[40:43], v[160:163]\n" \
  "v_mfma_f32_16x16x32_bf16 v[160:163], v[140:143], v[44:47], v[160:163]\n" \
  "v_cvt_pk_bf16_f32 v180, v168, v169\n" \
  "v_cvt_pk_bf16_f32 v181, v170, v171\n" \
  "v_mfma_f32_16x16x32_bf16 v[164:167], v[156:159], v[44:47], 0\n" \
  "v_mfma_f32_16x16x32_bf16 v[164:167], v[152:155], v[40:43], v[164:167]\n" \
  "v_mfma_f32_16x16x32_bf16 v[164:167], v[148:151], v[36:39], v[164:167]\n" \
  "v_mfma_f32_16x16x32_bf16 v[164:167], v[144:147], v[32:35], v[164:167]\n" \
  "s_waitcnt vmcnt(63)\n" \
  "global_store_dwordx4 v186, v[180:183], s[46:47] offset:256 sc1\n" \
  "v_mfma_f32_16x16x32_bf16 v[172:175], v[144:147], v[48:51], 0\n" \
  "v_cvt_pk_bf16_f32 v176, v160, v161\n" \
  "v_cvt_pk_bf16_f32 v177, v162, v163\n" \
  "v_mfma_f32_16x16x32_bf16 v[172:175], v[148:151], v[52:55], v[172:175]\n" \
  "v_mfma_f32_16x16x32_bf16 v[172:175], v[152:155], v[56:59], v[172:175]\n" \
  "v_mfma_f32_16x16x32_bf16 v[172:175], v[156:159], v[60:63], v[172:175]\n" \
  "v_cvt_pk_bf16_f32 v178, v164, v165\n" \
  "v_cvt_pk_bf16_f32 v179, v166, v167\n" \
  "v_mfma_f32_16x16x32_bf16 v[168:171], v[140:143], v[60:63], 0\n" \
  "v_mfma_f32_16x16x32_bf16 v[168:171], v[136:139], v[56:59], v[168:171]\n" \
  "v_mfma_f32_16x16x32_bf16 v[168:171], v[132:135], v[52:55], v[168:171]\n" \
  "v_mfma_f32_16x16x32_bf16 v[168:171], v[128:131], v[48:51], v[168:171]\n" \
  "s_waitcnt vmcnt(63)\n" \
  "global_store_dwordx4 v186, v[176:179], s[46:47] offset:512 sc1\n" \
  "v_cvt_pk_bf16_f32 v182, v172, v173\n" \
  "v_cvt_pk_bf16_f32 v183, v174, v175\n" \
  "s_nop 3\n" \
  "v_cvt_pk_bf16_f32 v180, v168, v169\n" \
  "v_cvt_pk_bf16_f32 v181, v170, v171\n" \
  "s_waitcnt vmcnt(63)\n" \
  "global_store_dwordx4 v186, v[180:183], s[46:47] offset:768 sc1\n" \
  "s_branch LB_t_e\n" \
  "LT_w:\n" \
  "L8_:\n" \
  "s_sleep 1\n" \
  "ds_read_b32 v192, v190 offset:0\n" \
  "s_waitcnt lgkmcnt(0)\n" \
  "v_readfirstlane_b32 s35, v192\n" \
  "s_cmp_lt_u32 s35, s99\n" \
  "s_cbranch_scc1 L8_\n" \
  "s_branch LT_c\n" \
  "LT_e:\n" \
  "s_bitcmp1_b32 10, s98\n" \
  "s_cbranch_scc0 LT_r\n" \
  "global_load_dwordx4 v[240:243], v238, s[48:49] offset:0 sc1\n" \
  "s_waitcnt vmcnt(0)\n" \
  "global_store_dwordx4 v238, v[240:243], s[48:49] offset:1024 sc1\n" \
  "s_branch LT_r\n" \
  "LT_a:\n" \
  "s_cmp_lg_u32 s35, 4\n" \
  "s_cbranch_scc1 L9_\n" \
  "global_load_dword v193, v193, s[48:49] offset:1024 sc1\n" \
  "s_branch LT_s\n" \
  "L9_:\n" \
  "s_cmp_lg_u32 s35, 5\n" \
  "s_cbranch_scc1 L10_\n" \
  "s_memtime s[58:59]\n" \
  "s_memrealtime s[40:41]\n" \
  "s_mov_b32 s5, 0x10000\n" \
  "LB_ec:\n" \
  "v_readfirstlane_b32 s35, v193\n" \
  "s_cmp_eq_u32 s35, s39\n" \
  "s_cbranch_scc1 LB_ok\n" \
  "s_lshl_b32 s35, s26, 2\n" \
  "v_mov_b32 v193, s35\n" \
  "s_sleep 8\n" \
  "global_load_dword v193, v193, s[48:49] offset:1024 sc1\n" \
  "s_waitcnt vmcnt(0)\n" \
  "s_sub_u32 s5, s5, 1\n" \
  "s_cbranch_scc0 LB_ec\n" \
  "LB_ok:\n" \
  "s_lshl_b32 s35, s26, 2\n" \
  "v_mov_b32 v244, s35\n" \
  "s_waitcnt lgkmcnt(0)\n" \
  "s_sub_u32 s88, s58, s88\n" \
  "s_sub_u32 s88, 106000, s88\n" \
  "s_sub_u32 s90, s58, s90\n" \
  "s_sub_u32 s42, s40, s42\n" \
  "s_lshl_b32 s40, s40, 4\n" \
  "v_cvt_f32_u32 v245, s90\n" \
  "v_cvt_f32_u32 v246, s42\n" \
  "v_rcp_f32 v245, v245\n" \
  "v_cvt_f32_i32 v247, s88\n" \
  "v_mul_f32 v246, 0x4111eb85, v246\n" \
  "v_mul_f32 v245, v245, v246\n" \
  "v_mul_f32 v245, v245, v247\n" \
  "v_cvt_i32_f32 v245, v245\n" \
  "v_add_u32 v245, s40, v245\n" \
  "v_or_b32 v245, 1, v245\n" \
  "s_mov_b64 exec, 1\n" \
  "global_store_dword v244, v245, s[48:49] offset:2048 sc1\n" \
  "s_mov_b64 exec, -1\n" \
  "s_branch LT_s\n" \
  "L10_:\n" \
  "s_cmp_lg_u32 s35, 7\n" \
  "s_cbranch_scc1 L11_\n" \
  "s_mov_b32 exec_hi, 0\n" \
  "global_atomic_add v196, v191, s[48:49] offset:3072 sc1\n" \
  "s_mov_b64 exec, -1\n" \
  "s_branch LT_s\n" \
  "L11_:\n" \
  "s_cmp_lg_u32 s35, 23\n" \
  "s_cbranch_scc1 L12_\n" \
  "s_lshl_b32 s35, s30, 5\n" \
  "v_lshrrev_b32 v206, 3, v255\n" \
  "v_and_b32 v208, 7, v255\n" \
  "v_lshl_add_u32 v210, v206, 2, s35\n" \
  "v_lshl_add_u32 v212, v208, 2, s35\n" \
  "global_load_dword v202, v210, s[48:49] offset:2048 sc1\n" \
  "global_load_dword v204, v212, s[48:49] offset:2048 sc1\n" \
  "s_branch LT_s\n" \
  "L12_:\n" \
  "s_cmp_lg_u32 s35, 24\n" \
  "s_cbranch_scc1 L13_\n" \
  "s_mov_b32 s5, 0x10000\n" \
  "LB_z:\n" \
  "v_min_u32 v214, v202, v204\n" \
  "v_cmp_eq_u32 s[58:59], 0, v214\n" \
  "s_sub_u32 s5, s5, 1\n" \
  "s_cbranch_scc1 LB_zx\n" \
  "s_cmp_eq_u64 s[58:59], 0\n" \
  "s_cbranch_scc1 LB_zx\n" \
  "s_cmp_lg_u32 s33, 1021\n" \
  "s_cbranch_scc1 LB_n1\n" \
  "global_load_dwordx4 v[240:243], v238, s[48:49] offset:0 sc1\n" \
  "s_waitcnt vmcnt(0)\n" \
  "global_store_dwordx4 v238, v[240:243], s[48:49] offset:1024 sc1\n" \
  "LB_n1:\n" \
  "s_sleep 4\n" \
  "global_load_dword v202, v210, s[48:49] offset:2048 sc1\n" \
  "global_load_dword v204, v212, s[48:49] offset:2048 sc1\n" \
  "s_waitcnt vmcnt(0)\n" \
  "s_branch LB_z\n" \
  "LB_zx:\n" \
  "v_and_or_b32 v202, v202, -8, v206\n" \
  "v_and_or_b32 v204, v204, -8, v208\n" \
  "v_cmp_lt_u32 s[58:59], v204, v202\n" \
  "v_sub_u32 v202, v204, v202\n" \
  "s_lshl_b32 s35, s29, 3\n" \
  "s_lshr_b64 s[40:41], s[58:59], s35\n" \
  "s_and_b32 s40, s40, 0xff\n" \
  "s_bcnt1_i32_b32 s40, s40\n" \
  "s_sub_u32 s40, 7, s40\n" \
  "s_mov_b32 s36, s29\n" \
  "s_bfe_u32 s41, s58, 0x80000\n" \
  "s_bcnt1_i32_b32 s41, s41\n" \
  "s_cmp_eq_u32 s41, s40\n" \
  "s_cselect_b32 s36, 0, s36\n" \
  "s_bfe_u32 s41, s58, 0x80008\n" \
  "s_bcnt1_i32_b32 s41, s41\n" \
  "s_cmp_eq_u32 s41, s40\n" \
  "s_cselect_b32 s36, 1, s36\n" \
  "s_bfe_u32 s41, s58, 0x80010\n" \
  "s_bcnt1_i32_b32 s41, s41\n" \
  "s_cmp_eq_u32 s41, s40\n" \
  "s_cselect_b32 s36, 2, s36\n" \
  "s_bfe_u32 s41, s58, 0x80018\n" \
  "s_bcnt1_i32_b32 s41, s41\n" \
  "s_cmp_eq_u32 s41, s40\n" \
  "s_cselect_b32 s36, 3, s36\n" \
  "s_bfe_u32 s41, s59, 0x80000\n" \
  "s_bcnt1_i32_b32 s41, s41\n" \
  "s_cmp_eq_u32 s41, s40\n" \
  "s_cselect_b32 s36, 4, s36\n" \
  "s_bfe_u32 s41, s59, 0x80008\n" \
  "s_bcnt1_i32_b32 s41, s41\n" \
  "s_cmp_eq_u32 s41, s40\n" \
  "s_cselect_b32 s36, 5, s36\n" \
  "s_bfe_u32 s41, s59, 0x80010\n" \
  "s_bcnt1_i32_b32 s41, s41\n" \
  "s_cmp_eq_u32 s41, s40\n" \
  "s_cselect_b32 s36, 6, s36\n" \
  "s_bfe_u32 s41, s59, 0x80018\n" \
  "s_bcnt1_i32_b32 s41, s41\n" \
  "s_cmp_eq_u32 s41, s40\n" \
  "s_cselect_b32 s36, 7, s36\n" \
  "s_add_u32 s35, s35, s36\n" \
  "v_readlane_b32 s41, v202, s35\n" \
  "s_sub_u32 s35, 0, s41\n" \
  "s_cmp_lt_u32 s40, 4\n" \
  "s_cselect_b32 s41, s35, s41\n" \
  "s_min_u32 s41, s41, 0xffff\n" \
  "s_mul_i32 s41, s41, 39\n" \
  "s_add_u32 s41, s41, 28672\n" \
  "s_lshr_b32 s41, s41, 16\n" \
  "s_min_u32 s41, s41, 4\n" \
  "s_sub_u32 s35, 0, s41\n" \
  "s_cmp_lt_u32 s40, 4\n" \
  "s_cselect_b32 s35, s35, s41\n" \
  "s_lshl4_add_u32 s35, s36, s35\n" \
  "s_add_u32 s35, s35, 132\n" \
  "v_mov_b32 v204, s35\n" \
  "ds_write_b32 v190, v204 offset:4\n" \
  "s_branch LT_s\n" \
  "L13_:\n" \
  "s_cmp_lg_u32 s35, 25\n" \
  "s_cbranch_scc1 L14_\n" \
  "s_mov_b64 exec, 0xff\n" \
  "global_load_dword v200, v198, s[48:49] offset:3072 sc1\n" \
  "s_mov_b64 exec, -1\n" \
  "s_branch LT_s\n" \
  "L14_:\n" \
  "s_cmp_lg_u32 s35, 26\n" \
  "s_cbranch_scc1 L15_\n" \
  "s_mov_b64 s[58:59], vcc\n" \
  "s_mov_b32 s5, 0x10000\n" \
  "LB_p:\n" \
  "s_mov_b64 exec, 0xff\n" \
  "v_cmp_le_u32 vcc, 32, v200\n" \
  "s_mov_b64 exec, -1\n" \
  "s_sub_u32 s5, s5, 1\n" \
  "s_cselect_b32 s35, 0xff, vcc_lo\n" \
  "s_cmp_eq_u32 s35, 0xff\n" \
  "s_cbranch_scc1 LB_p_x\n" \
  "s_cmp_lg_u32 s33, 1021\n" \
  "s_cbranch_scc1 LB_n2\n" \
  "global_load_dwordx4 v[240:243], v238, s[48:49] offset:0 sc1\n" \
  "s_waitcnt vmcnt(0)\n" \
  "global_store_dwordx4 v238, v[240:243], s[48:49] offset:1024 sc1\n" \
  "LB_n2:\n" \
  "s_sleep 2\n" \
  "s_mov_b64 exec, 0xff\n" \
  "global_load_dword v200, v198, s[48:49] offset:3072 sc1\n" \
  "s_mov_b64 exec, -1\n" \
  "s_waitcnt vmcnt(0)\n" \
  "s_branch LB_p\n" \
  "LB_p_x:\n" \
  "s_mov_b64 vcc, s[58:59]\n" \
  "s_branch LT_s\n" \
  "L15_:\n" \
  "s_branch LT_s\n" \
  "LT_m:\n" \
  "ds_write_b16_d16_hi v195, v196 offset:0\n" \
  "ds_write_b16_d16_hi v195, v198 offset:16\n" \
  "ds_write_b16_d16_hi v195, v200 offset:32\n" \
  "ds_write_b16_d16_hi v235, v202 offset:64\n" \
  "ds_write_b16_d16_hi v235, v204 offset:80\n" \
  "ds_write_b16_d16_hi v235, v206 offset:96\n" \
  "ds_write_b16_d16_hi v235, v208 offset:112\n" \
  "ds_write_b16_d16_hi v235, v210 offset:128\n" \
  "ds_write_b16_d16_hi v235, v212 offset:144\n" \
  "ds_write_b16_d16_hi v235, v214 offset:160\n" \
  "ds_write_b16_d16_hi v235, v216 offset:176\n" \
  "ds_write_b16_d16_hi v195, v218 offset:48\n" \
  "ds_write_b16_d16_hi v195, v220 offset:192\n" \
  "ds_write_b16_d16_hi v195, v222 offset:208\n" \
  "ds_write_b16_d16_hi v195, v224 offset:224\n" \
  "ds_write_b16_d16_hi v195, v226 offset:240\n" \
  "ds_read_b128 v[244:247], v254 offset:0\n" \
  "ds_read_b128 v[248:251], v254 offset:1088\n" \
  "s_lshr_b32 s35, s7, 5\n" \
  "s_mul_i32 s35, s35, 819200\n" \
  "s_and_b32 s36, s7, 31\n" \
  "s_lshl_b32 s36, s36, 10\n" \
  "s_add_u32 s35, s35, s36\n" \
  "s_lshl_b32 s36, s4, 9\n" \
  "s_add_u32 s35, s35, s36\n" \
  "s_add_u32 s35, s35, 102400000\n" \
  "s_add_u32 s58, s24, s35\n" \
  "s_addc_u32 s59, s25, 0\n" \
  "s_waitcnt lgkmcnt(0)\n" \
  "s_cmp_ge_u32 s4, 768\n" \
  "s_cbranch_scc1 L16_\n" \
  "global_store_dwordx4 v238, v[244:247], s[58:59] sc1\n" \
  "s_cmp_ge_u32 s4, 768\n" \
  "s_cbranch_scc1 L16_\n" \
  "s_add_u32 s58, s58, 32768\n" \
  "s_addc_u32 s59, s59, 0\n" \
  "global_store_dwordx4 v238, v[248:251], s[58:59] sc1\n" \
  "L16_:\n" \
  "v_mov_b32 v234, 0\n" \
  "v_mov_b32 v236, v237\n" \
  "s_mov_b32 s38, 1\n" \
  "s_lshl_b32 s35, s29, 8\n" \
  "v_lshl_add_u32 v196, v255, 11, s35\n" \
  "s_lshl_b32 s35, s30, 11\n" \
  "v_lshl_add_u32 v198, v255, 8, s35\n" \
  "s_branch LT\n" \
  "LB_t_h:\n" \
  "s_waitcnt lgkmcnt(12)\n" \
  "v_mfma_f32_16x16x32_bf16 v[160:163], v[128:131], v[64:67], 0\n" \
  "v_mfma_f32_16x16x32_bf16 v[160:163], v[132:135], v[68:71], v[160:163]\n" \
  "s_waitcnt lgkmcnt(8)\n" \
  "v_mfma_f32_16x16x32_bf16 v[160:163], v[136:139], v[72:75], v[160:163]\n" \
  "v_mfma_f32_16x16x32_bf16 v[160:163], v[140:143], v[76:79], v[160:163]\n" \
  "s_waitcnt lgkmcnt(0)\n" \
  "v_mfma_f32_16x16x32_bf16 v[164:167], v[156:159], v[76:79], 0\n" \
  "v_mfma_f32_16x16x32_bf16 v[164:167], v[152:155], v[72:75], v[164:167]\n" \
  "v_mfma_f32_16x16x32_bf16 v[164:167], v[148:151], v[68:71], v[164:167]\n" \
  "v_mfma_f32_16x16x32_bf16 v[164:167], v[144:147], v[64:67], v[164:167]\n" \
  "v_mfma_f32_16x16x32_bf16 v[172:175], v[144:147], v[80:83], 0\n" \
  "v_mfma_f32_16x16x32_bf16 v[172:175], v[148:151], v[84:87], v[172:175]\n" \
  "v_mfma_f32_16x16x32_bf16 v[172:175], v[152:155], v[88:91], v[172:175]\n" \
  "v_cvt_pk_bf16_f32 v176, v160, v161\n" \
  "v_cvt_pk_bf16_f32 v177, v162, v163\n" \
  "v_mfma_f32_16x16x32_bf16 v[172:175], v[156:159], v[92:95], v[172:175]\n" \
  "v_mfma_f32_16x16x32_bf16 v[168:171], v[140:143], v[92:95], 0\n" \
  "v_mfma_f32_16x16x32_bf16 v[168:171], v[136:139], v[88:91], v[168:171]\n" \
  "v_cvt_pk_bf16_f32 v178, v164, v165\n" \
  "v_cvt_pk_bf16_f32 v179, v166, v167\n" \
  "v_mfma_f32_16x16x32_bf16 v[168:171], v[132:135], v[84:87], v[168:171]\n" \
  "v_mfma_f32_16x16x32_bf16 v[168:171], v[128:131], v[80:83], v[168:171]\n" \
  "s_waitcnt vmcnt(63)\n" \
  "global_store_dwordx4 v186, v[176:179], s[46:47] offset:1024 sc1\n" \
  "v_mfma_f32_16x16x32_bf16 v[160:163], v[128:131], v[96:99], 0\n" \
  "v_cvt_pk_bf16_f32 v182, v172, v173\n" \
  "v_cvt_pk_bf16_f32 v183, v174, v175\n" \
  "v_mfma_f32_16x16x32_bf16 v[160:163], v[132:135], v[100:103], v[160:163]\n" \
  "v_mfma_f32_16x16x32_bf16 v[160:163], v[136:139], v[104:107], v[160:163]\n" \
  "v_mfma_f32_16x16x32_bf16 v[160:163], v[140:143], v[108:111], v[160:163]\n" \
  "v_cvt_pk_bf16_f32 v180, v168, v169\n" \
  "v_cvt_pk_bf16_f32 v181, v170, v171\n" \
  "v_mfma_f32_16x16x32_bf16 v[164:167], v[156:159], v[108:111], 0\n" \
  "v_mfma_f32_16x16x32_bf16 v[164:167], v[152:155], v[104:107], v[164:167]\n" \
  "v_mfma_f32_16x16x32_bf16 v[164:167], v[148:151], v[100:103], v[164:167]\n" \
  "v_mfma_f32_16x16x32_bf16 v[164:167], v[144:147], v[96:99], v[164:167]\n" \
  "s_waitcnt vmcnt(63)\n" \
  "global_store_dwordx4 v186, v[180:183], s[46:47] offset:1280 sc1\n" \
  "v_mfma_f32_16x16x32_bf16 v[168:171], v[128:131], v[112:115], 0\n" \
  "v_cvt_pk_bf16_f32 v176, v160, v161\n" \
  "v_cvt_pk_bf16_f32 v177, v162, v163\n" \
  "v_mfma_f32_16x16x32_bf16 v[168:171], v[132:135], v[116:119], v[168:171]\n" \
  "v_mfma_f32_16x16x32_bf16 v[168:171], v[136:139], v[120:123], v[168:171]\n" \
  "v_mfma_f32_16x16x32_bf16 v[168:171], v[140:143], v[124:127], v[168:171]\n" \
  "v_cvt_pk_bf16_f32 v178, v164, v165\n" \
  "v_cvt_pk_bf16_f32 v179, v166, v167\n" \
  "v_mfma_f32_16x16x32_bf16 v[172:175], v[156:159], v[124:127], 0\n" \
  "v_mfma_f32_16x16x32_bf16 v[172:175], v[152:155], v[120:123], v[172:175]\n" \
  "v_mfma_f32_16x16x32_bf16 v[172:175], v[148:151], v[116:119], v[172:175]\n" \
  "v_mfma_f32_16x16x32_bf16 v[172:175], v[144:147], v[112:115], v[172:175]\n" \
  "s_waitcnt vmcnt(63)\n" \
  "global_store_dwordx4 v187, v[176:179], s[46:47] offset:0 sc1\n" \
  "v_cvt_pk_bf16_f32 v180, v168, v169\n" \
  "v_cvt_pk_bf16_f32 v181, v170, v171\n" \
  "s_nop 3\n" \
  "v_cvt_pk_bf16_f32 v182, v172, v173\n" \
  "v_cvt_pk_bf16_f32 v183, v174, v175\n" \
  "s_waitcnt vmcnt(63)\n" \
  "global_store_dwordx4 v188, v[180:183], s[46:47] offset:0 sc1\n" \
  "LB_t_e:\n" \
  "s_setprio 2\n" \
  "v_mov_b64 v[240:241], -1\n" \
  "v_mov_b64 v[242:243], -1\n" \
  "s_lshl2_add_u32 s35, s33, s34\n" \
  "s_lshl2_add_u32 s35, s35, s28\n" \
  "s_lshl_b32 s35, s35, 12\n" \
  "s_add_u32 s35, s35, 105676800\n" \
  "s_add_u32 s40, s24, s35\n" \
  "s_addc_u32 s41, s25, 0\n" \
  "v_mbcnt_lo_u32_b32 v189, -1, 0\n" \
  "v_mbcnt_hi_u32_b32 v189, -1, v189\n" \
  "v_lshlrev_b32 v188, 4, v189\n" \
  "s_lshl_b32 s35, s28, 12\n" \
  "v_add_u32 v128, s35, v188\n" \
  "v_mov_b32 v199, 1\n" \
  "v_lshrrev_b32 v180, 1, v189\n" \
  "v_xor_b32 v180, v180, v189\n" \
  "v_and_b32 v180, 8, v180\n" \
  "v_bfe_u32 v181, v189, 2, 1\n" \
  "v_lshlrev_b32_e64 v181, v181, 5\n" \
  "v_lshlrev_b32 v180, v180, v181\n" \
  "v_bfe_u32 v181, v189, 2, 2\n" \
  "v_lshlrev_b32_e64 v181, v181, 1\n" \
  "v_cmp_gt_u32 vcc, 32, v189\n" \
  "v_mov_b32 v191, 0x3f80\n" \
  "v_add_u32 v190, 32000, v188\n" \
  "v_cndmask_b32 v192, v181, v180, vcc\n" \
  "v_bfe_i32 v180, v192, 0, 1\n" \
  "v_and_b32 v180, v180, v191\n" \
  "v_bfe_i32 v181, v192, 1, 1\n" \
  "v_and_b32 v181, v181, v191\n" \
  "v_bfe_i32 v182, v192, 2, 1\n" \
  "v_and_b32 v182, v182, v191\n" \
  "v_bfe_i32 v183, v192, 3, 1\n" \
  "v_and_b32 v183, v183, v191\n" \
  "v_and_b32 v192, 3, v189\n" \
  "v_mul_u32_u24 v192, 0x5555, v192\n" \
  "v_add_u32 v189, 16000, v188\n" \
  "v_add_u32 v191, 48000, v188\n" \
  "s_waitcnt vmcnt(4)\n" \
  "v_lshlrev_b32 v184, 16, v204\n" \
  "v_and_b32 v185, 0xffff0000, v204\n" \
  "v_lshlrev_b32 v186, 16, v205\n" \
  "v_and_b32 v187, 0xffff0000, v205\n" \
  "global_store_dwordx4 v202, v[240:243], s[40:41] offset:0 sc1\n" \
  "global_store_dwordx4 v202, v[240:243], s[40:41] offset:1024 sc1\n" \
  "global_store_dwordx4 v202, v[240:243], s[40:41] offset:2048 sc1\n" \
  "global_store_dwordx4 v202, v[240:243], s[40:41] offset:3072 sc1\n" \
  "s_setprio 0\n" \
  "s_barrier\n" \
  "s_bitcmp1_b32 s30, 2\n" \
  "s_cbranch_scc1 LB_w_h\n" \
  "ds_write_b128 v128, v[64:67] offset:0\n" \
  "ds_write_b128 v128, v[68:71] offset:1024\n" \
  "ds_write_b128 v128, v[72:75] offset:2048\n" \
  "ds_write_b128 v128, v[76:79] offset:3072\n" \
  "s_add_u32 s35, s59, 64000\n" \
  "s_add_u32 s54, s54, s35\n" \
  "s_addc_u32 s55, s55, 0\n" \
  "s_add_u32 s58, s59, 0x10000\n" \
  "s_mov_b32 m0, s58\n" \
  "s_add_u32 s35, s58, 0x8000\n" \
  "global_load_lds_dwordx4 v188, s[54:55] sc1\n" \
  "global_load_lds_dwordx4 v188, s[54:55] offset:1024 sc1\n" \
  "ds_write_b128 v128, v[80:83] offset:16384\n" \
  "ds_write_b128 v128, v[84:87] offset:17408\n" \
  "ds_write_b128 v128, v[88:91] offset:18432\n" \
  "ds_write_b128 v128, v[92:95] offset:19456\n" \
  "ds_write_b128 v128, v[96:99] offset:32768\n" \
  "ds_write_b128 v128, v[100:103] offset:33792\n" \
  "ds_write_b128 v128, v[104:107] offset:34816\n" \
  "ds_write_b128 v128, v[108:111] offset:35840\n" \
  "s_add_u32 m0, s58, 0x4000\n" \
  "s_cmp_lt_u32 s35, 0x28000\n" \
  "global_load_lds_dwordx4 v189, s[54:55] sc1\n" \
  "global_load_lds_dwordx4 v189, s[54:55] offset:1024 sc1\n" \
  "s_cselect_b32 s58, s35, s59\n" \
  "s_add_u32 s54, s54, 32000\n" \
  "s_addc_u32 s55, s55, 0\n" \
  "ds_write_b128 v128, v[112:115] offset:49152\n" \
  "ds_write_b128 v128, v[116:119] offset:50176\n" \
  "ds_write_b128 v128, v[120:123] offset:51200\n" \
  "ds_write_b128 v128, v[124:127] offset:52224\n" \
  "s_branch LB_w_e\n" \
  "LB_w_h:\n" \
  "ds_write_b128 v128, v[0:3] offset:0\n" \
  "ds_write_b128 v128, v[4:7] offset:1024\n" \
  "ds_write_b128 v128, v[8:11] offset:2048\n" \
  "ds_write_b128 v128, v[12:15] offset:3072\n" \
  "s_add_u32 s35, s59, 64000\n" \
  "s_add_u32 s54, s54, s35\n" \
  "s_addc_u32 s55, s55, 0\n" \
  "s_add_u32 s58, s59, 0x10000\n" \
  "s_mov_b32 m0, s58\n" \
  "s_add_u32 s35, s58, 0x8000\n" \
  "global_load_lds_dwordx4 v188, s[54:55] sc1\n" \
  "global_load_lds_dwordx4 v188, s[54:55] offset:1024 sc1\n" \
  "ds_write_b128 v128, v[16:19] offset:16384\n" \
  "ds_write_b128 v128, v[20:23] offset:17408\n" \
  "ds_write_b128 v128, v[24:27] offset:18432\n" \
  "ds_write_b128 v128, v[28:31] offset:19456\n" \
  "ds_write_b128 v128, v[32:35] offset:32768\n" \
  "ds_write_b128 v128, v[36:39] offset:33792\n" \
  "ds_write_b128 v128, v[40:43] offset:34816\n" \
  "ds_write_b128 v128, v[44:47] offset:35840\n" \
  "s_add_u32 m0, s58, 0x4000\n" \
  "s_cmp_lt_u32 s35, 0x28000\n" \
  "global_load_lds_dwordx4 v189, s[54:55] sc1\n" \
  "global_load_lds_dwordx4 v189, s[54:55] offset:1024 sc1\n" \
  "s_cselect_b32 s58, s35, s59\n" \
  "s_add_u32 s54, s54, 32000\n" \
  "s_addc_u32 s55, s55, 0\n" \
  "ds_write_b128 v128, v[48:51] offset:49152\n" \
  "ds_write_b128 v128, v[52:55] offset:50176\n" \
  "ds_write_b128 v128, v[56:59] offset:51200\n" \
  "ds_write_b128 v128, v[60:63] offset:52224\n" \
  "LB_w_e:\n" \
  "s_waitcnt lgkmcnt(0)\n" \
  "s_mov_b32 s51, 7\n" \
  "s_setprio 0\n" \
  "s_barrier\n" \
  "LG_start:\n" \
  "s_sub_u32 s35, 14960, s59\n" \
  "v_min_u32 v156, s35, v188\n" \
  "v_add_u32 v157, 16000, v156\n" \
  "v_add_u32 v158, 32000, v156\n" \
  "v_add_u32 v159, 48000, v156\n" \
  "v_lshlrev_b32_sdwa v64, 4, v208 dst_sel:DWORD dst_unused:UNUSED_PAD src0_sel:DWORD src1_sel:WORD_0\n" \
  "ds_read_b128 v[68:71], v64\n" \
  "v_mov_b64 v[0:1], v[184:185]\n" \
  "v_lshlrev_b32_sdwa v65, 4, v208 dst_sel:DWORD dst_unused:UNUSED_PAD src0_sel:DWORD src1_sel:WORD_1\n" \
  "ds_read_b128 v[72:75], v65\n" \
  "v_mov_b64 v[2:3], v[186:187]\n" \
  "v_lshlrev_b32_sdwa v66, 4, v209 dst_sel:DWORD dst_unused:UNUSED_PAD src0_sel:DWORD src1_sel:WORD_0\n" \
  "ds_read_b128 v[76:79], v66\n" \
  "v_mov_b64 v[4:5], v[184:185]\n" \
  "v_lshlrev_b32_sdwa v67, 4, v209 dst_sel:DWORD dst_unused:UNUSED_PAD src0_sel:DWORD src1_sel:WORD_1\n" \
  "ds_read_b128 v[80:83], v67\n" \
  "v_mov_b64 v[6:7], v[186:187]\n" \
  "s_mov_b32 m0, s58\n" \
  "s_add_u32 s35, s58, 0x8000\n" \
  "global_load_lds_dwordx4 v188, s[54:55] sc1\n" \
  "global_load_lds_dwordx4 v156, s[54:55] offset:1024 sc1\n" \
  "v_lshlrev_b32_sdwa v64, 4, v210 dst_sel:DWORD dst_unused:UNUSED_PAD src0_sel:DWORD src1_sel:WORD_0\n" \
  "ds_read_b128 v[84:87], v64\n" \
  "v_mov_b64 v[8:9], v[184:185]\n" \
  "v_lshlrev_b32_sdwa v65, 4, v210 dst_sel:DWORD dst_unused:UNUSED_PAD src0_sel:DWORD src1_sel:WORD_1\n" \
  "ds_read_b128 v[88:91], v65\n" \
  "v_mov_b64 v[10:11], v[186:187]\n" \
  "v_lshlrev_b32_sdwa v66, 4, v211 dst_sel:DWORD dst_unused:UNUSED_PAD src0_sel:DWORD src1_sel:WORD_0\n" \
  "ds_read_b128 v[92:95], v66\n" \
  "v_mov_b64 v[12:13], v[184:185]\n" \
  "v_lshlrev_b32_sdwa v67, 4, v211 dst_sel:DWORD dst_unused:UNUSED_PAD src0_sel:DWORD src1_sel:WORD_1\n" \
  "ds_read_b128 v[96:99], v67\n" \
  "v_mov_b64 v[14:15], v[186:187]\n" \
  "s_add_u32 m0, s58, 0x4000\n" \
  "s_cmp_lt_u32 s35, 0x28000\n" \
  "global_load_lds_dwordx4 v189, s[54:55] sc1\n" \
  "global_load_lds_dwordx4 v157, s[54:55] offset:1024 sc1\n" \
  "s_cselect_b32 s58, s35, s59\n" \
  "v_lshlrev_b32_sdwa v64, 4, v212 dst_sel:DWORD dst_unused:UNUSED_PAD src0_sel:DWORD src1_sel:WORD_0\n" \
  "ds_read_b128 v[100:103], v64\n" \
  "v_mov_b64 v[16:17], v[184:185]\n" \
  "v_lshlrev_b32_sdwa v65, 4, v212 dst_sel:DWORD dst_unused:UNUSED_PAD src0_sel:DWORD src1_sel:WORD_1\n" \
  "ds_read_b128 v[104:107], v65\n" \
  "v_mov_b64 v[18:19], v[186:187]\n" \
  "v_lshlrev_b32_sdwa v66, 4, v213 dst_sel:DWORD dst_unused:UNUSED_PAD src0_sel:DWORD src1_sel:WORD_0\n" \
  "ds_read_b128 v[108:111], v66\n" \
  "v_mov_b64 v[20:21], v[184:185]\n" \
  "v_lshlrev_b32_sdwa v67, 4, v213 dst_sel:DWORD dst_unused:UNUSED_PAD src0_sel:DWORD src1_sel:WORD_1\n" \
  "ds_read_b128 v[112:115], v67\n" \
  "v_mov_b64 v[22:23], v[186:187]\n" \
  "s_waitcnt lgkmcnt(10)\n" \
  "v_smfmac_f32_16x16x64_bf16 v[0:3], v[180:183], v[68:75], v192\n" \
  "v_lshlrev_b32_sdwa v64, 4, v214 dst_sel:DWORD dst_unused:UNUSED_PAD src0_sel:DWORD src1_sel:WORD_0\n" \
  "ds_read_b128 v[68:71], v64\n" \
  "v_mov_b64 v[24:25], v[184:185]\n" \
  "v_lshlrev_b32_sdwa v65, 4, v214 dst_sel:DWORD dst_unused:UNUSED_PAD src0_sel:DWORD src1_sel:WORD_1\n" \
  "ds_read_b128 v[72:75], v65\n" \
  "v_mov_b64 v[26:27], v[186:187]\n" \
  "s_mov_b32 m0, s58\n" \
  "s_add_u32 s35, s58, 0x8000\n" \
  "global_load_lds_dwordx4 v190, s[54:55] sc1\n" \
  "global_load_lds_dwordx4 v158, s[54:55] offset:1024 sc1\n" \
  "s_waitcnt lgkmcnt(10)\n" \
  "v_smfmac_f32_16x16x64_bf16 v[4:7], v[180:183], v[76:83], v192\n" \
  "v_lshlrev_b32_sdwa v66, 4, v215 dst_sel:DWORD dst_unused:UNUSED_PAD src0_sel:DWORD src1_sel:WORD_0\n" \
  "ds_read_b128 v[76:79], v66\n" \
  "v_mov_b64 v[28:29], v[184:185]\n" \
  "v_lshlrev_b32_sdwa v67, 4, v215 dst_sel:DWORD dst_unused:UNUSED_PAD src0_sel:DWORD src1_sel:WORD_1\n" \
  "ds_read_b128 v[80:83], v67\n" \
  "v_mov_b64 v[30:31], v[186:187]\n" \
  "s_waitcnt lgkmcnt(10)\n" \
  "v_smfmac_f32_16x16x64_bf16 v[8:11], v[180:183], v[84:91], v192\n" \
  "v_lshlrev_b32_sdwa v64, 4, v216 dst_sel:DWORD dst_unused:UNUSED_PAD src0_sel:DWORD src1_sel:WORD_0\n" \
  "ds_read_b128 v[84:87], v64\n" \
  "v_mov_b64 v[32:33], v[184:185]\n" \
  "v_lshlrev_b32_sdwa v65, 4, v216 dst_sel:DWORD dst_unused:UNUSED_PAD src0_sel:DWORD src1_sel:WORD_1\n" \
  "ds_read_b128 v[88:91], v65\n" \
  "v_mov_b64 v[34:35], v[186:187]\n" \
  "s_add_u32 m0, s58, 0x4000\n" \
  "s_cmp_lt_u32 s35, 0x28000\n" \
  "global_load_lds_dwordx4 v191, s[54:55] sc1\n" \
  "global_load_lds_dwordx4 v159, s[54:55] offset:1024 sc1\n" \
  "s_cselect_b32 s58, s35, s59\n" \
  "s_waitcnt lgkmcnt(10)\n" \
  "v_smfmac_f32_16x16x64_bf16 v[12:15], v[180:183], v[92:99], v192\n" \
  "v_lshlrev_b32_sdwa v66, 4, v217 dst_sel:DWORD dst_unused:UNUSED_PAD src0_sel:DWORD src1_sel:WORD_0\n" \
  "ds_read_b128 v[92:95], v66\n" \
  "v_mov_b64 v[36:37], v[184:185]\n" \
  "v_lshlrev_b32_sdwa v67, 4, v217 dst_sel:DWORD dst_unused:UNUSED_PAD src0_sel:DWORD src1_sel:WORD_1\n" \
  "ds_read_b128 v[96:99], v67\n" \
  "v_mov_b64 v[38:39], v[186:187]\n" \
  "s_waitcnt lgkmcnt(10)\n" \
  "v_smfmac_f32_16x16x64_bf16 v[16:19], v[180:183], v[100:107], v192\n" \
  "v_lshlrev_b32_sdwa v64, 4, v218 dst_sel:DWORD dst_unused:UNUSED_PAD src0_sel:DWORD src1_sel:WORD_0\n" \
  "ds_read_b128 v[100:103], v64\n" \
  "v_mov_b64 v[40:41], v[184:185]\n" \
  "v_lshlrev_b32_sdwa v65, 4, v218 dst_sel:DWORD dst_unused:UNUSED_PAD src0_sel:DWORD src1_sel:WORD_1\n" \
  "ds_read_b128 v[104:107], v65\n" \
  "v_mov_b64 v[42:43], v[186:187]\n" \
  "global_load_dwordx4 v[240:243], v188, s[56:57] sc1\n" \
  "global_load_dwordx4 v[244:247], v188, s[56:57] offset:1024 sc1\n" \
  "s_waitcnt lgkmcnt(10)\n" \
  "v_smfmac_f32_16x16x64_bf16 v[20:23], v[180:183], v[108:115], v192\n" \
  "v_lshlrev_b32_sdwa v66, 4, v219 dst_sel:DWORD dst_unused:UNUSED_PAD src0_sel:DWORD src1_sel:WORD_0\n" \
  "ds_read_b128 v[108:111], v66\n" \
  "v_mov_b64 v[44:45], v[184:185]\n" \
  "v_lshlrev_b32_sdwa v67, 4, v219 dst_sel:DWORD dst_unused:UNUSED_PAD src0_sel:DWORD src1_sel:WORD_1\n" \
  "ds_read_b128 v[112:115], v67\n" \
  "v_mov_b64 v[46:47], v[186:187]\n" \
  "s_waitcnt lgkmcnt(10)\n" \
  "v_smfmac_f32_16x16x64_bf16 v[24:27], v[180:183], v[68:75], v192\n" \
  "v_lshlrev_b32_sdwa v64, 4, v220 dst_sel:DWORD dst_unused:UNUSED_PAD src0_sel:DWORD src1_sel:WORD_0\n" \
  "ds_read_b128 v[68:71], v64\n" \
  "v_mov_b64 v[48:49], v[184:185]\n" \
  "v_lshlrev_b32_sdwa v65, 4, v220 dst_sel:DWORD dst_unused:UNUSED_PAD src0_sel:DWORD src1_sel:WORD_1\n" \
  "ds_read_b128 v[72:75], v65\n" \
  "v_mov_b64 v[50:51], v[186:187]\n" \
  "global_load_dwordx4 v[248:251], v188, s[56:57] offset:2048 sc1\n" \
  "global_load_dwordx4 v[252:255], v188, s[56:57] offset:3072 sc1\n" \
  "s_waitcnt lgkmcnt(10)\n" \
  "v_smfmac_f32_16x16x64_bf16 v[28:31], v[180:183], v[76:83], v192\n" \
  "v_lshlrev_b32_sdwa v66, 4, v221 dst_sel:DWORD dst_unused:UNUSED_PAD src0_sel:DWORD src1_sel:WORD_0\n" \
  "ds_read_b128 v[76:79], v66\n" \
  "v_mov_b64 v[52:53], v[184:185]\n" \
  "v_lshlrev_b32_sdwa v67, 4, v221 dst_sel:DWORD dst_unused:UNUSED_PAD src0_sel:DWORD src1_sel:WORD_1\n" \
  "ds_read_b128 v[80:83], v67\n" \
  "v_mov_b64 v[54:55], v[186:187]\n" \
  "s_waitcnt lgkmcnt(10)\n" \
  "v_smfmac_f32_16x16x64_bf16 v[32:35], v[180:183], v[84:91], v192\n" \
  "v_lshlrev_b32_sdwa v64, 4, v222 dst_sel:DWORD dst_unused:UNUSED_PAD src0_sel:DWORD src1_sel:WORD_0\n" \
  "ds_read_b128 v[84:87], v64\n" \
  "v_mov_b64 v[56:57], v[184:185]\n" \
  "v_lshlrev_b32_sdwa v65, 4, v222 dst_sel:DWORD dst_unused:UNUSED_PAD src0_sel:DWORD src1_sel:WORD_1\n" \
  "ds_read_b128 v[88:91], v65\n" \
  "v_mov_b64 v[58:59], v[186:187]\n" \
  "s_waitcnt lgkmcnt(10)\n" \
  "v_smfmac_f32_16x16x64_bf16 v[36:39], v[180:183], v[92:99], v192\n" \
  "v_lshlrev_b32_sdwa v66, 4, v223 dst_sel:DWORD dst_unused:UNUSED_PAD src0_sel:DWORD src1_sel:WORD_0\n" \
  "ds_read_b128 v[92:95], v66\n" \
  "v_mov_b64 v[60:61], v[184:185]\n" \
  "v_lshlrev_b32_sdwa v67, 4, v223 dst_sel:DWORD dst_unused:UNUSED_PAD src0_sel:DWORD src1_sel:WORD_1\n" \
  "ds_read_b128 v[96:99], v67\n" \
  "v_mov_b64 v[62:63], v[186:187]\n" \
  "s_add_u32 s56, s56, 32768\n" \
  "s_addc_u32 s57, s57, 0\n" \
  "s_add_u32 s54, s54, 64000\n" \
  "s_addc_u32 s55, s55, 0\n" \
  "s_cmp_eq_u32 s27, 4\n" \
  "s_cselect_b32 s37, 0x90, 0\n" \
  ".p2align 6\n" \
  "LG_loop:\n" \
  "s_bitcmp1_b32 s37, s51\n" \
  "s_waitcnt lgkmcnt(10)\n" \
  "v_smfmac_f32_16x16x64_bf16 v[40:43], v[180:183], v[100:107], v192\n" \
  "s_waitcnt lgkmcnt(8)\n" \
  "v_smfmac_f32_16x16x64_bf16 v[44:47], v[180:183], v[108:115], v192\n" \
  "s_waitcnt vmcnt(8)\n" \
  "s_cbranch_scc1 LG_h\n" \
  "LG_ret:\n" \
  "s_barrier\n" \
  "LG_aft:\n" \
  "v_lshlrev_b32_sdwa v64, 4, v224 dst_sel:DWORD dst_unused:UNUSED_PAD src0_sel:DWORD src1_sel:WORD_0\n" \
  "ds_read_b128 v[100:103], v64\n" \
  "v_lshlrev_b32_sdwa v65, 4, v224 dst_sel:DWORD dst_unused:UNUSED_PAD src0_sel:DWORD src1_sel:WORD_1\n" \
  "ds_read_b128 v[104:107], v65\n" \
  "v_lshlrev_b32_sdwa v66, 4, v225 dst_sel:DWORD dst_unused:UNUSED_PAD src0_sel:DWORD src1_sel:WORD_0\n" \
  "ds_read_b128 v[108:111], v66\n" \
  "v_lshlrev_b32_sdwa v67, 4, v225 dst_sel:DWORD dst_unused:UNUSED_PAD src0_sel:DWORD src1_sel:WORD_1\n" \
  "ds_read_b128 v[112:115], v67\n" \
  "s_waitcnt lgkmcnt(10)\n" \
  "v_smfmac_f32_16x16x64_bf16 v[48:51], v[180:183], v[68:75], v192\n" \
  "s_waitcnt lgkmcnt(8)\n" \
  "v_smfmac_f32_16x16x64_bf16 v[52:55], v[180:183], v[76:83], v192\n" \
  "s_waitcnt lgkmcnt(6)\n" \
  "v_smfmac_f32_16x16x64_bf16 v[56:59], v[180:183], v[84:91], v192\n" \
  "s_waitcnt lgkmcnt(4)\n" \
  "v_smfmac_f32_16x16x64_bf16 v[60:63], v[180:183], v[92:99], v192\n" \
  "s_barrier\n" \
  "s_mov_b32 m0, s58\n" \
  "s_add_u32 s35, s58, 0x8000\n" \
  "global_load_lds_dwordx4 v188, s[54:55] sc1\n" \
  "global_load_lds_dwordx4 v156, s[54:55] offset:1024 sc1\n" \
  "v_lshlrev_b32_sdwa v64, 4, v226 dst_sel:DWORD dst_unused:UNUSED_PAD src0_sel:DWORD src1_sel:WORD_0\n" \
  "ds_read_b128 v[68:71], v64\n" \
  "v_lshlrev_b32_sdwa v65, 4, v226 dst_sel:DWORD dst_unused:UNUSED_PAD src0_sel:DWORD src1_sel:WORD_1\n" \
  "ds_read_b128 v[72:75], v65\n" \
  "v_lshlrev_b32_sdwa v66, 4, v227 dst_sel:DWORD dst_unused:UNUSED_PAD src0_sel:DWORD src1_sel:WORD_0\n" \
  "ds_read_b128 v[76:79], v66\n" \
  "v_lshlrev_b32_sdwa v67, 4, v227 dst_sel:DWORD dst_unused:UNUSED_PAD src0_sel:DWORD src1_sel:WORD_1\n" \
  "ds_read_b128 v[80:83], v67\n" \
  "s_add_u32 m0, s58, 0x4000\n" \
  "s_cmp_lt_u32 s35, 0x28000\n" \
  "global_load_lds_dwordx4 v189, s[54:55] sc1\n" \
  "global_load_lds_dwordx4 v157, s[54:55] offset:1024 sc1\n" \
  "s_cselect_b32 s58, s35, s59\n" \
  "v_lshlrev_b32_sdwa v64, 4, v228 dst_sel:DWORD dst_unused:UNUSED_PAD src0_sel:DWORD src1_sel:WORD_0\n" \
  "ds_read_b128 v[84:87], v64\n" \
  "v_lshlrev_b32_sdwa v65, 4, v228 dst_sel:DWORD dst_unused:UNUSED_PAD src0_sel:DWORD src1_sel:WORD_1\n" \
  "ds_read_b128 v[88:91], v65\n" \
  "v_lshlrev_b32_sdwa v66, 4, v229 dst_sel:DWORD dst_unused:UNUSED_PAD src0_sel:DWORD src1_sel:WORD_0\n" \
  "ds_read_b128 v[92:95], v66\n" \
  "v_lshlrev_b32_sdwa v67, 4, v229 dst_sel:DWORD dst_unused:UNUSED_PAD src0_sel:DWORD src1_sel:WORD_1\n" \
  "ds_read_b128 v[96:99], v67\n" \
  "s_waitcnt lgkmcnt(10)\n" \
  "v_smfmac_f32_16x16x64_bf16 v[0:3], v[180:183], v[100:107], v192\n" \
  "v_lshlrev_b32_sdwa v64, 4, v230 dst_sel:DWORD dst_unused:UNUSED_PAD src0_sel:DWORD src1_sel:WORD_0\n" \
  "ds_read_b128 v[100:103], v64\n" \
  "v_lshlrev_b32_sdwa v65, 4, v230 dst_sel:DWORD dst_unused:UNUSED_PAD src0_sel:DWORD src1_sel:WORD_1\n" \
  "ds_read_b128 v[104:107], v65\n" \
  "s_mov_b32 m0, s58\n" \
  "s_add_u32 s35, s58, 0x8000\n" \
  "global_load_lds_dwordx4 v190, s[54:55] sc1\n" \
  "global_load_lds_dwordx4 v158, s[54:55] offset:1024 sc1\n" \
  "s_waitcnt lgkmcnt(10)\n" \
  "v_smfmac_f32_16x16x64_bf16 v[4:7], v[180:183], v[108:115], v192\n" \
  "v_lshlrev_b32_sdwa v66, 4, v231 dst_sel:DWORD dst_unused:UNUSED_PAD src0_sel:DWORD src1_sel:WORD_0\n" \
  "ds_read_b128 v[108:111], v66\n" \
  "v_lshlrev_b32_sdwa v67, 4, v231 dst_sel:DWORD dst_unused:UNUSED_PAD src0_sel:DWORD src1_sel:WORD_1\n" \
  "ds_read_b128 v[112:115], v67\n" \
  "s_waitcnt lgkmcnt(10)\n" \
  "v_smfmac_f32_16x16x64_bf16 v[8:11], v[180:183], v[68:75], v192\n" \
  "v_lshlrev_b32_sdwa v64, 4, v232 dst_sel:DWORD dst_unused:UNUSED_PAD src0_sel:DWORD src1_sel:WORD_0\n" \
  "ds_read_b128 v[68:71], v64\n" \
  "v_lshlrev_b32_sdwa v65, 4, v232 dst_sel:DWORD dst_unused:UNUSED_PAD src0_sel:DWORD src1_sel:WORD_1\n" \
  "ds_read_b128 v[72:75], v65\n" \
  "s_add_u32 m0, s58, 0x4000\n" \
  "s_cmp_lt_u32 s35, 0x28000\n" \
  "global_load_lds_dwordx4 v191, s[54:55] sc1\n" \
  "global_load_lds_dwordx4 v159, s[54:55] offset:1024 sc1\n" \
  "s_cselect_b32 s58, s35, s59\n" \
  "s_waitcnt lgkmcnt(10)\n" \
  "v_smfmac_f32_16x16x64_bf16 v[12:15], v[180:183], v[76:83], v192\n" \
  "v_lshlrev_b32_sdwa v66, 4, v233 dst_sel:DWORD dst_unused:UNUSED_PAD src0_sel:DWORD src1_sel:WORD_0\n" \
  "ds_read_b128 v[76:79], v66\n" \
  "v_lshlrev_b32_sdwa v67, 4, v233 dst_sel:DWORD dst_unused:UNUSED_PAD src0_sel:DWORD src1_sel:WORD_1\n" \
  "ds_read_b128 v[80:83], v67\n" \
  "s_waitcnt lgkmcnt(10)\n" \
  "v_smfmac_f32_16x16x64_bf16 v[16:19], v[180:183], v[84:91], v192\n" \
  "v_lshlrev_b32_sdwa v64, 4, v234 dst_sel:DWORD dst_unused:UNUSED_PAD src0_sel:DWORD src1_sel:WORD_0\n" \
  "ds_read_b128 v[84:87], v64\n" \
  "v_lshlrev_b32_sdwa v65, 4, v234 dst_sel:DWORD dst_unused:UNUSED_PAD src0_sel:DWORD src1_sel:WORD_1\n" \
  "ds_read_b128 v[88:91], v65\n" \
  "global_load_dwordx4 v[208:211], v188, s[56:57] sc1\n" \
  "global_load_dwordx4 v[212:215], v188, s[56:57] offset:1024 sc1\n" \
  "s_waitcnt lgkmcnt(10)\n" \
  "v_smfmac_f32_16x16x64_bf16 v[20:23], v[180:183], v[92:99], v192\n" \
  "v_lshlrev_b32_sdwa v66, 4, v235 dst_sel:DWORD dst_unused:UNUSED_PAD src0_sel:DWORD src1_sel:WORD_0\n" \
  "ds_read_b128 v[92:95], v66\n" \
  "v_lshlrev_b32_sdwa v67, 4, v235 dst_sel:DWORD dst_unused:UNUSED_PAD src0_sel:DWORD src1_sel:WORD_1\n" \
  "ds_read_b128 v[96:99], v67\n" \
  "s_waitcnt lgkmcnt(10)\n" \
  "v_smfmac_f32_16x16x64_bf16 v[24:27], v[180:183], v[100:107], v192\n" \
  "v_lshlrev_b32_sdwa v64, 4, v236 dst_sel:DWORD dst_unused:UNUSED_PAD src0_sel:DWORD src1_sel:WORD_0\n" \
  "ds_read_b128 v[100:103], v64\n" \
  "v_lshlrev_b32_sdwa v65, 4, v236 dst_sel:DWORD dst_unused:UNUSED_PAD src0_sel:DWORD src1_sel:WORD_1\n" \
  "ds_read_b128 v[104:107], v65\n" \
  "global_load_dwordx4 v[216:219], v188, s[56:57] offset:2048 sc1\n" \
  "global_load_dwordx4 v[220:223], v188, s[56:57] offset:3072 sc1\n" \
  "s_waitcnt lgkmcnt(10)\n" \
  "v_smfmac_f32_16x16x64_bf16 v[28:31], v[180:183], v[108:115], v192\n" \
  "v_lshlrev_b32_sdwa v66, 4, v237 dst_sel:DWORD dst_unused:UNUSED_PAD src0_sel:DWORD src1_sel:WORD_0\n" \
  "ds_read_b128 v[108:111], v66\n" \
  "v_lshlrev_b32_sdwa v67, 4, v237 dst_sel:DWORD dst_unused:UNUSED_PAD src0_sel:DWORD src1_sel:WORD_1\n" \
  "ds_read_b128 v[112:115], v67\n" \
  "s_waitcnt lgkmcnt(10)\n" \
  "v_smfmac_f32_16x16x64_bf16 v[32:35], v[180:183], v[68:75], v192\n" \
  "v_lshlrev_b32_sdwa v64, 4, v238 dst_sel:DWORD dst_unused:UNUSED_PAD src0_sel:DWORD src1_sel:WORD_0\n" \
  "ds_read_b128 v[68:71], v64\n" \
  "v_lshlrev_b32_sdwa v65, 4, v238 dst_sel:DWORD dst_unused:UNUSED_PAD src0_sel:DWORD src1_sel:WORD_1\n" \
  "ds_read_b128 v[72:75], v65\n" \
  "s_waitcnt lgkmcnt(10)\n" \
  "v_smfmac_f32_16x16x64_bf16 v[36:39], v[180:183], v[76:83], v192\n" \
  "v_lshlrev_b32_sdwa v66, 4, v239 dst_sel:DWORD dst_unused:UNUSED_PAD src0_sel:DWORD src1_sel:WORD_0\n" \
  "ds_read_b128 v[76:79], v66\n" \
  "v_lshlrev_b32_sdwa v67, 4, v239 dst_sel:DWORD dst_unused:UNUSED_PAD src0_sel:DWORD src1_sel:WORD_1\n" \
  "ds_read_b128 v[80:83], v67\n" \
  "s_add_u32 s56, s56, 32768\n" \
  "s_addc_u32 s57, s57, 0\n" \
  "s_add_u32 s54, s54, 64000\n" \
  "s_addc_u32 s55, s55, 0\n" \
  "s_waitcnt lgkmcnt(10)\n" \
  "v_smfmac_f32_16x16x64_bf16 v[40:43], v[180:183], v[84:91], v192\n" \
  "s_waitcnt lgkmcnt(8)\n" \
  "v_smfmac_f32_16x16x64_bf16 v[44:47], v[180:183], v[92:99], v192\n" \
  "s_waitcnt vmcnt(8)\n" \
  "s_barrier\n" \
  "v_lshlrev_b32_sdwa v64, 4, v240 dst_sel:DWORD dst_unused:UNUSED_PAD src0_sel:DWORD src1_sel:WORD_0\n" \
  "ds_read_b128 v[84:87], v64\n" \
  "v_lshlrev_b32_sdwa v65, 4, v240 dst_sel:DWORD dst_unused:UNUSED_PAD src0_sel:DWORD src1_sel:WORD_1\n" \
  "ds_read_b128 v[88:91], v65\n" \
  "v_lshlrev_b32_sdwa v66, 4, v241 dst_sel:DWORD dst_unused:UNUSED_PAD src0_sel:DWORD src1_sel:WORD_0\n" \
  "ds_read_b128 v[92:95], v66\n" \
  "v_lshlrev_b32_sdwa v67, 4, v241 dst_sel:DWORD dst_unused:UNUSED_PAD src0_sel:DWORD src1_sel:WORD_1\n" \
  "ds_read_b128 v[96:99], v67\n" \
  "s_waitcnt lgkmcnt(10)\n" \
  "v_smfmac_f32_16x16x64_bf16 v[48:51], v[180:183], v[100:107], v192\n" \
  "s_waitcnt lgkmcnt(8)\n" \
  "v_smfmac_f32_16x16x64_bf16 v[52:55], v[180:183], v[108:115], v192\n" \
  "s_waitcnt lgkmcnt(6)\n" \
  "v_smfmac_f32_16x16x64_bf16 v[56:59], v[180:183], v[68:75], v192\n" \
  "s_waitcnt lgkmcnt(4)\n" \
  "v_smfmac_f32_16x16x64_bf16 v[60:63], v[180:183], v[76:83], v192\n" \
  "s_barrier\n" \
  "s_mov_b32 m0, s58\n" \
  "s_add_u32 s35, s58, 0x8000\n" \
  "global_load_lds_dwordx4 v188, s[54:55] sc1\n" \
  "global_load_lds_dwordx4 v156, s[54:55] offset:1024 sc1\n" \
  "v_lshlrev_b32_sdwa v64, 4, v242 dst_sel:DWORD dst_unused:UNUSED_PAD src0_sel:DWORD src1_sel:WORD_0\n" \
  "ds_read_b128 v[100:103], v64\n" \
  "v_lshlrev_b32_sdwa v65, 4, v242 dst_sel:DWORD dst_unused:UNUSED_PAD src0_sel:DWORD src1_sel:WORD_1\n" \
  "ds_read_b128 v[104:107], v65\n" \
  "v_lshlrev_b32_sdwa v66, 4, v243 dst_sel:DWORD dst_unused:UNUSED_PAD src0_sel:DWORD src1_sel:WORD_0\n" \
  "ds_read_b128 v[108:111], v66\n" \
  "v_lshlrev_b32_sdwa v67, 4, v243 dst_sel:DWORD dst_unused:UNUSED_PAD src0_sel:DWORD src1_sel:WORD_1\n" \
  "ds_read_b128 v[112:115], v67\n" \
  "s_add_u32 m0, s58, 0x4000\n" \
  "s_cmp_lt_u32 s35, 0x28000\n" \
  "global_load_lds_dwordx4 v189, s[54:55] sc1\n" \
  "global_load_lds_dwordx4 v157, s[54:55] offset:1024 sc1\n" \
  "s_cselect_b32 s58, s35, s59\n" \
  "v_lshlrev_b32_sdwa v64, 4, v244 dst_sel:DWORD dst_unused:UNUSED_PAD src0_sel:DWORD src1_sel:WORD_0\n" \
  "ds_read_b128 v[68:71], v64\n" \
  "v_lshlrev_b32_sdwa v65, 4, v244 dst_sel:DWORD dst_unused:UNUSED_PAD src0_sel:DWORD src1_sel:WORD_1\n" \
  "ds_read_b128 v[72:75], v65\n" \
  "v_lshlrev_b32_sdwa v66, 4, v245 dst_sel:DWORD dst_unused:UNUSED_PAD src0_sel:DWORD src1_sel:WORD_0\n" \
  "ds_read_b128 v[76:79], v66\n" \
  "v_lshlrev_b32_sdwa v67, 4, v245 dst_sel:DWORD dst_unused:UNUSED_PAD src0_sel:DWORD src1_sel:WORD_1\n" \
  "ds_read_b128 v[80:83], v67\n" \
  "s_waitcnt lgkmcnt(10)\n" \
  "v_smfmac_f32_16x16x64_bf16 v[0:3], v[180:183], v[84:91], v192\n" \
  "v_lshlrev_b32_sdwa v64, 4, v246 dst_sel:DWORD dst_unused:UNUSED_PAD src0_sel:DWORD src1_sel:WORD_0\n" \
  "ds_read_b128 v[84:87], v64\n" \
  "v_lshlrev_b32_sdwa v65, 4, v246 dst_sel:DWORD dst_unused:UNUSED_PAD src0_sel:DWORD src1_sel:WORD_1\n" \
  "ds_read_b128 v[88:91], v65\n" \
  "s_mov_b32 m0, s58\n" \
  "s_add_u32 s35, s58, 0x8000\n" \
  "global_load_lds_dwordx4 v190, s[54:55] sc1\n" \
  "global_load_lds_dwordx4 v158, s[54:55] offset:1024 sc1\n" \
  "s_waitcnt lgkmcnt(10)\n" \
  "v_smfmac_f32_16x16x64_bf16 v[4:7], v[180:183], v[92:99], v192\n" \
  "v_lshlrev_b32_sdwa v66, 4, v247 dst_sel:DWORD dst_unused:UNUSED_PAD src0_sel:DWORD src1_sel:WORD_0\n" \
  "ds_read_b128 v[92:95], v66\n" \
  "v_lshlrev_b32_sdwa v67, 4, v247 dst_sel:DWORD dst_unused:UNUSED_PAD src0_sel:DWORD src1_sel:WORD_1\n" \
  "ds_read_b128 v[96:99], v67\n" \
  "s_waitcnt lgkmcnt(10)\n" \
  "v_smfmac_f32_16x16x64_bf16 v[8:11], v[180:183], v[100:107], v192\n" \
  "v_lshlrev_b32_sdwa v64, 4, v248 dst_sel:DWORD dst_unused:UNUSED_PAD src0_sel:DWORD src1_sel:WORD_0\n" \
  "ds_read_b128 v[100:103], v64\n" \
  "v_lshlrev_b32_sdwa v65, 4, v248 dst_sel:DWORD dst_unused:UNUSED_PAD src0_sel:DWORD src1_sel:WORD_1\n" \
  "ds_read_b128 v[104:107], v65\n" \
  "s_add_u32 m0, s58, 0x4000\n" \
  "s_cmp_lt_u32 s35, 0x28000\n" \
  "global_load_lds_dwordx4 v191, s[54:55] sc1\n" \
  "global_load_lds_dwordx4 v159, s[54:55] offset:1024 sc1\n" \
  "s_cselect_b32 s58, s35, s59\n" \
  "s_waitcnt lgkmcnt(10)\n" \
  "v_smfmac_f32_16x16x64_bf16 v[12:15], v[180:183], v[108:115], v192\n" \
  "v_lshlrev_b32_sdwa v66, 4, v249 dst_sel:DWORD dst_unused:UNUSED_PAD src0_sel:DWORD src1_sel:WORD_0\n" \
  "ds_read_b128 v[108:111], v66\n" \
  "v_lshlrev_b32_sdwa v67, 4, v249 dst_sel:DWORD dst_unused:UNUSED_PAD src0_sel:DWORD src1_sel:WORD_1\n" \
  "ds_read_b128 v[112:115], v67\n" \
  "s_waitcnt lgkmcnt(10)\n" \
  "v_smfmac_f32_16x16x64_bf16 v[16:19], v[180:183], v[68:75], v192\n" \
  "v_lshlrev_b32_sdwa v64, 4, v250 dst_sel:DWORD dst_unused:UNUSED_PAD src0_sel:DWORD src1_sel:WORD_0\n" \
  "ds_read_b128 v[68:71], v64\n" \
  "v_lshlrev_b32_sdwa v65, 4, v250 dst_sel:DWORD dst_unused:UNUSED_PAD src0_sel:DWORD src1_sel:WORD_1\n" \
  "ds_read_b128 v[72:75], v65\n" \
  "global_load_dwordx4 v[224:227], v188, s[56:57] sc1\n" \
  "global_load_dwordx4 v[228:231], v188, s[56:57] offset:1024 sc1\n" \
  "s_waitcnt lgkmcnt(10)\n" \
  "v_smfmac_f32_16x16x64_bf16 v[20:23], v[180:183], v[76:83], v192\n" \
  "v_lshlrev_b32_sdwa v66, 4, v251 dst_sel:DWORD dst_unused:UNUSED_PAD src0_sel:DWORD src1_sel:WORD_0\n" \
  "ds_read_b128 v[76:79], v66\n" \
  "v_lshlrev_b32_sdwa v67, 4, v251 dst_sel:DWORD dst_unused:UNUSED_PAD src0_sel:DWORD src1_sel:WORD_1\n" \
  "ds_read_b128 v[80:83], v67\n" \
  "s_waitcnt lgkmcnt(10)\n" \
  "v_smfmac_f32_16x16x64_bf16 v[24:27], v[180:183], v[84:91], v192\n" \
  "v_lshlrev_b32_sdwa v64, 4, v252 dst_sel:DWORD dst_unused:UNUSED_PAD src0_sel:DWORD src1_sel:WORD_0\n" \
  "ds_read_b128 v[84:87], v64\n" \
  "v_lshlrev_b32_sdwa v65, 4, v252 dst_sel:DWORD dst_unused:UNUSED_PAD src0_sel:DWORD src1_sel:WORD_1\n" \
  "ds_read_b128 v[88:91], v65\n" \
  "global_load_dwordx4 v[232:235], v188, s[56:57] offset:2048 sc1\n" \
  "global_load_dwordx4 v[236:239], v188, s[56:57] offset:3072 sc1\n" \
  "s_waitcnt lgkmcnt(10)\n" \
  "v_smfmac_f32_16x16x64_bf16 v[28:31], v[180:183], v[92:99], v192\n" \
  "v_lshlrev_b32_sdwa v66, 4, v253 dst_sel:DWORD dst_unused:UNUSED_PAD src0_sel:DWORD src1_sel:WORD_0\n" \
  "ds_read_b128 v[92:95], v66\n" \
  "v_lshlrev_b32_sdwa v67, 4, v253 dst_sel:DWORD dst_unused:UNUSED_PAD src0_sel:DWORD src1_sel:WORD_1\n" \
  "ds_read_b128 v[96:99], v67\n" \
  "s_waitcnt lgkmcnt(10)\n" \
  "v_smfmac_f32_16x16x64_bf16 v[32:35], v[180:183], v[100:107], v192\n" \
  "v_lshlrev_b32_sdwa v64, 4, v254 dst_sel:DWORD dst_unused:UNUSED_PAD src0_sel:DWORD src1_sel:WORD_0\n" \
  "ds_read_b128 v[100:103], v64\n" \
  "v_lshlrev_b32_sdwa v65, 4, v254 dst_sel:DWORD dst_unused:UNUSED_PAD src0_sel:DWORD src1_sel:WORD_1\n" \
  "ds_read_b128 v[104:107], v65\n" \
  "s_waitcnt lgkmcnt(10)\n" \
  "v_smfmac_f32_16x16x64_bf16 v[36:39], v[180:183], v[108:115], v192\n" \
  "v_lshlrev_b32_sdwa v66, 4, v255 dst_sel:DWORD dst_unused:UNUSED_PAD src0_sel:DWORD src1_sel:WORD_0\n" \
  "ds_read_b128 v[108:111], v66\n" \
  "v_lshlrev_b32_sdwa v67, 4, v255 dst_sel:DWORD dst_unused:UNUSED_PAD src0_sel:DWORD src1_sel:WORD_1\n" \
  "ds_read_b128 v[112:115], v67\n" \
  "s_add_u32 s56, s56, 32768\n" \
  "s_addc_u32 s57, s57, 0\n" \
  "s_add_u32 s54, s54, 64000\n" \
  "s_addc_u32 s55, s55, 0\n" \
  "s_lshl3_add_u32 s36, s27, s51\n" \
  "s_cmp_eq_u32 s36, 37\n" \
  "s_waitcnt lgkmcnt(10)\n" \
  "v_smfmac_f32_16x16x64_bf16 v[40:43], v[180:183], v[68:75], v192\n" \
  "s_waitcnt lgkmcnt(8)\n" \
  "v_smfmac_f32_16x16x64_bf16 v[44:47], v[180:183], v[76:83], v192\n" \
  "s_waitcnt vmcnt(8)\n" \
  "s_cbranch_scc1 LG_pol\n" \
  "LG_rt2:\n" \
  "s_barrier\n" \
  "v_lshlrev_b32_sdwa v64, 4, v208 dst_sel:DWORD dst_unused:UNUSED_PAD src0_sel:DWORD src1_sel:WORD_0\n" \
  "ds_read_b128 v[68:71], v64\n" \
  "v_lshlrev_b32_sdwa v65, 4, v208 dst_sel:DWORD dst_unused:UNUSED_PAD src0_sel:DWORD src1_sel:WORD_1\n" \
  "ds_read_b128 v[72:75], v65\n" \
  "v_lshlrev_b32_sdwa v66, 4, v209 dst_sel:DWORD dst_unused:UNUSED_PAD src0_sel:DWORD src1_sel:WORD_0\n" \
  "ds_read_b128 v[76:79], v66\n" \
  "v_lshlrev_b32_sdwa v67, 4, v209 dst_sel:DWORD dst_unused:UNUSED_PAD src0_sel:DWORD src1_sel:WORD_1\n" \
  "ds_read_b128 v[80:83], v67\n" \
  "s_waitcnt lgkmcnt(10)\n" \
  "v_smfmac_f32_16x16x64_bf16 v[48:51], v[180:183], v[84:91], v192\n" \
  "s_waitcnt lgkmcnt(8)\n" \
  "v_smfmac_f32_16x16x64_bf16 v[52:55], v[180:183], v[92:99], v192\n" \
  "s_waitcnt lgkmcnt(6)\n" \
  "v_smfmac_f32_16x16x64_bf16 v[56:59], v[180:183], v[100:107], v192\n" \
  "s_waitcnt lgkmcnt(4)\n" \
  "v_smfmac_f32_16x16x64_bf16 v[60:63], v[180:183], v[108:115], v192\n" \
  "s_barrier\n" \
  "s_mov_b32 m0, s58\n" \
  "s_add_u32 s35, s58, 0x8000\n" \
  "global_load_lds_dwordx4 v188, s[54:55] sc1\n" \
  "global_load_lds_dwordx4 v156, s[54:55] offset:1024 sc1\n" \
  "v_lshlrev_b32_sdwa v64, 4, v210 dst_sel:DWORD dst_unused:UNUSED_PAD src0_sel:DWORD src1_sel:WORD_0\n" \
  "ds_read_b128 v[84:87], v64\n" \
  "v_lshlrev_b32_sdwa v65, 4, v210 dst_sel:DWORD dst_unused:UNUSED_PAD src0_sel:DWORD src1_sel:WORD_1\n" \
  "ds_read_b128 v[88:91], v65\n" \
  "v_lshlrev_b32_sdwa v66, 4, v211 dst_sel:DWORD dst_unused:UNUSED_PAD src0_sel:DWORD src1_sel:WORD_0\n" \
  "ds_read_b128 v[92:95], v66\n" \
  "v_lshlrev_b32_sdwa v67, 4, v211 dst_sel:DWORD dst_unused:UNUSED_PAD src0_sel:DWORD src1_sel:WORD_1\n" \
  "ds_read_b128 v[96:99], v67\n" \
  "s_add_u32 m0, s58, 0x4000\n" \
  "s_cmp_lt_u32 s35, 0x28000\n" \
  "global_load_lds_dwordx4 v189, s[54:55] sc1\n" \
  "global_load_lds_dwordx4 v157, s[54:55] offset:1024 sc1\n" \
  "s_cselect_b32 s58, s35, s59\n" \
  "v_lshlrev_b32_sdwa v64, 4, v212 dst_sel:DWORD dst_unused:UNUSED_PAD src0_sel:DWORD src1_sel:WORD_0\n" \
  "ds_read_b128 v[100:103], v64\n" \
  "v_lshlrev_b32_sdwa v65, 4, v212 dst_sel:DWORD dst_unused:UNUSED_PAD src0_sel:DWORD src1_sel:WORD_1\n" \
  "ds_read_b128 v[104:107], v65\n" \
  "v_lshlrev_b32_sdwa v66, 4, v213 dst_sel:DWORD dst_unused:UNUSED_PAD src0_sel:DWORD src1_sel:WORD_0\n" \
  "ds_read_b128 v[108:111], v66\n" \
  "v_lshlrev_b32_sdwa v67, 4, v213 dst_sel:DWORD dst_unused:UNUSED_PAD src0_sel:DWORD src1_sel:WORD_1\n" \
  "ds_read_b128 v[112:115], v67\n" \
  "s_waitcnt lgkmcnt(10)\n" \
  "v_smfmac_f32_16x16x64_bf16 v[0:3], v[180:183], v[68:75], v192\n" \
  "v_lshlrev_b32_sdwa v64, 4, v214 dst_sel:DWORD dst_unused:UNUSED_PAD src0_sel:DWORD src1_sel:WORD_0\n" \
  "ds_read_b128 v[68:71], v64\n" \
  "v_lshlrev_b32_sdwa v65, 4, v214 dst_sel:DWORD dst_unused:UNUSED_PAD src0_sel:DWORD src1_sel:WORD_1\n" \
  "ds_read_b128 v[72:75], v65\n" \
  "s_mov_b32 m0, s58\n" \
  "s_add_u32 s35, s58, 0x8000\n" \
  "global_load_lds_dwordx4 v190, s[54:55] sc1\n" \
  "global_load_lds_dwordx4 v158, s[54:55] offset:1024 sc1\n" \
  "s_waitcnt lgkmcnt(10)\n" \
  "v_smfmac_f32_16x16x64_bf16 v[4:7], v[180:183], v[76:83], v192\n" \
  "v_lshlrev_b32_sdwa v66, 4, v215 dst_sel:DWORD dst_unused:UNUSED_PAD src0_sel:DWORD src1_sel:WORD_0\n" \
  "ds_read_b128 v[76:79], v66\n" \
  "v_lshlrev_b32_sdwa v67, 4, v215 dst_sel:DWORD dst_unused:UNUSED_PAD src0_sel:DWORD src1_sel:WORD_1\n" \
  "ds_read_b128 v[80:83], v67\n" \
  "s_waitcnt lgkmcnt(10)\n" \
  "v_smfmac_f32_16x16x64_bf16 v[8:11], v[180:183], v[84:91], v192\n" \
  "v_lshlrev_b32_sdwa v64, 4, v216 dst_sel:DWORD dst_unused:UNUSED_PAD src0_sel:DWORD src1_sel:WORD_0\n" \
  "ds_read_b128 v[84:87], v64\n" \
  "v_lshlrev_b32_sdwa v65, 4, v216 dst_sel:DWORD dst_unused:UNUSED_PAD src0_sel:DWORD src1_sel:WORD_1\n" \
  "ds_read_b128 v[88:91], v65\n" \
  "s_add_u32 m0, s58, 0x4000\n" \
  "s_cmp_lt_u32 s35, 0x28000\n" \
  "global_load_lds_dwordx4 v191, s[54:55] sc1\n" \
  "global_load_lds_dwordx4 v159, s[54:55] offset:1024 sc1\n" \
  "s_cselect_b32 s58, s35, s59\n" \
  "s_waitcnt lgkmcnt(10)\n" \
  "v_smfmac_f32_16x16x64_bf16 v[12:15], v[180:183], v[92:99], v192\n" \
  "v_lshlrev_b32_sdwa v66, 4, v217 dst_sel:DWORD dst_unused:UNUSED_PAD src0_sel:DWORD src1_sel:WORD_0\n" \
  "ds_read_b128 v[92:95], v66\n" \
  "v_lshlrev_b32_sdwa v67, 4, v217 dst_sel:DWORD dst_unused:UNUSED_PAD src0_sel:DWORD src1_sel:WORD_1\n" \
  "ds_read_b128 v[96:99], v67\n" \
  "s_waitcnt lgkmcnt(10)\n" \
  "v_smfmac_f32_16x16x64_bf16 v[16:19], v[180:183], v[100:107], v192\n" \
  "v_lshlrev_b32_sdwa v64, 4, v218 dst_sel:DWORD dst_unused:UNUSED_PAD src0_sel:DWORD src1_sel:WORD_0\n" \
  "ds_read_b128 v[100:103], v64\n" \
  "v_lshlrev_b32_sdwa v65, 4, v218 dst_sel:DWORD dst_unused:UNUSED_PAD src0_sel:DWORD src1_sel:WORD_1\n" \
  "ds_read_b128 v[104:107], v65\n" \
  "global_load_dwordx4 v[240:243], v188, s[56:57] sc1\n" \
  "global_load_dwordx4 v[244:247], v188, s[56:57] offset:1024 sc1\n" \
  "s_waitcnt lgkmcnt(10)\n" \
  "v_smfmac_f32_16x16x64_bf16 v[20:23], v[180:183], v[108:115], v192\n" \
  "v_lshlrev_b32_sdwa v66, 4, v219 dst_sel:DWORD dst_unused:UNUSED_PAD src0_sel:DWORD src1_sel:WORD_0\n" \
  "ds_read_b128 v[108:111], v66\n" \
  "v_lshlrev_b32_sdwa v67, 4, v219 dst_sel:DWORD dst_unused:UNUSED_PAD src0_sel:DWORD src1_sel:WORD_1\n" \
  "ds_read_b128 v[112:115], v67\n" \
  "s_waitcnt lgkmcnt(10)\n" \
  "v_smfmac_f32_16x16x64_bf16 v[24:27], v[180:183], v[68:75], v192\n" \
  "v_lshlrev_b32_sdwa v64, 4, v220 dst_sel:DWORD dst_unused:UNUSED_PAD src0_sel:DWORD src1_sel:WORD_0\n" \
  "ds_read_b128 v[68:71], v64\n" \
  "v_lshlrev_b32_sdwa v65, 4, v220 dst_sel:DWORD dst_unused:UNUSED_PAD src0_sel:DWORD src1_sel:WORD_1\n" \
  "ds_read_b128 v[72:75], v65\n" \
  "global_load_dwordx4 v[248:251], v188, s[56:57] offset:2048 sc1\n" \
  "global_load_dwordx4 v[252:255], v188, s[56:57] offset:3072 sc1\n" \
  "s_waitcnt lgkmcnt(10)\n" \
  "v_smfmac_f32_16x16x64_bf16 v[28:31], v[180:183], v[76:83], v192\n" \
  "v_lshlrev_b32_sdwa v66, 4, v221 dst_sel:DWORD dst_unused:UNUSED_PAD src0_sel:DWORD src1_sel:WORD_0\n" \
  "ds_read_b128 v[76:79], v66\n" \
  "v_lshlrev_b32_sdwa v67, 4, v221 dst_sel:DWORD dst_unused:UNUSED_PAD src0_sel:DWORD src1_sel:WORD_1\n" \
  "ds_read_b128 v[80:83], v67\n" \
  "s_waitcnt lgkmcnt(10)\n" \
  "v_smfmac_f32_16x16x64_bf16 v[32:35], v[180:183], v[84:91], v192\n" \
  "v_lshlrev_b32_sdwa v64, 4, v222 dst_sel:DWORD dst_unused:UNUSED_PAD src0_sel:DWORD src1_sel:WORD_0\n" \
  "ds_read_b128 v[84:87], v64\n" \
  "v_lshlrev_b32_sdwa v65, 4, v222 dst_sel:DWORD dst_unused:UNUSED_PAD src0_sel:DWORD src1_sel:WORD_1\n" \
  "ds_read_b128 v[88:91], v65\n" \
  "s_waitcnt lgkmcnt(10)\n" \
  "v_smfmac_f32_16x16x64_bf16 v[36:39], v[180:183], v[92:99], v192\n" \
  "v_lshlrev_b32_sdwa v66, 4, v223 dst_sel:DWORD dst_unused:UNUSED_PAD src0_sel:DWORD src1_sel:WORD_0\n" \
  "ds_read_b128 v[92:95], v66\n" \
  "v_lshlrev_b32_sdwa v67, 4, v223 dst_sel:DWORD dst_unused:UNUSED_PAD src0_sel:DWORD src1_sel:WORD_1\n" \
  "ds_read_b128 v[96:99], v67\n" \
  "s_add_u32 s56, s56, 32768\n" \
  "s_addc_u32 s57, s57, 0\n" \
  "s_add_u32 s54, s54, 64000\n" \
  "s_addc_u32 s55, s55, 0\n" \
  "s_sub_u32 s51, s51, 1\n" \
  "s_cmp_lg_u32 s51, 0\n" \
  "s_cbranch_scc1 LG_loop\n" \
  "s_waitcnt lgkmcnt(10)\n" \
  "v_smfmac_f32_16x16x64_bf16 v[40:43], v[180:183], v[100:107], v192\n" \
  "s_waitcnt lgkmcnt(8)\n" \
  "v_smfmac_f32_16x16x64_bf16 v[44:47], v[180:183], v[108:115], v192\n" \
  "s_waitcnt vmcnt(8)\n" \
  "s_barrier\n" \
  "v_lshlrev_b32_sdwa v64, 4, v224 dst_sel:DWORD dst_unused:UNUSED_PAD src0_sel:DWORD src1_sel:WORD_0\n" \
  "ds_read_b128 v[100:103], v64\n" \
  "v_lshlrev_b32_sdwa v65, 4, v224 dst_sel:DWORD dst_unused:UNUSED_PAD src0_sel:DWORD src1_sel:WORD_1\n" \
  "ds_read_b128 v[104:107], v65\n" \
  "v_lshlrev_b32_sdwa v66, 4, v225 dst_sel:DWORD dst_unused:UNUSED_PAD src0_sel:DWORD src1_sel:WORD_0\n" \
  "ds_read_b128 v[108:111], v66\n" \
  "v_lshlrev_b32_sdwa v67, 4, v225 dst_sel:DWORD dst_unused:UNUSED_PAD src0_sel:DWORD src1_sel:WORD_1\n" \
  "ds_read_b128 v[112:115], v67\n" \
  "s_waitcnt lgkmcnt(10)\n" \
  "v_smfmac_f32_16x16x64_bf16 v[48:51], v[180:183], v[68:75], v192\n" \
  "s_waitcnt lgkmcnt(8)\n" \
  "v_smfmac_f32_16x16x64_bf16 v[52:55], v[180:183], v[76:83], v192\n" \
  "s_waitcnt lgkmcnt(6)\n" \
  "v_smfmac_f32_16x16x64_bf16 v[56:59], v[180:183], v[84:91], v192\n" \
  "s_waitcnt lgkmcnt(4)\n" \
  "v_smfmac_f32_16x16x64_bf16 v[60:63], v[180:183], v[92:99], v192\n" \
  "s_barrier\n" \
  "s_mov_b32 m0, s58\n" \
  "s_add_u32 s35, s58, 0x8000\n" \
  "global_load_lds_dwordx4 v188, s[54:55] sc1\n" \
  "global_load_lds_dwordx4 v156, s[54:55] offset:1024 sc1\n" \
  "v_lshlrev_b32_sdwa v64, 4, v226 dst_sel:DWORD dst_unused:UNUSED_PAD src0_sel:DWORD src1_sel:WORD_0\n" \
  "ds_read_b128 v[68:71], v64\n" \
  "v_lshlrev_b32_sdwa v65, 4, v226 dst_sel:DWORD dst_unused:UNUSED_PAD src0_sel:DWORD src1_sel:WORD_1\n" \
  "ds_read_b128 v[72:75], v65\n" \
  "v_lshlrev_b32_sdwa v66, 4, v227 dst_sel:DWORD dst_unused:UNUSED_PAD src0_sel:DWORD src1_sel:WORD_0\n" \
  "ds_read_b128 v[76:79], v66\n" \
  "v_lshlrev_b32_sdwa v67, 4, v227 dst_sel:DWORD dst_unused:UNUSED_PAD src0_sel:DWORD src1_sel:WORD_1\n" \
  "ds_read_b128 v[80:83], v67\n" \
  "s_add_u32 m0, s58, 0x4000\n" \
  "s_cmp_lt_u32 s35, 0x28000\n" \
  "global_load_lds_dwordx4 v189, s[54:55] sc1\n" \
  "global_load_lds_dwordx4 v157, s[54:55] offset:1024 sc1\n" \
  "s_cselect_b32 s58, s35, s59\n" \
  "v_lshlrev_b32_sdwa v64, 4, v228 dst_sel:DWORD dst_unused:UNUSED_PAD src0_sel:DWORD src1_sel:WORD_0\n" \
  "ds_read_b128 v[84:87], v64\n" \
  "v_lshlrev_b32_sdwa v65, 4, v228 dst_sel:DWORD dst_unused:UNUSED_PAD src0_sel:DWORD src1_sel:WORD_1\n" \
  "ds_read_b128 v[88:91], v65\n" \
  "v_lshlrev_b32_sdwa v66, 4, v229 dst_sel:DWORD dst_unused:UNUSED_PAD src0_sel:DWORD src1_sel:WORD_0\n" \
  "ds_read_b128 v[92:95], v66\n" \
  "v_lshlrev_b32_sdwa v67, 4, v229 dst_sel:DWORD dst_unused:UNUSED_PAD src0_sel:DWORD src1_sel:WORD_1\n" \
  "ds_read_b128 v[96:99], v67\n" \
  "s_waitcnt lgkmcnt(10)\n" \
  "v_smfmac_f32_16x16x64_bf16 v[0:3], v[180:183], v[100:107], v192\n" \
  "v_lshlrev_b32_sdwa v64, 4, v230 dst_sel:DWORD dst_unused:UNUSED_PAD src0_sel:DWORD src1_sel:WORD_0\n" \
  "ds_read_b128 v[100:103], v64\n" \
  "v_lshlrev_b32_sdwa v65, 4, v230 dst_sel:DWORD dst_unused:UNUSED_PAD src0_sel:DWORD src1_sel:WORD_1\n" \
  "ds_read_b128 v[104:107], v65\n" \
  "s_mov_b32 m0, s58\n" \
  "s_add_u32 s35, s58, 0x8000\n" \
  "global_load_lds_dwordx4 v190, s[54:55] sc1\n" \
  "global_load_lds_dwordx4 v158, s[54:55] offset:1024 sc1\n" \
  "s_waitcnt lgkmcnt(10)\n" \
  "v_smfmac_f32_16x16x64_bf16 v[4:7], v[180:183], v[108:115], v192\n" \
  "v_lshlrev_b32_sdwa v66, 4, v231 dst_sel:DWORD dst_unused:UNUSED_PAD src0_sel:DWORD src1_sel:WORD_0\n" \
  "ds_read_b128 v[108:111], v66\n" \
  "v_lshlrev_b32_sdwa v67, 4, v231 dst_sel:DWORD dst_unused:UNUSED_PAD src0_sel:DWORD src1_sel:WORD_1\n" \
  "ds_read_b128 v[112:115], v67\n" \
  "s_waitcnt lgkmcnt(10)\n" \
  "v_smfmac_f32_16x16x64_bf16 v[8:11], v[180:183], v[68:75], v192\n" \
  "v_lshlrev_b32_sdwa v64, 4, v232 dst_sel:DWORD dst_unused:UNUSED_PAD src0_sel:DWORD src1_sel:WORD_0\n" \
  "ds_read_b128 v[68:71], v64\n" \
  "v_lshlrev_b32_sdwa v65, 4, v232 dst_sel:DWORD dst_unused:UNUSED_PAD src0_sel:DWORD src1_sel:WORD_1\n" \
  "ds_read_b128 v[72:75], v65\n" \
  "s_add_u32 m0, s58, 0x4000\n" \
  "s_cmp_lt_u32 s35, 0x28000\n" \
  "global_load_lds_dwordx4 v191, s[54:55] sc1\n" \
  "global_load_lds_dwordx4 v159, s[54:55] offset:1024 sc1\n" \
  "s_cselect_b32 s58, s35, s59\n" \
  "s_waitcnt lgkmcnt(10)\n" \
  "v_smfmac_f32_16x16x64_bf16 v[12:15], v[180:183], v[76:83], v192\n" \
  "v_lshlrev_b32_sdwa v66, 4, v233 dst_sel:DWORD dst_unused:UNUSED_PAD src0_sel:DWORD src1_sel:WORD_0\n" \
  "ds_read_b128 v[76:79], v66\n" \
  "v_lshlrev_b32_sdwa v67, 4, v233 dst_sel:DWORD dst_unused:UNUSED_PAD src0_sel:DWORD src1_sel:WORD_1\n" \
  "ds_read_b128 v[80:83], v67\n" \
  "s_waitcnt lgkmcnt(10)\n" \
  "v_smfmac_f32_16x16x64_bf16 v[16:19], v[180:183], v[84:91], v192\n" \
  "v_lshlrev_b32_sdwa v64, 4, v234 dst_sel:DWORD dst_unused:UNUSED_PAD src0_sel:DWORD src1_sel:WORD_0\n" \
  "ds_read_b128 v[84:87], v64\n" \
  "v_lshlrev_b32_sdwa v65, 4, v234 dst_sel:DWORD dst_unused:UNUSED_PAD src0_sel:DWORD src1_sel:WORD_1\n" \
  "ds_read_b128 v[88:91], v65\n" \
  "global_load_dwordx4 v[208:211], v188, s[56:57] sc1\n" \
  "global_load_dwordx4 v[212:215], v188, s[56:57] offset:1024 sc1\n" \
  "s_waitcnt lgkmcnt(10)\n" \
  "v_smfmac_f32_16x16x64_bf16 v[20:23], v[180:183], v[92:99], v192\n" \
  "v_lshlrev_b32_sdwa v66, 4, v235 dst_sel:DWORD dst_unused:UNUSED_PAD src0_sel:DWORD src1_sel:WORD_0\n" \
  "ds_read_b128 v[92:95], v66\n" \
  "v_lshlrev_b32_sdwa v67, 4, v235 dst_sel:DWORD dst_unused:UNUSED_PAD src0_sel:DWORD src1_sel:WORD_1\n" \
  "ds_read_b128 v[96:99], v67\n" \
  "s_waitcnt lgkmcnt(10)\n" \
  "v_smfmac_f32_16x16x64_bf16 v[24:27], v[180:183], v[100:107], v192\n" \
  "v_lshlrev_b32_sdwa v64, 4, v236 dst_sel:DWORD dst_unused:UNUSED_PAD src0_sel:DWORD src1_sel:WORD_0\n" \
  "ds_read_b128 v[100:103], v64\n" \
  "v_lshlrev_b32_sdwa v65, 4, v236 dst_sel:DWORD dst_unused:UNUSED_PAD src0_sel:DWORD src1_sel:WORD_1\n" \
  "ds_read_b128 v[104:107], v65\n" \
  "global_load_dwordx4 v[216:219], v188, s[56:57] offset:2048 sc1\n" \
  "global_load_dwordx4 v[220:223], v188, s[56:57] offset:3072 sc1\n" \
  "s_waitcnt lgkmcnt(10)\n" \
  "v_smfmac_f32_16x16x64_bf16 v[28:31], v[180:183], v[108:115], v192\n" \
  "v_lshlrev_b32_sdwa v66, 4, v237 dst_sel:DWORD dst_unused:UNUSED_PAD src0_sel:DWORD src1_sel:WORD_0\n" \
  "ds_read_b128 v[108:111], v66\n" \
  "v_lshlrev_b32_sdwa v67, 4, v237 dst_sel:DWORD dst_unused:UNUSED_PAD src0_sel:DWORD src1_sel:WORD_1\n" \
  "ds_read_b128 v[112:115], v67\n" \
  "s_waitcnt lgkmcnt(10)\n" \
  "v_smfmac_f32_16x16x64_bf16 v[32:35], v[180:183], v[68:75], v192\n" \
  "v_lshlrev_b32_sdwa v64, 4, v238 dst_sel:DWORD dst_unused:UNUSED_PAD src0_sel:DWORD src1_sel:WORD_0\n" \
  "ds_read_b128 v[68:71], v64\n" \
  "v_lshlrev_b32_sdwa v65, 4, v238 dst_sel:DWORD dst_unused:UNUSED_PAD src0_sel:DWORD src1_sel:WORD_1\n" \
  "ds_read_b128 v[72:75], v65\n" \
  "s_waitcnt lgkmcnt(10)\n" \
  "v_smfmac_f32_16x16x64_bf16 v[36:39], v[180:183], v[76:83], v192\n" \
  "v_lshlrev_b32_sdwa v66, 4, v239 dst_sel:DWORD dst_unused:UNUSED_PAD src0_sel:DWORD src1_sel:WORD_0\n" \
  "ds_read_b128 v[76:79], v66\n" \
  "v_lshlrev_b32_sdwa v67, 4, v239 dst_sel:DWORD dst_unused:UNUSED_PAD src0_sel:DWORD src1_sel:WORD_1\n" \
  "ds_read_b128 v[80:83], v67\n" \
  "s_add_u32 s56, s56, 32768\n" \
  "s_addc_u32 s57, s57, 0\n" \
  "s_add_u32 s54, s54, 64000\n" \
  "s_addc_u32 s55, s55, 0\n" \
  "s_waitcnt lgkmcnt(10)\n" \
  "v_smfmac_f32_16x16x64_bf16 v[40:43], v[180:183], v[84:91], v192\n" \
  "s_waitcnt lgkmcnt(8)\n" \
  "v_smfmac_f32_16x16x64_bf16 v[44:47], v[180:183], v[92:99], v192\n" \
  "s_waitcnt vmcnt(8)\n" \
  "s_barrier\n" \
  "v_lshlrev_b32_sdwa v64, 4, v240 dst_sel:DWORD dst_unused:UNUSED_PAD src0_sel:DWORD src1_sel:WORD_0\n" \
  "ds_read_b128 v[84:87], v64\n" \
  "v_lshlrev_b32_sdwa v65, 4, v240 dst_sel:DWORD dst_unused:UNUSED_PAD src0_sel:DWORD src1_sel:WORD_1\n" \
  "ds_read_b128 v[88:91], v65\n" \
  "v_lshlrev_b32_sdwa v66, 4, v241 dst_sel:DWORD dst_unused:UNUSED_PAD src0_sel:DWORD src1_sel:WORD_0\n" \
  "ds_read_b128 v[92:95], v66\n" \
  "v_lshlrev_b32_sdwa v67, 4, v241 dst_sel:DWORD dst_unused:UNUSED_PAD src0_sel:DWORD src1_sel:WORD_1\n" \
  "ds_read_b128 v[96:99], v67\n" \
  "s_waitcnt lgkmcnt(10)\n" \
  "v_smfmac_f32_16x16x64_bf16 v[48:51], v[180:183], v[100:107], v192\n" \
  "s_waitcnt lgkmcnt(8)\n" \
  "v_smfmac_f32_16x16x64_bf16 v[52:55], v[180:183], v[108:115], v192\n" \
  "s_waitcnt lgkmcnt(6)\n" \
  "v_smfmac_f32_16x16x64_bf16 v[56:59], v[180:183], v[68:75], v192\n" \
  "s_waitcnt lgkmcnt(4)\n" \
  "v_smfmac_f32_16x16x64_bf16 v[60:63], v[180:183], v[76:83], v192\n" \
  "s_barrier\n" \
  "s_mov_b32 m0, s58\n" \
  "s_add_u32 s35, s58, 0x8000\n" \
  "global_load_lds_dwordx4 v188, s[54:55] sc1\n" \
  "global_load_lds_dwordx4 v188, s[54:55] offset:1024 sc1\n" \
  "s_add_u32 m0, s58, 0x4000\n" \
  "s_cmp_lt_u32 s35, 0x28000\n" \
  "global_load_lds_dwordx4 v189, s[54:55] sc1\n" \
  "global_load_lds_dwordx4 v189, s[54:55] offset:1024 sc1\n" \
  "s_cselect_b32 s58, s35, s59\n" \
  "v_lshlrev_b32 v120, 16, v206\n" \
  "v_and_b32 v121, 0xffff0000, v206\n" \
  "v_lshlrev_b32 v122, 16, v207\n" \
  "v_and_b32 v123, 0xffff0000, v207\n" \
  "v_mov_b32 v124, 0xc0135761\n" \
  "v_mov_b32 v125, 0xc0135761\n" \
  "s_mov_b32 s52, 0xbdd2d3e8\n" \
  "s_mov_b32 s53, s52\n" \
  "s_lshl2_add_u32 s35, s33, s34\n" \
  "s_lshl_b32 s35, s35, 14\n" \
  "s_add_u32 s35, s35, s59\n" \
  "s_add_u32 s35, s35, 105676800\n" \
  "s_add_u32 s40, s24, s35\n" \
  "s_addc_u32 s41, s25, 0\n" \
  "v_lshlrev_b32_sdwa v64, 4, v242 dst_sel:DWORD dst_unused:UNUSED_PAD src0_sel:DWORD src1_sel:WORD_0\n" \
  "ds_read_b128 v[100:103], v64\n" \
  "v_lshlrev_b32_sdwa v65, 4, v242 dst_sel:DWORD dst_unused:UNUSED_PAD src0_sel:DWORD src1_sel:WORD_1\n" \
  "ds_read_b128 v[104:107], v65\n" \
  "v_lshlrev_b32_sdwa v66, 4, v243 dst_sel:DWORD dst_unused:UNUSED_PAD src0_sel:DWORD src1_sel:WORD_0\n" \
  "ds_read_b128 v[108:111], v66\n" \
  "v_lshlrev_b32_sdwa v67, 4, v243 dst_sel:DWORD dst_unused:UNUSED_PAD src0_sel:DWORD src1_sel:WORD_1\n" \
  "ds_read_b128 v[112:115], v67\n" \
  "v_lshlrev_b32_sdwa v64, 4, v244 dst_sel:DWORD dst_unused:UNUSED_PAD src0_sel:DWORD src1_sel:WORD_0\n" \
  "ds_read_b128 v[68:71], v64\n" \
  "v_lshlrev_b32_sdwa v65, 4, v244 dst_sel:DWORD dst_unused:UNUSED_PAD src0_sel:DWORD src1_sel:WORD_1\n" \
  "ds_read_b128 v[72:75], v65\n" \
  "v_lshlrev_b32_sdwa v66, 4, v245 dst_sel:DWORD dst_unused:UNUSED_PAD src0_sel:DWORD src1_sel:WORD_0\n" \
  "ds_read_b128 v[76:79], v66\n" \
  "v_lshlrev_b32_sdwa v67, 4, v245 dst_sel:DWORD dst_unused:UNUSED_PAD src0_sel:DWORD src1_sel:WORD_1\n" \
  "ds_read_b128 v[80:83], v67\n" \
  "s_waitcnt lgkmcnt(10)\n" \
  "v_smfmac_f32_16x16x64_bf16 v[0:3], v[180:183], v[84:91], v192\n" \
  "v_lshlrev_b32_sdwa v64, 4, v246 dst_sel:DWORD dst_unused:UNUSED_PAD src0_sel:DWORD src1_sel:WORD_0\n" \
  "ds_read_b128 v[84:87], v64\n" \
  "v_lshlrev_b32_sdwa v65, 4, v246 dst_sel:DWORD dst_unused:UNUSED_PAD src0_sel:DWORD src1_sel:WORD_1\n" \
  "ds_read_b128 v[88:91], v65\n" \
  "s_waitcnt lgkmcnt(10)\n" \
  "v_smfmac_f32_16x16x64_bf16 v[4:7], v[180:183], v[92:99], v192\n" \
  "v_lshlrev_b32_sdwa v66, 4, v247 dst_sel:DWORD dst_unused:UNUSED_PAD src0_sel:DWORD src1_sel:WORD_0\n" \
  "ds_read_b128 v[92:95], v66\n" \
  "v_lshlrev_b32_sdwa v67, 4, v247 dst_sel:DWORD dst_unused:UNUSED_PAD src0_sel:DWORD src1_sel:WORD_1\n" \
  "ds_read_b128 v[96:99], v67\n" \
  "s_waitcnt lgkmcnt(10)\n" \
  "v_smfmac_f32_16x16x64_bf16 v[8:11], v[180:183], v[100:107], v192\n" \
  "v_lshlrev_b32_sdwa v64, 4, v248 dst_sel:DWORD dst_unused:UNUSED_PAD src0_sel:DWORD src1_sel:WORD_0\n" \
  "ds_read_b128 v[100:103], v64\n" \
  "v_lshlrev_b32_sdwa v65, 4, v248 dst_sel:DWORD dst_unused:UNUSED_PAD src0_sel:DWORD src1_sel:WORD_1\n" \
  "ds_read_b128 v[104:107], v65\n" \
  "s_waitcnt lgkmcnt(10)\n" \
  "v_smfmac_f32_16x16x64_bf16 v[12:15], v[180:183], v[108:115], v192\n" \
  "v_lshlrev_b32_sdwa v66, 4, v249 dst_sel:DWORD dst_unused:UNUSED_PAD src0_sel:DWORD src1_sel:WORD_0\n" \
  "ds_read_b128 v[108:111], v66\n" \
  "v_lshlrev_b32_sdwa v67, 4, v249 dst_sel:DWORD dst_unused:UNUSED_PAD src0_sel:DWORD src1_sel:WORD_1\n" \
  "ds_read_b128 v[112:115], v67\n" \
  "s_waitcnt vmcnt(0)\n" \
  "s_barrier\n" \
  "s_waitcnt lgkmcnt(10)\n" \
  "v_smfmac_f32_16x16x64_bf16 v[16:19], v[180:183], v[68:75], v192\n" \
  "s_waitcnt lgkmcnt(8)\n" \
  "v_smfmac_f32_16x16x64_bf16 v[20:23], v[180:183], v[76:83], v192\n" \
  "s_waitcnt lgkmcnt(6)\n" \
  "v_smfmac_f32_16x16x64_bf16 v[24:27], v[180:183], v[84:91], v192\n" \
  "s_waitcnt lgkmcnt(4)\n" \
  "v_smfmac_f32_16x16x64_bf16 v[28:31], v[180:183], v[92:99], v192\n" \
  "v_lshlrev_b32_sdwa v64, 4, v208 dst_sel:DWORD dst_unused:UNUSED_PAD src0_sel:DWORD src1_sel:WORD_0\n" \
  "ds_read_b128 v[68:71], v64\n" \
  "v_lshlrev_b32_sdwa v65, 4, v208 dst_sel:DWORD dst_unused:UNUSED_PAD src0_sel:DWORD src1_sel:WORD_1\n" \
  "ds_read_b128 v[72:75], v65\n" \
  "s_waitcnt lgkmcnt(4)\n" \
  "v_smfmac_f32_16x16x64_bf16 v[32:35], v[180:183], v[100:107], v192\n" \
  "v_lshlrev_b32_sdwa v66, 4, v209 dst_sel:DWORD dst_unused:UNUSED_PAD src0_sel:DWORD src1_sel:WORD_0\n" \
  "ds_read_b128 v[76:79], v66\n" \
  "v_lshlrev_b32_sdwa v67, 4, v209 dst_sel:DWORD dst_unused:UNUSED_PAD src0_sel:DWORD src1_sel:WORD_1\n" \
  "ds_read_b128 v[80:83], v67\n" \
  "s_waitcnt lgkmcnt(4)\n" \
  "v_smfmac_f32_16x16x64_bf16 v[36:39], v[180:183], v[108:115], v192\n" \
  "v_lshlrev_b32_sdwa v64, 4, v210 dst_sel:DWORD dst_unused:UNUSED_PAD src0_sel:DWORD src1_sel:WORD_0\n" \
  "ds_read_b128 v[84:87], v64\n" \
  "v_lshlrev_b32_sdwa v65, 4, v210 dst_sel:DWORD dst_unused:UNUSED_PAD src0_sel:DWORD src1_sel:WORD_1\n" \
  "ds_read_b128 v[88:91], v65\n" \
  "s_waitcnt lgkmcnt(4)\n" \
  "v_smfmac_f32_16x16x64_bf16 v[0:3], v[180:183], v[68:75], v192\n" \
  "v_lshlrev_b32_sdwa v66, 4, v211 dst_sel:DWORD dst_unused:UNUSED_PAD src0_sel:DWORD src1_sel:WORD_0\n" \
  "ds_read_b128 v[92:95], v66\n" \
  "v_lshlrev_b32_sdwa v67, 4, v211 dst_sel:DWORD dst_unused:UNUSED_PAD src0_sel:DWORD src1_sel:WORD_1\n" \
  "ds_read_b128 v[96:99], v67\n" \
  "s_waitcnt lgkmcnt(4)\n" \
  "v_smfmac_f32_16x16x64_bf16 v[4:7], v[180:183], v[76:83], v192\n" \
  "v_lshlrev_b32_sdwa v64, 4, v212 dst_sel:DWORD dst_unused:UNUSED_PAD src0_sel:DWORD src1_sel:WORD_0\n" \
  "ds_read_b128 v[100:103], v64\n" \
  "v_lshlrev_b32_sdwa v65, 4, v212 dst_sel:DWORD dst_unused:UNUSED_PAD src0_sel:DWORD src1_sel:WORD_1\n" \
  "ds_read_b128 v[104:107], v65\n" \
  "v_pk_mul_f32 v[128:129], v[0:1], v[0:1]\n" \
  "v_pk_mul_f32 v[130:131], v[2:3], v[2:3]\n" \
  "v_pk_fma_f32 v[128:129], v[128:129], s[52:53], v[124:125]\n" \
  "v_pk_fma_f32 v[130:131], v[130:131], s[52:53], v[124:125]\n" \
  "v_pk_mul_f32 v[128:129], v[128:129], v[0:1]\n" \
  "v_pk_mul_f32 v[130:131], v[130:131], v[2:3]\n" \
  "v_exp_f32 v128, v128\n" \
  "v_exp_f32 v129, v129\n" \
  "v_pk_mul_f32 v[0:1], v[0:1], v[120:121]\n" \
  "v_exp_f32 v130, v130\n" \
  "v_exp_f32 v131, v131\n" \
  "v_pk_mul_f32 v[2:3], v[2:3], v[122:123]\n" \
  "v_pk_add_f32 v[128:129], v[128:129], 1.0 op_sel_hi:[1,0]\n" \
  "v_pk_add_f32 v[130:131], v[130:131], 1.0 op_sel_hi:[1,0]\n" \
  "v_rcp_f32 v128, v128\n" \
  "v_rcp_f32 v129, v129\n" \
  "s_waitcnt lgkmcnt(4)\n" \
  "v_smfmac_f32_16x16x64_bf16 v[8:11], v[180:183], v[84:91], v192\n" \
  "v_lshlrev_b32_sdwa v66, 4, v213 dst_sel:DWORD dst_unused:UNUSED_PAD src0_sel:DWORD src1_sel:WORD_0\n" \
  "ds_read_b128 v[108:111], v66\n" \
  "v_lshlrev_b32_sdwa v67, 4, v213 dst_sel:DWORD dst_unused:UNUSED_PAD src0_sel:DWORD src1_sel:WORD_1\n" \
  "ds_read_b128 v[112:115], v67\n" \
  "v_rcp_f32 v130, v130\n" \
  "v_rcp_f32 v131, v131\n" \
  "v_pk_mul_f32 v[0:1], v[0:1], v[128:129]\n" \
  "v_pk_fma_f32 v[0:1], v[2:3], v[130:131], v[0:1]\n" \
  "v_add_f32 v0, v0, v1\n" \
  "v_pk_mul_f32 v[132:133], v[4:5], v[4:5]\n" \
  "v_pk_mul_f32 v[134:135], v[6:7], v[6:7]\n" \
  "v_pk_fma_f32 v[132:133], v[132:133], s[52:53], v[124:125]\n" \
  "v_pk_fma_f32 v[134:135], v[134:135], s[52:53], v[124:125]\n" \
  "v_pk_mul_f32 v[132:133], v[132:133], v[4:5]\n" \
  "v_pk_mul_f32 v[134:135], v[134:135], v[6:7]\n" \
  "v_exp_f32 v132, v132\n" \
  "v_exp_f32 v133, v133\n" \
  "v_pk_mul_f32 v[4:5], v[4:5], v[120:121]\n" \
  "v_exp_f32 v134, v134\n" \
  "v_exp_f32 v135, v135\n" \
  "s_waitcnt lgkmcnt(4)\n" \
  "v_smfmac_f32_16x16x64_bf16 v[12:15], v[180:183], v[92:99], v192\n" \
  "v_lshlrev_b32_sdwa v64, 4, v214 dst_sel:DWORD dst_unused:UNUSED_PAD src0_sel:DWORD src1_sel:WORD_0\n" \
  "ds_read_b128 v[68:71], v64\n" \
  "v_lshlrev_b32_sdwa v65, 4, v214 dst_sel:DWORD dst_unused:UNUSED_PAD src0_sel:DWORD src1_sel:WORD_1\n" \
  "ds_read_b128 v[72:75], v65\n" \
  "v_pk_mul_f32 v[6:7], v[6:7], v[122:123]\n" \
  "v_pk_add_f32 v[132:133], v[132:133], 1.0 op_sel_hi:[1,0]\n" \
  "v_pk_add_f32 v[134:135], v[134:135], 1.0 op_sel_hi:[1,0]\n" \
  "v_rcp_f32 v132, v132\n" \
  "v_rcp_f32 v133, v133\n" \
  "v_rcp_f32 v134, v134\n" \
  "v_rcp_f32 v135, v135\n" \
  "v_pk_mul_f32 v[4:5], v[4:5], v[132:133]\n" \
  "v_pk_fma_f32 v[4:5], v[6:7], v[134:135], v[4:5]\n" \
  "v_add_f32 v4, v4, v5\n" \
  "v_pk_mul_f32 v[136:137], v[8:9], v[8:9]\n" \
  "v_pk_mul_f32 v[138:139], v[10:11], v[10:11]\n" \
  "v_pk_fma_f32 v[136:137], v[136:137], s[52:53], v[124:125]\n" \
  "v_pk_fma_f32 v[138:139], v[138:139], s[52:53], v[124:125]\n" \
  "v_pk_mul_f32 v[136:137], v[136:137], v[8:9]\n" \
  "v_pk_mul_f32 v[138:139], v[138:139], v[10:11]\n" \
  "s_waitcnt lgkmcnt(4)\n" \
  "v_smfmac_f32_16x16x64_bf16 v[16:19], v[180:183], v[100:107], v192\n" \
  "v_lshlrev_b32_sdwa v66, 4, v215 dst_sel:DWORD dst_unused:UNUSED_PAD src0_sel:DWORD src1_sel:WORD_0\n" \
  "ds_read_b128 v[76:79], v66\n" \
  "v_lshlrev_b32_sdwa v67, 4, v215 dst_sel:DWORD dst_unused:UNUSED_PAD src0_sel:DWORD src1_sel:WORD_1\n" \
  "ds_read_b128 v[80:83], v67\n" \
  "v_exp_f32 v136, v136\n" \
  "v_exp_f32 v137, v137\n" \
  "v_pk_mul_f32 v[8:9], v[8:9], v[120:121]\n" \
  "v_exp_f32 v138, v138\n" \
  "v_exp_f32 v139, v139\n" \
  "v_pk_mul_f32 v[10:11], v[10:11], v[122:123]\n" \
  "v_pk_add_f32 v[136:137], v[136:137], 1.0 op_sel_hi:[1,0]\n" \
  "v_pk_add_f32 v[138:139], v[138:139], 1.0 op_sel_hi:[1,0]\n" \
  "v_rcp_f32 v136, v136\n" \
  "v_rcp_f32 v137, v137\n" \
  "v_rcp_f32 v138, v138\n" \
  "v_rcp_f32 v139, v139\n" \
  "v_pk_mul_f32 v[8:9], v[8:9], v[136:137]\n" \
  "v_pk_fma_f32 v[8:9], v[10:11], v[138:139], v[8:9]\n" \
  "v_add_f32 v8, v8, v9\n" \
  "v_pk_mul_f32 v[140:141], v[12:13], v[12:13]\n" \
  "s_waitcnt lgkmcnt(4)\n" \
  "v_smfmac_f32_16x16x64_bf16 v[20:23], v[180:183], v[108:115], v192\n" \
  "v_lshlrev_b32_sdwa v64, 4, v216 dst_sel:DWORD dst_unused:UNUSED_PAD src0_sel:DWORD src1_sel:WORD_0\n" \
  "ds_read_b128 v[84:87], v64\n" \
  "v_lshlrev_b32_sdwa v65, 4, v216 dst_sel:DWORD dst_unused:UNUSED_PAD src0_sel:DWORD src1_sel:WORD_1\n" \
  "ds_read_b128 v[88:91], v65\n" \
  "v_pk_mul_f32 v[142:143], v[14:15], v[14:15]\n" \
  "v_pk_fma_f32 v[140:141], v[140:141], s[52:53], v[124:125]\n" \
  "v_pk_fma_f32 v[142:143], v[142:143], s[52:53], v[124:125]\n" \
  "v_pk_mul_f32 v[140:141], v[140:141], v[12:13]\n" \
  "v_pk_mul_f32 v[142:143], v[142:143], v[14:15]\n" \
  "v_exp_f32 v140, v140\n" \
  "v_exp_f32 v141, v141\n" \
  "v_pk_mul_f32 v[12:13], v[12:13], v[120:121]\n" \
  "v_exp_f32 v142, v142\n" \
  "v_exp_f32 v143, v143\n" \
  "v_pk_mul_f32 v[14:15], v[14:15], v[122:123]\n" \
  "v_pk_add_f32 v[140:141], v[140:141], 1.0 op_sel_hi:[1,0]\n" \
  "v_pk_add_f32 v[142:143], v[142:143], 1.0 op_sel_hi:[1,0]\n" \
  "v_rcp_f32 v140, v140\n" \
  "v_rcp_f32 v141, v141\n" \
  "v_rcp_f32 v142, v142\n" \
  "s_waitcnt lgkmcnt(4)\n" \
  "v_smfmac_f32_16x16x64_bf16 v[24:27], v[180:183], v[68:75], v192\n" \
  "v_lshlrev_b32_sdwa v66, 4, v217 dst_sel:DWORD dst_unused:UNUSED_PAD src0_sel:DWORD src1_sel:WORD_0\n" \
  "ds_read_b128 v[92:95], v66\n" \
  "v_lshlrev_b32_sdwa v67, 4, v217 dst_sel:DWORD dst_unused:UNUSED_PAD src0_sel:DWORD src1_sel:WORD_1\n" \
  "ds_read_b128 v[96:99], v67\n" \
  "v_rcp_f32 v143, v143\n" \
  "v_pk_mul_f32 v[12:13], v[12:13], v[140:141]\n" \
  "v_pk_fma_f32 v[12:13], v[14:15], v[142:143], v[12:13]\n" \
  "v_add_f32 v12, v12, v13\n" \
  "v_pk_mul_f32 v[128:129], v[16:17], v[16:17]\n" \
  "v_pk_mul_f32 v[130:131], v[18:19], v[18:19]\n" \
  "v_pk_fma_f32 v[128:129], v[128:129], s[52:53], v[124:125]\n" \
  "v_pk_fma_f32 v[130:131], v[130:131], s[52:53], v[124:125]\n" \
  "v_pk_mul_f32 v[128:129], v[128:129], v[16:17]\n" \
  "v_pk_mul_f32 v[130:131], v[130:131], v[18:19]\n" \
  "v_exp_f32 v128, v128\n" \
  "v_exp_f32 v129, v129\n" \
  "v_pk_mul_f32 v[16:17], v[16:17], v[120:121]\n" \
  "v_exp_f32 v130, v130\n" \
  "v_exp_f32 v131, v131\n" \
  "v_pk_mul_f32 v[18:19], v[18:19], v[122:123]\n" \
  "s_waitcnt lgkmcnt(4)\n" \
  "v_smfmac_f32_16x16x64_bf16 v[28:31], v[180:183], v[76:83], v192\n" \
  "v_lshlrev_b32_sdwa v64, 4, v250 dst_sel:DWORD dst_unused:UNUSED_PAD src0_sel:DWORD src1_sel:WORD_0\n" \
  "ds_read_b128 v[100:103], v64\n" \
  "v_lshlrev_b32_sdwa v65, 4, v250 dst_sel:DWORD dst_unused:UNUSED_PAD src0_sel:DWORD src1_sel:WORD_1\n" \
  "ds_read_b128 v[104:107], v65\n" \
  "v_pk_add_f32 v[128:129], v[128:129], 1.0 op_sel_hi:[1,0]\n" \
  "v_pk_add_f32 v[130:131], v[130:131], 1.0 op_sel_hi:[1,0]\n" \
  "v_rcp_f32 v128, v128\n" \
  "v_rcp_f32 v129, v129\n" \
  "v_rcp_f32 v130, v130\n" \
  "v_rcp_f32 v131, v131\n" \
  "v_pk_mul_f32 v[16:17], v[16:17], v[128:129]\n" \
  "v_pk_fma_f32 v[16:17], v[18:19], v[130:131], v[16:17]\n" \
  "v_add_f32 v16, v16, v17\n" \
  "v_pk_mul_f32 v[132:133], v[20:21], v[20:21]\n" \
  "v_pk_mul_f32 v[134:135], v[22:23], v[22:23]\n" \
  "v_pk_fma_f32 v[132:133], v[132:133], s[52:53], v[124:125]\n" \
  "v_pk_fma_f32 v[134:135], v[134:135], s[52:53], v[124:125]\n" \
  "v_pk_mul_f32 v[132:133], v[132:133], v[20:21]\n" \
  "v_pk_mul_f32 v[134:135], v[134:135], v[22:23]\n" \
  "v_exp_f32 v132, v132\n" \
  "s_waitcnt lgkmcnt(4)\n" \
  "v_smfmac_f32_16x16x64_bf16 v[32:35], v[180:183], v[84:91], v192\n" \
  "v_lshlrev_b32_sdwa v66, 4, v218 dst_sel:DWORD dst_unused:UNUSED_PAD src0_sel:DWORD src1_sel:WORD_0\n" \
  "ds_read_b128 v[108:111], v66\n" \
  "v_lshlrev_b32_sdwa v67, 4, v218 dst_sel:DWORD dst_unused:UNUSED_PAD src0_sel:DWORD src1_sel:WORD_1\n" \
  "ds_read_b128 v[112:115], v67\n" \
  "v_exp_f32 v133, v133\n" \
  "v_pk_mul_f32 v[20:21], v[20:21], v[120:121]\n" \
  "v_exp_f32 v134, v134\n" \
  "v_exp_f32 v135, v135\n" \
  "v_pk_mul_f32 v[22:23], v[22:23], v[122:123]\n" \
  "v_pk_add_f32 v[132:133], v[132:133], 1.0 op_sel_hi:[1,0]\n" \
  "v_pk_add_f32 v[134:135], v[134:135], 1.0 op_sel_hi:[1,0]\n" \
  "v_rcp_f32 v132, v132\n" \
  "v_rcp_f32 v133, v133\n" \
  "v_rcp_f32 v134, v134\n" \
  "v_rcp_f32 v135, v135\n" \
  "v_pk_mul_f32 v[20:21], v[20:21], v[132:133]\n" \
  "v_pk_fma_f32 v[20:21], v[22:23], v[134:135], v[20:21]\n" \
  "v_add_f32 v20, v20, v21\n" \
  "v_pk_mul_f32 v[136:137], v[24:25], v[24:25]\n" \
  "v_pk_mul_f32 v[138:139], v[26:27], v[26:27]\n" \
  "s_waitcnt lgkmcnt(4)\n" \
  "v_smfmac_f32_16x16x64_bf16 v[36:39], v[180:183], v[92:99], v192\n" \
  "v_lshlrev_b32_sdwa v64, 4, v251 dst_sel:DWORD dst_unused:UNUSED_PAD src0_sel:DWORD src1_sel:WORD_0\n" \
  "ds_read_b128 v[68:71], v64\n" \
  "v_lshlrev_b32_sdwa v65, 4, v251 dst_sel:DWORD dst_unused:UNUSED_PAD src0_sel:DWORD src1_sel:WORD_1\n" \
  "ds_read_b128 v[72:75], v65\n" \
  "v_pk_fma_f32 v[136:137], v[136:137], s[52:53], v[124:125]\n" \
  "v_pk_fma_f32 v[138:139], v[138:139], s[52:53], v[124:125]\n" \
  "v_pk_mul_f32 v[136:137], v[136:137], v[24:25]\n" \
  "v_pk_mul_f32 v[138:139], v[138:139], v[26:27]\n" \
  "v_exp_f32 v136, v136\n" \
  "v_exp_f32 v137, v137\n" \
  "v_pk_mul_f32 v[24:25], v[24:25], v[120:121]\n" \
  "v_exp_f32 v138, v138\n" \
  "v_exp_f32 v139, v139\n" \
  "v_pk_mul_f32 v[26:27], v[26:27], v[122:123]\n" \
  "v_pk_add_f32 v[136:137], v[136:137], 1.0 op_sel_hi:[1,0]\n" \
  "v_pk_add_f32 v[138:139], v[138:139], 1.0 op_sel_hi:[1,0]\n" \
  "v_rcp_f32 v136, v136\n" \
  "v_rcp_f32 v137, v137\n" \
  "v_rcp_f32 v138, v138\n" \
  "v_rcp_f32 v139, v139\n" \
  "s_waitcnt lgkmcnt(4)\n" \
  "v_smfmac_f32_16x16x64_bf16 v[40:43], v[180:183], v[100:107], v192\n" \
  "v_lshlrev_b32_sdwa v66, 4, v219 dst_sel:DWORD dst_unused:UNUSED_PAD src0_sel:DWORD src1_sel:WORD_0\n" \
  "ds_read_b128 v[76:79], v66\n" \
  "v_lshlrev_b32_sdwa v67, 4, v219 dst_sel:DWORD dst_unused:UNUSED_PAD src0_sel:DWORD src1_sel:WORD_1\n" \
  "ds_read_b128 v[80:83], v67\n" \
  "v_pk_mul_f32 v[24:25], v[24:25], v[136:137]\n" \
  "v_pk_fma_f32 v[24:25], v[26:27], v[138:139], v[24:25]\n" \
  "v_add_f32 v24, v24, v25\n" \
  "v_pk_mul_f32 v[140:141], v[28:29], v[28:29]\n" \
  "v_pk_mul_f32 v[142:143], v[30:31], v[30:31]\n" \
  "v_pk_fma_f32 v[140:141], v[140:141], s[52:53], v[124:125]\n" \
  "v_pk_fma_f32 v[142:143], v[142:143], s[52:53], v[124:125]\n" \
  "v_pk_mul_f32 v[140:141], v[140:141], v[28:29]\n" \
  "v_pk_mul_f32 v[142:143], v[142:143], v[30:31]\n" \
  "v_exp_f32 v140, v140\n" \
  "v_exp_f32 v141, v141\n" \
  "v_pk_mul_f32 v[28:29], v[28:29], v[120:121]\n" \
  "v_exp_f32 v142, v142\n" \
  "v_exp_f32 v143, v143\n" \
  "v_pk_mul_f32 v[30:31], v[30:31], v[122:123]\n" \
  "v_pk_add_f32 v[140:141], v[140:141], 1.0 op_sel_hi:[1,0]\n" \
  "s_waitcnt lgkmcnt(4)\n" \
  "v_smfmac_f32_16x16x64_bf16 v[40:43], v[180:183], v[108:115], v192\n" \
  "v_lshlrev_b32_sdwa v64, 4, v252 dst_sel:DWORD dst_unused:UNUSED_PAD src0_sel:DWORD src1_sel:WORD_0\n" \
  "ds_read_b128 v[84:87], v64\n" \
  "v_lshlrev_b32_sdwa v65, 4, v252 dst_sel:DWORD dst_unused:UNUSED_PAD src0_sel:DWORD src1_sel:WORD_1\n" \
  "ds_read_b128 v[88:91], v65\n" \
  "v_pk_add_f32 v[142:143], v[142:143], 1.0 op_sel_hi:[1,0]\n" \
  "v_rcp_f32 v140, v140\n" \
  "v_rcp_f32 v141, v141\n" \
  "v_rcp_f32 v142, v142\n" \
  "v_rcp_f32 v143, v143\n" \
  "v_pk_mul_f32 v[28:29], v[28:29], v[140:141]\n" \
  "v_pk_fma_f32 v[28:29], v[30:31], v[142:143], v[28:29]\n" \
  "v_add_f32 v28, v28, v29\n" \
  "v_permlane16_swap_b32 v0, v16\n" \
  "v_permlane16_swap_b32 v4, v20\n" \
  "v_permlane16_swap_b32 v8, v24\n" \
  "v_permlane16_swap_b32 v12, v28\n" \
  "v_add_f32 v144, v0, v16\n" \
  "v_add_f32 v145, v4, v20\n" \
  "v_add_f32 v146, v8, v24\n" \
  "v_add_f32 v147, v12, v28\n" \
  "s_waitcnt lgkmcnt(4)\n" \
  "v_smfmac_f32_16x16x64_bf16 v[44:47], v[180:183], v[68:75], v192\n" \
  "v_lshlrev_b32_sdwa v66, 4, v220 dst_sel:DWORD dst_unused:UNUSED_PAD src0_sel:DWORD src1_sel:WORD_0\n" \
  "ds_read_b128 v[92:95], v66\n" \
  "v_lshlrev_b32_sdwa v67, 4, v220 dst_sel:DWORD dst_unused:UNUSED_PAD src0_sel:DWORD src1_sel:WORD_1\n" \
  "ds_read_b128 v[96:99], v67\n" \
  "v_min_u32 v144, -2, v144\n" \
  "v_min_u32 v145, -2, v145\n" \
  "v_min_u32 v146, -2, v146\n" \
  "v_min_u32 v147, -2, v147\n" \
  "global_store_dwordx4 v188, v[144:147], s[40:41] offset:0 sc1\n" \
  "v_pk_mul_f32 v[128:129], v[32:33], v[32:33]\n" \
  "v_pk_mul_f32 v[130:131], v[34:35], v[34:35]\n" \
  "v_pk_fma_f32 v[128:129], v[128:129], s[52:53], v[124:125]\n" \
  "v_pk_fma_f32 v[130:131], v[130:131], s[52:53], v[124:125]\n" \
  "v_pk_mul_f32 v[128:129], v[128:129], v[32:33]\n" \
  "v_pk_mul_f32 v[130:131], v[130:131], v[34:35]\n" \
  "v_exp_f32 v128, v128\n" \
  "v_exp_f32 v129, v129\n" \
  "v_pk_mul_f32 v[32:33], v[32:33], v[120:121]\n" \
  "v_exp_f32 v130, v130\n" \
  "v_exp_f32 v131, v131\n" \
  "s_waitcnt lgkmcnt(4)\n" \
  "v_smfmac_f32_16x16x64_bf16 v[44:47], v[180:183], v[76:83], v192\n" \
  "v_lshlrev_b32_sdwa v64, 4, v253 dst_sel:DWORD dst_unused:UNUSED_PAD src0_sel:DWORD src1_sel:WORD_0\n" \
  "ds_read_b128 v[100:103], v64\n" \
  "v_lshlrev_b32_sdwa v65, 4, v253 dst_sel:DWORD dst_unused:UNUSED_PAD src0_sel:DWORD src1_sel:WORD_1\n" \
  "ds_read_b128 v[104:107], v65\n" \
  "v_pk_mul_f32 v[34:35], v[34:35], v[122:123]\n" \
  "v_pk_add_f32 v[128:129], v[128:129], 1.0 op_sel_hi:[1,0]\n" \
  "v_pk_add_f32 v[130:131], v[130:131], 1.0 op_sel_hi:[1,0]\n" \
  "v_rcp_f32 v128, v128\n" \
  "v_rcp_f32 v129, v129\n" \
  "v_rcp_f32 v130, v130\n" \
  "v_rcp_f32 v131, v131\n" \
  "v_pk_mul_f32 v[32:33], v[32:33], v[128:129]\n" \
  "v_pk_fma_f32 v[32:33], v[34:35], v[130:131], v[32:33]\n" \
  "v_add_f32 v32, v32, v33\n" \
  "v_pk_mul_f32 v[132:133], v[36:37], v[36:37]\n" \
  "v_pk_mul_f32 v[134:135], v[38:39], v[38:39]\n" \
  "v_pk_fma_f32 v[132:133], v[132:133], s[52:53], v[124:125]\n" \
  "v_pk_fma_f32 v[134:135], v[134:135], s[52:53], v[124:125]\n" \
  "v_pk_mul_f32 v[132:133], v[132:133], v[36:37]\n" \
  "v_pk_mul_f32 v[134:135], v[134:135], v[38:39]\n" \
  "s_waitcnt lgkmcnt(4)\n" \
  "v_smfmac_f32_16x16x64_bf16 v[48:51], v[180:183], v[84:91], v192\n" \
  "v_lshlrev_b32_sdwa v66, 4, v221 dst_sel:DWORD dst_unused:UNUSED_PAD src0_sel:DWORD src1_sel:WORD_0\n" \
  "ds_read_b128 v[108:111], v66\n" \
  "v_lshlrev_b32_sdwa v67, 4, v221 dst_sel:DWORD dst_unused:UNUSED_PAD src0_sel:DWORD src1_sel:WORD_1\n" \
  "ds_read_b128 v[112:115], v67\n" \
  "v_exp_f32 v132, v132\n" \
  "v_exp_f32 v133, v133\n" \
  "v_pk_mul_f32 v[36:37], v[36:37], v[120:121]\n" \
  "v_exp_f32 v134, v134\n" \
  "v_exp_f32 v135, v135\n" \
  "v_pk_mul_f32 v[38:39], v[38:39], v[122:123]\n" \
  "v_pk_add_f32 v[132:133], v[132:133], 1.0 op_sel_hi:[1,0]\n" \
  "v_pk_add_f32 v[134:135], v[134:135], 1.0 op_sel_hi:[1,0]\n" \
  "v_rcp_f32 v132, v132\n" \
  "v_rcp_f32 v133, v133\n" \
  "v_rcp_f32 v134, v134\n" \
  "v_rcp_f32 v135, v135\n" \
  "v_pk_mul_f32 v[36:37], v[36:37], v[132:133]\n" \
  "v_pk_fma_f32 v[36:37], v[38:39], v[134:135], v[36:37]\n" \
  "v_add_f32 v36, v36, v37\n" \
  "v_pk_mul_f32 v[136:137], v[40:41], v[40:41]\n" \
  "s_waitcnt lgkmcnt(4)\n" \
  "v_smfmac_f32_16x16x64_bf16 v[48:51], v[180:183], v[92:99], v192\n" \
  "v_lshlrev_b32_sdwa v64, 4, v254 dst_sel:DWORD dst_unused:UNUSED_PAD src0_sel:DWORD src1_sel:WORD_0\n" \
  "ds_read_b128 v[68:71], v64\n" \
  "v_lshlrev_b32_sdwa v65, 4, v254 dst_sel:DWORD dst_unused:UNUSED_PAD src0_sel:DWORD src1_sel:WORD_1\n" \
  "ds_read_b128 v[72:75], v65\n" \
  "v_pk_mul_f32 v[138:139], v[42:43], v[42:43]\n" \
  "v_pk_fma_f32 v[136:137], v[136:137], s[52:53], v[124:125]\n" \
  "v_pk_fma_f32 v[138:139], v[138:139], s[52:53], v[124:125]\n" \
  "v_pk_mul_f32 v[136:137], v[136:137], v[40:41]\n" \
  "v_pk_mul_f32 v[138:139], v[138:139], v[42:43]\n" \
  "v_exp_f32 v136, v136\n" \
  "v_exp_f32 v137, v137\n" \
  "v_pk_mul_f32 v[40:41], v[40:41], v[120:121]\n" \
  "v_exp_f32 v138, v138\n" \
  "v_exp_f32 v139, v139\n" \
  "v_pk_mul_f32 v[42:43], v[42:43], v[122:123]\n" \
  "v_pk_add_f32 v[136:137], v[136:137], 1.0 op_sel_hi:[1,0]\n" \
  "v_pk_add_f32 v[138:139], v[138:139], 1.0 op_sel_hi:[1,0]\n" \
  "v_rcp_f32 v136, v136\n" \
  "v_rcp_f32 v137, v137\n" \
  "v_rcp_f32 v138, v138\n" \
  "s_waitcnt lgkmcnt(4)\n" \
  "v_smfmac_f32_16x16x64_bf16 v[52:55], v[180:183], v[100:107], v192\n" \
  "v_lshlrev_b32_sdwa v66, 4, v222 dst_sel:DWORD dst_unused:UNUSED_PAD src0_sel:DWORD src1_sel:WORD_0\n" \
  "ds_read_b128 v[76:79], v66\n" \
  "v_lshlrev_b32_sdwa v67, 4, v222 dst_sel:DWORD dst_unused:UNUSED_PAD src0_sel:DWORD src1_sel:WORD_1\n" \
  "ds_read_b128 v[80:83], v67\n" \
  "v_rcp_f32 v139, v139\n" \
  "v_pk_mul_f32 v[40:41], v[40:41], v[136:137]\n" \
  "v_pk_fma_f32 v[40:41], v[42:43], v[138:139], v[40:41]\n" \
  "v_add_f32 v40, v40, v41\n" \
  "v_pk_mul_f32 v[140:141], v[44:45], v[44:45]\n" \
  "v_pk_mul_f32 v[142:143], v[46:47], v[46:47]\n" \
  "v_pk_fma_f32 v[140:141], v[140:141], s[52:53], v[124:125]\n" \
  "v_pk_fma_f32 v[142:143], v[142:143], s[52:53], v[124:125]\n" \
  "v_pk_mul_f32 v[140:141], v[140:141], v[44:45]\n" \
  "v_pk_mul_f32 v[142:143], v[142:143], v[46:47]\n" \
  "v_exp_f32 v140, v140\n" \
  "v_exp_f32 v141, v141\n" \
  "v_pk_mul_f32 v[44:45], v[44:45], v[120:121]\n" \
  "v_exp_f32 v142, v142\n" \
  "v_exp_f32 v143, v143\n" \
  "v_pk_mul_f32 v[46:47], v[46:47], v[122:123]\n" \
  "s_waitcnt lgkmcnt(4)\n" \
  "v_smfmac_f32_16x16x64_bf16 v[52:55], v[180:183], v[108:115], v192\n" \
  "v_lshlrev_b32_sdwa v64, 4, v255 dst_sel:DWORD dst_unused:UNUSED_PAD src0_sel:DWORD src1_sel:WORD_0\n" \
  "ds_read_b128 v[84:87], v64\n" \
  "v_lshlrev_b32_sdwa v65, 4, v255 dst_sel:DWORD dst_unused:UNUSED_PAD src0_sel:DWORD src1_sel:WORD_1\n" \
  "ds_read_b128 v[88:91], v65\n" \
  "v_pk_add_f32 v[140:141], v[140:141], 1.0 op_sel_hi:[1,0]\n" \
  "v_pk_add_f32 v[142:143], v[142:143], 1.0 op_sel_hi:[1,0]\n" \
  "v_rcp_f32 v140, v140\n" \
  "v_rcp_f32 v141, v141\n" \
  "v_rcp_f32 v142, v142\n" \
  "v_rcp_f32 v143, v143\n" \
  "v_pk_mul_f32 v[44:45], v[44:45], v[140:141]\n" \
  "v_pk_fma_f32 v[44:45], v[46:47], v[142:143], v[44:45]\n" \
  "v_add_f32 v44, v44, v45\n" \
  "v_pk_mul_f32 v[128:129], v[48:49], v[48:49]\n" \
  "v_pk_mul_f32 v[130:131], v[50:51], v[50:51]\n" \
  "v_pk_fma_f32 v[128:129], v[128:129], s[52:53], v[124:125]\n" \
  "v_pk_fma_f32 v[130:131], v[130:131], s[52:53], v[124:125]\n" \
  "v_pk_mul_f32 v[128:129], v[128:129], v[48:49]\n" \
  "v_pk_mul_f32 v[130:131], v[130:131], v[50:51]\n" \
  "v_exp_f32 v128, v128\n" \
  "s_waitcnt lgkmcnt(4)\n" \
  "v_smfmac_f32_16x16x64_bf16 v[56:59], v[180:183], v[68:75], v192\n" \
  "v_lshlrev_b32_sdwa v66, 4, v223 dst_sel:DWORD dst_unused:UNUSED_PAD src0_sel:DWORD src1_sel:WORD_0\n" \
  "ds_read_b128 v[92:95], v66\n" \
  "v_lshlrev_b32_sdwa v67, 4, v223 dst_sel:DWORD dst_unused:UNUSED_PAD src0_sel:DWORD src1_sel:WORD_1\n" \
  "ds_read_b128 v[96:99], v67\n" \
  "v_exp_f32 v129, v129\n" \
  "v_pk_mul_f32 v[48:49], v[48:49], v[120:121]\n" \
  "v_exp_f32 v130, v130\n" \
  "v_exp_f32 v131, v131\n" \
  "v_pk_mul_f32 v[50:51], v[50:51], v[122:123]\n" \
  "v_pk_add_f32 v[128:129], v[128:129], 1.0 op_sel_hi:[1,0]\n" \
  "v_pk_add_f32 v[130:131], v[130:131], 1.0 op_sel_hi:[1,0]\n" \
  "v_rcp_f32 v128, v128\n" \
  "v_rcp_f32 v129, v129\n" \
  "v_rcp_f32 v130, v130\n" \
  "v_rcp_f32 v131, v131\n" \
  "v_pk_mul_f32 v[48:49], v[48:49], v[128:129]\n" \
  "v_pk_fma_f32 v[48:49], v[50:51], v[130:131], v[48:49]\n" \
  "v_add_f32 v48, v48, v49\n" \
  "v_pk_mul_f32 v[132:133], v[52:53], v[52:53]\n" \
  "v_pk_mul_f32 v[134:135], v[54:55], v[54:55]\n" \
  "s_waitcnt lgkmcnt(4)\n" \
  "v_smfmac_f32_16x16x64_bf16 v[56:59], v[180:183], v[76:83], v192\n" \
  "v_pk_fma_f32 v[132:133], v[132:133], s[52:53], v[124:125]\n" \
  "v_pk_fma_f32 v[134:135], v[134:135], s[52:53], v[124:125]\n" \
  "v_pk_mul_f32 v[132:133], v[132:133], v[52:53]\n" \
  "v_pk_mul_f32 v[134:135], v[134:135], v[54:55]\n" \
  "v_exp_f32 v132, v132\n" \
  "v_exp_f32 v133, v133\n" \
  "v_pk_mul_f32 v[52:53], v[52:53], v[120:121]\n" \
  "v_exp_f32 v134, v134\n" \
  "v_exp_f32 v135, v135\n" \
  "v_pk_mul_f32 v[54:55], v[54:55], v[122:123]\n" \
  "v_pk_add_f32 v[132:133], v[132:133], 1.0 op_sel_hi:[1,0]\n" \
  "v_pk_add_f32 v[134:135], v[134:135], 1.0 op_sel_hi:[1,0]\n" \
  "v_rcp_f32 v132, v132\n" \
  "v_rcp_f32 v133, v133\n" \
  "v_rcp_f32 v134, v134\n" \
  "v_rcp_f32 v135, v135\n" \
  "v_pk_mul_f32 v[52:53], v[52:53], v[132:133]\n" \
  "v_pk_fma_f32 v[52:53], v[54:55], v[134:135], v[52:53]\n" \
  "v_add_f32 v52, v52, v53\n" \
  "s_waitcnt lgkmcnt(2)\n" \
  "v_smfmac_f32_16x16x64_bf16 v[60:63], v[180:183], v[84:91], v192\n" \
  "v_pk_mul_f32 v[136:137], v[56:57], v[56:57]\n" \
  "v_pk_mul_f32 v[138:139], v[58:59], v[58:59]\n" \
  "v_pk_fma_f32 v[136:137], v[136:137], s[52:53], v[124:125]\n" \
  "v_pk_fma_f32 v[138:139], v[138:139], s[52:53], v[124:125]\n" \
  "v_pk_mul_f32 v[136:137], v[136:137], v[56:57]\n" \
  "v_pk_mul_f32 v[138:139], v[138:139], v[58:59]\n" \
  "v_exp_f32 v136, v136\n" \
  "v_exp_f32 v137, v137\n" \
  "v_pk_mul_f32 v[56:57], v[56:57], v[120:121]\n" \
  "v_exp_f32 v138, v138\n" \
  "v_exp_f32 v139, v139\n" \
  "v_pk_mul_f32 v[58:59], v[58:59], v[122:123]\n" \
  "v_pk_add_f32 v[136:137], v[136:137], 1.0 op_sel_hi:[1,0]\n" \
  "v_pk_add_f32 v[138:139], v[138:139], 1.0 op_sel_hi:[1,0]\n" \
  "v_rcp_f32 v136, v136\n" \
  "v_rcp_f32 v137, v137\n" \
  "v_rcp_f32 v138, v138\n" \
  "v_rcp_f32 v139, v139\n" \
  "v_pk_mul_f32 v[56:57], v[56:57], v[136:137]\n" \
  "v_pk_fma_f32 v[56:57], v[58:59], v[138:139], v[56:57]\n" \
  "v_add_f32 v56, v56, v57\n" \
  "s_waitcnt lgkmcnt(0)\n" \
  "v_smfmac_f32_16x16x64_bf16 v[60:63], v[180:183], v[92:99], v192\n" \
  "s_nop 7\n" \
  "v_pk_mul_f32 v[140:141], v[60:61], v[60:61]\n" \
  "v_pk_mul_f32 v[142:143], v[62:63], v[62:63]\n" \
  "v_pk_fma_f32 v[140:141], v[140:141], s[52:53], v[124:125]\n" \
  "v_pk_fma_f32 v[142:143], v[142:143], s[52:53], v[124:125]\n" \
  "v_pk_mul_f32 v[140:141], v[140:141], v[60:61]\n" \
  "v_pk_mul_f32 v[142:143], v[142:143], v[62:63]\n" \
  "v_exp_f32 v140, v140\n" \
  "v_exp_f32 v141, v141\n" \
  "v_pk_mul_f32 v[60:61], v[60:61], v[120:121]\n" \
  "v_exp_f32 v142, v142\n" \
  "v_exp_f32 v143, v143\n" \
  "v_pk_mul_f32 v[62:63], v[62:63], v[122:123]\n" \
  "v_pk_add_f32 v[140:141], v[140:141], 1.0 op_sel_hi:[1,0]\n" \
  "v_pk_add_f32 v[142:143], v[142:143], 1.0 op_sel_hi:[1,0]\n" \
  "v_rcp_f32 v140, v140\n" \
  "v_rcp_f32 v141, v141\n" \
  "v_rcp_f32 v142, v142\n" \
  "v_rcp_f32 v143, v143\n" \
  "v_pk_mul_f32 v[60:61], v[60:61], v[140:141]\n" \
  "v_pk_fma_f32 v[60:61], v[62:63], v[142:143], v[60:61]\n" \
  "v_add_f32 v60, v60, v61\n" \
  "v_permlane16_swap_b32 v32, v48\n" \
  "v_permlane16_swap_b32 v36, v52\n" \
  "v_permlane16_swap_b32 v40, v56\n" \
  "v_permlane16_swap_b32 v44, v60\n" \
  "v_add_f32 v148, v32, v48\n" \
  "v_add_f32 v149, v36, v52\n" \
  "v_add_f32 v150, v40, v56\n" \
  "v_add_f32 v151, v44, v60\n" \
  "v_min_u32 v148, -2, v148\n" \
  "v_min_u32 v149, -2, v149\n" \
  "v_min_u32 v150, -2, v150\n" \
  "v_min_u32 v151, -2, v151\n" \
  "global_store_dwordx4 v188, v[148:151], s[40:41] offset:1024 sc1\n" \
  "v_lshrrev_b32 v0, 4, v188\n" \
  "v_lshrrev_b32 v1, 4, v0\n" \
  "v_and_b32 v2, 15, v0\n" \
  "v_lshlrev_b32 v2, 4, v2\n" \
  "v_lshl_add_u32 v1, v1, 17, v2\n" \
  "s_lshl_b32 s35, s27, 19\n" \
  "s_lshl_b32 s36, s26, 8\n" \
  "s_add_u32 s35, s35, s36\n" \
  "s_add_u32 s35, s35, 105676800\n" \
  "s_add_u32 s40, s24, s35\n" \
  "s_addc_u32 s41, s25, 0\n" \
  "s_add_u32 s42, s40, 65536\n" \
  "s_addc_u32 s43, s41, 0\n" \
  "s_add_u32 s44, s24, 109871104\n" \
  "s_addc_u32 s45, s25, 0\n" \
  "s_add_u32 s46, s44, 65536\n" \
  "s_addc_u32 s47, s45, 0\n" \
  "s_lshr_b32 s35, s26, 2\n" \
  "s_lshl_b32 s35, s35, 8\n" \
  "s_and_b32 s36, s26, 1\n" \
  "s_lshl_b32 s36, s36, 7\n" \
  "s_add_u32 s35, s35, s36\n" \
  "s_and_b32 s36, s26, 2\n" \
  "s_lshl3_add_u32 s35, s36, s35\n" \
  "v_and_b32 v2, 3, v0\n" \
  "v_lshrrev_b32 v3, 2, v0\n" \
  "v_lshl_add_u32 v2, v2, 5, v3\n" \
  "v_add_u32 v2, s35, v2\n" \
  "v_lshlrev_b32 v3, 2, v2\n" \
  "s_cmp_eq_u32 s27, 0\n" \
  "s_cselect_b32 s35, -1, 0\n" \
  "v_and_b32 v3, s35, v3\n" \
  "v_mov_b32 v4, s6\n" \
  "s_mov_b32 s5, 0x10000\n" \
  "s_barrier\n" \
  "s_sleep 10\n" \
  "LF_poll:\n" \
  "global_load_dwordx4 v[8:11], v1, s[40:41] sc1\n" \
  "global_load_dwordx4 v[12:15], v1, s[42:43] sc1\n" \
  "global_load_dword v16, v3, s[44:45] sc1\n" \
  "global_load_dword v17, v3, s[46:47] sc1\n" \
  "s_waitcnt vmcnt(0)\n" \
  "v_max3_u32 v5, v8, v9, v10\n" \
  "v_max3_u32 v6, v11, v12, v13\n" \
  "v_max3_u32 v5, v5, v14, v15\n" \
  "v_max3_u32 v6, v6, v16, v17\n" \
  "v_max_u32 v5, v5, v6\n" \
  "v_cmp_eq_u32 vcc, -1, v5\n" \
  "s_sub_u32 s5, s5, 1\n" \
  "s_cbranch_scc1 LF_go\n" \
  "s_nop 2\n" \
  "s_cbranch_vccz LF_go\n" \
  "s_mov_b64 exec, vcc\n" \
  "s_sleep 2\n" \
  "s_branch LF_poll\n" \
  "LF_go:\n" \
  "s_mov_b64 exec, -1\n" \
  "v_pk_add_f32 v[8:9], v[8:9], v[12:13]\n" \
  "v_pk_add_f32 v[10:11], v[10:11], v[14:15]\n" \
  "s_nop 0\n" \
  "v_permlane16_swap_b32 v8, v9\n" \
  "v_permlane16_swap_b32 v10, v11\n" \
  "v_add_f32 v8, v8, v9\n" \
  "v_add_f32 v10, v10, v11\n" \
  "s_nop 1\n" \
  "v_permlane32_swap_b32 v8, v10\n" \
  "v_add_f32 v8, v8, v10\n" \
  "v_and_b32 v5, 15, v0\n" \
  "v_lshrrev_b32 v6, 4, v0\n" \
  "v_lshl_add_u32 v5, v5, 2, v6\n" \
  "s_lshl_b32 s35, s27, 8\n" \
  "v_lshl_add_u32 v5, v5, 2, s35\n" \
  "ds_write_b32 v5, v8\n" \
  "s_waitcnt lgkmcnt(0)\n" \
  "s_barrier\n" \
  "s_cmp_lg_u32 s27, 0\n" \
  "s_cbranch_scc1 LF_end\n" \
  "v_lshlrev_b32 v5, 2, v0\n" \
  "ds_read2st64_b32 v[8:9], v5 offset0:0 offset1:1\n" \
  "ds_read2st64_b32 v[10:11], v5 offset0:2 offset1:3\n" \
  "ds_read2st64_b32 v[12:13], v5 offset0:4 offset1:5\n" \
  "ds_read2st64_b32 v[14:15], v5 offset0:6 offset1:7\n" \
  "v_add_f32 v16, v16, v17\n" \
  "v_lshlrev_b32 v4, 16, v4\n" \
  "v_cvt_pk_bf16_f32 v16, v16, v16\n" \
  "v_lshlrev_b32 v2, 1, v2\n" \
  "s_waitcnt lgkmcnt(0)\n" \
  "v_pk_add_f32 v[8:9], v[8:9], v[10:11]\n" \
  "v_pk_add_f32 v[12:13], v[12:13], v[14:15]\n" \
  "v_pk_add_f32 v[8:9], v[8:9], v[12:13]\n" \
  "v_add_f32 v8, v8, v9\n" \
  "v_cvt_pk_bf16_f32 v8, v8, v8\n" \
  "v_lshlrev_b32 v8, 16, v8\n" \
  "v_add_f32 v8, v8, v4\n" \
  "v_cvt_pk_bf16_f32 v8, v8, v8\n" \
  "v_lshlrev_b32 v8, 16, v8\n" \
  "v_lshlrev_b32 v16, 16, v16\n" \
  "v_add_f32 v8, v8, v16\n" \
  "v_cvt_pk_bf16_f32 v8, v8, v8\n" \
  "global_store_short v2, v8, s[22:23]\n" \
  "LF_end:\n" \
  "s_branch LZ_end\n" \
  "LG_h:\n" \
  "s_cmp_eq_u32 s51, 4\n" \
  "s_cbranch_scc1 LG_chk\n" \
  "s_cmp_lg_u32 s51, 7\n" \
  "s_cbranch_scc1 LG_ret\n" \
  "s_barrier\n" \
  "s_mov_b32 exec_hi, 0\n" \
  "global_atomic_add v196, v199, s[48:49] offset:3076 sc1\n" \
  "s_mov_b64 exec, -1\n" \
  "s_branch LG_aft\n" \
  "LG_pol:\n" \
  "s_mov_b64 exec, 0xff\n" \
  "global_load_dword v200, v198, s[48:49] offset:3076 sc1\n" \
  "s_mov_b64 exec, -1\n" \
  "s_branch LG_rt2\n" \
  "LG_chk:\n" \
  "s_mov_b32 s5, 0x10000\n" \
  "LG_p:\n" \
  "s_mov_b64 exec, 0xff\n" \
  "v_cmp_le_u32 vcc, 32, v200\n" \
  "s_mov_b64 exec, -1\n" \
  "s_sub_u32 s5, s5, 1\n" \
  "s_cselect_b32 s35, 0xff, vcc_lo\n" \
  "s_cmp_eq_u32 s35, 0xff\n" \
  "s_cbranch_scc1 LG_p_x\n" \
  "s_sleep 2\n" \
  "s_mov_b64 exec, 0xff\n" \
  "global_load_dword v200, v198, s[48:49] offset:3076 sc1\n" \
  "s_mov_b64 exec, -1\n" \
  "s_waitcnt vmcnt(0)\n" \
  "s_branch LG_p\n" \
  "LG_p_x:\n" \
  "s_branch LG_ret\n" \
  "LZ_end:\n" \
  "s_waitcnt vmcnt(0) lgkmcnt(0)\n"

#define R10(p, b) p #b "0", p #b "1", p #b "2", p #b "3", p #b "4", p #b "5", p #b "6", p #b "7", p #b "8", p #b "9"
__global__ __launch_bounds__(512, 2) void k1(const int* __restrict__ idx, const unsigned short* __restrict__ table,
                                             const unsigned short* __restrict__ w_p, const unsigned short* __restrict__ w1,
                                             const unsigned short* __restrict__ b1, const unsigned short* __restrict__ w2,
                                             const unsigned short* __restrict__ b2, __hip_bfloat16* __restrict__ out, char* __restrict__ ws) {
  __shared__ __attribute__((aligned(1024))) char smem[163840];
  unsigned tid = threadIdx.x;
  asm volatile(K_ASM
               : "+{v0}"(tid)
               : "{s[0:1]}"(__builtin_amdgcn_kernarg_segment_ptr()), "{s26}"(blockIdx.x), "s"((unsigned)(uintptr_t)smem)
               : "memory", "scc", "vcc", "s3", "s4", "s5", "s6", "s7", "s8", "s9", R10("s", 1), "s20", "s21", "s22", "s23", "s24", "s25", "s27", "s28", "s29", "s30", "s31", "s33", "s34", "s35", "s36", "s37", "s38", "s39", R10("s", 4), R10("s", 5), R10("s", 6), R10("s", 7), R10("s", 8), R10("s", 9),
                 "v1", "v2", "v3", "v4", "v5", "v6", "v7", "v8", "v9", R10("v", 1), R10("v", 2), R10("v", 3), R10("v", 4), R10("v", 5), R10("v", 6), R10("v", 7), R10("v", 8), R10("v", 9),
                 R10("v", 10), R10("v", 11), R10("v", 12), R10("v", 13), R10("v", 14), R10("v", 15), R10("v", 16), R10("v", 17), R10("v", 18), R10("v", 19),
                 R10("v", 20), R10("v", 21), R10("v", 22), R10("v", 23), R10("v", 24), "v250", "v251", "v252", "v253", "v254", "v255");
}

extern "C" void kernel_launch(void* const* d_in, const int* in_sizes, int n_in, void* d_out, int out_size, void* d_ws,
                              size_t ws_size, hipStream_t stream) {
  k1<<<dim3(256), dim3(512), 0, stream>>>((const int*)d_in[0], (const unsigned short*)d_in[1], (const unsigned short*)d_in[2], (const unsigned short*)d_in[3],
                                          (const unsigned short*)d_in[4], (const unsigned short*)d_in[5], (const unsigned short*)d_in[6], (__hip_bfloat16*)d_out, (char*)d_ws);
}
```

```cpp
#include <hip/hip_runtime.h>
#include <hip/hip_bf16.h>
#include <stdint.h>

#define K_ASM \
  "LZ_first:\n" \
  "s_load_dwordx16 s[8:23], s[0:1], 0x0\n" \
  "s_load_dwordx2 s[24:25], s[0:1], 0x40\n" \
  "v_readfirstlane_b32 s27, v0\n" \
  "v_and_b32 v255, 63, v0\n" \
  "s_and_b32 s29, s26, 7\n" \
  "s_lshr_b32 s30, s26, 3\n" \
  "s_lshr_b32 s27, s27, 6\n" \
  "s_and_b32 s31, s30, 1\n" \
  "s_and_b32 s28, s27, 3\n" \
  "s_lshr_b32 s35, s30, 1\n" \
  "s_lshl3_add_u32 s7, s35, s29\n" \
  "v_lshrrev_b32 v1, 3, v255\n" \
  "v_and_b32 v2, 7, v255\n" \
  "v_lshlrev_b32 v1, 8, v1\n" \
  "v_lshl_or_b32 v254, v2, 4, v1\n" \
  "s_mul_i32 s36, s7, 37\n" \
  "s_and_b32 s36, s36, 63\n" \
  "s_add_u32 s36, s36, s28\n" \
  "s_cmp_ge_u32 s27, 4\n" \
  "s_cbranch_scc1 LB_start\n" \
  "s_setprio 3\n" \
  "v_and_b32 v252, 15, v255\n" \
  "v_lshrrev_b32 v253, 4, v255\n" \
  "v_mul_u32_u24 v2, 400, v253\n" \
  "v_min_u32 v3, 8, v252\n" \
  "v_lshl_add_u32 v250, v252, 4, v2\n" \
  "v_lshl_add_u32 v251, v3, 4, v2\n" \
  "s_lshl2_add_u32 s36, s7, s28\n" \
  "s_mul_i32 s36, s36, 12800\n" \
  "s_lshl_b32 s35, s28, 10\n" \
  "v_lshl_add_u32 v1, v255, 4, s35\n" \
  "s_add_u32 m0, s35, 138240\n" \
  "s_waitcnt lgkmcnt(0)\n" \
  "s_add_u32 s44, s8, s36\n" \
  "s_addc_u32 s45, s9, 0\n" \
  "global_load_lds_dwordx4 v1, s[12:13]\n" \
  "s_add_u32 m0, m0, 0x1000\n" \
  "v_add_u32 v1, 0x1000, v1\n" \
  "global_load_lds_dwordx4 v1, s[12:13]\n" \
  "s_add_u32 m0, m0, 0x1000\n" \
  "v_add_u32 v1, 0x1000, v1\n" \
  "global_load_lds_dwordx4 v1, s[12:13]\n" \
  "s_add_u32 m0, m0, 0x1000\n" \
  "v_add_u32 v1, 0x1000, v1\n" \
  "global_load_lds_dwordx4 v1, s[12:13]\n" \
  "s_add_u32 m0, m0, 0x1000\n" \
  "v_add_u32 v1, 0x1000, v1\n" \
  "v_min_u32 v1, 19984, v1\n" \
  "global_load_lds_dwordx4 v1, s[12:13]\n" \
  "global_load_dwordx4 v[232:235], v250, s[44:45] offset:0\n" \
  "global_load_dwordx4 v[236:239], v251, s[44:45] offset:256\n" \
  "global_load_dwordx4 v[240:243], v250, s[44:45] offset:1600\n" \
  "global_load_dwordx4 v[244:247], v251, s[44:45] offset:1856\n" \
  "s_setprio 1\n" \
  "v_lshrrev_b32 v1, 2, v252\n" \
  "v_and_b32 v2, 3, v252\n" \
  "v_cmp_eq_u32 vcc, v1, v253\n" \
  "v_mov_b32 v4, 0x3f80\n" \
  "v_and_b32 v13, 1, v253\n" \
  "v_lshlrev_b32 v14, 1, v13\n" \
  "v_xor_b32 v2, v2, v14\n" \
  "v_cndmask_b32 v2, 4, v2, vcc\n" \
  "v_cmp_eq_u32 vcc, 0, v2\n" \
  "s_nop 1\n" \
  "v_cndmask_b32 v216, 0, v4, vcc\n" \
  "v_lshlrev_b32 v220, 16, v216\n" \
  "v_cmp_eq_u32 vcc, 1, v2\n" \
  "s_nop 1\n" \
  "v_cndmask_b32 v217, 0, v4, vcc\n" \
  "v_lshlrev_b32 v221, 16, v217\n" \
  "v_cmp_eq_u32 vcc, 2, v2\n" \
  "s_nop 1\n" \
  "v_cndmask_b32 v218, 0, v4, vcc\n" \
  "v_lshlrev_b32 v222, 16, v218\n" \
  "v_cmp_eq_u32 vcc, 3, v2\n" \
  "s_nop 1\n" \
  "v_cndmask_b32 v219, 0, v4, vcc\n" \
  "v_lshlrev_b32 v223, 16, v219\n" \
  "v_and_b32 v1, 2, v252\n" \
  "v_lshlrev_b32 v1, 3, v1\n" \
  "v_lshlrev_b32 v1, v1, v4\n" \
  "v_and_b32 v2, 0x31, v255\n" \
  "v_mov_b64 v[224:225], 0\n" \
  "v_cmp_eq_u32 vcc, 0, v2\n" \
  "s_nop 1\n" \
  "v_cndmask_b32 v226, 0, v1, vcc\n" \
  "v_cmp_eq_u32 vcc, 1, v2\n" \
  "s_nop 1\n" \
  "v_cndmask_b32 v227, 0, v1, vcc\n" \
  "v_mul_u32_u24 v5, 400, v252\n" \
  "v_add_u32 v5, 138240, v5\n" \
  "v_lshl_add_u32 v6, v253, 4, v5\n" \
  "v_add_u32 v7, 6400, v5\n" \
  "v_add_u32 v8, 6400, v6\n" \
  "v_add_u32 v9, 12800, v5\n" \
  "v_add_u32 v10, 12800, v6\n" \
  "v_bfe_u32 v1, v252, 1, 1\n" \
  "v_and_b32 v2, 1, v252\n" \
  "v_lshl_add_u32 v1, v2, 1, v1\n" \
  "v_mul_u32_u24 v1, 200, v1\n" \
  "v_add_u32 v11, 157440, v1\n" \
  "v_lshl_add_u32 v12, v253, 4, v11\n" \
  "v_lshlrev_b32 v13, 3, v13\n" \
  "v_sub_u32 v16, v5, v13\n" \
  "v_add_u32 v5, v5, v13\n" \
  "v_sub_u32 v17, v6, v13\n" \
  "v_add_u32 v6, v6, v13\n" \
  "v_sub_u32 v18, v7, v13\n" \
  "v_add_u32 v7, v7, v13\n" \
  "v_sub_u32 v19, v8, v13\n" \
  "v_add_u32 v8, v8, v13\n" \
  "v_sub_u32 v20, v9, v13\n" \
  "v_add_u32 v9, v9, v13\n" \
  "v_sub_u32 v21, v10, v13\n" \
  "v_add_u32 v10, v10, v13\n" \
  "v_sub_u32 v22, v11, v13\n" \
  "v_add_u32 v11, v11, v13\n" \
  "v_sub_u32 v23, v12, v13\n" \
  "v_add_u32 v12, v12, v13\n" \
  "v_lshlrev_b32 v188, 4, v253\n" \
  "v_and_b32 v96, 4, v252\n" \
  "v_lshl_add_u32 v96, v253, 5, v96\n" \
  "v_mov_b32 v191, 0x40000000\n" \
  "v_cmp_gt_u32 vcc, 8, v252\n" \
  "v_mov_b32 v193, 0x4444\n" \
  "v_mov_b32 v98, 0xeeee\n" \
  "v_cndmask_b32 v189, v191, v96, vcc\n" \
  "v_cndmask_b32 v190, v96, v191, vcc\n" \
  "v_and_b32 v99, 4, v252\n" \
  "v_cmp_ne_u32 vcc, 0, v99\n" \
  "s_lshl_b32 s35, s28, 11\n" \
  "s_add_u32 s35, s35, 130048\n" \
  "v_cndmask_b32 v193, v193, v98, vcc\n" \
  "v_lshl_add_u32 v194, v252, 4, s35\n" \
  "v_and_b32 v96, 7, v252\n" \
  "v_lshlrev_b32 v96, 5, v96\n" \
  "v_lshl_add_u32 v96, v253, 8, v96\n" \
  "v_lshrrev_b32 v97, 3, v252\n" \
  "v_lshl_add_u32 v96, v97, 2, v96\n" \
  "v_add_u32 v195, s35, v96\n" \
  "v_and_b32 v97, v97, v252\n" \
  "v_lshlrev_b32 v192, 4, v97\n" \
  "s_waitcnt vmcnt(4)\n" \
  "s_barrier\n" \
  "ds_read_b64 v[200:201], v12 offset:128\n" \
  "ds_read_b64 v[202:203], v23 offset:136\n" \
  "ds_read_b64 v[184:185], v9 offset:184\n" \
  "ds_read_b64 v[186:187], v20 offset:192\n" \
  "ds_read_b64 v[212:213], v9 offset:384\n" \
  "ds_read_b64 v[214:215], v20 offset:392\n" \
  "ds_read_b64 v[196:197], v12 offset:64\n" \
  "ds_read_b64 v[198:199], v23 offset:72\n" \
  "ds_read_b64 v[176:177], v7 offset:184\n" \
  "ds_read_b64 v[178:179], v18 offset:192\n" \
  "ds_read_b64 v[180:181], v7 offset:384\n" \
  "ds_read_b64 v[182:183], v18 offset:392\n" \
  "ds_read_b64 v[228:229], v12 offset:0\n" \
  "ds_read_b64 v[230:231], v23 offset:8\n" \
  "s_waitcnt lgkmcnt(12)\n" \
  "v_mfma_f32_16x16x32_bf16 v[44:47], v[200:203], v[216:219], 0\n" \
  "ds_read_b64 v[168:169], v5 offset:184\n" \
  "ds_read_b64 v[170:171], v16 offset:192\n" \
  "s_waitcnt lgkmcnt(12)\n" \
  "v_mfma_f32_16x16x32_bf16 v[44:47], v[224:227], v[184:187], v[44:47]\n" \
  "ds_read_b64 v[172:173], v5 offset:384\n" \
  "ds_read_b64 v[174:175], v16 offset:392\n" \
  "v_mfma_f32_16x16x32_bf16 v[208:211], v[200:203], v[220:223], 0\n" \
  "ds_read_b64 v[96:97], v6 offset:0\n" \
  "ds_read_b64 v[98:99], v17 offset:8\n" \
  "s_waitcnt lgkmcnt(14)\n" \
  "v_mfma_f32_16x16x32_bf16 v[208:211], v[224:227], v[212:215], v[208:211]\n" \
  "ds_read_b64 v[124:125], v8 offset:64\n" \
  "ds_read_b64 v[126:127], v19 offset:72\n" \
  "s_waitcnt lgkmcnt(14)\n" \
  "v_mfma_f32_16x16x32_bf16 v[60:63], v[196:199], v[216:219], 0\n" \
  "ds_read_b64 v[136:137], v8 offset:264\n" \
  "ds_read_b64 v[138:139], v19 offset:272\n" \
  "s_waitcnt lgkmcnt(14)\n" \
  "v_mfma_f32_16x16x32_bf16 v[60:63], v[224:227], v[176:179], v[60:63]\n" \
  "ds_read_b64 v[140:141], v8 offset:328\n" \
  "ds_read_b64 v[142:143], v19 offset:336\n" \
  "v_mfma_f32_16x16x32_bf16 v[184:187], v[196:199], v[220:223], 0\n" \
  "ds_read_b64 v[160:161], v10 offset:264\n" \
  "ds_read_b64 v[162:163], v21 offset:272\n" \
  "s_waitcnt lgkmcnt(15)\n" \
  "v_mfma_f32_16x16x32_bf16 v[184:187], v[224:227], v[180:183], v[184:187]\n" \
  "ds_read_b64 v[116:117], v6 offset:328\n" \
  "ds_read_b64 v[118:119], v17 offset:336\n" \
  "s_waitcnt lgkmcnt(15)\n" \
  "v_mfma_f32_16x16x32_bf16 v[68:71], v[228:231], v[216:219], 0\n" \
  "ds_read_b64 v[144:145], v10 offset:0\n" \
  "ds_read_b64 v[146:147], v21 offset:8\n" \
  "s_waitcnt lgkmcnt(15)\n" \
  "v_mfma_f32_16x16x32_bf16 v[68:71], v[224:227], v[168:171], v[68:71]\n" \
  "ds_read_b64 v[148:149], v10 offset:64\n" \
  "ds_read_b64 v[150:151], v21 offset:72\n" \
  "v_mfma_f32_16x16x32_bf16 v[212:215], v[228:231], v[220:223], 0\n" \
  "ds_read_b64 v[104:105], v6 offset:128\n" \
  "ds_read_b64 v[106:107], v17 offset:136\n" \
  "s_waitcnt lgkmcnt(15)\n" \
  "v_mfma_f32_16x16x32_bf16 v[212:215], v[224:227], v[172:175], v[212:215]\n" \
  "ds_read_b64 v[100:101], v6 offset:64\n" \
  "ds_read_b64 v[102:103], v17 offset:72\n" \
  "v_mfma_f32_16x16x32_bf16 v[92:95], v[96:99], v[216:219], 0\n" \
  "ds_read_b64 v[120:121], v8 offset:0\n" \
  "ds_read_b64 v[122:123], v19 offset:8\n" \
  "s_waitcnt lgkmcnt(15)\n" \
  "v_mfma_f32_16x16x32_bf16 v[24:27], v[124:127], v[220:223], 0\n" \
  "ds_read_b64 v[112:113], v6 offset:264\n" \
  "ds_read_b64 v[114:115], v17 offset:272\n" \
  "v_mfma_f32_16x16x32_bf16 v[24:27], v[216:219], v[136:139], v[24:27]\n" \
  "ds_read_b64 v[156:157], v10 offset:200\n" \
  "ds_read_b64 v[158:159], v21 offset:208\n" \
  "s_waitcnt lgkmcnt(15)\n" \
  "v_mfma_f32_16x16x32_bf16 v[28:31], v[140:143], v[220:223], 0\n" \
  "ds_read_b64 v[132:133], v8 offset:200\n" \
  "ds_read_b64 v[134:135], v19 offset:208\n" \
  "v_mfma_f32_16x16x32_bf16 v[28:31], v[220:223], v[160:163], v[28:31]\n" \
  "ds_read_b64 v[108:109], v6 offset:200\n" \
  "ds_read_b64 v[110:111], v17 offset:208\n" \
  "s_waitcnt lgkmcnt(15)\n" \
  "v_mfma_f32_16x16x32_bf16 v[32:35], v[116:119], v[216:219], 0\n" \
  "ds_read_b64 v[152:153], v10 offset:128\n" \
  "ds_read_b64 v[154:155], v21 offset:136\n" \
  "v_mfma_f32_16x16x32_bf16 v[32:35], v[220:223], v[144:147], v[32:35]\n" \
  "ds_read_b64 v[164:165], v10 offset:328\n" \
  "ds_read_b64 v[166:167], v21 offset:336\n" \
  "v_mfma_f32_16x16x32_bf16 v[36:39], v[140:143], v[216:219], 0\n" \
  "ds_read_b64 v[128:129], v8 offset:128\n" \
  "ds_read_b64 v[130:131], v19 offset:136\n" \
  "s_waitcnt lgkmcnt(15)\n" \
  "v_mfma_f32_16x16x32_bf16 v[36:39], v[220:223], v[148:151], v[36:39]\n" \
  "ds_read_b64 v[204:205], v11 offset:184\n" \
  "ds_read_b64 v[206:207], v22 offset:192\n" \
  "v_mfma_f32_16x16x32_bf16 v[40:43], v[104:107], v[216:219], 0\n" \
  "v_mfma_f32_16x16x32_bf16 v[40:43], v[216:219], v[144:147], v[40:43]\n" \
  "v_mfma_f32_16x16x32_bf16 v[48:51], v[100:103], v[216:219], 0\n" \
  "s_waitcnt lgkmcnt(15)\n" \
  "v_mfma_f32_16x16x32_bf16 v[48:51], v[216:219], v[120:123], v[48:51]\n" \
  "s_waitcnt lgkmcnt(14)\n" \
  "v_mfma_f32_16x16x32_bf16 v[52:55], v[112:115], v[216:219], 0\n" \
  "v_mfma_f32_16x16x32_bf16 v[52:55], v[220:223], v[120:123], v[52:55]\n" \
  "v_mfma_f32_16x16x32_bf16 v[56:59], v[116:119], v[220:223], 0\n" \
  "s_waitcnt lgkmcnt(12)\n" \
  "v_mfma_f32_16x16x32_bf16 v[56:59], v[220:223], v[156:159], v[56:59]\n" \
  "s_waitcnt vmcnt(0) lgkmcnt(0)\n" \
  "v_mfma_f32_16x16x32_bf16 v[64:67], v[104:107], v[220:223], 0\n" \
  "v_cvt_pk_u16_u32 v232, v232, v233\n" \
  "v_cvt_pk_u16_u32 v234, v234, v235\n" \
  "v_mfma_f32_16x16x32_bf16 v[64:67], v[216:219], v[156:159], v[64:67]\n" \
  "v_cvt_pk_u16_u32 v236, v236, v237\n" \
  "v_cvt_pk_u16_u32 v238, v238, v239\n" \
  "v_mfma_f32_16x16x32_bf16 v[72:75], v[136:139], v[220:223], 0\n" \
  "v_pk_lshrrev_b16 v233, 3, v232 op_sel_hi:[0,1]\n" \
  "v_pk_lshrrev_b16 v235, 3, v234 op_sel_hi:[0,1]\n" \
  "v_mfma_f32_16x16x32_bf16 v[76:79], v[112:115], v[220:223], 0\n" \
  "v_pk_lshrrev_b16 v237, 3, v236 op_sel_hi:[0,1]\n" \
  "v_pk_lshrrev_b16 v239, 3, v238 op_sel_hi:[0,1]\n" \
  "v_mfma_f32_16x16x32_bf16 v[76:79], v[220:223], v[132:135], v[76:79]\n" \
  "v_pk_mad_u16 v180, v232, 8, v233 op_sel_hi:[1,0,1]\n" \
  "v_pk_mad_u16 v181, v234, 8, v235 op_sel_hi:[1,0,1]\n" \
  "v_mfma_f32_16x16x32_bf16 v[80:83], v[100:103], v[220:223], 0\n" \
  "v_pk_mad_u16 v182, v236, 8, v237 op_sel_hi:[1,0,1]\n" \
  "v_pk_mad_u16 v183, v238, 8, v239 op_sel_hi:[1,0,1]\n" \
  "v_mfma_f32_16x16x32_bf16 v[80:83], v[216:219], v[132:135], v[80:83]\n" \
  "v_alignbit_b32 v182, v182, v182, v192\n" \
  "v_alignbit_b32 v183, v183, v183, v192\n" \
  "v_mfma_f32_16x16x32_bf16 v[84:87], v[96:99], v[220:223], 0\n" \
  "v_cvt_pk_u16_u32 v240, v240, v241\n" \
  "v_cvt_pk_u16_u32 v242, v242, v243\n" \
  "v_mfma_f32_16x16x32_bf16 v[84:87], v[216:219], v[108:111], v[84:87]\n" \
  "v_cvt_pk_u16_u32 v244, v244, v245\n" \
  "v_cvt_pk_u16_u32 v246, v246, v247\n" \
  "v_mfma_f32_16x16x32_bf16 v[88:91], v[108:111], v[220:223], 0\n" \
  "v_pk_lshrrev_b16 v241, 3, v240 op_sel_hi:[0,1]\n" \
  "v_pk_lshrrev_b16 v243, 3, v242 op_sel_hi:[0,1]\n" \
  "v_mfma_f32_16x16x32_bf16 v[0:3], v[124:127], v[216:219], 0\n" \
  "v_pk_lshrrev_b16 v245, 3, v244 op_sel_hi:[0,1]\n" \
  "v_pk_lshrrev_b16 v247, 3, v246 op_sel_hi:[0,1]\n" \
  "v_mfma_f32_16x16x32_bf16 v[4:7], v[152:155], v[216:219], 0\n" \
  "v_pk_mad_u16 v196, v240, 8, v241 op_sel_hi:[1,0,1]\n" \
  "v_pk_mad_u16 v197, v242, 8, v243 op_sel_hi:[1,0,1]\n" \
  "v_mfma_f32_16x16x32_bf16 v[8:11], v[152:155], v[220:223], 0\n" \
  "v_pk_mad_u16 v198, v244, 8, v245 op_sel_hi:[1,0,1]\n" \
  "v_pk_mad_u16 v199, v246, 8, v247 op_sel_hi:[1,0,1]\n" \
  "v_mfma_f32_16x16x32_bf16 v[8:11], v[216:219], v[164:167], v[8:11]\n" \
  "v_alignbit_b32 v198, v198, v198, v192\n" \
  "v_alignbit_b32 v199, v199, v199, v192\n" \
  "v_mfma_f32_16x16x32_bf16 v[12:15], v[164:167], v[220:223], 0\n" \
  "v_add_u32 v200, 0x400, v195\n" \
  "v_mfma_f32_16x16x32_bf16 v[16:19], v[128:131], v[220:223], 0\n" \
  "v_mfma_f32_16x16x32_bf16 v[16:19], v[216:219], v[160:163], v[16:19]\n" \
  "v_mfma_f32_16x16x32_bf16 v[20:23], v[128:131], v[216:219], 0\n" \
  "v_mfma_f32_16x16x32_bf16 v[20:23], v[216:219], v[148:151], v[20:23]\n" \
  "v_mfma_f32_16x16x32_bf16 v[168:171], v[216:219], v[96:99], 0\n" \
  "v_mfma_f32_16x16x32_bf16 v[172:175], v[204:207], v[224:227], 0\n" \
  "v_mfma_f32_16x16x32_bf16 v[176:179], v[224:227], v[204:207], 0\n" \
  "s_mov_b32 exec_lo, 0x0\n" \
  "s_mov_b32 exec_hi, 0xffffffff\n" \
  "v_mov_b64 v[44:45], v[208:209]\n" \
  "v_mov_b64 v[46:47], v[210:211]\n" \
  "v_mov_b64 v[60:61], v[184:185]\n" \
  "v_mov_b64 v[62:63], v[186:187]\n" \
  "v_mov_b64 v[68:69], v[212:213]\n" \
  "v_mov_b64 v[70:71], v[214:215]\n" \
  "s_mov_b32 exec_lo, 0xff00ff00\n" \
  "s_mov_b32 exec_hi, 0x0\n" \
  "v_add_f32 v92, v92, v168\n" \
  "v_add_f32 v93, v93, v169\n" \
  "v_add_f32 v94, v94, v170\n" \
  "v_add_f32 v95, v95, v171\n" \
  "s_mov_b32 exec_lo, 0xf0000f00\n" \
  "s_mov_b32 exec_hi, 0x0\n" \
  "v_sub_f32 v92, v92, v176\n" \
  "v_sub_f32 v93, v93, v177\n" \
  "v_sub_f32 v94, v94, v178\n" \
  "v_sub_f32 v95, v95, v179\n" \
  "s_mov_b32 exec_lo, 0x0\n" \
  "s_mov_b32 exec_hi, 0xff00ff\n" \
  "v_mov_b64 v[92:93], 0\n" \
  "v_mov_b64 v[94:95], 0\n" \
  "s_mov_b32 exec_lo, 0x0\n" \
  "s_mov_b32 exec_hi, 0xf0000f\n" \
  "v_mov_b64 v[92:93], v[172:173]\n" \
  "v_mov_b64 v[94:95], v[174:175]\n" \
  "s_mov_b64 exec, -1\n" \
  "v_add_u32 v96, 6400, v250\n" \
  "v_add_u32 v97, 6400, v251\n" \
  "v_mov_b32 v98, 163328\n" \
  "v_mov_b32 v99, 1\n" \
  "v_lshlrev_b32 v179, 4, v253\n" \
  "s_lshr_b32 s35, s30, 4\n" \
  "s_lshl1_add_u32 s35, s29, s35\n" \
  "s_lshl_b32 s35, s35, 6\n" \
  "s_bfe_u32 s37, s30, 0x20002\n" \
  "s_lshl3_add_u32 s35, s37, s35\n" \
  "v_and_b32 v179, 16, v179\n" \
  "v_lshl_add_u32 v179, v179, 1, s35\n" \
  "s_mov_b64 exec, 1\n" \
  "ds_add_u32 v98, v99 offset:16\n" \
  "s_mov_b64 exec, -1\n" \
  "ds_write2_b32 v195, v180, v181 offset0:0 offset1:4\n" \
  "ds_write2_b32 v195, v182, v183 offset0:2 offset1:6\n" \
  "ds_write2_b32 v200, v196, v197 offset0:0 offset1:4\n" \
  "ds_write2_b32 v200, v198, v199 offset0:2 offset1:6\n" \
  "global_load_dwordx2 v[204:205], v179, s[16:17]\n" \
  "global_load_dwordx2 v[206:207], v179, s[18:19]\n" \
  "v_mov_b32 v179, 0\n" \
  "global_load_ushort v179, v179, s[20:21]\n" \
  "s_add_u32 s46, s44, 4800\n" \
  "s_addc_u32 s47, s45, 0\n" \
  "global_load_dwordx4 v[208:211], v96, s[44:45] offset:-3200\n" \
  "global_load_dwordx4 v[212:215], v97, s[44:45] offset:-2944\n" \
  "global_load_dwordx4 v[216:219], v96, s[44:45] offset:-1600\n" \
  "global_load_dwordx4 v[220:223], v97, s[44:45] offset:-1344\n" \
  "global_load_dwordx4 v[224:227], v96, s[44:45] offset:0\n" \
  "global_load_dwordx4 v[228:231], v97, s[44:45] offset:256\n" \
  "global_load_dwordx4 v[232:235], v96, s[44:45] offset:1600\n" \
  "global_load_dwordx4 v[236:239], v97, s[44:45] offset:1856\n" \
  "global_load_dwordx4 v[240:243], v96, s[44:45] offset:3200\n" \
  "global_load_dwordx4 v[244:247], v97, s[44:45] offset:3456\n" \
  "global_load_dwordx4 v[248:251], v96, s[46:47] offset:0\n" \
  "global_load_dwordx4 v[252:255], v97, s[46:47] offset:256\n" \
  "ds_read_b128 v[180:183], v194\n" \
  "v_add_u32 v194, 0x100, v194\n" \
  "L1_:\n" \
  "ds_read_b32 v99, v98 offset:12\n" \
  "s_waitcnt lgkmcnt(0)\n" \
  "v_readfirstlane_b32 s35, v99\n" \
  "s_cmp_lt_u32 s35, 4\n" \
  "s_cbranch_scc1 L1_\n" \
  "s_setprio 1\n" \
  "v_mad_u32_u16 v184, v182, 16, v188\n" \
  "ds_read_b128 v[128:131], v184 offset:0\n" \
  "ds_read_b128 v[132:135], v184 offset:64\n" \
  "ds_read_b128 v[172:175], v191\n" \
  "v_mad_u32_u16 v185, v181, 16, v188\n" \
  "v_mad_u32_u16 v186, v182, 16, v188 op_sel:[1,0,0,0]\n" \
  "ds_read_b128 v[112:115], v185 offset:0\n" \
  "ds_read_b128 v[116:119], v185 offset:64\n" \
  "ds_read_b128 v[136:139], v186 offset:0\n" \
  "ds_read_b128 v[140:143], v186 offset:64\n" \
  "ds_read_b128 v[168:171], v191\n" \
  "v_mad_u32_u16 v187, v181, 16, v188 op_sel:[1,0,0,0]\n" \
  "v_mad_u32_u16 v184, v180, 16, v188 op_sel:[1,0,0,0]\n" \
  "ds_read_b128 v[120:123], v187 offset:0\n" \
  "v_mad_u32_u16 v185, v180, 16, v188\n" \
  "ds_read_b128 v[124:127], v187 offset:64\n" \
  "ds_read_b128 v[104:107], v184 offset:0\n" \
  "v_mad_u32_u16 v186, v183, 16, v189\n" \
  "ds_read_b128 v[108:111], v184 offset:64\n" \
  "ds_read_b128 v[96:99], v185 offset:0\n" \
  "s_waitcnt lgkmcnt(9)\n" \
  "s_mov_b32 s51, 3\n" \
  "s_mov_b32 s50, 8\n" \
  "s_call_b64 s[52:53], LA_loop\n" \
  "LA_ev0:\n" \
  "s_waitcnt vmcnt(10)\n" \
  "v_cvt_pk_u16_u32 v208, v208, v209\n" \
  "v_cvt_pk_u16_u32 v210, v210, v211\n" \
  "v_cvt_pk_u16_u32 v212, v212, v213\n" \
  "v_cvt_pk_u16_u32 v214, v214, v215\n" \
  "v_pk_lshrrev_b16 v209, 3, v208 op_sel_hi:[0,1]\n" \
  "v_pk_lshrrev_b16 v211, 3, v210 op_sel_hi:[0,1]\n" \
  "v_pk_lshrrev_b16 v213, 3, v212 op_sel_hi:[0,1]\n" \
  "v_pk_lshrrev_b16 v215, 3, v214 op_sel_hi:[0,1]\n" \
  "v_pk_mad_u16 v208, v208, 8, v209 op_sel_hi:[1,0,1]\n" \
  "v_pk_mad_u16 v210, v210, 8, v211 op_sel_hi:[1,0,1]\n" \
  "v_pk_mad_u16 v212, v212, 8, v213 op_sel_hi:[1,0,1]\n" \
  "v_pk_mad_u16 v214, v214, 8, v215 op_sel_hi:[1,0,1]\n" \
  "v_alignbit_b32 v212, v212, v212, v192\n" \
  "v_alignbit_b32 v214, v214, v214, v192\n" \
  "ds_write2_b32 v195, v208, v210 offset0:0 offset1:4\n" \
  "ds_write2_b32 v195, v212, v214 offset0:2 offset1:6\n" \
  "v_mov_b32 v196, v178\n" \
  "v_readfirstlane_b32 s6, v179\n" \
  "v_mov_b32 v208, 163328\n" \
  "s_mov_b32 s51, 4\n" \
  "s_mov_b32 s50, 1\n" \
  "s_mov_b32 s38, 0x800\n" \
  "s_call_b64 s[52:53], LA_loop\n" \
  "LA_ev6:\n" \
  "v_mov_b32 v202, v178\n" \
  "v_readfirstlane_b32 s35, v209\n" \
  "s_cmp_ge_u32 s35, 128\n" \
  "s_cbranch_scc1 LA_pl\n" \
  "L2_:\n" \
  "ds_read_b32 v209, v208 offset:4\n" \
  "s_waitcnt lgkmcnt(0)\n" \
  "v_readfirstlane_b32 s35, v209\n" \
  "s_cmp_lt_u32 s35, 128\n" \
  "s_cbranch_scc1 L2_\n" \
  "LA_pl:\n" \
  "s_and_b32 s4, s35, 15\n" \
  "s_sub_u32 s4, s4, 4\n" \
  "s_bfe_u32 s5, s35, 0x30004\n" \
  "s_sub_u32 s5, s5, s29\n" \
  "s_cmp_le_i32 s4, 0\n" \
  "s_cbranch_scc1 LA_nt\n" \
  "s_mul_i32 s35, s5, 51200\n" \
  "s_ashr_i32 s36, s35, 31\n" \
  "s_add_u32 s46, s46, s35\n" \
  "s_addc_u32 s47, s47, s36\n" \
  "v_lshrrev_b32 v216, 4, v188\n" \
  "v_mul_u32_u24 v216, 400, v216\n" \
  "v_add_u32 v216, 6400, v216\n" \
  "v_bfe_u32 v218, v194, 4, 4\n" \
  "v_min_u32 v217, 8, v218\n" \
  "v_lshl_add_u32 v217, v217, 4, v216\n" \
  "v_lshl_add_u32 v216, v218, 4, v216\n" \
  "global_load_dwordx4 v[208:211], v216, s[46:47] offset:0\n" \
  "global_load_dwordx4 v[212:215], v217, s[46:47] offset:256\n" \
  "LA_nt:\n" \
  "s_lshl_b32 s5, s5, 7\n" \
  "s_sub_u32 s5, s5, s4\n" \
  "s_min_i32 s51, s4, 0\n" \
  "s_add_u32 s51, s51, 4\n" \
  "s_cmp_eq_u32 s51, 0\n" \
  "s_cbranch_scc1 LA_ev7\n" \
  "s_call_b64 s[52:53], LA_loop\n" \
  "LA_ev7:\n" \
  "v_mov_b32 v203, v178\n" \
  "s_cmp_le_i32 s4, 0\n" \
  "s_cbranch_scc1 LA_ev8\n" \
  "s_waitcnt vmcnt(0)\n" \
  "v_cvt_pk_u16_u32 v208, v208, v209\n" \
  "v_cvt_pk_u16_u32 v210, v210, v211\n" \
  "v_cvt_pk_u16_u32 v212, v212, v213\n" \
  "v_cvt_pk_u16_u32 v214, v214, v215\n" \
  "v_pk_lshrrev_b16 v209, 3, v208 op_sel_hi:[0,1]\n" \
  "v_pk_lshrrev_b16 v211, 3, v210 op_sel_hi:[0,1]\n" \
  "v_pk_lshrrev_b16 v213, 3, v212 op_sel_hi:[0,1]\n" \
  "v_pk_lshrrev_b16 v215, 3, v214 op_sel_hi:[0,1]\n" \
  "v_pk_mad_u16 v208, v208, 8, v209 op_sel_hi:[1,0,1]\n" \
  "v_pk_mad_u16 v210, v210, 8, v211 op_sel_hi:[1,0,1]\n" \
  "v_pk_mad_u16 v212, v212, 8, v213 op_sel_hi:[1,0,1]\n" \
  "v_pk_mad_u16 v214, v214, 8, v215 op_sel_hi:[1,0,1]\n" \
  "v_alignbit_b32 v212, v212, v212, v192\n" \
  "v_alignbit_b32 v214, v214, v214, v192\n" \
  "ds_write2_b32 v195, v208, v210 offset0:0 offset1:4\n" \
  "ds_write2_b32 v195, v212, v214 offset0:2 offset1:6\n" \
  "s_lshl_b32 s35, s4, 8\n" \
  "s_add_u32 s35, s35, 0x400\n" \
  "v_subrev_u32 v194, s35, v194\n" \
  "s_mov_b32 s51, s4\n" \
  "s_call_b64 s[52:53], LA_loop\n" \
  "LA_ev8:\n" \
  "v_mov_b32 v179, v178\n" \
  "ds_read_b128 v[100:103], v185 offset:64\n" \
  "ds_read2_b32 v[144:145], v186 offset0:0 offset1:2\n" \
  "ds_read2_b32 v[146:147], v186 offset0:4 offset1:6\n" \
  "v_mad_u32_u16 v187, v183, 16, v190\n" \
  "ds_read2_b32 v[148:149], v187 offset0:0 offset1:2\n" \
  "ds_read2_b32 v[150:151], v187 offset0:4 offset1:6\n" \
  "ds_read_b128 v[164:167], v191\n" \
  "s_lshr_b32 s35, s30, 2\n" \
  "s_lshl3_add_u32 s33, s29, s35\n" \
  "s_and_b32 s34, s30, 3\n" \
  "s_lshl_b32 s59, s27, 11\n" \
  "s_mul_i32 s35, s33, 1600000\n" \
  "s_add_u32 s54, s24, s35\n" \
  "s_addc_u32 s55, s25, 0\n" \
  "s_mul_i32 s35, s34, 819200\n" \
  "s_lshl1_add_u32 s35, s59, s35\n" \
  "s_add_u32 s35, s35, 102400000\n" \
  "s_add_u32 s56, s24, s35\n" \
  "s_addc_u32 s57, s25, 0\n" \
  "v_mbcnt_lo_u32_b32 v189, -1, 0\n" \
  "v_mbcnt_hi_u32_b32 v189, -1, v189\n" \
  "v_lshlrev_b32 v188, 4, v189\n" \
  "s_waitcnt lgkmcnt(6)\n" \
  "v_mfma_f32_16x16x32_bf16 v[152:155], v[112:115], v[112:115], 0\n" \
  "v_fmac_f32 v176, v156, v88\n" \
  "v_fmac_f32 v176, v157, v89\n" \
  "v_mfma_f32_16x16x32_bf16 v[152:155], v[116:119], v[116:119], v[152:155]\n" \
  "v_fmac_f32 v176, v158, v90\n" \
  "v_fmac_f32 v176, v159, v91\n" \
  "v_mfma_f32_16x16x32_bf16 v[156:159], v[128:131], v[128:131], 0\n" \
  "v_fmac_f32 v176, v160, v92\n" \
  "v_fmac_f32 v176, v161, v93\n" \
  "v_mfma_f32_16x16x32_bf16 v[156:159], v[132:135], v[132:135], v[156:159]\n" \
  "v_fmac_f32 v176, v162, v94\n" \
  "v_fma_f32 v177, v163, v95, v176\n" \
  "v_mfma_f32_16x16x32_bf16 v[160:163], v[128:131], v[136:139], 0\n" \
  "v_mul_f32 v176, v152, v0\n" \
  "v_fmac_f32 v176, v153, v1\n" \
  "v_mfma_f32_16x16x32_bf16 v[160:163], v[132:135], v[140:143], v[160:163]\n" \
  "v_fmac_f32 v176, v154, v2\n" \
  "v_fmac_f32 v176, v155, v3\n" \
  "v_mfma_f32_16x16x32_bf16 v[152:155], v[136:139], v[136:139], 0\n" \
  "v_fmac_f32 v176, v156, v4\n" \
  "v_fmac_f32 v176, v157, v5\n" \
  "v_mfma_f32_16x16x32_bf16 v[152:155], v[140:143], v[140:143], v[152:155]\n" \
  "v_fmac_f32 v176, v158, v6\n" \
  "v_fmac_f32 v176, v159, v7\n" \
  "s_waitcnt lgkmcnt(0)\n" \
  "s_setprio 3\n" \
  "s_barrier\n" \
  "global_load_dwordx4 v[208:211], v188, s[56:57] offset:0 sc1\n" \
  "global_load_dwordx4 v[212:215], v188, s[56:57] offset:1024 sc1\n" \
  "global_load_dwordx4 v[216:219], v188, s[56:57] offset:2048 sc1\n" \
  "global_load_dwordx4 v[220:223], v188, s[56:57] offset:3072 sc1\n" \
  "v_mfma_f32_16x16x32_bf16 v[156:159], v[112:115], v[136:139], 0\n" \
  "v_fmac_f32 v176, v160, v8\n" \
  "v_fmac_f32 v176, v161, v9\n" \
  "v_lshrrev_b32 v180, 1, v189\n" \
  "v_mfma_f32_16x16x32_bf16 v[156:159], v[116:119], v[140:143], v[156:159]\n" \
  "v_fmac_f32 v176, v162, v10\n" \
  "v_fmac_f32 v176, v163, v11\n" \
  "v_xor_b32 v180, v180, v189\n" \
  "v_mfma_f32_16x16x32_bf16 v[160:163], v[112:115], v[128:131], 0\n" \
  "v_fmac_f32 v176, v152, v12\n" \
  "v_fmac_f32 v176, v153, v13\n" \
  "v_add_f32_dpp v177, v177, v177 row_ror:8 row_mask:0xf bank_mask:0xf\n" \
  "v_and_b32 v180, 8, v180\n" \
  "v_mfma_f32_16x16x32_bf16 v[160:163], v[116:119], v[132:135], v[160:163]\n" \
  "v_fmac_f32 v176, v154, v14\n" \
  "v_fmac_f32 v176, v155, v15\n" \
  "v_bfe_u32 v181, v189, 2, 1\n" \
  "v_mfma_f32_16x16x32_bf16 v[152:155], v[112:115], v[120:123], 0\n" \
  "v_fmac_f32 v176, v156, v16\n" \
  "v_fmac_f32 v176, v157, v17\n" \
  "v_lshlrev_b32_e64 v181, v181, 5\n" \
  "v_mfma_f32_16x16x32_bf16 v[152:155], v[116:119], v[124:127], v[152:155]\n" \
  "v_fmac_f32 v176, v158, v18\n" \
  "v_fmac_f32 v176, v159, v19\n" \
  "v_mov_b32_dpp v178, v178 row_ror:4 row_mask:0xf bank_mask:0xf\n" \
  "v_lshlrev_b32 v180, v180, v181\n" \
  "v_mfma_f32_16x16x32_bf16 v[156:159], v[120:123], v[136:139], 0\n" \
  "v_fmac_f32 v176, v160, v20\n" \
  "v_fmac_f32 v176, v161, v21\n" \
  "v_bfe_u32 v181, v189, 2, 2\n" \
  "v_mfma_f32_16x16x32_bf16 v[156:159], v[124:127], v[140:143], v[156:159]\n" \
  "v_fmac_f32 v176, v162, v22\n" \
  "v_fmac_f32 v176, v163, v23\n" \
  "v_lshlrev_b32_e64 v181, v181, 1\n" \
  "v_mfma_f32_16x16x32_bf16 v[160:163], v[104:107], v[128:131], 0\n" \
  "v_fmac_f32 v176, v152, v24\n" \
  "v_fmac_f32 v176, v153, v25\n" \
  "v_cmp_gt_u32 vcc, 32, v189\n" \
  "v_mfma_f32_16x16x32_bf16 v[160:163], v[108:111], v[132:135], v[160:163]\n" \
  "v_fmac_f32 v176, v154, v26\n" \
  "v_fmac_f32 v176, v155, v27\n" \
  "v_mov_b32 v191, 0x3f80\n" \
  "v_mfma_f32_16x16x32_bf16 v[152:155], v[120:123], v[128:131], 0\n" \
  "v_fmac_f32 v176, v156, v28\n" \
  "v_fmac_f32 v176, v157, v29\n" \
  "v_add_u32 v190, 32000, v188\n" \
  "v_mfma_f32_16x16x32_bf16 v[152:155], v[124:127], v[132:135], v[152:155]\n" \
  "v_fmac_f32 v176, v158, v30\n" \
  "v_fmac_f32 v176, v159, v31\n" \
  "v_add_f32_dpp v178, v177, v177 row_ror:4 row_mask:0xf bank_mask:0x1\n" \
  "v_cndmask_b32 v192, v181, v180, vcc\n" \
  "v_mfma_f32_16x16x32_bf16 v[156:159], v[96:99], v[128:131], 0\n" \
  "v_fmac_f32 v176, v160, v32\n" \
  "v_fmac_f32 v176, v161, v33\n" \
  "v_bfe_i32 v180, v192, 0, 1\n" \
  "v_mfma_f32_16x16x32_bf16 v[156:159], v[100:103], v[132:135], v[156:159]\n" \
  "v_fmac_f32 v176, v162, v34\n" \
  "v_fmac_f32 v176, v163, v35\n" \
  "v_and_b32 v180, v180, v191\n" \
  "v_smfmac_f32_16x16x64_bf16 v[172:175], v[144:147], v[128:135], v193\n" \
  "v_fmac_f32 v176, v152, v36\n" \
  "v_fmac_f32 v176, v153, v37\n" \
  "v_bfe_i32 v181, v192, 1, 1\n" \
  "v_smfmac_f32_16x16x64_bf16 v[172:175], v[148:151], v[136:143], v193\n" \
  "v_fmac_f32 v176, v154, v38\n" \
  "v_fmac_f32 v176, v155, v39\n" \
  "v_and_b32 v181, v181, v191\n" \
  "v_mfma_f32_16x16x32_bf16 v[160:163], v[96:99], v[112:115], 0\n" \
  "v_fmac_f32 v176, v156, v40\n" \
  "v_fmac_f32 v176, v157, v41\n" \
  "v_bfe_i32 v182, v192, 2, 1\n" \
  "v_mfma_f32_16x16x32_bf16 v[160:163], v[100:103], v[116:119], v[160:163]\n" \
  "v_fmac_f32 v176, v158, v42\n" \
  "v_fmac_f32 v176, v159, v43\n" \
  "v_and_b32 v182, v182, v191\n" \
  "v_mfma_f32_16x16x32_bf16 v[152:155], v[104:107], v[112:115], 0\n" \
  "v_fmac_f32 v176, v172, v44\n" \
  "v_fmac_f32 v176, v173, v45\n" \
  "v_bfe_i32 v183, v192, 3, 1\n" \
  "v_mfma_f32_16x16x32_bf16 v[152:155], v[108:111], v[116:119], v[152:155]\n" \
  "v_fmac_f32 v176, v174, v46\n" \
  "v_fmac_f32 v176, v175, v47\n" \
  "v_and_b32 v183, v183, v191\n" \
  "v_mfma_f32_16x16x32_bf16 v[156:159], v[104:107], v[136:139], 0\n" \
  "v_fmac_f32 v176, v160, v48\n" \
  "v_fmac_f32 v176, v161, v49\n" \
  "v_and_b32 v192, 3, v189\n" \
  "v_mfma_f32_16x16x32_bf16 v[156:159], v[108:111], v[140:143], v[156:159]\n" \
  "v_fmac_f32 v176, v162, v50\n" \
  "v_fmac_f32 v176, v163, v51\n" \
  "v_mul_u32_u24 v192, 0x5555, v192\n" \
  "v_smfmac_f32_16x16x64_bf16 v[168:171], v[144:147], v[112:119], v193\n" \
  "v_fmac_f32 v176, v152, v52\n" \
  "v_fmac_f32 v176, v153, v53\n" \
  "v_add_u32 v189, 16000, v188\n" \
  "v_smfmac_f32_16x16x64_bf16 v[168:171], v[148:151], v[120:127], v193\n" \
  "v_fmac_f32 v176, v154, v54\n" \
  "v_fmac_f32 v176, v155, v55\n" \
  "v_add_u32 v191, 48000, v188\n" \
  "v_mfma_f32_16x16x32_bf16 v[160:163], v[96:99], v[136:139], 0\n" \
  "v_fmac_f32 v176, v156, v56\n" \
  "v_fmac_f32 v176, v157, v57\n" \
  "v_mfma_f32_16x16x32_bf16 v[160:163], v[100:103], v[140:143], v[160:163]\n" \
  "v_fmac_f32 v176, v158, v58\n" \
  "v_fmac_f32 v176, v159, v59\n" \
  "s_add_u32 s35, s59, 64000\n" \
  "s_add_u32 s54, s54, s35\n" \
  "s_addc_u32 s55, s55, 0\n" \
  "s_add_u32 s58, s59, 0x10000\n" \
  "s_mov_b32 m0, s58\n" \
  "s_add_u32 s35, s58, 0x8000\n" \
  "global_load_lds_dwordx4 v188, s[54:55] sc1\n" \
  "global_load_lds_dwordx4 v188, s[54:55] offset:1024 sc1\n" \
  "v_smfmac_f32_16x16x64_bf16 v[164:167], v[148:151], v[104:111], v193\n" \
  "v_fmac_f32 v176, v168, v60\n" \
  "v_fmac_f32 v176, v169, v61\n" \
  "v_smfmac_f32_16x16x64_bf16 v[164:167], v[144:147], v[96:103], v193\n" \
  "v_fmac_f32 v176, v170, v62\n" \
  "v_fmac_f32 v176, v171, v63\n" \
  "v_mfma_f32_16x16x32_bf16 v[152:155], v[120:123], v[120:123], 0\n" \
  "v_fmac_f32 v176, v160, v64\n" \
  "v_fmac_f32 v176, v161, v65\n" \
  "v_mfma_f32_16x16x32_bf16 v[152:155], v[124:127], v[124:127], v[152:155]\n" \
  "v_fmac_f32 v176, v162, v66\n" \
  "v_fmac_f32 v176, v163, v67\n" \
  "v_mfma_f32_16x16x32_bf16 v[156:159], v[104:107], v[120:123], 0\n" \
  "v_fmac_f32 v176, v164, v68\n" \
  "v_fmac_f32 v176, v165, v69\n" \
  "v_mfma_f32_16x16x32_bf16 v[156:159], v[108:111], v[124:127], v[156:159]\n" \
  "v_fmac_f32 v176, v166, v70\n" \
  "v_fmac_f32 v176, v167, v71\n" \
  "s_add_u32 m0, s58, 0x4000\n" \
  "s_cmp_lt_u32 s35, 0x28000\n" \
  "global_load_lds_dwordx4 v189, s[54:55] sc1\n" \
  "global_load_lds_dwordx4 v189, s[54:55] offset:1024 sc1\n" \
  "s_cselect_b32 s58, s35, s59\n" \
  "s_add_u32 s54, s54, 32000\n" \
  "s_addc_u32 s55, s55, 0\n" \
  "v_mfma_f32_16x16x32_bf16 v[160:163], v[96:99], v[120:123], 0\n" \
  "v_fmac_f32 v176, v152, v72\n" \
  "v_fmac_f32 v176, v153, v73\n" \
  "v_mfma_f32_16x16x32_bf16 v[160:163], v[100:103], v[124:127], v[160:163]\n" \
  "v_fmac_f32 v176, v154, v74\n" \
  "v_fmac_f32 v176, v155, v75\n" \
  "v_mfma_f32_16x16x32_bf16 v[152:155], v[96:99], v[104:107], 0\n" \
  "v_fmac_f32 v176, v156, v76\n" \
  "v_fmac_f32 v176, v157, v77\n" \
  "v_mfma_f32_16x16x32_bf16 v[152:155], v[100:103], v[108:111], v[152:155]\n" \
  "v_fmac_f32 v176, v158, v78\n" \
  "v_fmac_f32 v176, v159, v79\n" \
  "v_mfma_f32_16x16x32_bf16 v[156:159], v[104:107], v[104:107], 0\n" \
  "v_fmac_f32 v176, v160, v80\n" \
  "v_fmac_f32 v176, v161, v81\n" \
  "v_mfma_f32_16x16x32_bf16 v[156:159], v[108:111], v[108:111], v[156:159]\n" \
  "v_fmac_f32 v176, v162, v82\n" \
  "v_fmac_f32 v176, v163, v83\n" \
  "v_mfma_f32_16x16x32_bf16 v[160:163], v[148:151], v[144:147], 0\n" \
  "v_fmac_f32 v176, v152, v84\n" \
  "v_fmac_f32 v176, v153, v85\n" \
  "v_mfma_f32_16x16x32_bf16 v[160:163], v[96:99], v[96:99], v[160:163]\n" \
  "v_fmac_f32 v176, v154, v86\n" \
  "v_fmac_f32 v176, v155, v87\n" \
  "v_mfma_f32_16x16x32_bf16 v[160:163], v[100:103], v[100:103], v[160:163]\n" \
  "s_mov_b32 s51, 7\n" \
  "v_fmac_f32 v176, v156, v88\n" \
  "v_fmac_f32 v176, v157, v89\n" \
  "v_fmac_f32 v176, v158, v90\n" \
  "v_fmac_f32 v176, v159, v91\n" \
  "s_nop 2\n" \
  "v_fmac_f32 v176, v160, v92\n" \
  "v_fmac_f32 v176, v161, v93\n" \
  "v_fmac_f32 v176, v162, v94\n" \
  "v_fma_f32 v177, v163, v95, v176\n" \
  "s_nop 1\n" \
  "v_add_f32_dpp v177, v177, v177 row_ror:8 row_mask:0xf bank_mask:0xf\n" \
  "v_mov_b32_dpp v178, v178 row_ror:4 row_mask:0xf bank_mask:0xf\n" \
  "s_nop 1\n" \
  "v_add_f32_dpp v178, v177, v177 row_ror:4 row_mask:0xf bank_mask:0x1\n" \
  "v_mov_b32 v99, v179\n" \
  "s_nop 0\n" \
  "v_mov_b32_dpp v196, v178 row_ror:8 row_mask:0xf bank_mask:0xc\n" \
  "s_nop 1\n" \
  "v_permlane16_swap_b32 v196, v197\n" \
  "v_permlane16_swap_b32 v198, v199\n" \
  "v_permlane16_swap_b32 v200, v201\n" \
  "v_permlane16_swap_b32 v202, v203\n" \
  "v_permlane16_swap_b32 v179, v99\n" \
  "v_add_f32 v196, v196, v197\n" \
  "v_add_f32 v198, v198, v199\n" \
  "v_add_f32 v200, v200, v201\n" \
  "v_add_f32 v202, v202, v203\n" \
  "v_add_f32 v179, v179, v99\n" \
  "v_lshrrev_b32 v96, 4, v188\n" \
  "v_and_b32 v97, 1, v96\n" \
  "v_mov_b32 v99, v179\n" \
  "v_lshrrev_b32 v98, 4, v96\n" \
  "v_permlane32_swap_b32 v196, v198\n" \
  "v_permlane32_swap_b32 v200, v202\n" \
  "v_permlane32_swap_b32 v179, v99\n" \
  "v_add_f32 v196, v196, v198\n" \
  "v_add_f32 v200, v200, v202\n" \
  "v_add_f32 v179, v179, v99\n" \
  "v_cmp_eq_u32 vcc, 1, v97\n" \
  "v_lshlrev_b32 v97, 2, v97\n" \
  "v_add_lshl_u32 v97, v97, v98, 2\n" \
  "v_bfe_u32 v98, v96, 2, 2\n" \
  "v_add_f32_dpp v196, v196, v196 quad_perm:[1,0,3,2] row_mask:0xf bank_mask:0xf\n" \
  "v_add_f32_dpp v200, v200, v200 quad_perm:[1,0,3,2] row_mask:0xf bank_mask:0xf\n" \
  "v_add_f32_dpp v179, v179, v179 quad_perm:[1,0,3,2] row_mask:0xf bank_mask:0xf\n" \
  "v_sub_u32 v97, v97, v98\n" \
  "v_cndmask_b32 v196, v196, v200, vcc\n" \
  "v_add_u32 v97, 1, v97\n" \
  "s_min_i32 s35, s4, 0\n" \
  "s_add_u32 s36, s4, 32\n" \
  "s_add_u32 s37, s4, 29\n" \
  "v_add_f32_dpp v179, v179, v179 quad_perm:[2,3,0,1] row_mask:0xf bank_mask:0xf\n" \
  "v_add_f32_dpp v196, v196, v196 quad_perm:[2,3,0,1] row_mask:0xf bank_mask:0xf\n" \
  "s_mov_b32 exec_lo, 0x0\n" \
  "s_mov_b32 exec_hi, 0xaaaa0000\n" \
  "v_add_u32 v97, s35, v97\n" \
  "s_mov_b32 exec_lo, 0x5500\n" \
  "s_mov_b32 exec_hi, 0x0\n" \
  "v_add_u32 v97, s36, v97\n" \
  "s_mov_b32 exec_lo, 0x4444\n" \
  "s_mov_b32 exec_hi, 0x0\n" \
  "v_sub_u32 v97, s37, v98\n" \
  "v_mov_b32 v196, v179\n" \
  "s_mov_b64 exec, -1\n" \
  "v_cmp_lt_u32 vcc, 31, v97\n" \
  "v_add_u32 v98, s5, v97\n" \
  "s_lshl2_add_u32 s35, s7, s28\n" \
  "s_lshl_b32 s35, s35, 5\n" \
  "v_cndmask_b32 v97, v97, v98, vcc\n" \
  "v_add_lshl_u32 v97, v97, s35, 2\n" \
  "s_lshl_b32 s35, s31, 16\n" \
  "s_add_u32 s35, s35, 109871104\n" \
  "s_add_u32 s40, s24, s35\n" \
  "s_addc_u32 s41, s25, 0\n" \
  "v_min_u32 v196, -2, v196\n" \
  "v_lshlrev_b32 v184, 16, v204\n" \
  "v_and_b32 v185, 0xffff0000, v204\n" \
  "v_lshlrev_b32 v186, 16, v205\n" \
  "v_and_b32 v187, 0xffff0000, v205\n" \
  "s_barrier\n" \
  "s_add_u32 s56, s56, 32768\n" \
  "s_addc_u32 s57, s57, 0\n" \
  "global_load_dwordx4 v[224:227], v188, s[56:57] sc1\n" \
  "global_load_dwordx4 v[228:231], v188, s[56:57] offset:1024 sc1\n" \
  "global_load_dwordx4 v[232:235], v188, s[56:57] offset:2048 sc1\n" \
  "global_load_dwordx4 v[236:239], v188, s[56:57] offset:3072 sc1\n" \
  "s_add_u32 s56, s56, 32768\n" \
  "s_addc_u32 s57, s57, 0\n" \
  "s_mov_b32 exec_lo, 0x33337777\n" \
  "s_mov_b32 exec_hi, 0x33333333\n" \
  "global_store_dword v97, v196, s[40:41] sc1\n" \
  "s_mov_b64 exec, -1\n" \
  "s_waitcnt vmcnt(9)\n" \
  "s_setprio 0\n" \
  "s_branch LG_start\n" \
  ".p2align 6\n" \
  "LA_loop:\n" \
  "LA_mid:\n" \
  "v_mfma_f32_16x16x32_bf16 v[152:155], v[112:115], v[112:115], 0\n" \
  "s_waitcnt lgkmcnt(8)\n" \
  "ds_read_b128 v[100:103], v185 offset:64\n" \
  "v_fmac_f32 v176, v156, v88\n" \
  "v_fmac_f32 v176, v157, v89\n" \
  "v_mfma_f32_16x16x32_bf16 v[152:155], v[116:119], v[116:119], v[152:155]\n" \
  "ds_read2_b32 v[144:145], v186 offset0:0 offset1:2\n" \
  "v_fmac_f32 v176, v158, v90\n" \
  "v_fmac_f32 v176, v159, v91\n" \
  "v_mfma_f32_16x16x32_bf16 v[156:159], v[128:131], v[128:131], 0\n" \
  "ds_read2_b32 v[146:147], v186 offset0:4 offset1:6\n" \
  "v_fmac_f32 v176, v160, v92\n" \
  "v_fmac_f32 v176, v161, v93\n" \
  "v_mad_u32_u16 v187, v183, 16, v190\n" \
  "v_mfma_f32_16x16x32_bf16 v[156:159], v[132:135], v[132:135], v[156:159]\n" \
  "s_waitcnt lgkmcnt(10)\n" \
  "ds_read2_b32 v[148:149], v187 offset0:0 offset1:2\n" \
  "v_fmac_f32 v176, v162, v94\n" \
  "v_fma_f32 v177, v163, v95, v176\n" \
  "v_mfma_f32_16x16x32_bf16 v[160:163], v[128:131], v[136:139], 0\n" \
  "s_waitcnt lgkmcnt(10)\n" \
  "ds_read2_b32 v[150:151], v187 offset0:4 offset1:6\n" \
  "v_mul_f32 v176, v152, v0\n" \
  "v_fmac_f32 v176, v153, v1\n" \
  "v_mfma_f32_16x16x32_bf16 v[160:163], v[132:135], v[140:143], v[160:163]\n" \
  "ds_read_b128 v[180:183], v194\n" \
  "v_fmac_f32 v176, v154, v2\n" \
  "v_fmac_f32 v176, v155, v3\n" \
  "v_mfma_f32_16x16x32_bf16 v[152:155], v[136:139], v[136:139], 0\n" \
  "v_fmac_f32 v176, v156, v4\n" \
  "v_fmac_f32 v176, v157, v5\n" \
  "v_mfma_f32_16x16x32_bf16 v[152:155], v[140:143], v[140:143], v[152:155]\n" \
  "ds_read_b128 v[164:167], v191\n" \
  "v_fmac_f32 v176, v158, v6\n" \
  "v_fmac_f32 v176, v159, v7\n" \
  "v_mfma_f32_16x16x32_bf16 v[156:159], v[112:115], v[136:139], 0\n" \
  "v_fmac_f32 v176, v160, v8\n" \
  "v_fmac_f32 v176, v161, v9\n" \
  "v_mfma_f32_16x16x32_bf16 v[156:159], v[116:119], v[140:143], v[156:159]\n" \
  "v_fmac_f32 v176, v162, v10\n" \
  "v_fmac_f32 v176, v163, v11\n" \
  "v_mfma_f32_16x16x32_bf16 v[160:163], v[112:115], v[128:131], 0\n" \
  "v_fmac_f32 v176, v152, v12\n" \
  "v_fmac_f32 v176, v153, v13\n" \
  "v_add_f32_dpp v177, v177, v177 row_ror:8 row_mask:0xf bank_mask:0xf\n" \
  "v_mfma_f32_16x16x32_bf16 v[160:163], v[116:119], v[132:135], v[160:163]\n" \
  "s_waitcnt lgkmcnt(11)\n" \
  "v_fmac_f32 v176, v154, v14\n" \
  "v_fmac_f32 v176, v155, v15\n" \
  "v_mfma_f32_16x16x32_bf16 v[152:155], v[112:115], v[120:123], 0\n" \
  "s_waitcnt lgkmcnt(10)\n" \
  "v_fmac_f32 v176, v156, v16\n" \
  "v_fmac_f32 v176, v157, v17\n" \
  "v_mfma_f32_16x16x32_bf16 v[152:155], v[116:119], v[124:127], v[152:155]\n" \
  "v_fmac_f32 v176, v158, v18\n" \
  "v_fmac_f32 v176, v159, v19\n" \
  "v_mov_b32_dpp v178, v178 row_ror:4 row_mask:0xf bank_mask:0xf\n" \
  "v_mfma_f32_16x16x32_bf16 v[156:159], v[120:123], v[136:139], 0\n" \
  "v_fmac_f32 v176, v160, v20\n" \
  "v_fmac_f32 v176, v161, v21\n" \
  "v_mfma_f32_16x16x32_bf16 v[156:159], v[124:127], v[140:143], v[156:159]\n" \
  "s_waitcnt lgkmcnt(9)\n" \
  "v_fmac_f32 v176, v162, v22\n" \
  "v_fmac_f32 v176, v163, v23\n" \
  "v_mfma_f32_16x16x32_bf16 v[160:163], v[104:107], v[128:131], 0\n" \
  "s_waitcnt lgkmcnt(1)\n" \
  "v_fmac_f32 v176, v152, v24\n" \
  "v_fmac_f32 v176, v153, v25\n" \
  "v_mad_u32_u16 v184, v182, 16, v188\n" \
  "v_mfma_f32_16x16x32_bf16 v[160:163], v[108:111], v[132:135], v[160:163]\n" \
  "v_fmac_f32 v176, v154, v26\n" \
  "v_fmac_f32 v176, v155, v27\n" \
  "v_mfma_f32_16x16x32_bf16 v[152:155], v[120:123], v[128:131], 0\n" \
  "v_fmac_f32 v176, v156, v28\n" \
  "v_fmac_f32 v176, v157, v29\n" \
  "v_mfma_f32_16x16x32_bf16 v[152:155], v[124:127], v[132:135], v[152:155]\n" \
  "v_fmac_f32 v176, v158, v30\n" \
  "v_fmac_f32 v176, v159, v31\n" \
  "v_add_f32_dpp v178, v177, v177 row_ror:4 row_mask:0xf bank_mask:0x1\n" \
  "v_mfma_f32_16x16x32_bf16 v[156:159], v[96:99], v[128:131], 0\n" \
  "v_fmac_f32 v176, v160, v32\n" \
  "v_fmac_f32 v176, v161, v33\n" \
  "v_mfma_f32_16x16x32_bf16 v[156:159], v[100:103], v[132:135], v[156:159]\n" \
  "v_fmac_f32 v176, v162, v34\n" \
  "v_fmac_f32 v176, v163, v35\n" \
  "v_smfmac_f32_16x16x64_bf16 v[172:175], v[144:147], v[128:135], v193\n" \
  "ds_read_b128 v[128:131], v184 offset:0\n" \
  "v_fmac_f32 v176, v152, v36\n" \
  "v_fmac_f32 v176, v153, v37\n" \
  "v_smfmac_f32_16x16x64_bf16 v[172:175], v[148:151], v[136:143], v193\n" \
  "ds_read_b128 v[132:135], v184 offset:64\n" \
  "v_fmac_f32 v176, v154, v38\n" \
  "v_fmac_f32 v176, v155, v39\n" \
  "v_mfma_f32_16x16x32_bf16 v[160:163], v[96:99], v[112:115], 0\n" \
  "v_fmac_f32 v176, v156, v40\n" \
  "v_fmac_f32 v176, v157, v41\n" \
  "v_mfma_f32_16x16x32_bf16 v[160:163], v[100:103], v[116:119], v[160:163]\n" \
  "v_fmac_f32 v176, v158, v42\n" \
  "v_fmac_f32 v176, v159, v43\n" \
  "v_add_u32 v194, 0x100, v194\n" \
  "v_mfma_f32_16x16x32_bf16 v[152:155], v[104:107], v[112:115], 0\n" \
  "v_fmac_f32 v176, v172, v44\n" \
  "v_fmac_f32 v176, v173, v45\n" \
  "v_mfma_f32_16x16x32_bf16 v[152:155], v[108:111], v[116:119], v[152:155]\n" \
  "v_fmac_f32 v176, v174, v46\n" \
  "v_fmac_f32 v176, v175, v47\n" \
  "v_mfma_f32_16x16x32_bf16 v[156:159], v[104:107], v[136:139], 0\n" \
  "ds_read_b128 v[172:175], v191\n" \
  "v_fmac_f32 v176, v160, v48\n" \
  "v_fmac_f32 v176, v161, v49\n" \
  "v_mad_u32_u16 v185, v181, 16, v188\n" \
  "v_mfma_f32_16x16x32_bf16 v[156:159], v[108:111], v[140:143], v[156:159]\n" \
  "v_fmac_f32 v176, v162, v50\n" \
  "v_fmac_f32 v176, v163, v51\n" \
  "v_mad_u32_u16 v186, v182, 16, v188 op_sel:[1,0,0,0]\n" \
  "v_smfmac_f32_16x16x64_bf16 v[168:171], v[144:147], v[112:119], v193\n" \
  "ds_read_b128 v[112:115], v185 offset:0\n" \
  "v_fmac_f32 v176, v152, v52\n" \
  "v_fmac_f32 v176, v153, v53\n" \
  "v_smfmac_f32_16x16x64_bf16 v[168:171], v[148:151], v[120:127], v193\n" \
  "ds_read_b128 v[116:119], v185 offset:64\n" \
  "v_fmac_f32 v176, v154, v54\n" \
  "v_fmac_f32 v176, v155, v55\n" \
  "v_mfma_f32_16x16x32_bf16 v[160:163], v[96:99], v[136:139], 0\n" \
  "v_fmac_f32 v176, v156, v56\n" \
  "v_fmac_f32 v176, v157, v57\n" \
  "v_mfma_f32_16x16x32_bf16 v[160:163], v[100:103], v[140:143], v[160:163]\n" \
  "s_waitcnt lgkmcnt(5)\n" \
  "v_fmac_f32 v176, v158, v58\n" \
  "v_fmac_f32 v176, v159, v59\n" \
  "v_smfmac_f32_16x16x64_bf16 v[164:167], v[148:151], v[104:111], v193\n" \
  "ds_read_b128 v[136:139], v186 offset:0\n" \
  "v_fmac_f32 v176, v168, v60\n" \
  "v_fmac_f32 v176, v169, v61\n" \
  "v_smfmac_f32_16x16x64_bf16 v[164:167], v[144:147], v[96:103], v193\n" \
  "ds_read_b128 v[140:143], v186 offset:64\n" \
  "v_fmac_f32 v176, v170, v62\n" \
  "v_fmac_f32 v176, v171, v63\n" \
  "v_mfma_f32_16x16x32_bf16 v[152:155], v[120:123], v[120:123], 0\n" \
  "v_fmac_f32 v176, v160, v64\n" \
  "v_fmac_f32 v176, v161, v65\n" \
  "v_mfma_f32_16x16x32_bf16 v[152:155], v[124:127], v[124:127], v[152:155]\n" \
  "ds_read_b128 v[168:171], v191\n" \
  "v_fmac_f32 v176, v162, v66\n" \
  "v_fmac_f32 v176, v163, v67\n" \
  "v_mad_u32_u16 v187, v181, 16, v188 op_sel:[1,0,0,0]\n" \
  "v_mfma_f32_16x16x32_bf16 v[156:159], v[104:107], v[120:123], 0\n" \
  "v_fmac_f32 v176, v164, v68\n" \
  "v_fmac_f32 v176, v165, v69\n" \
  "v_mad_u32_u16 v184, v180, 16, v188 op_sel:[1,0,0,0]\n" \
  "v_mfma_f32_16x16x32_bf16 v[156:159], v[108:111], v[124:127], v[156:159]\n" \
  "v_fmac_f32 v176, v166, v70\n" \
  "v_fmac_f32 v176, v167, v71\n" \
  "v_mfma_f32_16x16x32_bf16 v[160:163], v[96:99], v[120:123], 0\n" \
  "ds_read_b128 v[120:123], v187 offset:0\n" \
  "v_fmac_f32 v176, v152, v72\n" \
  "v_fmac_f32 v176, v153, v73\n" \
  "v_mfma_f32_16x16x32_bf16 v[160:163], v[100:103], v[124:127], v[160:163]\n" \
  "v_fmac_f32 v176, v154, v74\n" \
  "v_fmac_f32 v176, v155, v75\n" \
  "v_mad_u32_u16 v185, v180, 16, v188\n" \
  "v_mfma_f32_16x16x32_bf16 v[152:155], v[96:99], v[104:107], 0\n" \
  "ds_read_b128 v[124:127], v187 offset:64\n" \
  "v_fmac_f32 v176, v156, v76\n" \
  "v_fmac_f32 v176, v157, v77\n" \
  "v_mfma_f32_16x16x32_bf16 v[152:155], v[100:103], v[108:111], v[152:155]\n" \
  "v_fmac_f32 v176, v158, v78\n" \
  "v_fmac_f32 v176, v159, v79\n" \
  "v_mfma_f32_16x16x32_bf16 v[156:159], v[104:107], v[104:107], 0\n" \
  "ds_read_b128 v[104:107], v184 offset:0\n" \
  "v_fmac_f32 v176, v160, v80\n" \
  "v_fmac_f32 v176, v161, v81\n" \
  "v_mfma_f32_16x16x32_bf16 v[156:159], v[108:111], v[108:111], v[156:159]\n" \
  "s_sub_u32 s51, s51, 1\n" \
  "v_fmac_f32 v176, v162, v82\n" \
  "v_fmac_f32 v176, v163, v83\n" \
  "v_mad_u32_u16 v186, v183, 16, v189\n" \
  "v_mfma_f32_16x16x32_bf16 v[160:163], v[148:151], v[144:147], 0\n" \
  "s_cmp_lg_u32 s51, 0\n" \
  "ds_read_b128 v[108:111], v184 offset:64\n" \
  "v_fmac_f32 v176, v152, v84\n" \
  "v_fmac_f32 v176, v153, v85\n" \
  "v_mfma_f32_16x16x32_bf16 v[160:163], v[96:99], v[96:99], v[160:163]\n" \
  "s_waitcnt lgkmcnt(8)\n" \
  "ds_read_b128 v[96:99], v185 offset:0\n" \
  "v_fmac_f32 v176, v154, v86\n" \
  "v_fmac_f32 v176, v155, v87\n" \
  "v_mfma_f32_16x16x32_bf16 v[160:163], v[100:103], v[100:103], v[160:163]\n" \
  "s_cbranch_scc1 LA_loop\n" \
  "s_cmp_ge_u32 s50, 6\n" \
  "s_cbranch_scc1 LA_ret\n" \
  "s_set_gpr_idx_on s50, gpr_idx(DST)\n" \
  "v_mov_b32 v196, v178\n" \
  "s_lshl_b32 s35, s50, 3\n" \
  "s_waitcnt vmcnt(0)\n" \
  "s_set_gpr_idx_on s35, gpr_idx(SRC0,SRC1)\n" \
  "v_cvt_pk_u16_u32 v210, v208, v209\n" \
  "v_cvt_pk_u16_u32 v211, v210, v211\n" \
  "v_cvt_pk_u16_u32 v212, v212, v213\n" \
  "v_cvt_pk_u16_u32 v213, v214, v215\n" \
  "s_set_gpr_idx_off\n" \
  "v_pk_lshrrev_b16 v214, 3, v210 op_sel_hi:[0,1]\n" \
  "v_pk_mad_u16 v210, v210, 8, v214 op_sel_hi:[1,0,1]\n" \
  "v_pk_lshrrev_b16 v215, 3, v211 op_sel_hi:[0,1]\n" \
  "v_pk_mad_u16 v211, v211, 8, v215 op_sel_hi:[1,0,1]\n" \
  "v_pk_lshrrev_b16 v214, 3, v212 op_sel_hi:[0,1]\n" \
  "v_pk_mad_u16 v212, v212, 8, v214 op_sel_hi:[1,0,1]\n" \
  "v_pk_lshrrev_b16 v215, 3, v213 op_sel_hi:[0,1]\n" \
  "v_pk_mad_u16 v213, v213, 8, v215 op_sel_hi:[1,0,1]\n" \
  "s_lshr_b32 s35, s38, 1\n" \
  "v_add_u32 v214, s35, v195\n" \
  "v_alignbit_b32 v212, v212, v212, v192\n" \
  "v_alignbit_b32 v213, v213, v213, v192\n" \
  "ds_write2_b32 v214, v210, v211 offset0:0 offset1:4\n" \
  "ds_write2_b32 v214, v212, v213 offset0:2 offset1:6\n" \
  "ds_read_b32 v209, v208 offset:4\n" \
  "v_subrev_u32 v194, s38, v194\n" \
  "s_xor_b32 s38, s38, 0x800\n" \
  "s_add_u32 s50, s50, 1\n" \
  "s_mov_b32 s51, 4\n" \
  "s_branch LA_loop\n" \
  "LA_ret:\n" \
  "s_setpc_b64 s[52:53]\n" \
  "LB_start:\n" \
  "s_setprio 2\n" \
  "s_lshl_b32 s35, s31, 7\n" \
  "s_waitcnt lgkmcnt(0)\n" \
  "s_add_u32 s40, s10, s35\n" \
  "s_addc_u32 s41, s11, 0\n" \
  "s_add_u32 s37, s36, 32\n" \
  "L3_:\n" \
  "s_sub_u32 s35, s36, 125\n" \
  "s_min_u32 s35, s35, s36\n" \
  "s_mul_i32 m0, s35, 1040\n" \
  "s_lshl_b32 s35, s35, 11\n" \
  "s_add_u32 s42, s40, s35\n" \
  "s_addc_u32 s43, s41, 0\n" \
  "global_load_lds_dwordx4 v254, s[42:43]\n" \
  "s_add_u32 s36, s36, 4\n" \
  "s_sub_u32 s35, s36, 125\n" \
  "s_min_u32 s35, s35, s36\n" \
  "s_mul_i32 m0, s35, 1040\n" \
  "s_lshl_b32 s35, s35, 11\n" \
  "s_add_u32 s42, s40, s35\n" \
  "s_addc_u32 s43, s41, 0\n" \
  "global_load_lds_dwordx4 v254, s[42:43]\n" \
  "s_add_u32 s36, s36, 4\n" \
  "s_cmp_lg_u32 s37, s36\n" \
  "s_cbranch_scc1 L3_\n" \
  "v_mov_b32 v190, 163328\n" \
  "v_mov_b64 v[2:3], 0\n" \
  "ds_write_b64 v190, v[2:3]\n" \
  "ds_write_b64 v190, v[2:3] offset:8\n" \
  "ds_write_b64 v190, v[2:3] offset:16\n" \
  "s_waitcnt lgkmcnt(0)\n" \
  "s_barrier\n" \
  "v_lshlrev_b32 v247, 7, v255\n" \
  "s_add_u32 s37, s36, 96\n" \
  "L4_:\n" \
  "s_sub_u32 s35, s36, 125\n" \
  "s_min_u32 s35, s35, s36\n" \
  "s_mul_i32 m0, s35, 1040\n" \
  "s_lshl_b32 s35, s35, 11\n" \
  "s_add_u32 s42, s40, s35\n" \
  "s_addc_u32 s43, s41, 0\n" \
  "global_load_lds_dwordx4 v254, s[42:43]\n" \
  "s_add_u32 s36, s36, 4\n" \
  "s_sub_u32 s35, s36, 125\n" \
  "s_min_u32 s35, s35, s36\n" \
  "s_mul_i32 m0, s35, 1040\n" \
  "s_lshl_b32 s35, s35, 11\n" \
  "s_add_u32 s42, s40, s35\n" \
  "s_addc_u32 s43, s41, 0\n" \
  "global_load_lds_dwordx4 v254, s[42:43]\n" \
  "s_add_u32 s36, s36, 4\n" \
  "s_cmp_lg_u32 s37, s36\n" \
  "s_cbranch_scc1 L4_\n" \
  "s_add_u32 s48, s24, 110002176\n" \
  "s_addc_u32 s49, s25, 0\n" \
  "s_lshl_b32 s35, s26, 2\n" \
  "v_mov_b32 v193, s35\n" \
  "global_load_dword v193, v193, s[48:49] offset:1024 sc1\n" \
  "v_mov_b32 v191, 1\n" \
  "v_and_b32 v252, 15, v255\n" \
  "v_lshrrev_b32 v253, 4, v255\n" \
  "s_lshl2_add_u32 s35, s31, s28\n" \
  "s_lshl_b32 s35, s35, 1\n" \
  "v_bfe_u32 v1, v255, 2, 1\n" \
  "v_add_u32 v1, s35, v1\n" \
  "v_and_b32 v2, 3, v255\n" \
  "v_lshlrev_b32 v3, 5, v1\n" \
  "v_lshl_add_u32 v3, v2, 3, v3\n" \
  "v_lshlrev_b32 v4, 2, v2\n" \
  "v_add_u32 v4, 380, v4\n" \
  "v_cmp_gt_u32 vcc, 12, v1\n" \
  "v_lshrrev_b32 v5, 3, v255\n" \
  "v_mul_u32_u24 v5, 6400, v5\n" \
  "v_cndmask_b32 v3, v4, v3, vcc\n" \
  "v_add_u32 v5, 3200, v5\n" \
  "v_add_u32 v227, v5, v3\n" \
  "v_lshl_add_u32 v3, v1, 2, v2\n" \
  "v_add_u32 v4, 48, v3\n" \
  "v_mul_u32_u24 v5, 205, v3\n" \
  "v_lshrrev_b32 v5, 11, v5\n" \
  "v_mul_u32_u24 v5, 10, v5\n" \
  "v_sub_u32 v5, v3, v5\n" \
  "v_lshlrev_b32 v236, 26, v5\n" \
  "v_mul_u32_u24 v5, 205, v4\n" \
  "v_lshrrev_b32 v5, 11, v5\n" \
  "v_mul_u32_u24 v5, 10, v5\n" \
  "v_sub_u32 v5, v4, v5\n" \
  "v_lshlrev_b32 v237, 26, v5\n" \
  "s_lshl2_add_u32 s4, s31, s28\n" \
  "s_lshl_b32 s4, s4, 7\n" \
  "s_add_u32 s36, s4, 96\n" \
  "s_min_u32 s36, s36, 984\n" \
  "s_add_u32 s37, s4, 112\n" \
  "s_min_u32 s37, s37, 984\n" \
  "v_add_u32 v186, s4, v252\n" \
  "v_add_u32 v187, s36, v252\n" \
  "v_add_u32 v188, s37, v252\n" \
  "s_mul_i32 s38, s7, 51200\n" \
  "s_add_u32 s42, s8, s38\n" \
  "s_addc_u32 s43, s9, 0\n" \
  "s_xor_b32 s38, s31, 1\n" \
  "s_lshl_b32 s38, s38, 7\n" \
  "s_add_u32 s94, s10, s38\n" \
  "s_addc_u32 s95, s11, 0\n" \
  "s_mul_i32 s5, s28, 4224\n" \
  "s_add_u32 s5, s5, 146432\n" \
  "v_lshrrev_b32 v2, 3, v186\n" \
  "v_lshl_add_u32 v2, v186, 3, v2\n" \
  "v_add_lshl_u32 v249, v2, v253, 4\n" \
  "v_lshrrev_b32 v2, 3, v187\n" \
  "v_lshl_add_u32 v2, v187, 3, v2\n" \
  "v_add_lshl_u32 v250, v2, v253, 4\n" \
  "v_lshrrev_b32 v2, 3, v188\n" \
  "v_lshl_add_u32 v2, v188, 3, v2\n" \
  "v_add_lshl_u32 v251, v2, v253, 4\n" \
  "v_mul_u32_u24 v1, 1600000, v253\n" \
  "v_lshl_add_u32 v186, v186, 4, v1\n" \
  "v_lshl_add_u32 v187, v187, 4, v1\n" \
  "v_lshl_add_u32 v188, v188, 4, v1\n" \
  "v_lshlrev_b32 v1, 4, v253\n" \
  "v_xor_b32 v244, v1, v254\n" \
  "v_lshl_add_u32 v245, v255, 4, s5\n" \
  "v_lshrrev_b32 v1, 1, v252\n" \
  "s_waitcnt vmcnt(1)\n" \
  "s_mov_b64 exec, 1\n" \
  "ds_add_u32 v190, v191 offset:12\n" \
  "s_mov_b64 exec, -1\n" \
  "s_memtime s[88:89]\n" \
  "v_xor_b32 v1, v1, v253\n" \
  "v_lshlrev_b32 v1, 4, v1\n" \
  "v_lshl_add_u32 v1, v252, 7, v1\n" \
  "v_add_u32 v246, s5, v1\n" \
  "v_xor_b32 v1, 64, v1\n" \
  "v_add_u32 v242, s5, v1\n" \
  "v_lshlrev_b32 v1, 4, v255\n" \
  "v_xor_b32 v1, 64, v1\n" \
  "v_add_u32 v243, s5, v1\n" \
  "v_lshrrev_b32 v1, 2, v255\n" \
  "s_lshl_b32 s35, s28, 5\n" \
  "s_lshl_b32 s36, s31, 6\n" \
  "s_add_u32 s35, s35, s36\n" \
  "s_and_b32 s35, s35, 127\n" \
  "v_add_u32 v1, s35, v1\n" \
  "v_lshrrev_b32 v2, 4, v255\n" \
  "v_and_b32 v2, 2, v2\n" \
  "v_xor_b32 v2, v2, v255\n" \
  "v_and_b32 v2, 3, v2\n" \
  "v_lshlrev_b32 v2, 4, v2\n" \
  "v_lshl_add_u32 v189, v1, 10, v2\n" \
  "v_add_u32 v194, 15360, v189\n" \
  "v_lshrrev_b32 v1, 2, v252\n" \
  "v_lshlrev_b32 v1, 6, v1\n" \
  "v_lshl_add_u32 v1, v253, 9, v1\n" \
  "v_and_b32 v2, 3, v252\n" \
  "v_lshl_add_u32 v1, v2, 3, v1\n" \
  "v_and_b32 v2, 1, v253\n" \
  "v_lshl_add_u32 v1, v2, 5, v1\n" \
  "v_add_u32 v184, 138240, v1\n" \
  "v_xor_b32 v1, 32, v1\n" \
  "v_add_u32 v185, 138240, v1\n" \
  "s_bfe_u32 s35, s30, 0x10001\n" \
  "s_lshl1_add_u32 s35, s29, s35\n" \
  "s_lshr_b32 s6, s30, 2\n" \
  "s_lshl_b32 s6, s6, 18\n" \
  "s_lshl_b32 s38, s35, 6\n" \
  "s_add_u32 s92, s14, s38\n" \
  "s_addc_u32 s93, s15, 0\n" \
  "s_lshr_b32 s37, s30, 3\n" \
  "s_add_u32 s37, s37, 96\n" \
  "s_lshl_b32 s34, s37, 17\n" \
  "s_mul_i32 s35, s35, 400\n" \
  "s_add_u32 s37, s37, s35\n" \
  "s_mul_i32 s37, s37, 16000\n" \
  "s_add_u32 s46, s24, s37\n" \
  "s_addc_u32 s47, s25, 0\n" \
  "s_lshr_b32 s36, s30, 2\n" \
  "s_add_u32 s36, s36, s35\n" \
  "s_mul_i32 s36, s36, 16000\n" \
  "s_add_u32 s44, s24, s36\n" \
  "s_addc_u32 s45, s25, 0\n" \
  "s_lshl_b32 s96, s28, 11\n" \
  "s_add_u32 s96, s96, 138240\n" \
  "v_lshlrev_b32 v238, 4, v255\n" \
  "v_mov_b32 v234, 0\n" \
  "s_lshl2_add_u32 s33, s26, s28\n" \
  "s_mov_b32 s52, 0x10002\n" \
  "s_mov_b32 s53, 0xc0c0400\n" \
  "s_mov_b32 s54, 0x4000c0c\n" \
  "s_mov_b32 s55, 0x3030303\n" \
  "s_mov_b32 s56, 0x55555555\n" \
  "s_mov_b32 s38, 0x0\n" \
  "s_mov_b32 s98, 0x0\n" \
  "s_mov_b32 s99, 0x8\n" \
  "s_mov_b32 s97, 0x2000\n" \
  "v_xor_b32 v245, 64, v244\n" \
  "v_add_u32 v245, 0x400, v245\n" \
  "s_setprio 0\n" \
  "L5_:\n" \
  "ds_read_b32 v192, v190 offset:16\n" \
  "s_waitcnt lgkmcnt(0)\n" \
  "v_readfirstlane_b32 s35, v192\n" \
  "s_cmp_lt_u32 s35, 4\n" \
  "s_cbranch_scc1 L5_\n" \
  "s_add_u32 s35, s4, 0\n" \
  "s_min_u32 s35, s35, 984\n" \
  "s_lshl_b32 s35, s35, 8\n" \
  "s_add_u32 m0, s5, 0\n" \
  "s_add_u32 s58, s94, s35\n" \
  "s_addc_u32 s59, s95, 0\n" \
  "global_load_lds_dwordx4 v244, s[58:59]\n" \
  "global_load_lds_dwordx4 v245, s[58:59] offset:1024\n" \
  "s_add_u32 s35, s4, 16\n" \
  "s_min_u32 s35, s35, 984\n" \
  "s_lshl_b32 s35, s35, 8\n" \
  "s_add_u32 m0, s5, 2048\n" \
  "s_add_u32 s58, s94, s35\n" \
  "s_addc_u32 s59, s95, 0\n" \
  "global_load_lds_dwordx4 v244, s[58:59]\n" \
  "global_load_lds_dwordx4 v245, s[58:59] offset:1024\n" \
  "L6_:\n" \
  "ds_read_b32 v192, v190 offset:12\n" \
  "s_waitcnt lgkmcnt(0)\n" \
  "v_readfirstlane_b32 s35, v192\n" \
  "s_cmp_lt_u32 s35, 4\n" \
  "s_cbranch_scc1 L6_\n" \
  "s_waitcnt vmcnt(0)\n" \
  "ds_read_b128 v[8:11], v246 offset:0\n" \
  "ds_read_b128 v[12:15], v242 offset:0\n" \
  "ds_read_b128 v[24:27], v246 offset:2048\n" \
  "ds_read_b128 v[28:31], v242 offset:2048\n" \
  "ds_read_b128 v[0:3], v249 offset:0\n" \
  "ds_read_b128 v[4:7], v249 offset:64\n" \
  "ds_read_b128 v[16:19], v249 offset:2080\n" \
  "ds_read_b128 v[20:23], v249 offset:2144\n" \
  "s_waitcnt lgkmcnt(4)\n" \
  "s_add_u32 s35, s4, 32\n" \
  "s_min_u32 s35, s35, 984\n" \
  "s_lshl_b32 s35, s35, 8\n" \
  "s_add_u32 m0, s5, 0\n" \
  "s_add_u32 s58, s94, s35\n" \
  "s_addc_u32 s59, s95, 0\n" \
  "global_load_lds_dwordx4 v244, s[58:59]\n" \
  "global_load_lds_dwordx4 v245, s[58:59] offset:1024\n" \
  "s_add_u32 s35, s4, 48\n" \
  "s_min_u32 s35, s35, 984\n" \
  "s_lshl_b32 s35, s35, 8\n" \
  "s_add_u32 m0, s5, 2048\n" \
  "s_add_u32 s58, s94, s35\n" \
  "s_addc_u32 s59, s95, 0\n" \
  "global_load_lds_dwordx4 v244, s[58:59]\n" \
  "global_load_lds_dwordx4 v245, s[58:59] offset:1024\n" \
  "s_waitcnt vmcnt(0)\n" \
  "ds_read_b128 v[40:43], v246 offset:0\n" \
  "ds_read_b128 v[44:47], v242 offset:0\n" \
  "ds_read_b128 v[56:59], v246 offset:2048\n" \
  "ds_read_b128 v[60:63], v242 offset:2048\n" \
  "ds_read_b128 v[32:35], v249 offset:4160\n" \
  "ds_read_b128 v[36:39], v249 offset:4224\n" \
  "ds_read_b128 v[48:51], v249 offset:6240\n" \
  "ds_read_b128 v[52:55], v249 offset:6304\n" \
  "s_waitcnt lgkmcnt(4)\n" \
  "s_add_u32 s40, s92, s6\n" \
  "s_addc_u32 s41, s93, 0\n" \
  "s_add_u32 s6, s6, 2097152\n" \
  "s_mov_b32 m0, s96\n" \
  "s_nop 0\n" \
  "global_load_lds_dwordx4 v189, s[40:41]\n" \
  "global_load_lds_dwordx4 v194, s[40:41] offset:1024\n" \
  "global_load_dwordx2 v[196:197], v227, s[42:43] offset:-3200\n" \
  "global_load_dwordx2 v[198:199], v227, s[42:43] offset:-2800\n" \
  "global_load_dwordx2 v[200:201], v227, s[42:43] offset:-2400\n" \
  "global_load_dwordx2 v[202:203], v227, s[42:43] offset:-1600\n" \
  "global_load_dwordx2 v[204:205], v227, s[42:43] offset:-1200\n" \
  "global_load_dwordx2 v[206:207], v227, s[42:43] offset:-800\n" \
  "global_load_dwordx2 v[208:209], v227, s[42:43] offset:-400\n" \
  "global_load_dwordx2 v[210:211], v227, s[42:43] offset:0\n" \
  "global_load_dwordx2 v[212:213], v227, s[42:43] offset:400\n" \
  "global_load_dwordx2 v[214:215], v227, s[42:43] offset:800\n" \
  "global_load_dwordx2 v[216:217], v227, s[42:43] offset:1200\n" \
  "global_load_dwordx2 v[218:219], v227, s[42:43] offset:-2000\n" \
  "global_load_dwordx2 v[220:221], v227, s[42:43] offset:1600\n" \
  "global_load_dwordx2 v[222:223], v227, s[42:43] offset:2000\n" \
  "global_load_dwordx2 v[224:225], v227, s[42:43] offset:2400\n" \
  "global_load_dwordx2 v[226:227], v227, s[42:43] offset:2800\n" \
  "s_add_u32 s35, s4, 64\n" \
  "s_min_u32 s35, s35, 984\n" \
  "s_lshl_b32 s35, s35, 8\n" \
  "s_add_u32 m0, s5, 0\n" \
  "s_add_u32 s58, s94, s35\n" \
  "s_addc_u32 s59, s95, 0\n" \
  "global_load_lds_dwordx4 v244, s[58:59]\n" \
  "global_load_lds_dwordx4 v245, s[58:59] offset:1024\n" \
  "s_add_u32 s35, s4, 80\n" \
  "s_min_u32 s35, s35, 984\n" \
  "s_lshl_b32 s35, s35, 8\n" \
  "s_add_u32 m0, s5, 2048\n" \
  "s_add_u32 s58, s94, s35\n" \
  "s_addc_u32 s59, s95, 0\n" \
  "global_load_lds_dwordx4 v244, s[58:59]\n" \
  "global_load_lds_dwordx4 v245, s[58:59] offset:1024\n" \
  "s_waitcnt vmcnt(0)\n" \
  "ds_read_b128 v[72:75], v246 offset:0\n" \
  "ds_read_b128 v[76:79], v242 offset:0\n" \
  "ds_read_b128 v[88:91], v246 offset:2048\n" \
  "ds_read_b128 v[92:95], v242 offset:2048\n" \
  "ds_read_b128 v[64:67], v249 offset:8320\n" \
  "ds_read_b128 v[68:71], v249 offset:8384\n" \
  "ds_read_b128 v[80:83], v249 offset:10400\n" \
  "ds_read_b128 v[84:87], v249 offset:10464\n" \
  "s_waitcnt lgkmcnt(4)\n" \
  "s_add_u32 s35, s4, 96\n" \
  "s_min_u32 s35, s35, 984\n" \
  "s_lshl_b32 s35, s35, 8\n" \
  "s_add_u32 m0, s5, 0\n" \
  "s_add_u32 s58, s94, s35\n" \
  "s_addc_u32 s59, s95, 0\n" \
  "global_load_lds_dwordx4 v244, s[58:59]\n" \
  "global_load_lds_dwordx4 v245, s[58:59] offset:1024\n" \
  "s_add_u32 s35, s4, 112\n" \
  "s_min_u32 s35, s35, 984\n" \
  "s_lshl_b32 s35, s35, 8\n" \
  "s_add_u32 m0, s5, 2048\n" \
  "s_add_u32 s58, s94, s35\n" \
  "s_addc_u32 s59, s95, 0\n" \
  "global_load_lds_dwordx4 v244, s[58:59]\n" \
  "global_load_lds_dwordx4 v245, s[58:59] offset:1024\n" \
  "s_lshl2_add_u32 s35, s26, s28\n" \
  "s_mul_i32 s35, s35, 6400\n" \
  "s_add_u32 s35, s35, 0\n" \
  "s_add_u32 s58, s14, s35\n" \
  "s_addc_u32 s59, s15, 0\n" \
  "s_mov_b32 exec_lo, 0xffffffff\n" \
  "s_mov_b32 exec_hi, 0x3ffff\n" \
  "global_load_dword v248, v247, s[58:59]\n" \
  "s_mov_b64 exec, -1\n" \
  "s_lshl2_add_u32 s35, s26, s28\n" \
  "s_mul_i32 s35, s35, 6400\n" \
  "s_add_u32 s35, s35, 6553600\n" \
  "s_add_u32 s58, s14, s35\n" \
  "s_addc_u32 s59, s15, 0\n" \
  "s_mov_b32 exec_lo, 0xffffffff\n" \
  "s_mov_b32 exec_hi, 0x3ffff\n" \
  "global_load_dword v248, v247, s[58:59]\n" \
  "s_mov_b64 exec, -1\n" \
  "s_getpc_b64 s[58:59]\n" \
  "LB_pc:\n" \
  "s_sub_u32 s58, s58, LB_pc-LZ_first\n" \
  "s_subb_u32 s59, s59, 0\n" \
  "s_mul_i32 s35, s28, 7168\n" \
  "v_lshl_add_u32 v195, v255, 7, s35\n" \
  "v_min_u32 v195, LZ_end-LZ_first, v195\n" \
  "s_mov_b32 exec_hi, 0xffffff\n" \
  "global_load_dword v248, v195, s[58:59]\n" \
  "s_mov_b64 exec, -1\n" \
  "s_waitcnt vmcnt(3)\n" \
  "ds_read_b128 v[104:107], v246 offset:0\n" \
  "ds_read_b128 v[108:111], v242 offset:0\n" \
  "ds_read_b128 v[120:123], v246 offset:2048\n" \
  "ds_read_b128 v[124:127], v242 offset:2048\n" \
  "ds_read_b128 v[96:99], v250 offset:0\n" \
  "ds_read_b128 v[100:103], v250 offset:64\n" \
  "ds_read_b128 v[112:115], v251 offset:0\n" \
  "ds_read_b128 v[116:119], v251 offset:64\n" \
  "s_waitcnt lgkmcnt(4)\n" \
  "s_waitcnt vmcnt(0)\n" \
  "s_sleep 8\n" \
  "s_setprio 2\n" \
  "s_mul_i32 s36, s28, 2176\n" \
  "s_add_u32 s36, s36, 154624\n" \
  "v_bfe_u32 v228, v255, 3, 1\n" \
  "v_and_b32 v229, 1, v255\n" \
  "v_lshl_add_u32 v230, v228, 1, v229\n" \
  "v_lshl_add_u32 v231, v229, 1, v228\n" \
  "v_xor_b32 v229, 1, v230\n" \
  "s_mov_b32 vcc_lo, 0xcccccccc\n" \
  "s_mov_b32 vcc_hi, vcc_lo\n" \
  "v_cndmask_b32 v228, v230, v231, vcc\n" \
  "v_cndmask_b32 v229, v229, v231, vcc\n" \
  "v_and_b32 v230, 4, v255\n" \
  "v_mul_u32_u24 v230, 272, v230\n" \
  "v_lshl_add_u32 v230, v253, 2, v230\n" \
  "v_and_or_b32 v230, v255, 2, v230\n" \
  "v_add_u32 v230, s36, v230\n" \
  "s_movk_i32 s35, 272\n" \
  "v_mad_u32_u24 v195, v228, s35, v230\n" \
  "v_mad_u32_u24 v235, v229, s35, v230\n" \
  "v_lshl_add_u32 v230, v253, 4, v238\n" \
  "v_add_u32 v254, s36, v230\n" \
  "v_readfirstlane_b32 s39, v193\n" \
  "s_lshl_b32 s35, s26, 2\n" \
  "s_add_u32 s39, s39, 1\n" \
  "v_mov_b32 v193, s35\n" \
  "v_mov_b64 v[240:241], -1\n" \
  "v_mov_b64 v[242:243], -1\n" \
  "s_waitcnt lgkmcnt(0)\n" \
  "s_mov_b64 exec, 1\n" \
  "ds_add_u32 v190, v191 offset:0\n" \
  "s_mov_b64 exec, -1\n" \
  "L7_:\n" \
  "ds_read_b32 v192, v190 offset:0\n" \
  "s_waitcnt lgkmcnt(0)\n" \
  "v_readfirstlane_b32 s35, v192\n" \
  "s_cmp_lt_u32 s35, 4\n" \
  "s_cbranch_scc1 L7_\n" \
  "s_add_u32 s96, s96, 8192\n" \
  "s_add_u32 s40, s92, s6\n" \
  "s_addc_u32 s41, s93, 0\n" \
  "s_add_u32 s6, s6, 2097152\n" \
  "s_mov_b32 m0, s96\n" \
  "s_nop 0\n" \
  "global_load_lds_dwordx4 v189, s[40:41]\n" \
  "global_load_lds_dwordx4 v194, s[40:41] offset:1024\n" \
  "s_sub_u32 s96, s96, 8192\n" \
  "ds_read_b64_tr_b16 v[128:129], v184 offset:0\n" \
  "ds_read_b64_tr_b16 v[130:131], v184 offset:256\n" \
  "ds_read_b64_tr_b16 v[132:133], v184 offset:2048\n" \
  "ds_read_b64_tr_b16 v[134:135], v184 offset:2304\n" \
  "ds_read_b64_tr_b16 v[136:137], v184 offset:4096\n" \
  "ds_read_b64_tr_b16 v[138:139], v184 offset:4352\n" \
  "ds_read_b64_tr_b16 v[140:141], v184 offset:6144\n" \
  "ds_read_b64_tr_b16 v[142:143], v184 offset:6400\n" \
  "ds_read_b64_tr_b16 v[156:157], v185 offset:6144\n" \
  "ds_read_b64_tr_b16 v[158:159], v185 offset:6400\n" \
  "ds_read_b64_tr_b16 v[152:153], v185 offset:4096\n" \
  "ds_read_b64_tr_b16 v[154:155], v185 offset:4352\n" \
  "ds_read_b64_tr_b16 v[148:149], v185 offset:2048\n" \
  "ds_read_b64_tr_b16 v[150:151], v185 offset:2304\n" \
  "ds_read_b64_tr_b16 v[144:145], v185 offset:0\n" \
  "ds_read_b64_tr_b16 v[146:147], v185 offset:256\n" \
  "v_mov_b32 v192, s39\n" \
  "s_cmp_lg_u32 s28, 0\n" \
  "s_cbranch_scc1 LB_nc\n" \
  "s_mov_b64 exec, 1\n" \
  "global_store_dword v193, v192, s[48:49] offset:0 sc1\n" \
  "s_mov_b64 exec, -1\n" \
  "LB_nc:\n" \
  "s_lshl2_add_u32 s35, s7, s28\n" \
  "s_lshl_b32 s35, s35, 7\n" \
  "s_lshl_b32 s36, s31, 16\n" \
  "s_add_u32 s35, s35, s36\n" \
  "s_add_u32 s35, s35, 109871104\n" \
  "s_add_u32 s58, s24, s35\n" \
  "s_addc_u32 s59, s25, 0\n" \
  "s_mov_b64 exec, 0xff\n" \
  "global_store_dwordx4 v238, v[240:243], s[58:59] sc1\n" \
  "s_mov_b64 exec, -1\n" \
  "s_cmp_lg_u32 s33, 1021\n" \
  "s_cbranch_scc1 LB_nm\n" \
  "v_mov_b64 v[240:241], 0\n" \
  "v_mov_b64 v[242:243], 0\n" \
  "global_store_dwordx4 v238, v[240:243], s[48:49] offset:2048 sc1\n" \
  "s_nop 1\n" \
  "v_lshlrev_b32 v242, 8, v255\n" \
  "global_store_dwordx2 v242, v[240:241], s[48:49] offset:3072 sc1\n" \
  "v_add_u32 v242, 0x4000, v242\n" \
  "global_store_dwordx2 v242, v[240:241], s[48:49] offset:3072 sc1\n" \
  "v_add_u32 v242, 0x4000, v242\n" \
  "global_store_dwordx2 v242, v[240:241], s[48:49] offset:3072 sc1\n" \
  "v_add_u32 v242, 0x4000, v242\n" \
  "global_store_dwordx2 v242, v[240:241], s[48:49] offset:3072 sc1\n" \
  "v_add_u32 v242, 0x4000, v242\n" \
  "LB_nm:\n" \
  "s_mov_b32 s86, 0x78000b0\n" \
  "s_mov_b32 s87, 0x0\n" \
  "s_or_b32 s86, s86, s87\n" \
  "s_setprio 2\n" \
  "s_memtime s[90:91]\n" \
  "s_memrealtime s[42:43]\n" \
  "LT:\n" \
  "s_cmp_eq_u32 s38, 6\n" \
  "s_cselect_b32 s78, 0x33333333, -1\n" \
  "s_cmp_eq_u32 s38, 7\n" \
  "s_cselect_b32 s78, 0x33333333, s78\n" \
  "s_cmp_eq_u32 s38, 18\n" \
  "s_cselect_b32 s80, 0x33333333, -1\n" \
  "s_cmp_eq_u32 s38, 19\n" \
  "s_cselect_b32 s80, 0x33333333, s80\n" \
  "s_mov_b32 s79, s78\n" \
  "s_mov_b32 s81, s80\n" \
  "s_mov_b32 s37, s38\n" \
  "s_add_u32 s57, s38, 2\n" \
  "s_add_u32 s3, s38, 4\n" \
  "s_add_u32 s38, s38, 6\n" \
  "s_cmp_lt_u32 s57, 32\n" \
  "s_cselect_b32 s57, s57, 37\n" \
  "s_cselect_b32 s3, s3, 37\n" \
  "s_waitcnt lgkmcnt(12)\n" \
  "v_mfma_f32_16x16x32_bf16 v[160:163], v[128:131], v[0:3], 0\n" \
  "s_mov_b64 vcc, s[78:79]\n" \
  "v_cndmask_b32_dpp v234, v234, v234, vcc row_ror:8 row_mask:0xf bank_mask:0xf\n" \
  "v_mfma_f32_16x16x32_bf16 v[160:163], v[132:135], v[4:7], v[160:163]\n" \
  "s_waitcnt lgkmcnt(8)\n" \
  "v_mfma_f32_16x16x32_bf16 v[160:163], v[136:139], v[8:11], v[160:163]\n" \
  "s_set_gpr_idx_on s37, gpr_idx(SRC0)\n" \
  "v_mad_u32_u24 v232, v196, s52, v236\n" \
  "s_set_gpr_idx_off\n" \
  "v_mfma_f32_16x16x32_bf16 v[160:163], v[140:143], v[12:15], v[160:163]\n" \
  "s_waitcnt lgkmcnt(0)\n" \
  "v_lshrrev_b32 v228, v232, v234\n" \
  "v_lshrrev_b32_dpp v229, v232, v234 quad_perm:[2,3,0,1] row_mask:0xf bank_mask:0xf\n" \
  "v_mfma_f32_16x16x32_bf16 v[164:167], v[156:159], v[12:15], 0\n" \
  "v_lshrrev_b32_dpp v230, v232, v234 quad_perm:[1,2,3,0] row_mask:0xf bank_mask:0xf\n" \
  "v_lshrrev_b32_dpp v231, v232, v234 quad_perm:[3,0,1,2] row_mask:0xf bank_mask:0xf\n" \
  "v_mfma_f32_16x16x32_bf16 v[164:167], v[152:155], v[8:11], v[164:167]\n" \
  "v_perm_b32 v228, v229, v228, s53\n" \
  "v_perm_b32 v230, v231, v230, s54\n" \
  "v_mfma_f32_16x16x32_bf16 v[164:167], v[148:151], v[4:7], v[164:167]\n" \
  "v_bitop3_b32 v228, v228, v230, s55 bitop3:0xa8\n" \
  "v_lshl_or_b32 v229, v234, 1, s56\n" \
  "v_mfma_f32_16x16x32_bf16 v[164:167], v[144:147], v[0:3], v[164:167]\n" \
  "v_add_u32_dpp v228, v228, v228 quad_perm:[2,3,0,1] row_mask:0xf bank_mask:0xf\n" \
  "v_min_u32_sdwa v230, v228, v228 dst_sel:DWORD dst_unused:UNUSED_PAD src0_sel:BYTE_0 src1_sel:BYTE_1\n" \
  "v_mfma_f32_16x16x32_bf16 v[172:175], v[144:147], v[16:19], 0\n" \
  "v_min_u32_sdwa v231, v228, v228 dst_sel:DWORD dst_unused:UNUSED_PAD src0_sel:BYTE_2 src1_sel:BYTE_3\n" \
  "v_sub_u32 v230, v230, v231\n" \
  "v_mfma_f32_16x16x32_bf16 v[172:175], v[148:151], v[20:23], v[172:175]\n" \
  "v_cvt_pk_bf16_f32 v176, v160, v161\n" \
  "v_add_u32_dpp v230, v230, v230 quad_perm:[1,0,3,2] row_mask:0xf bank_mask:0xf\n" \
  "v_mfma_f32_16x16x32_bf16 v[172:175], v[152:155], v[24:27], v[172:175]\n" \
  "v_cmp_ge_i32 vcc, 0, v230\n" \
  "v_cvt_pk_bf16_f32 v177, v162, v163\n" \
  "v_mfma_f32_16x16x32_bf16 v[172:175], v[156:159], v[28:31], v[172:175]\n" \
  "v_cndmask_b32_sdwa v228, v228, v228, vcc dst_sel:DWORD dst_unused:UNUSED_PAD src0_sel:WORD_1 src1_sel:WORD_0\n" \
  "v_cvt_pk_bf16_f32 v178, v164, v165\n" \
  "v_mfma_f32_16x16x32_bf16 v[168:171], v[140:143], v[28:31], 0\n" \
  "s_mov_b64 s[50:51], vcc\n" \
  "v_cmp_le_u32_sdwa vcc, v228, v228 src0_sel:BYTE_0 src1_sel:BYTE_1\n" \
  "v_cvt_pk_bf16_f32 v179, v166, v167\n" \
  "v_mfma_f32_16x16x32_bf16 v[168:171], v[136:139], v[24:27], v[168:171]\n" \
  "v_cndmask_b32_dpp v232, v232, v232, vcc quad_perm:[2,3,0,1] row_mask:0xf bank_mask:0xf\n" \
  "v_mfma_f32_16x16x32_bf16 v[168:171], v[132:135], v[20:23], v[168:171]\n" \
  "s_mov_b64 vcc, s[50:51]\n" \
  "v_cndmask_b32_dpp v232, v232, v232, vcc quad_perm:[3,0,1,2] row_mask:0xf bank_mask:0xf\n" \
  "v_lshlrev_b32_e64 v230, v232, 3\n" \
  "v_mfma_f32_16x16x32_bf16 v[168:171], v[128:131], v[16:19], v[168:171]\n" \
  "v_and_or_b32 v234, v229, v230, v234\n" \
  "s_cmp_lg_u32 s33, 1021\n" \
  "s_cbranch_scc0 LT_e\n" \
  "LT_r:\n" \
  "s_waitcnt vmcnt(5)\n" \
  "global_store_dwordx4 v186, v[176:179], s[44:45] offset:0 sc1\n" \
  "v_mfma_f32_16x16x32_bf16 v[160:163], v[128:131], v[32:35], 0\n" \
  "s_set_gpr_idx_on s37, gpr_idx(DST)\n" \
  "v_mov_b32 v196, v232\n" \
  "s_set_gpr_idx_off\n" \
  "v_mfma_f32_16x16x32_bf16 v[160:163], v[132:135], v[36:39], v[160:163]\n" \
  "s_set_gpr_idx_on s57, gpr_idx(SRC0)\n" \
  "v_mad_u32_u24 v232, v196, s52, v236\n" \
  "s_set_gpr_idx_off\n" \
  "v_mfma_f32_16x16x32_bf16 v[160:163], v[136:139], v[40:43], v[160:163]\n" \
  "v_lshrrev_b32 v228, v232, v234\n" \
  "v_lshrrev_b32_dpp v229, v232, v234 quad_perm:[2,3,0,1] row_mask:0xf bank_mask:0xf\n" \
  "v_mfma_f32_16x16x32_bf16 v[160:163], v[140:143], v[44:47], v[160:163]\n" \
  "v_lshrrev_b32_dpp v230, v232, v234 quad_perm:[1,2,3,0] row_mask:0xf bank_mask:0xf\n" \
  "v_lshrrev_b32_dpp v231, v232, v234 quad_perm:[3,0,1,2] row_mask:0xf bank_mask:0xf\n" \
  "v_mfma_f32_16x16x32_bf16 v[164:167], v[156:159], v[44:47], 0\n" \
  "v_perm_b32 v228, v229, v228, s53\n" \
  "v_perm_b32 v230, v231, v230, s54\n" \
  "v_mfma_f32_16x16x32_bf16 v[164:167], v[152:155], v[40:43], v[164:167]\n" \
  "v_bitop3_b32 v228, v228, v230, s55 bitop3:0xa8\n" \
  "v_lshl_or_b32 v229, v234, 1, s56\n" \
  "v_mfma_f32_16x16x32_bf16 v[164:167], v[148:151], v[36:39], v[164:167]\n" \
  "v_cvt_pk_bf16_f32 v182, v172, v173\n" \
  "v_cvt_pk_bf16_f32 v183, v174, v175\n" \
  "v_mfma_f32_16x16x32_bf16 v[164:167], v[144:147], v[32:35], v[164:167]\n" \
  "v_cvt_pk_bf16_f32 v180, v168, v169\n" \
  "v_cvt_pk_bf16_f32 v181, v170, v171\n" \
  "s_waitcnt vmcnt(5)\n" \
  "global_store_dwordx4 v186, v[180:183], s[44:45] offset:256 sc1\n" \
  "v_mfma_f32_16x16x32_bf16 v[172:175], v[144:147], v[48:51], 0\n" \
  "v_add_u32_dpp v228, v228, v228 quad_perm:[2,3,0,1] row_mask:0xf bank_mask:0xf\n" \
  "v_min_u32_sdwa v230, v228, v228 dst_sel:DWORD dst_unused:UNUSED_PAD src0_sel:BYTE_0 src1_sel:BYTE_1\n" \
  "v_mfma_f32_16x16x32_bf16 v[172:175], v[148:151], v[52:55], v[172:175]\n" \
  "v_min_u32_sdwa v231, v228, v228 dst_sel:DWORD dst_unused:UNUSED_PAD src0_sel:BYTE_2 src1_sel:BYTE_3\n" \
  "v_sub_u32 v230, v230, v231\n" \
  "v_mfma_f32_16x16x32_bf16 v[172:175], v[152:155], v[56:59], v[172:175]\n" \
  "v_cvt_pk_bf16_f32 v176, v160, v161\n" \
  "v_add_u32_dpp v230, v230, v230 quad_perm:[1,0,3,2] row_mask:0xf bank_mask:0xf\n" \
  "v_mfma_f32_16x16x32_bf16 v[172:175], v[156:159], v[60:63], v[172:175]\n" \
  "v_cmp_ge_i32 vcc, 0, v230\n" \
  "v_cvt_pk_bf16_f32 v177, v162, v163\n" \
  "v_mfma_f32_16x16x32_bf16 v[168:171], v[140:143], v[60:63], 0\n" \
  "v_cndmask_b32_sdwa v228, v228, v228, vcc dst_sel:DWORD dst_unused:UNUSED_PAD src0_sel:WORD_1 src1_sel:WORD_0\n" \
  "v_cvt_pk_bf16_f32 v178, v164, v165\n" \
  "v_mfma_f32_16x16x32_bf16 v[168:171], v[136:139], v[56:59], v[168:171]\n" \
  "s_mov_b64 s[50:51], vcc\n" \
  "v_cmp_le_u32_sdwa vcc, v228, v228 src0_sel:BYTE_0 src1_sel:BYTE_1\n" \
  "v_cvt_pk_bf16_f32 v179, v166, v167\n" \
  "v_mfma_f32_16x16x32_bf16 v[168:171], v[132:135], v[52:55], v[168:171]\n" \
  "v_cndmask_b32_dpp v232, v232, v232, vcc quad_perm:[2,3,0,1] row_mask:0xf bank_mask:0xf\n" \
  "v_mfma_f32_16x16x32_bf16 v[168:171], v[128:131], v[48:51], v[168:171]\n" \
  "s_mov_b64 vcc, s[50:51]\n" \
  "v_cndmask_b32_dpp v232, v232, v232, vcc quad_perm:[3,0,1,2] row_mask:0xf bank_mask:0xf\n" \
  "v_lshlrev_b32_e64 v230, v232, 3\n" \
  "s_waitcnt vmcnt(5)\n" \
  "global_store_dwordx4 v186, v[176:179], s[44:45] offset:512 sc1\n" \
  "v_mfma_f32_16x16x32_bf16 v[160:163], v[128:131], v[64:67], 0\n" \
  "v_and_or_b32 v234, v229, v230, v234\n" \
  "v_cvt_pk_bf16_f32 v182, v172, v173\n" \
  "v_mfma_f32_16x16x32_bf16 v[160:163], v[132:135], v[68:71], v[160:163]\n" \
  "s_set_gpr_idx_on s57, gpr_idx(DST)\n" \
  "v_mov_b32 v196, v232\n" \
  "s_set_gpr_idx_off\n" \
  "v_mfma_f32_16x16x32_bf16 v[160:163], v[136:139], v[72:75], v[160:163]\n" \
  "s_mov_b64 vcc, s[80:81]\n" \
  "v_cndmask_b32_dpp v234, v234, v234, vcc row_ror:8 row_mask:0xf bank_mask:0xf\n" \
  "v_cvt_pk_bf16_f32 v183, v174, v175\n" \
  "v_mfma_f32_16x16x32_bf16 v[160:163], v[140:143], v[76:79], v[160:163]\n" \
  "s_set_gpr_idx_on s3, gpr_idx(SRC0)\n" \
  "v_mad_u32_u24 v232, v196, s52, v236\n" \
  "s_set_gpr_idx_off\n" \
  "v_mfma_f32_16x16x32_bf16 v[164:167], v[156:159], v[76:79], 0\n" \
  "v_lshrrev_b32 v228, v232, v234\n" \
  "v_lshrrev_b32_dpp v229, v232, v234 quad_perm:[2,3,0,1] row_mask:0xf bank_mask:0xf\n" \
  "v_mfma_f32_16x16x32_bf16 v[164:167], v[152:155], v[72:75], v[164:167]\n" \
  "v_lshrrev_b32_dpp v230, v232, v234 quad_perm:[1,2,3,0] row_mask:0xf bank_mask:0xf\n" \
  "v_lshrrev_b32_dpp v231, v232, v234 quad_perm:[3,0,1,2] row_mask:0xf bank_mask:0xf\n" \
  "v_mfma_f32_16x16x32_bf16 v[164:167], v[148:151], v[68:71], v[164:167]\n" \
  "v_perm_b32 v228, v229, v228, s53\n" \
  "v_perm_b32 v230, v231, v230, s54\n" \
  "v_mfma_f32_16x16x32_bf16 v[164:167], v[144:147], v[64:67], v[164:167]\n" \
  "v_cvt_pk_bf16_f32 v180, v168, v169\n" \
  "v_cvt_pk_bf16_f32 v181, v170, v171\n" \
  "s_waitcnt vmcnt(5)\n" \
  "global_store_dwordx4 v186, v[180:183], s[44:45] offset:768 sc1\n" \
  "v_mfma_f32_16x16x32_bf16 v[172:175], v[144:147], v[80:83], 0\n" \
  "s_waitcnt vmcnt(5)\n" \
  "s_mov_b64 exec, 1\n" \
  "ds_add_u32 v190, v191 offset:0\n" \
  "s_mov_b64 exec, -1\n" \
  "v_mfma_f32_16x16x32_bf16 v[172:175], v[148:151], v[84:87], v[172:175]\n" \
  "v_bitop3_b32 v228, v228, v230, s55 bitop3:0xa8\n" \
  "v_lshl_or_b32 v229, v234, 1, s56\n" \
  "v_mfma_f32_16x16x32_bf16 v[172:175], v[152:155], v[88:91], v[172:175]\n" \
  "v_add_u32_dpp v228, v228, v228 quad_perm:[2,3,0,1] row_mask:0xf bank_mask:0xf\n" \
  "v_min_u32_sdwa v230, v228, v228 dst_sel:DWORD dst_unused:UNUSED_PAD src0_sel:BYTE_0 src1_sel:BYTE_1\n" \
  "v_mfma_f32_16x16x32_bf16 v[172:175], v[156:159], v[92:95], v[172:175]\n" \
  "v_min_u32_sdwa v231, v228, v228 dst_sel:DWORD dst_unused:UNUSED_PAD src0_sel:BYTE_2 src1_sel:BYTE_3\n" \
  "v_sub_u32 v230, v230, v231\n" \
  "v_mfma_f32_16x16x32_bf16 v[168:171], v[140:143], v[92:95], 0\n" \
  "v_cvt_pk_bf16_f32 v176, v160, v161\n" \
  "v_add_u32_dpp v230, v230, v230 quad_perm:[1,0,3,2] row_mask:0xf bank_mask:0xf\n" \
  "v_mfma_f32_16x16x32_bf16 v[168:171], v[136:139], v[88:91], v[168:171]\n" \
  "v_cmp_ge_i32 vcc, 0, v230\n" \
  "v_cvt_pk_bf16_f32 v177, v162, v163\n" \
  "v_mfma_f32_16x16x32_bf16 v[168:171], v[132:135], v[84:87], v[168:171]\n" \
  "v_cndmask_b32_sdwa v228, v228, v228, vcc dst_sel:DWORD dst_unused:UNUSED_PAD src0_sel:WORD_1 src1_sel:WORD_0\n" \
  "v_cvt_pk_bf16_f32 v178, v164, v165\n" \
  "v_mfma_f32_16x16x32_bf16 v[168:171], v[128:131], v[80:83], v[168:171]\n" \
  "s_mov_b64 s[50:51], vcc\n" \
  "v_cmp_le_u32_sdwa vcc, v228, v228 src0_sel:BYTE_0 src1_sel:BYTE_1\n" \
  "v_cvt_pk_bf16_f32 v179, v166, v167\n" \
  "s_cmp_eq_u32 s98, 4\n" \
  "s_cselect_b32 s36, 12451840, 0\n" \
  "s_sub_u32 s6, s6, s36\n" \
  "s_cmp_lt_u32 s98, 10\n" \
  "s_cselect_b32 s6, s6, s34\n" \
  "s_add_u32 s40, s92, s6\n" \
  "s_addc_u32 s41, s93, 0\n" \
  "s_add_u32 s6, s6, 2097152\n" \
  "s_waitcnt vmcnt(5)\n" \
  "global_store_dwordx4 v186, v[176:179], s[44:45] offset:1024 sc1\n" \
  "v_mfma_f32_16x16x32_bf16 v[160:163], v[128:131], v[96:99], 0\n" \
  "ds_read_b32 v192, v190\n" \
  "v_add_u32 v184, s97, v184\n" \
  "v_mfma_f32_16x16x32_bf16 v[160:163], v[132:135], v[100:103], v[160:163]\n" \
  "v_add_u32 v185, s97, v185\n" \
  "v_cndmask_b32_dpp v232, v232, v232, vcc quad_perm:[2,3,0,1] row_mask:0xf bank_mask:0xf\n" \
  "v_mfma_f32_16x16x32_bf16 v[160:163], v[136:139], v[104:107], v[160:163]\n" \
  "s_mov_b64 vcc, s[50:51]\n" \
  "v_cndmask_b32_dpp v232, v232, v232, vcc quad_perm:[3,0,1,2] row_mask:0xf bank_mask:0xf\n" \
  "v_lshlrev_b32_e64 v230, v232, 3\n" \
  "v_mfma_f32_16x16x32_bf16 v[160:163], v[140:143], v[108:111], v[160:163]\n" \
  "v_and_or_b32 v234, v229, v230, v234\n" \
  "v_cvt_pk_bf16_f32 v182, v172, v173\n" \
  "v_mfma_f32_16x16x32_bf16 v[164:167], v[156:159], v[108:111], 0\n" \
  "s_set_gpr_idx_on s3, gpr_idx(DST)\n" \
  "v_mov_b32 v196, v232\n" \
  "s_set_gpr_idx_off\n" \
  "v_mfma_f32_16x16x32_bf16 v[164:167], v[152:155], v[104:107], v[164:167]\n" \
  "s_waitcnt lgkmcnt(0)\n" \
  "v_readfirstlane_b32 s35, v192\n" \
  "v_cvt_pk_bf16_f32 v183, v174, v175\n" \
  "v_mfma_f32_16x16x32_bf16 v[164:167], v[148:151], v[100:103], v[164:167]\n" \
  "v_cvt_pk_bf16_f32 v180, v168, v169\n" \
  "v_cvt_pk_bf16_f32 v181, v170, v171\n" \
  "v_mfma_f32_16x16x32_bf16 v[164:167], v[144:147], v[96:99], v[164:167]\n" \
  "s_cmp_lt_u32 s35, s99\n" \
  "s_cbranch_scc1 LT_w\n" \
  "LT_c:\n" \
  "s_mov_b32 m0, s96\n" \
  "s_nop 0\n" \
  "global_load_lds_dwordx4 v189, s[40:41]\n" \
  "global_load_lds_dwordx4 v194, s[40:41] offset:1024\n" \
  "s_add_u32 s96, s96, s97\n" \
  "s_sub_u32 s97, 0, s97\n" \
  "s_add_u32 s99, s99, 4\n" \
  "s_lshl4_add_u32 s35, s28, s98\n" \
  "s_bitcmp1_b64 s[86:87], s35\n" \
  "s_cbranch_scc1 LT_a\n" \
  "LT_s:\n" \
  "s_waitcnt vmcnt(5)\n" \
  "global_store_dwordx4 v186, v[180:183], s[44:45] offset:1280 sc1\n" \
  "v_mfma_f32_16x16x32_bf16 v[168:171], v[128:131], v[112:115], 0\n" \
  "ds_read_b64_tr_b16 v[128:129], v184 offset:0\n" \
  "ds_read_b64_tr_b16 v[130:131], v184 offset:256\n" \
  "v_mfma_f32_16x16x32_bf16 v[168:171], v[132:135], v[116:119], v[168:171]\n" \
  "ds_read_b64_tr_b16 v[132:133], v184 offset:2048\n" \
  "ds_read_b64_tr_b16 v[134:135], v184 offset:2304\n" \
  "v_mfma_f32_16x16x32_bf16 v[168:171], v[136:139], v[120:123], v[168:171]\n" \
  "ds_read_b64_tr_b16 v[136:137], v184 offset:4096\n" \
  "v_cvt_pk_bf16_f32 v176, v160, v161\n" \
  "v_mfma_f32_16x16x32_bf16 v[168:171], v[140:143], v[124:127], v[168:171]\n" \
  "ds_read_b64_tr_b16 v[138:139], v184 offset:4352\n" \
  "v_cvt_pk_bf16_f32 v177, v162, v163\n" \
  "v_mfma_f32_16x16x32_bf16 v[172:175], v[156:159], v[124:127], 0\n" \
  "ds_read_b64_tr_b16 v[140:141], v184 offset:6144\n" \
  "v_cvt_pk_bf16_f32 v178, v164, v165\n" \
  "v_mfma_f32_16x16x32_bf16 v[172:175], v[152:155], v[120:123], v[172:175]\n" \
  "ds_read_b64_tr_b16 v[142:143], v184 offset:6400\n" \
  "v_cvt_pk_bf16_f32 v179, v166, v167\n" \
  "v_mfma_f32_16x16x32_bf16 v[172:175], v[148:151], v[116:119], v[172:175]\n" \
  "ds_read_b64_tr_b16 v[156:157], v185 offset:6144\n" \
  "ds_read_b64_tr_b16 v[158:159], v185 offset:6400\n" \
  "v_mfma_f32_16x16x32_bf16 v[172:175], v[144:147], v[112:115], v[172:175]\n" \
  "ds_read_b64_tr_b16 v[152:153], v185 offset:4096\n" \
  "ds_read_b64_tr_b16 v[154:155], v185 offset:4352\n" \
  "ds_read_b64_tr_b16 v[148:149], v185 offset:2048\n" \
  "ds_read_b64_tr_b16 v[150:151], v185 offset:2304\n" \
  "ds_read_b64_tr_b16 v[144:145], v185 offset:0\n" \
  "ds_read_b64_tr_b16 v[146:147], v185 offset:256\n" \
  "s_waitcnt vmcnt(5)\n" \
  "global_store_dwordx4 v187, v[176:179], s[44:45] offset:0 sc1\n" \
  "s_add_u32 s98, s98, 1\n" \
  "v_cvt_pk_bf16_f32 v180, v168, v169\n" \
  "v_cvt_pk_bf16_f32 v181, v170, v171\n" \
  "v_cvt_pk_bf16_f32 v182, v172, v173\n" \
  "v_cvt_pk_bf16_f32 v183, v174, v175\n" \
  "s_waitcnt vmcnt(5)\n" \
  "global_store_dwordx4 v188, v[180:183], s[44:45] offset:0 sc1\n" \
  "s_add_u32 s44, s44, 128000\n" \
  "s_addc_u32 s45, s45, 0\n" \
  "s_bitcmp0_b32 4160, s98\n" \
  "s_cbranch_scc1 LT\n" \
  "s_cmp_eq_u32 s98, 6\n" \
  "s_cbranch_scc1 LT_m\n" \
  "s_lshr_b32 s35, s30, 2\n" \
  "s_lshl3_add_u32 s35, s29, s35\n" \
  "s_mul_i32 s35, s35, 1600000\n" \
  "s_lshl_b32 s36, s28, 12\n" \
  "s_add_u32 s35, s35, s36\n" \
  "s_add_u32 s40, s24, s35\n" \
  "s_addc_u32 s41, s25, 0\n" \
  "s_bitcmp1_b32 s30, 2\n" \
  "s_cbranch_scc1 LB_l_h\n" \
  "global_load_dwordx4 v[64:67], v238, s[40:41] offset:0 sc1\n" \
  "global_load_dwordx4 v[68:71], v238, s[40:41] offset:1024 sc1\n" \
  "global_load_dwordx4 v[72:75], v238, s[40:41] offset:2048 sc1\n" \
  "global_load_dwordx4 v[76:79], v238, s[40:41] offset:3072 sc1\n" \
  "s_add_u32 s40, s40, 16000\n" \
  "s_addc_u32 s41, s41, 0\n" \
  "global_load_dwordx4 v[80:83], v238, s[40:41] offset:0 sc1\n" \
  "global_load_dwordx4 v[84:87], v238, s[40:41] offset:1024 sc1\n" \
  "global_load_dwordx4 v[88:91], v238, s[40:41] offset:2048 sc1\n" \
  "global_load_dwordx4 v[92:95], v238, s[40:41] offset:3072 sc1\n" \
  "s_add_u32 s40, s40, 16000\n" \
  "s_addc_u32 s41, s41, 0\n" \
  "global_load_dwordx4 v[96:99], v238, s[40:41] offset:0 sc1\n" \
  "global_load_dwordx4 v[100:103], v238, s[40:41] offset:1024 sc1\n" \
  "global_load_dwordx4 v[104:107], v238, s[40:41] offset:2048 sc1\n" \
  "global_load_dwordx4 v[108:111], v238, s[40:41] offset:3072 sc1\n" \
  "s_add_u32 s40, s40, 16000\n" \
  "s_addc_u32 s41, s41, 0\n" \
  "global_load_dwordx4 v[112:115], v238, s[40:41] offset:0 sc1\n" \
  "global_load_dwordx4 v[116:119], v238, s[40:41] offset:1024 sc1\n" \
  "global_load_dwordx4 v[120:123], v238, s[40:41] offset:2048 sc1\n" \
  "global_load_dwordx4 v[124:127], v238, s[40:41] offset:3072 sc1\n" \
  "s_add_u32 s40, s40, 16000\n" \
  "s_addc_u32 s41, s41, 0\n" \
  "s_branch LB_l_e\n" \
  "LB_l_h:\n" \
  "global_load_dwordx4 v[0:3], v238, s[40:41] offset:0 sc1\n" \
  "global_load_dwordx4 v[4:7], v238, s[40:41] offset:1024 sc1\n" \
  "global_load_dwordx4 v[8:11], v238, s[40:41] offset:2048 sc1\n" \
  "global_load_dwordx4 v[12:15], v238, s[40:41] offset:3072 sc1\n" \
  "s_add_u32 s40, s40, 16000\n" \
  "s_addc_u32 s41, s41, 0\n" \
  "global_load_dwordx4 v[16:19], v238, s[40:41] offset:0 sc1\n" \
  "global_load_dwordx4 v[20:23], v238, s[40:41] offset:1024 sc1\n" \
  "global_load_dwordx4 v[24:27], v238, s[40:41] offset:2048 sc1\n" \
  "global_load_dwordx4 v[28:31], v238, s[40:41] offset:3072 sc1\n" \
  "s_add_u32 s40, s40, 16000\n" \
  "s_addc_u32 s41, s41, 0\n" \
  "global_load_dwordx4 v[32:35], v238, s[40:41] offset:0 sc1\n" \
  "global_load_dwordx4 v[36:39], v238, s[40:41] offset:1024 sc1\n" \
  "global_load_dwordx4 v[40:43], v238, s[40:41] offset:2048 sc1\n" \
  "global_load_dwordx4 v[44:47], v238, s[40:41] offset:3072 sc1\n" \
  "s_add_u32 s40, s40, 16000\n" \
  "s_addc_u32 s41, s41, 0\n" \
  "global_load_dwordx4 v[48:51], v238, s[40:41] offset:0 sc1\n" \
  "global_load_dwordx4 v[52:55], v238, s[40:41] offset:1024 sc1\n" \
  "global_load_dwordx4 v[56:59], v238, s[40:41] offset:2048 sc1\n" \
  "global_load_dwordx4 v[60:63], v238, s[40:41] offset:3072 sc1\n" \
  "s_add_u32 s40, s40, 16000\n" \
  "s_addc_u32 s41, s41, 0\n" \
  "LB_l_e:\n" \
  "ds_write_b16_d16_hi v195, v197 offset:0\n" \
  "ds_write_b16_d16_hi v195, v199 offset:16\n" \
  "ds_write_b16_d16_hi v195, v201 offset:32\n" \
  "ds_write_b16_d16_hi v235, v203 offset:64\n" \
  "ds_write_b16_d16_hi v235, v205 offset:80\n" \
  "ds_write_b16_d16_hi v235, v207 offset:96\n" \
  "ds_write_b16_d16_hi v235, v209 offset:112\n" \
  "ds_write_b16_d16_hi v235, v211 offset:128\n" \
  "ds_write_b16_d16_hi v235, v213 offset:144\n" \
  "ds_write_b16_d16_hi v235, v215 offset:160\n" \
  "ds_write_b16_d16_hi v235, v217 offset:176\n" \
  "ds_write_b16_d16_hi v195, v219 offset:48\n" \
  "ds_write_b16_d16_hi v195, v221 offset:192\n" \
  "ds_write_b16_d16_hi v195, v223 offset:208\n" \
  "ds_write_b16_d16_hi v195, v225 offset:224\n" \
  "ds_write_b16_d16_hi v195, v227 offset:240\n" \
  "ds_read_b128 v[244:247], v254 offset:0\n" \
  "ds_read_b128 v[248:251], v254 offset:1088\n" \
  "s_lshr_b32 s35, s7, 5\n" \
  "s_mul_i32 s35, s35, 819200\n" \
  "s_and_b32 s36, s7, 31\n" \
  "s_lshl_b32 s36, s36, 10\n" \
  "s_add_u32 s35, s35, s36\n" \
  "s_lshl_b32 s36, s4, 9\n" \
  "s_add_u32 s35, s35, s36\n" \
  "s_add_u32 s35, s35, 102793216\n" \
  "s_add_u32 s58, s24, s35\n" \
  "s_addc_u32 s59, s25, 0\n" \
  "s_waitcnt lgkmcnt(0)\n" \
  "s_cmp_ge_u32 s4, 896\n" \
  "s_cbranch_scc1 L17_\n" \
  "global_store_dwordx4 v238, v[244:247], s[58:59] sc1\n" \
  "s_cmp_ge_u32 s4, 768\n" \
  "s_cbranch_scc1 L17_\n" \
  "s_add_u32 s58, s58, 32768\n" \
  "s_addc_u32 s59, s59, 0\n" \
  "global_store_dwordx4 v238, v[248:251], s[58:59] sc1\n" \
  "L17_:\n" \
  "s_lshr_b32 s35, s30, 2\n" \
  "s_lshl3_add_u32 s33, s29, s35\n" \
  "s_and_b32 s34, s30, 3\n" \
  "s_lshl_b32 s59, s27, 11\n" \
  "s_mul_i32 s35, s33, 1600000\n" \
  "s_add_u32 s54, s24, s35\n" \
  "s_addc_u32 s55, s25, 0\n" \
  "s_mul_i32 s35, s34, 819200\n" \
  "s_lshl1_add_u32 s35, s59, s35\n" \
  "s_add_u32 s35, s35, 102400000\n" \
  "s_add_u32 s56, s24, s35\n" \
  "s_addc_u32 s57, s25, 0\n" \
  "s_lshr_b32 s35, s30, 4\n" \
  "s_lshl1_add_u32 s35, s29, s35\n" \
  "s_lshl_b32 s35, s35, 6\n" \
  "s_bfe_u32 s37, s30, 0x20002\n" \
  "s_lshl3_add_u32 s35, s37, s35\n" \
  "v_and_b32 v202, 16, v255\n" \
  "v_lshl_add_u32 v202, v202, 1, s35\n" \
  "global_load_dwordx2 v[204:205], v202, s[16:17]\n" \
  "global_load_dwordx2 v[206:207], v202, s[18:19]\n" \
  "v_mov_b32 v202, v238\n" \
  "global_load_dwordx4 v[208:211], v238, s[56:57] offset:0 sc1\n" \
  "global_load_dwordx4 v[212:215], v238, s[56:57] offset:1024 sc1\n" \
  "global_load_dwordx4 v[216:219], v238, s[56:57] offset:2048 sc1\n" \
  "global_load_dwordx4 v[220:223], v238, s[56:57] offset:3072 sc1\n" \
  "s_add_u32 s56, s56, 32768\n" \
  "s_addc_u32 s57, s57, 0\n" \
  "global_load_dwordx4 v[224:227], v238, s[56:57] sc1\n" \
  "global_load_dwordx4 v[228:231], v238, s[56:57] offset:1024 sc1\n" \
  "global_load_dwordx4 v[232:235], v238, s[56:57] offset:2048 sc1\n" \
  "global_load_dwordx4 v[236:239], v238, s[56:57] offset:3072 sc1\n" \
  "s_add_u32 s56, s56, 32768\n" \
  "s_addc_u32 s57, s57, 0\n" \
  "s_bitcmp1_b32 s30, 2\n" \
  "s_cbranch_scc1 LB_t_h\n" \
  "s_waitcnt lgkmcnt(12)\n" \
  "v_mfma_f32_16x16x32_bf16 v[160:163], v[128:131], v[0:3], 0\n" \
  "v_mfma_f32_16x16x32_bf16 v[160:163], v[132:135], v[4:7], v[160:163]\n" \
  "s_waitcnt lgkmcnt(8)\n" \
  "v_mfma_f32_16x16x32_bf16 v[160:163], v[136:139], v[8:11], v[160:163]\n" \
  "v_mfma_f32_16x16x32_bf16 v[160:163], v[140:143], v[12:15], v[160:163]\n" \
  "s_waitcnt lgkmcnt(0)\n" \
  "v_mfma_f32_16x16x32_bf16 v[164:167], v[156:159], v[12:15], 0\n" \
  "v_mfma_f32_16x16x32_bf16 v[164:167], v[152:155], v[8:11], v[164:167]\n" \
  "v_mfma_f32_16x16x32_bf16 v[164:167], v[148:151], v[4:7], v[164:167]\n" \
  "v_mfma_f32_16x16x32_bf16 v[164:167], v[144:147], v[0:3], v[164:167]\n" \
  "v_mfma_f32_16x16x32_bf16 v[172:175], v[144:147], v[16:19], 0\n" \
  "v_mfma_f32_16x16x32_bf16 v[172:175], v[148:151], v[20:23], v[172:175]\n" \
  "v_mfma_f32_16x16x32_bf16 v[172:175], v[152:155], v[24:27], v[172:175]\n" \
  "v_cvt_pk_bf16_f32 v176, v160, v161\n" \
  "v_cvt_pk_bf16_f32 v177, v162, v163\n" \
  "v_mfma_f32_16x16x32_bf16 v[172:175], v[156:159], v[28:31], v[172:175]\n" \
  "v_mfma_f32_16x16x32_bf16 v[168:171], v[140:143], v[28:31], 0\n" \
  "v_mfma_f32_16x16x32_bf16 v[168:171], v[136:139], v[24:27], v[168:171]\n" \
  "v_cvt_pk_bf16_f32 v178, v164, v165\n" \
  "v_cvt_pk_bf16_f32 v179, v166, v167\n" \
  "v_mfma_f32_16x16x32_bf16 v[168:171], v[132:135], v[20:23], v[168:171]\n" \
  "v_mfma_f32_16x16x32_bf16 v[168:171], v[128:131], v[16:19], v[168:171]\n" \
  "s_waitcnt vmcnt(63)\n" \
  "global_store_dwordx4 v186, v[176:179], s[46:47] offset:0 sc1\n" \
  "v_mfma_f32_16x16x32_bf16 v[160:163], v[128:131], v[32:35], 0\n" \
  "v_cvt_pk_bf16_f32 v182, v172, v173\n" \
  "v_cvt_pk_bf16_f32 v183, v174, v175\n" \
  "v_mfma_f32_16x16x32_bf16 v[160:163], v[132:135], v[36:39], v[160:163]\n" \
  "v_mfma_f32_16x16x32_bf16 v[160:163], v[136:139], v[40:43], v[160:163]\n" \
  "v_mfma_f32_16x16x32_bf16 v[160:163], v[140:143], v[44:47], v[160:163]\n" \
  "v_cvt_pk_bf16_f32 v180, v168, v169\n" \
  "v_cvt_pk_bf16_f32 v181, v170, v171\n" \
  "v_mfma_f32_16x16x32_bf16 v[164:167], v[156:159], v[44:47], 0\n" \
  "v_mfma_f32_16x16x32_bf16 v[164:167], v[152:155], v[40:43], v[164:167]\n" \
  "v_mfma_f32_16x16x32_bf16 v[164:167], v[148:151], v[36:39], v[164:167]\n" \
  "v_mfma_f32_16x16x32_bf16 v[164:167], v[144:147], v[32:35], v[164:167]\n" \
  "s_waitcnt vmcnt(63)\n" \
  "global_store_dwordx4 v186, v[180:183], s[46:47] offset:256 sc1\n" \
  "v_mfma_f32_16x16x32_bf16 v[172:175], v[144:147], v[48:51], 0\n" \
  "v_cvt_pk_bf16_f32 v176, v160, v161\n" \
  "v_cvt_pk_bf16_f32 v177, v162, v163\n" \
  "v_mfma_f32_16x16x32_bf16 v[172:175], v[148:151], v[52:55], v[172:175]\n" \
  "v_mfma_f32_16x16x32_bf16 v[172:175], v[152:155], v[56:59], v[172:175]\n" \
  "v_mfma_f32_16x16x32_bf16 v[172:175], v[156:159], v[60:63], v[172:175]\n" \
  "v_cvt_pk_bf16_f32 v178, v164, v165\n" \
  "v_cvt_pk_bf16_f32 v179, v166, v167\n" \
  "v_mfma_f32_16x16x32_bf16 v[168:171], v[140:143], v[60:63], 0\n" \
  "v_mfma_f32_16x16x32_bf16 v[168:171], v[136:139], v[56:59], v[168:171]\n" \
  "v_mfma_f32_16x16x32_bf16 v[168:171], v[132:135], v[52:55], v[168:171]\n" \
  "v_mfma_f32_16x16x32_bf16 v[168:171], v[128:131], v[48:51], v[168:171]\n" \
  "s_waitcnt vmcnt(63)\n" \
  "global_store_dwordx4 v186, v[176:179], s[46:47] offset:512 sc1\n" \
  "v_cvt_pk_bf16_f32 v182, v172, v173\n" \
  "v_cvt_pk_bf16_f32 v183, v174, v175\n" \
  "s_nop 3\n" \
  "v_cvt_pk_bf16_f32 v180, v168, v169\n" \
  "v_cvt_pk_bf16_f32 v181, v170, v171\n" \
  "s_waitcnt vmcnt(63)\n" \
  "global_store_dwordx4 v186, v[180:183], s[46:47] offset:768 sc1\n" \
  "s_branch LB_t_e\n" \
  "LT_w:\n" \
  "L8_:\n" \
  "s_sleep 1\n" \
  "ds_read_b32 v192, v190 offset:0\n" \
  "s_waitcnt lgkmcnt(0)\n" \
  "v_readfirstlane_b32 s35, v192\n" \
  "s_cmp_lt_u32 s35, s99\n" \
  "s_cbranch_scc1 L8_\n" \
  "s_branch LT_c\n" \
  "LT_e:\n" \
  "s_bitcmp1_b32 10, s98\n" \
  "s_cbranch_scc0 LT_r\n" \
  "global_load_dwordx4 v[240:243], v238, s[48:49] offset:0 sc1\n" \
  "s_waitcnt vmcnt(0)\n" \
  "global_store_dwordx4 v238, v[240:243], s[48:49] offset:1024 sc1\n" \
  "s_branch LT_r\n" \
  "LT_a:\n" \
  "s_cmp_lg_u32 s35, 4\n" \
  "s_cbranch_scc1 L9_\n" \
  "global_load_dword v193, v193, s[48:49] offset:1024 sc1\n" \
  "s_branch LT_s\n" \
  "L9_:\n" \
  "s_cmp_lg_u32 s35, 5\n" \
  "s_cbranch_scc1 L10_\n" \
  "s_memtime s[58:59]\n" \
  "s_memrealtime s[40:41]\n" \
  "s_mov_b32 s5, 0x10000\n" \
  "LB_ec:\n" \
  "v_readfirstlane_b32 s35, v193\n" \
  "s_cmp_eq_u32 s35, s39\n" \
  "s_cbranch_scc1 LB_ok\n" \
  "s_lshl_b32 s35, s26, 2\n" \
  "v_mov_b32 v193, s35\n" \
  "s_sleep 8\n" \
  "global_load_dword v193, v193, s[48:49] offset:1024 sc1\n" \
  "s_waitcnt vmcnt(0)\n" \
  "s_sub_u32 s5, s5, 1\n" \
  "s_cbranch_scc0 LB_ec\n" \
  "LB_ok:\n" \
  "s_lshl_b32 s35, s26, 2\n" \
  "v_mov_b32 v244, s35\n" \
  "s_waitcnt lgkmcnt(0)\n" \
  "s_sub_u32 s88, s58, s88\n" \
  "s_sub_u32 s88, 106000, s88\n" \
  "s_sub_u32 s90, s58, s90\n" \
  "s_sub_u32 s42, s40, s42\n" \
  "s_lshl_b32 s40, s40, 4\n" \
  "v_cvt_f32_u32 v245, s90\n" \
  "v_cvt_f32_u32 v246, s42\n" \
  "v_rcp_f32 v245, v245\n" \
  "v_cvt_f32_i32 v247, s88\n" \
  "v_mul_f32 v246, 0x4111eb85, v246\n" \
  "v_mul_f32 v245, v245, v246\n" \
  "v_mul_f32 v245, v245, v247\n" \
  "v_cvt_i32_f32 v245, v245\n" \
  "v_add_u32 v245, s40, v245\n" \
  "v_or_b32 v245, 1, v245\n" \
  "s_mov_b64 exec, 1\n" \
  "global_store_dword v244, v245, s[48:49] offset:2048 sc1\n" \
  "s_mov_b64 exec, -1\n" \
  "s_branch LT_s\n" \
  "L10_:\n" \
  "s_cmp_lg_u32 s35, 7\n" \
  "s_cbranch_scc1 L11_\n" \
  "s_mov_b32 exec_hi, 0\n" \
  "global_atomic_add v196, v191, s[48:49] offset:3072 sc1\n" \
  "s_mov_b64 exec, -1\n" \
  "s_branch LT_s\n" \
  "L11_:\n" \
  "s_cmp_lg_u32 s35, 23\n" \
  "s_cbranch_scc1 L12_\n" \
  "s_lshl_b32 s35, s30, 5\n" \
  "v_lshrrev_b32 v206, 3, v255\n" \
  "v_and_b32 v208, 7, v255\n" \
  "v_lshl_add_u32 v210, v206, 2, s35\n" \
  "v_lshl_add_u32 v212, v208, 2, s35\n" \
  "global_load_dword v202, v210, s[48:49] offset:2048 sc1\n" \
  "global_load_dword v204, v212, s[48:49] offset:2048 sc1\n" \
  "s_branch LT_s\n" \
  "L12_:\n" \
  "s_cmp_lg_u32 s35, 24\n" \
  "s_cbranch_scc1 L13_\n" \
  "s_mov_b32 s5, 0x10000\n" \
  "LB_z:\n" \
  "v_min_u32 v214, v202, v204\n" \
  "v_cmp_eq_u32 s[58:59], 0, v214\n" \
  "s_sub_u32 s5, s5, 1\n" \
  "s_cbranch_scc1 LB_zx\n" \
  "s_cmp_eq_u64 s[58:59], 0\n" \
  "s_cbranch_scc1 LB_zx\n" \
  "s_cmp_lg_u32 s33, 1021\n" \
  "s_cbranch_scc1 LB_n1\n" \
  "global_load_dwordx4 v[240:243], v238, s[48:49] offset:0 sc1\n" \
  "s_waitcnt vmcnt(0)\n" \
  "global_store_dwordx4 v238, v[240:243], s[48:49] offset:1024 sc1\n" \
  "LB_n1:\n" \
  "s_sleep 4\n" \
  "global_load_dword v202, v210, s[48:49] offset:2048 sc1\n" \
  "global_load_dword v204, v212, s[48:49] offset:2048 sc1\n" \
  "s_waitcnt vmcnt(0)\n" \
  "s_branch LB_z\n" \
  "LB_zx:\n" \
  "v_and_or_b32 v202, v202, -8, v206\n" \
  "v_and_or_b32 v204, v204, -8, v208\n" \
  "v_cmp_lt_u32 s[58:59], v204, v202\n" \
  "v_sub_u32 v202, v204, v202\n" \
  "s_lshl_b32 s35, s29, 3\n" \
  "s_lshr_b64 s[40:41], s[58:59], s35\n" \
  "s_and_b32 s40, s40, 0xff\n" \
  "s_bcnt1_i32_b32 s40, s40\n" \
  "s_sub_u32 s40, 7, s40\n" \
  "s_mov_b32 s36, s29\n" \
  "s_bfe_u32 s41, s58, 0x80000\n" \
  "s_bcnt1_i32_b32 s41, s41\n" \
  "s_cmp_eq_u32 s41, s40\n" \
  "s_cselect_b32 s36, 0, s36\n" \
  "s_bfe_u32 s41, s58, 0x80008\n" \
  "s_bcnt1_i32_b32 s41, s41\n" \
  "s_cmp_eq_u32 s41, s40\n" \
  "s_cselect_b32 s36, 1, s36\n" \
  "s_bfe_u32 s41, s58, 0x80010\n" \
  "s_bcnt1_i32_b32 s41, s41\n" \
  "s_cmp_eq_u32 s41, s40\n" \
  "s_cselect_b32 s36, 2, s36\n" \
  "s_bfe_u32 s41, s58, 0x80018\n" \
  "s_bcnt1_i32_b32 s41, s41\n" \
  "s_cmp_eq_u32 s41, s40\n" \
  "s_cselect_b32 s36, 3, s36\n" \
  "s_bfe_u32 s41, s59, 0x80000\n" \
  "s_bcnt1_i32_b32 s41, s41\n" \
  "s_cmp_eq_u32 s41, s40\n" \
  "s_cselect_b32 s36, 4, s36\n" \
  "s_bfe_u32 s41, s59, 0x80008\n" \
  "s_bcnt1_i32_b32 s41, s41\n" \
  "s_cmp_eq_u32 s41, s40\n" \
  "s_cselect_b32 s36, 5, s36\n" \
  "s_bfe_u32 s41, s59, 0x80010\n" \
  "s_bcnt1_i32_b32 s41, s41\n" \
  "s_cmp_eq_u32 s41, s40\n" \
  "s_cselect_b32 s36, 6, s36\n" \
  "s_bfe_u32 s41, s59, 0x80018\n" \
  "s_bcnt1_i32_b32 s41, s41\n" \
  "s_cmp_eq_u32 s41, s40\n" \
  "s_cselect_b32 s36, 7, s36\n" \
  "s_add_u32 s35, s35, s36\n" \
  "v_readlane_b32 s41, v202, s35\n" \
  "s_sub_u32 s35, 0, s41\n" \
  "s_cmp_lt_u32 s40, 4\n" \
  "s_cselect_b32 s41, s35, s41\n" \
  "s_min_u32 s41, s41, 0xffff\n" \
  "s_mul_i32 s41, s41, 39\n" \
  "s_add_u32 s41, s41, 28672\n" \
  "s_lshr_b32 s41, s41, 16\n" \
  "s_min_u32 s41, s41, 4\n" \
  "s_sub_u32 s35, 0, s41\n" \
  "s_cmp_lt_u32 s40, 4\n" \
  "s_cselect_b32 s35, s35, s41\n" \
  "s_lshl4_add_u32 s35, s36, s35\n" \
  "s_add_u32 s35, s35, 132\n" \
  "v_mov_b32 v204, s35\n" \
  "ds_write_b32 v190, v204 offset:4\n" \
  "s_branch LT_s\n" \
  "L13_:\n" \
  "s_cmp_lg_u32 s35, 25\n" \
  "s_cbranch_scc1 L14_\n" \
  "s_mov_b64 exec, 0xff\n" \
  "global_load_dword v200, v198, s[48:49] offset:3072 sc1\n" \
  "s_mov_b64 exec, -1\n" \
  "s_branch LT_s\n" \
  "L14_:\n" \
  "s_cmp_lg_u32 s35, 26\n" \
  "s_cbranch_scc1 L15_\n" \
  "s_mov_b64 s[58:59], vcc\n" \
  "s_mov_b32 s5, 0x10000\n" \
  "LB_p:\n" \
  "s_mov_b64 exec, 0xff\n" \
  "v_cmp_le_u32 vcc, 32, v200\n" \
  "s_mov_b64 exec, -1\n" \
  "s_sub_u32 s5, s5, 1\n" \
  "s_cselect_b32 s35, 0xff, vcc_lo\n" \
  "s_cmp_eq_u32 s35, 0xff\n" \
  "s_cbranch_scc1 LB_p_x\n" \
  "s_cmp_lg_u32 s33, 1021\n" \
  "s_cbranch_scc1 LB_n2\n" \
  "global_load_dwordx4 v[240:243], v238, s[48:49] offset:0 sc1\n" \
  "s_waitcnt vmcnt(0)\n" \
  "global_store_dwordx4 v238, v[240:243], s[48:49] offset:1024 sc1\n" \
  "LB_n2:\n" \
  "s_sleep 2\n" \
  "s_mov_b64 exec, 0xff\n" \
  "global_load_dword v200, v198, s[48:49] offset:3072 sc1\n" \
  "s_mov_b64 exec, -1\n" \
  "s_waitcnt vmcnt(0)\n" \
  "s_branch LB_p\n" \
  "LB_p_x:\n" \
  "s_mov_b64 vcc, s[58:59]\n" \
  "s_branch LT_s\n" \
  "L15_:\n" \
  "s_branch LT_s\n" \
  "LT_m:\n" \
  "ds_write_b16_d16_hi v195, v196 offset:0\n" \
  "ds_write_b16_d16_hi v195, v198 offset:16\n" \
  "ds_write_b16_d16_hi v195, v200 offset:32\n" \
  "ds_write_b16_d16_hi v235, v202 offset:64\n" \
  "ds_write_b16_d16_hi v235, v204 offset:80\n" \
  "ds_write_b16_d16_hi v235, v206 offset:96\n" \
  "ds_write_b16_d16_hi v235, v208 offset:112\n" \
  "ds_write_b16_d16_hi v235, v210 offset:128\n" \
  "ds_write_b16_d16_hi v235, v212 offset:144\n" \
  "ds_write_b16_d16_hi v235, v214 offset:160\n" \
  "ds_write_b16_d16_hi v235, v216 offset:176\n" \
  "ds_write_b16_d16_hi v195, v218 offset:48\n" \
  "ds_write_b16_d16_hi v195, v220 offset:192\n" \
  "ds_write_b16_d16_hi v195, v222 offset:208\n" \
  "ds_write_b16_d16_hi v195, v224 offset:224\n" \
  "ds_write_b16_d16_hi v195, v226 offset:240\n" \
  "ds_read_b128 v[244:247], v254 offset:0\n" \
  "ds_read_b128 v[248:251], v254 offset:1088\n" \
  "s_lshr_b32 s35, s7, 5\n" \
  "s_mul_i32 s35, s35, 819200\n" \
  "s_and_b32 s36, s7, 31\n" \
  "s_lshl_b32 s36, s36, 10\n" \
  "s_add_u32 s35, s35, s36\n" \
  "s_lshl_b32 s36, s4, 9\n" \
  "s_add_u32 s35, s35, s36\n" \
  "s_add_u32 s35, s35, 102400000\n" \
  "s_add_u32 s58, s24, s35\n" \
  "s_addc_u32 s59, s25, 0\n" \
  "s_waitcnt lgkmcnt(0)\n" \
  "s_cmp_ge_u32 s4, 768\n" \
  "s_cbranch_scc1 L16_\n" \
  "global_store_dwordx4 v238, v[244:247], s[58:59] sc1\n" \
  "s_cmp_ge_u32 s4, 768\n" \
  "s_cbranch_scc1 L16_\n" \
  "s_add_u32 s58, s58, 32768\n" \
  "s_addc_u32 s59, s59, 0\n" \
  "global_store_dwordx4 v238, v[248:251], s[58:59] sc1\n" \
  "L16_:\n" \
  "v_mov_b32 v234, 0\n" \
  "v_mov_b32 v236, v237\n" \
  "s_mov_b32 s38, 1\n" \
  "s_lshl_b32 s35, s29, 8\n" \
  "v_lshl_add_u32 v196, v255, 11, s35\n" \
  "s_lshl_b32 s35, s30, 11\n" \
  "v_lshl_add_u32 v198, v255, 8, s35\n" \
  "s_branch LT\n" \
  "LB_t_h:\n" \
  "s_waitcnt lgkmcnt(12)\n" \
  "v_mfma_f32_16x16x32_bf16 v[160:163], v[128:131], v[64:67], 0\n" \
  "v_mfma_f32_16x16x32_bf16 v[160:163], v[132:135], v[68:71], v[160:163]\n" \
  "s_waitcnt lgkmcnt(8)\n" \
  "v_mfma_f32_16x16x32_bf16 v[160:163], v[136:139], v[72:75], v[160:163]\n" \
  "v_mfma_f32_16x16x32_bf16 v[160:163], v[140:143], v[76:79], v[160:163]\n" \
  "s_waitcnt lgkmcnt(0)\n" \
  "v_mfma_f32_16x16x32_bf16 v[164:167], v[156:159], v[76:79], 0\n" \
  "v_mfma_f32_16x16x32_bf16 v[164:167], v[152:155], v[72:75], v[164:167]\n" \
  "v_mfma_f32_16x16x32_bf16 v[164:167], v[148:151], v[68:71], v[164:167]\n" \
  "v_mfma_f32_16x16x32_bf16 v[164:167], v[144:147], v[64:67], v[164:167]\n" \
  "v_mfma_f32_16x16x32_bf16 v[172:175], v[144:147], v[80:83], 0\n" \
  "v_mfma_f32_16x16x32_bf16 v[172:175], v[148:151], v[84:87], v[172:175]\n" \
  "v_mfma_f32_16x16x32_bf16 v[172:175], v[152:155], v[88:91], v[172:175]\n" \
  "v_cvt_pk_bf16_f32 v176, v160, v161\n" \
  "v_cvt_pk_bf16_f32 v177, v162, v163\n" \
  "v_mfma_f32_16x16x32_bf16 v[172:175], v[156:159], v[92:95], v[172:175]\n" \
  "v_mfma_f32_16x16x32_bf16 v[168:171], v[140:143], v[92:95], 0\n" \
  "v_mfma_f32_16x16x32_bf16 v[168:171], v[136:139], v[88:91], v[168:171]\n" \
  "v_cvt_pk_bf16_f32 v178, v164, v165\n" \
  "v_cvt_pk_bf16_f32 v179, v166, v167\n" \
  "v_mfma_f32_16x16x32_bf16 v[168:171], v[132:135], v[84:87], v[168:171]\n" \
  "v_mfma_f32_16x16x32_bf16 v[168:171], v[128:131], v[80:83], v[168:171]\n" \
  "s_waitcnt vmcnt(63)\n" \
  "global_store_dwordx4 v186, v[176:179], s[46:47] offset:1024 sc1\n" \
  "v_mfma_f32_16x16x32_bf16 v[160:163], v[128:131], v[96:99], 0\n" \
  "v_cvt_pk_bf16_f32 v182, v172, v173\n" \
  "v_cvt_pk_bf16_f32 v183, v174, v175\n" \
  "v_mfma_f32_16x16x32_bf16 v[160:163], v[132:135], v[100:103], v[160:163]\n" \
  "v_mfma_f32_16x16x32_bf16 v[160:163], v[136:139], v[104:107], v[160:163]\n" \
  "v_mfma_f32_16x16x32_bf16 v[160:163], v[140:143], v[108:111], v[160:163]\n" \
  "v_cvt_pk_bf16_f32 v180, v168, v169\n" \
  "v_cvt_pk_bf16_f32 v181, v170, v171\n" \
  "v_mfma_f32_16x16x32_bf16 v[164:167], v[156:159], v[108:111], 0\n" \
  "v_mfma_f32_16x16x32_bf16 v[164:167], v[152:155], v[104:107], v[164:167]\n" \
  "v_mfma_f32_16x16x32_bf16 v[164:167], v[148:151], v[100:103], v[164:167]\n" \
  "v_mfma_f32_16x16x32_bf16 v[164:167], v[144:147], v[96:99], v[164:167]\n" \
  "s_waitcnt vmcnt(63)\n" \
  "global_store_dwordx4 v186, v[180:183], s[46:47] offset:1280 sc1\n" \
  "v_mfma_f32_16x16x32_bf16 v[168:171], v[128:131], v[112:115], 0\n" \
  "v_cvt_pk_bf16_f32 v176, v160, v161\n" \
  "v_cvt_pk_bf16_f32 v177, v162, v163\n" \
  "v_mfma_f32_16x16x32_bf16 v[168:171], v[132:135], v[116:119], v[168:171]\n" \
  "v_mfma_f32_16x16x32_bf16 v[168:171], v[136:139], v[120:123], v[168:171]\n" \
  "v_mfma_f32_16x16x32_bf16 v[168:171], v[140:143], v[124:127], v[168:171]\n" \
  "v_cvt_pk_bf16_f32 v178, v164, v165\n" \
  "v_cvt_pk_bf16_f32 v179, v166, v167\n" \
  "v_mfma_f32_16x16x32_bf16 v[172:175], v[156:159], v[124:127], 0\n" \
  "v_mfma_f32_16x16x32_bf16 v[172:175], v[152:155], v[120:123], v[172:175]\n" \
  "v_mfma_f32_16x16x32_bf16 v[172:175], v[148:151], v[116:119], v[172:175]\n" \
  "v_mfma_f32_16x16x32_bf16 v[172:175], v[144:147], v[112:115], v[172:175]\n" \
  "s_waitcnt vmcnt(63)\n" \
  "global_store_dwordx4 v187, v[176:179], s[46:47] offset:0 sc1\n" \
  "v_cvt_pk_bf16_f32 v180, v168, v169\n" \
  "v_cvt_pk_bf16_f32 v181, v170, v171\n" \
  "s_nop 3\n" \
  "v_cvt_pk_bf16_f32 v182, v172, v173\n" \
  "v_cvt_pk_bf16_f32 v183, v174, v175\n" \
  "s_waitcnt vmcnt(63)\n" \
  "global_store_dwordx4 v188, v[180:183], s[46:47] offset:0 sc1\n" \
  "LB_t_e:\n" \
  "s_setprio 2\n" \
  "v_mov_b64 v[240:241], -1\n" \
  "v_mov_b64 v[242:243], -1\n" \
  "s_lshl2_add_u32 s35, s33, s34\n" \
  "s_lshl2_add_u32 s35, s35, s28\n" \
  "s_lshl_b32 s35, s35, 12\n" \
  "s_add_u32 s35, s35, 105676800\n" \
  "s_add_u32 s40, s24, s35\n" \
  "s_addc_u32 s41, s25, 0\n" \
  "v_mbcnt_lo_u32_b32 v189, -1, 0\n" \
  "v_mbcnt_hi_u32_b32 v189, -1, v189\n" \
  "v_lshlrev_b32 v188, 4, v189\n" \
  "s_lshl_b32 s35, s28, 12\n" \
  "v_add_u32 v128, s35, v188\n" \
  "v_mov_b32 v199, 1\n" \
  "v_lshrrev_b32 v180, 1, v189\n" \
  "v_xor_b32 v180, v180, v189\n" \
  "v_and_b32 v180, 8, v180\n" \
  "v_bfe_u32 v181, v189, 2, 1\n" \
  "v_lshlrev_b32_e64 v181, v181, 5\n" \
  "v_lshlrev_b32 v180, v180, v181\n" \
  "v_bfe_u32 v181, v189, 2, 2\n" \
  "v_lshlrev_b32_e64 v181, v181, 1\n" \
  "v_cmp_gt_u32 vcc, 32, v189\n" \
  "v_mov_b32 v191, 0x3f80\n" \
  "v_add_u32 v190, 32000, v188\n" \
  "v_cndmask_b32 v192, v181, v180, vcc\n" \
  "v_bfe_i32 v180, v192, 0, 1\n" \
  "v_and_b32 v180, v180, v191\n" \
  "v_bfe_i32 v181, v192, 1, 1\n" \
  "v_and_b32 v181, v181, v191\n" \
  "v_bfe_i32 v182, v192, 2, 1\n" \
  "v_and_b32 v182, v182, v191\n" \
  "v_bfe_i32 v183, v192, 3, 1\n" \
  "v_and_b32 v183, v183, v191\n" \
  "v_and_b32 v192, 3, v189\n" \
  "v_mul_u32_u24 v192, 0x5555, v192\n" \
  "v_add_u32 v189, 16000, v188\n" \
  "v_add_u32 v191, 48000, v188\n" \
  "s_waitcnt vmcnt(4)\n" \
  "v_lshlrev_b32 v184, 16, v204\n" \
  "v_and_b32 v185, 0xffff0000, v204\n" \
  "v_lshlrev_b32 v186, 16, v205\n" \
  "v_and_b32 v187, 0xffff0000, v205\n" \
  "global_store_dwordx4 v202, v[240:243], s[40:41] offset:0 sc1\n" \
  "global_store_dwordx4 v202, v[240:243], s[40:41] offset:1024 sc1\n" \
  "global_store_dwordx4 v202, v[240:243], s[40:41] offset:2048 sc1\n" \
  "global_store_dwordx4 v202, v[240:243], s[40:41] offset:3072 sc1\n" \
  "s_setprio 0\n" \
  "s_barrier\n" \
  "s_bitcmp1_b32 s30, 2\n" \
  "s_cbranch_scc1 LB_w_h\n" \
  "ds_write_b128 v128, v[64:67] offset:0\n" \
  "ds_write_b128 v128, v[68:71] offset:1024\n" \
  "ds_write_b128 v128, v[72:75] offset:2048\n" \
  "ds_write_b128 v128, v[76:79] offset:3072\n" \
  "s_add_u32 s35, s59, 64000\n" \
  "s_add_u32 s54, s54, s35\n" \
  "s_addc_u32 s55, s55, 0\n" \
  "s_add_u32 s58, s59, 0x10000\n" \
  "s_mov_b32 m0, s58\n" \
  "s_add_u32 s35, s58, 0x8000\n" \
  "global_load_lds_dwordx4 v188, s[54:55] sc1\n" \
  "global_load_lds_dwordx4 v188, s[54:55] offset:1024 sc1\n" \
  "ds_write_b128 v128, v[80:83] offset:16384\n" \
  "ds_write_b128 v128, v[84:87] offset:17408\n" \
  "ds_write_b128 v128, v[88:91] offset:18432\n" \
  "ds_write_b128 v128, v[92:95] offset:19456\n" \
  "ds_write_b128 v128, v[96:99] offset:32768\n" \
  "ds_write_b128 v128, v[100:103] offset:33792\n" \
  "ds_write_b128 v128, v[104:107] offset:34816\n" \
  "ds_write_b128 v128, v[108:111] offset:35840\n" \
  "s_add_u32 m0, s58, 0x4000\n" \
  "s_cmp_lt_u32 s35, 0x28000\n" \
  "global_load_lds_dwordx4 v189, s[54:55] sc1\n" \
  "global_load_lds_dwordx4 v189, s[54:55] offset:1024 sc1\n" \
  "s_cselect_b32 s58, s35, s59\n" \
  "s_add_u32 s54, s54, 32000\n" \
  "s_addc_u32 s55, s55, 0\n" \
  "ds_write_b128 v128, v[112:115] offset:49152\n" \
  "ds_write_b128 v128, v[116:119] offset:50176\n" \
  "ds_write_b128 v128, v[120:123] offset:51200\n" \
  "ds_write_b128 v128, v[124:127] offset:52224\n" \
  "s_branch LB_w_e\n" \
  "LB_w_h:\n" \
  "ds_write_b128 v128, v[0:3] offset:0\n" \
  "ds_write_b128 v128, v[4:7] offset:1024\n" \
  "ds_write_b128 v128, v[8:11] offset:2048\n" \
  "ds_write_b128 v128, v[12:15] offset:3072\n" \
  "s_add_u32 s35, s59, 64000\n" \
  "s_add_u32 s54, s54, s35\n" \
  "s_addc_u32 s55, s55, 0\n" \
  "s_add_u32 s58, s59, 0x10000\n" \
  "s_mov_b32 m0, s58\n" \
  "s_add_u32 s35, s58, 0x8000\n" \
  "global_load_lds_dwordx4 v188, s[54:55] sc1\n" \
  "global_load_lds_dwordx4 v188, s[54:55] offset:1024 sc1\n" \
  "ds_write_b128 v128, v[16:19] offset:16384\n" \
  "ds_write_b128 v128, v[20:23] offset:17408\n" \
  "ds_write_b128 v128, v[24:27] offset:18432\n" \
  "ds_write_b128 v128, v[28:31] offset:19456\n" \
  "ds_write_b128 v128, v[32:35] offset:32768\n" \
  "ds_write_b128 v128, v[36:39] offset:33792\n" \
  "ds_write_b128 v128, v[40:43] offset:34816\n" \
  "ds_write_b128 v128, v[44:47] offset:35840\n" \
  "s_add_u32 m0, s58, 0x4000\n" \
  "s_cmp_lt_u32 s35, 0x28000\n" \
  "global_load_lds_dwordx4 v189, s[54:55] sc1\n" \
  "global_load_lds_dwordx4 v189, s[54:55] offset:1024 sc1\n" \
  "s_cselect_b32 s58, s35, s59\n" \
  "s_add_u32 s54, s54, 32000\n" \
  "s_addc_u32 s55, s55, 0\n" \
  "ds_write_b128 v128, v[48:51] offset:49152\n" \
  "ds_write_b128 v128, v[52:55] offset:50176\n" \
  "ds_write_b128 v128, v[56:59] offset:51200\n" \
  "ds_write_b128 v128, v[60:63] offset:52224\n" \
  "LB_w_e:\n" \
  "s_waitcnt lgkmcnt(0)\n" \
  "s_mov_b32 s51, 7\n" \
  "s_setprio 0\n" \
  "s_barrier\n" \
  "LG_start:\n" \
  "s_sub_u32 s35, 14960, s59\n" \
  "v_min_u32 v156, s35, v188\n" \
  "v_add_u32 v157, 16000, v156\n" \
  "v_add_u32 v158, 32000, v156\n" \
  "v_add_u32 v159, 48000, v156\n" \
  "v_lshlrev_b32_sdwa v64, 4, v208 dst_sel:DWORD dst_unused:UNUSED_PAD src0_sel:DWORD src1_sel:WORD_0\n" \
  "ds_read_b128 v[68:71], v64\n" \
  "v_mov_b64 v[0:1], v[184:185]\n" \
  "v_lshlrev_b32_sdwa v65, 4, v208 dst_sel:DWORD dst_unused:UNUSED_PAD src0_sel:DWORD src1_sel:WORD_1\n" \
  "ds_read_b128 v[72:75], v65\n" \
  "v_mov_b64 v[2:3], v[186:187]\n" \
  "v_lshlrev_b32_sdwa v66, 4, v209 dst_sel:DWORD dst_unused:UNUSED_PAD src0_sel:DWORD src1_sel:WORD_0\n" \
  "ds_read_b128 v[76:79], v66\n" \
  "v_mov_b64 v[4:5], v[184:185]\n" \
  "v_lshlrev_b32_sdwa v67, 4, v209 dst_sel:DWORD dst_unused:UNUSED_PAD src0_sel:DWORD src1_sel:WORD_1\n" \
  "ds_read_b128 v[80:83], v67\n" \
  "v_mov_b64 v[6:7], v[186:187]\n" \
  "s_mov_b32 m0, s58\n" \
  "s_add_u32 s35, s58, 0x8000\n" \
  "global_load_lds_dwordx4 v188, s[54:55] sc1\n" \
  "global_load_lds_dwordx4 v156, s[54:55] offset:1024 sc1\n" \
  "v_lshlrev_b32_sdwa v64, 4, v210 dst_sel:DWORD dst_unused:UNUSED_PAD src0_sel:DWORD src1_sel:WORD_0\n" \
  "ds_read_b128 v[84:87], v64\n" \
  "v_mov_b64 v[8:9], v[184:185]\n" \
  "v_lshlrev_b32_sdwa v65, 4, v210 dst_sel:DWORD dst_unused:UNUSED_PAD src0_sel:DWORD src1_sel:WORD_1\n" \
  "ds_read_b128 v[88:91], v65\n" \
  "v_mov_b64 v[10:11], v[186:187]\n" \
  "v_lshlrev_b32_sdwa v66, 4, v211 dst_sel:DWORD dst_unused:UNUSED_PAD src0_sel:DWORD src1_sel:WORD_0\n" \
  "ds_read_b128 v[92:95], v66\n" \
  "v_mov_b64 v[12:13], v[184:185]\n" \
  "v_lshlrev_b32_sdwa v67, 4, v211 dst_sel:DWORD dst_unused:UNUSED_PAD src0_sel:DWORD src1_sel:WORD_1\n" \
  "ds_read_b128 v[96:99], v67\n" \
  "v_mov_b64 v[14:15], v[186:187]\n" \
  "s_add_u32 m0, s58, 0x4000\n" \
  "s_cmp_lt_u32 s35, 0x28000\n" \
  "global_load_lds_dwordx4 v189, s[54:55] sc1\n" \
  "global_load_lds_dwordx4 v157, s[54:55] offset:1024 sc1\n" \
  "s_cselect_b32 s58, s35, s59\n" \
  "v_lshlrev_b32_sdwa v64, 4, v212 dst_sel:DWORD dst_unused:UNUSED_PAD src0_sel:DWORD src1_sel:WORD_0\n" \
  "ds_read_b128 v[100:103], v64\n" \
  "v_mov_b64 v[16:17], v[184:185]\n" \
  "v_lshlrev_b32_sdwa v65, 4, v212 dst_sel:DWORD dst_unused:UNUSED_PAD src0_sel:DWORD src1_sel:WORD_1\n" \
  "ds_read_b128 v[104:107], v65\n" \
  "v_mov_b64 v[18:19], v[186:187]\n" \
  "v_lshlrev_b32_sdwa v66, 4, v213 dst_sel:DWORD dst_unused:UNUSED_PAD src0_sel:DWORD src1_sel:WORD_0\n" \
  "ds_read_b128 v[108:111], v66\n" \
  "v_mov_b64 v[20:21], v[184:185]\n" \
  "v_lshlrev_b32_sdwa v67, 4, v213 dst_sel:DWORD dst_unused:UNUSED_PAD src0_sel:DWORD src1_sel:WORD_1\n" \
  "ds_read_b128 v[112:115], v67\n" \
  "v_mov_b64 v[22:23], v[186:187]\n" \
  "s_waitcnt lgkmcnt(10)\n" \
  "v_smfmac_f32_16x16x64_bf16 v[0:3], v[180:183], v[68:75], v192\n" \
  "v_lshlrev_b32_sdwa v64, 4, v214 dst_sel:DWORD dst_unused:UNUSED_PAD src0_sel:DWORD src1_sel:WORD_0\n" \
  "ds_read_b128 v[68:71], v64\n" \
  "v_mov_b64 v[24:25], v[184:185]\n" \
  "v_lshlrev_b32_sdwa v65, 4, v214 dst_sel:DWORD dst_unused:UNUSED_PAD src0_sel:DWORD src1_sel:WORD_1\n" \
  "ds_read_b128 v[72:75], v65\n" \
  "v_mov_b64 v[26:27], v[186:187]\n" \
  "s_mov_b32 m0, s58\n" \
  "s_add_u32 s35, s58, 0x8000\n" \
  "global_load_lds_dwordx4 v190, s[54:55] sc1\n" \
  "global_load_lds_dwordx4 v158, s[54:55] offset:1024 sc1\n" \
  "s_waitcnt lgkmcnt(10)\n" \
  "v_smfmac_f32_16x16x64_bf16 v[4:7], v[180:183], v[76:83], v192\n" \
  "v_lshlrev_b32_sdwa v66, 4, v215 dst_sel:DWORD dst_unused:UNUSED_PAD src0_sel:DWORD src1_sel:WORD_0\n" \
  "ds_read_b128 v[76:79], v66\n" \
  "v_mov_b64 v[28:29], v[184:185]\n" \
  "v_lshlrev_b32_sdwa v67, 4, v215 dst_sel:DWORD dst_unused:UNUSED_PAD src0_sel:DWORD src1_sel:WORD_1\n" \
  "ds_read_b128 v[80:83], v67\n" \
  "v_mov_b64 v[30:31], v[186:187]\n" \
  "s_waitcnt lgkmcnt(10)\n" \
  "v_smfmac_f32_16x16x64_bf16 v[8:11], v[180:183], v[84:91], v192\n" \
  "v_lshlrev_b32_sdwa v64, 4, v216 dst_sel:DWORD dst_unused:UNUSED_PAD src0_sel:DWORD src1_sel:WORD_0\n" \
  "ds_read_b128 v[84:87], v64\n" \
  "v_mov_b64 v[32:33], v[184:185]\n" \
  "v_lshlrev_b32_sdwa v65, 4, v216 dst_sel:DWORD dst_unused:UNUSED_PAD src0_sel:DWORD src1_sel:WORD_1\n" \
  "ds_read_b128 v[88:91], v65\n" \
  "v_mov_b64 v[34:35], v[186:187]\n" \
  "s_add_u32 m0, s58, 0x4000\n" \
  "s_cmp_lt_u32 s35, 0x28000\n" \
  "global_load_lds_dwordx4 v191, s[54:55] sc1\n" \
  "global_load_lds_dwordx4 v159, s[54:55] offset:1024 sc1\n" \
  "s_cselect_b32 s58, s35, s59\n" \
  "s_waitcnt lgkmcnt(10)\n" \
  "v_smfmac_f32_16x16x64_bf16 v[12:15], v[180:183], v[92:99], v192\n" \
  "v_lshlrev_b32_sdwa v66, 4, v217 dst_sel:DWORD dst_unused:UNUSED_PAD src0_sel:DWORD src1_sel:WORD_0\n" \
  "ds_read_b128 v[92:95], v66\n" \
  "v_mov_b64 v[36:37], v[184:185]\n" \
  "v_lshlrev_b32_sdwa v67, 4, v217 dst_sel:DWORD dst_unused:UNUSED_PAD src0_sel:DWORD src1_sel:WORD_1\n" \
  "ds_read_b128 v[96:99], v67\n" \
  "v_mov_b64 v[38:39], v[186:187]\n" \
  "s_waitcnt lgkmcnt(10)\n" \
  "v_smfmac_f32_16x16x64_bf16 v[16:19], v[180:183], v[100:107], v192\n" \
  "v_lshlrev_b32_sdwa v64, 4, v218 dst_sel:DWORD dst_unused:UNUSED_PAD src0_sel:DWORD src1_sel:WORD_0\n" \
  "ds_read_b128 v[100:103], v64\n" \
  "v_mov_b64 v[40:41], v[184:185]\n" \
  "v_lshlrev_b32_sdwa v65, 4, v218 dst_sel:DWORD dst_unused:UNUSED_PAD src0_sel:DWORD src1_sel:WORD_1\n" \
  "ds_read_b128 v[104:107], v65\n" \
  "v_mov_b64 v[42:43], v[186:187]\n" \
  "global_load_dwordx4 v[240:243], v188, s[56:57] sc1\n" \
  "global_load_dwordx4 v[244:247], v188, s[56:57] offset:1024 sc1\n" \
  "s_waitcnt lgkmcnt(10)\n" \
  "v_smfmac_f32_16x16x64_bf16 v[20:23], v[180:183], v[108:115], v192\n" \
  "v_lshlrev_b32_sdwa v66, 4, v219 dst_sel:DWORD dst_unused:UNUSED_PAD src0_sel:DWORD src1_sel:WORD_0\n" \
  "ds_read_b128 v[108:111], v66\n" \
  "v_mov_b64 v[44:45], v[184:185]\n" \
  "v_lshlrev_b32_sdwa v67, 4, v219 dst_sel:DWORD dst_unused:UNUSED_PAD src0_sel:DWORD src1_sel:WORD_1\n" \
  "ds_read_b128 v[112:115], v67\n" \
  "v_mov_b64 v[46:47], v[186:187]\n" \
  "s_waitcnt lgkmcnt(10)\n" \
  "v_smfmac_f32_16x16x64_bf16 v[24:27], v[180:183], v[68:75], v192\n" \
  "v_lshlrev_b32_sdwa v64, 4, v220 dst_sel:DWORD dst_unused:UNUSED_PAD src0_sel:DWORD src1_sel:WORD_0\n" \
  "ds_read_b128 v[68:71], v64\n" \
  "v_mov_b64 v[48:49], v[184:185]\n" \
  "v_lshlrev_b32_sdwa v65, 4, v220 dst_sel:DWORD dst_unused:UNUSED_PAD src0_sel:DWORD src1_sel:WORD_1\n" \
  "ds_read_b128 v[72:75], v65\n" \
  "v_mov_b64 v[50:51], v[186:187]\n" \
  "global_load_dwordx4 v[248:251], v188, s[56:57] offset:2048 sc1\n" \
  "global_load_dwordx4 v[252:255], v188, s[56:57] offset:3072 sc1\n" \
  "s_waitcnt lgkmcnt(10)\n" \
  "v_smfmac_f32_16x16x64_bf16 v[28:31], v[180:183], v[76:83], v192\n" \
  "v_lshlrev_b32_sdwa v66, 4, v221 dst_sel:DWORD dst_unused:UNUSED_PAD src0_sel:DWORD src1_sel:WORD_0\n" \
  "ds_read_b128 v[76:79], v66\n" \
  "v_mov_b64 v[52:53], v[184:185]\n" \
  "v_lshlrev_b32_sdwa v67, 4, v221 dst_sel:DWORD dst_unused:UNUSED_PAD src0_sel:DWORD src1_sel:WORD_1\n" \
  "ds_read_b128 v[80:83], v67\n" \
  "v_mov_b64 v[54:55], v[186:187]\n" \
  "s_waitcnt lgkmcnt(10)\n" \
  "v_smfmac_f32_16x16x64_bf16 v[32:35], v[180:183], v[84:91], v192\n" \
  "v_lshlrev_b32_sdwa v64, 4, v222 dst_sel:DWORD dst_unused:UNUSED_PAD src0_sel:DWORD src1_sel:WORD_0\n" \
  "ds_read_b128 v[84:87], v64\n" \
  "v_mov_b64 v[56:57], v[184:185]\n" \
  "v_lshlrev_b32_sdwa v65, 4, v222 dst_sel:DWORD dst_unused:UNUSED_PAD src0_sel:DWORD src1_sel:WORD_1\n" \
  "ds_read_b128 v[88:91], v65\n" \
  "v_mov_b64 v[58:59], v[186:187]\n" \
  "s_waitcnt lgkmcnt(10)\n" \
  "v_smfmac_f32_16x16x64_bf16 v[36:39], v[180:183], v[92:99], v192\n" \
  "v_lshlrev_b32_sdwa v66, 4, v223 dst_sel:DWORD dst_unused:UNUSED_PAD src0_sel:DWORD src1_sel:WORD_0\n" \
  "ds_read_b128 v[92:95], v66\n" \
  "v_mov_b64 v[60:61], v[184:185]\n" \
  "v_lshlrev_b32_sdwa v67, 4, v223 dst_sel:DWORD dst_unused:UNUSED_PAD src0_sel:DWORD src1_sel:WORD_1\n" \
  "ds_read_b128 v[96:99], v67\n" \
  "v_mov_b64 v[62:63], v[186:187]\n" \
  "s_add_u32 s56, s56, 32768\n" \
  "s_addc_u32 s57, s57, 0\n" \
  "s_add_u32 s54, s54, 64000\n" \
  "s_addc_u32 s55, s55, 0\n" \
  "s_cmp_eq_u32 s27, 4\n" \
  "s_cselect_b32 s37, 0x90, 0\n" \
  ".p2align 6\n" \
  "LG_loop:\n" \
  "s_bitcmp1_b32 s37, s51\n" \
  "s_waitcnt lgkmcnt(10)\n" \
  "v_smfmac_f32_16x16x64_bf16 v[40:43], v[180:183], v[100:107], v192\n" \
  "s_waitcnt lgkmcnt(8)\n" \
  "v_smfmac_f32_16x16x64_bf16 v[44:47], v[180:183], v[108:115], v192\n" \
  "s_waitcnt vmcnt(8)\n" \
  "s_cbranch_scc1 LG_h\n" \
  "LG_ret:\n" \
  "s_barrier\n" \
  "LG_aft:\n" \
  "v_lshlrev_b32_sdwa v64, 4, v224 dst_sel:DWORD dst_unused:UNUSED_PAD src0_sel:DWORD src1_sel:WORD_0\n" \
  "ds_read_b128 v[100:103], v64\n" \
  "v_lshlrev_b32_sdwa v65, 4, v224 dst_sel:DWORD dst_unused:UNUSED_PAD src0_sel:DWORD src1_sel:WORD_1\n" \
  "ds_read_b128 v[104:107], v65\n" \
  "v_lshlrev_b32_sdwa v66, 4, v225 dst_sel:DWORD dst_unused:UNUSED_PAD src0_sel:DWORD src1_sel:WORD_0\n" \
  "ds_read_b128 v[108:111], v66\n" \
  "v_lshlrev_b32_sdwa v67, 4, v225 dst_sel:DWORD dst_unused:UNUSED_PAD src0_sel:DWORD src1_sel:WORD_1\n" \
  "ds_read_b128 v[112:115], v67\n" \
  "s_waitcnt lgkmcnt(10)\n" \
  "v_smfmac_f32_16x16x64_bf16 v[48:51], v[180:183], v[68:75], v192\n" \
  "s_waitcnt lgkmcnt(8)\n" \
  "v_smfmac_f32_16x16x64_bf16 v[52:55], v[180:183], v[76:83], v192\n" \
  "s_waitcnt lgkmcnt(6)\n" \
  "v_smfmac_f32_16x16x64_bf16 v[56:59], v[180:183], v[84:91], v192\n" \
  "s_waitcnt lgkmcnt(4)\n" \
  "v_smfmac_f32_16x16x64_bf16 v[60:63], v[180:183], v[92:99], v192\n" \
  "s_barrier\n" \
  "s_mov_b32 m0, s58\n" \
  "s_add_u32 s35, s58, 0x8000\n" \
  "global_load_lds_dwordx4 v188, s[54:55] sc1\n" \
  "global_load_lds_dwordx4 v156, s[54:55] offset:1024 sc1\n" \
  "v_lshlrev_b32_sdwa v64, 4, v226 dst_sel:DWORD dst_unused:UNUSED_PAD src0_sel:DWORD src1_sel:WORD_0\n" \
  "ds_read_b128 v[68:71], v64\n" \
  "v_lshlrev_b32_sdwa v65, 4, v226 dst_sel:DWORD dst_unused:UNUSED_PAD src0_sel:DWORD src1_sel:WORD_1\n" \
  "ds_read_b128 v[72:75], v65\n" \
  "v_lshlrev_b32_sdwa v66, 4, v227 dst_sel:DWORD dst_unused:UNUSED_PAD src0_sel:DWORD src1_sel:WORD_0\n" \
  "ds_read_b128 v[76:79], v66\n" \
  "v_lshlrev_b32_sdwa v67, 4, v227 dst_sel:DWORD dst_unused:UNUSED_PAD src0_sel:DWORD src1_sel:WORD_1\n" \
  "ds_read_b128 v[80:83], v67\n" \
  "s_add_u32 m0, s58, 0x4000\n" \
  "s_cmp_lt_u32 s35, 0x28000\n" \
  "global_load_lds_dwordx4 v189, s[54:55] sc1\n" \
  "global_load_lds_dwordx4 v157, s[54:55] offset:1024 sc1\n" \
  "s_cselect_b32 s58, s35, s59\n" \
  "v_lshlrev_b32_sdwa v64, 4, v228 dst_sel:DWORD dst_unused:UNUSED_PAD src0_sel:DWORD src1_sel:WORD_0\n" \
  "ds_read_b128 v[84:87], v64\n" \
  "v_lshlrev_b32_sdwa v65, 4, v228 dst_sel:DWORD dst_unused:UNUSED_PAD src0_sel:DWORD src1_sel:WORD_1\n" \
  "ds_read_b128 v[88:91], v65\n" \
  "v_lshlrev_b32_sdwa v66, 4, v229 dst_sel:DWORD dst_unused:UNUSED_PAD src0_sel:DWORD src1_sel:WORD_0\n" \
  "ds_read_b128 v[92:95], v66\n" \
  "v_lshlrev_b32_sdwa v67, 4, v229 dst_sel:DWORD dst_unused:UNUSED_PAD src0_sel:DWORD src1_sel:WORD_1\n" \
  "ds_read_b128 v[96:99], v67\n" \
  "s_waitcnt lgkmcnt(10)\n" \
  "v_smfmac_f32_16x16x64_bf16 v[0:3], v[180:183], v[100:107], v192\n" \
  "v_lshlrev_b32_sdwa v64, 4, v230 dst_sel:DWORD dst_unused:UNUSED_PAD src0_sel:DWORD src1_sel:WORD_0\n" \
  "ds_read_b128 v[100:103], v64\n" \
  "v_lshlrev_b32_sdwa v65, 4, v230 dst_sel:DWORD dst_unused:UNUSED_PAD src0_sel:DWORD src1_sel:WORD_1\n" \
  "ds_read_b128 v[104:107], v65\n" \
  "s_mov_b32 m0, s58\n" \
  "s_add_u32 s35, s58, 0x8000\n" \
  "global_load_lds_dwordx4 v190, s[54:55] sc1\n" \
  "global_load_lds_dwordx4 v158, s[54:55] offset:1024 sc1\n" \
  "s_waitcnt lgkmcnt(10)\n" \
  "v_smfmac_f32_16x16x64_bf16 v[4:7], v[180:183], v[108:115], v192\n" \
  "v_lshlrev_b32_sdwa v66, 4, v231 dst_sel:DWORD dst_unused:UNUSED_PAD src0_sel:DWORD src1_sel:WORD_0\n" \
  "ds_read_b128 v[108:111], v66\n" \
  "v_lshlrev_b32_sdwa v67, 4, v231 dst_sel:DWORD dst_unused:UNUSED_PAD src0_sel:DWORD src1_sel:WORD_1\n" \
  "ds_read_b128 v[112:115], v67\n" \
  "s_waitcnt lgkmcnt(10)\n" \
  "v_smfmac_f32_16x16x64_bf16 v[8:11], v[180:183], v[68:75], v192\n" \
  "v_lshlrev_b32_sdwa v64, 4, v232 dst_sel:DWORD dst_unused:UNUSED_PAD src0_sel:DWORD src1_sel:WORD_0\n" \
  "ds_read_b128 v[68:71], v64\n" \
  "v_lshlrev_b32_sdwa v65, 4, v232 dst_sel:DWORD dst_unused:UNUSED_PAD src0_sel:DWORD src1_sel:WORD_1\n" \
  "ds_read_b128 v[72:75], v65\n" \
  "s_add_u32 m0, s58, 0x4000\n" \
  "s_cmp_lt_u32 s35, 0x28000\n" \
  "global_load_lds_dwordx4 v191, s[54:55] sc1\n" \
  "global_load_lds_dwordx4 v159, s[54:55] offset:1024 sc1\n" \
  "s_cselect_b32 s58, s35, s59\n" \
  "s_waitcnt lgkmcnt(10)\n" \
  "v_smfmac_f32_16x16x64_bf16 v[12:15], v[180:183], v[76:83], v192\n" \
  "v_lshlrev_b32_sdwa v66, 4, v233 dst_sel:DWORD dst_unused:UNUSED_PAD src0_sel:DWORD src1_sel:WORD_0\n" \
  "ds_read_b128 v[76:79], v66\n" \
  "v_lshlrev_b32_sdwa v67, 4, v233 dst_sel:DWORD dst_unused:UNUSED_PAD src0_sel:DWORD src1_sel:WORD_1\n" \
  "ds_read_b128 v[80:83], v67\n" \
  "s_waitcnt lgkmcnt(10)\n" \
  "v_smfmac_f32_16x16x64_bf16 v[16:19], v[180:183], v[84:91], v192\n" \
  "v_lshlrev_b32_sdwa v64, 4, v234 dst_sel:DWORD dst_unused:UNUSED_PAD src0_sel:DWORD src1_sel:WORD_0\n" \
  "ds_read_b128 v[84:87], v64\n" \
  "v_lshlrev_b32_sdwa v65, 4, v234 dst_sel:DWORD dst_unused:UNUSED_PAD src0_sel:DWORD src1_sel:WORD_1\n" \
  "ds_read_b128 v[88:91], v65\n" \
  "global_load_dwordx4 v[208:211], v188, s[56:57] sc1\n" \
  "global_load_dwordx4 v[212:215], v188, s[56:57] offset:1024 sc1\n" \
  "s_waitcnt lgkmcnt(10)\n" \
  "v_smfmac_f32_16x16x64_bf16 v[20:23], v[180:183], v[92:99], v192\n" \
  "v_lshlrev_b32_sdwa v66, 4, v235 dst_sel:DWORD dst_unused:UNUSED_PAD src0_sel:DWORD src1_sel:WORD_0\n" \
  "ds_read_b128 v[92:95], v66\n" \
  "v_lshlrev_b32_sdwa v67, 4, v235 dst_sel:DWORD dst_unused:UNUSED_PAD src0_sel:DWORD src1_sel:WORD_1\n" \
  "ds_read_b128 v[96:99], v67\n" \
  "s_waitcnt lgkmcnt(10)\n" \
  "v_smfmac_f32_16x16x64_bf16 v[24:27], v[180:183], v[100:107], v192\n" \
  "v_lshlrev_b32_sdwa v64, 4, v236 dst_sel:DWORD dst_unused:UNUSED_PAD src0_sel:DWORD src1_sel:WORD_0\n" \
  "ds_read_b128 v[100:103], v64\n" \
  "v_lshlrev_b32_sdwa v65, 4, v236 dst_sel:DWORD dst_unused:UNUSED_PAD src0_sel:DWORD src1_sel:WORD_1\n" \
  "ds_read_b128 v[104:107], v65\n" \
  "global_load_dwordx4 v[216:219], v188, s[56:57] offset:2048 sc1\n" \
  "global_load_dwordx4 v[220:223], v188, s[56:57] offset:3072 sc1\n" \
  "s_waitcnt lgkmcnt(10)\n" \
  "v_smfmac_f32_16x16x64_bf16 v[28:31], v[180:183], v[108:115], v192\n" \
  "v_lshlrev_b32_sdwa v66, 4, v237 dst_sel:DWORD dst_unused:UNUSED_PAD src0_sel:DWORD src1_sel:WORD_0\n" \
  "ds_read_b128 v[108:111], v66\n" \
  "v_lshlrev_b32_sdwa v67, 4, v237 dst_sel:DWORD dst_unused:UNUSED_PAD src0_sel:DWORD src1_sel:WORD_1\n" \
  "ds_read_b128 v[112:115], v67\n" \
  "s_waitcnt lgkmcnt(10)\n" \
  "v_smfmac_f32_16x16x64_bf16 v[32:35], v[180:183], v[68:75], v192\n" \
  "v_lshlrev_b32_sdwa v64, 4, v238 dst_sel:DWORD dst_unused:UNUSED_PAD src0_sel:DWORD src1_sel:WORD_0\n" \
  "ds_read_b128 v[68:71], v64\n" \
  "v_lshlrev_b32_sdwa v65, 4, v238 dst_sel:DWORD dst_unused:UNUSED_PAD src0_sel:DWORD src1_sel:WORD_1\n" \
  "ds_read_b128 v[72:75], v65\n" \
  "s_waitcnt lgkmcnt(10)\n" \
  "v_smfmac_f32_16x16x64_bf16 v[36:39], v[180:183], v[76:83], v192\n" \
  "v_lshlrev_b32_sdwa v66, 4, v239 dst_sel:DWORD dst_unused:UNUSED_PAD src0_sel:DWORD src1_sel:WORD_0\n" \
  "ds_read_b128 v[76:79], v66\n" \
  "v_lshlrev_b32_sdwa v67, 4, v239 dst_sel:DWORD dst_unused:UNUSED_PAD src0_sel:DWORD src1_sel:WORD_1\n" \
  "ds_read_b128 v[80:83], v67\n" \
  "s_add_u32 s56, s56, 32768\n" \
  "s_addc_u32 s57, s57, 0\n" \
  "s_add_u32 s54, s54, 64000\n" \
  "s_addc_u32 s55, s55, 0\n" \
  "s_waitcnt lgkmcnt(10)\n" \
  "v_smfmac_f32_16x16x64_bf16 v[40:43], v[180:183], v[84:91], v192\n" \
  "s_waitcnt lgkmcnt(8)\n" \
  "v_smfmac_f32_16x16x64_bf16 v[44:47], v[180:183], v[92:99], v192\n" \
  "s_waitcnt vmcnt(8)\n" \
  "s_barrier\n" \
  "v_lshlrev_b32_sdwa v64, 4, v240 dst_sel:DWORD dst_unused:UNUSED_PAD src0_sel:DWORD src1_sel:WORD_0\n" \
  "ds_read_b128 v[84:87], v64\n" \
  "v_lshlrev_b32_sdwa v65, 4, v240 dst_sel:DWORD dst_unused:UNUSED_PAD src0_sel:DWORD src1_sel:WORD_1\n" \
  "ds_read_b128 v[88:91], v65\n" \
  "v_lshlrev_b32_sdwa v66, 4, v241 dst_sel:DWORD dst_unused:UNUSED_PAD src0_sel:DWORD src1_sel:WORD_0\n" \
  "ds_read_b128 v[92:95], v66\n" \
  "v_lshlrev_b32_sdwa v67, 4, v241 dst_sel:DWORD dst_unused:UNUSED_PAD src0_sel:DWORD src1_sel:WORD_1\n" \
  "ds_read_b128 v[96:99], v67\n" \
  "s_waitcnt lgkmcnt(10)\n" \
  "v_smfmac_f32_16x16x64_bf16 v[48:51], v[180:183], v[100:107], v192\n" \
  "s_waitcnt lgkmcnt(8)\n" \
  "v_smfmac_f32_16x16x64_bf16 v[52:55], v[180:183], v[108:115], v192\n" \
  "s_waitcnt lgkmcnt(6)\n" \
  "v_smfmac_f32_16x16x64_bf16 v[56:59], v[180:183], v[68:75], v192\n" \
  "s_waitcnt lgkmcnt(4)\n" \
  "v_smfmac_f32_16x16x64_bf16 v[60:63], v[180:183], v[76:83], v192\n" \
  "s_barrier\n" \
  "s_mov_b32 m0, s58\n" \
  "s_add_u32 s35, s58, 0x8000\n" \
  "global_load_lds_dwordx4 v188, s[54:55] sc1\n" \
  "global_load_lds_dwordx4 v156, s[54:55] offset:1024 sc1\n" \
  "v_lshlrev_b32_sdwa v64, 4, v242 dst_sel:DWORD dst_unused:UNUSED_PAD src0_sel:DWORD src1_sel:WORD_0\n" \
  "ds_read_b128 v[100:103], v64\n" \
  "v_lshlrev_b32_sdwa v65, 4, v242 dst_sel:DWORD dst_unused:UNUSED_PAD src0_sel:DWORD src1_sel:WORD_1\n" \
  "ds_read_b128 v[104:107], v65\n" \
  "v_lshlrev_b32_sdwa v66, 4, v243 dst_sel:DWORD dst_unused:UNUSED_PAD src0_sel:DWORD src1_sel:WORD_0\n" \
  "ds_read_b128 v[108:111], v66\n" \
  "v_lshlrev_b32_sdwa v67, 4, v243 dst_sel:DWORD dst_unused:UNUSED_PAD src0_sel:DWORD src1_sel:WORD_1\n" \
  "ds_read_b128 v[112:115], v67\n" \
  "s_add_u32 m0, s58, 0x4000\n" \
  "s_cmp_lt_u32 s35, 0x28000\n" \
  "global_load_lds_dwordx4 v189, s[54:55] sc1\n" \
  "global_load_lds_dwordx4 v157, s[54:55] offset:1024 sc1\n" \
  "s_cselect_b32 s58, s35, s59\n" \
  "v_lshlrev_b32_sdwa v64, 4, v244 dst_sel:DWORD dst_unused:UNUSED_PAD src0_sel:DWORD src1_sel:WORD_0\n" \
  "ds_read_b128 v[68:71], v64\n" \
  "v_lshlrev_b32_sdwa v65, 4, v244 dst_sel:DWORD dst_unused:UNUSED_PAD src0_sel:DWORD src1_sel:WORD_1\n" \
  "ds_read_b128 v[72:75], v65\n" \
  "v_lshlrev_b32_sdwa v66, 4, v245 dst_sel:DWORD dst_unused:UNUSED_PAD src0_sel:DWORD src1_sel:WORD_0\n" \
  "ds_read_b128 v[76:79], v66\n" \
  "v_lshlrev_b32_sdwa v67, 4, v245 dst_sel:DWORD dst_unused:UNUSED_PAD src0_sel:DWORD src1_sel:WORD_1\n" \
  "ds_read_b128 v[80:83], v67\n" \
  "s_waitcnt lgkmcnt(10)\n" \
  "v_smfmac_f32_16x16x64_bf16 v[0:3], v[180:183], v[84:91], v192\n" \
  "v_lshlrev_b32_sdwa v64, 4, v246 dst_sel:DWORD dst_unused:UNUSED_PAD src0_sel:DWORD src1_sel:WORD_0\n" \
  "ds_read_b128 v[84:87], v64\n" \
  "v_lshlrev_b32_sdwa v65, 4, v246 dst_sel:DWORD dst_unused:UNUSED_PAD src0_sel:DWORD src1_sel:WORD_1\n" \
  "ds_read_b128 v[88:91], v65\n" \
  "s_mov_b32 m0, s58\n" \
  "s_add_u32 s35, s58, 0x8000\n" \
  "global_load_lds_dwordx4 v190, s[54:55] sc1\n" \
  "global_load_lds_dwordx4 v158, s[54:55] offset:1024 sc1\n" \
  "s_waitcnt lgkmcnt(10)\n" \
  "v_smfmac_f32_16x16x64_bf16 v[4:7], v[180:183], v[92:99], v192\n" \
  "v_lshlrev_b32_sdwa v66, 4, v247 dst_sel:DWORD dst_unused:UNUSED_PAD src0_sel:DWORD src1_sel:WORD_0\n" \
  "ds_read_b128 v[92:95], v66\n" \
  "v_lshlrev_b32_sdwa v67, 4, v247 dst_sel:DWORD dst_unused:UNUSED_PAD src0_sel:DWORD src1_sel:WORD_1\n" \
  "ds_read_b128 v[96:99], v67\n" \
  "s_waitcnt lgkmcnt(10)\n" \
  "v_smfmac_f32_16x16x64_bf16 v[8:11], v[180:183], v[100:107], v192\n" \
  "v_lshlrev_b32_sdwa v64, 4, v248 dst_sel:DWORD dst_unused:UNUSED_PAD src0_sel:DWORD src1_sel:WORD_0\n" \
  "ds_read_b128 v[100:103], v64\n" \
  "v_lshlrev_b32_sdwa v65, 4, v248 dst_sel:DWORD dst_unused:UNUSED_PAD src0_sel:DWORD src1_sel:WORD_1\n" \
  "ds_read_b128 v[104:107], v65\n" \
  "s_add_u32 m0, s58, 0x4000\n" \
  "s_cmp_lt_u32 s35, 0x28000\n" \
  "global_load_lds_dwordx4 v191, s[54:55] sc1\n" \
  "global_load_lds_dwordx4 v159, s[54:55] offset:1024 sc1\n" \
  "s_cselect_b32 s58, s35, s59\n" \
  "s_waitcnt lgkmcnt(10)\n" \
  "v_smfmac_f32_16x16x64_bf16 v[12:15], v[180:183], v[108:115], v192\n" \
  "v_lshlrev_b32_sdwa v66, 4, v249 dst_sel:DWORD dst_unused:UNUSED_PAD src0_sel:DWORD src1_sel:WORD_0\n" \
  "ds_read_b128 v[108:111], v66\n" \
  "v_lshlrev_b32_sdwa v67, 4, v249 dst_sel:DWORD dst_unused:UNUSED_PAD src0_sel:DWORD src1_sel:WORD_1\n" \
  "ds_read_b128 v[112:115], v67\n" \
  "s_waitcnt lgkmcnt(10)\n" \
  "v_smfmac_f32_16x16x64_bf16 v[16:19], v[180:183], v[68:75], v192\n" \
  "v_lshlrev_b32_sdwa v64, 4, v250 dst_sel:DWORD dst_unused:UNUSED_PAD src0_sel:DWORD src1_sel:WORD_0\n" \
  "ds_read_b128 v[68:71], v64\n" \
  "v_lshlrev_b32_sdwa v65, 4, v250 dst_sel:DWORD dst_unused:UNUSED_PAD src0_sel:DWORD src1_sel:WORD_1\n" \
  "ds_read_b128 v[72:75], v65\n" \
  "global_load_dwordx4 v[224:227], v188, s[56:57] sc1\n" \
  "global_load_dwordx4 v[228:231], v188, s[56:57] offset:1024 sc1\n" \
  "s_waitcnt lgkmcnt(10)\n" \
  "v_smfmac_f32_16x16x64_bf16 v[20:23], v[180:183], v[76:83], v192\n" \
  "v_lshlrev_b32_sdwa v66, 4, v251 dst_sel:DWORD dst_unused:UNUSED_PAD src0_sel:DWORD src1_sel:WORD_0\n" \
  "ds_read_b128 v[76:79], v66\n" \
  "v_lshlrev_b32_sdwa v67, 4, v251 dst_sel:DWORD dst_unused:UNUSED_PAD src0_sel:DWORD src1_sel:WORD_1\n" \
  "ds_read_b128 v[80:83], v67\n" \
  "s_waitcnt lgkmcnt(10)\n" \
  "v_smfmac_f32_16x16x64_bf16 v[24:27], v[180:183], v[84:91], v192\n" \
  "v_lshlrev_b32_sdwa v64, 4, v252 dst_sel:DWORD dst_unused:UNUSED_PAD src0_sel:DWORD src1_sel:WORD_0\n" \
  "ds_read_b128 v[84:87], v64\n" \
  "v_lshlrev_b32_sdwa v65, 4, v252 dst_sel:DWORD dst_unused:UNUSED_PAD src0_sel:DWORD src1_sel:WORD_1\n" \
  "ds_read_b128 v[88:91], v65\n" \
  "global_load_dwordx4 v[232:235], v188, s[56:57] offset:2048 sc1\n" \
  "global_load_dwordx4 v[236:239], v188, s[56:57] offset:3072 sc1\n" \
  "s_waitcnt lgkmcnt(10)\n" \
  "v_smfmac_f32_16x16x64_bf16 v[28:31], v[180:183], v[92:99], v192\n" \
  "v_lshlrev_b32_sdwa v66, 4, v253 dst_sel:DWORD dst_unused:UNUSED_PAD src0_sel:DWORD src1_sel:WORD_0\n" \
  "ds_read_b128 v[92:95], v66\n" \
  "v_lshlrev_b32_sdwa v67, 4, v253 dst_sel:DWORD dst_unused:UNUSED_PAD src0_sel:DWORD src1_sel:WORD_1\n" \
  "ds_read_b128 v[96:99], v67\n" \
  "s_waitcnt lgkmcnt(10)\n" \
  "v_smfmac_f32_16x16x64_bf16 v[32:35], v[180:183], v[100:107], v192\n" \
  "v_lshlrev_b32_sdwa v64, 4, v254 dst_sel:DWORD dst_unused:UNUSED_PAD src0_sel:DWORD src1_sel:WORD_0\n" \
  "ds_read_b128 v[100:103], v64\n" \
  "v_lshlrev_b32_sdwa v65, 4, v254 dst_sel:DWORD dst_unused:UNUSED_PAD src0_sel:DWORD src1_sel:WORD_1\n" \
  "ds_read_b128 v[104:107], v65\n" \
  "s_waitcnt lgkmcnt(10)\n" \
  "v_smfmac_f32_16x16x64_bf16 v[36:39], v[180:183], v[108:115], v192\n" \
  "v_lshlrev_b32_sdwa v66, 4, v255 dst_sel:DWORD dst_unused:UNUSED_PAD src0_sel:DWORD src1_sel:WORD_0\n" \
  "ds_read_b128 v[108:111], v66\n" \
  "v_lshlrev_b32_sdwa v67, 4, v255 dst_sel:DWORD dst_unused:UNUSED_PAD src0_sel:DWORD src1_sel:WORD_1\n" \
  "ds_read_b128 v[112:115], v67\n" \
  "s_add_u32 s56, s56, 32768\n" \
  "s_addc_u32 s57, s57, 0\n" \
  "s_add_u32 s54, s54, 64000\n" \
  "s_addc_u32 s55, s55, 0\n" \
  "s_lshl3_add_u32 s36, s27, s51\n" \
  "s_cmp_eq_u32 s36, 37\n" \
  "s_waitcnt lgkmcnt(10)\n" \
  "v_smfmac_f32_16x16x64_bf16 v[40:43], v[180:183], v[68:75], v192\n" \
  "s_waitcnt lgkmcnt(8)\n" \
  "v_smfmac_f32_16x16x64_bf16 v[44:47], v[180:183], v[76:83], v192\n" \
  "s_waitcnt vmcnt(8)\n" \
  "s_cbranch_scc1 LG_pol\n" \
  "LG_rt2:\n" \
  "s_barrier\n" \
  "v_lshlrev_b32_sdwa v64, 4, v208 dst_sel:DWORD dst_unused:UNUSED_PAD src0_sel:DWORD src1_sel:WORD_0\n" \
  "ds_read_b128 v[68:71], v64\n" \
  "v_lshlrev_b32_sdwa v65, 4, v208 dst_sel:DWORD dst_unused:UNUSED_PAD src0_sel:DWORD src1_sel:WORD_1\n" \
  "ds_read_b128 v[72:75], v65\n" \
  "v_lshlrev_b32_sdwa v66, 4, v209 dst_sel:DWORD dst_unused:UNUSED_PAD src0_sel:DWORD src1_sel:WORD_0\n" \
  "ds_read_b128 v[76:79], v66\n" \
  "v_lshlrev_b32_sdwa v67, 4, v209 dst_sel:DWORD dst_unused:UNUSED_PAD src0_sel:DWORD src1_sel:WORD_1\n" \
  "ds_read_b128 v[80:83], v67\n" \
  "s_waitcnt lgkmcnt(10)\n" \
  "v_smfmac_f32_16x16x64_bf16 v[48:51], v[180:183], v[84:91], v192\n" \
  "s_waitcnt lgkmcnt(8)\n" \
  "v_smfmac_f32_16x16x64_bf16 v[52:55], v[180:183], v[92:99], v192\n" \
  "s_waitcnt lgkmcnt(6)\n" \
  "v_smfmac_f32_16x16x64_bf16 v[56:59], v[180:183], v[100:107], v192\n" \
  "s_waitcnt lgkmcnt(4)\n" \
  "v_smfmac_f32_16x16x64_bf16 v[60:63], v[180:183], v[108:115], v192\n" \
  "s_barrier\n" \
  "s_mov_b32 m0, s58\n" \
  "s_add_u32 s35, s58, 0x8000\n" \
  "global_load_lds_dwordx4 v188, s[54:55] sc1\n" \
  "global_load_lds_dwordx4 v156, s[54:55] offset:1024 sc1\n" \
  "v_lshlrev_b32_sdwa v64, 4, v210 dst_sel:DWORD dst_unused:UNUSED_PAD src0_sel:DWORD src1_sel:WORD_0\n" \
  "ds_read_b128 v[84:87], v64\n" \
  "v_lshlrev_b32_sdwa v65, 4, v210 dst_sel:DWORD dst_unused:UNUSED_PAD src0_sel:DWORD src1_sel:WORD_1\n" \
  "ds_read_b128 v[88:91], v65\n" \
  "v_lshlrev_b32_sdwa v66, 4, v211 dst_sel:DWORD dst_unused:UNUSED_PAD src0_sel:DWORD src1_sel:WORD_0\n" \
  "ds_read_b128 v[92:95], v66\n" \
  "v_lshlrev_b32_sdwa v67, 4, v211 dst_sel:DWORD dst_unused:UNUSED_PAD src0_sel:DWORD src1_sel:WORD_1\n" \
  "ds_read_b128 v[96:99], v67\n" \
  "s_add_u32 m0, s58, 0x4000\n" \
  "s_cmp_lt_u32 s35, 0x28000\n" \
  "global_load_lds_dwordx4 v189, s[54:55] sc1\n" \
  "global_load_lds_dwordx4 v157, s[54:55] offset:1024 sc1\n" \
  "s_cselect_b32 s58, s35, s59\n" \
  "v_lshlrev_b32_sdwa v64, 4, v212 dst_sel:DWORD dst_unused:UNUSED_PAD src0_sel:DWORD src1_sel:WORD_0\n" \
  "ds_read_b128 v[100:103], v64\n" \
  "v_lshlrev_b32_sdwa v65, 4, v212 dst_sel:DWORD dst_unused:UNUSED_PAD src0_sel:DWORD src1_sel:WORD_1\n" \
  "ds_read_b128 v[104:107], v65\n" \
  "v_lshlrev_b32_sdwa v66, 4, v213 dst_sel:DWORD dst_unused:UNUSED_PAD src0_sel:DWORD src1_sel:WORD_0\n" \
  "ds_read_b128 v[108:111], v66\n" \
  "v_lshlrev_b32_sdwa v67, 4, v213 dst_sel:DWORD dst_unused:UNUSED_PAD src0_sel:DWORD src1_sel:WORD_1\n" \
  "ds_read_b128 v[112:115], v67\n" \
  "s_waitcnt lgkmcnt(10)\n" \
  "v_smfmac_f32_16x16x64_bf16 v[0:3], v[180:183], v[68:75], v192\n" \
  "v_lshlrev_b32_sdwa v64, 4, v214 dst_sel:DWORD dst_unused:UNUSED_PAD src0_sel:DWORD src1_sel:WORD_0\n" \
  "ds_read_b128 v[68:71], v64\n" \
  "v_lshlrev_b32_sdwa v65, 4, v214 dst_sel:DWORD dst_unused:UNUSED_PAD src0_sel:DWORD src1_sel:WORD_1\n" \
  "ds_read_b128 v[72:75], v65\n" \
  "s_mov_b32 m0, s58\n" \
  "s_add_u32 s35, s58, 0x8000\n" \
  "global_load_lds_dwordx4 v190, s[54:55] sc1\n" \
  "global_load_lds_dwordx4 v158, s[54:55] offset:1024 sc1\n" \
  "s_waitcnt lgkmcnt(10)\n" \
  "v_smfmac_f32_16x16x64_bf16 v[4:7], v[180:183], v[76:83], v192\n" \
  "v_lshlrev_b32_sdwa v66, 4, v215 dst_sel:DWORD dst_unused:UNUSED_PAD src0_sel:DWORD src1_sel:WORD_0\n" \
  "ds_read_b128 v[76:79], v66\n" \
  "v_lshlrev_b32_sdwa v67, 4, v215 dst_sel:DWORD dst_unused:UNUSED_PAD src0_sel:DWORD src1_sel:WORD_1\n" \
  "ds_read_b128 v[80:83], v67\n" \
  "s_waitcnt lgkmcnt(10)\n" \
  "v_smfmac_f32_16x16x64_bf16 v[8:11], v[180:183], v[84:91], v192\n" \
  "v_lshlrev_b32_sdwa v64, 4, v216 dst_sel:DWORD dst_unused:UNUSED_PAD src0_sel:DWORD src1_sel:WORD_0\n" \
  "ds_read_b128 v[84:87], v64\n" \
  "v_lshlrev_b32_sdwa v65, 4, v216 dst_sel:DWORD dst_unused:UNUSED_PAD src0_sel:DWORD src1_sel:WORD_1\n" \
  "ds_read_b128 v[88:91], v65\n" \
  "s_add_u32 m0, s58, 0x4000\n" \
  "s_cmp_lt_u32 s35, 0x28000\n" \
  "global_load_lds_dwordx4 v191, s[54:55] sc1\n" \
  "global_load_lds_dwordx4 v159, s[54:55] offset:1024 sc1\n" \
  "s_cselect_b32 s58, s35, s59\n" \
  "s_waitcnt lgkmcnt(10)\n" \
  "v_smfmac_f32_16x16x64_bf16 v[12:15], v[180:183], v[92:99], v192\n" \
  "v_lshlrev_b32_sdwa v66, 4, v217 dst_sel:DWORD dst_unused:UNUSED_PAD src0_sel:DWORD src1_sel:WORD_0\n" \
  "ds_read_b128 v[92:95], v66\n" \
  "v_lshlrev_b32_sdwa v67, 4, v217 dst_sel:DWORD dst_unused:UNUSED_PAD src0_sel:DWORD src1_sel:WORD_1\n" \
  "ds_read_b128 v[96:99], v67\n" \
  "s_waitcnt lgkmcnt(10)\n" \
  "v_smfmac_f32_16x16x64_bf16 v[16:19], v[180:183], v[100:107], v192\n" \
  "v_lshlrev_b32_sdwa v64, 4, v218 dst_sel:DWORD dst_unused:UNUSED_PAD src0_sel:DWORD src1_sel:WORD_0\n" \
  "ds_read_b128 v[100:103], v64\n" \
  "v_lshlrev_b32_sdwa v65, 4, v218 dst_sel:DWORD dst_unused:UNUSED_PAD src0_sel:DWORD src1_sel:WORD_1\n" \
  "ds_read_b128 v[104:107], v65\n" \
  "global_load_dwordx4 v[240:243], v188, s[56:57] sc1\n" \
  "global_load_dwordx4 v[244:247], v188, s[56:57] offset:1024 sc1\n" \
  "s_waitcnt lgkmcnt(10)\n" \
  "v_smfmac_f32_16x16x64_bf16 v[20:23], v[180:183], v[108:115], v192\n" \
  "v_lshlrev_b32_sdwa v66, 4, v219 dst_sel:DWORD dst_unused:UNUSED_PAD src0_sel:DWORD src1_sel:WORD_0\n" \
  "ds_read_b128 v[108:111], v66\n" \
  "v_lshlrev_b32_sdwa v67, 4, v219 dst_sel:DWORD dst_unused:UNUSED_PAD src0_sel:DWORD src1_sel:WORD_1\n" \
  "ds_read_b128 v[112:115], v67\n" \
  "s_waitcnt lgkmcnt(10)\n" \
  "v_smfmac_f32_16x16x64_bf16 v[24:27], v[180:183], v[68:75], v192\n" \
  "v_lshlrev_b32_sdwa v64, 4, v220 dst_sel:DWORD dst_unused:UNUSED_PAD src0_sel:DWORD src1_sel:WORD_0\n" \
  "ds_read_b128 v[68:71], v64\n" \
  "v_lshlrev_b32_sdwa v65, 4, v220 dst_sel:DWORD dst_unused:UNUSED_PAD src0_sel:DWORD src1_sel:WORD_1\n" \
  "ds_read_b128 v[72:75], v65\n" \
  "global_load_dwordx4 v[248:251], v188, s[56:57] offset:2048 sc1\n" \
  "global_load_dwordx4 v[252:255], v188, s[56:57] offset:3072 sc1\n" \
  "s_waitcnt lgkmcnt(10)\n" \
  "v_smfmac_f32_16x16x64_bf16 v[28:31], v[180:183], v[76:83], v192\n" \
  "v_lshlrev_b32_sdwa v66, 4, v221 dst_sel:DWORD dst_unused:UNUSED_PAD src0_sel:DWORD src1_sel:WORD_0\n" \
  "ds_read_b128 v[76:79], v66\n" \
  "v_lshlrev_b32_sdwa v67, 4, v221 dst_sel:DWORD dst_unused:UNUSED_PAD src0_sel:DWORD src1_sel:WORD_1\n" \
  "ds_read_b128 v[80:83], v67\n" \
  "s_waitcnt lgkmcnt(10)\n" \
  "v_smfmac_f32_16x16x64_bf16 v[32:35], v[180:183], v[84:91], v192\n" \
  "v_lshlrev_b32_sdwa v64, 4, v222 dst_sel:DWORD dst_unused:UNUSED_PAD src0_sel:DWORD src1_sel:WORD_0\n" \
  "ds_read_b128 v[84:87], v64\n" \
  "v_lshlrev_b32_sdwa v65, 4, v222 dst_sel:DWORD dst_unused:UNUSED_PAD src0_sel:DWORD src1_sel:WORD_1\n" \
  "ds_read_b128 v[88:91], v65\n" \
  "s_waitcnt lgkmcnt(10)\n" \
  "v_smfmac_f32_16x16x64_bf16 v[36:39], v[180:183], v[92:99], v192\n" \
  "v_lshlrev_b32_sdwa v66, 4, v223 dst_sel:DWORD dst_unused:UNUSED_PAD src0_sel:DWORD src1_sel:WORD_0\n" \
  "ds_read_b128 v[92:95], v66\n" \
  "v_lshlrev_b32_sdwa v67, 4, v223 dst_sel:DWORD dst_unused:UNUSED_PAD src0_sel:DWORD src1_sel:WORD_1\n" \
  "ds_read_b128 v[96:99], v67\n" \
  "s_add_u32 s56, s56, 32768\n" \
  "s_addc_u32 s57, s57, 0\n" \
  "s_add_u32 s54, s54, 64000\n" \
  "s_addc_u32 s55, s55, 0\n" \
  "s_sub_u32 s51, s51, 1\n" \
  "s_cmp_lg_u32 s51, 0\n" \
  "s_cbranch_scc1 LG_loop\n" \
  "s_waitcnt lgkmcnt(10)\n" \
  "v_smfmac_f32_16x16x64_bf16 v[40:43], v[180:183], v[100:107], v192\n" \
  "s_waitcnt lgkmcnt(8)\n" \
  "v_smfmac_f32_16x16x64_bf16 v[44:47], v[180:183], v[108:115], v192\n" \
  "s_waitcnt vmcnt(8)\n" \
  "s_barrier\n" \
  "v_lshlrev_b32_sdwa v64, 4, v224 dst_sel:DWORD dst_unused:UNUSED_PAD src0_sel:DWORD src1_sel:WORD_0\n" \
  "ds_read_b128 v[100:103], v64\n" \
  "v_lshlrev_b32_sdwa v65, 4, v224 dst_sel:DWORD dst_unused:UNUSED_PAD src0_sel:DWORD src1_sel:WORD_1\n" \
  "ds_read_b128 v[104:107], v65\n" \
  "v_lshlrev_b32_sdwa v66, 4, v225 dst_sel:DWORD dst_unused:UNUSED_PAD src0_sel:DWORD src1_sel:WORD_0\n" \
  "ds_read_b128 v[108:111], v66\n" \
  "v_lshlrev_b32_sdwa v67, 4, v225 dst_sel:DWORD dst_unused:UNUSED_PAD src0_sel:DWORD src1_sel:WORD_1\n" \
  "ds_read_b128 v[112:115], v67\n" \
  "s_waitcnt lgkmcnt(10)\n" \
  "v_smfmac_f32_16x16x64_bf16 v[48:51], v[180:183], v[68:75], v192\n" \
  "s_waitcnt lgkmcnt(8)\n" \
  "v_smfmac_f32_16x16x64_bf16 v[52:55], v[180:183], v[76:83], v192\n" \
  "s_waitcnt lgkmcnt(6)\n" \
  "v_smfmac_f32_16x16x64_bf16 v[56:59], v[180:183], v[84:91], v192\n" \
  "s_waitcnt lgkmcnt(4)\n" \
  "v_smfmac_f32_16x16x64_bf16 v[60:63], v[180:183], v[92:99], v192\n" \
  "s_barrier\n" \
  "s_mov_b32 m0, s58\n" \
  "s_add_u32 s35, s58, 0x8000\n" \
  "global_load_lds_dwordx4 v188, s[54:55] sc1\n" \
  "global_load_lds_dwordx4 v156, s[54:55] offset:1024 sc1\n" \
  "v_lshlrev_b32_sdwa v64, 4, v226 dst_sel:DWORD dst_unused:UNUSED_PAD src0_sel:DWORD src1_sel:WORD_0\n" \
  "ds_read_b128 v[68:71], v64\n" \
  "v_lshlrev_b32_sdwa v65, 4, v226 dst_sel:DWORD dst_unused:UNUSED_PAD src0_sel:DWORD src1_sel:WORD_1\n" \
  "ds_read_b128 v[72:75], v65\n" \
  "v_lshlrev_b32_sdwa v66, 4, v227 dst_sel:DWORD dst_unused:UNUSED_PAD src0_sel:DWORD src1_sel:WORD_0\n" \
  "ds_read_b128 v[76:79], v66\n" \
  "v_lshlrev_b32_sdwa v67, 4, v227 dst_sel:DWORD dst_unused:UNUSED_PAD src0_sel:DWORD src1_sel:WORD_1\n" \
  "ds_read_b128 v[80:83], v67\n" \
  "s_add_u32 m0, s58, 0x4000\n" \
  "s_cmp_lt_u32 s35, 0x28000\n" \
  "global_load_lds_dwordx4 v189, s[54:55] sc1\n" \
  "global_load_lds_dwordx4 v157, s[54:55] offset:1024 sc1\n" \
  "s_cselect_b32 s58, s35, s59\n" \
  "v_lshlrev_b32_sdwa v64, 4, v228 dst_sel:DWORD dst_unused:UNUSED_PAD src0_sel:DWORD src1_sel:WORD_0\n" \
  "ds_read_b128 v[84:87], v64\n" \
  "v_lshlrev_b32_sdwa v65, 4, v228 dst_sel:DWORD dst_unused:UNUSED_PAD src0_sel:DWORD src1_sel:WORD_1\n" \
  "ds_read_b128 v[88:91], v65\n" \
  "v_lshlrev_b32_sdwa v66, 4, v229 dst_sel:DWORD dst_unused:UNUSED_PAD src0_sel:DWORD src1_sel:WORD_0\n" \
  "ds_read_b128 v[92:95], v66\n" \
  "v_lshlrev_b32_sdwa v67, 4, v229 dst_sel:DWORD dst_unused:UNUSED_PAD src0_sel:DWORD src1_sel:WORD_1\n" \
  "ds_read_b128 v[96:99], v67\n" \
  "s_waitcnt lgkmcnt(10)\n" \
  "v_smfmac_f32_16x16x64_bf16 v[0:3], v[180:183], v[100:107], v192\n" \
  "v_lshlrev_b32_sdwa v64, 4, v230 dst_sel:DWORD dst_unused:UNUSED_PAD src0_sel:DWORD src1_sel:WORD_0\n" \
  "ds_read_b128 v[100:103], v64\n" \
  "v_lshlrev_b32_sdwa v65, 4, v230 dst_sel:DWORD dst_unused:UNUSED_PAD src0_sel:DWORD src1_sel:WORD_1\n" \
  "ds_read_b128 v[104:107], v65\n" \
  "s_mov_b32 m0, s58\n" \
  "s_add_u32 s35, s58, 0x8000\n" \
  "global_load_lds_dwordx4 v190, s[54:55] sc1\n" \
  "global_load_lds_dwordx4 v158, s[54:55] offset:1024 sc1\n" \
  "s_waitcnt lgkmcnt(10)\n" \
  "v_smfmac_f32_16x16x64_bf16 v[4:7], v[180:183], v[108:115], v192\n" \
  "v_lshlrev_b32_sdwa v66, 4, v231 dst_sel:DWORD dst_unused:UNUSED_PAD src0_sel:DWORD src1_sel:WORD_0\n" \
  "ds_read_b128 v[108:111], v66\n" \
  "v_lshlrev_b32_sdwa v67, 4, v231 dst_sel:DWORD dst_unused:UNUSED_PAD src0_sel:DWORD src1_sel:WORD_1\n" \
  "ds_read_b128 v[112:115], v67\n" \
  "s_waitcnt lgkmcnt(10)\n" \
  "v_smfmac_f32_16x16x64_bf16 v[8:11], v[180:183], v[68:75], v192\n" \
  "v_lshlrev_b32_sdwa v64, 4, v232 dst_sel:DWORD dst_unused:UNUSED_PAD src0_sel:DWORD src1_sel:WORD_0\n" \
  "ds_read_b128 v[68:71], v64\n" \
  "v_lshlrev_b32_sdwa v65, 4, v232 dst_sel:DWORD dst_unused:UNUSED_PAD src0_sel:DWORD src1_sel:WORD_1\n" \
  "ds_read_b128 v[72:75], v65\n" \
  "s_add_u32 m0, s58, 0x4000\n" \
  "s_cmp_lt_u32 s35, 0x28000\n" \
  "global_load_lds_dwordx4 v191, s[54:55] sc1\n" \
  "global_load_lds_dwordx4 v159, s[54:55] offset:1024 sc1\n" \
  "s_cselect_b32 s58, s35, s59\n" \
  "s_waitcnt lgkmcnt(10)\n" \
  "v_smfmac_f32_16x16x64_bf16 v[12:15], v[180:183], v[76:83], v192\n" \
  "v_lshlrev_b32_sdwa v66, 4, v233 dst_sel:DWORD dst_unused:UNUSED_PAD src0_sel:DWORD src1_sel:WORD_0\n" \
  "ds_read_b128 v[76:79], v66\n" \
  "v_lshlrev_b32_sdwa v67, 4, v233 dst_sel:DWORD dst_unused:UNUSED_PAD src0_sel:DWORD src1_sel:WORD_1\n" \
  "ds_read_b128 v[80:83], v67\n" \
  "s_waitcnt lgkmcnt(10)\n" \
  "v_smfmac_f32_16x16x64_bf16 v[16:19], v[180:183], v[84:91], v192\n" \
  "v_lshlrev_b32_sdwa v64, 4, v234 dst_sel:DWORD dst_unused:UNUSED_PAD src0_sel:DWORD src1_sel:WORD_0\n" \
  "ds_read_b128 v[84:87], v64\n" \
  "v_lshlrev_b32_sdwa v65, 4, v234 dst_sel:DWORD dst_unused:UNUSED_PAD src0_sel:DWORD src1_sel:WORD_1\n" \
  "ds_read_b128 v[88:91], v65\n" \
  "global_load_dwordx4 v[208:211], v188, s[56:57] sc1\n" \
  "global_load_dwordx4 v[212:215], v188, s[56:57] offset:1024 sc1\n" \
  "s_waitcnt lgkmcnt(10)\n" \
  "v_smfmac_f32_16x16x64_bf16 v[20:23], v[180:183], v[92:99], v192\n" \
  "v_lshlrev_b32_sdwa v66, 4, v235 dst_sel:DWORD dst_unused:UNUSED_PAD src0_sel:DWORD src1_sel:WORD_0\n" \
  "ds_read_b128 v[92:95], v66\n" \
  "v_lshlrev_b32_sdwa v67, 4, v235 dst_sel:DWORD dst_unused:UNUSED_PAD src0_sel:DWORD src1_sel:WORD_1\n" \
  "ds_read_b128 v[96:99], v67\n" \
  "s_waitcnt lgkmcnt(10)\n" \
  "v_smfmac_f32_16x16x64_bf16 v[24:27], v[180:183], v[100:107], v192\n" \
  "v_lshlrev_b32_sdwa v64, 4, v236 dst_sel:DWORD dst_unused:UNUSED_PAD src0_sel:DWORD src1_sel:WORD_0\n" \
  "ds_read_b128 v[100:103], v64\n" \
  "v_lshlrev_b32_sdwa v65, 4, v236 dst_sel:DWORD dst_unused:UNUSED_PAD src0_sel:DWORD src1_sel:WORD_1\n" \
  "ds_read_b128 v[104:107], v65\n" \
  "global_load_dwordx4 v[216:219], v188, s[56:57] offset:2048 sc1\n" \
  "global_load_dwordx4 v[220:223], v188, s[56:57] offset:3072 sc1\n" \
  "s_waitcnt lgkmcnt(10)\n" \
  "v_smfmac_f32_16x16x64_bf16 v[28:31], v[180:183], v[108:115], v192\n" \
  "v_lshlrev_b32_sdwa v66, 4, v237 dst_sel:DWORD dst_unused:UNUSED_PAD src0_sel:DWORD src1_sel:WORD_0\n" \
  "ds_read_b128 v[108:111], v66\n" \
  "v_lshlrev_b32_sdwa v67, 4, v237 dst_sel:DWORD dst_unused:UNUSED_PAD src0_sel:DWORD src1_sel:WORD_1\n" \
  "ds_read_b128 v[112:115], v67\n" \
  "s_waitcnt lgkmcnt(10)\n" \
  "v_smfmac_f32_16x16x64_bf16 v[32:35], v[180:183], v[68:75], v192\n" \
  "v_lshlrev_b32_sdwa v64, 4, v238 dst_sel:DWORD dst_unused:UNUSED_PAD src0_sel:DWORD src1_sel:WORD_0\n" \
  "ds_read_b128 v[68:71], v64\n" \
  "v_lshlrev_b32_sdwa v65, 4, v238 dst_sel:DWORD dst_unused:UNUSED_PAD src0_sel:DWORD src1_sel:WORD_1\n" \
  "ds_read_b128 v[72:75], v65\n" \
  "s_waitcnt lgkmcnt(10)\n" \
  "v_smfmac_f32_16x16x64_bf16 v[36:39], v[180:183], v[76:83], v192\n" \
  "v_lshlrev_b32_sdwa v66, 4, v239 dst_sel:DWORD dst_unused:UNUSED_PAD src0_sel:DWORD src1_sel:WORD_0\n" \
  "ds_read_b128 v[76:79], v66\n" \
  "v_lshlrev_b32_sdwa v67, 4, v239 dst_sel:DWORD dst_unused:UNUSED_PAD src0_sel:DWORD src1_sel:WORD_1\n" \
  "ds_read_b128 v[80:83], v67\n" \
  "s_add_u32 s56, s56, 32768\n" \
  "s_addc_u32 s57, s57, 0\n" \
  "s_add_u32 s54, s54, 64000\n" \
  "s_addc_u32 s55, s55, 0\n" \
  "s_waitcnt lgkmcnt(10)\n" \
  "v_smfmac_f32_16x16x64_bf16 v[40:43], v[180:183], v[84:91], v192\n" \
  "s_waitcnt lgkmcnt(8)\n" \
  "v_smfmac_f32_16x16x64_bf16 v[44:47], v[180:183], v[92:99], v192\n" \
  "s_waitcnt vmcnt(8)\n" \
  "s_barrier\n" \
  "v_lshlrev_b32_sdwa v64, 4, v240 dst_sel:DWORD dst_unused:UNUSED_PAD src0_sel:DWORD src1_sel:WORD_0\n" \
  "ds_read_b128 v[84:87], v64\n" \
  "v_lshlrev_b32_sdwa v65, 4, v240 dst_sel:DWORD dst_unused:UNUSED_PAD src0_sel:DWORD src1_sel:WORD_1\n" \
  "ds_read_b128 v[88:91], v65\n" \
  "v_lshlrev_b32_sdwa v66, 4, v241 dst_sel:DWORD dst_unused:UNUSED_PAD src0_sel:DWORD src1_sel:WORD_0\n" \
  "ds_read_b128 v[92:95], v66\n" \
  "v_lshlrev_b32_sdwa v67, 4, v241 dst_sel:DWORD dst_unused:UNUSED_PAD src0_sel:DWORD src1_sel:WORD_1\n" \
  "ds_read_b128 v[96:99], v67\n" \
  "s_waitcnt lgkmcnt(10)\n" \
  "v_smfmac_f32_16x16x64_bf16 v[48:51], v[180:183], v[100:107], v192\n" \
  "s_waitcnt lgkmcnt(8)\n" \
  "v_smfmac_f32_16x16x64_bf16 v[52:55], v[180:183], v[108:115], v192\n" \
  "s_waitcnt lgkmcnt(6)\n" \
  "v_smfmac_f32_16x16x64_bf16 v[56:59], v[180:183], v[68:75], v192\n" \
  "s_waitcnt lgkmcnt(4)\n" \
  "v_smfmac_f32_16x16x64_bf16 v[60:63], v[180:183], v[76:83], v192\n" \
  "s_barrier\n" \
  "s_mov_b32 m0, s58\n" \
  "s_add_u32 s35, s58, 0x8000\n" \
  "global_load_lds_dwordx4 v188, s[54:55] sc1\n" \
  "global_load_lds_dwordx4 v188, s[54:55] offset:1024 sc1\n" \
  "s_add_u32 m0, s58, 0x4000\n" \
  "s_cmp_lt_u32 s35, 0x28000\n" \
  "global_load_lds_dwordx4 v189, s[54:55] sc1\n" \
  "global_load_lds_dwordx4 v189, s[54:55] offset:1024 sc1\n" \
  "s_cselect_b32 s58, s35, s59\n" \
  "v_lshlrev_b32 v120, 16, v206\n" \
  "v_and_b32 v121, 0xffff0000, v206\n" \
  "v_lshlrev_b32 v122, 16, v207\n" \
  "v_and_b32 v123, 0xffff0000, v207\n" \
  "v_mov_b32 v124, 0xc0135761\n" \
  "v_mov_b32 v125, 0xc0135761\n" \
  "s_mov_b32 s52, 0xbdd2d3e8\n" \
  "s_mov_b32 s53, s52\n" \
  "s_lshl2_add_u32 s35, s33, s34\n" \
  "s_lshl_b32 s35, s35, 14\n" \
  "s_add_u32 s35, s35, s59\n" \
  "s_add_u32 s35, s35, 105676800\n" \
  "s_add_u32 s40, s24, s35\n" \
  "s_addc_u32 s41, s25, 0\n" \
  "v_lshlrev_b32_sdwa v64, 4, v242 dst_sel:DWORD dst_unused:UNUSED_PAD src0_sel:DWORD src1_sel:WORD_0\n" \
  "ds_read_b128 v[100:103], v64\n" \
  "v_lshlrev_b32_sdwa v65, 4, v242 dst_sel:DWORD dst_unused:UNUSED_PAD src0_sel:DWORD src1_sel:WORD_1\n" \
  "ds_read_b128 v[104:107], v65\n" \
  "v_lshlrev_b32_sdwa v66, 4, v243 dst_sel:DWORD dst_unused:UNUSED_PAD src0_sel:DWORD src1_sel:WORD_0\n" \
  "ds_read_b128 v[108:111], v66\n" \
  "v_lshlrev_b32_sdwa v67, 4, v243 dst_sel:DWORD dst_unused:UNUSED_PAD src0_sel:DWORD src1_sel:WORD_1\n" \
  "ds_read_b128 v[112:115], v67\n" \
  "v_lshlrev_b32_sdwa v64, 4, v244 dst_sel:DWORD dst_unused:UNUSED_PAD src0_sel:DWORD src1_sel:WORD_0\n" \
  "ds_read_b128 v[68:71], v64\n" \
  "v_lshlrev_b32_sdwa v65, 4, v244 dst_sel:DWORD dst_unused:UNUSED_PAD src0_sel:DWORD src1_sel:WORD_1\n" \
  "ds_read_b128 v[72:75], v65\n" \
  "v_lshlrev_b32_sdwa v66, 4, v245 dst_sel:DWORD dst_unused:UNUSED_PAD src0_sel:DWORD src1_sel:WORD_0\n" \
  "ds_read_b128 v[76:79], v66\n" \
  "v_lshlrev_b32_sdwa v67, 4, v245 dst_sel:DWORD dst_unused:UNUSED_PAD src0_sel:DWORD src1_sel:WORD_1\n" \
  "ds_read_b128 v[80:83], v67\n" \
  "s_waitcnt lgkmcnt(10)\n" \
  "v_smfmac_f32_16x16x64_bf16 v[0:3], v[180:183], v[84:91], v192\n" \
  "v_lshlrev_b32_sdwa v64, 4, v246 dst_sel:DWORD dst_unused:UNUSED_PAD src0_sel:DWORD src1_sel:WORD_0\n" \
  "ds_read_b128 v[84:87], v64\n" \
  "v_lshlrev_b32_sdwa v65, 4, v246 dst_sel:DWORD dst_unused:UNUSED_PAD src0_sel:DWORD src1_sel:WORD_1\n" \
  "ds_read_b128 v[88:91], v65\n" \
  "s_waitcnt lgkmcnt(10)\n" \
  "v_smfmac_f32_16x16x64_bf16 v[4:7], v[180:183], v[92:99], v192\n" \
  "v_lshlrev_b32_sdwa v66, 4, v247 dst_sel:DWORD dst_unused:UNUSED_PAD src0_sel:DWORD src1_sel:WORD_0\n" \
  "ds_read_b128 v[92:95], v66\n" \
  "v_lshlrev_b32_sdwa v67, 4, v247 dst_sel:DWORD dst_unused:UNUSED_PAD src0_sel:DWORD src1_sel:WORD_1\n" \
  "ds_read_b128 v[96:99], v67\n" \
  "s_waitcnt lgkmcnt(10)\n" \
  "v_smfmac_f32_16x16x64_bf16 v[8:11], v[180:183], v[100:107], v192\n" \
  "v_lshlrev_b32_sdwa v64, 4, v248 dst_sel:DWORD dst_unused:UNUSED_PAD src0_sel:DWORD src1_sel:WORD_0\n" \
  "ds_read_b128 v[100:103], v64\n" \
  "v_lshlrev_b32_sdwa v65, 4, v248 dst_sel:DWORD dst_unused:UNUSED_PAD src0_sel:DWORD src1_sel:WORD_1\n" \
  "ds_read_b128 v[104:107], v65\n" \
  "s_waitcnt lgkmcnt(10)\n" \
  "v_smfmac_f32_16x16x64_bf16 v[12:15], v[180:183], v[108:115], v192\n" \
  "v_lshlrev_b32_sdwa v66, 4, v249 dst_sel:DWORD dst_unused:UNUSED_PAD src0_sel:DWORD src1_sel:WORD_0\n" \
  "ds_read_b128 v[108:111], v66\n" \
  "v_lshlrev_b32_sdwa v67, 4, v249 dst_sel:DWORD dst_unused:UNUSED_PAD src0_sel:DWORD src1_sel:WORD_1\n" \
  "ds_read_b128 v[112:115], v67\n" \
  "s_waitcnt vmcnt(0)\n" \
  "s_barrier\n" \
  "s_waitcnt lgkmcnt(10)\n" \
  "v_smfmac_f32_16x16x64_bf16 v[16:19], v[180:183], v[68:75], v192\n" \
  "s_waitcnt lgkmcnt(8)\n" \
  "v_smfmac_f32_16x16x64_bf16 v[20:23], v[180:183], v[76:83], v192\n" \
  "s_waitcnt lgkmcnt(6)\n" \
  "v_smfmac_f32_16x16x64_bf16 v[24:27], v[180:183], v[84:91], v192\n" \
  "s_waitcnt lgkmcnt(4)\n" \
  "v_smfmac_f32_16x16x64_bf16 v[28:31], v[180:183], v[92:99], v192\n" \
  "v_lshlrev_b32_sdwa v64, 4, v208 dst_sel:DWORD dst_unused:UNUSED_PAD src0_sel:DWORD src1_sel:WORD_0\n" \
  "ds_read_b128 v[68:71], v64\n" \
  "v_lshlrev_b32_sdwa v65, 4, v208 dst_sel:DWORD dst_unused:UNUSED_PAD src0_sel:DWORD src1_sel:WORD_1\n" \
  "ds_read_b128 v[72:75], v65\n" \
  "s_waitcnt lgkmcnt(4)\n" \
  "v_smfmac_f32_16x16x64_bf16 v[32:35], v[180:183], v[100:107], v192\n" \
  "v_lshlrev_b32_sdwa v66, 4, v209 dst_sel:DWORD dst_unused:UNUSED_PAD src0_sel:DWORD src1_sel:WORD_0\n" \
  "ds_read_b128 v[76:79], v66\n" \
  "v_lshlrev_b32_sdwa v67, 4, v209 dst_sel:DWORD dst_unused:UNUSED_PAD src0_sel:DWORD src1_sel:WORD_1\n" \
  "ds_read_b128 v[80:83], v67\n" \
  "s_waitcnt lgkmcnt(4)\n" \
  "v_smfmac_f32_16x16x64_bf16 v[36:39], v[180:183], v[108:115], v192\n" \
  "v_lshlrev_b32_sdwa v64, 4, v210 dst_sel:DWORD dst_unused:UNUSED_PAD src0_sel:DWORD src1_sel:WORD_0\n" \
  "ds_read_b128 v[84:87], v64\n" \
  "v_lshlrev_b32_sdwa v65, 4, v210 dst_sel:DWORD dst_unused:UNUSED_PAD src0_sel:DWORD src1_sel:WORD_1\n" \
  "ds_read_b128 v[88:91], v65\n" \
  "s_waitcnt lgkmcnt(4)\n" \
  "v_smfmac_f32_16x16x64_bf16 v[0:3], v[180:183], v[68:75], v192\n" \
  "v_lshlrev_b32_sdwa v66, 4, v211 dst_sel:DWORD dst_unused:UNUSED_PAD src0_sel:DWORD src1_sel:WORD_0\n" \
  "ds_read_b128 v[92:95], v66\n" \
  "v_lshlrev_b32_sdwa v67, 4, v211 dst_sel:DWORD dst_unused:UNUSED_PAD src0_sel:DWORD src1_sel:WORD_1\n" \
  "ds_read_b128 v[96:99], v67\n" \
  "s_waitcnt lgkmcnt(4)\n" \
  "v_smfmac_f32_16x16x64_bf16 v[4:7], v[180:183], v[76:83], v192\n" \
  "v_lshlrev_b32_sdwa v64, 4, v212 dst_sel:DWORD dst_unused:UNUSED_PAD src0_sel:DWORD src1_sel:WORD_0\n" \
  "ds_read_b128 v[100:103], v64\n" \
  "v_lshlrev_b32_sdwa v65, 4, v212 dst_sel:DWORD dst_unused:UNUSED_PAD src0_sel:DWORD src1_sel:WORD_1\n" \
  "ds_read_b128 v[104:107], v65\n" \
  "v_pk_mul_f32 v[128:129], v[0:1], v[0:1]\n" \
  "v_pk_mul_f32 v[130:131], v[2:3], v[2:3]\n" \
  "v_pk_fma_f32 v[128:129], v[128:129], s[52:53], v[124:125]\n" \
  "v_pk_fma_f32 v[130:131], v[130:131], s[52:53], v[124:125]\n" \
  "v_pk_mul_f32 v[128:129], v[128:129], v[0:1]\n" \
  "v_pk_mul_f32 v[130:131], v[130:131], v[2:3]\n" \
  "v_exp_f32 v128, v128\n" \
  "v_exp_f32 v129, v129\n" \
  "v_pk_mul_f32 v[0:1], v[0:1], v[120:121]\n" \
  "v_exp_f32 v130, v130\n" \
  "v_exp_f32 v131, v131\n" \
  "v_pk_mul_f32 v[2:3], v[2:3], v[122:123]\n" \
  "v_pk_add_f32 v[128:129], v[128:129], 1.0 op_sel_hi:[1,0]\n" \
  "v_pk_add_f32 v[130:131], v[130:131], 1.0 op_sel_hi:[1,0]\n" \
  "v_rcp_f32 v128, v128\n" \
  "v_rcp_f32 v129, v129\n" \
  "s_waitcnt lgkmcnt(4)\n" \
  "v_smfmac_f32_16x16x64_bf16 v[8:11], v[180:183], v[84:91], v192\n" \
  "v_lshlrev_b32_sdwa v66, 4, v213 dst_sel:DWORD dst_unused:UNUSED_PAD src0_sel:DWORD src1_sel:WORD_0\n" \
  "ds_read_b128 v[108:111], v66\n" \
  "v_lshlrev_b32_sdwa v67, 4, v213 dst_sel:DWORD dst_unused:UNUSED_PAD src0_sel:DWORD src1_sel:WORD_1\n" \
  "ds_read_b128 v[112:115], v67\n" \
  "v_rcp_f32 v130, v130\n" \
  "v_rcp_f32 v131, v131\n" \
  "v_pk_mul_f32 v[0:1], v[0:1], v[128:129]\n" \
  "v_pk_fma_f32 v[0:1], v[2:3], v[130:131], v[0:1]\n" \
  "v_add_f32 v0, v0, v1\n" \
  "v_pk_mul_f32 v[132:133], v[4:5], v[4:5]\n" \
  "v_pk_mul_f32 v[134:135], v[6:7], v[6:7]\n" \
  "v_pk_fma_f32 v[132:133], v[132:133], s[52:53], v[124:125]\n" \
  "v_pk_fma_f32 v[134:135], v[134:135], s[52:53], v[124:125]\n" \
  "v_pk_mul_f32 v[132:133], v[132:133], v[4:5]\n" \
  "v_pk_mul_f32 v[134:135], v[134:135], v[6:7]\n" \
  "v_exp_f32 v132, v132\n" \
  "v_exp_f32 v133, v133\n" \
  "v_pk_mul_f32 v[4:5], v[4:5], v[120:121]\n" \
  "v_exp_f32 v134, v134\n" \
  "v_exp_f32 v135, v135\n" \
  "s_waitcnt lgkmcnt(4)\n" \
  "v_smfmac_f32_16x16x64_bf16 v[12:15], v[180:183], v[92:99], v192\n" \
  "v_lshlrev_b32_sdwa v64, 4, v214 dst_sel:DWORD dst_unused:UNUSED_PAD src0_sel:DWORD src1_sel:WORD_0\n" \
  "ds_read_b128 v[68:71], v64\n" \
  "v_lshlrev_b32_sdwa v65, 4, v214 dst_sel:DWORD dst_unused:UNUSED_PAD src0_sel:DWORD src1_sel:WORD_1\n" \
  "ds_read_b128 v[72:75], v65\n" \
  "v_pk_mul_f32 v[6:7], v[6:7], v[122:123]\n" \
  "v_pk_add_f32 v[132:133], v[132:133], 1.0 op_sel_hi:[1,0]\n" \
  "v_pk_add_f32 v[134:135], v[134:135], 1.0 op_sel_hi:[1,0]\n" \
  "v_rcp_f32 v132, v132\n" \
  "v_rcp_f32 v133, v133\n" \
  "v_rcp_f32 v134, v134\n" \
  "v_rcp_f32 v135, v135\n" \
  "v_pk_mul_f32 v[4:5], v[4:5], v[132:133]\n" \
  "v_pk_fma_f32 v[4:5], v[6:7], v[134:135], v[4:5]\n" \
  "v_add_f32 v4, v4, v5\n" \
  "v_pk_mul_f32 v[136:137], v[8:9], v[8:9]\n" \
  "v_pk_mul_f32 v[138:139], v[10:11], v[10:11]\n" \
  "v_pk_fma_f32 v[136:137], v[136:137], s[52:53], v[124:125]\n" \
  "v_pk_fma_f32 v[138:139], v[138:139], s[52:53], v[124:125]\n" \
  "v_pk_mul_f32 v[136:137], v[136:137], v[8:9]\n" \
  "v_pk_mul_f32 v[138:139], v[138:139], v[10:11]\n" \
  "s_waitcnt lgkmcnt(4)\n" \
  "v_smfmac_f32_16x16x64_bf16 v[16:19], v[180:183], v[100:107], v192\n" \
  "v_lshlrev_b32_sdwa v66, 4, v215 dst_sel:DWORD dst_unused:UNUSED_PAD src0_sel:DWORD src1_sel:WORD_0\n" \
  "ds_read_b128 v[76:79], v66\n" \
  "v_lshlrev_b32_sdwa v67, 4, v215 dst_sel:DWORD dst_unused:UNUSED_PAD src0_sel:DWORD src1_sel:WORD_1\n" \
  "ds_read_b128 v[80:83], v67\n" \
  "v_exp_f32 v136, v136\n" \
  "v_exp_f32 v137, v137\n" \
  "v_pk_mul_f32 v[8:9], v[8:9], v[120:121]\n" \
  "v_exp_f32 v138, v138\n" \
  "v_exp_f32 v139, v139\n" \
  "v_pk_mul_f32 v[10:11], v[10:11], v[122:123]\n" \
  "v_pk_add_f32 v[136:137], v[136:137], 1.0 op_sel_hi:[1,0]\n" \
  "v_pk_add_f32 v[138:139], v[138:139], 1.0 op_sel_hi:[1,0]\n" \
  "v_rcp_f32 v136, v136\n" \
  "v_rcp_f32 v137, v137\n" \
  "v_rcp_f32 v138, v138\n" \
  "v_rcp_f32 v139, v139\n" \
  "v_pk_mul_f32 v[8:9], v[8:9], v[136:137]\n" \
  "v_pk_fma_f32 v[8:9], v[10:11], v[138:139], v[8:9]\n" \
  "v_add_f32 v8, v8, v9\n" \
  "v_pk_mul_f32 v[140:141], v[12:13], v[12:13]\n" \
  "s_waitcnt lgkmcnt(4)\n" \
  "v_smfmac_f32_16x16x64_bf16 v[20:23], v[180:183], v[108:115], v192\n" \
  "v_lshlrev_b32_sdwa v64, 4, v216 dst_sel:DWORD dst_unused:UNUSED_PAD src0_sel:DWORD src1_sel:WORD_0\n" \
  "ds_read_b128 v[84:87], v64\n" \
  "v_lshlrev_b32_sdwa v65, 4, v216 dst_sel:DWORD dst_unused:UNUSED_PAD src0_sel:DWORD src1_sel:WORD_1\n" \
  "ds_read_b128 v[88:91], v65\n" \
  "v_pk_mul_f32 v[142:143], v[14:15], v[14:15]\n" \
  "v_pk_fma_f32 v[140:141], v[140:141], s[52:53], v[124:125]\n" \
  "v_pk_fma_f32 v[142:143], v[142:143], s[52:53], v[124:125]\n" \
  "v_pk_mul_f32 v[140:141], v[140:141], v[12:13]\n" \
  "v_pk_mul_f32 v[142:143], v[142:143], v[14:15]\n" \
  "v_exp_f32 v140, v140\n" \
  "v_exp_f32 v141, v141\n" \
  "v_pk_mul_f32 v[12:13], v[12:13], v[120:121]\n" \
  "v_exp_f32 v142, v142\n" \
  "v_exp_f32 v143, v143\n" \
  "v_pk_mul_f32 v[14:15], v[14:15], v[122:123]\n" \
  "v_pk_add_f32 v[140:141], v[140:141], 1.0 op_sel_hi:[1,0]\n" \
  "v_pk_add_f32 v[142:143], v[142:143], 1.0 op_sel_hi:[1,0]\n" \
  "v_rcp_f32 v140, v140\n" \
  "v_rcp_f32 v141, v141\n" \
  "v_rcp_f32 v142, v142\n" \
  "s_waitcnt lgkmcnt(4)\n" \
  "v_smfmac_f32_16x16x64_bf16 v[24:27], v[180:183], v[68:75], v192\n" \
  "v_lshlrev_b32_sdwa v66, 4, v217 dst_sel:DWORD dst_unused:UNUSED_PAD src0_sel:DWORD src1_sel:WORD_0\n" \
  "ds_read_b128 v[92:95], v66\n" \
  "v_lshlrev_b32_sdwa v67, 4, v217 dst_sel:DWORD dst_unused:UNUSED_PAD src0_sel:DWORD src1_sel:WORD_1\n" \
  "ds_read_b128 v[96:99], v67\n" \
  "v_rcp_f32 v143, v143\n" \
  "v_pk_mul_f32 v[12:13], v[12:13], v[140:141]\n" \
  "v_pk_fma_f32 v[12:13], v[14:15], v[142:143], v[12:13]\n" \
  "v_add_f32 v12, v12, v13\n" \
  "v_pk_mul_f32 v[128:129], v[16:17], v[16:17]\n" \
  "v_pk_mul_f32 v[130:131], v[18:19], v[18:19]\n" \
  "v_pk_fma_f32 v[128:129], v[128:129], s[52:53], v[124:125]\n" \
  "v_pk_fma_f32 v[130:131], v[130:131], s[52:53], v[124:125]\n" \
  "v_pk_mul_f32 v[128:129], v[128:129], v[16:17]\n" \
  "v_pk_mul_f32 v[130:131], v[130:131], v[18:19]\n" \
  "v_exp_f32 v128, v128\n" \
  "v_exp_f32 v129, v129\n" \
  "v_pk_mul_f32 v[16:17], v[16:17], v[120:121]\n" \
  "v_exp_f32 v130, v130\n" \
  "v_exp_f32 v131, v131\n" \
  "v_pk_mul_f32 v[18:19], v[18:19], v[122:123]\n" \
  "s_waitcnt lgkmcnt(4)\n" \
  "v_smfmac_f32_16x16x64_bf16 v[28:31], v[180:183], v[76:83], v192\n" \
  "v_lshlrev_b32_sdwa v64, 4, v250 dst_sel:DWORD dst_unused:UNUSED_PAD src0_sel:DWORD src1_sel:WORD_0\n" \
  "ds_read_b128 v[100:103], v64\n" \
  "v_lshlrev_b32_sdwa v65, 4, v250 dst_sel:DWORD dst_unused:UNUSED_PAD src0_sel:DWORD src1_sel:WORD_1\n" \
  "ds_read_b128 v[104:107], v65\n" \
  "v_pk_add_f32 v[128:129], v[128:129], 1.0 op_sel_hi:[1,0]\n" \
  "v_pk_add_f32 v[130:131], v[130:131], 1.0 op_sel_hi:[1,0]\n" \
  "v_rcp_f32 v128, v128\n" \
  "v_rcp_f32 v129, v129\n" \
  "v_rcp_f32 v130, v130\n" \
  "v_rcp_f32 v131, v131\n" \
  "v_pk_mul_f32 v[16:17], v[16:17], v[128:129]\n" \
  "v_pk_fma_f32 v[16:17], v[18:19], v[130:131], v[16:17]\n" \
  "v_add_f32 v16, v16, v17\n" \
  "v_pk_mul_f32 v[132:133], v[20:21], v[20:21]\n" \
  "v_pk_mul_f32 v[134:135], v[22:23], v[22:23]\n" \
  "v_pk_fma_f32 v[132:133], v[132:133], s[52:53], v[124:125]\n" \
  "v_pk_fma_f32 v[134:135], v[134:135], s[52:53], v[124:125]\n" \
  "v_pk_mul_f32 v[132:133], v[132:133], v[20:21]\n" \
  "v_pk_mul_f32 v[134:135], v[134:135], v[22:23]\n" \
  "v_exp_f32 v132, v132\n" \
  "s_waitcnt lgkmcnt(4)\n" \
  "v_smfmac_f32_16x16x64_bf16 v[32:35], v[180:183], v[84:91], v192\n" \
  "v_lshlrev_b32_sdwa v66, 4, v218 dst_sel:DWORD dst_unused:UNUSED_PAD src0_sel:DWORD src1_sel:WORD_0\n" \
  "ds_read_b128 v[108:111], v66\n" \
  "v_lshlrev_b32_sdwa v67, 4, v218 dst_sel:DWORD dst_unused:UNUSED_PAD src0_sel:DWORD src1_sel:WORD_1\n" \
  "ds_read_b128 v[112:115], v67\n" \
  "v_exp_f32 v133, v133\n" \
  "v_pk_mul_f32 v[20:21], v[20:21], v[120:121]\n" \
  "v_exp_f32 v134, v134\n" \
  "v_exp_f32 v135, v135\n" \
  "v_pk_mul_f32 v[22:23], v[22:23], v[122:123]\n" \
  "v_pk_add_f32 v[132:133], v[132:133], 1.0 op_sel_hi:[1,0]\n" \
  "v_pk_add_f32 v[134:135], v[134:135], 1.0 op_sel_hi:[1,0]\n" \
  "v_rcp_f32 v132, v132\n" \
  "v_rcp_f32 v133, v133\n" \
  "v_rcp_f32 v134, v134\n" \
  "v_rcp_f32 v135, v135\n" \
  "v_pk_mul_f32 v[20:21], v[20:21], v[132:133]\n" \
  "v_pk_fma_f32 v[20:21], v[22:23], v[134:135], v[20:21]\n" \
  "v_add_f32 v20, v20, v21\n" \
  "v_pk_mul_f32 v[136:137], v[24:25], v[24:25]\n" \
  "v_pk_mul_f32 v[138:139], v[26:27], v[26:27]\n" \
  "s_waitcnt lgkmcnt(4)\n" \
  "v_smfmac_f32_16x16x64_bf16 v[36:39], v[180:183], v[92:99], v192\n" \
  "v_lshlrev_b32_sdwa v64, 4, v251 dst_sel:DWORD dst_unused:UNUSED_PAD src0_sel:DWORD src1_sel:WORD_0\n" \
  "ds_read_b128 v[68:71], v64\n" \
  "v_lshlrev_b32_sdwa v65, 4, v251 dst_sel:DWORD dst_unused:UNUSED_PAD src0_sel:DWORD src1_sel:WORD_1\n" \
  "ds_read_b128 v[72:75], v65\n" \
  "v_pk_fma_f32 v[136:137], v[136:137], s[52:53], v[124:125]\n" \
  "v_pk_fma_f32 v[138:139], v[138:139], s[52:53], v[124:125]\n" \
  "v_pk_mul_f32 v[136:137], v[136:137], v[24:25]\n" \
  "v_pk_mul_f32 v[138:139], v[138:139], v[26:27]\n" \
  "v_exp_f32 v136, v136\n" \
  "v_exp_f32 v137, v137\n" \
  "v_pk_mul_f32 v[24:25], v[24:25], v[120:121]\n" \
  "v_exp_f32 v138, v138\n" \
  "v_exp_f32 v139, v139\n" \
  "v_pk_mul_f32 v[26:27], v[26:27], v[122:123]\n" \
  "v_pk_add_f32 v[136:137], v[136:137], 1.0 op_sel_hi:[1,0]\n" \
  "v_pk_add_f32 v[138:139], v[138:139], 1.0 op_sel_hi:[1,0]\n" \
  "v_rcp_f32 v136, v136\n" \
  "v_rcp_f32 v137, v137\n" \
  "v_rcp_f32 v138, v138\n" \
  "v_rcp_f32 v139, v139\n" \
  "s_waitcnt lgkmcnt(4)\n" \
  "v_smfmac_f32_16x16x64_bf16 v[40:43], v[180:183], v[100:107], v192\n" \
  "v_lshlrev_b32_sdwa v66, 4, v219 dst_sel:DWORD dst_unused:UNUSED_PAD src0_sel:DWORD src1_sel:WORD_0\n" \
  "ds_read_b128 v[76:79], v66\n" \
  "v_lshlrev_b32_sdwa v67, 4, v219 dst_sel:DWORD dst_unused:UNUSED_PAD src0_sel:DWORD src1_sel:WORD_1\n" \
  "ds_read_b128 v[80:83], v67\n" \
  "v_pk_mul_f32 v[24:25], v[24:25], v[136:137]\n" \
  "v_pk_fma_f32 v[24:25], v[26:27], v[138:139], v[24:25]\n" \
  "v_add_f32 v24, v24, v25\n" \
  "v_pk_mul_f32 v[140:141], v[28:29], v[28:29]\n" \
  "v_pk_mul_f32 v[142:143], v[30:31], v[30:31]\n" \
  "v_pk_fma_f32 v[140:141], v[140:141], s[52:53], v[124:125]\n" \
  "v_pk_fma_f32 v[142:143], v[142:143], s[52:53], v[124:125]\n" \
  "v_pk_mul_f32 v[140:141], v[140:141], v[28:29]\n" \
  "v_pk_mul_f32 v[142:143], v[142:143], v[30:31]\n" \
  "v_exp_f32 v140, v140\n" \
  "v_exp_f32 v141, v141\n" \
  "v_pk_mul_f32 v[28:29], v[28:29], v[120:121]\n" \
  "v_exp_f32 v142, v142\n" \
  "v_exp_f32 v143, v143\n" \
  "v_pk_mul_f32 v[30:31], v[30:31], v[122:123]\n" \
  "v_pk_add_f32 v[140:141], v[140:141], 1.0 op_sel_hi:[1,0]\n" \
  "s_waitcnt lgkmcnt(4)\n" \
  "v_smfmac_f32_16x16x64_bf16 v[40:43], v[180:183], v[108:115], v192\n" \
  "v_lshlrev_b32_sdwa v64, 4, v252 dst_sel:DWORD dst_unused:UNUSED_PAD src0_sel:DWORD src1_sel:WORD_0\n" \
  "ds_read_b128 v[84:87], v64\n" \
  "v_lshlrev_b32_sdwa v65, 4, v252 dst_sel:DWORD dst_unused:UNUSED_PAD src0_sel:DWORD src1_sel:WORD_1\n" \
  "ds_read_b128 v[88:91], v65\n" \
  "v_pk_add_f32 v[142:143], v[142:143], 1.0 op_sel_hi:[1,0]\n" \
  "v_rcp_f32 v140, v140\n" \
  "v_rcp_f32 v141, v141\n" \
  "v_rcp_f32 v142, v142\n" \
  "v_rcp_f32 v143, v143\n" \
  "v_pk_mul_f32 v[28:29], v[28:29], v[140:141]\n" \
  "v_pk_fma_f32 v[28:29], v[30:31], v[142:143], v[28:29]\n" \
  "v_add_f32 v28, v28, v29\n" \
  "v_permlane16_swap_b32 v0, v16\n" \
  "v_permlane16_swap_b32 v4, v20\n" \
  "v_permlane16_swap_b32 v8, v24\n" \
  "v_permlane16_swap_b32 v12, v28\n" \
  "v_add_f32 v144, v0, v16\n" \
  "v_add_f32 v145, v4, v20\n" \
  "v_add_f32 v146, v8, v24\n" \
  "v_add_f32 v147, v12, v28\n" \
  "s_waitcnt lgkmcnt(4)\n" \
  "v_smfmac_f32_16x16x64_bf16 v[44:47], v[180:183], v[68:75], v192\n" \
  "v_lshlrev_b32_sdwa v66, 4, v220 dst_sel:DWORD dst_unused:UNUSED_PAD src0_sel:DWORD src1_sel:WORD_0\n" \
  "ds_read_b128 v[92:95], v66\n" \
  "v_lshlrev_b32_sdwa v67, 4, v220 dst_sel:DWORD dst_unused:UNUSED_PAD src0_sel:DWORD src1_sel:WORD_1\n" \
  "ds_read_b128 v[96:99], v67\n" \
  "v_min_u32 v144, -2, v144\n" \
  "v_min_u32 v145, -2, v145\n" \
  "v_min_u32 v146, -2, v146\n" \
  "v_min_u32 v147, -2, v147\n" \
  "global_store_dwordx4 v188, v[144:147], s[40:41] offset:0 sc1\n" \
  "v_pk_mul_f32 v[128:129], v[32:33], v[32:33]\n" \
  "v_pk_mul_f32 v[130:131], v[34:35], v[34:35]\n" \
  "v_pk_fma_f32 v[128:129], v[128:129], s[52:53], v[124:125]\n" \
  "v_pk_fma_f32 v[130:131], v[130:131], s[52:53], v[124:125]\n" \
  "v_pk_mul_f32 v[128:129], v[128:129], v[32:33]\n" \
  "v_pk_mul_f32 v[130:131], v[130:131], v[34:35]\n" \
  "v_exp_f32 v128, v128\n" \
  "v_exp_f32 v129, v129\n" \
  "v_pk_mul_f32 v[32:33], v[32:33], v[120:121]\n" \
  "v_exp_f32 v130, v130\n" \
  "v_exp_f32 v131, v131\n" \
  "s_waitcnt lgkmcnt(4)\n" \
  "v_smfmac_f32_16x16x64_bf16 v[44:47], v[180:183], v[76:83], v192\n" \
  "v_lshlrev_b32_sdwa v64, 4, v253 dst_sel:DWORD dst_unused:UNUSED_PAD src0_sel:DWORD src1_sel:WORD_0\n" \
  "ds_read_b128 v[100:103], v64\n" \
  "v_lshlrev_b32_sdwa v65, 4, v253 dst_sel:DWORD dst_unused:UNUSED_PAD src0_sel:DWORD src1_sel:WORD_1\n" \
  "ds_read_b128 v[104:107], v65\n" \
  "v_pk_mul_f32 v[34:35], v[34:35], v[122:123]\n" \
  "v_pk_add_f32 v[128:129], v[128:129], 1.0 op_sel_hi:[1,0]\n" \
  "v_pk_add_f32 v[130:131], v[130:131], 1.0 op_sel_hi:[1,0]\n" \
  "v_rcp_f32 v128, v128\n" \
  "v_rcp_f32 v129, v129\n" \
  "v_rcp_f32 v130, v130\n" \
  "v_rcp_f32 v131, v131\n" \
  "v_pk_mul_f32 v[32:33], v[32:33], v[128:129]\n" \
  "v_pk_fma_f32 v[32:33], v[34:35], v[130:131], v[32:33]\n" \
  "v_add_f32 v32, v32, v33\n" \
  "v_pk_mul_f32 v[132:133], v[36:37], v[36:37]\n" \
  "v_pk_mul_f32 v[134:135], v[38:39], v[38:39]\n" \
  "v_pk_fma_f32 v[132:133], v[132:133], s[52:53], v[124:125]\n" \
  "v_pk_fma_f32 v[134:135], v[134:135], s[52:53], v[124:125]\n" \
  "v_pk_mul_f32 v[132:133], v[132:133], v[36:37]\n" \
  "v_pk_mul_f32 v[134:135], v[134:135], v[38:39]\n" \
  "s_waitcnt lgkmcnt(4)\n" \
  "v_smfmac_f32_16x16x64_bf16 v[48:51], v[180:183], v[84:91], v192\n" \
  "v_lshlrev_b32_sdwa v66, 4, v221 dst_sel:DWORD dst_unused:UNUSED_PAD src0_sel:DWORD src1_sel:WORD_0\n" \
  "ds_read_b128 v[108:111], v66\n" \
  "v_lshlrev_b32_sdwa v67, 4, v221 dst_sel:DWORD dst_unused:UNUSED_PAD src0_sel:DWORD src1_sel:WORD_1\n" \
  "ds_read_b128 v[112:115], v67\n" \
  "v_exp_f32 v132, v132\n" \
  "v_exp_f32 v133, v133\n" \
  "v_pk_mul_f32 v[36:37], v[36:37], v[120:121]\n" \
  "v_exp_f32 v134, v134\n" \
  "v_exp_f32 v135, v135\n" \
  "v_pk_mul_f32 v[38:39], v[38:39], v[122:123]\n" \
  "v_pk_add_f32 v[132:133], v[132:133], 1.0 op_sel_hi:[1,0]\n" \
  "v_pk_add_f32 v[134:135], v[134:135], 1.0 op_sel_hi:[1,0]\n" \
  "v_rcp_f32 v132, v132\n" \
  "v_rcp_f32 v133, v133\n" \
  "v_rcp_f32 v134, v134\n" \
  "v_rcp_f32 v135, v135\n" \
  "v_pk_mul_f32 v[36:37], v[36:37], v[132:133]\n" \
  "v_pk_fma_f32 v[36:37], v[38:39], v[134:135], v[36:37]\n" \
  "v_add_f32 v36, v36, v37\n" \
  "v_pk_mul_f32 v[136:137], v[40:41], v[40:41]\n" \
  "s_waitcnt lgkmcnt(4)\n" \
  "v_smfmac_f32_16x16x64_bf16 v[48:51], v[180:183], v[92:99], v192\n" \
  "v_lshlrev_b32_sdwa v64, 4, v254 dst_sel:DWORD dst_unused:UNUSED_PAD src0_sel:DWORD src1_sel:WORD_0\n" \
  "ds_read_b128 v[68:71], v64\n" \
  "v_lshlrev_b32_sdwa v65, 4, v254 dst_sel:DWORD dst_unused:UNUSED_PAD src0_sel:DWORD src1_sel:WORD_1\n" \
  "ds_read_b128 v[72:75], v65\n" \
  "v_pk_mul_f32 v[138:139], v[42:43], v[42:43]\n" \
  "v_pk_fma_f32 v[136:137], v[136:137], s[52:53], v[124:125]\n" \
  "v_pk_fma_f32 v[138:139], v[138:139], s[52:53], v[124:125]\n" \
  "v_pk_mul_f32 v[136:137], v[136:137], v[40:41]\n" \
  "v_pk_mul_f32 v[138:139], v[138:139], v[42:43]\n" \
  "v_exp_f32 v136, v136\n" \
  "v_exp_f32 v137, v137\n" \
  "v_pk_mul_f32 v[40:41], v[40:41], v[120:121]\n" \
  "v_exp_f32 v138, v138\n" \
  "v_exp_f32 v139, v139\n" \
  "v_pk_mul_f32 v[42:43], v[42:43], v[122:123]\n" \
  "v_pk_add_f32 v[136:137], v[136:137], 1.0 op_sel_hi:[1,0]\n" \
  "v_pk_add_f32 v[138:139], v[138:139], 1.0 op_sel_hi:[1,0]\n" \
  "v_rcp_f32 v136, v136\n" \
  "v_rcp_f32 v137, v137\n" \
  "v_rcp_f32 v138, v138\n" \
  "s_waitcnt lgkmcnt(4)\n" \
  "v_smfmac_f32_16x16x64_bf16 v[52:55], v[180:183], v[100:107], v192\n" \
  "v_lshlrev_b32_sdwa v66, 4, v222 dst_sel:DWORD dst_unused:UNUSED_PAD src0_sel:DWORD src1_sel:WORD_0\n" \
  "ds_read_b128 v[76:79], v66\n" \
  "v_lshlrev_b32_sdwa v67, 4, v222 dst_sel:DWORD dst_unused:UNUSED_PAD src0_sel:DWORD src1_sel:WORD_1\n" \
  "ds_read_b128 v[80:83], v67\n" \
  "v_rcp_f32 v139, v139\n" \
  "v_pk_mul_f32 v[40:41], v[40:41], v[136:137]\n" \
  "v_pk_fma_f32 v[40:41], v[42:43], v[138:139], v[40:41]\n" \
  "v_add_f32 v40, v40, v41\n" \
  "v_pk_mul_f32 v[140:141], v[44:45], v[44:45]\n" \
  "v_pk_mul_f32 v[142:143], v[46:47], v[46:47]\n" \
  "v_pk_fma_f32 v[140:141], v[140:141], s[52:53], v[124:125]\n" \
  "v_pk_fma_f32 v[142:143], v[142:143], s[52:53], v[124:125]\n" \
  "v_pk_mul_f32 v[140:141], v[140:141], v[44:45]\n" \
  "v_pk_mul_f32 v[142:143], v[142:143], v[46:47]\n" \
  "v_exp_f32 v140, v140\n" \
  "v_exp_f32 v141, v141\n" \
  "v_pk_mul_f32 v[44:45], v[44:45], v[120:121]\n" \
  "v_exp_f32 v142, v142\n" \
  "v_exp_f32 v143, v143\n" \
  "v_pk_mul_f32 v[46:47], v[46:47], v[122:123]\n" \
  "s_waitcnt lgkmcnt(4)\n" \
  "v_smfmac_f32_16x16x64_bf16 v[52:55], v[180:183], v[108:115], v192\n" \
  "v_lshlrev_b32_sdwa v64, 4, v255 dst_sel:DWORD dst_unused:UNUSED_PAD src0_sel:DWORD src1_sel:WORD_0\n" \
  "ds_read_b128 v[84:87], v64\n" \
  "v_lshlrev_b32_sdwa v65, 4, v255 dst_sel:DWORD dst_unused:UNUSED_PAD src0_sel:DWORD src1_sel:WORD_1\n" \
  "ds_read_b128 v[88:91], v65\n" \
  "v_pk_add_f32 v[140:141], v[140:141], 1.0 op_sel_hi:[1,0]\n" \
  "v_pk_add_f32 v[142:143], v[142:143], 1.0 op_sel_hi:[1,0]\n" \
  "v_rcp_f32 v140, v140\n" \
  "v_rcp_f32 v141, v141\n" \
  "v_rcp_f32 v142, v142\n" \
  "v_rcp_f32 v143, v143\n" \
  "v_pk_mul_f32 v[44:45], v[44:45], v[140:141]\n" \
  "v_pk_fma_f32 v[44:45], v[46:47], v[142:143], v[44:45]\n" \
  "v_add_f32 v44, v44, v45\n" \
  "v_pk_mul_f32 v[128:129], v[48:49], v[48:49]\n" \
  "v_pk_mul_f32 v[130:131], v[50:51], v[50:51]\n" \
  "v_pk_fma_f32 v[128:129], v[128:129], s[52:53], v[124:125]\n" \
  "v_pk_fma_f32 v[130:131], v[130:131], s[52:53], v[124:125]\n" \
  "v_pk_mul_f32 v[128:129], v[128:129], v[48:49]\n" \
  "v_pk_mul_f32 v[130:131], v[130:131], v[50:51]\n" \
  "v_exp_f32 v128, v128\n" \
  "s_waitcnt lgkmcnt(4)\n" \
  "v_smfmac_f32_16x16x64_bf16 v[56:59], v[180:183], v[68:75], v192\n" \
  "v_lshlrev_b32_sdwa v66, 4, v223 dst_sel:DWORD dst_unused:UNUSED_PAD src0_sel:DWORD src1_sel:WORD_0\n" \
  "ds_read_b128 v[92:95], v66\n" \
  "v_lshlrev_b32_sdwa v67, 4, v223 dst_sel:DWORD dst_unused:UNUSED_PAD src0_sel:DWORD src1_sel:WORD_1\n" \
  "ds_read_b128 v[96:99], v67\n" \
  "v_exp_f32 v129, v129\n" \
  "v_pk_mul_f32 v[48:49], v[48:49], v[120:121]\n" \
  "v_exp_f32 v130, v130\n" \
  "v_exp_f32 v131, v131\n" \
  "v_pk_mul_f32 v[50:51], v[50:51], v[122:123]\n" \
  "v_pk_add_f32 v[128:129], v[128:129], 1.0 op_sel_hi:[1,0]\n" \
  "v_pk_add_f32 v[130:131], v[130:131], 1.0 op_sel_hi:[1,0]\n" \
  "v_rcp_f32 v128, v128\n" \
  "v_rcp_f32 v129, v129\n" \
  "v_rcp_f32 v130, v130\n" \
  "v_rcp_f32 v131, v131\n" \
  "v_pk_mul_f32 v[48:49], v[48:49], v[128:129]\n" \
  "v_pk_fma_f32 v[48:49], v[50:51], v[130:131], v[48:49]\n" \
  "v_add_f32 v48, v48, v49\n" \
  "v_pk_mul_f32 v[132:133], v[52:53], v[52:53]\n" \
  "v_pk_mul_f32 v[134:135], v[54:55], v[54:55]\n" \
  "s_waitcnt lgkmcnt(4)\n" \
  "v_smfmac_f32_16x16x64_bf16 v[56:59], v[180:183], v[76:83], v192\n" \
  "v_pk_fma_f32 v[132:133], v[132:133], s[52:53], v[124:125]\n" \
  "v_pk_fma_f32 v[134:135], v[134:135], s[52:53], v[124:125]\n" \
  "v_pk_mul_f32 v[132:133], v[132:133], v[52:53]\n" \
  "v_pk_mul_f32 v[134:135], v[134:135], v[54:55]\n" \
  "v_exp_f32 v132, v132\n" \
  "v_exp_f32 v133, v133\n" \
  "v_pk_mul_f32 v[52:53], v[52:53], v[120:121]\n" \
  "v_exp_f32 v134, v134\n" \
  "v_exp_f32 v135, v135\n" \
  "v_pk_mul_f32 v[54:55], v[54:55], v[122:123]\n" \
  "v_pk_add_f32 v[132:133], v[132:133], 1.0 op_sel_hi:[1,0]\n" \
  "v_pk_add_f32 v[134:135], v[134:135], 1.0 op_sel_hi:[1,0]\n" \
  "v_rcp_f32 v132, v132\n" \
  "v_rcp_f32 v133, v133\n" \
  "v_rcp_f32 v134, v134\n" \
  "v_rcp_f32 v135, v135\n" \
  "v_pk_mul_f32 v[52:53], v[52:53], v[132:133]\n" \
  "v_pk_fma_f32 v[52:53], v[54:55], v[134:135], v[52:53]\n" \
  "v_add_f32 v52, v52, v53\n" \
  "s_waitcnt lgkmcnt(2)\n" \
  "v_smfmac_f32_16x16x64_bf16 v[60:63], v[180:183], v[84:91], v192\n" \
  "v_pk_mul_f32 v[136:137], v[56:57], v[56:57]\n" \
  "v_pk_mul_f32 v[138:139], v[58:59], v[58:59]\n" \
  "v_pk_fma_f32 v[136:137], v[136:137], s[52:53], v[124:125]\n" \
  "v_pk_fma_f32 v[138:139], v[138:139], s[52:53], v[124:125]\n" \
  "v_pk_mul_f32 v[136:137], v[136:137], v[56:57]\n" \
  "v_pk_mul_f32 v[138:139], v[138:139], v[58:59]\n" \
  "v_exp_f32 v136, v136\n" \
  "v_exp_f32 v137, v137\n" \
  "v_pk_mul_f32 v[56:57], v[56:57], v[120:121]\n" \
  "v_exp_f32 v138, v138\n" \
  "v_exp_f32 v139, v139\n" \
  "v_pk_mul_f32 v[58:59], v[58:59], v[122:123]\n" \
  "v_pk_add_f32 v[136:137], v[136:137], 1.0 op_sel_hi:[1,0]\n" \
  "v_pk_add_f32 v[138:139], v[138:139], 1.0 op_sel_hi:[1,0]\n" \
  "v_rcp_f32 v136, v136\n" \
  "v_rcp_f32 v137, v137\n" \
  "v_rcp_f32 v138, v138\n" \
  "v_rcp_f32 v139, v139\n" \
  "v_pk_mul_f32 v[56:57], v[56:57], v[136:137]\n" \
  "v_pk_fma_f32 v[56:57], v[58:59], v[138:139], v[56:57]\n" \
  "v_add_f32 v56, v56, v57\n" \
  "s_waitcnt lgkmcnt(0)\n" \
  "v_smfmac_f32_16x16x64_bf16 v[60:63], v[180:183], v[92:99], v192\n" \
  "s_nop 7\n" \
  "v_pk_mul_f32 v[140:141], v[60:61], v[60:61]\n" \
  "v_pk_mul_f32 v[142:143], v[62:63], v[62:63]\n" \
  "v_pk_fma_f32 v[140:141], v[140:141], s[52:53], v[124:125]\n" \
  "v_pk_fma_f32 v[142:143], v[142:143], s[52:53], v[124:125]\n" \
  "v_pk_mul_f32 v[140:141], v[140:141], v[60:61]\n" \
  "v_pk_mul_f32 v[142:143], v[142:143], v[62:63]\n" \
  "v_exp_f32 v140, v140\n" \
  "v_exp_f32 v141, v141\n" \
  "v_pk_mul_f32 v[60:61], v[60:61], v[120:121]\n" \
  "v_exp_f32 v142, v142\n" \
  "v_exp_f32 v143, v143\n" \
  "v_pk_mul_f32 v[62:63], v[62:63], v[122:123]\n" \
  "v_pk_add_f32 v[140:141], v[140:141], 1.0 op_sel_hi:[1,0]\n" \
  "v_pk_add_f32 v[142:143], v[142:143], 1.0 op_sel_hi:[1,0]\n" \
  "v_rcp_f32 v140, v140\n" \
  "v_rcp_f32 v141, v141\n" \
  "v_rcp_f32 v142, v142\n" \
  "v_rcp_f32 v143, v143\n" \
  "v_pk_mul_f32 v[60:61], v[60:61], v[140:141]\n" \
  "v_pk_fma_f32 v[60:61], v[62:63], v[142:143], v[60:61]\n" \
  "v_add_f32 v60, v60, v61\n" \
  "v_permlane16_swap_b32 v32, v48\n" \
  "v_permlane16_swap_b32 v36, v52\n" \
  "v_permlane16_swap_b32 v40, v56\n" \
  "v_permlane16_swap_b32 v44, v60\n" \
  "v_add_f32 v148, v32, v48\n" \
  "v_add_f32 v149, v36, v52\n" \
  "v_add_f32 v150, v40, v56\n" \
  "v_add_f32 v151, v44, v60\n" \
  "v_min_u32 v148, -2, v148\n" \
  "v_min_u32 v149, -2, v149\n" \
  "v_min_u32 v150, -2, v150\n" \
  "v_min_u32 v151, -2, v151\n" \
  "global_store_dwordx4 v188, v[148:151], s[40:41] offset:1024 sc1\n" \
  "s_cmp_ge_u32 s27, 4\n" \
  "s_cbranch_scc1 LF_b\n" \
  "v_lshrrev_b32 v0, 4, v188\n" \
  "v_lshrrev_b32 v1, 4, v0\n" \
  "v_and_b32 v2, 15, v0\n" \
  "v_lshlrev_b32 v2, 4, v2\n" \
  "v_lshl_add_u32 v1, v1, 18, v2\n" \
  "s_lshl_b32 s35, s27, 20\n" \
  "s_lshl_b32 s36, s26, 8\n" \
  "s_add_u32 s35, s35, s36\n" \
  "s_add_u32 s35, s35, 105676800\n" \
  "s_add_u32 s40, s24, s35\n" \
  "s_addc_u32 s41, s25, 0\n" \
  "s_add_u32 s42, s40, 65536\n" \
  "s_addc_u32 s43, s41, 0\n" \
  "s_add_u32 s54, s42, 65536\n" \
  "s_addc_u32 s55, s43, 0\n" \
  "s_add_u32 s56, s54, 65536\n" \
  "s_addc_u32 s57, s55, 0\n" \
  "s_add_u32 s44, s24, 109871104\n" \
  "s_addc_u32 s45, s25, 0\n" \
  "s_add_u32 s46, s44, 65536\n" \
  "s_addc_u32 s47, s45, 0\n" \
  "s_lshr_b32 s35, s26, 2\n" \
  "s_lshl_b32 s35, s35, 8\n" \
  "s_and_b32 s36, s26, 1\n" \
  "s_lshl_b32 s36, s36, 7\n" \
  "s_add_u32 s35, s35, s36\n" \
  "s_and_b32 s36, s26, 2\n" \
  "s_lshl3_add_u32 s35, s36, s35\n" \
  "v_and_b32 v2, 3, v0\n" \
  "v_lshrrev_b32 v3, 2, v0\n" \
  "v_lshl_add_u32 v2, v2, 5, v3\n" \
  "v_add_u32 v2, s35, v2\n" \
  "v_lshlrev_b32 v3, 2, v2\n" \
  "s_cmp_eq_u32 s27, 0\n" \
  "s_cselect_b32 s35, -1, 0\n" \
  "v_and_b32 v3, s35, v3\n" \
  "s_lshl_b32 s6, s6, 16\n" \
  "v_and_b32 v6, 15, v0\n" \
  "v_lshrrev_b32 v7, 4, v0\n" \
  "v_lshl_add_u32 v6, v6, 2, v7\n" \
  "s_lshl_b32 s35, s27, 8\n" \
  "v_lshl_add_u32 v6, v6, 2, s35\n" \
  "v_lshlrev_b32 v7, 2, v0\n" \
  "v_lshlrev_b32 v2, 1, v2\n" \
  "s_mov_b32 s5, 0x10000\n" \
  "s_barrier\n" \
  "s_sleep 12\n" \
  "LF_poll:\n" \
  "global_load_dwordx4 v[8:11], v1, s[40:41] sc1\n" \
  "global_load_dwordx4 v[12:15], v1, s[42:43] sc1\n" \
  "global_load_dwordx4 v[16:19], v1, s[54:55] sc1\n" \
  "global_load_dwordx4 v[20:23], v1, s[56:57] sc1\n" \
  "global_load_dword v24, v3, s[44:45] sc1\n" \
  "global_load_dword v25, v3, s[46:47] sc1\n" \
  "s_waitcnt vmcnt(0)\n" \
  "v_max3_u32 v4, v8, v9, v10\n" \
  "v_max3_u32 v5, v11, v12, v13\n" \
  "v_max3_u32 v4, v4, v14, v15\n" \
  "v_max3_u32 v5, v5, v16, v17\n" \
  "v_max3_u32 v4, v4, v18, v19\n" \
  "v_max3_u32 v5, v5, v20, v21\n" \
  "v_max3_u32 v4, v4, v22, v23\n" \
  "v_max3_u32 v5, v5, v24, v25\n" \
  "v_max_u32 v5, v5, v4\n" \
  "v_cmp_eq_u32 vcc, -1, v5\n" \
  "s_sub_u32 s5, s5, 1\n" \
  "s_cbranch_scc1 LF_go\n" \
  "s_nop 2\n" \
  "s_cbranch_vccz LF_go\n" \
  "s_mov_b64 exec, vcc\n" \
  "s_sleep 1\n" \
  "s_branch LF_poll\n" \
  "LF_go:\n" \
  "s_mov_b64 exec, -1\n" \
  "v_pk_add_f32 v[8:9], v[8:9], v[12:13]\n" \
  "v_pk_add_f32 v[10:11], v[10:11], v[14:15]\n" \
  "v_pk_add_f32 v[16:17], v[16:17], v[20:21]\n" \
  "v_pk_add_f32 v[18:19], v[18:19], v[22:23]\n" \
  "v_pk_add_f32 v[8:9], v[8:9], v[16:17]\n" \
  "v_pk_add_f32 v[10:11], v[10:11], v[18:19]\n" \
  "v_add_f32 v24, v24, v25\n" \
  "v_permlane16_swap_b32 v8, v9\n" \
  "v_permlane16_swap_b32 v10, v11\n" \
  "v_add_f32 v8, v8, v9\n" \
  "v_add_f32 v10, v10, v11\n" \
  "v_cvt_pk_bf16_f32 v24, v24, v24\n" \
  "s_cmp_lg_u32 s27, 0\n" \
  "v_permlane32_swap_b32 v8, v10\n" \
  "v_add_f32 v8, v8, v10\n" \
  "ds_write_b32 v6, v8\n" \
  "s_waitcnt lgkmcnt(0)\n" \
  "s_barrier\n" \
  "s_cbranch_scc1 LF_end\n" \
  "ds_read2st64_b32 v[8:9], v7 offset0:0 offset1:1\n" \
  "ds_read2st64_b32 v[10:11], v7 offset0:2 offset1:3\n" \
  "v_lshlrev_b32 v24, 16, v24\n" \
  "s_waitcnt lgkmcnt(0)\n" \
  "v_pk_add_f32 v[8:9], v[8:9], v[10:11]\n" \
  "v_add_f32 v8, v8, v9\n" \
  "v_cvt_pk_bf16_f32 v8, v8, v8\n" \
  "v_lshlrev_b32 v8, 16, v8\n" \
  "v_add_f32 v8, s6, v8\n" \
  "v_cvt_pk_bf16_f32 v8, v8, v8\n" \
  "v_lshlrev_b32 v8, 16, v8\n" \
  "v_add_f32 v8, v8, v24\n" \
  "v_cvt_pk_bf16_f32 v8, v8, v8\n" \
  "global_store_short v2, v8, s[22:23]\n" \
  "LF_end:\n" \
  "s_endpgm\n" \
  "LF_b:\n" \
  "s_barrier\n" \
  "s_endpgm\n" \
  "LG_h:\n" \
  "s_cmp_eq_u32 s51, 4\n" \
  "s_cbranch_scc1 LG_chk\n" \
  "s_cmp_lg_u32 s51, 7\n" \
  "s_cbranch_scc1 LG_ret\n" \
  "s_barrier\n" \
  "s_mov_b32 exec_hi, 0\n" \
  "global_atomic_add v196, v199, s[48:49] offset:3076 sc1\n" \
  "s_mov_b64 exec, -1\n" \
  "s_branch LG_aft\n" \
  "LG_pol:\n" \
  "s_mov_b64 exec, 0xff\n" \
  "global_load_dword v200, v198, s[48:49] offset:3076 sc1\n" \
  "s_mov_b64 exec, -1\n" \
  "s_branch LG_rt2\n" \
  "LG_chk:\n" \
  "s_mov_b32 s5, 0x10000\n" \
  "LG_p:\n" \
  "s_mov_b64 exec, 0xff\n" \
  "v_cmp_le_u32 vcc, 32, v200\n" \
  "s_mov_b64 exec, -1\n" \
  "s_sub_u32 s5, s5, 1\n" \
  "s_cselect_b32 s35, 0xff, vcc_lo\n" \
  "s_cmp_eq_u32 s35, 0xff\n" \
  "s_cbranch_scc1 LG_p_x\n" \
  "s_sleep 2\n" \
  "s_mov_b64 exec, 0xff\n" \
  "global_load_dword v200, v198, s[48:49] offset:3076 sc1\n" \
  "s_mov_b64 exec, -1\n" \
  "s_waitcnt vmcnt(0)\n" \
  "s_branch LG_p\n" \
  "LG_p_x:\n" \
  "s_branch LG_ret\n" \
  "LZ_end:\n"

#define R10(p, b) p #b "0", p #b "1", p #b "2", p #b "3", p #b "4", p #b "5", p #b "6", p #b "7", p #b "8", p #b "9"
__global__ __launch_bounds__(512, 2) void k1(const int* __restrict__ idx, const unsigned short* __restrict__ table,
                                             const unsigned short* __restrict__ w_p, const unsigned short* __restrict__ w1,
                                             const unsigned short* __restrict__ b1, const unsigned short* __restrict__ w2,
                                             const unsigned short* __restrict__ b2, __hip_bfloat16* __restrict__ out, char* __restrict__ ws) {
  __shared__ __attribute__((aligned(1024))) char smem[163840];
  unsigned tid = threadIdx.x;
  asm volatile(K_ASM
               : "+{v0}"(tid)
               : "{s[0:1]}"(__builtin_amdgcn_kernarg_segment_ptr()), "{s26}"(blockIdx.x), "s"((unsigned)(uintptr_t)smem)
               : "memory", "scc", "vcc", "s3", "s4", "s5", "s6", "s7", "s8", "s9", R10("s", 1), "s20", "s21", "s22", "s23", "s24", "s25", "s27", "s28", "s29", "s30", "s31", "s33", "s34", "s35", "s36", "s37", "s38", "s39", R10("s", 4), R10("s", 5), R10("s", 6), R10("s", 7), R10("s", 8), R10("s", 9),
                 "v1", "v2", "v3", "v4", "v5", "v6", "v7", "v8", "v9", R10("v", 1), R10("v", 2), R10("v", 3), R10("v", 4), R10("v", 5), R10("v", 6), R10("v", 7), R10("v", 8), R10("v", 9),
                 R10("v", 10), R10("v", 11), R10("v", 12), R10("v", 13), R10("v", 14), R10("v", 15), R10("v", 16), R10("v", 17), R10("v", 18), R10("v", 19),
                 R10("v", 20), R10("v", 21), R10("v", 22), R10("v", 23), R10("v", 24), "v250", "v251", "v252", "v253", "v254", "v255");
}

extern "C" void kernel_launch(void* const* d_in, const int* in_sizes, int n_in, void* d_out, int out_size, void* d_ws,
                              size_t ws_size, hipStream_t stream) {
  k1<<<dim3(256), dim3(512), 0, stream>>>((const int*)d_in[0], (const unsigned short*)d_in[1], (const unsigned short*)d_in[2], (const unsigned short*)d_in[3],
                                          (const unsigned short*)d_in[4], (const unsigned short*)d_in[5], (const unsigned short*)d_in[6], (__hip_bfloat16*)d_out, (char*)d_ws);
}
```

```cpp
#include <hip/hip_runtime.h>
#include <hip/hip_bf16.h>
#include <stdint.h>

#define K_ASM \
  "LZ_first:\n" \
  "s_load_dwordx16 s[8:23], s[0:1], 0x0\n" \
  "v_readfirstlane_b32 s27, v0\n" \
  "v_and_b32 v255, 63, v0\n" \
  "s_and_b32 s29, s26, 7\n" \
  "s_lshr_b32 s30, s26, 3\n" \
  "s_lshr_b32 s27, s27, 6\n" \
  "s_and_b32 s31, s30, 1\n" \
  "s_and_b32 s28, s27, 3\n" \
  "s_lshr_b32 s35, s30, 1\n" \
  "s_lshl3_add_u32 s7, s35, s29\n" \
  "v_lshrrev_b32 v1, 3, v255\n" \
  "v_and_b32 v2, 7, v255\n" \
  "v_lshlrev_b32 v1, 8, v1\n" \
  "v_lshl_or_b32 v254, v2, 4, v1\n" \
  "s_mul_i32 s36, s7, 37\n" \
  "s_and_b32 s36, s36, 63\n" \
  "s_add_u32 s36, s36, s28\n" \
  "s_cmp_ge_u32 s27, 4\n" \
  "s_cbranch_scc1 LB_start\n" \
  "s_setprio 3\n" \
  "v_and_b32 v252, 15, v255\n" \
  "v_lshrrev_b32 v253, 4, v255\n" \
  "v_mul_u32_u24 v2, 400, v253\n" \
  "v_min_u32 v3, 8, v252\n" \
  "v_lshl_add_u32 v250, v252, 4, v2\n" \
  "v_lshl_add_u32 v251, v3, 4, v2\n" \
  "s_lshl2_add_u32 s36, s7, s28\n" \
  "s_mul_i32 s36, s36, 12800\n" \
  "s_lshl_b32 s35, s28, 10\n" \
  "v_lshl_add_u32 v1, v255, 4, s35\n" \
  "s_add_u32 m0, s35, 138240\n" \
  "s_waitcnt lgkmcnt(0)\n" \
  "s_add_u32 s44, s8, s36\n" \
  "s_addc_u32 s45, s9, 0\n" \
  "global_load_lds_dwordx4 v1, s[12:13]\n" \
  "s_add_u32 m0, m0, 0x1000\n" \
  "v_add_u32 v1, 0x1000, v1\n" \
  "global_load_lds_dwordx4 v1, s[12:13]\n" \
  "s_add_u32 m0, m0, 0x1000\n" \
  "v_add_u32 v1, 0x1000, v1\n" \
  "global_load_lds_dwordx4 v1, s[12:13]\n" \
  "s_add_u32 m0, m0, 0x1000\n" \
  "v_add_u32 v1, 0x1000, v1\n" \
  "global_load_lds_dwordx4 v1, s[12:13]\n" \
  "s_add_u32 m0, m0, 0x1000\n" \
  "v_add_u32 v1, 0x1000, v1\n" \
  "v_min_u32 v1, 19984, v1\n" \
  "global_load_lds_dwordx4 v1, s[12:13]\n" \
  "global_load_dwordx4 v[232:235], v250, s[44:45] offset:0\n" \
  "global_load_dwordx4 v[236:239], v251, s[44:45] offset:256\n" \
  "global_load_dwordx4 v[240:243], v250, s[44:45] offset:1600\n" \
  "global_load_dwordx4 v[244:247], v251, s[44:45] offset:1856\n" \
  "s_setprio 1\n" \
  "v_lshrrev_b32 v1, 2, v252\n" \
  "v_and_b32 v2, 3, v252\n" \
  "v_cmp_eq_u32 vcc, v1, v253\n" \
  "v_mov_b32 v4, 0x3f80\n" \
  "v_and_b32 v13, 1, v253\n" \
  "v_lshlrev_b32 v14, 1, v13\n" \
  "v_xor_b32 v2, v2, v14\n" \
  "v_cndmask_b32 v2, 4, v2, vcc\n" \
  "v_cmp_eq_u32 vcc, 0, v2\n" \
  "s_nop 1\n" \
  "v_cndmask_b32 v216, 0, v4, vcc\n" \
  "v_lshlrev_b32 v220, 16, v216\n" \
  "v_cmp_eq_u32 vcc, 1, v2\n" \
  "s_nop 1\n" \
  "v_cndmask_b32 v217, 0, v4, vcc\n" \
  "v_lshlrev_b32 v221, 16, v217\n" \
  "v_cmp_eq_u32 vcc, 2, v2\n" \
  "s_nop 1\n" \
  "v_cndmask_b32 v218, 0, v4, vcc\n" \
  "v_lshlrev_b32 v222, 16, v218\n" \
  "v_cmp_eq_u32 vcc, 3, v2\n" \
  "s_nop 1\n" \
  "v_cndmask_b32 v219, 0, v4, vcc\n" \
  "v_lshlrev_b32 v223, 16, v219\n" \
  "v_and_b32 v1, 2, v252\n" \
  "v_lshlrev_b32 v1, 3, v1\n" \
  "v_lshlrev_b32 v1, v1, v4\n" \
  "v_and_b32 v2, 0x31, v255\n" \
  "v_mov_b64 v[224:225], 0\n" \
  "v_cmp_eq_u32 vcc, 0, v2\n" \
  "s_nop 1\n" \
  "v_cndmask_b32 v226, 0, v1, vcc\n" \
  "v_cmp_eq_u32 vcc, 1, v2\n" \
  "s_nop 1\n" \
  "v_cndmask_b32 v227, 0, v1, vcc\n" \
  "v_mul_u32_u24 v5, 400, v252\n" \
  "v_add_u32 v5, 138240, v5\n" \
  "v_lshl_add_u32 v6, v253, 4, v5\n" \
  "v_add_u32 v7, 6400, v5\n" \
  "v_add_u32 v8, 6400, v6\n" \
  "v_add_u32 v9, 12800, v5\n" \
  "v_add_u32 v10, 12800, v6\n" \
  "v_bfe_u32 v1, v252, 1, 1\n" \
  "v_and_b32 v2, 1, v252\n" \
  "v_lshl_add_u32 v1, v2, 1, v1\n" \
  "v_mul_u32_u24 v1, 200, v1\n" \
  "v_add_u32 v11, 157440, v1\n" \
  "v_lshl_add_u32 v12, v253, 4, v11\n" \
  "v_lshlrev_b32 v13, 3, v13\n" \
  "v_sub_u32 v16, v5, v13\n" \
  "v_add_u32 v5, v5, v13\n" \
  "v_sub_u32 v17, v6, v13\n" \
  "v_add_u32 v6, v6, v13\n" \
  "v_sub_u32 v18, v7, v13\n" \
  "v_add_u32 v7, v7, v13\n" \
  "v_sub_u32 v19, v8, v13\n" \
  "v_add_u32 v8, v8, v13\n" \
  "v_sub_u32 v20, v9, v13\n" \
  "v_add_u32 v9, v9, v13\n" \
  "v_sub_u32 v21, v10, v13\n" \
  "v_add_u32 v10, v10, v13\n" \
  "v_sub_u32 v22, v11, v13\n" \
  "v_add_u32 v11, v11, v13\n" \
  "v_sub_u32 v23, v12, v13\n" \
  "v_add_u32 v12, v12, v13\n" \
  "v_lshlrev_b32 v188, 4, v253\n" \
  "v_and_b32 v96, 4, v252\n" \
  "v_lshl_add_u32 v96, v253, 5, v96\n" \
  "v_mov_b32 v191, 0x40000000\n" \
  "v_cmp_gt_u32 vcc, 8, v252\n" \
  "v_mov_b32 v193, 0x4444\n" \
  "v_mov_b32 v98, 0xeeee\n" \
  "v_cndmask_b32 v189, v191, v96, vcc\n" \
  "v_cndmask_b32 v190, v96, v191, vcc\n" \
  "v_and_b32 v99, 4, v252\n" \
  "v_cmp_ne_u32 vcc, 0, v99\n" \
  "s_lshl_b32 s35, s28, 11\n" \
  "s_add_u32 s35, s35, 130048\n" \
  "v_cndmask_b32 v193, v193, v98, vcc\n" \
  "v_lshl_add_u32 v194, v252, 4, s35\n" \
  "v_and_b32 v96, 7, v252\n" \
  "v_lshlrev_b32 v96, 5, v96\n" \
  "v_lshl_add_u32 v96, v253, 8, v96\n" \
  "v_lshrrev_b32 v97, 3, v252\n" \
  "v_lshl_add_u32 v96, v97, 2, v96\n" \
  "v_add_u32 v195, s35, v96\n" \
  "v_and_b32 v97, v97, v252\n" \
  "v_lshlrev_b32 v192, 4, v97\n" \
  "s_waitcnt vmcnt(4)\n" \
  "s_barrier\n" \
  "ds_read_b64 v[200:201], v12 offset:128\n" \
  "ds_read_b64 v[202:203], v23 offset:136\n" \
  "ds_read_b64 v[184:185], v9 offset:184\n" \
  "ds_read_b64 v[186:187], v20 offset:192\n" \
  "ds_read_b64 v[212:213], v9 offset:384\n" \
  "ds_read_b64 v[214:215], v20 offset:392\n" \
  "ds_read_b64 v[196:197], v12 offset:64\n" \
  "ds_read_b64 v[198:199], v23 offset:72\n" \
  "ds_read_b64 v[176:177], v7 offset:184\n" \
  "ds_read_b64 v[178:179], v18 offset:192\n" \
  "ds_read_b64 v[180:181], v7 offset:384\n" \
  "ds_read_b64 v[182:183], v18 offset:392\n" \
  "ds_read_b64 v[228:229], v12 offset:0\n" \
  "ds_read_b64 v[230:231], v23 offset:8\n" \
  "s_waitcnt lgkmcnt(12)\n" \
  "v_mfma_f32_16x16x32_bf16 v[44:47], v[200:203], v[216:219], 0\n" \
  "ds_read_b64 v[168:169], v5 offset:184\n" \
  "ds_read_b64 v[170:171], v16 offset:192\n" \
  "s_waitcnt lgkmcnt(12)\n" \
  "v_mfma_f32_16x16x32_bf16 v[44:47], v[224:227], v[184:187], v[44:47]\n" \
  "ds_read_b64 v[172:173], v5 offset:384\n" \
  "ds_read_b64 v[174:175], v16 offset:392\n" \
  "v_mfma_f32_16x16x32_bf16 v[208:211], v[200:203], v[220:223], 0\n" \
  "ds_read_b64 v[96:97], v6 offset:0\n" \
  "ds_read_b64 v[98:99], v17 offset:8\n" \
  "s_waitcnt lgkmcnt(14)\n" \
  "v_mfma_f32_16x16x32_bf16 v[208:211], v[224:227], v[212:215], v[208:211]\n" \
  "ds_read_b64 v[124:125], v8 offset:64\n" \
  "ds_read_b64 v[126:127], v19 offset:72\n" \
  "s_waitcnt lgkmcnt(14)\n" \
  "v_mfma_f32_16x16x32_bf16 v[60:63], v[196:199], v[216:219], 0\n" \
  "ds_read_b64 v[136:137], v8 offset:264\n" \
  "ds_read_b64 v[138:139], v19 offset:272\n" \
  "s_waitcnt lgkmcnt(14)\n" \
  "v_mfma_f32_16x16x32_bf16 v[60:63], v[224:227], v[176:179], v[60:63]\n" \
  "ds_read_b64 v[140:141], v8 offset:328\n" \
  "ds_read_b64 v[142:143], v19 offset:336\n" \
  "v_mfma_f32_16x16x32_bf16 v[184:187], v[196:199], v[220:223], 0\n" \
  "ds_read_b64 v[160:161], v10 offset:264\n" \
  "ds_read_b64 v[162:163], v21 offset:272\n" \
  "s_waitcnt lgkmcnt(15)\n" \
  "v_mfma_f32_16x16x32_bf16 v[184:187], v[224:227], v[180:183], v[184:187]\n" \
  "ds_read_b64 v[116:117], v6 offset:328\n" \
  "ds_read_b64 v[118:119], v17 offset:336\n" \
  "s_waitcnt lgkmcnt(15)\n" \
  "v_mfma_f32_16x16x32_bf16 v[68:71], v[228:231], v[216:219], 0\n" \
  "ds_read_b64 v[144:145], v10 offset:0\n" \
  "ds_read_b64 v[146:147], v21 offset:8\n" \
  "s_waitcnt lgkmcnt(15)\n" \
  "v_mfma_f32_16x16x32_bf16 v[68:71], v[224:227], v[168:171], v[68:71]\n" \
  "ds_read_b64 v[148:149], v10 offset:64\n" \
  "ds_read_b64 v[150:151], v21 offset:72\n" \
  "v_mfma_f32_16x16x32_bf16 v[212:215], v[228:231], v[220:223], 0\n" \
  "ds_read_b64 v[104:105], v6 offset:128\n" \
  "ds_read_b64 v[106:107], v17 offset:136\n" \
  "s_waitcnt lgkmcnt(15)\n" \
  "v_mfma_f32_16x16x32_bf16 v[212:215], v[224:227], v[172:175], v[212:215]\n" \
  "ds_read_b64 v[100:101], v6 offset:64\n" \
  "ds_read_b64 v[102:103], v17 offset:72\n" \
  "v_mfma_f32_16x16x32_bf16 v[92:95], v[96:99], v[216:219], 0\n" \
  "ds_read_b64 v[120:121], v8 offset:0\n" \
  "ds_read_b64 v[122:123], v19 offset:8\n" \
  "s_waitcnt lgkmcnt(15)\n" \
  "v_mfma_f32_16x16x32_bf16 v[24:27], v[124:127], v[220:223], 0\n" \
  "ds_read_b64 v[112:113], v6 offset:264\n" \
  "ds_read_b64 v[114:115], v17 offset:272\n" \
  "v_mfma_f32_16x16x32_bf16 v[24:27], v[216:219], v[136:139], v[24:27]\n" \
  "ds_read_b64 v[156:157], v10 offset:200\n" \
  "ds_read_b64 v[158:159], v21 offset:208\n" \
  "s_waitcnt lgkmcnt(15)\n" \
  "v_mfma_f32_16x16x32_bf16 v[28:31], v[140:143], v[220:223], 0\n" \
  "ds_read_b64 v[132:133], v8 offset:200\n" \
  "ds_read_b64 v[134:135], v19 offset:208\n" \
  "v_mfma_f32_16x16x32_bf16 v[28:31], v[220:223], v[160:163], v[28:31]\n" \
  "ds_read_b64 v[108:109], v6 offset:200\n" \
  "ds_read_b64 v[110:111], v17 offset:208\n" \
  "s_waitcnt lgkmcnt(15)\n" \
  "v_mfma_f32_16x16x32_bf16 v[32:35], v[116:119], v[216:219], 0\n" \
  "ds_read_b64 v[152:153], v10 offset:128\n" \
  "ds_read_b64 v[154:155], v21 offset:136\n" \
  "v_mfma_f32_16x16x32_bf16 v[32:35], v[220:223], v[144:147], v[32:35]\n" \
  "ds_read_b64 v[164:165], v10 offset:328\n" \
  "ds_read_b64 v[166:167], v21 offset:336\n" \
  "v_mfma_f32_16x16x32_bf16 v[36:39], v[140:143], v[216:219], 0\n" \
  "ds_read_b64 v[128:129], v8 offset:128\n" \
  "ds_read_b64 v[130:131], v19 offset:136\n" \
  "s_waitcnt lgkmcnt(15)\n" \
  "v_mfma_f32_16x16x32_bf16 v[36:39], v[220:223], v[148:151], v[36:39]\n" \
  "ds_read_b64 v[204:205], v11 offset:184\n" \
  "ds_read_b64 v[206:207], v22 offset:192\n" \
  "v_mfma_f32_16x16x32_bf16 v[40:43], v[104:107], v[216:219], 0\n" \
  "v_mfma_f32_16x16x32_bf16 v[40:43], v[216:219], v[144:147], v[40:43]\n" \
  "v_mfma_f32_16x16x32_bf16 v[48:51], v[100:103], v[216:219], 0\n" \
  "s_waitcnt lgkmcnt(15)\n" \
  "v_mfma_f32_16x16x32_bf16 v[48:51], v[216:219], v[120:123], v[48:51]\n" \
  "s_waitcnt lgkmcnt(14)\n" \
  "v_mfma_f32_16x16x32_bf16 v[52:55], v[112:115], v[216:219], 0\n" \
  "v_mfma_f32_16x16x32_bf16 v[52:55], v[220:223], v[120:123], v[52:55]\n" \
  "v_mfma_f32_16x16x32_bf16 v[56:59], v[116:119], v[220:223], 0\n" \
  "s_waitcnt lgkmcnt(12)\n" \
  "v_mfma_f32_16x16x32_bf16 v[56:59], v[220:223], v[156:159], v[56:59]\n" \
  "s_waitcnt vmcnt(0) lgkmcnt(0)\n" \
  "v_mfma_f32_16x16x32_bf16 v[64:67], v[104:107], v[220:223], 0\n" \
  "v_cvt_pk_u16_u32 v232, v232, v233\n" \
  "v_cvt_pk_u16_u32 v234, v234, v235\n" \
  "v_mfma_f32_16x16x32_bf16 v[64:67], v[216:219], v[156:159], v[64:67]\n" \
  "v_cvt_pk_u16_u32 v236, v236, v237\n" \
  "v_cvt_pk_u16_u32 v238, v238, v239\n" \
  "v_mfma_f32_16x16x32_bf16 v[72:75], v[136:139], v[220:223], 0\n" \
  "v_pk_lshrrev_b16 v233, 3, v232 op_sel_hi:[0,1]\n" \
  "v_pk_lshrrev_b16 v235, 3, v234 op_sel_hi:[0,1]\n" \
  "v_mfma_f32_16x16x32_bf16 v[76:79], v[112:115], v[220:223], 0\n" \
  "v_pk_lshrrev_b16 v237, 3, v236 op_sel_hi:[0,1]\n" \
  "v_pk_lshrrev_b16 v239, 3, v238 op_sel_hi:[0,1]\n" \
  "v_mfma_f32_16x16x32_bf16 v[76:79], v[220:223], v[132:135], v[76:79]\n" \
  "v_pk_mad_u16 v180, v232, 8, v233 op_sel_hi:[1,0,1]\n" \
  "v_pk_mad_u16 v181, v234, 8, v235 op_sel_hi:[1,0,1]\n" \
  "v_mfma_f32_16x16x32_bf16 v[80:83], v[100:103], v[220:223], 0\n" \
  "v_pk_mad_u16 v182, v236, 8, v237 op_sel_hi:[1,0,1]\n" \
  "v_pk_mad_u16 v183, v238, 8, v239 op_sel_hi:[1,0,1]\n" \
  "v_mfma_f32_16x16x32_bf16 v[80:83], v[216:219], v[132:135], v[80:83]\n" \
  "v_alignbit_b32 v182, v182, v182, v192\n" \
  "v_alignbit_b32 v183, v183, v183, v192\n" \
  "v_mfma_f32_16x16x32_bf16 v[84:87], v[96:99], v[220:223], 0\n" \
  "v_cvt_pk_u16_u32 v240, v240, v241\n" \
  "v_cvt_pk_u16_u32 v242, v242, v243\n" \
  "v_mfma_f32_16x16x32_bf16 v[84:87], v[216:219], v[108:111], v[84:87]\n" \
  "v_cvt_pk_u16_u32 v244, v244, v245\n" \
  "v_cvt_pk_u16_u32 v246, v246, v247\n" \
  "v_mfma_f32_16x16x32_bf16 v[88:91], v[108:111], v[220:223], 0\n" \
  "v_pk_lshrrev_b16 v241, 3, v240 op_sel_hi:[0,1]\n" \
  "v_pk_lshrrev_b16 v243, 3, v242 op_sel_hi:[0,1]\n" \
  "v_mfma_f32_16x16x32_bf16 v[0:3], v[124:127], v[216:219], 0\n" \
  "v_pk_lshrrev_b16 v245, 3, v244 op_sel_hi:[0,1]\n" \
  "v_pk_lshrrev_b16 v247, 3, v246 op_sel_hi:[0,1]\n" \
  "v_mfma_f32_16x16x32_bf16 v[4:7], v[152:155], v[216:219], 0\n" \
  "v_pk_mad_u16 v196, v240, 8, v241 op_sel_hi:[1,0,1]\n" \
  "v_pk_mad_u16 v197, v242, 8, v243 op_sel_hi:[1,0,1]\n" \
  "v_mfma_f32_16x16x32_bf16 v[8:11], v[152:155], v[220:223], 0\n" \
  "v_pk_mad_u16 v198, v244, 8, v245 op_sel_hi:[1,0,1]\n" \
  "v_pk_mad_u16 v199, v246, 8, v247 op_sel_hi:[1,0,1]\n" \
  "v_mfma_f32_16x16x32_bf16 v[8:11], v[216:219], v[164:167], v[8:11]\n" \
  "v_alignbit_b32 v198, v198, v198, v192\n" \
  "v_alignbit_b32 v199, v199, v199, v192\n" \
  "v_mfma_f32_16x16x32_bf16 v[12:15], v[164:167], v[220:223], 0\n" \
  "v_add_u32 v200, 0x400, v195\n" \
  "v_mfma_f32_16x16x32_bf16 v[16:19], v[128:131], v[220:223], 0\n" \
  "v_mfma_f32_16x16x32_bf16 v[16:19], v[216:219], v[160:163], v[16:19]\n" \
  "v_mfma_f32_16x16x32_bf16 v[20:23], v[128:131], v[216:219], 0\n" \
  "v_mfma_f32_16x16x32_bf16 v[20:23], v[216:219], v[148:151], v[20:23]\n" \
  "v_mfma_f32_16x16x32_bf16 v[168:171], v[216:219], v[96:99], 0\n" \
  "v_mfma_f32_16x16x32_bf16 v[172:175], v[204:207], v[224:227], 0\n" \
  "v_mfma_f32_16x16x32_bf16 v[176:179], v[224:227], v[204:207], 0\n" \
  "s_mov_b32 exec_lo, 0x0\n" \
  "s_mov_b32 exec_hi, 0xffffffff\n" \
  "v_mov_b64 v[44:45], v[208:209]\n" \
  "v_mov_b64 v[46:47], v[210:211]\n" \
  "v_mov_b64 v[60:61], v[184:185]\n" \
  "v_mov_b64 v[62:63], v[186:187]\n" \
  "v_mov_b64 v[68:69], v[212:213]\n" \
  "v_mov_b64 v[70:71], v[214:215]\n" \
  "s_mov_b32 exec_lo, 0xff00ff00\n" \
  "s_mov_b32 exec_hi, 0x0\n" \
  "v_add_f32 v92, v92, v168\n" \
  "v_add_f32 v93, v93, v169\n" \
  "v_add_f32 v94, v94, v170\n" \
  "v_add_f32 v95, v95, v171\n" \
  "s_mov_b32 exec_lo, 0xf0000f00\n" \
  "s_mov_b32 exec_hi, 0x0\n" \
  "v_sub_f32 v92, v92, v176\n" \
  "v_sub_f32 v93, v93, v177\n" \
  "v_sub_f32 v94, v94, v178\n" \
  "v_sub_f32 v95, v95, v179\n" \
  "s_mov_b32 exec_lo, 0x0\n" \
  "s_mov_b32 exec_hi, 0xff00ff\n" \
  "v_mov_b64 v[92:93], 0\n" \
  "v_mov_b64 v[94:95], 0\n" \
  "s_mov_b32 exec_lo, 0x0\n" \
  "s_mov_b32 exec_hi, 0xf0000f\n" \
  "v_mov_b64 v[92:93], v[172:173]\n" \
  "v_mov_b64 v[94:95], v[174:175]\n" \
  "s_mov_b64 exec, -1\n" \
  "v_add_u32 v96, 6400, v250\n" \
  "v_add_u32 v97, 6400, v251\n" \
  "v_mov_b32 v98, 163328\n" \
  "v_mov_b32 v99, 1\n" \
  "v_lshlrev_b32 v179, 4, v253\n" \
  "s_lshr_b32 s35, s30, 4\n" \
  "s_lshl1_add_u32 s35, s29, s35\n" \
  "s_lshl_b32 s35, s35, 6\n" \
  "s_bfe_u32 s37, s30, 0x20002\n" \
  "s_lshl3_add_u32 s35, s37, s35\n" \
  "v_and_b32 v179, 16, v179\n" \
  "v_lshl_add_u32 v179, v179, 1, s35\n" \
  "s_mov_b64 exec, 1\n" \
  "ds_add_u32 v98, v99 offset:16\n" \
  "s_mov_b64 exec, -1\n" \
  "ds_write2_b32 v195, v180, v181 offset0:0 offset1:4\n" \
  "ds_write2_b32 v195, v182, v183 offset0:2 offset1:6\n" \
  "ds_write2_b32 v200, v196, v197 offset0:0 offset1:4\n" \
  "ds_write2_b32 v200, v198, v199 offset0:2 offset1:6\n" \
  "global_load_dwordx2 v[204:205], v179, s[16:17]\n" \
  "global_load_dwordx2 v[206:207], v179, s[18:19]\n" \
  "v_mov_b32 v179, 0\n" \
  "global_load_ushort v179, v179, s[20:21]\n" \
  "s_add_u32 s46, s44, 4800\n" \
  "s_addc_u32 s47, s45, 0\n" \
  "global_load_dwordx4 v[208:211], v96, s[44:45] offset:-3200\n" \
  "global_load_dwordx4 v[212:215], v97, s[44:45] offset:-2944\n" \
  "global_load_dwordx4 v[216:219], v96, s[44:45] offset:-1600\n" \
  "global_load_dwordx4 v[220:223], v97, s[44:45] offset:-1344\n" \
  "global_load_dwordx4 v[224:227], v96, s[44:45] offset:0\n" \
  "global_load_dwordx4 v[228:231], v97, s[44:45] offset:256\n" \
  "global_load_dwordx4 v[232:235], v96, s[44:45] offset:1600\n" \
  "global_load_dwordx4 v[236:239], v97, s[44:45] offset:1856\n" \
  "global_load_dwordx4 v[240:243], v96, s[44:45] offset:3200\n" \
  "global_load_dwordx4 v[244:247], v97, s[44:45] offset:3456\n" \
  "global_load_dwordx4 v[248:251], v96, s[46:47] offset:0\n" \
  "global_load_dwordx4 v[252:255], v97, s[46:47] offset:256\n" \
  "ds_read_b128 v[180:183], v194\n" \
  "v_add_u32 v194, 0x100, v194\n" \
  "L1_:\n" \
  "ds_read_b32 v99, v98 offset:12\n" \
  "s_waitcnt lgkmcnt(0)\n" \
  "v_readfirstlane_b32 s35, v99\n" \
  "s_cmp_lt_u32 s35, 4\n" \
  "s_cbranch_scc1 L1_\n" \
  "s_setprio 1\n" \
  "v_mad_u32_u16 v184, v182, 16, v188\n" \
  "ds_read_b128 v[128:131], v184 offset:0\n" \
  "ds_read_b128 v[132:135], v184 offset:64\n" \
  "ds_read_b128 v[172:175], v191\n" \
  "v_mad_u32_u16 v185, v181, 16, v188\n" \
  "v_mad_u32_u16 v186, v182, 16, v188 op_sel:[1,0,0,0]\n" \
  "ds_read_b128 v[112:115], v185 offset:0\n" \
  "ds_read_b128 v[116:119], v185 offset:64\n" \
  "ds_read_b128 v[136:139], v186 offset:0\n" \
  "ds_read_b128 v[140:143], v186 offset:64\n" \
  "ds_read_b128 v[168:171], v191\n" \
  "v_mad_u32_u16 v187, v181, 16, v188 op_sel:[1,0,0,0]\n" \
  "v_mad_u32_u16 v184, v180, 16, v188 op_sel:[1,0,0,0]\n" \
  "ds_read_b128 v[120:123], v187 offset:0\n" \
  "v_mad_u32_u16 v185, v180, 16, v188\n" \
  "ds_read_b128 v[124:127], v187 offset:64\n" \
  "ds_read_b128 v[104:107], v184 offset:0\n" \
  "v_mad_u32_u16 v186, v183, 16, v189\n" \
  "ds_read_b128 v[108:111], v184 offset:64\n" \
  "ds_read_b128 v[96:99], v185 offset:0\n" \
  "s_waitcnt lgkmcnt(9)\n" \
  "s_mov_b32 s51, 3\n" \
  "s_mov_b32 s50, 8\n" \
  "s_call_b64 s[52:53], LA_loop\n" \
  "LA_ev0:\n" \
  "s_waitcnt vmcnt(10)\n" \
  "v_cvt_pk_u16_u32 v208, v208, v209\n" \
  "v_cvt_pk_u16_u32 v210, v210, v211\n" \
  "v_cvt_pk_u16_u32 v212, v212, v213\n" \
  "v_cvt_pk_u16_u32 v214, v214, v215\n" \
  "v_pk_lshrrev_b16 v209, 3, v208 op_sel_hi:[0,1]\n" \
  "v_pk_lshrrev_b16 v211, 3, v210 op_sel_hi:[0,1]\n" \
  "v_pk_lshrrev_b16 v213, 3, v212 op_sel_hi:[0,1]\n" \
  "v_pk_lshrrev_b16 v215, 3, v214 op_sel_hi:[0,1]\n" \
  "v_pk_mad_u16 v208, v208, 8, v209 op_sel_hi:[1,0,1]\n" \
  "v_pk_mad_u16 v210, v210, 8, v211 op_sel_hi:[1,0,1]\n" \
  "v_pk_mad_u16 v212, v212, 8, v213 op_sel_hi:[1,0,1]\n" \
  "v_pk_mad_u16 v214, v214, 8, v215 op_sel_hi:[1,0,1]\n" \
  "v_alignbit_b32 v212, v212, v212, v192\n" \
  "v_alignbit_b32 v214, v214, v214, v192\n" \
  "ds_write2_b32 v195, v208, v210 offset0:0 offset1:4\n" \
  "ds_write2_b32 v195, v212, v214 offset0:2 offset1:6\n" \
  "v_mov_b32 v196, v178\n" \
  "v_readfirstlane_b32 s6, v179\n" \
  "v_mov_b32 v208, 163328\n" \
  "s_mov_b32 s51, 4\n" \
  "s_mov_b32 s50, 1\n" \
  "s_mov_b32 s38, 0x800\n" \
  "s_call_b64 s[52:53], LA_loop\n" \
  "LA_ev6:\n" \
  "v_mov_b32 v202, v178\n" \
  "v_readfirstlane_b32 s35, v209\n" \
  "s_cmp_ge_u32 s35, 128\n" \
  "s_cbranch_scc1 LA_pl\n" \
  "L2_:\n" \
  "ds_read_b32 v209, v208 offset:4\n" \
  "s_waitcnt lgkmcnt(0)\n" \
  "v_readfirstlane_b32 s35, v209\n" \
  "s_cmp_lt_u32 s35, 128\n" \
  "s_cbranch_scc1 L2_\n" \
  "LA_pl:\n" \
  "s_and_b32 s4, s35, 15\n" \
  "s_sub_u32 s4, s4, 4\n" \
  "s_bfe_u32 s5, s35, 0x30004\n" \
  "s_sub_u32 s5, s5, s29\n" \
  "s_cmp_le_i32 s4, 0\n" \
  "s_cbranch_scc1 LA_nt\n" \
  "s_mul_i32 s35, s5, 51200\n" \
  "s_ashr_i32 s36, s35, 31\n" \
  "s_add_u32 s46, s46, s35\n" \
  "s_addc_u32 s47, s47, s36\n" \
  "v_lshrrev_b32 v216, 4, v188\n" \
  "v_mul_u32_u24 v216, 400, v216\n" \
  "v_add_u32 v216, 6400, v216\n" \
  "v_bfe_u32 v218, v194, 4, 4\n" \
  "v_min_u32 v217, 8, v218\n" \
  "v_lshl_add_u32 v217, v217, 4, v216\n" \
  "v_lshl_add_u32 v216, v218, 4, v216\n" \
  "global_load_dwordx4 v[208:211], v216, s[46:47] offset:0\n" \
  "global_load_dwordx4 v[212:215], v217, s[46:47] offset:256\n" \
  "LA_nt:\n" \
  "s_lshl_b32 s5, s5, 7\n" \
  "s_sub_u32 s5, s5, s4\n" \
  "s_min_i32 s51, s4, 0\n" \
  "s_add_u32 s51, s51, 4\n" \
  "s_cmp_eq_u32 s51, 0\n" \
  "s_cbranch_scc1 LA_ev7\n" \
  "s_call_b64 s[52:53], LA_loop\n" \
  "LA_ev7:\n" \
  "v_mov_b32 v203, v178\n" \
  "s_cmp_le_i32 s4, 0\n" \
  "s_cbranch_scc1 LA_ev8\n" \
  "s_waitcnt vmcnt(0)\n" \
  "v_cvt_pk_u16_u32 v208, v208, v209\n" \
  "v_cvt_pk_u16_u32 v210, v210, v211\n" \
  "v_cvt_pk_u16_u32 v212, v212, v213\n" \
  "v_cvt_pk_u16_u32 v214, v214, v215\n" \
  "v_pk_lshrrev_b16 v209, 3, v208 op_sel_hi:[0,1]\n" \
  "v_pk_lshrrev_b16 v211, 3, v210 op_sel_hi:[0,1]\n" \
  "v_pk_lshrrev_b16 v213, 3, v212 op_sel_hi:[0,1]\n" \
  "v_pk_lshrrev_b16 v215, 3, v214 op_sel_hi:[0,1]\n" \
  "v_pk_mad_u16 v208, v208, 8, v209 op_sel_hi:[1,0,1]\n" \
  "v_pk_mad_u16 v210, v210, 8, v211 op_sel_hi:[1,0,1]\n" \
  "v_pk_mad_u16 v212, v212, 8, v213 op_sel_hi:[1,0,1]\n" \
  "v_pk_mad_u16 v214, v214, 8, v215 op_sel_hi:[1,0,1]\n" \
  "v_alignbit_b32 v212, v212, v212, v192\n" \
  "v_alignbit_b32 v214, v214, v214, v192\n" \
  "ds_write2_b32 v195, v208, v210 offset0:0 offset1:4\n" \
  "ds_write2_b32 v195, v212, v214 offset0:2 offset1:6\n" \
  "s_lshl_b32 s35, s4, 8\n" \
  "s_add_u32 s35, s35, 0x400\n" \
  "v_subrev_u32 v194, s35, v194\n" \
  "s_mov_b32 s51, s4\n" \
  "s_call_b64 s[52:53], LA_loop\n" \
  "LA_ev8:\n" \
  "v_mov_b32 v179, v178\n" \
  "ds_read_b128 v[100:103], v185 offset:64\n" \
  "ds_read2_b32 v[144:145], v186 offset0:0 offset1:2\n" \
  "ds_read2_b32 v[146:147], v186 offset0:4 offset1:6\n" \
  "v_mad_u32_u16 v187, v183, 16, v190\n" \
  "ds_read2_b32 v[148:149], v187 offset0:0 offset1:2\n" \
  "ds_read2_b32 v[150:151], v187 offset0:4 offset1:6\n" \
  "ds_read_b128 v[164:167], v191\n" \
  "s_lshr_b32 s35, s30, 2\n" \
  "s_lshl3_add_u32 s33, s29, s35\n" \
  "s_and_b32 s34, s30, 3\n" \
  "s_lshl_b32 s59, s27, 11\n" \
  "s_mul_i32 s35, s33, 1600000\n" \
  "s_add_u32 s54, s22, s35\n" \
  "s_addc_u32 s55, s23, 0\n" \
  "s_mul_i32 s35, s34, 819200\n" \
  "s_lshl1_add_u32 s35, s59, s35\n" \
  "s_add_u32 s35, s35, 102400000\n" \
  "s_add_u32 s56, s22, s35\n" \
  "s_addc_u32 s57, s23, 0\n" \
  "v_mbcnt_lo_u32_b32 v189, -1, 0\n" \
  "v_mbcnt_hi_u32_b32 v189, -1, v189\n" \
  "v_lshlrev_b32 v188, 4, v189\n" \
  "s_waitcnt lgkmcnt(6)\n" \
  "v_mfma_f32_16x16x32_bf16 v[152:155], v[112:115], v[112:115], 0\n" \
  "v_fmac_f32 v176, v156, v88\n" \
  "v_fmac_f32 v176, v157, v89\n" \
  "v_mfma_f32_16x16x32_bf16 v[152:155], v[116:119], v[116:119], v[152:155]\n" \
  "v_fmac_f32 v176, v158, v90\n" \
  "v_fmac_f32 v176, v159, v91\n" \
  "v_mfma_f32_16x16x32_bf16 v[156:159], v[128:131], v[128:131], 0\n" \
  "v_fmac_f32 v176, v160, v92\n" \
  "v_fmac_f32 v176, v161, v93\n" \
  "v_mfma_f32_16x16x32_bf16 v[156:159], v[132:135], v[132:135], v[156:159]\n" \
  "v_fmac_f32 v176, v162, v94\n" \
  "v_fma_f32 v177, v163, v95, v176\n" \
  "v_mfma_f32_16x16x32_bf16 v[160:163], v[128:131], v[136:139], 0\n" \
  "v_mul_f32 v176, v152, v0\n" \
  "v_fmac_f32 v176, v153, v1\n" \
  "v_mfma_f32_16x16x32_bf16 v[160:163], v[132:135], v[140:143], v[160:163]\n" \
  "v_fmac_f32 v176, v154, v2\n" \
  "v_fmac_f32 v176, v155, v3\n" \
  "v_mfma_f32_16x16x32_bf16 v[152:155], v[136:139], v[136:139], 0\n" \
  "v_fmac_f32 v176, v156, v4\n" \
  "v_fmac_f32 v176, v157, v5\n" \
  "v_mfma_f32_16x16x32_bf16 v[152:155], v[140:143], v[140:143], v[152:155]\n" \
  "v_fmac_f32 v176, v158, v6\n" \
  "v_fmac_f32 v176, v159, v7\n" \
  "s_waitcnt lgkmcnt(0)\n" \
  "s_setprio 3\n" \
  "s_barrier\n" \
  "global_load_dwordx4 v[208:211], v188, s[56:57] offset:0 sc1\n" \
  "global_load_dwordx4 v[212:215], v188, s[56:57] offset:1024 sc1\n" \
  "global_load_dwordx4 v[216:219], v188, s[56:57] offset:2048 sc1\n" \
  "global_load_dwordx4 v[220:223], v188, s[56:57] offset:3072 sc1\n" \
  "v_mfma_f32_16x16x32_bf16 v[156:159], v[112:115], v[136:139], 0\n" \
  "v_fmac_f32 v176, v160, v8\n" \
  "v_fmac_f32 v176, v161, v9\n" \
  "v_lshrrev_b32 v180, 1, v189\n" \
  "v_mfma_f32_16x16x32_bf16 v[156:159], v[116:119], v[140:143], v[156:159]\n" \
  "v_fmac_f32 v176, v162, v10\n" \
  "v_fmac_f32 v176, v163, v11\n" \
  "v_xor_b32 v180, v180, v189\n" \
  "v_mfma_f32_16x16x32_bf16 v[160:163], v[112:115], v[128:131], 0\n" \
  "v_fmac_f32 v176, v152, v12\n" \
  "v_fmac_f32 v176, v153, v13\n" \
  "v_add_f32_dpp v177, v177, v177 row_ror:8 row_mask:0xf bank_mask:0xf\n" \
  "v_and_b32 v180, 8, v180\n" \
  "v_mfma_f32_16x16x32_bf16 v[160:163], v[116:119], v[132:135], v[160:163]\n" \
  "v_fmac_f32 v176, v154, v14\n" \
  "v_fmac_f32 v176, v155, v15\n" \
  "v_bfe_u32 v181, v189, 2, 1\n" \
  "v_mfma_f32_16x16x32_bf16 v[152:155], v[112:115], v[120:123], 0\n" \
  "v_fmac_f32 v176, v156, v16\n" \
  "v_fmac_f32 v176, v157, v17\n" \
  "v_lshlrev_b32_e64 v181, v181, 5\n" \
  "v_mfma_f32_16x16x32_bf16 v[152:155], v[116:119], v[124:127], v[152:155]\n" \
  "v_fmac_f32 v176, v158, v18\n" \
  "v_fmac_f32 v176, v159, v19\n" \
  "v_mov_b32_dpp v178, v178 row_ror:4 row_mask:0xf bank_mask:0xf\n" \
  "v_lshlrev_b32 v180, v180, v181\n" \
  "v_mfma_f32_16x16x32_bf16 v[156:159], v[120:123], v[136:139], 0\n" \
  "v_fmac_f32 v176, v160, v20\n" \
  "v_fmac_f32 v176, v161, v21\n" \
  "v_bfe_u32 v181, v189, 2, 2\n" \
  "v_mfma_f32_16x16x32_bf16 v[156:159], v[124:127], v[140:143], v[156:159]\n" \
  "v_fmac_f32 v176, v162, v22\n" \
  "v_fmac_f32 v176, v163, v23\n" \
  "v_lshlrev_b32_e64 v181, v181, 1\n" \
  "v_mfma_f32_16x16x32_bf16 v[160:163], v[104:107], v[128:131], 0\n" \
  "v_fmac_f32 v176, v152, v24\n" \
  "v_fmac_f32 v176, v153, v25\n" \
  "v_cmp_gt_u32 vcc, 32, v189\n" \
  "v_mfma_f32_16x16x32_bf16 v[160:163], v[108:111], v[132:135], v[160:163]\n" \
  "v_fmac_f32 v176, v154, v26\n" \
  "v_fmac_f32 v176, v155, v27\n" \
  "v_mov_b32 v191, 0x3f80\n" \
  "v_mfma_f32_16x16x32_bf16 v[152:155], v[120:123], v[128:131], 0\n" \
  "v_fmac_f32 v176, v156, v28\n" \
  "v_fmac_f32 v176, v157, v29\n" \
  "v_add_u32 v190, 32000, v188\n" \
  "v_mfma_f32_16x16x32_bf16 v[152:155], v[124:127], v[132:135], v[152:155]\n" \
  "v_fmac_f32 v176, v158, v30\n" \
  "v_fmac_f32 v176, v159, v31\n" \
  "v_add_f32_dpp v178, v177, v177 row_ror:4 row_mask:0xf bank_mask:0x1\n" \
  "v_cndmask_b32 v192, v181, v180, vcc\n" \
  "v_mfma_f32_16x16x32_bf16 v[156:159], v[96:99], v[128:131], 0\n" \
  "v_fmac_f32 v176, v160, v32\n" \
  "v_fmac_f32 v176, v161, v33\n" \
  "v_bfe_i32 v180, v192, 0, 1\n" \
  "v_mfma_f32_16x16x32_bf16 v[156:159], v[100:103], v[132:135], v[156:159]\n" \
  "v_fmac_f32 v176, v162, v34\n" \
  "v_fmac_f32 v176, v163, v35\n" \
  "v_and_b32 v180, v180, v191\n" \
  "v_smfmac_f32_16x16x64_bf16 v[172:175], v[144:147], v[128:135], v193\n" \
  "v_fmac_f32 v176, v152, v36\n" \
  "v_fmac_f32 v176, v153, v37\n" \
  "v_bfe_i32 v181, v192, 1, 1\n" \
  "v_smfmac_f32_16x16x64_bf16 v[172:175], v[148:151], v[136:143], v193\n" \
  "v_fmac_f32 v176, v154, v38\n" \
  "v_fmac_f32 v176, v155, v39\n" \
  "v_and_b32 v181, v181, v191\n" \
  "v_mfma_f32_16x16x32_bf16 v[160:163], v[96:99], v[112:115], 0\n" \
  "v_fmac_f32 v176, v156, v40\n" \
  "v_fmac_f32 v176, v157, v41\n" \
  "v_bfe_i32 v182, v192, 2, 1\n" \
  "v_mfma_f32_16x16x32_bf16 v[160:163], v[100:103], v[116:119], v[160:163]\n" \
  "v_fmac_f32 v176, v158, v42\n" \
  "v_fmac_f32 v176, v159, v43\n" \
  "v_and_b32 v182, v182, v191\n" \
  "v_mfma_f32_16x16x32_bf16 v[152:155], v[104:107], v[112:115], 0\n" \
  "v_fmac_f32 v176, v172, v44\n" \
  "v_fmac_f32 v176, v173, v45\n" \
  "v_bfe_i32 v183, v192, 3, 1\n" \
  "v_mfma_f32_16x16x32_bf16 v[152:155], v[108:111], v[116:119], v[152:155]\n" \
  "v_fmac_f32 v176, v174, v46\n" \
  "v_fmac_f32 v176, v175, v47\n" \
  "v_and_b32 v183, v183, v191\n" \
  "v_mfma_f32_16x16x32_bf16 v[156:159], v[104:107], v[136:139], 0\n" \
  "v_fmac_f32 v176, v160, v48\n" \
  "v_fmac_f32 v176, v161, v49\n" \
  "v_and_b32 v192, 3, v189\n" \
  "v_mfma_f32_16x16x32_bf16 v[156:159], v[108:111], v[140:143], v[156:159]\n" \
  "v_fmac_f32 v176, v162, v50\n" \
  "v_fmac_f32 v176, v163, v51\n" \
  "v_mul_u32_u24 v192, 0x5555, v192\n" \
  "v_smfmac_f32_16x16x64_bf16 v[168:171], v[144:147], v[112:119], v193\n" \
  "v_fmac_f32 v176, v152, v52\n" \
  "v_fmac_f32 v176, v153, v53\n" \
  "v_add_u32 v189, 16000, v188\n" \
  "v_smfmac_f32_16x16x64_bf16 v[168:171], v[148:151], v[120:127], v193\n" \
  "v_fmac_f32 v176, v154, v54\n" \
  "v_fmac_f32 v176, v155, v55\n" \
  "v_add_u32 v191, 48000, v188\n" \
  "v_mfma_f32_16x16x32_bf16 v[160:163], v[96:99], v[136:139], 0\n" \
  "v_fmac_f32 v176, v156, v56\n" \
  "v_fmac_f32 v176, v157, v57\n" \
  "v_mfma_f32_16x16x32_bf16 v[160:163], v[100:103], v[140:143], v[160:163]\n" \
  "v_fmac_f32 v176, v158, v58\n" \
  "v_fmac_f32 v176, v159, v59\n" \
  "s_add_u32 s35, s59, 64000\n" \
  "s_add_u32 s54, s54, s35\n" \
  "s_addc_u32 s55, s55, 0\n" \
  "s_add_u32 s58, s59, 0x10000\n" \
  "s_mov_b32 m0, s58\n" \
  "s_add_u32 s35, s58, 0x8000\n" \
  "global_load_lds_dwordx4 v188, s[54:55] sc1\n" \
  "global_load_lds_dwordx4 v188, s[54:55] offset:1024 sc1\n" \
  "v_smfmac_f32_16x16x64_bf16 v[164:167], v[148:151], v[104:111], v193\n" \
  "v_fmac_f32 v176, v168, v60\n" \
  "v_fmac_f32 v176, v169, v61\n" \
  "v_smfmac_f32_16x16x64_bf16 v[164:167], v[144:147], v[96:103], v193\n" \
  "v_fmac_f32 v176, v170, v62\n" \
  "v_fmac_f32 v176, v171, v63\n" \
  "v_mfma_f32_16x16x32_bf16 v[152:155], v[120:123], v[120:123], 0\n" \
  "v_fmac_f32 v176, v160, v64\n" \
  "v_fmac_f32 v176, v161, v65\n" \
  "v_mfma_f32_16x16x32_bf16 v[152:155], v[124:127], v[124:127], v[152:155]\n" \
  "v_fmac_f32 v176, v162, v66\n" \
  "v_fmac_f32 v176, v163, v67\n" \
  "v_mfma_f32_16x16x32_bf16 v[156:159], v[104:107], v[120:123], 0\n" \
  "v_fmac_f32 v176, v164, v68\n" \
  "v_fmac_f32 v176, v165, v69\n" \
  "v_mfma_f32_16x16x32_bf16 v[156:159], v[108:111], v[124:127], v[156:159]\n" \
  "v_fmac_f32 v176, v166, v70\n" \
  "v_fmac_f32 v176, v167, v71\n" \
  "s_add_u32 m0, s58, 0x4000\n" \
  "s_cmp_lt_u32 s35, 0x28000\n" \
  "global_load_lds_dwordx4 v189, s[54:55] sc1\n" \
  "global_load_lds_dwordx4 v189, s[54:55] offset:1024 sc1\n" \
  "s_cselect_b32 s58, s35, s59\n" \
  "s_add_u32 s54, s54, 32000\n" \
  "s_addc_u32 s55, s55, 0\n" \
  "v_mfma_f32_16x16x32_bf16 v[160:163], v[96:99], v[120:123], 0\n" \
  "v_fmac_f32 v176, v152, v72\n" \
  "v_fmac_f32 v176, v153, v73\n" \
  "v_mfma_f32_16x16x32_bf16 v[160:163], v[100:103], v[124:127], v[160:163]\n" \
  "v_fmac_f32 v176, v154, v74\n" \
  "v_fmac_f32 v176, v155, v75\n" \
  "v_mfma_f32_16x16x32_bf16 v[152:155], v[96:99], v[104:107], 0\n" \
  "v_fmac_f32 v176, v156, v76\n" \
  "v_fmac_f32 v176, v157, v77\n" \
  "v_mfma_f32_16x16x32_bf16 v[152:155], v[100:103], v[108:111], v[152:155]\n" \
  "v_fmac_f32 v176, v158, v78\n" \
  "v_fmac_f32 v176, v159, v79\n" \
  "v_mfma_f32_16x16x32_bf16 v[156:159], v[104:107], v[104:107], 0\n" \
  "v_fmac_f32 v176, v160, v80\n" \
  "v_fmac_f32 v176, v161, v81\n" \
  "v_mfma_f32_16x16x32_bf16 v[156:159], v[108:111], v[108:111], v[156:159]\n" \
  "v_fmac_f32 v176, v162, v82\n" \
  "v_fmac_f32 v176, v163, v83\n" \
  "v_mfma_f32_16x16x32_bf16 v[160:163], v[148:151], v[144:147], 0\n" \
  "v_fmac_f32 v176, v152, v84\n" \
  "v_fmac_f32 v176, v153, v85\n" \
  "v_mfma_f32_16x16x32_bf16 v[160:163], v[96:99], v[96:99], v[160:163]\n" \
  "v_fmac_f32 v176, v154, v86\n" \
  "v_fmac_f32 v176, v155, v87\n" \
  "v_mfma_f32_16x16x32_bf16 v[160:163], v[100:103], v[100:103], v[160:163]\n" \
  "s_mov_b32 s51, 7\n" \
  "v_fmac_f32 v176, v156, v88\n" \
  "v_fmac_f32 v176, v157, v89\n" \
  "v_fmac_f32 v176, v158, v90\n" \
  "v_fmac_f32 v176, v159, v91\n" \
  "s_nop 2\n" \
  "v_fmac_f32 v176, v160, v92\n" \
  "v_fmac_f32 v176, v161, v93\n" \
  "v_fmac_f32 v176, v162, v94\n" \
  "v_fma_f32 v177, v163, v95, v176\n" \
  "s_nop 1\n" \
  "v_add_f32_dpp v177, v177, v177 row_ror:8 row_mask:0xf bank_mask:0xf\n" \
  "v_mov_b32_dpp v178, v178 row_ror:4 row_mask:0xf bank_mask:0xf\n" \
  "s_nop 1\n" \
  "v_add_f32_dpp v178, v177, v177 row_ror:4 row_mask:0xf bank_mask:0x1\n" \
  "v_mov_b32 v99, v179\n" \
  "s_nop 0\n" \
  "v_mov_b32_dpp v196, v178 row_ror:8 row_mask:0xf bank_mask:0xc\n" \
  "s_nop 1\n" \
  "v_permlane16_swap_b32 v196, v197\n" \
  "v_permlane16_swap_b32 v198, v199\n" \
  "v_permlane16_swap_b32 v200, v201\n" \
  "v_permlane16_swap_b32 v202, v203\n" \
  "v_permlane16_swap_b32 v179, v99\n" \
  "v_add_f32 v196, v196, v197\n" \
  "v_add_f32 v198, v198, v199\n" \
  "v_add_f32 v200, v200, v201\n" \
  "v_add_f32 v202, v202, v203\n" \
  "v_add_f32 v179, v179, v99\n" \
  "v_lshrrev_b32 v96, 4, v188\n" \
  "v_and_b32 v97, 1, v96\n" \
  "v_mov_b32 v99, v179\n" \
  "v_lshrrev_b32 v98, 4, v96\n" \
  "v_permlane32_swap_b32 v196, v198\n" \
  "v_permlane32_swap_b32 v200, v202\n" \
  "v_permlane32_swap_b32 v179, v99\n" \
  "v_add_f32 v196, v196, v198\n" \
  "v_add_f32 v200, v200, v202\n" \
  "v_add_f32 v179, v179, v99\n" \
  "v_cmp_eq_u32 vcc, 1, v97\n" \
  "v_lshlrev_b32 v97, 2, v97\n" \
  "v_add_lshl_u32 v97, v97, v98, 2\n" \
  "v_bfe_u32 v98, v96, 2, 2\n" \
  "v_add_f32_dpp v196, v196, v196 quad_perm:[1,0,3,2] row_mask:0xf bank_mask:0xf\n" \
  "v_add_f32_dpp v200, v200, v200 quad_perm:[1,0,3,2] row_mask:0xf bank_mask:0xf\n" \
  "v_add_f32_dpp v179, v179, v179 quad_perm:[1,0,3,2] row_mask:0xf bank_mask:0xf\n" \
  "v_sub_u32 v97, v97, v98\n" \
  "v_cndmask_b32 v196, v196, v200, vcc\n" \
  "v_add_u32 v97, 1, v97\n" \
  "s_min_i32 s35, s4, 0\n" \
  "s_add_u32 s36, s4, 32\n" \
  "s_add_u32 s37, s4, 29\n" \
  "v_add_f32_dpp v179, v179, v179 quad_perm:[2,3,0,1] row_mask:0xf bank_mask:0xf\n" \
  "v_add_f32_dpp v196, v196, v196 quad_perm:[2,3,0,1] row_mask:0xf bank_mask:0xf\n" \
  "s_mov_b32 exec_lo, 0x0\n" \
  "s_mov_b32 exec_hi, 0xaaaa0000\n" \
  "v_add_u32 v97, s35, v97\n" \
  "s_mov_b32 exec_lo, 0x5500\n" \
  "s_mov_b32 exec_hi, 0x0\n" \
  "v_add_u32 v97, s36, v97\n" \
  "s_mov_b32 exec_lo, 0x4444\n" \
  "s_mov_b32 exec_hi, 0x0\n" \
  "v_sub_u32 v97, s37, v98\n" \
  "v_mov_b32 v196, v179\n" \
  "s_mov_b64 exec, -1\n" \
  "v_cmp_lt_u32 vcc, 31, v97\n" \
  "v_add_u32 v98, s5, v97\n" \
  "s_lshl2_add_u32 s35, s7, s28\n" \
  "s_lshl_b32 s35, s35, 5\n" \
  "v_cndmask_b32 v97, v97, v98, vcc\n" \
  "v_add_lshl_u32 v97, v97, s35, 2\n" \
  "s_lshl_b32 s35, s31, 16\n" \
  "s_add_u32 s35, s35, 109871104\n" \
  "s_add_u32 s40, s22, s35\n" \
  "s_addc_u32 s41, s23, 0\n" \
  "v_min_u32 v196, -2, v196\n" \
  "v_lshlrev_b32 v184, 16, v204\n" \
  "v_and_b32 v185, 0xffff0000, v204\n" \
  "v_lshlrev_b32 v186, 16, v205\n" \
  "v_and_b32 v187, 0xffff0000, v205\n" \
  "s_barrier\n" \
  "s_add_u32 s56, s56, 32768\n" \
  "s_addc_u32 s57, s57, 0\n" \
  "global_load_dwordx4 v[224:227], v188, s[56:57] sc1\n" \
  "global_load_dwordx4 v[228:231], v188, s[56:57] offset:1024 sc1\n" \
  "global_load_dwordx4 v[232:235], v188, s[56:57] offset:2048 sc1\n" \
  "global_load_dwordx4 v[236:239], v188, s[56:57] offset:3072 sc1\n" \
  "s_add_u32 s56, s56, 32768\n" \
  "s_addc_u32 s57, s57, 0\n" \
  "s_mov_b32 exec_lo, 0x33337777\n" \
  "s_mov_b32 exec_hi, 0x33333333\n" \
  "global_store_dword v97, v196, s[40:41] sc1\n" \
  "s_mov_b64 exec, -1\n" \
  "s_waitcnt vmcnt(9)\n" \
  "s_setprio 0\n" \
  "s_branch LG_start\n" \
  ".p2align 6\n" \
  "LA_loop:\n" \
  "LA_mid:\n" \
  "v_mfma_f32_16x16x32_bf16 v[152:155], v[112:115], v[112:115], 0\n" \
  "s_waitcnt lgkmcnt(8)\n" \
  "ds_read_b128 v[100:103], v185 offset:64\n" \
  "v_fmac_f32 v176, v156, v88\n" \
  "v_fmac_f32 v176, v157, v89\n" \
  "v_mfma_f32_16x16x32_bf16 v[152:155], v[116:119], v[116:119], v[152:155]\n" \
  "ds_read2_b32 v[144:145], v186 offset0:0 offset1:2\n" \
  "v_fmac_f32 v176, v158, v90\n" \
  "v_fmac_f32 v176, v159, v91\n" \
  "v_mfma_f32_16x16x32_bf16 v[156:159], v[128:131], v[128:131], 0\n" \
  "ds_read2_b32 v[146:147], v186 offset0:4 offset1:6\n" \
  "v_fmac_f32 v176, v160, v92\n" \
  "v_fmac_f32 v176, v161, v93\n" \
  "v_mad_u32_u16 v187, v183, 16, v190\n" \
  "v_mfma_f32_16x16x32_bf16 v[156:159], v[132:135], v[132:135], v[156:159]\n" \
  "s_waitcnt lgkmcnt(10)\n" \
  "ds_read2_b32 v[148:149], v187 offset0:0 offset1:2\n" \
  "v_fmac_f32 v176, v162, v94\n" \
  "v_fma_f32 v177, v163, v95, v176\n" \
  "v_mfma_f32_16x16x32_bf16 v[160:163], v[128:131], v[136:139], 0\n" \
  "s_waitcnt lgkmcnt(10)\n" \
  "ds_read2_b32 v[150:151], v187 offset0:4 offset1:6\n" \
  "v_mul_f32 v176, v152, v0\n" \
  "v_fmac_f32 v176, v153, v1\n" \
  "v_mfma_f32_16x16x32_bf16 v[160:163], v[132:135], v[140:143], v[160:163]\n" \
  "ds_read_b128 v[180:183], v194\n" \
  "v_fmac_f32 v176, v154, v2\n" \
  "v_fmac_f32 v176, v155, v3\n" \
  "v_mfma_f32_16x16x32_bf16 v[152:155], v[136:139], v[136:139], 0\n" \
  "v_fmac_f32 v176, v156, v4\n" \
  "v_fmac_f32 v176, v157, v5\n" \
  "v_mfma_f32_16x16x32_bf16 v[152:155], v[140:143], v[140:143], v[152:155]\n" \
  "ds_read_b128 v[164:167], v191\n" \
  "v_fmac_f32 v176, v158, v6\n" \
  "v_fmac_f32 v176, v159, v7\n" \
  "v_mfma_f32_16x16x32_bf16 v[156:159], v[112:115], v[136:139], 0\n" \
  "v_fmac_f32 v176, v160, v8\n" \
  "v_fmac_f32 v176, v161, v9\n" \
  "v_mfma_f32_16x16x32_bf16 v[156:159], v[116:119], v[140:143], v[156:159]\n" \
  "v_fmac_f32 v176, v162, v10\n" \
  "v_fmac_f32 v176, v163, v11\n" \
  "v_mfma_f32_16x16x32_bf16 v[160:163], v[112:115], v[128:131], 0\n" \
  "v_fmac_f32 v176, v152, v12\n" \
  "v_fmac_f32 v176, v153, v13\n" \
  "v_add_f32_dpp v177, v177, v177 row_ror:8 row_mask:0xf bank_mask:0xf\n" \
  "v_mfma_f32_16x16x32_bf16 v[160:163], v[116:119], v[132:135], v[160:163]\n" \
  "s_waitcnt lgkmcnt(11)\n" \
  "v_fmac_f32 v176, v154, v14\n" \
  "v_fmac_f32 v176, v155, v15\n" \
  "v_mfma_f32_16x16x32_bf16 v[152:155], v[112:115], v[120:123], 0\n" \
  "s_waitcnt lgkmcnt(10)\n" \
  "v_fmac_f32 v176, v156, v16\n" \
  "v_fmac_f32 v176, v157, v17\n" \
  "v_mfma_f32_16x16x32_bf16 v[152:155], v[116:119], v[124:127], v[152:155]\n" \
  "v_fmac_f32 v176, v158, v18\n" \
  "v_fmac_f32 v176, v159, v19\n" \
  "v_mov_b32_dpp v178, v178 row_ror:4 row_mask:0xf bank_mask:0xf\n" \
  "v_mfma_f32_16x16x32_bf16 v[156:159], v[120:123], v[136:139], 0\n" \
  "v_fmac_f32 v176, v160, v20\n" \
  "v_fmac_f32 v176, v161, v21\n" \
  "v_mfma_f32_16x16x32_bf16 v[156:159], v[124:127], v[140:143], v[156:159]\n" \
  "s_waitcnt lgkmcnt(9)\n" \
  "v_fmac_f32 v176, v162, v22\n" \
  "v_fmac_f32 v176, v163, v23\n" \
  "v_mfma_f32_16x16x32_bf16 v[160:163], v[104:107], v[128:131], 0\n" \
  "s_waitcnt lgkmcnt(1)\n" \
  "v_fmac_f32 v176, v152, v24\n" \
  "v_fmac_f32 v176, v153, v25\n" \
  "v_mad_u32_u16 v184, v182, 16, v188\n" \
  "v_mfma_f32_16x16x32_bf16 v[160:163], v[108:111], v[132:135], v[160:163]\n" \
  "v_fmac_f32 v176, v154, v26\n" \
  "v_fmac_f32 v176, v155, v27\n" \
  "v_mfma_f32_16x16x32_bf16 v[152:155], v[120:123], v[128:131], 0\n" \
  "v_fmac_f32 v176, v156, v28\n" \
  "v_fmac_f32 v176, v157, v29\n" \
  "v_mfma_f32_16x16x32_bf16 v[152:155], v[124:127], v[132:135], v[152:155]\n" \
  "v_fmac_f32 v176, v158, v30\n" \
  "v_fmac_f32 v176, v159, v31\n" \
  "v_add_f32_dpp v178, v177, v177 row_ror:4 row_mask:0xf bank_mask:0x1\n" \
  "v_mfma_f32_16x16x32_bf16 v[156:159], v[96:99], v[128:131], 0\n" \
  "v_fmac_f32 v176, v160, v32\n" \
  "v_fmac_f32 v176, v161, v33\n" \
  "v_mfma_f32_16x16x32_bf16 v[156:159], v[100:103], v[132:135], v[156:159]\n" \
  "v_fmac_f32 v176, v162, v34\n" \
  "v_fmac_f32 v176, v163, v35\n" \
  "v_smfmac_f32_16x16x64_bf16 v[172:175], v[144:147], v[128:135], v193\n" \
  "ds_read_b128 v[128:131], v184 offset:0\n" \
  "v_fmac_f32 v176, v152, v36\n" \
  "v_fmac_f32 v176, v153, v37\n" \
  "v_smfmac_f32_16x16x64_bf16 v[172:175], v[148:151], v[136:143], v193\n" \
  "ds_read_b128 v[132:135], v184 offset:64\n" \
  "v_fmac_f32 v176, v154, v38\n" \
  "v_fmac_f32 v176, v155, v39\n" \
  "v_mfma_f32_16x16x32_bf16 v[160:163], v[96:99], v[112:115], 0\n" \
  "v_fmac_f32 v176, v156, v40\n" \
  "v_fmac_f32 v176, v157, v41\n" \
  "v_mfma_f32_16x16x32_bf16 v[160:163], v[100:103], v[116:119], v[160:163]\n" \
  "v_fmac_f32 v176, v158, v42\n" \
  "v_fmac_f32 v176, v159, v43\n" \
  "v_add_u32 v194, 0x100, v194\n" \
  "v_mfma_f32_16x16x32_bf16 v[152:155], v[104:107], v[112:115], 0\n" \
  "v_fmac_f32 v176, v172, v44\n" \
  "v_fmac_f32 v176, v173, v45\n" \
  "v_mfma_f32_16x16x32_bf16 v[152:155], v[108:111], v[116:119], v[152:155]\n" \
  "v_fmac_f32 v176, v174, v46\n" \
  "v_fmac_f32 v176, v175, v47\n" \
  "v_mfma_f32_16x16x32_bf16 v[156:159], v[104:107], v[136:139], 0\n" \
  "ds_read_b128 v[172:175], v191\n" \
  "v_fmac_f32 v176, v160, v48\n" \
  "v_fmac_f32 v176, v161, v49\n" \
  "v_mad_u32_u16 v185, v181, 16, v188\n" \
  "v_mfma_f32_16x16x32_bf16 v[156:159], v[108:111], v[140:143], v[156:159]\n" \
  "v_fmac_f32 v176, v162, v50\n" \
  "v_fmac_f32 v176, v163, v51\n" \
  "v_mad_u32_u16 v186, v182, 16, v188 op_sel:[1,0,0,0]\n" \
  "v_smfmac_f32_16x16x64_bf16 v[168:171], v[144:147], v[112:119], v193\n" \
  "ds_read_b128 v[112:115], v185 offset:0\n" \
  "v_fmac_f32 v176, v152, v52\n" \
  "v_fmac_f32 v176, v153, v53\n" \
  "v_smfmac_f32_16x16x64_bf16 v[168:171], v[148:151], v[120:127], v193\n" \
  "ds_read_b128 v[116:119], v185 offset:64\n" \
  "v_fmac_f32 v176, v154, v54\n" \
  "v_fmac_f32 v176, v155, v55\n" \
  "v_mfma_f32_16x16x32_bf16 v[160:163], v[96:99], v[136:139], 0\n" \
  "v_fmac_f32 v176, v156, v56\n" \
  "v_fmac_f32 v176, v157, v57\n" \
  "v_mfma_f32_16x16x32_bf16 v[160:163], v[100:103], v[140:143], v[160:163]\n" \
  "s_waitcnt lgkmcnt(5)\n" \
  "v_fmac_f32 v176, v158, v58\n" \
  "v_fmac_f32 v176, v159, v59\n" \
  "v_smfmac_f32_16x16x64_bf16 v[164:167], v[148:151], v[104:111], v193\n" \
  "ds_read_b128 v[136:139], v186 offset:0\n" \
  "v_fmac_f32 v176, v168, v60\n" \
  "v_fmac_f32 v176, v169, v61\n" \
  "v_smfmac_f32_16x16x64_bf16 v[164:167], v[144:147], v[96:103], v193\n" \
  "ds_read_b128 v[140:143], v186 offset:64\n" \
  "v_fmac_f32 v176, v170, v62\n" \
  "v_fmac_f32 v176, v171, v63\n" \
  "v_mfma_f32_16x16x32_bf16 v[152:155], v[120:123], v[120:123], 0\n" \
  "v_fmac_f32 v176, v160, v64\n" \
  "v_fmac_f32 v176, v161, v65\n" \
  "v_mfma_f32_16x16x32_bf16 v[152:155], v[124:127], v[124:127], v[152:155]\n" \
  "ds_read_b128 v[168:171], v191\n" \
  "v_fmac_f32 v176, v162, v66\n" \
  "v_fmac_f32 v176, v163, v67\n" \
  "v_mad_u32_u16 v187, v181, 16, v188 op_sel:[1,0,0,0]\n" \
  "v_mfma_f32_16x16x32_bf16 v[156:159], v[104:107], v[120:123], 0\n" \
  "v_fmac_f32 v176, v164, v68\n" \
  "v_fmac_f32 v176, v165, v69\n" \
  "v_mad_u32_u16 v184, v180, 16, v188 op_sel:[1,0,0,0]\n" \
  "v_mfma_f32_16x16x32_bf16 v[156:159], v[108:111], v[124:127], v[156:159]\n" \
  "v_fmac_f32 v176, v166, v70\n" \
  "v_fmac_f32 v176, v167, v71\n" \
  "v_mfma_f32_16x16x32_bf16 v[160:163], v[96:99], v[120:123], 0\n" \
  "ds_read_b128 v[120:123], v187 offset:0\n" \
  "v_fmac_f32 v176, v152, v72\n" \
  "v_fmac_f32 v176, v153, v73\n" \
  "v_mfma_f32_16x16x32_bf16 v[160:163], v[100:103], v[124:127], v[160:163]\n" \
  "v_fmac_f32 v176, v154, v74\n" \
  "v_fmac_f32 v176, v155, v75\n" \
  "v_mad_u32_u16 v185, v180, 16, v188\n" \
  "v_mfma_f32_16x16x32_bf16 v[152:155], v[96:99], v[104:107], 0\n" \
  "ds_read_b128 v[124:127], v187 offset:64\n" \
  "v_fmac_f32 v176, v156, v76\n" \
  "v_fmac_f32 v176, v157, v77\n" \
  "v_mfma_f32_16x16x32_bf16 v[152:155], v[100:103], v[108:111], v[152:155]\n" \
  "v_fmac_f32 v176, v158, v78\n" \
  "v_fmac_f32 v176, v159, v79\n" \
  "v_mfma_f32_16x16x32_bf16 v[156:159], v[104:107], v[104:107], 0\n" \
  "ds_read_b128 v[104:107], v184 offset:0\n" \
  "v_fmac_f32 v176, v160, v80\n" \
  "v_fmac_f32 v176, v161, v81\n" \
  "v_mfma_f32_16x16x32_bf16 v[156:159], v[108:111], v[108:111], v[156:159]\n" \
  "s_sub_u32 s51, s51, 1\n" \
  "v_fmac_f32 v176, v162, v82\n" \
  "v_fmac_f32 v176, v163, v83\n" \
  "v_mad_u32_u16 v186, v183, 16, v189\n" \
  "v_mfma_f32_16x16x32_bf16 v[160:163], v[148:151], v[144:147], 0\n" \
  "s_cmp_lg_u32 s51, 0\n" \
  "ds_read_b128 v[108:111], v184 offset:64\n" \
  "v_fmac_f32 v176, v152, v84\n" \
  "v_fmac_f32 v176, v153, v85\n" \
  "v_mfma_f32_16x16x32_bf16 v[160:163], v[96:99], v[96:99], v[160:163]\n" \
  "s_waitcnt lgkmcnt(8)\n" \
  "ds_read_b128 v[96:99], v185 offset:0\n" \
  "v_fmac_f32 v176, v154, v86\n" \
  "v_fmac_f32 v176, v155, v87\n" \
  "v_mfma_f32_16x16x32_bf16 v[160:163], v[100:103], v[100:103], v[160:163]\n" \
  "s_cbranch_scc1 LA_loop\n" \
  "s_cmp_ge_u32 s50, 6\n" \
  "s_cbranch_scc1 LA_ret\n" \
  "s_set_gpr_idx_on s50, gpr_idx(DST)\n" \
  "v_mov_b32 v196, v178\n" \
  "s_lshl_b32 s35, s50, 3\n" \
  "s_waitcnt vmcnt(0)\n" \
  "s_set_gpr_idx_on s35, gpr_idx(SRC0,SRC1)\n" \
  "v_cvt_pk_u16_u32 v210, v208, v209\n" \
  "v_cvt_pk_u16_u32 v211, v210, v211\n" \
  "v_cvt_pk_u16_u32 v212, v212, v213\n" \
  "v_cvt_pk_u16_u32 v213, v214, v215\n" \
  "s_set_gpr_idx_off\n" \
  "v_pk_lshrrev_b16 v214, 3, v210 op_sel_hi:[0,1]\n" \
  "v_pk_mad_u16 v210, v210, 8, v214 op_sel_hi:[1,0,1]\n" \
  "v_pk_lshrrev_b16 v215, 3, v211 op_sel_hi:[0,1]\n" \
  "v_pk_mad_u16 v211, v211, 8, v215 op_sel_hi:[1,0,1]\n" \
  "v_pk_lshrrev_b16 v214, 3, v212 op_sel_hi:[0,1]\n" \
  "v_pk_mad_u16 v212, v212, 8, v214 op_sel_hi:[1,0,1]\n" \
  "v_pk_lshrrev_b16 v215, 3, v213 op_sel_hi:[0,1]\n" \
  "v_pk_mad_u16 v213, v213, 8, v215 op_sel_hi:[1,0,1]\n" \
  "s_lshr_b32 s35, s38, 1\n" \
  "v_add_u32 v214, s35, v195\n" \
  "v_alignbit_b32 v212, v212, v212, v192\n" \
  "v_alignbit_b32 v213, v213, v213, v192\n" \
  "ds_write2_b32 v214, v210, v211 offset0:0 offset1:4\n" \
  "ds_write2_b32 v214, v212, v213 offset0:2 offset1:6\n" \
  "ds_read_b32 v209, v208 offset:4\n" \
  "v_subrev_u32 v194, s38, v194\n" \
  "s_xor_b32 s38, s38, 0x800\n" \
  "s_add_u32 s50, s50, 1\n" \
  "s_mov_b32 s51, 4\n" \
  "s_branch LA_loop\n" \
  "LA_ret:\n" \
  "s_setpc_b64 s[52:53]\n" \
  "LB_start:\n" \
  "s_setprio 2\n" \
  "s_lshl_b32 s35, s31, 7\n" \
  "s_waitcnt lgkmcnt(0)\n" \
  "s_add_u32 s40, s10, s35\n" \
  "s_addc_u32 s41, s11, 0\n" \
  "s_add_u32 s37, s36, 32\n" \
  "L3_:\n" \
  "s_sub_u32 s35, s36, 125\n" \
  "s_min_u32 s35, s35, s36\n" \
  "s_mul_i32 m0, s35, 1040\n" \
  "s_lshl_b32 s35, s35, 11\n" \
  "s_add_u32 s42, s40, s35\n" \
  "s_addc_u32 s43, s41, 0\n" \
  "global_load_lds_dwordx4 v254, s[42:43]\n" \
  "s_add_u32 s36, s36, 4\n" \
  "s_sub_u32 s35, s36, 125\n" \
  "s_min_u32 s35, s35, s36\n" \
  "s_mul_i32 m0, s35, 1040\n" \
  "s_lshl_b32 s35, s35, 11\n" \
  "s_add_u32 s42, s40, s35\n" \
  "s_addc_u32 s43, s41, 0\n" \
  "global_load_lds_dwordx4 v254, s[42:43]\n" \
  "s_add_u32 s36, s36, 4\n" \
  "s_cmp_lg_u32 s37, s36\n" \
  "s_cbranch_scc1 L3_\n" \
  "v_mov_b32 v190, 163328\n" \
  "v_mov_b64 v[2:3], 0\n" \
  "ds_write_b64 v190, v[2:3]\n" \
  "ds_write_b64 v190, v[2:3] offset:8\n" \
  "ds_write_b64 v190, v[2:3] offset:16\n" \
  "s_waitcnt lgkmcnt(0)\n" \
  "s_barrier\n" \
  "v_lshlrev_b32 v247, 7, v255\n" \
  "s_add_u32 s37, s36, 96\n" \
  "L4_:\n" \
  "s_sub_u32 s35, s36, 125\n" \
  "s_min_u32 s35, s35, s36\n" \
  "s_mul_i32 m0, s35, 1040\n" \
  "s_lshl_b32 s35, s35, 11\n" \
  "s_add_u32 s42, s40, s35\n" \
  "s_addc_u32 s43, s41, 0\n" \
  "global_load_lds_dwordx4 v254, s[42:43]\n" \
  "s_add_u32 s36, s36, 4\n" \
  "s_sub_u32 s35, s36, 125\n" \
  "s_min_u32 s35, s35, s36\n" \
  "s_mul_i32 m0, s35, 1040\n" \
  "s_lshl_b32 s35, s35, 11\n" \
  "s_add_u32 s42, s40, s35\n" \
  "s_addc_u32 s43, s41, 0\n" \
  "global_load_lds_dwordx4 v254, s[42:43]\n" \
  "s_add_u32 s36, s36, 4\n" \
  "s_cmp_lg_u32 s37, s36\n" \
  "s_cbranch_scc1 L4_\n" \
  "s_add_u32 s48, s22, 110002176\n" \
  "s_addc_u32 s49, s23, 0\n" \
  "s_lshl_b32 s35, s26, 2\n" \
  "v_mov_b32 v193, s35\n" \
  "global_load_dword v193, v193, s[48:49] offset:1024 sc1\n" \
  "v_mov_b32 v191, 1\n" \
  "v_and_b32 v252, 15, v255\n" \
  "v_lshrrev_b32 v253, 4, v255\n" \
  "s_lshl2_add_u32 s35, s31, s28\n" \
  "s_lshl_b32 s35, s35, 1\n" \
  "v_bfe_u32 v1, v255, 2, 1\n" \
  "v_add_u32 v1, s35, v1\n" \
  "v_and_b32 v2, 3, v255\n" \
  "v_lshlrev_b32 v3, 5, v1\n" \
  "v_lshl_add_u32 v3, v2, 3, v3\n" \
  "v_lshlrev_b32 v4, 2, v2\n" \
  "v_add_u32 v4, 380, v4\n" \
  "v_cmp_gt_u32 vcc, 12, v1\n" \
  "v_lshrrev_b32 v5, 3, v255\n" \
  "v_mul_u32_u24 v5, 6400, v5\n" \
  "v_cndmask_b32 v3, v4, v3, vcc\n" \
  "v_add_u32 v5, 3200, v5\n" \
  "v_add_u32 v227, v5, v3\n" \
  "v_lshl_add_u32 v3, v1, 2, v2\n" \
  "v_add_u32 v4, 48, v3\n" \
  "v_mul_u32_u24 v5, 205, v3\n" \
  "v_lshrrev_b32 v5, 11, v5\n" \
  "v_mul_u32_u24 v5, 10, v5\n" \
  "v_sub_u32 v5, v3, v5\n" \
  "v_lshlrev_b32 v236, 26, v5\n" \
  "v_mul_u32_u24 v5, 205, v4\n" \
  "v_lshrrev_b32 v5, 11, v5\n" \
  "v_mul_u32_u24 v5, 10, v5\n" \
  "v_sub_u32 v5, v4, v5\n" \
  "v_lshlrev_b32 v237, 26, v5\n" \
  "s_lshl2_add_u32 s4, s31, s28\n" \
  "s_lshl_b32 s4, s4, 7\n" \
  "s_add_u32 s36, s4, 96\n" \
  "s_min_u32 s36, s36, 984\n" \
  "s_add_u32 s37, s4, 112\n" \
  "s_min_u32 s37, s37, 984\n" \
  "v_add_u32 v186, s4, v252\n" \
  "v_add_u32 v187, s36, v252\n" \
  "v_add_u32 v188, s37, v252\n" \
  "s_mul_i32 s38, s7, 51200\n" \
  "s_add_u32 s42, s8, s38\n" \
  "s_addc_u32 s43, s9, 0\n" \
  "s_xor_b32 s38, s31, 1\n" \
  "s_lshl_b32 s38, s38, 7\n" \
  "s_add_u32 s94, s10, s38\n" \
  "s_addc_u32 s95, s11, 0\n" \
  "s_mul_i32 s5, s28, 4224\n" \
  "s_add_u32 s5, s5, 146432\n" \
  "v_lshrrev_b32 v2, 3, v186\n" \
  "v_lshl_add_u32 v2, v186, 3, v2\n" \
  "v_add_lshl_u32 v249, v2, v253, 4\n" \
  "v_lshrrev_b32 v2, 3, v187\n" \
  "v_lshl_add_u32 v2, v187, 3, v2\n" \
  "v_add_lshl_u32 v250, v2, v253, 4\n" \
  "v_lshrrev_b32 v2, 3, v188\n" \
  "v_lshl_add_u32 v2, v188, 3, v2\n" \
  "v_add_lshl_u32 v251, v2, v253, 4\n" \
  "v_mul_u32_u24 v1, 1600000, v253\n" \
  "v_lshl_add_u32 v186, v186, 4, v1\n" \
  "v_lshl_add_u32 v187, v187, 4, v1\n" \
  "v_lshl_add_u32 v188, v188, 4, v1\n" \
  "v_lshlrev_b32 v1, 4, v253\n" \
  "v_xor_b32 v244, v1, v254\n" \
  "v_lshl_add_u32 v245, v255, 4, s5\n" \
  "v_lshrrev_b32 v1, 1, v252\n" \
  "s_waitcnt vmcnt(1)\n" \
  "s_mov_b64 exec, 1\n" \
  "ds_add_u32 v190, v191 offset:12\n" \
  "s_mov_b64 exec, -1\n" \
  "s_memtime s[88:89]\n" \
  "v_xor_b32 v1, v1, v253\n" \
  "v_lshlrev_b32 v1, 4, v1\n" \
  "v_lshl_add_u32 v1, v252, 7, v1\n" \
  "v_add_u32 v246, s5, v1\n" \
  "v_xor_b32 v1, 64, v1\n" \
  "v_add_u32 v242, s5, v1\n" \
  "v_lshlrev_b32 v1, 4, v255\n" \
  "v_xor_b32 v1, 64, v1\n" \
  "v_add_u32 v243, s5, v1\n" \
  "v_lshrrev_b32 v1, 2, v255\n" \
  "s_lshl_b32 s35, s28, 5\n" \
  "s_lshl_b32 s36, s31, 6\n" \
  "s_add_u32 s35, s35, s36\n" \
  "s_and_b32 s35, s35, 127\n" \
  "v_add_u32 v1, s35, v1\n" \
  "v_lshrrev_b32 v2, 4, v255\n" \
  "v_and_b32 v2, 2, v2\n" \
  "v_xor_b32 v2, v2, v255\n" \
  "v_and_b32 v2, 3, v2\n" \
  "v_lshlrev_b32 v2, 4, v2\n" \
  "v_lshl_add_u32 v189, v1, 10, v2\n" \
  "v_add_u32 v194, 15360, v189\n" \
  "v_lshrrev_b32 v1, 2, v252\n" \
  "v_lshlrev_b32 v1, 6, v1\n" \
  "v_lshl_add_u32 v1, v253, 9, v1\n" \
  "v_and_b32 v2, 3, v252\n" \
  "v_lshl_add_u32 v1, v2, 3, v1\n" \
  "v_and_b32 v2, 1, v253\n" \
  "v_lshl_add_u32 v1, v2, 5, v1\n" \
  "v_add_u32 v184, 138240, v1\n" \
  "v_xor_b32 v1, 32, v1\n" \
  "v_add_u32 v185, 138240, v1\n" \
  "s_bfe_u32 s35, s30, 0x10001\n" \
  "s_lshl1_add_u32 s35, s29, s35\n" \
  "s_lshr_b32 s6, s30, 2\n" \
  "s_lshl_b32 s6, s6, 18\n" \
  "s_lshl_b32 s38, s35, 6\n" \
  "s_add_u32 s92, s14, s38\n" \
  "s_addc_u32 s93, s15, 0\n" \
  "s_lshr_b32 s37, s30, 3\n" \
  "s_add_u32 s37, s37, 96\n" \
  "s_lshl_b32 s34, s37, 17\n" \
  "s_mul_i32 s35, s35, 400\n" \
  "s_add_u32 s37, s37, s35\n" \
  "s_mul_i32 s37, s37, 16000\n" \
  "s_add_u32 s46, s22, s37\n" \
  "s_addc_u32 s47, s23, 0\n" \
  "s_lshr_b32 s36, s30, 2\n" \
  "s_add_u32 s36, s36, s35\n" \
  "s_mul_i32 s36, s36, 16000\n" \
  "s_add_u32 s44, s22, s36\n" \
  "s_addc_u32 s45, s23, 0\n" \
  "s_lshl_b32 s96, s28, 11\n" \
  "s_add_u32 s96, s96, 138240\n" \
  "v_lshlrev_b32 v238, 4, v255\n" \
  "v_mov_b32 v234, 0\n" \
  "s_lshl2_add_u32 s33, s26, s28\n" \
  "s_mov_b32 s52, 0x10002\n" \
  "s_mov_b32 s53, 0xc0c0400\n" \
  "s_mov_b32 s54, 0x4000c0c\n" \
  "s_mov_b32 s55, 0x3030303\n" \
  "s_mov_b32 s56, 0x55555555\n" \
  "s_mov_b32 s38, 0x0\n" \
  "s_mov_b32 s98, 0x0\n" \
  "s_mov_b32 s99, 0x8\n" \
  "s_mov_b32 s97, 0x2000\n" \
  "v_xor_b32 v245, 64, v244\n" \
  "v_add_u32 v245, 0x400, v245\n" \
  "s_setprio 0\n" \
  "L5_:\n" \
  "ds_read_b32 v192, v190 offset:16\n" \
  "s_waitcnt lgkmcnt(0)\n" \
  "v_readfirstlane_b32 s35, v192\n" \
  "s_cmp_lt_u32 s35, 4\n" \
  "s_cbranch_scc1 L5_\n" \
  "s_add_u32 s35, s4, 0\n" \
  "s_min_u32 s35, s35, 984\n" \
  "s_lshl_b32 s35, s35, 8\n" \
  "s_add_u32 m0, s5, 0\n" \
  "s_add_u32 s58, s94, s35\n" \
  "s_addc_u32 s59, s95, 0\n" \
  "global_load_lds_dwordx4 v244, s[58:59]\n" \
  "global_load_lds_dwordx4 v245, s[58:59] offset:1024\n" \
  "s_add_u32 s35, s4, 16\n" \
  "s_min_u32 s35, s35, 984\n" \
  "s_lshl_b32 s35, s35, 8\n" \
  "s_add_u32 m0, s5, 2048\n" \
  "s_add_u32 s58, s94, s35\n" \
  "s_addc_u32 s59, s95, 0\n" \
  "global_load_lds_dwordx4 v244, s[58:59]\n" \
  "global_load_lds_dwordx4 v245, s[58:59] offset:1024\n" \
  "L6_:\n" \
  "ds_read_b32 v192, v190 offset:12\n" \
  "s_waitcnt lgkmcnt(0)\n" \
  "v_readfirstlane_b32 s35, v192\n" \
  "s_cmp_lt_u32 s35, 4\n" \
  "s_cbranch_scc1 L6_\n" \
  "s_waitcnt vmcnt(0)\n" \
  "ds_read_b128 v[8:11], v246 offset:0\n" \
  "ds_read_b128 v[12:15], v242 offset:0\n" \
  "ds_read_b128 v[24:27], v246 offset:2048\n" \
  "ds_read_b128 v[28:31], v242 offset:2048\n" \
  "ds_read_b128 v[0:3], v249 offset:0\n" \
  "ds_read_b128 v[4:7], v249 offset:64\n" \
  "ds_read_b128 v[16:19], v249 offset:2080\n" \
  "ds_read_b128 v[20:23], v249 offset:2144\n" \
  "s_waitcnt lgkmcnt(4)\n" \
  "s_lshl2_add_u32 s35, s26, s28\n" \
  "s_mul_i32 s35, s35, 3200\n" \
  "s_add_u32 s35, s35, 0\n" \
  "s_add_u32 s58, s14, s35\n" \
  "s_addc_u32 s59, s15, 0\n" \
  "s_mov_b32 exec_lo, 0x1ffffff\n" \
  "s_mov_b32 exec_hi, 0x0\n" \
  "global_load_dword v248, v247, s[58:59]\n" \
  "s_mov_b64 exec, -1\n" \
  "s_add_u32 s35, s4, 32\n" \
  "s_min_u32 s35, s35, 984\n" \
  "s_lshl_b32 s35, s35, 8\n" \
  "s_add_u32 m0, s5, 0\n" \
  "s_add_u32 s58, s94, s35\n" \
  "s_addc_u32 s59, s95, 0\n" \
  "global_load_lds_dwordx4 v244, s[58:59]\n" \
  "global_load_lds_dwordx4 v245, s[58:59] offset:1024\n" \
  "s_add_u32 s35, s4, 48\n" \
  "s_min_u32 s35, s35, 984\n" \
  "s_lshl_b32 s35, s35, 8\n" \
  "s_add_u32 m0, s5, 2048\n" \
  "s_add_u32 s58, s94, s35\n" \
  "s_addc_u32 s59, s95, 0\n" \
  "global_load_lds_dwordx4 v244, s[58:59]\n" \
  "global_load_lds_dwordx4 v245, s[58:59] offset:1024\n" \
  "s_waitcnt vmcnt(0)\n" \
  "ds_read_b128 v[40:43], v246 offset:0\n" \
  "ds_read_b128 v[44:47], v242 offset:0\n" \
  "ds_read_b128 v[56:59], v246 offset:2048\n" \
  "ds_read_b128 v[60:63], v242 offset:2048\n" \
  "ds_read_b128 v[32:35], v249 offset:4160\n" \
  "ds_read_b128 v[36:39], v249 offset:4224\n" \
  "ds_read_b128 v[48:51], v249 offset:6240\n" \
  "ds_read_b128 v[52:55], v249 offset:6304\n" \
  "s_waitcnt lgkmcnt(4)\n" \
  "s_sleep 8\n" \
  "s_lshl2_add_u32 s35, s26, s28\n" \
  "s_mul_i32 s35, s35, 3200\n" \
  "s_add_u32 s35, s35, 3276800\n" \
  "s_add_u32 s58, s14, s35\n" \
  "s_addc_u32 s59, s15, 0\n" \
  "s_mov_b32 exec_lo, 0x1ffffff\n" \
  "s_mov_b32 exec_hi, 0x0\n" \
  "global_load_dword v248, v247, s[58:59]\n" \
  "s_mov_b64 exec, -1\n" \
  "s_add_u32 s35, s4, 64\n" \
  "s_min_u32 s35, s35, 984\n" \
  "s_lshl_b32 s35, s35, 8\n" \
  "s_add_u32 m0, s5, 0\n" \
  "s_add_u32 s58, s94, s35\n" \
  "s_addc_u32 s59, s95, 0\n" \
  "global_load_lds_dwordx4 v244, s[58:59]\n" \
  "global_load_lds_dwordx4 v245, s[58:59] offset:1024\n" \
  "s_add_u32 s35, s4, 80\n" \
  "s_min_u32 s35, s35, 984\n" \
  "s_lshl_b32 s35, s35, 8\n" \
  "s_add_u32 m0, s5, 2048\n" \
  "s_add_u32 s58, s94, s35\n" \
  "s_addc_u32 s59, s95, 0\n" \
  "global_load_lds_dwordx4 v244, s[58:59]\n" \
  "global_load_lds_dwordx4 v245, s[58:59] offset:1024\n" \
  "s_waitcnt vmcnt(0)\n" \
  "ds_read_b128 v[72:75], v246 offset:0\n" \
  "ds_read_b128 v[76:79], v242 offset:0\n" \
  "ds_read_b128 v[88:91], v246 offset:2048\n" \
  "ds_read_b128 v[92:95], v242 offset:2048\n" \
  "ds_read_b128 v[64:67], v249 offset:8320\n" \
  "ds_read_b128 v[68:71], v249 offset:8384\n" \
  "ds_read_b128 v[80:83], v249 offset:10400\n" \
  "ds_read_b128 v[84:87], v249 offset:10464\n" \
  "s_waitcnt lgkmcnt(4)\n" \
  "s_sleep 8\n" \
  "s_lshl2_add_u32 s35, s26, s28\n" \
  "s_mul_i32 s35, s35, 3200\n" \
  "s_add_u32 s35, s35, 6553600\n" \
  "s_add_u32 s58, s14, s35\n" \
  "s_addc_u32 s59, s15, 0\n" \
  "s_mov_b32 exec_lo, 0x1ffffff\n" \
  "s_mov_b32 exec_hi, 0x0\n" \
  "global_load_dword v248, v247, s[58:59]\n" \
  "s_mov_b64 exec, -1\n" \
  "s_add_u32 s35, s4, 96\n" \
  "s_min_u32 s35, s35, 984\n" \
  "s_lshl_b32 s35, s35, 8\n" \
  "s_add_u32 m0, s5, 0\n" \
  "s_add_u32 s58, s94, s35\n" \
  "s_addc_u32 s59, s95, 0\n" \
  "global_load_lds_dwordx4 v244, s[58:59]\n" \
  "global_load_lds_dwordx4 v245, s[58:59] offset:1024\n" \
  "s_add_u32 s35, s4, 112\n" \
  "s_min_u32 s35, s35, 984\n" \
  "s_lshl_b32 s35, s35, 8\n" \
  "s_add_u32 m0, s5, 2048\n" \
  "s_add_u32 s58, s94, s35\n" \
  "s_addc_u32 s59, s95, 0\n" \
  "global_load_lds_dwordx4 v244, s[58:59]\n" \
  "global_load_lds_dwordx4 v245, s[58:59] offset:1024\n" \
  "s_waitcnt vmcnt(0)\n" \
  "ds_read_b128 v[104:107], v246 offset:0\n" \
  "ds_read_b128 v[108:111], v242 offset:0\n" \
  "ds_read_b128 v[120:123], v246 offset:2048\n" \
  "ds_read_b128 v[124:127], v242 offset:2048\n" \
  "ds_read_b128 v[96:99], v250 offset:0\n" \
  "ds_read_b128 v[100:103], v250 offset:64\n" \
  "ds_read_b128 v[112:115], v251 offset:0\n" \
  "ds_read_b128 v[116:119], v251 offset:64\n" \
  "s_waitcnt lgkmcnt(4)\n" \
  "s_sleep 8\n" \
  "s_lshl2_add_u32 s35, s26, s28\n" \
  "s_mul_i32 s35, s35, 3200\n" \
  "s_add_u32 s35, s35, 9830400\n" \
  "s_add_u32 s58, s14, s35\n" \
  "s_addc_u32 s59, s15, 0\n" \
  "s_mov_b32 exec_lo, 0x1ffffff\n" \
  "s_mov_b32 exec_hi, 0x0\n" \
  "global_load_dword v248, v247, s[58:59]\n" \
  "s_mov_b64 exec, -1\n" \
  "s_add_u32 s40, s92, s6\n" \
  "s_addc_u32 s41, s93, 0\n" \
  "s_add_u32 s6, s6, 2097152\n" \
  "s_mov_b32 m0, s96\n" \
  "s_nop 0\n" \
  "global_load_lds_dwordx4 v189, s[40:41]\n" \
  "global_load_lds_dwordx4 v194, s[40:41] offset:1024\n" \
  "s_getpc_b64 s[58:59]\n" \
  "LB_pc:\n" \
  "s_sub_u32 s58, s58, LB_pc-LZ_first\n" \
  "s_subb_u32 s59, s59, 0\n" \
  "s_mul_i32 s35, s28, 7168\n" \
  "v_lshl_add_u32 v195, v255, 7, s35\n" \
  "v_min_u32 v195, LZ_end-LZ_first, v195\n" \
  "s_mov_b32 exec_hi, 0xffffff\n" \
  "global_load_dword v248, v195, s[58:59]\n" \
  "s_mov_b64 exec, -1\n" \
  "global_load_dwordx2 v[196:197], v227, s[42:43] offset:-3200\n" \
  "global_load_dwordx2 v[198:199], v227, s[42:43] offset:-2800\n" \
  "global_load_dwordx2 v[200:201], v227, s[42:43] offset:-2400\n" \
  "global_load_dwordx2 v[202:203], v227, s[42:43] offset:-1600\n" \
  "global_load_dwordx2 v[204:205], v227, s[42:43] offset:-1200\n" \
  "global_load_dwordx2 v[206:207], v227, s[42:43] offset:-800\n" \
  "global_load_dwordx2 v[208:209], v227, s[42:43] offset:-400\n" \
  "global_load_dwordx2 v[210:211], v227, s[42:43] offset:0\n" \
  "global_load_dwordx2 v[212:213], v227, s[42:43] offset:400\n" \
  "global_load_dwordx2 v[214:215], v227, s[42:43] offset:800\n" \
  "global_load_dwordx2 v[216:217], v227, s[42:43] offset:1200\n" \
  "global_load_dwordx2 v[218:219], v227, s[42:43] offset:-2000\n" \
  "global_load_dwordx2 v[220:221], v227, s[42:43] offset:1600\n" \
  "global_load_dwordx2 v[222:223], v227, s[42:43] offset:2000\n" \
  "global_load_dwordx2 v[224:225], v227, s[42:43] offset:2400\n" \
  "global_load_dwordx2 v[226:227], v227, s[42:43] offset:2800\n" \
  "s_setprio 2\n" \
  "s_mul_i32 s36, s28, 2176\n" \
  "s_add_u32 s36, s36, 154624\n" \
  "v_bfe_u32 v228, v255, 3, 1\n" \
  "v_and_b32 v229, 1, v255\n" \
  "v_lshl_add_u32 v230, v228, 1, v229\n" \
  "v_lshl_add_u32 v231, v229, 1, v228\n" \
  "v_xor_b32 v229, 1, v230\n" \
  "s_mov_b32 vcc_lo, 0xcccccccc\n" \
  "s_mov_b32 vcc_hi, vcc_lo\n" \
  "v_cndmask_b32 v228, v230, v231, vcc\n" \
  "v_cndmask_b32 v229, v229, v231, vcc\n" \
  "v_and_b32 v230, 4, v255\n" \
  "v_mul_u32_u24 v230, 272, v230\n" \
  "v_lshl_add_u32 v230, v253, 2, v230\n" \
  "v_and_or_b32 v230, v255, 2, v230\n" \
  "v_add_u32 v230, s36, v230\n" \
  "s_movk_i32 s35, 272\n" \
  "v_mad_u32_u24 v195, v228, s35, v230\n" \
  "v_mad_u32_u24 v235, v229, s35, v230\n" \
  "v_lshl_add_u32 v230, v253, 4, v238\n" \
  "v_add_u32 v254, s36, v230\n" \
  "v_readfirstlane_b32 s39, v193\n" \
  "s_lshl_b32 s35, s26, 2\n" \
  "s_add_u32 s39, s39, 1\n" \
  "v_mov_b32 v193, s35\n" \
  "v_mov_b64 v[240:241], -1\n" \
  "v_mov_b64 v[242:243], -1\n" \
  "s_waitcnt vmcnt(0)\n" \
  "s_waitcnt lgkmcnt(0)\n" \
  "s_mov_b64 exec, 1\n" \
  "ds_add_u32 v190, v191 offset:0\n" \
  "s_mov_b64 exec, -1\n" \
  "L7_:\n" \
  "s_sleep 2\n" \
  "ds_read_b32 v192, v190 offset:0\n" \
  "s_waitcnt lgkmcnt(0)\n" \
  "v_readfirstlane_b32 s35, v192\n" \
  "s_cmp_lt_u32 s35, 4\n" \
  "s_cbranch_scc1 L7_\n" \
  "s_add_u32 s96, s96, 8192\n" \
  "s_add_u32 s40, s92, s6\n" \
  "s_addc_u32 s41, s93, 0\n" \
  "s_add_u32 s6, s6, 2097152\n" \
  "s_mov_b32 m0, s96\n" \
  "s_nop 0\n" \
  "global_load_lds_dwordx4 v189, s[40:41]\n" \
  "global_load_lds_dwordx4 v194, s[40:41] offset:1024\n" \
  "s_sub_u32 s96, s96, 8192\n" \
  "ds_read_b64_tr_b16 v[128:129], v184 offset:0\n" \
  "ds_read_b64_tr_b16 v[130:131], v184 offset:256\n" \
  "ds_read_b64_tr_b16 v[132:133], v184 offset:2048\n" \
  "ds_read_b64_tr_b16 v[134:135], v184 offset:2304\n" \
  "ds_read_b64_tr_b16 v[136:137], v184 offset:4096\n" \
  "ds_read_b64_tr_b16 v[138:139], v184 offset:4352\n" \
  "ds_read_b64_tr_b16 v[140:141], v184 offset:6144\n" \
  "ds_read_b64_tr_b16 v[142:143], v184 offset:6400\n" \
  "ds_read_b64_tr_b16 v[156:157], v185 offset:6144\n" \
  "ds_read_b64_tr_b16 v[158:159], v185 offset:6400\n" \
  "ds_read_b64_tr_b16 v[152:153], v185 offset:4096\n" \
  "ds_read_b64_tr_b16 v[154:155], v185 offset:4352\n" \
  "ds_read_b64_tr_b16 v[148:149], v185 offset:2048\n" \
  "ds_read_b64_tr_b16 v[150:151], v185 offset:2304\n" \
  "ds_read_b64_tr_b16 v[144:145], v185 offset:0\n" \
  "ds_read_b64_tr_b16 v[146:147], v185 offset:256\n" \
  "v_mov_b32 v192, s39\n" \
  "s_cmp_lg_u32 s28, 0\n" \
  "s_cbranch_scc1 LB_nc\n" \
  "s_mov_b64 exec, 1\n" \
  "global_store_dword v193, v192, s[48:49] offset:0 sc1\n" \
  "s_mov_b64 exec, -1\n" \
  "LB_nc:\n" \
  "s_lshl2_add_u32 s35, s7, s28\n" \
  "s_lshl_b32 s35, s35, 7\n" \
  "s_lshl_b32 s36, s31, 16\n" \
  "s_add_u32 s35, s35, s36\n" \
  "s_add_u32 s35, s35, 109871104\n" \
  "s_add_u32 s58, s22, s35\n" \
  "s_addc_u32 s59, s23, 0\n" \
  "s_mov_b64 exec, 0xff\n" \
  "global_store_dwordx4 v238, v[240:243], s[58:59] sc1\n" \
  "s_mov_b64 exec, -1\n" \
  "s_cmp_lg_u32 s33, 1021\n" \
  "s_cbranch_scc1 LB_nm\n" \
  "v_mov_b64 v[240:241], 0\n" \
  "v_mov_b64 v[242:243], 0\n" \
  "global_store_dwordx4 v238, v[240:243], s[48:49] offset:2048 sc1\n" \
  "s_nop 1\n" \
  "v_lshlrev_b32 v242, 8, v255\n" \
  "global_store_dwordx2 v242, v[240:241], s[48:49] offset:3072 sc1\n" \
  "v_add_u32 v242, 0x4000, v242\n" \
  "global_store_dwordx2 v242, v[240:241], s[48:49] offset:3072 sc1\n" \
  "v_add_u32 v242, 0x4000, v242\n" \
  "global_store_dwordx2 v242, v[240:241], s[48:49] offset:3072 sc1\n" \
  "v_add_u32 v242, 0x4000, v242\n" \
  "global_store_dwordx2 v242, v[240:241], s[48:49] offset:3072 sc1\n" \
  "v_add_u32 v242, 0x4000, v242\n" \
  "LB_nm:\n" \
  "s_mov_b32 s86, 0x78000b0\n" \
  "s_mov_b32 s87, 0x0\n" \
  "s_or_b32 s86, s86, s87\n" \
  "s_setprio 2\n" \
  "s_memtime s[90:91]\n" \
  "s_memrealtime s[42:43]\n" \
  "LT:\n" \
  "s_cmp_eq_u32 s38, 6\n" \
  "s_cselect_b32 s78, 0x33333333, -1\n" \
  "s_cmp_eq_u32 s38, 7\n" \
  "s_cselect_b32 s78, 0x33333333, s78\n" \
  "s_cmp_eq_u32 s38, 18\n" \
  "s_cselect_b32 s80, 0x33333333, -1\n" \
  "s_cmp_eq_u32 s38, 19\n" \
  "s_cselect_b32 s80, 0x33333333, s80\n" \
  "s_mov_b32 s79, s78\n" \
  "s_mov_b32 s81, s80\n" \
  "s_mov_b32 s37, s38\n" \
  "s_add_u32 s57, s38, 2\n" \
  "s_add_u32 s3, s38, 4\n" \
  "s_add_u32 s38, s38, 6\n" \
  "s_cmp_lt_u32 s57, 32\n" \
  "s_cselect_b32 s57, s57, 37\n" \
  "s_cselect_b32 s3, s3, 37\n" \
  "s_waitcnt lgkmcnt(12)\n" \
  "v_mfma_f32_16x16x32_bf16 v[160:163], v[128:131], v[0:3], 0\n" \
  "s_mov_b64 vcc, s[78:79]\n" \
  "v_cndmask_b32_dpp v234, v234, v234, vcc row_ror:8 row_mask:0xf bank_mask:0xf\n" \
  "v_mfma_f32_16x16x32_bf16 v[160:163], v[132:135], v[4:7], v[160:163]\n" \
  "s_waitcnt lgkmcnt(8)\n" \
  "v_mfma_f32_16x16x32_bf16 v[160:163], v[136:139], v[8:11], v[160:163]\n" \
  "s_set_gpr_idx_on s37, gpr_idx(SRC0)\n" \
  "v_mad_u32_u24 v232, v196, s52, v236\n" \
  "s_set_gpr_idx_off\n" \
  "v_mfma_f32_16x16x32_bf16 v[160:163], v[140:143], v[12:15], v[160:163]\n" \
  "s_waitcnt lgkmcnt(0)\n" \
  "v_lshrrev_b32 v228, v232, v234\n" \
  "v_lshrrev_b32_dpp v229, v232, v234 quad_perm:[2,3,0,1] row_mask:0xf bank_mask:0xf\n" \
  "v_mfma_f32_16x16x32_bf16 v[164:167], v[156:159], v[12:15], 0\n" \
  "v_lshrrev_b32_dpp v230, v232, v234 quad_perm:[1,2,3,0] row_mask:0xf bank_mask:0xf\n" \
  "v_lshrrev_b32_dpp v231, v232, v234 quad_perm:[3,0,1,2] row_mask:0xf bank_mask:0xf\n" \
  "v_mfma_f32_16x16x32_bf16 v[164:167], v[152:155], v[8:11], v[164:167]\n" \
  "v_perm_b32 v228, v229, v228, s53\n" \
  "v_perm_b32 v230, v231, v230, s54\n" \
  "v_mfma_f32_16x16x32_bf16 v[164:167], v[148:151], v[4:7], v[164:167]\n" \
  "v_bitop3_b32 v228, v228, v230, s55 bitop3:0xa8\n" \
  "v_lshl_or_b32 v229, v234, 1, s56\n" \
  "v_mfma_f32_16x16x32_bf16 v[164:167], v[144:147], v[0:3], v[164:167]\n" \
  "v_add_u32_dpp v228, v228, v228 quad_perm:[2,3,0,1] row_mask:0xf bank_mask:0xf\n" \
  "v_min_u32_sdwa v230, v228, v228 dst_sel:DWORD dst_unused:UNUSED_PAD src0_sel:BYTE_0 src1_sel:BYTE_1\n" \
  "v_mfma_f32_16x16x32_bf16 v[172:175], v[144:147], v[16:19], 0\n" \
  "v_min_u32_sdwa v231, v228, v228 dst_sel:DWORD dst_unused:UNUSED_PAD src0_sel:BYTE_2 src1_sel:BYTE_3\n" \
  "v_sub_u32 v230, v230, v231\n" \
  "v_mfma_f32_16x16x32_bf16 v[172:175], v[148:151], v[20:23], v[172:175]\n" \
  "v_cvt_pk_bf16_f32 v176, v160, v161\n" \
  "v_add_u32_dpp v230, v230, v230 quad_perm:[1,0,3,2] row_mask:0xf bank_mask:0xf\n" \
  "v_mfma_f32_16x16x32_bf16 v[172:175], v[152:155], v[24:27], v[172:175]\n" \
  "v_cmp_ge_i32 vcc, 0, v230\n" \
  "v_cvt_pk_bf16_f32 v177, v162, v163\n" \
  "v_mfma_f32_16x16x32_bf16 v[172:175], v[156:159], v[28:31], v[172:175]\n" \
  "v_cndmask_b32_sdwa v228, v228, v228, vcc dst_sel:DWORD dst_unused:UNUSED_PAD src0_sel:WORD_1 src1_sel:WORD_0\n" \
  "v_cvt_pk_bf16_f32 v178, v164, v165\n" \
  "v_mfma_f32_16x16x32_bf16 v[168:171], v[140:143], v[28:31], 0\n" \
  "s_mov_b64 s[50:51], vcc\n" \
  "v_cmp_le_u32_sdwa vcc, v228, v228 src0_sel:BYTE_0 src1_sel:BYTE_1\n" \
  "v_cvt_pk_bf16_f32 v179, v166, v167\n" \
  "v_mfma_f32_16x16x32_bf16 v[168:171], v[136:139], v[24:27], v[168:171]\n" \
  "v_cndmask_b32_dpp v232, v232, v232, vcc quad_perm:[2,3,0,1] row_mask:0xf bank_mask:0xf\n" \
  "v_mfma_f32_16x16x32_bf16 v[168:171], v[132:135], v[20:23], v[168:171]\n" \
  "s_mov_b64 vcc, s[50:51]\n" \
  "v_cndmask_b32_dpp v232, v232, v232, vcc quad_perm:[3,0,1,2] row_mask:0xf bank_mask:0xf\n" \
  "v_lshlrev_b32_e64 v230, v232, 3\n" \
  "v_mfma_f32_16x16x32_bf16 v[168:171], v[128:131], v[16:19], v[168:171]\n" \
  "v_and_or_b32 v234, v229, v230, v234\n" \
  "s_cmp_lg_u32 s33, 1021\n" \
  "s_cbranch_scc0 LT_e\n" \
  "LT_r:\n" \
  "s_waitcnt vmcnt(5)\n" \
  "global_store_dwordx4 v186, v[176:179], s[44:45] offset:0 sc1\n" \
  "v_mfma_f32_16x16x32_bf16 v[160:163], v[128:131], v[32:35], 0\n" \
  "s_set_gpr_idx_on s37, gpr_idx(DST)\n" \
  "v_mov_b32 v196, v232\n" \
  "s_set_gpr_idx_off\n" \
  "v_mfma_f32_16x16x32_bf16 v[160:163], v[132:135], v[36:39], v[160:163]\n" \
  "s_set_gpr_idx_on s57, gpr_idx(SRC0)\n" \
  "v_mad_u32_u24 v232, v196, s52, v236\n" \
  "s_set_gpr_idx_off\n" \
  "v_mfma_f32_16x16x32_bf16 v[160:163], v[136:139], v[40:43], v[160:163]\n" \
  "v_lshrrev_b32 v228, v232, v234\n" \
  "v_lshrrev_b32_dpp v229, v232, v234 quad_perm:[2,3,0,1] row_mask:0xf bank_mask:0xf\n" \
  "v_mfma_f32_16x16x32_bf16 v[160:163], v[140:143], v[44:47], v[160:163]\n" \
  "v_lshrrev_b32_dpp v230, v232, v234 quad_perm:[1,2,3,0] row_mask:0xf bank_mask:0xf\n" \
  "v_lshrrev_b32_dpp v231, v232, v234 quad_perm:[3,0,1,2] row_mask:0xf bank_mask:0xf\n" \
  "v_mfma_f32_16x16x32_bf16 v[164:167], v[156:159], v[44:47], 0\n" \
  "v_perm_b32 v228, v229, v228, s53\n" \
  "v_perm_b32 v230, v231, v230, s54\n" \
  "v_mfma_f32_16x16x32_bf16 v[164:167], v[152:155], v[40:43], v[164:167]\n" \
  "v_bitop3_b32 v228, v228, v230, s55 bitop3:0xa8\n" \
  "v_lshl_or_b32 v229, v234, 1, s56\n" \
  "v_mfma_f32_16x16x32_bf16 v[164:167], v[148:151], v[36:39], v[164:167]\n" \
  "v_cvt_pk_bf16_f32 v182, v172, v173\n" \
  "v_cvt_pk_bf16_f32 v183, v174, v175\n" \
  "v_mfma_f32_16x16x32_bf16 v[164:167], v[144:147], v[32:35], v[164:167]\n" \
  "v_cvt_pk_bf16_f32 v180, v168, v169\n" \
  "v_cvt_pk_bf16_f32 v181, v170, v171\n" \
  "s_waitcnt vmcnt(5)\n" \
  "global_store_dwordx4 v186, v[180:183], s[44:45] offset:256 sc1\n" \
  "v_mfma_f32_16x16x32_bf16 v[172:175], v[144:147], v[48:51], 0\n" \
  "v_add_u32_dpp v228, v228, v228 quad_perm:[2,3,0,1] row_mask:0xf bank_mask:0xf\n" \
  "v_min_u32_sdwa v230, v228, v228 dst_sel:DWORD dst_unused:UNUSED_PAD src0_sel:BYTE_0 src1_sel:BYTE_1\n" \
  "v_mfma_f32_16x16x32_bf16 v[172:175], v[148:151], v[52:55], v[172:175]\n" \
  "v_min_u32_sdwa v231, v228, v228 dst_sel:DWORD dst_unused:UNUSED_PAD src0_sel:BYTE_2 src1_sel:BYTE_3\n" \
  "v_sub_u32 v230, v230, v231\n" \
  "v_mfma_f32_16x16x32_bf16 v[172:175], v[152:155], v[56:59], v[172:175]\n" \
  "v_cvt_pk_bf16_f32 v176, v160, v161\n" \
  "v_add_u32_dpp v230, v230, v230 quad_perm:[1,0,3,2] row_mask:0xf bank_mask:0xf\n" \
  "v_mfma_f32_16x16x32_bf16 v[172:175], v[156:159], v[60:63], v[172:175]\n" \
  "v_cmp_ge_i32 vcc, 0, v230\n" \
  "v_cvt_pk_bf16_f32 v177, v162, v163\n" \
  "v_mfma_f32_16x16x32_bf16 v[168:171], v[140:143], v[60:63], 0\n" \
  "v_cndmask_b32_sdwa v228, v228, v228, vcc dst_sel:DWORD dst_unused:UNUSED_PAD src0_sel:WORD_1 src1_sel:WORD_0\n" \
  "v_cvt_pk_bf16_f32 v178, v164, v165\n" \
  "v_mfma_f32_16x16x32_bf16 v[168:171], v[136:139], v[56:59], v[168:171]\n" \
  "s_mov_b64 s[50:51], vcc\n" \
  "v_cmp_le_u32_sdwa vcc, v228, v228 src0_sel:BYTE_0 src1_sel:BYTE_1\n" \
  "v_cvt_pk_bf16_f32 v179, v166, v167\n" \
  "v_mfma_f32_16x16x32_bf16 v[168:171], v[132:135], v[52:55], v[168:171]\n" \
  "v_cndmask_b32_dpp v232, v232, v232, vcc quad_perm:[2,3,0,1] row_mask:0xf bank_mask:0xf\n" \
  "v_mfma_f32_16x16x32_bf16 v[168:171], v[128:131], v[48:51], v[168:171]\n" \
  "s_mov_b64 vcc, s[50:51]\n" \
  "v_cndmask_b32_dpp v232, v232, v232, vcc quad_perm:[3,0,1,2] row_mask:0xf bank_mask:0xf\n" \
  "v_lshlrev_b32_e64 v230, v232, 3\n" \
  "s_waitcnt vmcnt(5)\n" \
  "global_store_dwordx4 v186, v[176:179], s[44:45] offset:512 sc1\n" \
  "v_mfma_f32_16x16x32_bf16 v[160:163], v[128:131], v[64:67], 0\n" \
  "v_and_or_b32 v234, v229, v230, v234\n" \
  "v_cvt_pk_bf16_f32 v182, v172, v173\n" \
  "v_mfma_f32_16x16x32_bf16 v[160:163], v[132:135], v[68:71], v[160:163]\n" \
  "s_set_gpr_idx_on s57, gpr_idx(DST)\n" \
  "v_mov_b32 v196, v232\n" \
  "s_set_gpr_idx_off\n" \
  "v_mfma_f32_16x16x32_bf16 v[160:163], v[136:139], v[72:75], v[160:163]\n" \
  "s_mov_b64 vcc, s[80:81]\n" \
  "v_cndmask_b32_dpp v234, v234, v234, vcc row_ror:8 row_mask:0xf bank_mask:0xf\n" \
  "v_cvt_pk_bf16_f32 v183, v174, v175\n" \
  "v_mfma_f32_16x16x32_bf16 v[160:163], v[140:143], v[76:79], v[160:163]\n" \
  "s_set_gpr_idx_on s3, gpr_idx(SRC0)\n" \
  "v_mad_u32_u24 v232, v196, s52, v236\n" \
  "s_set_gpr_idx_off\n" \
  "v_mfma_f32_16x16x32_bf16 v[164:167], v[156:159], v[76:79], 0\n" \
  "v_lshrrev_b32 v228, v232, v234\n" \
  "v_lshrrev_b32_dpp v229, v232, v234 quad_perm:[2,3,0,1] row_mask:0xf bank_mask:0xf\n" \
  "v_mfma_f32_16x16x32_bf16 v[164:167], v[152:155], v[72:75], v[164:167]\n" \
  "v_lshrrev_b32_dpp v230, v232, v234 quad_perm:[1,2,3,0] row_mask:0xf bank_mask:0xf\n" \
  "v_lshrrev_b32_dpp v231, v232, v234 quad_perm:[3,0,1,2] row_mask:0xf bank_mask:0xf\n" \
  "v_mfma_f32_16x16x32_bf16 v[164:167], v[148:151], v[68:71], v[164:167]\n" \
  "v_perm_b32 v228, v229, v228, s53\n" \
  "v_perm_b32 v230, v231, v230, s54\n" \
  "v_mfma_f32_16x16x32_bf16 v[164:167], v[144:147], v[64:67], v[164:167]\n" \
  "v_cvt_pk_bf16_f32 v180, v168, v169\n" \
  "v_cvt_pk_bf16_f32 v181, v170, v171\n" \
  "s_waitcnt vmcnt(5)\n" \
  "global_store_dwordx4 v186, v[180:183], s[44:45] offset:768 sc1\n" \
  "v_mfma_f32_16x16x32_bf16 v[172:175], v[144:147], v[80:83], 0\n" \
  "s_waitcnt vmcnt(5)\n" \
  "s_mov_b64 exec, 1\n" \
  "ds_add_u32 v190, v191 offset:0\n" \
  "s_mov_b64 exec, -1\n" \
  "v_mfma_f32_16x16x32_bf16 v[172:175], v[148:151], v[84:87], v[172:175]\n" \
  "v_bitop3_b32 v228, v228, v230, s55 bitop3:0xa8\n" \
  "v_lshl_or_b32 v229, v234, 1, s56\n" \
  "v_mfma_f32_16x16x32_bf16 v[172:175], v[152:155], v[88:91], v[172:175]\n" \
  "v_add_u32_dpp v228, v228, v228 quad_perm:[2,3,0,1] row_mask:0xf bank_mask:0xf\n" \
  "v_min_u32_sdwa v230, v228, v228 dst_sel:DWORD dst_unused:UNUSED_PAD src0_sel:BYTE_0 src1_sel:BYTE_1\n" \
  "v_mfma_f32_16x16x32_bf16 v[172:175], v[156:159], v[92:95], v[172:175]\n" \
  "v_min_u32_sdwa v231, v228, v228 dst_sel:DWORD dst_unused:UNUSED_PAD src0_sel:BYTE_2 src1_sel:BYTE_3\n" \
  "v_sub_u32 v230, v230, v231\n" \
  "v_mfma_f32_16x16x32_bf16 v[168:171], v[140:143], v[92:95], 0\n" \
  "v_cvt_pk_bf16_f32 v176, v160, v161\n" \
  "v_add_u32_dpp v230, v230, v230 quad_perm:[1,0,3,2] row_mask:0xf bank_mask:0xf\n" \
  "v_mfma_f32_16x16x32_bf16 v[168:171], v[136:139], v[88:91], v[168:171]\n" \
  "v_cmp_ge_i32 vcc, 0, v230\n" \
  "v_cvt_pk_bf16_f32 v177, v162, v163\n" \
  "v_mfma_f32_16x16x32_bf16 v[168:171], v[132:135], v[84:87], v[168:171]\n" \
  "v_cndmask_b32_sdwa v228, v228, v228, vcc dst_sel:DWORD dst_unused:UNUSED_PAD src0_sel:WORD_1 src1_sel:WORD_0\n" \
  "v_cvt_pk_bf16_f32 v178, v164, v165\n" \
  "v_mfma_f32_16x16x32_bf16 v[168:171], v[128:131], v[80:83], v[168:171]\n" \
  "s_mov_b64 s[50:51], vcc\n" \
  "v_cmp_le_u32_sdwa vcc, v228, v228 src0_sel:BYTE_0 src1_sel:BYTE_1\n" \
  "v_cvt_pk_bf16_f32 v179, v166, v167\n" \
  "s_cmp_eq_u32 s98, 4\n" \
  "s_cselect_b32 s36, 12451840, 0\n" \
  "s_sub_u32 s6, s6, s36\n" \
  "s_cmp_lt_u32 s98, 10\n" \
  "s_cselect_b32 s6, s6, s34\n" \
  "s_add_u32 s40, s92, s6\n" \
  "s_addc_u32 s41, s93, 0\n" \
  "s_add_u32 s6, s6, 2097152\n" \
  "s_waitcnt vmcnt(5)\n" \
  "global_store_dwordx4 v186, v[176:179], s[44:45] offset:1024 sc1\n" \
  "v_mfma_f32_16x16x32_bf16 v[160:163], v[128:131], v[96:99], 0\n" \
  "ds_read_b32 v192, v190\n" \
  "v_add_u32 v184, s97, v184\n" \
  "v_mfma_f32_16x16x32_bf16 v[160:163], v[132:135], v[100:103], v[160:163]\n" \
  "v_add_u32 v185, s97, v185\n" \
  "v_cndmask_b32_dpp v232, v232, v232, vcc quad_perm:[2,3,0,1] row_mask:0xf bank_mask:0xf\n" \
  "v_mfma_f32_16x16x32_bf16 v[160:163], v[136:139], v[104:107], v[160:163]\n" \
  "s_mov_b64 vcc, s[50:51]\n" \
  "v_cndmask_b32_dpp v232, v232, v232, vcc quad_perm:[3,0,1,2] row_mask:0xf bank_mask:0xf\n" \
  "v_lshlrev_b32_e64 v230, v232, 3\n" \
  "v_mfma_f32_16x16x32_bf16 v[160:163], v[140:143], v[108:111], v[160:163]\n" \
  "v_and_or_b32 v234, v229, v230, v234\n" \
  "v_cvt_pk_bf16_f32 v182, v172, v173\n" \
  "v_mfma_f32_16x16x32_bf16 v[164:167], v[156:159], v[108:111], 0\n" \
  "s_set_gpr_idx_on s3, gpr_idx(DST)\n" \
  "v_mov_b32 v196, v232\n" \
  "s_set_gpr_idx_off\n" \
  "v_mfma_f32_16x16x32_bf16 v[164:167], v[152:155], v[104:107], v[164:167]\n" \
  "s_waitcnt lgkmcnt(0)\n" \
  "v_readfirstlane_b32 s35, v192\n" \
  "v_cvt_pk_bf16_f32 v183, v174, v175\n" \
  "v_mfma_f32_16x16x32_bf16 v[164:167], v[148:151], v[100:103], v[164:167]\n" \
  "v_cvt_pk_bf16_f32 v180, v168, v169\n" \
  "v_cvt_pk_bf16_f32 v181, v170, v171\n" \
  "v_mfma_f32_16x16x32_bf16 v[164:167], v[144:147], v[96:99], v[164:167]\n" \
  "s_cmp_lt_u32 s35, s99\n" \
  "s_cbranch_scc1 LT_w\n" \
  "LT_c:\n" \
  "s_mov_b32 m0, s96\n" \
  "s_nop 0\n" \
  "global_load_lds_dwordx4 v189, s[40:41]\n" \
  "global_load_lds_dwordx4 v194, s[40:41] offset:1024\n" \
  "s_add_u32 s96, s96, s97\n" \
  "s_sub_u32 s97, 0, s97\n" \
  "s_add_u32 s99, s99, 4\n" \
  "s_lshl4_add_u32 s35, s28, s98\n" \
  "s_bitcmp1_b64 s[86:87], s35\n" \
  "s_cbranch_scc1 LT_a\n" \
  "LT_s:\n" \
  "s_waitcnt vmcnt(5)\n" \
  "global_store_dwordx4 v186, v[180:183], s[44:45] offset:1280 sc1\n" \
  "v_mfma_f32_16x16x32_bf16 v[168:171], v[128:131], v[112:115], 0\n" \
  "ds_read_b64_tr_b16 v[128:129], v184 offset:0\n" \
  "ds_read_b64_tr_b16 v[130:131], v184 offset:256\n" \
  "v_mfma_f32_16x16x32_bf16 v[168:171], v[132:135], v[116:119], v[168:171]\n" \
  "ds_read_b64_tr_b16 v[132:133], v184 offset:2048\n" \
  "ds_read_b64_tr_b16 v[134:135], v184 offset:2304\n" \
  "v_mfma_f32_16x16x32_bf16 v[168:171], v[136:139], v[120:123], v[168:171]\n" \
  "ds_read_b64_tr_b16 v[136:137], v184 offset:4096\n" \
  "v_cvt_pk_bf16_f32 v176, v160, v161\n" \
  "v_mfma_f32_16x16x32_bf16 v[168:171], v[140:143], v[124:127], v[168:171]\n" \
  "ds_read_b64_tr_b16 v[138:139], v184 offset:4352\n" \
  "v_cvt_pk_bf16_f32 v177, v162, v163\n" \
  "v_mfma_f32_16x16x32_bf16 v[172:175], v[156:159], v[124:127], 0\n" \
  "ds_read_b64_tr_b16 v[140:141], v184 offset:6144\n" \
  "v_cvt_pk_bf16_f32 v178, v164, v165\n" \
  "v_mfma_f32_16x16x32_bf16 v[172:175], v[152:155], v[120:123], v[172:175]\n" \
  "ds_read_b64_tr_b16 v[142:143], v184 offset:6400\n" \
  "v_cvt_pk_bf16_f32 v179, v166, v167\n" \
  "v_mfma_f32_16x16x32_bf16 v[172:175], v[148:151], v[116:119], v[172:175]\n" \
  "ds_read_b64_tr_b16 v[156:157], v185 offset:6144\n" \
  "ds_read_b64_tr_b16 v[158:159], v185 offset:6400\n" \
  "v_mfma_f32_16x16x32_bf16 v[172:175], v[144:147], v[112:115], v[172:175]\n" \
  "ds_read_b64_tr_b16 v[152:153], v185 offset:4096\n" \
  "ds_read_b64_tr_b16 v[154:155], v185 offset:4352\n" \
  "ds_read_b64_tr_b16 v[148:149], v185 offset:2048\n" \
  "ds_read_b64_tr_b16 v[150:151], v185 offset:2304\n" \
  "ds_read_b64_tr_b16 v[144:145], v185 offset:0\n" \
  "ds_read_b64_tr_b16 v[146:147], v185 offset:256\n" \
  "s_waitcnt vmcnt(5)\n" \
  "global_store_dwordx4 v187, v[176:179], s[44:45] offset:0 sc1\n" \
  "s_add_u32 s98, s98, 1\n" \
  "v_cvt_pk_bf16_f32 v180, v168, v169\n" \
  "v_cvt_pk_bf16_f32 v181, v170, v171\n" \
  "v_cvt_pk_bf16_f32 v182, v172, v173\n" \
  "v_cvt_pk_bf16_f32 v183, v174, v175\n" \
  "s_waitcnt vmcnt(5)\n" \
  "global_store_dwordx4 v188, v[180:183], s[44:45] offset:0 sc1\n" \
  "s_add_u32 s44, s44, 128000\n" \
  "s_addc_u32 s45, s45, 0\n" \
  "s_bitcmp0_b32 4160, s98\n" \
  "s_cbranch_scc1 LT\n" \
  "s_cmp_eq_u32 s98, 6\n" \
  "s_cbranch_scc1 LT_m\n" \
  "s_lshr_b32 s35, s30, 2\n" \
  "s_lshl3_add_u32 s35, s29, s35\n" \
  "s_mul_i32 s35, s35, 1600000\n" \
  "s_lshl_b32 s36, s28, 12\n" \
  "s_add_u32 s35, s35, s36\n" \
  "s_add_u32 s40, s22, s35\n" \
  "s_addc_u32 s41, s23, 0\n" \
  "s_bitcmp1_b32 s30, 2\n" \
  "s_cbranch_scc1 LB_l_h\n" \
  "global_load_dwordx4 v[64:67], v238, s[40:41] offset:0 sc1\n" \
  "global_load_dwordx4 v[68:71], v238, s[40:41] offset:1024 sc1\n" \
  "global_load_dwordx4 v[72:75], v238, s[40:41] offset:2048 sc1\n" \
  "global_load_dwordx4 v[76:79], v238, s[40:41] offset:3072 sc1\n" \
  "s_add_u32 s40, s40, 16000\n" \
  "s_addc_u32 s41, s41, 0\n" \
  "global_load_dwordx4 v[80:83], v238, s[40:41] offset:0 sc1\n" \
  "global_load_dwordx4 v[84:87], v238, s[40:41] offset:1024 sc1\n" \
  "global_load_dwordx4 v[88:91], v238, s[40:41] offset:2048 sc1\n" \
  "global_load_dwordx4 v[92:95], v238, s[40:41] offset:3072 sc1\n" \
  "s_add_u32 s40, s40, 16000\n" \
  "s_addc_u32 s41, s41, 0\n" \
  "global_load_dwordx4 v[96:99], v238, s[40:41] offset:0 sc1\n" \
  "global_load_dwordx4 v[100:103], v238, s[40:41] offset:1024 sc1\n" \
  "global_load_dwordx4 v[104:107], v238, s[40:41] offset:2048 sc1\n" \
  "global_load_dwordx4 v[108:111], v238, s[40:41] offset:3072 sc1\n" \
  "s_add_u32 s40, s40, 16000\n" \
  "s_addc_u32 s41, s41, 0\n" \
  "global_load_dwordx4 v[112:115], v238, s[40:41] offset:0 sc1\n" \
  "global_load_dwordx4 v[116:119], v238, s[40:41] offset:1024 sc1\n" \
  "global_load_dwordx4 v[120:123], v238, s[40:41] offset:2048 sc1\n" \
  "global_load_dwordx4 v[124:127], v238, s[40:41] offset:3072 sc1\n" \
  "s_add_u32 s40, s40, 16000\n" \
  "s_addc_u32 s41, s41, 0\n" \
  "s_branch LB_l_e\n" \
  "LB_l_h:\n" \
  "global_load_dwordx4 v[0:3], v238, s[40:41] offset:0 sc1\n" \
  "global_load_dwordx4 v[4:7], v238, s[40:41] offset:1024 sc1\n" \
  "global_load_dwordx4 v[8:11], v238, s[40:41] offset:2048 sc1\n" \
  "global_load_dwordx4 v[12:15], v238, s[40:41] offset:3072 sc1\n" \
  "s_add_u32 s40, s40, 16000\n" \
  "s_addc_u32 s41, s41, 0\n" \
  "global_load_dwordx4 v[16:19], v238, s[40:41] offset:0 sc1\n" \
  "global_load_dwordx4 v[20:23], v238, s[40:41] offset:1024 sc1\n" \
  "global_load_dwordx4 v[24:27], v238, s[40:41] offset:2048 sc1\n" \
  "global_load_dwordx4 v[28:31], v238, s[40:41] offset:3072 sc1\n" \
  "s_add_u32 s40, s40, 16000\n" \
  "s_addc_u32 s41, s41, 0\n" \
  "global_load_dwordx4 v[32:35], v238, s[40:41] offset:0 sc1\n" \
  "global_load_dwordx4 v[36:39], v238, s[40:41] offset:1024 sc1\n" \
  "global_load_dwordx4 v[40:43], v238, s[40:41] offset:2048 sc1\n" \
  "global_load_dwordx4 v[44:47], v238, s[40:41] offset:3072 sc1\n" \
  "s_add_u32 s40, s40, 16000\n" \
  "s_addc_u32 s41, s41, 0\n" \
  "global_load_dwordx4 v[48:51], v238, s[40:41] offset:0 sc1\n" \
  "global_load_dwordx4 v[52:55], v238, s[40:41] offset:1024 sc1\n" \
  "global_load_dwordx4 v[56:59], v238, s[40:41] offset:2048 sc1\n" \
  "global_load_dwordx4 v[60:63], v238, s[40:41] offset:3072 sc1\n" \
  "s_add_u32 s40, s40, 16000\n" \
  "s_addc_u32 s41, s41, 0\n" \
  "LB_l_e:\n" \
  "ds_write_b16_d16_hi v195, v197 offset:0\n" \
  "ds_write_b16_d16_hi v195, v199 offset:16\n" \
  "ds_write_b16_d16_hi v195, v201 offset:32\n" \
  "ds_write_b16_d16_hi v235, v203 offset:64\n" \
  "ds_write_b16_d16_hi v235, v205 offset:80\n" \
  "ds_write_b16_d16_hi v235, v207 offset:96\n" \
  "ds_write_b16_d16_hi v235, v209 offset:112\n" \
  "ds_write_b16_d16_hi v235, v211 offset:128\n" \
  "ds_write_b16_d16_hi v235, v213 offset:144\n" \
  "ds_write_b16_d16_hi v235, v215 offset:160\n" \
  "ds_write_b16_d16_hi v235, v217 offset:176\n" \
  "ds_write_b16_d16_hi v195, v219 offset:48\n" \
  "ds_write_b16_d16_hi v195, v221 offset:192\n" \
  "ds_write_b16_d16_hi v195, v223 offset:208\n" \
  "ds_write_b16_d16_hi v195, v225 offset:224\n" \
  "ds_write_b16_d16_hi v195, v227 offset:240\n" \
  "ds_read_b128 v[244:247], v254 offset:0\n" \
  "ds_read_b128 v[248:251], v254 offset:1088\n" \
  "s_lshr_b32 s35, s7, 5\n" \
  "s_mul_i32 s35, s35, 819200\n" \
  "s_and_b32 s36, s7, 31\n" \
  "s_lshl_b32 s36, s36, 10\n" \
  "s_add_u32 s35, s35, s36\n" \
  "s_lshl_b32 s36, s4, 9\n" \
  "s_add_u32 s35, s35, s36\n" \
  "s_add_u32 s35, s35, 102793216\n" \
  "s_add_u32 s58, s22, s35\n" \
  "s_addc_u32 s59, s23, 0\n" \
  "s_waitcnt lgkmcnt(0)\n" \
  "s_cmp_ge_u32 s4, 896\n" \
  "s_cbranch_scc1 L17_\n" \
  "global_store_dwordx4 v238, v[244:247], s[58:59] sc1\n" \
  "s_cmp_ge_u32 s4, 768\n" \
  "s_cbranch_scc1 L17_\n" \
  "s_add_u32 s58, s58, 32768\n" \
  "s_addc_u32 s59, s59, 0\n" \
  "global_store_dwordx4 v238, v[248:251], s[58:59] sc1\n" \
  "L17_:\n" \
  "s_lshr_b32 s35, s30, 2\n" \
  "s_lshl3_add_u32 s33, s29, s35\n" \
  "s_and_b32 s34, s30, 3\n" \
  "s_lshl_b32 s59, s27, 11\n" \
  "s_mul_i32 s35, s33, 1600000\n" \
  "s_add_u32 s54, s22, s35\n" \
  "s_addc_u32 s55, s23, 0\n" \
  "s_mul_i32 s35, s34, 819200\n" \
  "s_lshl1_add_u32 s35, s59, s35\n" \
  "s_add_u32 s35, s35, 102400000\n" \
  "s_add_u32 s56, s22, s35\n" \
  "s_addc_u32 s57, s23, 0\n" \
  "s_lshr_b32 s35, s30, 4\n" \
  "s_lshl1_add_u32 s35, s29, s35\n" \
  "s_lshl_b32 s35, s35, 6\n" \
  "s_bfe_u32 s37, s30, 0x20002\n" \
  "s_lshl3_add_u32 s35, s37, s35\n" \
  "v_and_b32 v202, 16, v255\n" \
  "v_lshl_add_u32 v202, v202, 1, s35\n" \
  "global_load_dwordx2 v[204:205], v202, s[16:17]\n" \
  "global_load_dwordx2 v[206:207], v202, s[18:19]\n" \
  "v_mov_b32 v202, v238\n" \
  "global_load_dwordx4 v[208:211], v238, s[56:57] offset:0 sc1\n" \
  "global_load_dwordx4 v[212:215], v238, s[56:57] offset:1024 sc1\n" \
  "global_load_dwordx4 v[216:219], v238, s[56:57] offset:2048 sc1\n" \
  "global_load_dwordx4 v[220:223], v238, s[56:57] offset:3072 sc1\n" \
  "s_add_u32 s56, s56, 32768\n" \
  "s_addc_u32 s57, s57, 0\n" \
  "global_load_dwordx4 v[224:227], v238, s[56:57] sc1\n" \
  "global_load_dwordx4 v[228:231], v238, s[56:57] offset:1024 sc1\n" \
  "global_load_dwordx4 v[232:235], v238, s[56:57] offset:2048 sc1\n" \
  "global_load_dwordx4 v[236:239], v238, s[56:57] offset:3072 sc1\n" \
  "s_add_u32 s56, s56, 32768\n" \
  "s_addc_u32 s57, s57, 0\n" \
  "s_bitcmp1_b32 s30, 2\n" \
  "s_cbranch_scc1 LB_t_h\n" \
  "s_waitcnt lgkmcnt(12)\n" \
  "v_mfma_f32_16x16x32_bf16 v[160:163], v[128:131], v[0:3], 0\n" \
  "v_mfma_f32_16x16x32_bf16 v[160:163], v[132:135], v[4:7], v[160:163]\n" \
  "s_waitcnt lgkmcnt(8)\n" \
  "v_mfma_f32_16x16x32_bf16 v[160:163], v[136:139], v[8:11], v[160:163]\n" \
  "v_mfma_f32_16x16x32_bf16 v[160:163], v[140:143], v[12:15], v[160:163]\n" \
  "s_waitcnt lgkmcnt(0)\n" \
  "v_mfma_f32_16x16x32_bf16 v[164:167], v[156:159], v[12:15], 0\n" \
  "v_mfma_f32_16x16x32_bf16 v[164:167], v[152:155], v[8:11], v[164:167]\n" \
  "v_mfma_f32_16x16x32_bf16 v[164:167], v[148:151], v[4:7], v[164:167]\n" \
  "v_mfma_f32_16x16x32_bf16 v[164:167], v[144:147], v[0:3], v[164:167]\n" \
  "v_mfma_f32_16x16x32_bf16 v[172:175], v[144:147], v[16:19], 0\n" \
  "v_mfma_f32_16x16x32_bf16 v[172:175], v[148:151], v[20:23], v[172:175]\n" \
  "v_mfma_f32_16x16x32_bf16 v[172:175], v[152:155], v[24:27], v[172:175]\n" \
  "v_cvt_pk_bf16_f32 v176, v160, v161\n" \
  "v_cvt_pk_bf16_f32 v177, v162, v163\n" \
  "v_mfma_f32_16x16x32_bf16 v[172:175], v[156:159], v[28:31], v[172:175]\n" \
  "v_mfma_f32_16x16x32_bf16 v[168:171], v[140:143], v[28:31], 0\n" \
  "v_mfma_f32_16x16x32_bf16 v[168:171], v[136:139], v[24:27], v[168:171]\n" \
  "v_cvt_pk_bf16_f32 v178, v164, v165\n" \
  "v_cvt_pk_bf16_f32 v179, v166, v167\n" \
  "v_mfma_f32_16x16x32_bf16 v[168:171], v[132:135], v[20:23], v[168:171]\n" \
  "v_mfma_f32_16x16x32_bf16 v[168:171], v[128:131], v[16:19], v[168:171]\n" \
  "s_waitcnt vmcnt(63)\n" \
  "global_store_dwordx4 v186, v[176:179], s[46:47] offset:0 sc1\n" \
  "v_mfma_f32_16x16x32_bf16 v[160:163], v[128:131], v[32:35], 0\n" \
  "v_cvt_pk_bf16_f32 v182, v172, v173\n" \
  "v_cvt_pk_bf16_f32 v183, v174, v175\n" \
  "v_mfma_f32_16x16x32_bf16 v[160:163], v[132:135], v[36:39], v[160:163]\n" \
  "v_mfma_f32_16x16x32_bf16 v[160:163], v[136:139], v[40:43], v[160:163]\n" \
  "v_mfma_f32_16x16x32_bf16 v[160:163], v[140:143], v[44:47], v[160:163]\n" \
  "v_cvt_pk_bf16_f32 v180, v168, v169\n" \
  "v_cvt_pk_bf16_f32 v181, v170, v171\n" \
  "v_mfma_f32_16x16x32_bf16 v[164:167], v[156:159], v[44:47], 0\n" \
  "v_mfma_f32_16x16x32_bf16 v[164:167], v[152:155], v[40:43], v[164:167]\n" \
  "v_mfma_f32_16x16x32_bf16 v[164:167], v[148:151], v[36:39], v[164:167]\n" \
  "v_mfma_f32_16x16x32_bf16 v[164:167], v[144:147], v[32:35], v[164:167]\n" \
  "s_waitcnt vmcnt(63)\n" \
  "global_store_dwordx4 v186, v[180:183], s[46:47] offset:256 sc1\n" \
  "v_mfma_f32_16x16x32_bf16 v[172:175], v[144:147], v[48:51], 0\n" \
  "v_cvt_pk_bf16_f32 v176, v160, v161\n" \
  "v_cvt_pk_bf16_f32 v177, v162, v163\n" \
  "v_mfma_f32_16x16x32_bf16 v[172:175], v[148:151], v[52:55], v[172:175]\n" \
  "v_mfma_f32_16x16x32_bf16 v[172:175], v[152:155], v[56:59], v[172:175]\n" \
  "v_mfma_f32_16x16x32_bf16 v[172:175], v[156:159], v[60:63], v[172:175]\n" \
  "v_cvt_pk_bf16_f32 v178, v164, v165\n" \
  "v_cvt_pk_bf16_f32 v179, v166, v167\n" \
  "v_mfma_f32_16x16x32_bf16 v[168:171], v[140:143], v[60:63], 0\n" \
  "v_mfma_f32_16x16x32_bf16 v[168:171], v[136:139], v[56:59], v[168:171]\n" \
  "v_mfma_f32_16x16x32_bf16 v[168:171], v[132:135], v[52:55], v[168:171]\n" \
  "v_mfma_f32_16x16x32_bf16 v[168:171], v[128:131], v[48:51], v[168:171]\n" \
  "s_waitcnt vmcnt(63)\n" \
  "global_store_dwordx4 v186, v[176:179], s[46:47] offset:512 sc1\n" \
  "v_cvt_pk_bf16_f32 v182, v172, v173\n" \
  "v_cvt_pk_bf16_f32 v183, v174, v175\n" \
  "s_nop 3\n" \
  "v_cvt_pk_bf16_f32 v180, v168, v169\n" \
  "v_cvt_pk_bf16_f32 v181, v170, v171\n" \
  "s_waitcnt vmcnt(63)\n" \
  "global_store_dwordx4 v186, v[180:183], s[46:47] offset:768 sc1\n" \
  "s_branch LB_t_e\n" \
  "LT_w:\n" \
  "L8_:\n" \
  "s_sleep 1\n" \
  "ds_read_b32 v192, v190 offset:0\n" \
  "s_waitcnt lgkmcnt(0)\n" \
  "v_readfirstlane_b32 s35, v192\n" \
  "s_cmp_lt_u32 s35, s99\n" \
  "s_cbranch_scc1 L8_\n" \
  "s_branch LT_c\n" \
  "LT_e:\n" \
  "s_bitcmp1_b32 10, s98\n" \
  "s_cbranch_scc0 LT_r\n" \
  "global_load_dwordx4 v[240:243], v238, s[48:49] offset:0 sc1\n" \
  "s_waitcnt vmcnt(0)\n" \
  "global_store_dwordx4 v238, v[240:243], s[48:49] offset:1024 sc1\n" \
  "s_branch LT_r\n" \
  "LT_a:\n" \
  "s_cmp_lg_u32 s35, 4\n" \
  "s_cbranch_scc1 L9_\n" \
  "global_load_dword v193, v193, s[48:49] offset:1024 sc1\n" \
  "s_branch LT_s\n" \
  "L9_:\n" \
  "s_cmp_lg_u32 s35, 5\n" \
  "s_cbranch_scc1 L10_\n" \
  "s_memtime s[58:59]\n" \
  "s_memrealtime s[40:41]\n" \
  "s_mov_b32 s5, 0x10000\n" \
  "LB_ec:\n" \
  "v_readfirstlane_b32 s35, v193\n" \
  "s_cmp_eq_u32 s35, s39\n" \
  "s_cbranch_scc1 LB_ok\n" \
  "s_lshl_b32 s35, s26, 2\n" \
  "v_mov_b32 v193, s35\n" \
  "s_sleep 8\n" \
  "global_load_dword v193, v193, s[48:49] offset:1024 sc1\n" \
  "s_waitcnt vmcnt(0)\n" \
  "s_sub_u32 s5, s5, 1\n" \
  "s_cbranch_scc0 LB_ec\n" \
  "LB_ok:\n" \
  "s_lshl_b32 s35, s26, 2\n" \
  "v_mov_b32 v244, s35\n" \
  "s_waitcnt lgkmcnt(0)\n" \
  "s_sub_u32 s88, s58, s88\n" \
  "s_sub_u32 s88, 106000, s88\n" \
  "s_sub_u32 s90, s58, s90\n" \
  "s_sub_u32 s42, s40, s42\n" \
  "s_lshl_b32 s40, s40, 4\n" \
  "v_cvt_f32_u32 v245, s90\n" \
  "v_cvt_f32_u32 v246, s42\n" \
  "v_rcp_f32 v245, v245\n" \
  "v_cvt_f32_i32 v247, s88\n" \
  "v_mul_f32 v246, 0x4111eb85, v246\n" \
  "v_mul_f32 v245, v245, v246\n" \
  "v_mul_f32 v245, v245, v247\n" \
  "v_cvt_i32_f32 v245, v245\n" \
  "v_add_u32 v245, s40, v245\n" \
  "v_or_b32 v245, 1, v245\n" \
  "s_mov_b64 exec, 1\n" \
  "global_store_dword v244, v245, s[48:49] offset:2048 sc1\n" \
  "s_mov_b64 exec, -1\n" \
  "s_branch LT_s\n" \
  "L10_:\n" \
  "s_cmp_lg_u32 s35, 7\n" \
  "s_cbranch_scc1 L11_\n" \
  "s_mov_b32 exec_hi, 0\n" \
  "global_atomic_add v196, v191, s[48:49] offset:3072 sc1\n" \
  "s_mov_b64 exec, -1\n" \
  "s_branch LT_s\n" \
  "L11_:\n" \
  "s_cmp_lg_u32 s35, 23\n" \
  "s_cbranch_scc1 L12_\n" \
  "s_lshl_b32 s35, s30, 5\n" \
  "v_lshrrev_b32 v206, 3, v255\n" \
  "v_and_b32 v208, 7, v255\n" \
  "v_lshl_add_u32 v210, v206, 2, s35\n" \
  "v_lshl_add_u32 v212, v208, 2, s35\n" \
  "global_load_dword v202, v210, s[48:49] offset:2048 sc1\n" \
  "global_load_dword v204, v212, s[48:49] offset:2048 sc1\n" \
  "s_branch LT_s\n" \
  "L12_:\n" \
  "s_cmp_lg_u32 s35, 24\n" \
  "s_cbranch_scc1 L13_\n" \
  "s_mov_b32 s5, 0x10000\n" \
  "LB_z:\n" \
  "v_min_u32 v214, v202, v204\n" \
  "v_cmp_eq_u32 s[58:59], 0, v214\n" \
  "s_sub_u32 s5, s5, 1\n" \
  "s_cbranch_scc1 LB_zx\n" \
  "s_cmp_eq_u64 s[58:59], 0\n" \
  "s_cbranch_scc1 LB_zx\n" \
  "s_cmp_lg_u32 s33, 1021\n" \
  "s_cbranch_scc1 LB_n1\n" \
  "global_load_dwordx4 v[240:243], v238, s[48:49] offset:0 sc1\n" \
  "s_waitcnt vmcnt(0)\n" \
  "global_store_dwordx4 v238, v[240:243], s[48:49] offset:1024 sc1\n" \
  "LB_n1:\n" \
  "s_sleep 4\n" \
  "global_load_dword v202, v210, s[48:49] offset:2048 sc1\n" \
  "global_load_dword v204, v212, s[48:49] offset:2048 sc1\n" \
  "s_waitcnt vmcnt(0)\n" \
  "s_branch LB_z\n" \
  "LB_zx:\n" \
  "v_and_or_b32 v202, v202, -8, v206\n" \
  "v_and_or_b32 v204, v204, -8, v208\n" \
  "v_cmp_lt_u32 s[58:59], v204, v202\n" \
  "v_sub_u32 v202, v204, v202\n" \
  "s_lshl_b32 s35, s29, 3\n" \
  "s_lshr_b64 s[40:41], s[58:59], s35\n" \
  "s_and_b32 s40, s40, 0xff\n" \
  "s_bcnt1_i32_b32 s40, s40\n" \
  "s_sub_u32 s40, 7, s40\n" \
  "s_mov_b32 s36, s29\n" \
  "s_bfe_u32 s41, s58, 0x80000\n" \
  "s_bcnt1_i32_b32 s41, s41\n" \
  "s_cmp_eq_u32 s41, s40\n" \
  "s_cselect_b32 s36, 0, s36\n" \
  "s_bfe_u32 s41, s58, 0x80008\n" \
  "s_bcnt1_i32_b32 s41, s41\n" \
  "s_cmp_eq_u32 s41, s40\n" \
  "s_cselect_b32 s36, 1, s36\n" \
  "s_bfe_u32 s41, s58, 0x80010\n" \
  "s_bcnt1_i32_b32 s41, s41\n" \
  "s_cmp_eq_u32 s41, s40\n" \
  "s_cselect_b32 s36, 2, s36\n" \
  "s_bfe_u32 s41, s58, 0x80018\n" \
  "s_bcnt1_i32_b32 s41, s41\n" \
  "s_cmp_eq_u32 s41, s40\n" \
  "s_cselect_b32 s36, 3, s36\n" \
  "s_bfe_u32 s41, s59, 0x80000\n" \
  "s_bcnt1_i32_b32 s41, s41\n" \
  "s_cmp_eq_u32 s41, s40\n" \
  "s_cselect_b32 s36, 4, s36\n" \
  "s_bfe_u32 s41, s59, 0x80008\n" \
  "s_bcnt1_i32_b32 s41, s41\n" \
  "s_cmp_eq_u32 s41, s40\n" \
  "s_cselect_b32 s36, 5, s36\n" \
  "s_bfe_u32 s41, s59, 0x80010\n" \
  "s_bcnt1_i32_b32 s41, s41\n" \
  "s_cmp_eq_u32 s41, s40\n" \
  "s_cselect_b32 s36, 6, s36\n" \
  "s_bfe_u32 s41, s59, 0x80018\n" \
  "s_bcnt1_i32_b32 s41, s41\n" \
  "s_cmp_eq_u32 s41, s40\n" \
  "s_cselect_b32 s36, 7, s36\n" \
  "s_add_u32 s35, s35, s36\n" \
  "v_readlane_b32 s41, v202, s35\n" \
  "s_sub_u32 s35, 0, s41\n" \
  "s_cmp_lt_u32 s40, 4\n" \
  "s_cselect_b32 s41, s35, s41\n" \
  "s_min_u32 s41, s41, 0xffff\n" \
  "s_mul_i32 s41, s41, 39\n" \
  "s_add_u32 s41, s41, 28672\n" \
  "s_lshr_b32 s41, s41, 16\n" \
  "s_min_u32 s41, s41, 4\n" \
  "s_sub_u32 s35, 0, s41\n" \
  "s_cmp_lt_u32 s40, 4\n" \
  "s_cselect_b32 s35, s35, s41\n" \
  "s_lshl4_add_u32 s35, s36, s35\n" \
  "s_add_u32 s35, s35, 132\n" \
  "v_mov_b32 v204, s35\n" \
  "ds_write_b32 v190, v204 offset:4\n" \
  "s_branch LT_s\n" \
  "L13_:\n" \
  "s_cmp_lg_u32 s35, 25\n" \
  "s_cbranch_scc1 L14_\n" \
  "s_mov_b64 exec, 0xff\n" \
  "global_load_dword v200, v198, s[48:49] offset:3072 sc1\n" \
  "s_mov_b64 exec, -1\n" \
  "s_branch LT_s\n" \
  "L14_:\n" \
  "s_cmp_lg_u32 s35, 26\n" \
  "s_cbranch_scc1 L15_\n" \
  "s_mov_b64 s[58:59], vcc\n" \
  "s_mov_b32 s5, 0x10000\n" \
  "LB_p:\n" \
  "s_mov_b64 exec, 0xff\n" \
  "v_cmp_le_u32 vcc, 32, v200\n" \
  "s_mov_b64 exec, -1\n" \
  "s_sub_u32 s5, s5, 1\n" \
  "s_cselect_b32 s35, 0xff, vcc_lo\n" \
  "s_cmp_eq_u32 s35, 0xff\n" \
  "s_cbranch_scc1 LB_p_x\n" \
  "s_cmp_lg_u32 s33, 1021\n" \
  "s_cbranch_scc1 LB_n2\n" \
  "global_load_dwordx4 v[240:243], v238, s[48:49] offset:0 sc1\n" \
  "s_waitcnt vmcnt(0)\n" \
  "global_store_dwordx4 v238, v[240:243], s[48:49] offset:1024 sc1\n" \
  "LB_n2:\n" \
  "s_sleep 2\n" \
  "s_mov_b64 exec, 0xff\n" \
  "global_load_dword v200, v198, s[48:49] offset:3072 sc1\n" \
  "s_mov_b64 exec, -1\n" \
  "s_waitcnt vmcnt(0)\n" \
  "s_branch LB_p\n" \
  "LB_p_x:\n" \
  "s_mov_b64 vcc, s[58:59]\n" \
  "s_branch LT_s\n" \
  "L15_:\n" \
  "s_branch LT_s\n" \
  "LT_m:\n" \
  "ds_write_b16_d16_hi v195, v196 offset:0\n" \
  "ds_write_b16_d16_hi v195, v198 offset:16\n" \
  "ds_write_b16_d16_hi v195, v200 offset:32\n" \
  "ds_write_b16_d16_hi v235, v202 offset:64\n" \
  "ds_write_b16_d16_hi v235, v204 offset:80\n" \
  "ds_write_b16_d16_hi v235, v206 offset:96\n" \
  "ds_write_b16_d16_hi v235, v208 offset:112\n" \
  "ds_write_b16_d16_hi v235, v210 offset:128\n" \
  "ds_write_b16_d16_hi v235, v212 offset:144\n" \
  "ds_write_b16_d16_hi v235, v214 offset:160\n" \
  "ds_write_b16_d16_hi v235, v216 offset:176\n" \
  "ds_write_b16_d16_hi v195, v218 offset:48\n" \
  "ds_write_b16_d16_hi v195, v220 offset:192\n" \
  "ds_write_b16_d16_hi v195, v222 offset:208\n" \
  "ds_write_b16_d16_hi v195, v224 offset:224\n" \
  "ds_write_b16_d16_hi v195, v226 offset:240\n" \
  "ds_read_b128 v[244:247], v254 offset:0\n" \
  "ds_read_b128 v[248:251], v254 offset:1088\n" \
  "s_lshr_b32 s35, s7, 5\n" \
  "s_mul_i32 s35, s35, 819200\n" \
  "s_and_b32 s36, s7, 31\n" \
  "s_lshl_b32 s36, s36, 10\n" \
  "s_add_u32 s35, s35, s36\n" \
  "s_lshl_b32 s36, s4, 9\n" \
  "s_add_u32 s35, s35, s36\n" \
  "s_add_u32 s35, s35, 102400000\n" \
  "s_add_u32 s58, s22, s35\n" \
  "s_addc_u32 s59, s23, 0\n" \
  "s_waitcnt lgkmcnt(0)\n" \
  "s_cmp_ge_u32 s4, 768\n" \
  "s_cbranch_scc1 L16_\n" \
  "global_store_dwordx4 v238, v[244:247], s[58:59] sc1\n" \
  "s_cmp_ge_u32 s4, 768\n" \
  "s_cbranch_scc1 L16_\n" \
  "s_add_u32 s58, s58, 32768\n" \
  "s_addc_u32 s59, s59, 0\n" \
  "global_store_dwordx4 v238, v[248:251], s[58:59] sc1\n" \
  "L16_:\n" \
  "v_mov_b32 v234, 0\n" \
  "v_mov_b32 v236, v237\n" \
  "s_mov_b32 s38, 1\n" \
  "s_lshl_b32 s35, s29, 8\n" \
  "v_lshl_add_u32 v196, v255, 11, s35\n" \
  "s_lshl_b32 s35, s30, 11\n" \
  "v_lshl_add_u32 v198, v255, 8, s35\n" \
  "s_branch LT\n" \
  "LB_t_h:\n" \
  "s_waitcnt lgkmcnt(12)\n" \
  "v_mfma_f32_16x16x32_bf16 v[160:163], v[128:131], v[64:67], 0\n" \
  "v_mfma_f32_16x16x32_bf16 v[160:163], v[132:135], v[68:71], v[160:163]\n" \
  "s_waitcnt lgkmcnt(8)\n" \
  "v_mfma_f32_16x16x32_bf16 v[160:163], v[136:139], v[72:75], v[160:163]\n" \
  "v_mfma_f32_16x16x32_bf16 v[160:163], v[140:143], v[76:79], v[160:163]\n" \
  "s_waitcnt lgkmcnt(0)\n" \
  "v_mfma_f32_16x16x32_bf16 v[164:167], v[156:159], v[76:79], 0\n" \
  "v_mfma_f32_16x16x32_bf16 v[164:167], v[152:155], v[72:75], v[164:167]\n" \
  "v_mfma_f32_16x16x32_bf16 v[164:167], v[148:151], v[68:71], v[164:167]\n" \
  "v_mfma_f32_16x16x32_bf16 v[164:167], v[144:147], v[64:67], v[164:167]\n" \
  "v_mfma_f32_16x16x32_bf16 v[172:175], v[144:147], v[80:83], 0\n" \
  "v_mfma_f32_16x16x32_bf16 v[172:175], v[148:151], v[84:87], v[172:175]\n" \
  "v_mfma_f32_16x16x32_bf16 v[172:175], v[152:155], v[88:91], v[172:175]\n" \
  "v_cvt_pk_bf16_f32 v176, v160, v161\n" \
  "v_cvt_pk_bf16_f32 v177, v162, v163\n" \
  "v_mfma_f32_16x16x32_bf16 v[172:175], v[156:159], v[92:95], v[172:175]\n" \
  "v_mfma_f32_16x16x32_bf16 v[168:171], v[140:143], v[92:95], 0\n" \
  "v_mfma_f32_16x16x32_bf16 v[168:171], v[136:139], v[88:91], v[168:171]\n" \
  "v_cvt_pk_bf16_f32 v178, v164, v165\n" \
  "v_cvt_pk_bf16_f32 v179, v166, v167\n" \
  "v_mfma_f32_16x16x32_bf16 v[168:171], v[132:135], v[84:87], v[168:171]\n" \
  "v_mfma_f32_16x16x32_bf16 v[168:171], v[128:131], v[80:83], v[168:171]\n" \
  "s_waitcnt vmcnt(63)\n" \
  "global_store_dwordx4 v186, v[176:179], s[46:47] offset:1024 sc1\n" \
  "v_mfma_f32_16x16x32_bf16 v[160:163], v[128:131], v[96:99], 0\n" \
  "v_cvt_pk_bf16_f32 v182, v172, v173\n" \
  "v_cvt_pk_bf16_f32 v183, v174, v175\n" \
  "v_mfma_f32_16x16x32_bf16 v[160:163], v[132:135], v[100:103], v[160:163]\n" \
  "v_mfma_f32_16x16x32_bf16 v[160:163], v[136:139], v[104:107], v[160:163]\n" \
  "v_mfma_f32_16x16x32_bf16 v[160:163], v[140:143], v[108:111], v[160:163]\n" \
  "v_cvt_pk_bf16_f32 v180, v168, v169\n" \
  "v_cvt_pk_bf16_f32 v181, v170, v171\n" \
  "v_mfma_f32_16x16x32_bf16 v[164:167], v[156:159], v[108:111], 0\n" \
  "v_mfma_f32_16x16x32_bf16 v[164:167], v[152:155], v[104:107], v[164:167]\n" \
  "v_mfma_f32_16x16x32_bf16 v[164:167], v[148:151], v[100:103], v[164:167]\n" \
  "v_mfma_f32_16x16x32_bf16 v[164:167], v[144:147], v[96:99], v[164:167]\n" \
  "s_waitcnt vmcnt(63)\n" \
  "global_store_dwordx4 v186, v[180:183], s[46:47] offset:1280 sc1\n" \
  "v_mfma_f32_16x16x32_bf16 v[168:171], v[128:131], v[112:115], 0\n" \
  "v_cvt_pk_bf16_f32 v176, v160, v161\n" \
  "v_cvt_pk_bf16_f32 v177, v162, v163\n" \
  "v_mfma_f32_16x16x32_bf16 v[168:171], v[132:135], v[116:119], v[168:171]\n" \
  "v_mfma_f32_16x16x32_bf16 v[168:171], v[136:139], v[120:123], v[168:171]\n" \
  "v_mfma_f32_16x16x32_bf16 v[168:171], v[140:143], v[124:127], v[168:171]\n" \
  "v_cvt_pk_bf16_f32 v178, v164, v165\n" \
  "v_cvt_pk_bf16_f32 v179, v166, v167\n" \
  "v_mfma_f32_16x16x32_bf16 v[172:175], v[156:159], v[124:127], 0\n" \
  "v_mfma_f32_16x16x32_bf16 v[172:175], v[152:155], v[120:123], v[172:175]\n" \
  "v_mfma_f32_16x16x32_bf16 v[172:175], v[148:151], v[116:119], v[172:175]\n" \
  "v_mfma_f32_16x16x32_bf16 v[172:175], v[144:147], v[112:115], v[172:175]\n" \
  "s_waitcnt vmcnt(63)\n" \
  "global_store_dwordx4 v187, v[176:179], s[46:47] offset:0 sc1\n" \
  "v_cvt_pk_bf16_f32 v180, v168, v169\n" \
  "v_cvt_pk_bf16_f32 v181, v170, v171\n" \
  "s_nop 3\n" \
  "v_cvt_pk_bf16_f32 v182, v172, v173\n" \
  "v_cvt_pk_bf16_f32 v183, v174, v175\n" \
  "s_waitcnt vmcnt(63)\n" \
  "global_store_dwordx4 v188, v[180:183], s[46:47] offset:0 sc1\n" \
  "LB_t_e:\n" \
  "s_setprio 2\n" \
  "v_mov_b64 v[240:241], -1\n" \
  "v_mov_b64 v[242:243], -1\n" \
  "s_lshl2_add_u32 s35, s33, s34\n" \
  "s_lshl2_add_u32 s35, s35, s28\n" \
  "s_lshl_b32 s35, s35, 12\n" \
  "s_add_u32 s35, s35, 105676800\n" \
  "s_add_u32 s40, s22, s35\n" \
  "s_addc_u32 s41, s23, 0\n" \
  "v_mbcnt_lo_u32_b32 v189, -1, 0\n" \
  "v_mbcnt_hi_u32_b32 v189, -1, v189\n" \
  "v_lshlrev_b32 v188, 4, v189\n" \
  "s_lshl_b32 s35, s28, 12\n" \
  "v_add_u32 v128, s35, v188\n" \
  "v_mov_b32 v199, 1\n" \
  "v_lshrrev_b32 v180, 1, v189\n" \
  "v_xor_b32 v180, v180, v189\n" \
  "v_and_b32 v180, 8, v180\n" \
  "v_bfe_u32 v181, v189, 2, 1\n" \
  "v_lshlrev_b32_e64 v181, v181, 5\n" \
  "v_lshlrev_b32 v180, v180, v181\n" \
  "v_bfe_u32 v181, v189, 2, 2\n" \
  "v_lshlrev_b32_e64 v181, v181, 1\n" \
  "v_cmp_gt_u32 vcc, 32, v189\n" \
  "v_mov_b32 v191, 0x3f80\n" \
  "v_add_u32 v190, 32000, v188\n" \
  "v_cndmask_b32 v192, v181, v180, vcc\n" \
  "v_bfe_i32 v180, v192, 0, 1\n" \
  "v_and_b32 v180, v180, v191\n" \
  "v_bfe_i32 v181, v192, 1, 1\n" \
  "v_and_b32 v181, v181, v191\n" \
  "v_bfe_i32 v182, v192, 2, 1\n" \
  "v_and_b32 v182, v182, v191\n" \
  "v_bfe_i32 v183, v192, 3, 1\n" \
  "v_and_b32 v183, v183, v191\n" \
  "v_and_b32 v192, 3, v189\n" \
  "v_mul_u32_u24 v192, 0x5555, v192\n" \
  "v_add_u32 v189, 16000, v188\n" \
  "v_add_u32 v191, 48000, v188\n" \
  "s_waitcnt vmcnt(4)\n" \
  "v_lshlrev_b32 v184, 16, v204\n" \
  "v_and_b32 v185, 0xffff0000, v204\n" \
  "v_lshlrev_b32 v186, 16, v205\n" \
  "v_and_b32 v187, 0xffff0000, v205\n" \
  "global_store_dwordx4 v202, v[240:243], s[40:41] offset:0 sc1\n" \
  "global_store_dwordx4 v202, v[240:243], s[40:41] offset:1024 sc1\n" \
  "global_store_dwordx4 v202, v[240:243], s[40:41] offset:2048 sc1\n" \
  "global_store_dwordx4 v202, v[240:243], s[40:41] offset:3072 sc1\n" \
  "s_load_dwordx2 s[24:25], s[0:1], 0x40\n" \
  "s_setprio 0\n" \
  "s_barrier\n" \
  "s_bitcmp1_b32 s30, 2\n" \
  "s_cbranch_scc1 LB_w_h\n" \
  "ds_write_b128 v128, v[64:67] offset:0\n" \
  "ds_write_b128 v128, v[68:71] offset:1024\n" \
  "ds_write_b128 v128, v[72:75] offset:2048\n" \
  "ds_write_b128 v128, v[76:79] offset:3072\n" \
  "s_add_u32 s35, s59, 64000\n" \
  "s_add_u32 s54, s54, s35\n" \
  "s_addc_u32 s55, s55, 0\n" \
  "s_add_u32 s58, s59, 0x10000\n" \
  "s_mov_b32 m0, s58\n" \
  "s_add_u32 s35, s58, 0x8000\n" \
  "global_load_lds_dwordx4 v188, s[54:55] sc1\n" \
  "global_load_lds_dwordx4 v188, s[54:55] offset:1024 sc1\n" \
  "ds_write_b128 v128, v[80:83] offset:16384\n" \
  "ds_write_b128 v128, v[84:87] offset:17408\n" \
  "ds_write_b128 v128, v[88:91] offset:18432\n" \
  "ds_write_b128 v128, v[92:95] offset:19456\n" \
  "ds_write_b128 v128, v[96:99] offset:32768\n" \
  "ds_write_b128 v128, v[100:103] offset:33792\n" \
  "ds_write_b128 v128, v[104:107] offset:34816\n" \
  "ds_write_b128 v128, v[108:111] offset:35840\n" \
  "s_add_u32 m0, s58, 0x4000\n" \
  "s_cmp_lt_u32 s35, 0x28000\n" \
  "global_load_lds_dwordx4 v189, s[54:55] sc1\n" \
  "global_load_lds_dwordx4 v189, s[54:55] offset:1024 sc1\n" \
  "s_cselect_b32 s58, s35, s59\n" \
  "s_add_u32 s54, s54, 32000\n" \
  "s_addc_u32 s55, s55, 0\n" \
  "ds_write_b128 v128, v[112:115] offset:49152\n" \
  "ds_write_b128 v128, v[116:119] offset:50176\n" \
  "ds_write_b128 v128, v[120:123] offset:51200\n" \
  "ds_write_b128 v128, v[124:127] offset:52224\n" \
  "s_branch LB_w_e\n" \
  "LB_w_h:\n" \
  "ds_write_b128 v128, v[0:3] offset:0\n" \
  "ds_write_b128 v128, v[4:7] offset:1024\n" \
  "ds_write_b128 v128, v[8:11] offset:2048\n" \
  "ds_write_b128 v128, v[12:15] offset:3072\n" \
  "s_add_u32 s35, s59, 64000\n" \
  "s_add_u32 s54, s54, s35\n" \
  "s_addc_u32 s55, s55, 0\n" \
  "s_add_u32 s58, s59, 0x10000\n" \
  "s_mov_b32 m0, s58\n" \
  "s_add_u32 s35, s58, 0x8000\n" \
  "global_load_lds_dwordx4 v188, s[54:55] sc1\n" \
  "global_load_lds_dwordx4 v188, s[54:55] offset:1024 sc1\n" \
  "ds_write_b128 v128, v[16:19] offset:16384\n" \
  "ds_write_b128 v128, v[20:23] offset:17408\n" \
  "ds_write_b128 v128, v[24:27] offset:18432\n" \
  "ds_write_b128 v128, v[28:31] offset:19456\n" \
  "ds_write_b128 v128, v[32:35] offset:32768\n" \
  "ds_write_b128 v128, v[36:39] offset:33792\n" \
  "ds_write_b128 v128, v[40:43] offset:34816\n" \
  "ds_write_b128 v128, v[44:47] offset:35840\n" \
  "s_add_u32 m0, s58, 0x4000\n" \
  "s_cmp_lt_u32 s35, 0x28000\n" \
  "global_load_lds_dwordx4 v189, s[54:55] sc1\n" \
  "global_load_lds_dwordx4 v189, s[54:55] offset:1024 sc1\n" \
  "s_cselect_b32 s58, s35, s59\n" \
  "s_add_u32 s54, s54, 32000\n" \
  "s_addc_u32 s55, s55, 0\n" \
  "ds_write_b128 v128, v[48:51] offset:49152\n" \
  "ds_write_b128 v128, v[52:55] offset:50176\n" \
  "ds_write_b128 v128, v[56:59] offset:51200\n" \
  "ds_write_b128 v128, v[60:63] offset:52224\n" \
  "LB_w_e:\n" \
  "s_waitcnt lgkmcnt(0)\n" \
  "s_mov_b32 s51, 7\n" \
  "s_setprio 0\n" \
  "s_barrier\n" \
  "LG_start:\n" \
  "s_sub_u32 s35, 14960, s59\n" \
  "v_min_u32 v156, s35, v188\n" \
  "v_add_u32 v157, 16000, v156\n" \
  "v_add_u32 v158, 32000, v156\n" \
  "v_add_u32 v159, 48000, v156\n" \
  "v_lshlrev_b32_sdwa v64, 4, v208 dst_sel:DWORD dst_unused:UNUSED_PAD src0_sel:DWORD src1_sel:WORD_0\n" \
  "ds_read_b128 v[68:71], v64\n" \
  "v_mov_b64 v[0:1], v[184:185]\n" \
  "v_lshlrev_b32_sdwa v65, 4, v208 dst_sel:DWORD dst_unused:UNUSED_PAD src0_sel:DWORD src1_sel:WORD_1\n" \
  "ds_read_b128 v[72:75], v65\n" \
  "v_mov_b64 v[2:3], v[186:187]\n" \
  "v_lshlrev_b32_sdwa v66, 4, v209 dst_sel:DWORD dst_unused:UNUSED_PAD src0_sel:DWORD src1_sel:WORD_0\n" \
  "ds_read_b128 v[76:79], v66\n" \
  "v_mov_b64 v[4:5], v[184:185]\n" \
  "v_lshlrev_b32_sdwa v67, 4, v209 dst_sel:DWORD dst_unused:UNUSED_PAD src0_sel:DWORD src1_sel:WORD_1\n" \
  "ds_read_b128 v[80:83], v67\n" \
  "v_mov_b64 v[6:7], v[186:187]\n" \
  "s_mov_b32 m0, s58\n" \
  "s_add_u32 s35, s58, 0x8000\n" \
  "global_load_lds_dwordx4 v188, s[54:55] sc1\n" \
  "global_load_lds_dwordx4 v156, s[54:55] offset:1024 sc1\n" \
  "v_lshlrev_b32_sdwa v64, 4, v210 dst_sel:DWORD dst_unused:UNUSED_PAD src0_sel:DWORD src1_sel:WORD_0\n" \
  "ds_read_b128 v[84:87], v64\n" \
  "v_mov_b64 v[8:9], v[184:185]\n" \
  "v_lshlrev_b32_sdwa v65, 4, v210 dst_sel:DWORD dst_unused:UNUSED_PAD src0_sel:DWORD src1_sel:WORD_1\n" \
  "ds_read_b128 v[88:91], v65\n" \
  "v_mov_b64 v[10:11], v[186:187]\n" \
  "v_lshlrev_b32_sdwa v66, 4, v211 dst_sel:DWORD dst_unused:UNUSED_PAD src0_sel:DWORD src1_sel:WORD_0\n" \
  "ds_read_b128 v[92:95], v66\n" \
  "v_mov_b64 v[12:13], v[184:185]\n" \
  "v_lshlrev_b32_sdwa v67, 4, v211 dst_sel:DWORD dst_unused:UNUSED_PAD src0_sel:DWORD src1_sel:WORD_1\n" \
  "ds_read_b128 v[96:99], v67\n" \
  "v_mov_b64 v[14:15], v[186:187]\n" \
  "s_add_u32 m0, s58, 0x4000\n" \
  "s_cmp_lt_u32 s35, 0x28000\n" \
  "global_load_lds_dwordx4 v189, s[54:55] sc1\n" \
  "global_load_lds_dwordx4 v157, s[54:55] offset:1024 sc1\n" \
  "s_cselect_b32 s58, s35, s59\n" \
  "v_lshlrev_b32_sdwa v64, 4, v212 dst_sel:DWORD dst_unused:UNUSED_PAD src0_sel:DWORD src1_sel:WORD_0\n" \
  "ds_read_b128 v[100:103], v64\n" \
  "v_mov_b64 v[16:17], v[184:185]\n" \
  "v_lshlrev_b32_sdwa v65, 4, v212 dst_sel:DWORD dst_unused:UNUSED_PAD src0_sel:DWORD src1_sel:WORD_1\n" \
  "ds_read_b128 v[104:107], v65\n" \
  "v_mov_b64 v[18:19], v[186:187]\n" \
  "v_lshlrev_b32_sdwa v66, 4, v213 dst_sel:DWORD dst_unused:UNUSED_PAD src0_sel:DWORD src1_sel:WORD_0\n" \
  "ds_read_b128 v[108:111], v66\n" \
  "v_mov_b64 v[20:21], v[184:185]\n" \
  "v_lshlrev_b32_sdwa v67, 4, v213 dst_sel:DWORD dst_unused:UNUSED_PAD src0_sel:DWORD src1_sel:WORD_1\n" \
  "ds_read_b128 v[112:115], v67\n" \
  "v_mov_b64 v[22:23], v[186:187]\n" \
  "s_waitcnt lgkmcnt(10)\n" \
  "v_smfmac_f32_16x16x64_bf16 v[0:3], v[180:183], v[68:75], v192\n" \
  "v_lshlrev_b32_sdwa v64, 4, v214 dst_sel:DWORD dst_unused:UNUSED_PAD src0_sel:DWORD src1_sel:WORD_0\n" \
  "ds_read_b128 v[68:71], v64\n" \
  "v_mov_b64 v[24:25], v[184:185]\n" \
  "v_lshlrev_b32_sdwa v65, 4, v214 dst_sel:DWORD dst_unused:UNUSED_PAD src0_sel:DWORD src1_sel:WORD_1\n" \
  "ds_read_b128 v[72:75], v65\n" \
  "v_mov_b64 v[26:27], v[186:187]\n" \
  "s_mov_b32 m0, s58\n" \
  "s_add_u32 s35, s58, 0x8000\n" \
  "global_load_lds_dwordx4 v190, s[54:55] sc1\n" \
  "global_load_lds_dwordx4 v158, s[54:55] offset:1024 sc1\n" \
  "s_waitcnt lgkmcnt(10)\n" \
  "v_smfmac_f32_16x16x64_bf16 v[4:7], v[180:183], v[76:83], v192\n" \
  "v_lshlrev_b32_sdwa v66, 4, v215 dst_sel:DWORD dst_unused:UNUSED_PAD src0_sel:DWORD src1_sel:WORD_0\n" \
  "ds_read_b128 v[76:79], v66\n" \
  "v_mov_b64 v[28:29], v[184:185]\n" \
  "v_lshlrev_b32_sdwa v67, 4, v215 dst_sel:DWORD dst_unused:UNUSED_PAD src0_sel:DWORD src1_sel:WORD_1\n" \
  "ds_read_b128 v[80:83], v67\n" \
  "v_mov_b64 v[30:31], v[186:187]\n" \
  "s_waitcnt lgkmcnt(10)\n" \
  "v_smfmac_f32_16x16x64_bf16 v[8:11], v[180:183], v[84:91], v192\n" \
  "v_lshlrev_b32_sdwa v64, 4, v216 dst_sel:DWORD dst_unused:UNUSED_PAD src0_sel:DWORD src1_sel:WORD_0\n" \
  "ds_read_b128 v[84:87], v64\n" \
  "v_mov_b64 v[32:33], v[184:185]\n" \
  "v_lshlrev_b32_sdwa v65, 4, v216 dst_sel:DWORD dst_unused:UNUSED_PAD src0_sel:DWORD src1_sel:WORD_1\n" \
  "ds_read_b128 v[88:91], v65\n" \
  "v_mov_b64 v[34:35], v[186:187]\n" \
  "s_add_u32 m0, s58, 0x4000\n" \
  "s_cmp_lt_u32 s35, 0x28000\n" \
  "global_load_lds_dwordx4 v191, s[54:55] sc1\n" \
  "global_load_lds_dwordx4 v159, s[54:55] offset:1024 sc1\n" \
  "s_cselect_b32 s58, s35, s59\n" \
  "s_waitcnt lgkmcnt(10)\n" \
  "v_smfmac_f32_16x16x64_bf16 v[12:15], v[180:183], v[92:99], v192\n" \
  "v_lshlrev_b32_sdwa v66, 4, v217 dst_sel:DWORD dst_unused:UNUSED_PAD src0_sel:DWORD src1_sel:WORD_0\n" \
  "ds_read_b128 v[92:95], v66\n" \
  "v_mov_b64 v[36:37], v[184:185]\n" \
  "v_lshlrev_b32_sdwa v67, 4, v217 dst_sel:DWORD dst_unused:UNUSED_PAD src0_sel:DWORD src1_sel:WORD_1\n" \
  "ds_read_b128 v[96:99], v67\n" \
  "v_mov_b64 v[38:39], v[186:187]\n" \
  "s_waitcnt lgkmcnt(10)\n" \
  "v_smfmac_f32_16x16x64_bf16 v[16:19], v[180:183], v[100:107], v192\n" \
  "v_lshlrev_b32_sdwa v64, 4, v218 dst_sel:DWORD dst_unused:UNUSED_PAD src0_sel:DWORD src1_sel:WORD_0\n" \
  "ds_read_b128 v[100:103], v64\n" \
  "v_mov_b64 v[40:41], v[184:185]\n" \
  "v_lshlrev_b32_sdwa v65, 4, v218 dst_sel:DWORD dst_unused:UNUSED_PAD src0_sel:DWORD src1_sel:WORD_1\n" \
  "ds_read_b128 v[104:107], v65\n" \
  "v_mov_b64 v[42:43], v[186:187]\n" \
  "global_load_dwordx4 v[240:243], v188, s[56:57] sc1\n" \
  "global_load_dwordx4 v[244:247], v188, s[56:57] offset:1024 sc1\n" \
  "s_waitcnt lgkmcnt(10)\n" \
  "v_smfmac_f32_16x16x64_bf16 v[20:23], v[180:183], v[108:115], v192\n" \
  "v_lshlrev_b32_sdwa v66, 4, v219 dst_sel:DWORD dst_unused:UNUSED_PAD src0_sel:DWORD src1_sel:WORD_0\n" \
  "ds_read_b128 v[108:111], v66\n" \
  "v_mov_b64 v[44:45], v[184:185]\n" \
  "v_lshlrev_b32_sdwa v67, 4, v219 dst_sel:DWORD dst_unused:UNUSED_PAD src0_sel:DWORD src1_sel:WORD_1\n" \
  "ds_read_b128 v[112:115], v67\n" \
  "v_mov_b64 v[46:47], v[186:187]\n" \
  "s_waitcnt lgkmcnt(10)\n" \
  "v_smfmac_f32_16x16x64_bf16 v[24:27], v[180:183], v[68:75], v192\n" \
  "v_lshlrev_b32_sdwa v64, 4, v220 dst_sel:DWORD dst_unused:UNUSED_PAD src0_sel:DWORD src1_sel:WORD_0\n" \
  "ds_read_b128 v[68:71], v64\n" \
  "v_mov_b64 v[48:49], v[184:185]\n" \
  "v_lshlrev_b32_sdwa v65, 4, v220 dst_sel:DWORD dst_unused:UNUSED_PAD src0_sel:DWORD src1_sel:WORD_1\n" \
  "ds_read_b128 v[72:75], v65\n" \
  "v_mov_b64 v[50:51], v[186:187]\n" \
  "global_load_dwordx4 v[248:251], v188, s[56:57] offset:2048 sc1\n" \
  "global_load_dwordx4 v[252:255], v188, s[56:57] offset:3072 sc1\n" \
  "s_waitcnt lgkmcnt(10)\n" \
  "v_smfmac_f32_16x16x64_bf16 v[28:31], v[180:183], v[76:83], v192\n" \
  "v_lshlrev_b32_sdwa v66, 4, v221 dst_sel:DWORD dst_unused:UNUSED_PAD src0_sel:DWORD src1_sel:WORD_0\n" \
  "ds_read_b128 v[76:79], v66\n" \
  "v_mov_b64 v[52:53], v[184:185]\n" \
  "v_lshlrev_b32_sdwa v67, 4, v221 dst_sel:DWORD dst_unused:UNUSED_PAD src0_sel:DWORD src1_sel:WORD_1\n" \
  "ds_read_b128 v[80:83], v67\n" \
  "v_mov_b64 v[54:55], v[186:187]\n" \
  "s_waitcnt lgkmcnt(10)\n" \
  "v_smfmac_f32_16x16x64_bf16 v[32:35], v[180:183], v[84:91], v192\n" \
  "v_lshlrev_b32_sdwa v64, 4, v222 dst_sel:DWORD dst_unused:UNUSED_PAD src0_sel:DWORD src1_sel:WORD_0\n" \
  "ds_read_b128 v[84:87], v64\n" \
  "v_mov_b64 v[56:57], v[184:185]\n" \
  "v_lshlrev_b32_sdwa v65, 4, v222 dst_sel:DWORD dst_unused:UNUSED_PAD src0_sel:DWORD src1_sel:WORD_1\n" \
  "ds_read_b128 v[88:91], v65\n" \
  "v_mov_b64 v[58:59], v[186:187]\n" \
  "s_waitcnt lgkmcnt(10)\n" \
  "v_smfmac_f32_16x16x64_bf16 v[36:39], v[180:183], v[92:99], v192\n" \
  "v_lshlrev_b32_sdwa v66, 4, v223 dst_sel:DWORD dst_unused:UNUSED_PAD src0_sel:DWORD src1_sel:WORD_0\n" \
  "ds_read_b128 v[92:95], v66\n" \
  "v_mov_b64 v[60:61], v[184:185]\n" \
  "v_lshlrev_b32_sdwa v67, 4, v223 dst_sel:DWORD dst_unused:UNUSED_PAD src0_sel:DWORD src1_sel:WORD_1\n" \
  "ds_read_b128 v[96:99], v67\n" \
  "v_mov_b64 v[62:63], v[186:187]\n" \
  "s_add_u32 s56, s56, 32768\n" \
  "s_addc_u32 s57, s57, 0\n" \
  "s_add_u32 s54, s54, 64000\n" \
  "s_addc_u32 s55, s55, 0\n" \
  "s_cmp_eq_u32 s27, 4\n" \
  "s_cselect_b32 s37, 0x90, 0\n" \
  ".p2align 6\n" \
  "LG_loop:\n" \
  "s_bitcmp1_b32 s37, s51\n" \
  "s_waitcnt lgkmcnt(10)\n" \
  "v_smfmac_f32_16x16x64_bf16 v[40:43], v[180:183], v[100:107], v192\n" \
  "s_waitcnt lgkmcnt(8)\n" \
  "v_smfmac_f32_16x16x64_bf16 v[44:47], v[180:183], v[108:115], v192\n" \
  "s_waitcnt vmcnt(8)\n" \
  "s_cbranch_scc1 LG_h\n" \
  "LG_ret:\n" \
  "s_barrier\n" \
  "LG_aft:\n" \
  "v_lshlrev_b32_sdwa v64, 4, v224 dst_sel:DWORD dst_unused:UNUSED_PAD src0_sel:DWORD src1_sel:WORD_0\n" \
  "ds_read_b128 v[100:103], v64\n" \
  "v_lshlrev_b32_sdwa v65, 4, v224 dst_sel:DWORD dst_unused:UNUSED_PAD src0_sel:DWORD src1_sel:WORD_1\n" \
  "ds_read_b128 v[104:107], v65\n" \
  "v_lshlrev_b32_sdwa v66, 4, v225 dst_sel:DWORD dst_unused:UNUSED_PAD src0_sel:DWORD src1_sel:WORD_0\n" \
  "ds_read_b128 v[108:111], v66\n" \
  "v_lshlrev_b32_sdwa v67, 4, v225 dst_sel:DWORD dst_unused:UNUSED_PAD src0_sel:DWORD src1_sel:WORD_1\n" \
  "ds_read_b128 v[112:115], v67\n" \
  "s_waitcnt lgkmcnt(10)\n" \
  "v_smfmac_f32_16x16x64_bf16 v[48:51], v[180:183], v[68:75], v192\n" \
  "s_waitcnt lgkmcnt(8)\n" \
  "v_smfmac_f32_16x16x64_bf16 v[52:55], v[180:183], v[76:83], v192\n" \
  "s_waitcnt lgkmcnt(6)\n" \
  "v_smfmac_f32_16x16x64_bf16 v[56:59], v[180:183], v[84:91], v192\n" \
  "s_waitcnt lgkmcnt(4)\n" \
  "v_smfmac_f32_16x16x64_bf16 v[60:63], v[180:183], v[92:99], v192\n" \
  "s_barrier\n" \
  "s_mov_b32 m0, s58\n" \
  "s_add_u32 s35, s58, 0x8000\n" \
  "global_load_lds_dwordx4 v188, s[54:55] sc1\n" \
  "global_load_lds_dwordx4 v156, s[54:55] offset:1024 sc1\n" \
  "v_lshlrev_b32_sdwa v64, 4, v226 dst_sel:DWORD dst_unused:UNUSED_PAD src0_sel:DWORD src1_sel:WORD_0\n" \
  "ds_read_b128 v[68:71], v64\n" \
  "v_lshlrev_b32_sdwa v65, 4, v226 dst_sel:DWORD dst_unused:UNUSED_PAD src0_sel:DWORD src1_sel:WORD_1\n" \
  "ds_read_b128 v[72:75], v65\n" \
  "v_lshlrev_b32_sdwa v66, 4, v227 dst_sel:DWORD dst_unused:UNUSED_PAD src0_sel:DWORD src1_sel:WORD_0\n" \
  "ds_read_b128 v[76:79], v66\n" \
  "v_lshlrev_b32_sdwa v67, 4, v227 dst_sel:DWORD dst_unused:UNUSED_PAD src0_sel:DWORD src1_sel:WORD_1\n" \
  "ds_read_b128 v[80:83], v67\n" \
  "s_add_u32 m0, s58, 0x4000\n" \
  "s_cmp_lt_u32 s35, 0x28000\n" \
  "global_load_lds_dwordx4 v189, s[54:55] sc1\n" \
  "global_load_lds_dwordx4 v157, s[54:55] offset:1024 sc1\n" \
  "s_cselect_b32 s58, s35, s59\n" \
  "v_lshlrev_b32_sdwa v64, 4, v228 dst_sel:DWORD dst_unused:UNUSED_PAD src0_sel:DWORD src1_sel:WORD_0\n" \
  "ds_read_b128 v[84:87], v64\n" \
  "v_lshlrev_b32_sdwa v65, 4, v228 dst_sel:DWORD dst_unused:UNUSED_PAD src0_sel:DWORD src1_sel:WORD_1\n" \
  "ds_read_b128 v[88:91], v65\n" \
  "v_lshlrev_b32_sdwa v66, 4, v229 dst_sel:DWORD dst_unused:UNUSED_PAD src0_sel:DWORD src1_sel:WORD_0\n" \
  "ds_read_b128 v[92:95], v66\n" \
  "v_lshlrev_b32_sdwa v67, 4, v229 dst_sel:DWORD dst_unused:UNUSED_PAD src0_sel:DWORD src1_sel:WORD_1\n" \
  "ds_read_b128 v[96:99], v67\n" \
  "s_waitcnt lgkmcnt(10)\n" \
  "v_smfmac_f32_16x16x64_bf16 v[0:3], v[180:183], v[100:107], v192\n" \
  "v_lshlrev_b32_sdwa v64, 4, v230 dst_sel:DWORD dst_unused:UNUSED_PAD src0_sel:DWORD src1_sel:WORD_0\n" \
  "ds_read_b128 v[100:103], v64\n" \
  "v_lshlrev_b32_sdwa v65, 4, v230 dst_sel:DWORD dst_unused:UNUSED_PAD src0_sel:DWORD src1_sel:WORD_1\n" \
  "ds_read_b128 v[104:107], v65\n" \
  "s_mov_b32 m0, s58\n" \
  "s_add_u32 s35, s58, 0x8000\n" \
  "global_load_lds_dwordx4 v190, s[54:55] sc1\n" \
  "global_load_lds_dwordx4 v158, s[54:55] offset:1024 sc1\n" \
  "s_waitcnt lgkmcnt(10)\n" \
  "v_smfmac_f32_16x16x64_bf16 v[4:7], v[180:183], v[108:115], v192\n" \
  "v_lshlrev_b32_sdwa v66, 4, v231 dst_sel:DWORD dst_unused:UNUSED_PAD src0_sel:DWORD src1_sel:WORD_0\n" \
  "ds_read_b128 v[108:111], v66\n" \
  "v_lshlrev_b32_sdwa v67, 4, v231 dst_sel:DWORD dst_unused:UNUSED_PAD src0_sel:DWORD src1_sel:WORD_1\n" \
  "ds_read_b128 v[112:115], v67\n" \
  "s_waitcnt lgkmcnt(10)\n" \
  "v_smfmac_f32_16x16x64_bf16 v[8:11], v[180:183], v[68:75], v192\n" \
  "v_lshlrev_b32_sdwa v64, 4, v232 dst_sel:DWORD dst_unused:UNUSED_PAD src0_sel:DWORD src1_sel:WORD_0\n" \
  "ds_read_b128 v[68:71], v64\n" \
  "v_lshlrev_b32_sdwa v65, 4, v232 dst_sel:DWORD dst_unused:UNUSED_PAD src0_sel:DWORD src1_sel:WORD_1\n" \
  "ds_read_b128 v[72:75], v65\n" \
  "s_add_u32 m0, s58, 0x4000\n" \
  "s_cmp_lt_u32 s35, 0x28000\n" \
  "global_load_lds_dwordx4 v191, s[54:55] sc1\n" \
  "global_load_lds_dwordx4 v159, s[54:55] offset:1024 sc1\n" \
  "s_cselect_b32 s58, s35, s59\n" \
  "s_waitcnt lgkmcnt(10)\n" \
  "v_smfmac_f32_16x16x64_bf16 v[12:15], v[180:183], v[76:83], v192\n" \
  "v_lshlrev_b32_sdwa v66, 4, v233 dst_sel:DWORD dst_unused:UNUSED_PAD src0_sel:DWORD src1_sel:WORD_0\n" \
  "ds_read_b128 v[76:79], v66\n" \
  "v_lshlrev_b32_sdwa v67, 4, v233 dst_sel:DWORD dst_unused:UNUSED_PAD src0_sel:DWORD src1_sel:WORD_1\n" \
  "ds_read_b128 v[80:83], v67\n" \
  "s_waitcnt lgkmcnt(10)\n" \
  "v_smfmac_f32_16x16x64_bf16 v[16:19], v[180:183], v[84:91], v192\n" \
  "v_lshlrev_b32_sdwa v64, 4, v234 dst_sel:DWORD dst_unused:UNUSED_PAD src0_sel:DWORD src1_sel:WORD_0\n" \
  "ds_read_b128 v[84:87], v64\n" \
  "v_lshlrev_b32_sdwa v65, 4, v234 dst_sel:DWORD dst_unused:UNUSED_PAD src0_sel:DWORD src1_sel:WORD_1\n" \
  "ds_read_b128 v[88:91], v65\n" \
  "global_load_dwordx4 v[208:211], v188, s[56:57] sc1\n" \
  "global_load_dwordx4 v[212:215], v188, s[56:57] offset:1024 sc1\n" \
  "s_waitcnt lgkmcnt(10)\n" \
  "v_smfmac_f32_16x16x64_bf16 v[20:23], v[180:183], v[92:99], v192\n" \
  "v_lshlrev_b32_sdwa v66, 4, v235 dst_sel:DWORD dst_unused:UNUSED_PAD src0_sel:DWORD src1_sel:WORD_0\n" \
  "ds_read_b128 v[92:95], v66\n" \
  "v_lshlrev_b32_sdwa v67, 4, v235 dst_sel:DWORD dst_unused:UNUSED_PAD src0_sel:DWORD src1_sel:WORD_1\n" \
  "ds_read_b128 v[96:99], v67\n" \
  "s_waitcnt lgkmcnt(10)\n" \
  "v_smfmac_f32_16x16x64_bf16 v[24:27], v[180:183], v[100:107], v192\n" \
  "v_lshlrev_b32_sdwa v64, 4, v236 dst_sel:DWORD dst_unused:UNUSED_PAD src0_sel:DWORD src1_sel:WORD_0\n" \
  "ds_read_b128 v[100:103], v64\n" \
  "v_lshlrev_b32_sdwa v65, 4, v236 dst_sel:DWORD dst_unused:UNUSED_PAD src0_sel:DWORD src1_sel:WORD_1\n" \
  "ds_read_b128 v[104:107], v65\n" \
  "global_load_dwordx4 v[216:219], v188, s[56:57] offset:2048 sc1\n" \
  "global_load_dwordx4 v[220:223], v188, s[56:57] offset:3072 sc1\n" \
  "s_waitcnt lgkmcnt(10)\n" \
  "v_smfmac_f32_16x16x64_bf16 v[28:31], v[180:183], v[108:115], v192\n" \
  "v_lshlrev_b32_sdwa v66, 4, v237 dst_sel:DWORD dst_unused:UNUSED_PAD src0_sel:DWORD src1_sel:WORD_0\n" \
  "ds_read_b128 v[108:111], v66\n" \
  "v_lshlrev_b32_sdwa v67, 4, v237 dst_sel:DWORD dst_unused:UNUSED_PAD src0_sel:DWORD src1_sel:WORD_1\n" \
  "ds_read_b128 v[112:115], v67\n" \
  "s_waitcnt lgkmcnt(10)\n" \
  "v_smfmac_f32_16x16x64_bf16 v[32:35], v[180:183], v[68:75], v192\n" \
  "v_lshlrev_b32_sdwa v64, 4, v238 dst_sel:DWORD dst_unused:UNUSED_PAD src0_sel:DWORD src1_sel:WORD_0\n" \
  "ds_read_b128 v[68:71], v64\n" \
  "v_lshlrev_b32_sdwa v65, 4, v238 dst_sel:DWORD dst_unused:UNUSED_PAD src0_sel:DWORD src1_sel:WORD_1\n" \
  "ds_read_b128 v[72:75], v65\n" \
  "s_waitcnt lgkmcnt(10)\n" \
  "v_smfmac_f32_16x16x64_bf16 v[36:39], v[180:183], v[76:83], v192\n" \
  "v_lshlrev_b32_sdwa v66, 4, v239 dst_sel:DWORD dst_unused:UNUSED_PAD src0_sel:DWORD src1_sel:WORD_0\n" \
  "ds_read_b128 v[76:79], v66\n" \
  "v_lshlrev_b32_sdwa v67, 4, v239 dst_sel:DWORD dst_unused:UNUSED_PAD src0_sel:DWORD src1_sel:WORD_1\n" \
  "ds_read_b128 v[80:83], v67\n" \
  "s_add_u32 s56, s56, 32768\n" \
  "s_addc_u32 s57, s57, 0\n" \
  "s_add_u32 s54, s54, 64000\n" \
  "s_addc_u32 s55, s55, 0\n" \
  "s_waitcnt lgkmcnt(10)\n" \
  "v_smfmac_f32_16x16x64_bf16 v[40:43], v[180:183], v[84:91], v192\n" \
  "s_waitcnt lgkmcnt(8)\n" \
  "v_smfmac_f32_16x16x64_bf16 v[44:47], v[180:183], v[92:99], v192\n" \
  "s_waitcnt vmcnt(8)\n" \
  "s_barrier\n" \
  "v_lshlrev_b32_sdwa v64, 4, v240 dst_sel:DWORD dst_unused:UNUSED_PAD src0_sel:DWORD src1_sel:WORD_0\n" \
  "ds_read_b128 v[84:87], v64\n" \
  "v_lshlrev_b32_sdwa v65, 4, v240 dst_sel:DWORD dst_unused:UNUSED_PAD src0_sel:DWORD src1_sel:WORD_1\n" \
  "ds_read_b128 v[88:91], v65\n" \
  "v_lshlrev_b32_sdwa v66, 4, v241 dst_sel:DWORD dst_unused:UNUSED_PAD src0_sel:DWORD src1_sel:WORD_0\n" \
  "ds_read_b128 v[92:95], v66\n" \
  "v_lshlrev_b32_sdwa v67, 4, v241 dst_sel:DWORD dst_unused:UNUSED_PAD src0_sel:DWORD src1_sel:WORD_1\n" \
  "ds_read_b128 v[96:99], v67\n" \
  "s_waitcnt lgkmcnt(10)\n" \
  "v_smfmac_f32_16x16x64_bf16 v[48:51], v[180:183], v[100:107], v192\n" \
  "s_waitcnt lgkmcnt(8)\n" \
  "v_smfmac_f32_16x16x64_bf16 v[52:55], v[180:183], v[108:115], v192\n" \
  "s_waitcnt lgkmcnt(6)\n" \
  "v_smfmac_f32_16x16x64_bf16 v[56:59], v[180:183], v[68:75], v192\n" \
  "s_waitcnt lgkmcnt(4)\n" \
  "v_smfmac_f32_16x16x64_bf16 v[60:63], v[180:183], v[76:83], v192\n" \
  "s_barrier\n" \
  "s_mov_b32 m0, s58\n" \
  "s_add_u32 s35, s58, 0x8000\n" \
  "global_load_lds_dwordx4 v188, s[54:55] sc1\n" \
  "global_load_lds_dwordx4 v156, s[54:55] offset:1024 sc1\n" \
  "v_lshlrev_b32_sdwa v64, 4, v242 dst_sel:DWORD dst_unused:UNUSED_PAD src0_sel:DWORD src1_sel:WORD_0\n" \
  "ds_read_b128 v[100:103], v64\n" \
  "v_lshlrev_b32_sdwa v65, 4, v242 dst_sel:DWORD dst_unused:UNUSED_PAD src0_sel:DWORD src1_sel:WORD_1\n" \
  "ds_read_b128 v[104:107], v65\n" \
  "v_lshlrev_b32_sdwa v66, 4, v243 dst_sel:DWORD dst_unused:UNUSED_PAD src0_sel:DWORD src1_sel:WORD_0\n" \
  "ds_read_b128 v[108:111], v66\n" \
  "v_lshlrev_b32_sdwa v67, 4, v243 dst_sel:DWORD dst_unused:UNUSED_PAD src0_sel:DWORD src1_sel:WORD_1\n" \
  "ds_read_b128 v[112:115], v67\n" \
  "s_add_u32 m0, s58, 0x4000\n" \
  "s_cmp_lt_u32 s35, 0x28000\n" \
  "global_load_lds_dwordx4 v189, s[54:55] sc1\n" \
  "global_load_lds_dwordx4 v157, s[54:55] offset:1024 sc1\n" \
  "s_cselect_b32 s58, s35, s59\n" \
  "v_lshlrev_b32_sdwa v64, 4, v244 dst_sel:DWORD dst_unused:UNUSED_PAD src0_sel:DWORD src1_sel:WORD_0\n" \
  "ds_read_b128 v[68:71], v64\n" \
  "v_lshlrev_b32_sdwa v65, 4, v244 dst_sel:DWORD dst_unused:UNUSED_PAD src0_sel:DWORD src1_sel:WORD_1\n" \
  "ds_read_b128 v[72:75], v65\n" \
  "v_lshlrev_b32_sdwa v66, 4, v245 dst_sel:DWORD dst_unused:UNUSED_PAD src0_sel:DWORD src1_sel:WORD_0\n" \
  "ds_read_b128 v[76:79], v66\n" \
  "v_lshlrev_b32_sdwa v67, 4, v245 dst_sel:DWORD dst_unused:UNUSED_PAD src0_sel:DWORD src1_sel:WORD_1\n" \
  "ds_read_b128 v[80:83], v67\n" \
  "s_waitcnt lgkmcnt(10)\n" \
  "v_smfmac_f32_16x16x64_bf16 v[0:3], v[180:183], v[84:91], v192\n" \
  "v_lshlrev_b32_sdwa v64, 4, v246 dst_sel:DWORD dst_unused:UNUSED_PAD src0_sel:DWORD src1_sel:WORD_0\n" \
  "ds_read_b128 v[84:87], v64\n" \
  "v_lshlrev_b32_sdwa v65, 4, v246 dst_sel:DWORD dst_unused:UNUSED_PAD src0_sel:DWORD src1_sel:WORD_1\n" \
  "ds_read_b128 v[88:91], v65\n" \
  "s_mov_b32 m0, s58\n" \
  "s_add_u32 s35, s58, 0x8000\n" \
  "global_load_lds_dwordx4 v190, s[54:55] sc1\n" \
  "global_load_lds_dwordx4 v158, s[54:55] offset:1024 sc1\n" \
  "s_waitcnt lgkmcnt(10)\n" \
  "v_smfmac_f32_16x16x64_bf16 v[4:7], v[180:183], v[92:99], v192\n" \
  "v_lshlrev_b32_sdwa v66, 4, v247 dst_sel:DWORD dst_unused:UNUSED_PAD src0_sel:DWORD src1_sel:WORD_0\n" \
  "ds_read_b128 v[92:95], v66\n" \
  "v_lshlrev_b32_sdwa v67, 4, v247 dst_sel:DWORD dst_unused:UNUSED_PAD src0_sel:DWORD src1_sel:WORD_1\n" \
  "ds_read_b128 v[96:99], v67\n" \
  "s_waitcnt lgkmcnt(10)\n" \
  "v_smfmac_f32_16x16x64_bf16 v[8:11], v[180:183], v[100:107], v192\n" \
  "v_lshlrev_b32_sdwa v64, 4, v248 dst_sel:DWORD dst_unused:UNUSED_PAD src0_sel:DWORD src1_sel:WORD_0\n" \
  "ds_read_b128 v[100:103], v64\n" \
  "v_lshlrev_b32_sdwa v65, 4, v248 dst_sel:DWORD dst_unused:UNUSED_PAD src0_sel:DWORD src1_sel:WORD_1\n" \
  "ds_read_b128 v[104:107], v65\n" \
  "s_add_u32 m0, s58, 0x4000\n" \
  "s_cmp_lt_u32 s35, 0x28000\n" \
  "global_load_lds_dwordx4 v191, s[54:55] sc1\n" \
  "global_load_lds_dwordx4 v159, s[54:55] offset:1024 sc1\n" \
  "s_cselect_b32 s58, s35, s59\n" \
  "s_waitcnt lgkmcnt(10)\n" \
  "v_smfmac_f32_16x16x64_bf16 v[12:15], v[180:183], v[108:115], v192\n" \
  "v_lshlrev_b32_sdwa v66, 4, v249 dst_sel:DWORD dst_unused:UNUSED_PAD src0_sel:DWORD src1_sel:WORD_0\n" \
  "ds_read_b128 v[108:111], v66\n" \
  "v_lshlrev_b32_sdwa v67, 4, v249 dst_sel:DWORD dst_unused:UNUSED_PAD src0_sel:DWORD src1_sel:WORD_1\n" \
  "ds_read_b128 v[112:115], v67\n" \
  "s_waitcnt lgkmcnt(10)\n" \
  "v_smfmac_f32_16x16x64_bf16 v[16:19], v[180:183], v[68:75], v192\n" \
  "v_lshlrev_b32_sdwa v64, 4, v250 dst_sel:DWORD dst_unused:UNUSED_PAD src0_sel:DWORD src1_sel:WORD_0\n" \
  "ds_read_b128 v[68:71], v64\n" \
  "v_lshlrev_b32_sdwa v65, 4, v250 dst_sel:DWORD dst_unused:UNUSED_PAD src0_sel:DWORD src1_sel:WORD_1\n" \
  "ds_read_b128 v[72:75], v65\n" \
  "global_load_dwordx4 v[224:227], v188, s[56:57] sc1\n" \
  "global_load_dwordx4 v[228:231], v188, s[56:57] offset:1024 sc1\n" \
  "s_waitcnt lgkmcnt(10)\n" \
  "v_smfmac_f32_16x16x64_bf16 v[20:23], v[180:183], v[76:83], v192\n" \
  "v_lshlrev_b32_sdwa v66, 4, v251 dst_sel:DWORD dst_unused:UNUSED_PAD src0_sel:DWORD src1_sel:WORD_0\n" \
  "ds_read_b128 v[76:79], v66\n" \
  "v_lshlrev_b32_sdwa v67, 4, v251 dst_sel:DWORD dst_unused:UNUSED_PAD src0_sel:DWORD src1_sel:WORD_1\n" \
  "ds_read_b128 v[80:83], v67\n" \
  "s_waitcnt lgkmcnt(10)\n" \
  "v_smfmac_f32_16x16x64_bf16 v[24:27], v[180:183], v[84:91], v192\n" \
  "v_lshlrev_b32_sdwa v64, 4, v252 dst_sel:DWORD dst_unused:UNUSED_PAD src0_sel:DWORD src1_sel:WORD_0\n" \
  "ds_read_b128 v[84:87], v64\n" \
  "v_lshlrev_b32_sdwa v65, 4, v252 dst_sel:DWORD dst_unused:UNUSED_PAD src0_sel:DWORD src1_sel:WORD_1\n" \
  "ds_read_b128 v[88:91], v65\n" \
  "global_load_dwordx4 v[232:235], v188, s[56:57] offset:2048 sc1\n" \
  "global_load_dwordx4 v[236:239], v188, s[56:57] offset:3072 sc1\n" \
  "s_waitcnt lgkmcnt(10)\n" \
  "v_smfmac_f32_16x16x64_bf16 v[28:31], v[180:183], v[92:99], v192\n" \
  "v_lshlrev_b32_sdwa v66, 4, v253 dst_sel:DWORD dst_unused:UNUSED_PAD src0_sel:DWORD src1_sel:WORD_0\n" \
  "ds_read_b128 v[92:95], v66\n" \
  "v_lshlrev_b32_sdwa v67, 4, v253 dst_sel:DWORD dst_unused:UNUSED_PAD src0_sel:DWORD src1_sel:WORD_1\n" \
  "ds_read_b128 v[96:99], v67\n" \
  "s_waitcnt lgkmcnt(10)\n" \
  "v_smfmac_f32_16x16x64_bf16 v[32:35], v[180:183], v[100:107], v192\n" \
  "v_lshlrev_b32_sdwa v64, 4, v254 dst_sel:DWORD dst_unused:UNUSED_PAD src0_sel:DWORD src1_sel:WORD_0\n" \
  "ds_read_b128 v[100:103], v64\n" \
  "v_lshlrev_b32_sdwa v65, 4, v254 dst_sel:DWORD dst_unused:UNUSED_PAD src0_sel:DWORD src1_sel:WORD_1\n" \
  "ds_read_b128 v[104:107], v65\n" \
  "s_waitcnt lgkmcnt(10)\n" \
  "v_smfmac_f32_16x16x64_bf16 v[36:39], v[180:183], v[108:115], v192\n" \
  "v_lshlrev_b32_sdwa v66, 4, v255 dst_sel:DWORD dst_unused:UNUSED_PAD src0_sel:DWORD src1_sel:WORD_0\n" \
  "ds_read_b128 v[108:111], v66\n" \
  "v_lshlrev_b32_sdwa v67, 4, v255 dst_sel:DWORD dst_unused:UNUSED_PAD src0_sel:DWORD src1_sel:WORD_1\n" \
  "ds_read_b128 v[112:115], v67\n" \
  "s_add_u32 s56, s56, 32768\n" \
  "s_addc_u32 s57, s57, 0\n" \
  "s_add_u32 s54, s54, 64000\n" \
  "s_addc_u32 s55, s55, 0\n" \
  "s_lshl3_add_u32 s36, s27, s51\n" \
  "s_cmp_eq_u32 s36, 37\n" \
  "s_waitcnt lgkmcnt(10)\n" \
  "v_smfmac_f32_16x16x64_bf16 v[40:43], v[180:183], v[68:75], v192\n" \
  "s_waitcnt lgkmcnt(8)\n" \
  "v_smfmac_f32_16x16x64_bf16 v[44:47], v[180:183], v[76:83], v192\n" \
  "s_waitcnt vmcnt(8)\n" \
  "s_cbranch_scc1 LG_pol\n" \
  "LG_rt2:\n" \
  "s_barrier\n" \
  "v_lshlrev_b32_sdwa v64, 4, v208 dst_sel:DWORD dst_unused:UNUSED_PAD src0_sel:DWORD src1_sel:WORD_0\n" \
  "ds_read_b128 v[68:71], v64\n" \
  "v_lshlrev_b32_sdwa v65, 4, v208 dst_sel:DWORD dst_unused:UNUSED_PAD src0_sel:DWORD src1_sel:WORD_1\n" \
  "ds_read_b128 v[72:75], v65\n" \
  "v_lshlrev_b32_sdwa v66, 4, v209 dst_sel:DWORD dst_unused:UNUSED_PAD src0_sel:DWORD src1_sel:WORD_0\n" \
  "ds_read_b128 v[76:79], v66\n" \
  "v_lshlrev_b32_sdwa v67, 4, v209 dst_sel:DWORD dst_unused:UNUSED_PAD src0_sel:DWORD src1_sel:WORD_1\n" \
  "ds_read_b128 v[80:83], v67\n" \
  "s_waitcnt lgkmcnt(10)\n" \
  "v_smfmac_f32_16x16x64_bf16 v[48:51], v[180:183], v[84:91], v192\n" \
  "s_waitcnt lgkmcnt(8)\n" \
  "v_smfmac_f32_16x16x64_bf16 v[52:55], v[180:183], v[92:99], v192\n" \
  "s_waitcnt lgkmcnt(6)\n" \
  "v_smfmac_f32_16x16x64_bf16 v[56:59], v[180:183], v[100:107], v192\n" \
  "s_waitcnt lgkmcnt(4)\n" \
  "v_smfmac_f32_16x16x64_bf16 v[60:63], v[180:183], v[108:115], v192\n" \
  "s_barrier\n" \
  "s_mov_b32 m0, s58\n" \
  "s_add_u32 s35, s58, 0x8000\n" \
  "global_load_lds_dwordx4 v188, s[54:55] sc1\n" \
  "global_load_lds_dwordx4 v156, s[54:55] offset:1024 sc1\n" \
  "v_lshlrev_b32_sdwa v64, 4, v210 dst_sel:DWORD dst_unused:UNUSED_PAD src0_sel:DWORD src1_sel:WORD_0\n" \
  "ds_read_b128 v[84:87], v64\n" \
  "v_lshlrev_b32_sdwa v65, 4, v210 dst_sel:DWORD dst_unused:UNUSED_PAD src0_sel:DWORD src1_sel:WORD_1\n" \
  "ds_read_b128 v[88:91], v65\n" \
  "v_lshlrev_b32_sdwa v66, 4, v211 dst_sel:DWORD dst_unused:UNUSED_PAD src0_sel:DWORD src1_sel:WORD_0\n" \
  "ds_read_b128 v[92:95], v66\n" \
  "v_lshlrev_b32_sdwa v67, 4, v211 dst_sel:DWORD dst_unused:UNUSED_PAD src0_sel:DWORD src1_sel:WORD_1\n" \
  "ds_read_b128 v[96:99], v67\n" \
  "s_add_u32 m0, s58, 0x4000\n" \
  "s_cmp_lt_u32 s35, 0x28000\n" \
  "global_load_lds_dwordx4 v189, s[54:55] sc1\n" \
  "global_load_lds_dwordx4 v157, s[54:55] offset:1024 sc1\n" \
  "s_cselect_b32 s58, s35, s59\n" \
  "v_lshlrev_b32_sdwa v64, 4, v212 dst_sel:DWORD dst_unused:UNUSED_PAD src0_sel:DWORD src1_sel:WORD_0\n" \
  "ds_read_b128 v[100:103], v64\n" \
  "v_lshlrev_b32_sdwa v65, 4, v212 dst_sel:DWORD dst_unused:UNUSED_PAD src0_sel:DWORD src1_sel:WORD_1\n" \
  "ds_read_b128 v[104:107], v65\n" \
  "v_lshlrev_b32_sdwa v66, 4, v213 dst_sel:DWORD dst_unused:UNUSED_PAD src0_sel:DWORD src1_sel:WORD_0\n" \
  "ds_read_b128 v[108:111], v66\n" \
  "v_lshlrev_b32_sdwa v67, 4, v213 dst_sel:DWORD dst_unused:UNUSED_PAD src0_sel:DWORD src1_sel:WORD_1\n" \
  "ds_read_b128 v[112:115], v67\n" \
  "s_waitcnt lgkmcnt(10)\n" \
  "v_smfmac_f32_16x16x64_bf16 v[0:3], v[180:183], v[68:75], v192\n" \
  "v_lshlrev_b32_sdwa v64, 4, v214 dst_sel:DWORD dst_unused:UNUSED_PAD src0_sel:DWORD src1_sel:WORD_0\n" \
  "ds_read_b128 v[68:71], v64\n" \
  "v_lshlrev_b32_sdwa v65, 4, v214 dst_sel:DWORD dst_unused:UNUSED_PAD src0_sel:DWORD src1_sel:WORD_1\n" \
  "ds_read_b128 v[72:75], v65\n" \
  "s_mov_b32 m0, s58\n" \
  "s_add_u32 s35, s58, 0x8000\n" \
  "global_load_lds_dwordx4 v190, s[54:55] sc1\n" \
  "global_load_lds_dwordx4 v158, s[54:55] offset:1024 sc1\n" \
  "s_waitcnt lgkmcnt(10)\n" \
  "v_smfmac_f32_16x16x64_bf16 v[4:7], v[180:183], v[76:83], v192\n" \
  "v_lshlrev_b32_sdwa v66, 4, v215 dst_sel:DWORD dst_unused:UNUSED_PAD src0_sel:DWORD src1_sel:WORD_0\n" \
  "ds_read_b128 v[76:79], v66\n" \
  "v_lshlrev_b32_sdwa v67, 4, v215 dst_sel:DWORD dst_unused:UNUSED_PAD src0_sel:DWORD src1_sel:WORD_1\n" \
  "ds_read_b128 v[80:83], v67\n" \
  "s_waitcnt lgkmcnt(10)\n" \
  "v_smfmac_f32_16x16x64_bf16 v[8:11], v[180:183], v[84:91], v192\n" \
  "v_lshlrev_b32_sdwa v64, 4, v216 dst_sel:DWORD dst_unused:UNUSED_PAD src0_sel:DWORD src1_sel:WORD_0\n" \
  "ds_read_b128 v[84:87], v64\n" \
  "v_lshlrev_b32_sdwa v65, 4, v216 dst_sel:DWORD dst_unused:UNUSED_PAD src0_sel:DWORD src1_sel:WORD_1\n" \
  "ds_read_b128 v[88:91], v65\n" \
  "s_add_u32 m0, s58, 0x4000\n" \
  "s_cmp_lt_u32 s35, 0x28000\n" \
  "global_load_lds_dwordx4 v191, s[54:55] sc1\n" \
  "global_load_lds_dwordx4 v159, s[54:55] offset:1024 sc1\n" \
  "s_cselect_b32 s58, s35, s59\n" \
  "s_waitcnt lgkmcnt(10)\n" \
  "v_smfmac_f32_16x16x64_bf16 v[12:15], v[180:183], v[92:99], v192\n" \
  "v_lshlrev_b32_sdwa v66, 4, v217 dst_sel:DWORD dst_unused:UNUSED_PAD src0_sel:DWORD src1_sel:WORD_0\n" \
  "ds_read_b128 v[92:95], v66\n" \
  "v_lshlrev_b32_sdwa v67, 4, v217 dst_sel:DWORD dst_unused:UNUSED_PAD src0_sel:DWORD src1_sel:WORD_1\n" \
  "ds_read_b128 v[96:99], v67\n" \
  "s_waitcnt lgkmcnt(10)\n" \
  "v_smfmac_f32_16x16x64_bf16 v[16:19], v[180:183], v[100:107], v192\n" \
  "v_lshlrev_b32_sdwa v64, 4, v218 dst_sel:DWORD dst_unused:UNUSED_PAD src0_sel:DWORD src1_sel:WORD_0\n" \
  "ds_read_b128 v[100:103], v64\n" \
  "v_lshlrev_b32_sdwa v65, 4, v218 dst_sel:DWORD dst_unused:UNUSED_PAD src0_sel:DWORD src1_sel:WORD_1\n" \
  "ds_read_b128 v[104:107], v65\n" \
  "global_load_dwordx4 v[240:243], v188, s[56:57] sc1\n" \
  "global_load_dwordx4 v[244:247], v188, s[56:57] offset:1024 sc1\n" \
  "s_waitcnt lgkmcnt(10)\n" \
  "v_smfmac_f32_16x16x64_bf16 v[20:23], v[180:183], v[108:115], v192\n" \
  "v_lshlrev_b32_sdwa v66, 4, v219 dst_sel:DWORD dst_unused:UNUSED_PAD src0_sel:DWORD src1_sel:WORD_0\n" \
  "ds_read_b128 v[108:111], v66\n" \
  "v_lshlrev_b32_sdwa v67, 4, v219 dst_sel:DWORD dst_unused:UNUSED_PAD src0_sel:DWORD src1_sel:WORD_1\n" \
  "ds_read_b128 v[112:115], v67\n" \
  "s_waitcnt lgkmcnt(10)\n" \
  "v_smfmac_f32_16x16x64_bf16 v[24:27], v[180:183], v[68:75], v192\n" \
  "v_lshlrev_b32_sdwa v64, 4, v220 dst_sel:DWORD dst_unused:UNUSED_PAD src0_sel:DWORD src1_sel:WORD_0\n" \
  "ds_read_b128 v[68:71], v64\n" \
  "v_lshlrev_b32_sdwa v65, 4, v220 dst_sel:DWORD dst_unused:UNUSED_PAD src0_sel:DWORD src1_sel:WORD_1\n" \
  "ds_read_b128 v[72:75], v65\n" \
  "global_load_dwordx4 v[248:251], v188, s[56:57] offset:2048 sc1\n" \
  "global_load_dwordx4 v[252:255], v188, s[56:57] offset:3072 sc1\n" \
  "s_waitcnt lgkmcnt(10)\n" \
  "v_smfmac_f32_16x16x64_bf16 v[28:31], v[180:183], v[76:83], v192\n" \
  "v_lshlrev_b32_sdwa v66, 4, v221 dst_sel:DWORD dst_unused:UNUSED_PAD src0_sel:DWORD src1_sel:WORD_0\n" \
  "ds_read_b128 v[76:79], v66\n" \
  "v_lshlrev_b32_sdwa v67, 4, v221 dst_sel:DWORD dst_unused:UNUSED_PAD src0_sel:DWORD src1_sel:WORD_1\n" \
  "ds_read_b128 v[80:83], v67\n" \
  "s_waitcnt lgkmcnt(10)\n" \
  "v_smfmac_f32_16x16x64_bf16 v[32:35], v[180:183], v[84:91], v192\n" \
  "v_lshlrev_b32_sdwa v64, 4, v222 dst_sel:DWORD dst_unused:UNUSED_PAD src0_sel:DWORD src1_sel:WORD_0\n" \
  "ds_read_b128 v[84:87], v64\n" \
  "v_lshlrev_b32_sdwa v65, 4, v222 dst_sel:DWORD dst_unused:UNUSED_PAD src0_sel:DWORD src1_sel:WORD_1\n" \
  "ds_read_b128 v[88:91], v65\n" \
  "s_waitcnt lgkmcnt(10)\n" \
  "v_smfmac_f32_16x16x64_bf16 v[36:39], v[180:183], v[92:99], v192\n" \
  "v_lshlrev_b32_sdwa v66, 4, v223 dst_sel:DWORD dst_unused:UNUSED_PAD src0_sel:DWORD src1_sel:WORD_0\n" \
  "ds_read_b128 v[92:95], v66\n" \
  "v_lshlrev_b32_sdwa v67, 4, v223 dst_sel:DWORD dst_unused:UNUSED_PAD src0_sel:DWORD src1_sel:WORD_1\n" \
  "ds_read_b128 v[96:99], v67\n" \
  "s_add_u32 s56, s56, 32768\n" \
  "s_addc_u32 s57, s57, 0\n" \
  "s_add_u32 s54, s54, 64000\n" \
  "s_addc_u32 s55, s55, 0\n" \
  "s_sub_u32 s51, s51, 1\n" \
  "s_cmp_lg_u32 s51, 0\n" \
  "s_cbranch_scc1 LG_loop\n" \
  "s_waitcnt lgkmcnt(10)\n" \
  "v_smfmac_f32_16x16x64_bf16 v[40:43], v[180:183], v[100:107], v192\n" \
  "s_waitcnt lgkmcnt(8)\n" \
  "v_smfmac_f32_16x16x64_bf16 v[44:47], v[180:183], v[108:115], v192\n" \
  "s_waitcnt vmcnt(8)\n" \
  "s_barrier\n" \
  "v_lshlrev_b32_sdwa v64, 4, v224 dst_sel:DWORD dst_unused:UNUSED_PAD src0_sel:DWORD src1_sel:WORD_0\n" \
  "ds_read_b128 v[100:103], v64\n" \
  "v_lshlrev_b32_sdwa v65, 4, v224 dst_sel:DWORD dst_unused:UNUSED_PAD src0_sel:DWORD src1_sel:WORD_1\n" \
  "ds_read_b128 v[104:107], v65\n" \
  "v_lshlrev_b32_sdwa v66, 4, v225 dst_sel:DWORD dst_unused:UNUSED_PAD src0_sel:DWORD src1_sel:WORD_0\n" \
  "ds_read_b128 v[108:111], v66\n" \
  "v_lshlrev_b32_sdwa v67, 4, v225 dst_sel:DWORD dst_unused:UNUSED_PAD src0_sel:DWORD src1_sel:WORD_1\n" \
  "ds_read_b128 v[112:115], v67\n" \
  "s_waitcnt lgkmcnt(10)\n" \
  "v_smfmac_f32_16x16x64_bf16 v[48:51], v[180:183], v[68:75], v192\n" \
  "s_waitcnt lgkmcnt(8)\n" \
  "v_smfmac_f32_16x16x64_bf16 v[52:55], v[180:183], v[76:83], v192\n" \
  "s_waitcnt lgkmcnt(6)\n" \
  "v_smfmac_f32_16x16x64_bf16 v[56:59], v[180:183], v[84:91], v192\n" \
  "s_waitcnt lgkmcnt(4)\n" \
  "v_smfmac_f32_16x16x64_bf16 v[60:63], v[180:183], v[92:99], v192\n" \
  "s_barrier\n" \
  "s_mov_b32 m0, s58\n" \
  "s_add_u32 s35, s58, 0x8000\n" \
  "global_load_lds_dwordx4 v188, s[54:55] sc1\n" \
  "global_load_lds_dwordx4 v156, s[54:55] offset:1024 sc1\n" \
  "v_lshlrev_b32_sdwa v64, 4, v226 dst_sel:DWORD dst_unused:UNUSED_PAD src0_sel:DWORD src1_sel:WORD_0\n" \
  "ds_read_b128 v[68:71], v64\n" \
  "v_lshlrev_b32_sdwa v65, 4, v226 dst_sel:DWORD dst_unused:UNUSED_PAD src0_sel:DWORD src1_sel:WORD_1\n" \
  "ds_read_b128 v[72:75], v65\n" \
  "v_lshlrev_b32_sdwa v66, 4, v227 dst_sel:DWORD dst_unused:UNUSED_PAD src0_sel:DWORD src1_sel:WORD_0\n" \
  "ds_read_b128 v[76:79], v66\n" \
  "v_lshlrev_b32_sdwa v67, 4, v227 dst_sel:DWORD dst_unused:UNUSED_PAD src0_sel:DWORD src1_sel:WORD_1\n" \
  "ds_read_b128 v[80:83], v67\n" \
  "s_add_u32 m0, s58, 0x4000\n" \
  "s_cmp_lt_u32 s35, 0x28000\n" \
  "global_load_lds_dwordx4 v189, s[54:55] sc1\n" \
  "global_load_lds_dwordx4 v157, s[54:55] offset:1024 sc1\n" \
  "s_cselect_b32 s58, s35, s59\n" \
  "v_lshlrev_b32_sdwa v64, 4, v228 dst_sel:DWORD dst_unused:UNUSED_PAD src0_sel:DWORD src1_sel:WORD_0\n" \
  "ds_read_b128 v[84:87], v64\n" \
  "v_lshlrev_b32_sdwa v65, 4, v228 dst_sel:DWORD dst_unused:UNUSED_PAD src0_sel:DWORD src1_sel:WORD_1\n" \
  "ds_read_b128 v[88:91], v65\n" \
  "v_lshlrev_b32_sdwa v66, 4, v229 dst_sel:DWORD dst_unused:UNUSED_PAD src0_sel:DWORD src1_sel:WORD_0\n" \
  "ds_read_b128 v[92:95], v66\n" \
  "v_lshlrev_b32_sdwa v67, 4, v229 dst_sel:DWORD dst_unused:UNUSED_PAD src0_sel:DWORD src1_sel:WORD_1\n" \
  "ds_read_b128 v[96:99], v67\n" \
  "s_waitcnt lgkmcnt(10)\n" \
  "v_smfmac_f32_16x16x64_bf16 v[0:3], v[180:183], v[100:107], v192\n" \
  "v_lshlrev_b32_sdwa v64, 4, v230 dst_sel:DWORD dst_unused:UNUSED_PAD src0_sel:DWORD src1_sel:WORD_0\n" \
  "ds_read_b128 v[100:103], v64\n" \
  "v_lshlrev_b32_sdwa v65, 4, v230 dst_sel:DWORD dst_unused:UNUSED_PAD src0_sel:DWORD src1_sel:WORD_1\n" \
  "ds_read_b128 v[104:107], v65\n" \
  "s_mov_b32 m0, s58\n" \
  "s_add_u32 s35, s58, 0x8000\n" \
  "global_load_lds_dwordx4 v190, s[54:55] sc1\n" \
  "global_load_lds_dwordx4 v158, s[54:55] offset:1024 sc1\n" \
  "s_waitcnt lgkmcnt(10)\n" \
  "v_smfmac_f32_16x16x64_bf16 v[4:7], v[180:183], v[108:115], v192\n" \
  "v_lshlrev_b32_sdwa v66, 4, v231 dst_sel:DWORD dst_unused:UNUSED_PAD src0_sel:DWORD src1_sel:WORD_0\n" \
  "ds_read_b128 v[108:111], v66\n" \
  "v_lshlrev_b32_sdwa v67, 4, v231 dst_sel:DWORD dst_unused:UNUSED_PAD src0_sel:DWORD src1_sel:WORD_1\n" \
  "ds_read_b128 v[112:115], v67\n" \
  "s_waitcnt lgkmcnt(10)\n" \
  "v_smfmac_f32_16x16x64_bf16 v[8:11], v[180:183], v[68:75], v192\n" \
  "v_lshlrev_b32_sdwa v64, 4, v232 dst_sel:DWORD dst_unused:UNUSED_PAD src0_sel:DWORD src1_sel:WORD_0\n" \
  "ds_read_b128 v[68:71], v64\n" \
  "v_lshlrev_b32_sdwa v65, 4, v232 dst_sel:DWORD dst_unused:UNUSED_PAD src0_sel:DWORD src1_sel:WORD_1\n" \
  "ds_read_b128 v[72:75], v65\n" \
  "s_add_u32 m0, s58, 0x4000\n" \
  "s_cmp_lt_u32 s35, 0x28000\n" \
  "global_load_lds_dwordx4 v191, s[54:55] sc1\n" \
  "global_load_lds_dwordx4 v159, s[54:55] offset:1024 sc1\n" \
  "s_cselect_b32 s58, s35, s59\n" \
  "s_waitcnt lgkmcnt(10)\n" \
  "v_smfmac_f32_16x16x64_bf16 v[12:15], v[180:183], v[76:83], v192\n" \
  "v_lshlrev_b32_sdwa v66, 4, v233 dst_sel:DWORD dst_unused:UNUSED_PAD src0_sel:DWORD src1_sel:WORD_0\n" \
  "ds_read_b128 v[76:79], v66\n" \
  "v_lshlrev_b32_sdwa v67, 4, v233 dst_sel:DWORD dst_unused:UNUSED_PAD src0_sel:DWORD src1_sel:WORD_1\n" \
  "ds_read_b128 v[80:83], v67\n" \
  "s_waitcnt lgkmcnt(10)\n" \
  "v_smfmac_f32_16x16x64_bf16 v[16:19], v[180:183], v[84:91], v192\n" \
  "v_lshlrev_b32_sdwa v64, 4, v234 dst_sel:DWORD dst_unused:UNUSED_PAD src0_sel:DWORD src1_sel:WORD_0\n" \
  "ds_read_b128 v[84:87], v64\n" \
  "v_lshlrev_b32_sdwa v65, 4, v234 dst_sel:DWORD dst_unused:UNUSED_PAD src0_sel:DWORD src1_sel:WORD_1\n" \
  "ds_read_b128 v[88:91], v65\n" \
  "global_load_dwordx4 v[208:211], v188, s[56:57] sc1\n" \
  "global_load_dwordx4 v[212:215], v188, s[56:57] offset:1024 sc1\n" \
  "s_waitcnt lgkmcnt(10)\n" \
  "v_smfmac_f32_16x16x64_bf16 v[20:23], v[180:183], v[92:99], v192\n" \
  "v_lshlrev_b32_sdwa v66, 4, v235 dst_sel:DWORD dst_unused:UNUSED_PAD src0_sel:DWORD src1_sel:WORD_0\n" \
  "ds_read_b128 v[92:95], v66\n" \
  "v_lshlrev_b32_sdwa v67, 4, v235 dst_sel:DWORD dst_unused:UNUSED_PAD src0_sel:DWORD src1_sel:WORD_1\n" \
  "ds_read_b128 v[96:99], v67\n" \
  "s_waitcnt lgkmcnt(10)\n" \
  "v_smfmac_f32_16x16x64_bf16 v[24:27], v[180:183], v[100:107], v192\n" \
  "v_lshlrev_b32_sdwa v64, 4, v236 dst_sel:DWORD dst_unused:UNUSED_PAD src0_sel:DWORD src1_sel:WORD_0\n" \
  "ds_read_b128 v[100:103], v64\n" \
  "v_lshlrev_b32_sdwa v65, 4, v236 dst_sel:DWORD dst_unused:UNUSED_PAD src0_sel:DWORD src1_sel:WORD_1\n" \
  "ds_read_b128 v[104:107], v65\n" \
  "global_load_dwordx4 v[216:219], v188, s[56:57] offset:2048 sc1\n" \
  "global_load_dwordx4 v[220:223], v188, s[56:57] offset:3072 sc1\n" \
  "s_waitcnt lgkmcnt(10)\n" \
  "v_smfmac_f32_16x16x64_bf16 v[28:31], v[180:183], v[108:115], v192\n" \
  "v_lshlrev_b32_sdwa v66, 4, v237 dst_sel:DWORD dst_unused:UNUSED_PAD src0_sel:DWORD src1_sel:WORD_0\n" \
  "ds_read_b128 v[108:111], v66\n" \
  "v_lshlrev_b32_sdwa v67, 4, v237 dst_sel:DWORD dst_unused:UNUSED_PAD src0_sel:DWORD src1_sel:WORD_1\n" \
  "ds_read_b128 v[112:115], v67\n" \
  "s_waitcnt lgkmcnt(10)\n" \
  "v_smfmac_f32_16x16x64_bf16 v[32:35], v[180:183], v[68:75], v192\n" \
  "v_lshlrev_b32_sdwa v64, 4, v238 dst_sel:DWORD dst_unused:UNUSED_PAD src0_sel:DWORD src1_sel:WORD_0\n" \
  "ds_read_b128 v[68:71], v64\n" \
  "v_lshlrev_b32_sdwa v65, 4, v238 dst_sel:DWORD dst_unused:UNUSED_PAD src0_sel:DWORD src1_sel:WORD_1\n" \
  "ds_read_b128 v[72:75], v65\n" \
  "s_waitcnt lgkmcnt(10)\n" \
  "v_smfmac_f32_16x16x64_bf16 v[36:39], v[180:183], v[76:83], v192\n" \
  "v_lshlrev_b32_sdwa v66, 4, v239 dst_sel:DWORD dst_unused:UNUSED_PAD src0_sel:DWORD src1_sel:WORD_0\n" \
  "ds_read_b128 v[76:79], v66\n" \
  "v_lshlrev_b32_sdwa v67, 4, v239 dst_sel:DWORD dst_unused:UNUSED_PAD src0_sel:DWORD src1_sel:WORD_1\n" \
  "ds_read_b128 v[80:83], v67\n" \
  "s_add_u32 s56, s56, 32768\n" \
  "s_addc_u32 s57, s57, 0\n" \
  "s_add_u32 s54, s54, 64000\n" \
  "s_addc_u32 s55, s55, 0\n" \
  "s_waitcnt lgkmcnt(10)\n" \
  "v_smfmac_f32_16x16x64_bf16 v[40:43], v[180:183], v[84:91], v192\n" \
  "s_waitcnt lgkmcnt(8)\n" \
  "v_smfmac_f32_16x16x64_bf16 v[44:47], v[180:183], v[92:99], v192\n" \
  "s_waitcnt vmcnt(8)\n" \
  "s_barrier\n" \
  "v_lshlrev_b32_sdwa v64, 4, v240 dst_sel:DWORD dst_unused:UNUSED_PAD src0_sel:DWORD src1_sel:WORD_0\n" \
  "ds_read_b128 v[84:87], v64\n" \
  "v_lshlrev_b32_sdwa v65, 4, v240 dst_sel:DWORD dst_unused:UNUSED_PAD src0_sel:DWORD src1_sel:WORD_1\n" \
  "ds_read_b128 v[88:91], v65\n" \
  "v_lshlrev_b32_sdwa v66, 4, v241 dst_sel:DWORD dst_unused:UNUSED_PAD src0_sel:DWORD src1_sel:WORD_0\n" \
  "ds_read_b128 v[92:95], v66\n" \
  "v_lshlrev_b32_sdwa v67, 4, v241 dst_sel:DWORD dst_unused:UNUSED_PAD src0_sel:DWORD src1_sel:WORD_1\n" \
  "ds_read_b128 v[96:99], v67\n" \
  "s_waitcnt lgkmcnt(10)\n" \
  "v_smfmac_f32_16x16x64_bf16 v[48:51], v[180:183], v[100:107], v192\n" \
  "s_waitcnt lgkmcnt(8)\n" \
  "v_smfmac_f32_16x16x64_bf16 v[52:55], v[180:183], v[108:115], v192\n" \
  "s_waitcnt lgkmcnt(6)\n" \
  "v_smfmac_f32_16x16x64_bf16 v[56:59], v[180:183], v[68:75], v192\n" \
  "s_waitcnt lgkmcnt(4)\n" \
  "v_smfmac_f32_16x16x64_bf16 v[60:63], v[180:183], v[76:83], v192\n" \
  "s_barrier\n" \
  "s_mov_b32 m0, s58\n" \
  "s_add_u32 s35, s58, 0x8000\n" \
  "global_load_lds_dwordx4 v188, s[54:55] sc1\n" \
  "global_load_lds_dwordx4 v188, s[54:55] offset:1024 sc1\n" \
  "s_add_u32 m0, s58, 0x4000\n" \
  "s_cmp_lt_u32 s35, 0x28000\n" \
  "global_load_lds_dwordx4 v189, s[54:55] sc1\n" \
  "global_load_lds_dwordx4 v189, s[54:55] offset:1024 sc1\n" \
  "s_cselect_b32 s58, s35, s59\n" \
  "v_lshlrev_b32 v120, 16, v206\n" \
  "v_and_b32 v121, 0xffff0000, v206\n" \
  "v_lshlrev_b32 v122, 16, v207\n" \
  "v_and_b32 v123, 0xffff0000, v207\n" \
  "v_mov_b32 v124, 0xc0135761\n" \
  "v_mov_b32 v125, 0xc0135761\n" \
  "s_mov_b32 s52, 0xbdd2d3e8\n" \
  "s_mov_b32 s53, s52\n" \
  "s_lshl2_add_u32 s35, s33, s34\n" \
  "s_lshl_b32 s35, s35, 14\n" \
  "s_add_u32 s35, s35, s59\n" \
  "s_add_u32 s35, s35, 105676800\n" \
  "s_add_u32 s40, s22, s35\n" \
  "s_addc_u32 s41, s23, 0\n" \
  "v_lshlrev_b32_sdwa v64, 4, v242 dst_sel:DWORD dst_unused:UNUSED_PAD src0_sel:DWORD src1_sel:WORD_0\n" \
  "ds_read_b128 v[100:103], v64\n" \
  "v_lshlrev_b32_sdwa v65, 4, v242 dst_sel:DWORD dst_unused:UNUSED_PAD src0_sel:DWORD src1_sel:WORD_1\n" \
  "ds_read_b128 v[104:107], v65\n" \
  "v_lshlrev_b32_sdwa v66, 4, v243 dst_sel:DWORD dst_unused:UNUSED_PAD src0_sel:DWORD src1_sel:WORD_0\n" \
  "ds_read_b128 v[108:111], v66\n" \
  "v_lshlrev_b32_sdwa v67, 4, v243 dst_sel:DWORD dst_unused:UNUSED_PAD src0_sel:DWORD src1_sel:WORD_1\n" \
  "ds_read_b128 v[112:115], v67\n" \
  "v_lshlrev_b32_sdwa v64, 4, v244 dst_sel:DWORD dst_unused:UNUSED_PAD src0_sel:DWORD src1_sel:WORD_0\n" \
  "ds_read_b128 v[68:71], v64\n" \
  "v_lshlrev_b32_sdwa v65, 4, v244 dst_sel:DWORD dst_unused:UNUSED_PAD src0_sel:DWORD src1_sel:WORD_1\n" \
  "ds_read_b128 v[72:75], v65\n" \
  "v_lshlrev_b32_sdwa v66, 4, v245 dst_sel:DWORD dst_unused:UNUSED_PAD src0_sel:DWORD src1_sel:WORD_0\n" \
  "ds_read_b128 v[76:79], v66\n" \
  "v_lshlrev_b32_sdwa v67, 4, v245 dst_sel:DWORD dst_unused:UNUSED_PAD src0_sel:DWORD src1_sel:WORD_1\n" \
  "ds_read_b128 v[80:83], v67\n" \
  "s_waitcnt lgkmcnt(10)\n" \
  "v_smfmac_f32_16x16x64_bf16 v[0:3], v[180:183], v[84:91], v192\n" \
  "v_lshlrev_b32_sdwa v64, 4, v246 dst_sel:DWORD dst_unused:UNUSED_PAD src0_sel:DWORD src1_sel:WORD_0\n" \
  "ds_read_b128 v[84:87], v64\n" \
  "v_lshlrev_b32_sdwa v65, 4, v246 dst_sel:DWORD dst_unused:UNUSED_PAD src0_sel:DWORD src1_sel:WORD_1\n" \
  "ds_read_b128 v[88:91], v65\n" \
  "s_waitcnt lgkmcnt(10)\n" \
  "v_smfmac_f32_16x16x64_bf16 v[4:7], v[180:183], v[92:99], v192\n" \
  "v_lshlrev_b32_sdwa v66, 4, v247 dst_sel:DWORD dst_unused:UNUSED_PAD src0_sel:DWORD src1_sel:WORD_0\n" \
  "ds_read_b128 v[92:95], v66\n" \
  "v_lshlrev_b32_sdwa v67, 4, v247 dst_sel:DWORD dst_unused:UNUSED_PAD src0_sel:DWORD src1_sel:WORD_1\n" \
  "ds_read_b128 v[96:99], v67\n" \
  "s_waitcnt lgkmcnt(10)\n" \
  "v_smfmac_f32_16x16x64_bf16 v[8:11], v[180:183], v[100:107], v192\n" \
  "v_lshlrev_b32_sdwa v64, 4, v248 dst_sel:DWORD dst_unused:UNUSED_PAD src0_sel:DWORD src1_sel:WORD_0\n" \
  "ds_read_b128 v[100:103], v64\n" \
  "v_lshlrev_b32_sdwa v65, 4, v248 dst_sel:DWORD dst_unused:UNUSED_PAD src0_sel:DWORD src1_sel:WORD_1\n" \
  "ds_read_b128 v[104:107], v65\n" \
  "s_waitcnt lgkmcnt(10)\n" \
  "v_smfmac_f32_16x16x64_bf16 v[12:15], v[180:183], v[108:115], v192\n" \
  "v_lshlrev_b32_sdwa v66, 4, v249 dst_sel:DWORD dst_unused:UNUSED_PAD src0_sel:DWORD src1_sel:WORD_0\n" \
  "ds_read_b128 v[108:111], v66\n" \
  "v_lshlrev_b32_sdwa v67, 4, v249 dst_sel:DWORD dst_unused:UNUSED_PAD src0_sel:DWORD src1_sel:WORD_1\n" \
  "ds_read_b128 v[112:115], v67\n" \
  "s_waitcnt vmcnt(0)\n" \
  "s_barrier\n" \
  "s_waitcnt lgkmcnt(10)\n" \
  "v_smfmac_f32_16x16x64_bf16 v[16:19], v[180:183], v[68:75], v192\n" \
  "s_waitcnt lgkmcnt(8)\n" \
  "v_smfmac_f32_16x16x64_bf16 v[20:23], v[180:183], v[76:83], v192\n" \
  "s_waitcnt lgkmcnt(6)\n" \
  "v_smfmac_f32_16x16x64_bf16 v[24:27], v[180:183], v[84:91], v192\n" \
  "s_waitcnt lgkmcnt(4)\n" \
  "v_smfmac_f32_16x16x64_bf16 v[28:31], v[180:183], v[92:99], v192\n" \
  "v_lshlrev_b32_sdwa v64, 4, v208 dst_sel:DWORD dst_unused:UNUSED_PAD src0_sel:DWORD src1_sel:WORD_0\n" \
  "ds_read_b128 v[68:71], v64\n" \
  "v_lshlrev_b32_sdwa v65, 4, v208 dst_sel:DWORD dst_unused:UNUSED_PAD src0_sel:DWORD src1_sel:WORD_1\n" \
  "ds_read_b128 v[72:75], v65\n" \
  "s_waitcnt lgkmcnt(4)\n" \
  "v_smfmac_f32_16x16x64_bf16 v[32:35], v[180:183], v[100:107], v192\n" \
  "v_lshlrev_b32_sdwa v66, 4, v209 dst_sel:DWORD dst_unused:UNUSED_PAD src0_sel:DWORD src1_sel:WORD_0\n" \
  "ds_read_b128 v[76:79], v66\n" \
  "v_lshlrev_b32_sdwa v67, 4, v209 dst_sel:DWORD dst_unused:UNUSED_PAD src0_sel:DWORD src1_sel:WORD_1\n" \
  "ds_read_b128 v[80:83], v67\n" \
  "s_waitcnt lgkmcnt(4)\n" \
  "v_smfmac_f32_16x16x64_bf16 v[36:39], v[180:183], v[108:115], v192\n" \
  "v_lshlrev_b32_sdwa v64, 4, v210 dst_sel:DWORD dst_unused:UNUSED_PAD src0_sel:DWORD src1_sel:WORD_0\n" \
  "ds_read_b128 v[84:87], v64\n" \
  "v_lshlrev_b32_sdwa v65, 4, v210 dst_sel:DWORD dst_unused:UNUSED_PAD src0_sel:DWORD src1_sel:WORD_1\n" \
  "ds_read_b128 v[88:91], v65\n" \
  "s_waitcnt lgkmcnt(4)\n" \
  "v_smfmac_f32_16x16x64_bf16 v[0:3], v[180:183], v[68:75], v192\n" \
  "v_lshlrev_b32_sdwa v66, 4, v211 dst_sel:DWORD dst_unused:UNUSED_PAD src0_sel:DWORD src1_sel:WORD_0\n" \
  "ds_read_b128 v[92:95], v66\n" \
  "v_lshlrev_b32_sdwa v67, 4, v211 dst_sel:DWORD dst_unused:UNUSED_PAD src0_sel:DWORD src1_sel:WORD_1\n" \
  "ds_read_b128 v[96:99], v67\n" \
  "s_waitcnt lgkmcnt(4)\n" \
  "v_smfmac_f32_16x16x64_bf16 v[4:7], v[180:183], v[76:83], v192\n" \
  "v_lshlrev_b32_sdwa v64, 4, v212 dst_sel:DWORD dst_unused:UNUSED_PAD src0_sel:DWORD src1_sel:WORD_0\n" \
  "ds_read_b128 v[100:103], v64\n" \
  "v_lshlrev_b32_sdwa v65, 4, v212 dst_sel:DWORD dst_unused:UNUSED_PAD src0_sel:DWORD src1_sel:WORD_1\n" \
  "ds_read_b128 v[104:107], v65\n" \
  "v_pk_mul_f32 v[128:129], v[0:1], v[0:1]\n" \
  "v_pk_mul_f32 v[130:131], v[2:3], v[2:3]\n" \
  "v_pk_fma_f32 v[128:129], v[128:129], s[52:53], v[124:125]\n" \
  "v_pk_fma_f32 v[130:131], v[130:131], s[52:53], v[124:125]\n" \
  "v_pk_mul_f32 v[128:129], v[128:129], v[0:1]\n" \
  "v_pk_mul_f32 v[130:131], v[130:131], v[2:3]\n" \
  "v_exp_f32 v128, v128\n" \
  "v_exp_f32 v129, v129\n" \
  "v_pk_mul_f32 v[0:1], v[0:1], v[120:121]\n" \
  "v_exp_f32 v130, v130\n" \
  "v_exp_f32 v131, v131\n" \
  "v_pk_mul_f32 v[2:3], v[2:3], v[122:123]\n" \
  "v_pk_add_f32 v[128:129], v[128:129], 1.0 op_sel_hi:[1,0]\n" \
  "v_pk_add_f32 v[130:131], v[130:131], 1.0 op_sel_hi:[1,0]\n" \
  "v_rcp_f32 v128, v128\n" \
  "v_rcp_f32 v129, v129\n" \
  "s_waitcnt lgkmcnt(4)\n" \
  "v_smfmac_f32_16x16x64_bf16 v[8:11], v[180:183], v[84:91], v192\n" \
  "v_lshlrev_b32_sdwa v66, 4, v213 dst_sel:DWORD dst_unused:UNUSED_PAD src0_sel:DWORD src1_sel:WORD_0\n" \
  "ds_read_b128 v[108:111], v66\n" \
  "v_lshlrev_b32_sdwa v67, 4, v213 dst_sel:DWORD dst_unused:UNUSED_PAD src0_sel:DWORD src1_sel:WORD_1\n" \
  "ds_read_b128 v[112:115], v67\n" \
  "v_rcp_f32 v130, v130\n" \
  "v_rcp_f32 v131, v131\n" \
  "v_pk_mul_f32 v[0:1], v[0:1], v[128:129]\n" \
  "v_pk_fma_f32 v[0:1], v[2:3], v[130:131], v[0:1]\n" \
  "v_add_f32 v0, v0, v1\n" \
  "v_pk_mul_f32 v[132:133], v[4:5], v[4:5]\n" \
  "v_pk_mul_f32 v[134:135], v[6:7], v[6:7]\n" \
  "v_pk_fma_f32 v[132:133], v[132:133], s[52:53], v[124:125]\n" \
  "v_pk_fma_f32 v[134:135], v[134:135], s[52:53], v[124:125]\n" \
  "v_pk_mul_f32 v[132:133], v[132:133], v[4:5]\n" \
  "v_pk_mul_f32 v[134:135], v[134:135], v[6:7]\n" \
  "v_exp_f32 v132, v132\n" \
  "v_exp_f32 v133, v133\n" \
  "v_pk_mul_f32 v[4:5], v[4:5], v[120:121]\n" \
  "v_exp_f32 v134, v134\n" \
  "v_exp_f32 v135, v135\n" \
  "s_waitcnt lgkmcnt(4)\n" \
  "v_smfmac_f32_16x16x64_bf16 v[12:15], v[180:183], v[92:99], v192\n" \
  "v_lshlrev_b32_sdwa v64, 4, v214 dst_sel:DWORD dst_unused:UNUSED_PAD src0_sel:DWORD src1_sel:WORD_0\n" \
  "ds_read_b128 v[68:71], v64\n" \
  "v_lshlrev_b32_sdwa v65, 4, v214 dst_sel:DWORD dst_unused:UNUSED_PAD src0_sel:DWORD src1_sel:WORD_1\n" \
  "ds_read_b128 v[72:75], v65\n" \
  "v_pk_mul_f32 v[6:7], v[6:7], v[122:123]\n" \
  "v_pk_add_f32 v[132:133], v[132:133], 1.0 op_sel_hi:[1,0]\n" \
  "v_pk_add_f32 v[134:135], v[134:135], 1.0 op_sel_hi:[1,0]\n" \
  "v_rcp_f32 v132, v132\n" \
  "v_rcp_f32 v133, v133\n" \
  "v_rcp_f32 v134, v134\n" \
  "v_rcp_f32 v135, v135\n" \
  "v_pk_mul_f32 v[4:5], v[4:5], v[132:133]\n" \
  "v_pk_fma_f32 v[4:5], v[6:7], v[134:135], v[4:5]\n" \
  "v_add_f32 v4, v4, v5\n" \
  "v_pk_mul_f32 v[136:137], v[8:9], v[8:9]\n" \
  "v_pk_mul_f32 v[138:139], v[10:11], v[10:11]\n" \
  "v_pk_fma_f32 v[136:137], v[136:137], s[52:53], v[124:125]\n" \
  "v_pk_fma_f32 v[138:139], v[138:139], s[52:53], v[124:125]\n" \
  "v_pk_mul_f32 v[136:137], v[136:137], v[8:9]\n" \
  "v_pk_mul_f32 v[138:139], v[138:139], v[10:11]\n" \
  "s_waitcnt lgkmcnt(4)\n" \
  "v_smfmac_f32_16x16x64_bf16 v[16:19], v[180:183], v[100:107], v192\n" \
  "v_lshlrev_b32_sdwa v66, 4, v215 dst_sel:DWORD dst_unused:UNUSED_PAD src0_sel:DWORD src1_sel:WORD_0\n" \
  "ds_read_b128 v[76:79], v66\n" \
  "v_lshlrev_b32_sdwa v67, 4, v215 dst_sel:DWORD dst_unused:UNUSED_PAD src0_sel:DWORD src1_sel:WORD_1\n" \
  "ds_read_b128 v[80:83], v67\n" \
  "v_exp_f32 v136, v136\n" \
  "v_exp_f32 v137, v137\n" \
  "v_pk_mul_f32 v[8:9], v[8:9], v[120:121]\n" \
  "v_exp_f32 v138, v138\n" \
  "v_exp_f32 v139, v139\n" \
  "v_pk_mul_f32 v[10:11], v[10:11], v[122:123]\n" \
  "v_pk_add_f32 v[136:137], v[136:137], 1.0 op_sel_hi:[1,0]\n" \
  "v_pk_add_f32 v[138:139], v[138:139], 1.0 op_sel_hi:[1,0]\n" \
  "v_rcp_f32 v136, v136\n" \
  "v_rcp_f32 v137, v137\n" \
  "v_rcp_f32 v138, v138\n" \
  "v_rcp_f32 v139, v139\n" \
  "v_pk_mul_f32 v[8:9], v[8:9], v[136:137]\n" \
  "v_pk_fma_f32 v[8:9], v[10:11], v[138:139], v[8:9]\n" \
  "v_add_f32 v8, v8, v9\n" \
  "v_pk_mul_f32 v[140:141], v[12:13], v[12:13]\n" \
  "s_waitcnt lgkmcnt(4)\n" \
  "v_smfmac_f32_16x16x64_bf16 v[20:23], v[180:183], v[108:115], v192\n" \
  "v_lshlrev_b32_sdwa v64, 4, v216 dst_sel:DWORD dst_unused:UNUSED_PAD src0_sel:DWORD src1_sel:WORD_0\n" \
  "ds_read_b128 v[84:87], v64\n" \
  "v_lshlrev_b32_sdwa v65, 4, v216 dst_sel:DWORD dst_unused:UNUSED_PAD src0_sel:DWORD src1_sel:WORD_1\n" \
  "ds_read_b128 v[88:91], v65\n" \
  "v_pk_mul_f32 v[142:143], v[14:15], v[14:15]\n" \
  "v_pk_fma_f32 v[140:141], v[140:141], s[52:53], v[124:125]\n" \
  "v_pk_fma_f32 v[142:143], v[142:143], s[52:53], v[124:125]\n" \
  "v_pk_mul_f32 v[140:141], v[140:141], v[12:13]\n" \
  "v_pk_mul_f32 v[142:143], v[142:143], v[14:15]\n" \
  "v_exp_f32 v140, v140\n" \
  "v_exp_f32 v141, v141\n" \
  "v_pk_mul_f32 v[12:13], v[12:13], v[120:121]\n" \
  "v_exp_f32 v142, v142\n" \
  "v_exp_f32 v143, v143\n" \
  "v_pk_mul_f32 v[14:15], v[14:15], v[122:123]\n" \
  "v_pk_add_f32 v[140:141], v[140:141], 1.0 op_sel_hi:[1,0]\n" \
  "v_pk_add_f32 v[142:143], v[142:143], 1.0 op_sel_hi:[1,0]\n" \
  "v_rcp_f32 v140, v140\n" \
  "v_rcp_f32 v141, v141\n" \
  "v_rcp_f32 v142, v142\n" \
  "s_waitcnt lgkmcnt(4)\n" \
  "v_smfmac_f32_16x16x64_bf16 v[24:27], v[180:183], v[68:75], v192\n" \
  "v_lshlrev_b32_sdwa v66, 4, v217 dst_sel:DWORD dst_unused:UNUSED_PAD src0_sel:DWORD src1_sel:WORD_0\n" \
  "ds_read_b128 v[92:95], v66\n" \
  "v_lshlrev_b32_sdwa v67, 4, v217 dst_sel:DWORD dst_unused:UNUSED_PAD src0_sel:DWORD src1_sel:WORD_1\n" \
  "ds_read_b128 v[96:99], v67\n" \
  "v_rcp_f32 v143, v143\n" \
  "v_pk_mul_f32 v[12:13], v[12:13], v[140:141]\n" \
  "v_pk_fma_f32 v[12:13], v[14:15], v[142:143], v[12:13]\n" \
  "v_add_f32 v12, v12, v13\n" \
  "v_pk_mul_f32 v[128:129], v[16:17], v[16:17]\n" \
  "v_pk_mul_f32 v[130:131], v[18:19], v[18:19]\n" \
  "v_pk_fma_f32 v[128:129], v[128:129], s[52:53], v[124:125]\n" \
  "v_pk_fma_f32 v[130:131], v[130:131], s[52:53], v[124:125]\n" \
  "v_pk_mul_f32 v[128:129], v[128:129], v[16:17]\n" \
  "v_pk_mul_f32 v[130:131], v[130:131], v[18:19]\n" \
  "v_exp_f32 v128, v128\n" \
  "v_exp_f32 v129, v129\n" \
  "v_pk_mul_f32 v[16:17], v[16:17], v[120:121]\n" \
  "v_exp_f32 v130, v130\n" \
  "v_exp_f32 v131, v131\n" \
  "v_pk_mul_f32 v[18:19], v[18:19], v[122:123]\n" \
  "s_waitcnt lgkmcnt(4)\n" \
  "v_smfmac_f32_16x16x64_bf16 v[28:31], v[180:183], v[76:83], v192\n" \
  "v_lshlrev_b32_sdwa v64, 4, v250 dst_sel:DWORD dst_unused:UNUSED_PAD src0_sel:DWORD src1_sel:WORD_0\n" \
  "ds_read_b128 v[100:103], v64\n" \
  "v_lshlrev_b32_sdwa v65, 4, v250 dst_sel:DWORD dst_unused:UNUSED_PAD src0_sel:DWORD src1_sel:WORD_1\n" \
  "ds_read_b128 v[104:107], v65\n" \
  "v_pk_add_f32 v[128:129], v[128:129], 1.0 op_sel_hi:[1,0]\n" \
  "v_pk_add_f32 v[130:131], v[130:131], 1.0 op_sel_hi:[1,0]\n" \
  "v_rcp_f32 v128, v128\n" \
  "v_rcp_f32 v129, v129\n" \
  "v_rcp_f32 v130, v130\n" \
  "v_rcp_f32 v131, v131\n" \
  "v_pk_mul_f32 v[16:17], v[16:17], v[128:129]\n" \
  "v_pk_fma_f32 v[16:17], v[18:19], v[130:131], v[16:17]\n" \
  "v_add_f32 v16, v16, v17\n" \
  "v_pk_mul_f32 v[132:133], v[20:21], v[20:21]\n" \
  "v_pk_mul_f32 v[134:135], v[22:23], v[22:23]\n" \
  "v_pk_fma_f32 v[132:133], v[132:133], s[52:53], v[124:125]\n" \
  "v_pk_fma_f32 v[134:135], v[134:135], s[52:53], v[124:125]\n" \
  "v_pk_mul_f32 v[132:133], v[132:133], v[20:21]\n" \
  "v_pk_mul_f32 v[134:135], v[134:135], v[22:23]\n" \
  "v_exp_f32 v132, v132\n" \
  "s_waitcnt lgkmcnt(4)\n" \
  "v_smfmac_f32_16x16x64_bf16 v[32:35], v[180:183], v[84:91], v192\n" \
  "v_lshlrev_b32_sdwa v66, 4, v218 dst_sel:DWORD dst_unused:UNUSED_PAD src0_sel:DWORD src1_sel:WORD_0\n" \
  "ds_read_b128 v[108:111], v66\n" \
  "v_lshlrev_b32_sdwa v67, 4, v218 dst_sel:DWORD dst_unused:UNUSED_PAD src0_sel:DWORD src1_sel:WORD_1\n" \
  "ds_read_b128 v[112:115], v67\n" \
  "v_exp_f32 v133, v133\n" \
  "v_pk_mul_f32 v[20:21], v[20:21], v[120:121]\n" \
  "v_exp_f32 v134, v134\n" \
  "v_exp_f32 v135, v135\n" \
  "v_pk_mul_f32 v[22:23], v[22:23], v[122:123]\n" \
  "v_pk_add_f32 v[132:133], v[132:133], 1.0 op_sel_hi:[1,0]\n" \
  "v_pk_add_f32 v[134:135], v[134:135], 1.0 op_sel_hi:[1,0]\n" \
  "v_rcp_f32 v132, v132\n" \
  "v_rcp_f32 v133, v133\n" \
  "v_rcp_f32 v134, v134\n" \
  "v_rcp_f32 v135, v135\n" \
  "v_pk_mul_f32 v[20:21], v[20:21], v[132:133]\n" \
  "v_pk_fma_f32 v[20:21], v[22:23], v[134:135], v[20:21]\n" \
  "v_add_f32 v20, v20, v21\n" \
  "v_pk_mul_f32 v[136:137], v[24:25], v[24:25]\n" \
  "v_pk_mul_f32 v[138:139], v[26:27], v[26:27]\n" \
  "s_waitcnt lgkmcnt(4)\n" \
  "v_smfmac_f32_16x16x64_bf16 v[36:39], v[180:183], v[92:99], v192\n" \
  "v_lshlrev_b32_sdwa v64, 4, v251 dst_sel:DWORD dst_unused:UNUSED_PAD src0_sel:DWORD src1_sel:WORD_0\n" \
  "ds_read_b128 v[68:71], v64\n" \
  "v_lshlrev_b32_sdwa v65, 4, v251 dst_sel:DWORD dst_unused:UNUSED_PAD src0_sel:DWORD src1_sel:WORD_1\n" \
  "ds_read_b128 v[72:75], v65\n" \
  "v_pk_fma_f32 v[136:137], v[136:137], s[52:53], v[124:125]\n" \
  "v_pk_fma_f32 v[138:139], v[138:139], s[52:53], v[124:125]\n" \
  "v_pk_mul_f32 v[136:137], v[136:137], v[24:25]\n" \
  "v_pk_mul_f32 v[138:139], v[138:139], v[26:27]\n" \
  "v_exp_f32 v136, v136\n" \
  "v_exp_f32 v137, v137\n" \
  "v_pk_mul_f32 v[24:25], v[24:25], v[120:121]\n" \
  "v_exp_f32 v138, v138\n" \
  "v_exp_f32 v139, v139\n" \
  "v_pk_mul_f32 v[26:27], v[26:27], v[122:123]\n" \
  "v_pk_add_f32 v[136:137], v[136:137], 1.0 op_sel_hi:[1,0]\n" \
  "v_pk_add_f32 v[138:139], v[138:139], 1.0 op_sel_hi:[1,0]\n" \
  "v_rcp_f32 v136, v136\n" \
  "v_rcp_f32 v137, v137\n" \
  "v_rcp_f32 v138, v138\n" \
  "v_rcp_f32 v139, v139\n" \
  "s_waitcnt lgkmcnt(4)\n" \
  "v_smfmac_f32_16x16x64_bf16 v[40:43], v[180:183], v[100:107], v192\n" \
  "v_lshlrev_b32_sdwa v66, 4, v219 dst_sel:DWORD dst_unused:UNUSED_PAD src0_sel:DWORD src1_sel:WORD_0\n" \
  "ds_read_b128 v[76:79], v66\n" \
  "v_lshlrev_b32_sdwa v67, 4, v219 dst_sel:DWORD dst_unused:UNUSED_PAD src0_sel:DWORD src1_sel:WORD_1\n" \
  "ds_read_b128 v[80:83], v67\n" \
  "v_pk_mul_f32 v[24:25], v[24:25], v[136:137]\n" \
  "v_pk_fma_f32 v[24:25], v[26:27], v[138:139], v[24:25]\n" \
  "v_add_f32 v24, v24, v25\n" \
  "v_pk_mul_f32 v[140:141], v[28:29], v[28:29]\n" \
  "v_pk_mul_f32 v[142:143], v[30:31], v[30:31]\n" \
  "v_pk_fma_f32 v[140:141], v[140:141], s[52:53], v[124:125]\n" \
  "v_pk_fma_f32 v[142:143], v[142:143], s[52:53], v[124:125]\n" \
  "v_pk_mul_f32 v[140:141], v[140:141], v[28:29]\n" \
  "v_pk_mul_f32 v[142:143], v[142:143], v[30:31]\n" \
  "v_exp_f32 v140, v140\n" \
  "v_exp_f32 v141, v141\n" \
  "v_pk_mul_f32 v[28:29], v[28:29], v[120:121]\n" \
  "v_exp_f32 v142, v142\n" \
  "v_exp_f32 v143, v143\n" \
  "v_pk_mul_f32 v[30:31], v[30:31], v[122:123]\n" \
  "v_pk_add_f32 v[140:141], v[140:141], 1.0 op_sel_hi:[1,0]\n" \
  "s_waitcnt lgkmcnt(4)\n" \
  "v_smfmac_f32_16x16x64_bf16 v[40:43], v[180:183], v[108:115], v192\n" \
  "v_lshlrev_b32_sdwa v64, 4, v252 dst_sel:DWORD dst_unused:UNUSED_PAD src0_sel:DWORD src1_sel:WORD_0\n" \
  "ds_read_b128 v[84:87], v64\n" \
  "v_lshlrev_b32_sdwa v65, 4, v252 dst_sel:DWORD dst_unused:UNUSED_PAD src0_sel:DWORD src1_sel:WORD_1\n" \
  "ds_read_b128 v[88:91], v65\n" \
  "v_pk_add_f32 v[142:143], v[142:143], 1.0 op_sel_hi:[1,0]\n" \
  "v_rcp_f32 v140, v140\n" \
  "v_rcp_f32 v141, v141\n" \
  "v_rcp_f32 v142, v142\n" \
  "v_rcp_f32 v143, v143\n" \
  "v_pk_mul_f32 v[28:29], v[28:29], v[140:141]\n" \
  "v_pk_fma_f32 v[28:29], v[30:31], v[142:143], v[28:29]\n" \
  "v_add_f32 v28, v28, v29\n" \
  "v_permlane16_swap_b32 v0, v16\n" \
  "v_permlane16_swap_b32 v4, v20\n" \
  "v_permlane16_swap_b32 v8, v24\n" \
  "v_permlane16_swap_b32 v12, v28\n" \
  "v_add_f32 v144, v0, v16\n" \
  "v_add_f32 v145, v4, v20\n" \
  "v_add_f32 v146, v8, v24\n" \
  "v_add_f32 v147, v12, v28\n" \
  "s_waitcnt lgkmcnt(4)\n" \
  "v_smfmac_f32_16x16x64_bf16 v[44:47], v[180:183], v[68:75], v192\n" \
  "v_lshlrev_b32_sdwa v66, 4, v220 dst_sel:DWORD dst_unused:UNUSED_PAD src0_sel:DWORD src1_sel:WORD_0\n" \
  "ds_read_b128 v[92:95], v66\n" \
  "v_lshlrev_b32_sdwa v67, 4, v220 dst_sel:DWORD dst_unused:UNUSED_PAD src0_sel:DWORD src1_sel:WORD_1\n" \
  "ds_read_b128 v[96:99], v67\n" \
  "v_min_u32 v144, -2, v144\n" \
  "v_min_u32 v145, -2, v145\n" \
  "v_min_u32 v146, -2, v146\n" \
  "v_min_u32 v147, -2, v147\n" \
  "global_store_dwordx4 v188, v[144:147], s[40:41] offset:0 sc1\n" \
  "v_pk_mul_f32 v[128:129], v[32:33], v[32:33]\n" \
  "v_pk_mul_f32 v[130:131], v[34:35], v[34:35]\n" \
  "v_pk_fma_f32 v[128:129], v[128:129], s[52:53], v[124:125]\n" \
  "v_pk_fma_f32 v[130:131], v[130:131], s[52:53], v[124:125]\n" \
  "v_pk_mul_f32 v[128:129], v[128:129], v[32:33]\n" \
  "v_pk_mul_f32 v[130:131], v[130:131], v[34:35]\n" \
  "v_exp_f32 v128, v128\n" \
  "v_exp_f32 v129, v129\n" \
  "v_pk_mul_f32 v[32:33], v[32:33], v[120:121]\n" \
  "v_exp_f32 v130, v130\n" \
  "v_exp_f32 v131, v131\n" \
  "s_waitcnt lgkmcnt(4)\n" \
  "v_smfmac_f32_16x16x64_bf16 v[44:47], v[180:183], v[76:83], v192\n" \
  "v_lshlrev_b32_sdwa v64, 4, v253 dst_sel:DWORD dst_unused:UNUSED_PAD src0_sel:DWORD src1_sel:WORD_0\n" \
  "ds_read_b128 v[100:103], v64\n" \
  "v_lshlrev_b32_sdwa v65, 4, v253 dst_sel:DWORD dst_unused:UNUSED_PAD src0_sel:DWORD src1_sel:WORD_1\n" \
  "ds_read_b128 v[104:107], v65\n" \
  "v_pk_mul_f32 v[34:35], v[34:35], v[122:123]\n" \
  "v_pk_add_f32 v[128:129], v[128:129], 1.0 op_sel_hi:[1,0]\n" \
  "v_pk_add_f32 v[130:131], v[130:131], 1.0 op_sel_hi:[1,0]\n" \
  "v_rcp_f32 v128, v128\n" \
  "v_rcp_f32 v129, v129\n" \
  "v_rcp_f32 v130, v130\n" \
  "v_rcp_f32 v131, v131\n" \
  "v_pk_mul_f32 v[32:33], v[32:33], v[128:129]\n" \
  "v_pk_fma_f32 v[32:33], v[34:35], v[130:131], v[32:33]\n" \
  "v_add_f32 v32, v32, v33\n" \
  "v_pk_mul_f32 v[132:133], v[36:37], v[36:37]\n" \
  "v_pk_mul_f32 v[134:135], v[38:39], v[38:39]\n" \
  "v_pk_fma_f32 v[132:133], v[132:133], s[52:53], v[124:125]\n" \
  "v_pk_fma_f32 v[134:135], v[134:135], s[52:53], v[124:125]\n" \
  "v_pk_mul_f32 v[132:133], v[132:133], v[36:37]\n" \
  "v_pk_mul_f32 v[134:135], v[134:135], v[38:39]\n" \
  "s_waitcnt lgkmcnt(4)\n" \
  "v_smfmac_f32_16x16x64_bf16 v[48:51], v[180:183], v[84:91], v192\n" \
  "v_lshlrev_b32_sdwa v66, 4, v221 dst_sel:DWORD dst_unused:UNUSED_PAD src0_sel:DWORD src1_sel:WORD_0\n" \
  "ds_read_b128 v[108:111], v66\n" \
  "v_lshlrev_b32_sdwa v67, 4, v221 dst_sel:DWORD dst_unused:UNUSED_PAD src0_sel:DWORD src1_sel:WORD_1\n" \
  "ds_read_b128 v[112:115], v67\n" \
  "v_exp_f32 v132, v132\n" \
  "v_exp_f32 v133, v133\n" \
  "v_pk_mul_f32 v[36:37], v[36:37], v[120:121]\n" \
  "v_exp_f32 v134, v134\n" \
  "v_exp_f32 v135, v135\n" \
  "v_pk_mul_f32 v[38:39], v[38:39], v[122:123]\n" \
  "v_pk_add_f32 v[132:133], v[132:133], 1.0 op_sel_hi:[1,0]\n" \
  "v_pk_add_f32 v[134:135], v[134:135], 1.0 op_sel_hi:[1,0]\n" \
  "v_rcp_f32 v132, v132\n" \
  "v_rcp_f32 v133, v133\n" \
  "v_rcp_f32 v134, v134\n" \
  "v_rcp_f32 v135, v135\n" \
  "v_pk_mul_f32 v[36:37], v[36:37], v[132:133]\n" \
  "v_pk_fma_f32 v[36:37], v[38:39], v[134:135], v[36:37]\n" \
  "v_add_f32 v36, v36, v37\n" \
  "v_pk_mul_f32 v[136:137], v[40:41], v[40:41]\n" \
  "s_waitcnt lgkmcnt(4)\n" \
  "v_smfmac_f32_16x16x64_bf16 v[48:51], v[180:183], v[92:99], v192\n" \
  "v_lshlrev_b32_sdwa v64, 4, v254 dst_sel:DWORD dst_unused:UNUSED_PAD src0_sel:DWORD src1_sel:WORD_0\n" \
  "ds_read_b128 v[68:71], v64\n" \
  "v_lshlrev_b32_sdwa v65, 4, v254 dst_sel:DWORD dst_unused:UNUSED_PAD src0_sel:DWORD src1_sel:WORD_1\n" \
  "ds_read_b128 v[72:75], v65\n" \
  "v_pk_mul_f32 v[138:139], v[42:43], v[42:43]\n" \
  "v_pk_fma_f32 v[136:137], v[136:137], s[52:53], v[124:125]\n" \
  "v_pk_fma_f32 v[138:139], v[138:139], s[52:53], v[124:125]\n" \
  "v_pk_mul_f32 v[136:137], v[136:137], v[40:41]\n" \
  "v_pk_mul_f32 v[138:139], v[138:139], v[42:43]\n" \
  "v_exp_f32 v136, v136\n" \
  "v_exp_f32 v137, v137\n" \
  "v_pk_mul_f32 v[40:41], v[40:41], v[120:121]\n" \
  "v_exp_f32 v138, v138\n" \
  "v_exp_f32 v139, v139\n" \
  "v_pk_mul_f32 v[42:43], v[42:43], v[122:123]\n" \
  "v_pk_add_f32 v[136:137], v[136:137], 1.0 op_sel_hi:[1,0]\n" \
  "v_pk_add_f32 v[138:139], v[138:139], 1.0 op_sel_hi:[1,0]\n" \
  "v_rcp_f32 v136, v136\n" \
  "v_rcp_f32 v137, v137\n" \
  "v_rcp_f32 v138, v138\n" \
  "s_waitcnt lgkmcnt(4)\n" \
  "v_smfmac_f32_16x16x64_bf16 v[52:55], v[180:183], v[100:107], v192\n" \
  "v_lshlrev_b32_sdwa v66, 4, v222 dst_sel:DWORD dst_unused:UNUSED_PAD src0_sel:DWORD src1_sel:WORD_0\n" \
  "ds_read_b128 v[76:79], v66\n" \
  "v_lshlrev_b32_sdwa v67, 4, v222 dst_sel:DWORD dst_unused:UNUSED_PAD src0_sel:DWORD src1_sel:WORD_1\n" \
  "ds_read_b128 v[80:83], v67\n" \
  "v_rcp_f32 v139, v139\n" \
  "v_pk_mul_f32 v[40:41], v[40:41], v[136:137]\n" \
  "v_pk_fma_f32 v[40:41], v[42:43], v[138:139], v[40:41]\n" \
  "v_add_f32 v40, v40, v41\n" \
  "v_pk_mul_f32 v[140:141], v[44:45], v[44:45]\n" \
  "v_pk_mul_f32 v[142:143], v[46:47], v[46:47]\n" \
  "v_pk_fma_f32 v[140:141], v[140:141], s[52:53], v[124:125]\n" \
  "v_pk_fma_f32 v[142:143], v[142:143], s[52:53], v[124:125]\n" \
  "v_pk_mul_f32 v[140:141], v[140:141], v[44:45]\n" \
  "v_pk_mul_f32 v[142:143], v[142:143], v[46:47]\n" \
  "v_exp_f32 v140, v140\n" \
  "v_exp_f32 v141, v141\n" \
  "v_pk_mul_f32 v[44:45], v[44:45], v[120:121]\n" \
  "v_exp_f32 v142, v142\n" \
  "v_exp_f32 v143, v143\n" \
  "v_pk_mul_f32 v[46:47], v[46:47], v[122:123]\n" \
  "s_waitcnt lgkmcnt(4)\n" \
  "v_smfmac_f32_16x16x64_bf16 v[52:55], v[180:183], v[108:115], v192\n" \
  "v_lshlrev_b32_sdwa v64, 4, v255 dst_sel:DWORD dst_unused:UNUSED_PAD src0_sel:DWORD src1_sel:WORD_0\n" \
  "ds_read_b128 v[84:87], v64\n" \
  "v_lshlrev_b32_sdwa v65, 4, v255 dst_sel:DWORD dst_unused:UNUSED_PAD src0_sel:DWORD src1_sel:WORD_1\n" \
  "ds_read_b128 v[88:91], v65\n" \
  "v_pk_add_f32 v[140:141], v[140:141], 1.0 op_sel_hi:[1,0]\n" \
  "v_pk_add_f32 v[142:143], v[142:143], 1.0 op_sel_hi:[1,0]\n" \
  "v_rcp_f32 v140, v140\n" \
  "v_rcp_f32 v141, v141\n" \
  "v_rcp_f32 v142, v142\n" \
  "v_rcp_f32 v143, v143\n" \
  "v_pk_mul_f32 v[44:45], v[44:45], v[140:141]\n" \
  "v_pk_fma_f32 v[44:45], v[46:47], v[142:143], v[44:45]\n" \
  "v_add_f32 v44, v44, v45\n" \
  "v_pk_mul_f32 v[128:129], v[48:49], v[48:49]\n" \
  "v_pk_mul_f32 v[130:131], v[50:51], v[50:51]\n" \
  "v_pk_fma_f32 v[128:129], v[128:129], s[52:53], v[124:125]\n" \
  "v_pk_fma_f32 v[130:131], v[130:131], s[52:53], v[124:125]\n" \
  "v_pk_mul_f32 v[128:129], v[128:129], v[48:49]\n" \
  "v_pk_mul_f32 v[130:131], v[130:131], v[50:51]\n" \
  "v_exp_f32 v128, v128\n" \
  "s_waitcnt lgkmcnt(4)\n" \
  "v_smfmac_f32_16x16x64_bf16 v[56:59], v[180:183], v[68:75], v192\n" \
  "v_lshlrev_b32_sdwa v66, 4, v223 dst_sel:DWORD dst_unused:UNUSED_PAD src0_sel:DWORD src1_sel:WORD_0\n" \
  "ds_read_b128 v[92:95], v66\n" \
  "v_lshlrev_b32_sdwa v67, 4, v223 dst_sel:DWORD dst_unused:UNUSED_PAD src0_sel:DWORD src1_sel:WORD_1\n" \
  "ds_read_b128 v[96:99], v67\n" \
  "v_exp_f32 v129, v129\n" \
  "v_pk_mul_f32 v[48:49], v[48:49], v[120:121]\n" \
  "v_exp_f32 v130, v130\n" \
  "v_exp_f32 v131, v131\n" \
  "v_pk_mul_f32 v[50:51], v[50:51], v[122:123]\n" \
  "v_pk_add_f32 v[128:129], v[128:129], 1.0 op_sel_hi:[1,0]\n" \
  "v_pk_add_f32 v[130:131], v[130:131], 1.0 op_sel_hi:[1,0]\n" \
  "v_rcp_f32 v128, v128\n" \
  "v_rcp_f32 v129, v129\n" \
  "v_rcp_f32 v130, v130\n" \
  "v_rcp_f32 v131, v131\n" \
  "v_pk_mul_f32 v[48:49], v[48:49], v[128:129]\n" \
  "v_pk_fma_f32 v[48:49], v[50:51], v[130:131], v[48:49]\n" \
  "v_add_f32 v48, v48, v49\n" \
  "v_pk_mul_f32 v[132:133], v[52:53], v[52:53]\n" \
  "v_pk_mul_f32 v[134:135], v[54:55], v[54:55]\n" \
  "s_waitcnt lgkmcnt(4)\n" \
  "v_smfmac_f32_16x16x64_bf16 v[56:59], v[180:183], v[76:83], v192\n" \
  "v_pk_fma_f32 v[132:133], v[132:133], s[52:53], v[124:125]\n" \
  "v_pk_fma_f32 v[134:135], v[134:135], s[52:53], v[124:125]\n" \
  "v_pk_mul_f32 v[132:133], v[132:133], v[52:53]\n" \
  "v_pk_mul_f32 v[134:135], v[134:135], v[54:55]\n" \
  "v_exp_f32 v132, v132\n" \
  "v_exp_f32 v133, v133\n" \
  "v_pk_mul_f32 v[52:53], v[52:53], v[120:121]\n" \
  "v_exp_f32 v134, v134\n" \
  "v_exp_f32 v135, v135\n" \
  "v_pk_mul_f32 v[54:55], v[54:55], v[122:123]\n" \
  "v_pk_add_f32 v[132:133], v[132:133], 1.0 op_sel_hi:[1,0]\n" \
  "v_pk_add_f32 v[134:135], v[134:135], 1.0 op_sel_hi:[1,0]\n" \
  "v_rcp_f32 v132, v132\n" \
  "v_rcp_f32 v133, v133\n" \
  "v_rcp_f32 v134, v134\n" \
  "v_rcp_f32 v135, v135\n" \
  "v_pk_mul_f32 v[52:53], v[52:53], v[132:133]\n" \
  "v_pk_fma_f32 v[52:53], v[54:55], v[134:135], v[52:53]\n" \
  "v_add_f32 v52, v52, v53\n" \
  "s_waitcnt lgkmcnt(2)\n" \
  "v_smfmac_f32_16x16x64_bf16 v[60:63], v[180:183], v[84:91], v192\n" \
  "v_pk_mul_f32 v[136:137], v[56:57], v[56:57]\n" \
  "v_pk_mul_f32 v[138:139], v[58:59], v[58:59]\n" \
  "v_pk_fma_f32 v[136:137], v[136:137], s[52:53], v[124:125]\n" \
  "v_pk_fma_f32 v[138:139], v[138:139], s[52:53], v[124:125]\n" \
  "v_pk_mul_f32 v[136:137], v[136:137], v[56:57]\n" \
  "v_pk_mul_f32 v[138:139], v[138:139], v[58:59]\n" \
  "v_exp_f32 v136, v136\n" \
  "v_exp_f32 v137, v137\n" \
  "v_pk_mul_f32 v[56:57], v[56:57], v[120:121]\n" \
  "v_exp_f32 v138, v138\n" \
  "v_exp_f32 v139, v139\n" \
  "v_pk_mul_f32 v[58:59], v[58:59], v[122:123]\n" \
  "v_pk_add_f32 v[136:137], v[136:137], 1.0 op_sel_hi:[1,0]\n" \
  "v_pk_add_f32 v[138:139], v[138:139], 1.0 op_sel_hi:[1,0]\n" \
  "v_rcp_f32 v136, v136\n" \
  "v_rcp_f32 v137, v137\n" \
  "v_rcp_f32 v138, v138\n" \
  "v_rcp_f32 v139, v139\n" \
  "v_pk_mul_f32 v[56:57], v[56:57], v[136:137]\n" \
  "v_pk_fma_f32 v[56:57], v[58:59], v[138:139], v[56:57]\n" \
  "v_add_f32 v56, v56, v57\n" \
  "s_waitcnt lgkmcnt(0)\n" \
  "v_smfmac_f32_16x16x64_bf16 v[60:63], v[180:183], v[92:99], v192\n" \
  "s_nop 7\n" \
  "v_pk_mul_f32 v[140:141], v[60:61], v[60:61]\n" \
  "v_pk_mul_f32 v[142:143], v[62:63], v[62:63]\n" \
  "v_pk_fma_f32 v[140:141], v[140:141], s[52:53], v[124:125]\n" \
  "v_pk_fma_f32 v[142:143], v[142:143], s[52:53], v[124:125]\n" \
  "v_pk_mul_f32 v[140:141], v[140:141], v[60:61]\n" \
  "v_pk_mul_f32 v[142:143], v[142:143], v[62:63]\n" \
  "v_exp_f32 v140, v140\n" \
  "v_exp_f32 v141, v141\n" \
  "v_pk_mul_f32 v[60:61], v[60:61], v[120:121]\n" \
  "v_exp_f32 v142, v142\n" \
  "v_exp_f32 v143, v143\n" \
  "v_pk_mul_f32 v[62:63], v[62:63], v[122:123]\n" \
  "v_pk_add_f32 v[140:141], v[140:141], 1.0 op_sel_hi:[1,0]\n" \
  "v_pk_add_f32 v[142:143], v[142:143], 1.0 op_sel_hi:[1,0]\n" \
  "v_rcp_f32 v140, v140\n" \
  "v_rcp_f32 v141, v141\n" \
  "v_rcp_f32 v142, v142\n" \
  "v_rcp_f32 v143, v143\n" \
  "v_pk_mul_f32 v[60:61], v[60:61], v[140:141]\n" \
  "v_pk_fma_f32 v[60:61], v[62:63], v[142:143], v[60:61]\n" \
  "v_add_f32 v60, v60, v61\n" \
  "v_permlane16_swap_b32 v32, v48\n" \
  "v_permlane16_swap_b32 v36, v52\n" \
  "v_permlane16_swap_b32 v40, v56\n" \
  "v_permlane16_swap_b32 v44, v60\n" \
  "v_add_f32 v148, v32, v48\n" \
  "v_add_f32 v149, v36, v52\n" \
  "v_add_f32 v150, v40, v56\n" \
  "v_add_f32 v151, v44, v60\n" \
  "v_min_u32 v148, -2, v148\n" \
  "v_min_u32 v149, -2, v149\n" \
  "v_min_u32 v150, -2, v150\n" \
  "v_min_u32 v151, -2, v151\n" \
  "global_store_dwordx4 v188, v[148:151], s[40:41] offset:1024 sc1\n" \
  "s_cmp_ge_u32 s27, 4\n" \
  "s_cbranch_scc1 LF_b\n" \
  "s_load_dwordx2 s[24:25], s[0:1], 0x40\n" \
  "v_lshrrev_b32 v0, 4, v188\n" \
  "v_lshrrev_b32 v1, 4, v0\n" \
  "v_and_b32 v2, 15, v0\n" \
  "v_lshlrev_b32 v2, 4, v2\n" \
  "v_lshl_add_u32 v1, v1, 18, v2\n" \
  "s_lshl_b32 s35, s27, 20\n" \
  "s_lshl_b32 s36, s26, 8\n" \
  "s_add_u32 s35, s35, s36\n" \
  "s_add_u32 s35, s35, 105676800\n" \
  "s_add_u32 s40, s22, s35\n" \
  "s_addc_u32 s41, s23, 0\n" \
  "s_add_u32 s42, s40, 65536\n" \
  "s_addc_u32 s43, s41, 0\n" \
  "s_add_u32 s54, s42, 65536\n" \
  "s_addc_u32 s55, s43, 0\n" \
  "s_add_u32 s56, s54, 65536\n" \
  "s_addc_u32 s57, s55, 0\n" \
  "s_add_u32 s44, s22, 109871104\n" \
  "s_addc_u32 s45, s23, 0\n" \
  "s_add_u32 s46, s44, 65536\n" \
  "s_addc_u32 s47, s45, 0\n" \
  "s_lshr_b32 s35, s26, 2\n" \
  "s_lshl_b32 s35, s35, 8\n" \
  "s_and_b32 s36, s26, 1\n" \
  "s_lshl_b32 s36, s36, 7\n" \
  "s_add_u32 s35, s35, s36\n" \
  "s_and_b32 s36, s26, 2\n" \
  "s_lshl3_add_u32 s35, s36, s35\n" \
  "v_and_b32 v2, 3, v0\n" \
  "v_lshrrev_b32 v3, 2, v0\n" \
  "v_lshl_add_u32 v2, v2, 5, v3\n" \
  "v_add_u32 v2, s35, v2\n" \
  "v_lshlrev_b32 v3, 2, v2\n" \
  "s_cmp_eq_u32 s27, 0\n" \
  "s_cselect_b32 s35, -1, 0\n" \
  "v_and_b32 v3, s35, v3\n" \
  "s_lshl_b32 s6, s6, 16\n" \
  "v_and_b32 v6, 15, v0\n" \
  "v_lshrrev_b32 v7, 4, v0\n" \
  "v_lshl_add_u32 v6, v6, 2, v7\n" \
  "s_lshl_b32 s35, s27, 8\n" \
  "v_lshl_add_u32 v6, v6, 2, s35\n" \
  "v_lshlrev_b32 v7, 2, v0\n" \
  "v_lshlrev_b32 v2, 1, v2\n" \
  "s_mov_b32 s5, 0x10000\n" \
  "s_barrier\n" \
  "s_sleep 12\n" \
  "LF_poll:\n" \
  "global_load_dwordx4 v[8:11], v1, s[40:41] sc1\n" \
  "global_load_dwordx4 v[12:15], v1, s[42:43] sc1\n" \
  "global_load_dwordx4 v[16:19], v1, s[54:55] sc1\n" \
  "global_load_dwordx4 v[20:23], v1, s[56:57] sc1\n" \
  "global_load_dword v24, v3, s[44:45] sc1\n" \
  "global_load_dword v25, v3, s[46:47] sc1\n" \
  "s_waitcnt vmcnt(0)\n" \
  "v_max3_u32 v4, v8, v9, v10\n" \
  "v_max3_u32 v5, v11, v12, v13\n" \
  "v_max3_u32 v4, v4, v14, v15\n" \
  "v_max3_u32 v5, v5, v16, v17\n" \
  "v_max3_u32 v4, v4, v18, v19\n" \
  "v_max3_u32 v5, v5, v20, v21\n" \
  "v_max3_u32 v4, v4, v22, v23\n" \
  "v_max3_u32 v5, v5, v24, v25\n" \
  "v_max_u32 v5, v5, v4\n" \
  "v_cmp_eq_u32 vcc, -1, v5\n" \
  "s_sub_u32 s5, s5, 1\n" \
  "s_cbranch_scc1 LF_go\n" \
  "s_nop 2\n" \
  "s_cbranch_vccz LF_go\n" \
  "s_mov_b64 exec, vcc\n" \
  "s_sleep 1\n" \
  "s_branch LF_poll\n" \
  "LF_go:\n" \
  "s_mov_b64 exec, -1\n" \
  "v_pk_add_f32 v[8:9], v[8:9], v[12:13]\n" \
  "v_pk_add_f32 v[10:11], v[10:11], v[14:15]\n" \
  "v_pk_add_f32 v[16:17], v[16:17], v[20:21]\n" \
  "v_pk_add_f32 v[18:19], v[18:19], v[22:23]\n" \
  "v_pk_add_f32 v[8:9], v[8:9], v[16:17]\n" \
  "v_pk_add_f32 v[10:11], v[10:11], v[18:19]\n" \
  "v_add_f32 v24, v24, v25\n" \
  "v_permlane16_swap_b32 v8, v9\n" \
  "v_permlane16_swap_b32 v10, v11\n" \
  "v_add_f32 v8, v8, v9\n" \
  "v_add_f32 v10, v10, v11\n" \
  "v_cvt_pk_bf16_f32 v24, v24, v24\n" \
  "s_cmp_lg_u32 s27, 0\n" \
  "v_permlane32_swap_b32 v8, v10\n" \
  "v_add_f32 v8, v8, v10\n" \
  "ds_write_b32 v6, v8\n" \
  "s_waitcnt lgkmcnt(0)\n" \
  "s_barrier\n" \
  "s_cbranch_scc1 LF_end\n" \
  "ds_read2st64_b32 v[8:9], v7 offset0:0 offset1:1\n" \
  "ds_read2st64_b32 v[10:11], v7 offset0:2 offset1:3\n" \
  "v_lshlrev_b32 v24, 16, v24\n" \
  "s_waitcnt lgkmcnt(0)\n" \
  "v_pk_add_f32 v[8:9], v[8:9], v[10:11]\n" \
  "v_add_f32 v8, v8, v9\n" \
  "v_cvt_pk_bf16_f32 v8, v8, v8\n" \
  "v_lshlrev_b32 v8, 16, v8\n" \
  "v_add_f32 v8, s6, v8\n" \
  "v_cvt_pk_bf16_f32 v8, v8, v8\n" \
  "v_lshlrev_b32 v8, 16, v8\n" \
  "v_add_f32 v8, v8, v24\n" \
  "v_cvt_pk_bf16_f32 v8, v8, v8\n" \
  "global_store_short v2, v8, s[24:25]\n" \
  "LF_end:\n" \
  "s_endpgm\n" \
  "LF_b:\n" \
  "s_barrier\n" \
  "s_endpgm\n" \
  "LG_h:\n" \
  "s_cmp_eq_u32 s51, 4\n" \
  "s_cbranch_scc1 LG_chk\n" \
  "s_cmp_lg_u32 s51, 7\n" \
  "s_cbranch_scc1 LG_ret\n" \
  "s_barrier\n" \
  "s_mov_b32 exec_hi, 0\n" \
  "global_atomic_add v196, v199, s[48:49] offset:3076 sc1\n" \
  "s_mov_b64 exec, -1\n" \
  "s_branch LG_aft\n" \
  "LG_pol:\n" \
  "s_mov_b64 exec, 0xff\n" \
  "global_load_dword v200, v198, s[48:49] offset:3076 sc1\n" \
  "s_mov_b64 exec, -1\n" \
  "s_branch LG_rt2\n" \
  "LG_chk:\n" \
  "s_mov_b32 s5, 0x10000\n" \
  "LG_p:\n" \
  "s_mov_b64 exec, 0xff\n" \
  "v_cmp_le_u32 vcc, 32, v200\n" \
  "s_mov_b64 exec, -1\n" \
  "s_sub_u32 s5, s5, 1\n" \
  "s_cselect_b32 s35, 0xff, vcc_lo\n" \
  "s_cmp_eq_u32 s35, 0xff\n" \
  "s_cbranch_scc1 LG_p_x\n" \
  "s_sleep 2\n" \
  "s_mov_b64 exec, 0xff\n" \
  "global_load_dword v200, v198, s[48:49] offset:3076 sc1\n" \
  "s_mov_b64 exec, -1\n" \
  "s_waitcnt vmcnt(0)\n" \
  "s_branch LG_p\n" \
  "LG_p_x:\n" \
  "s_branch LG_ret\n" \
  "LZ_end:\n"

#define R10(p, b) p #b "0", p #b "1", p #b "2", p #b "3", p #b "4", p #b "5", p #b "6", p #b "7", p #b "8", p #b "9"
__global__ __launch_bounds__(512, 2) void k1(const int* __restrict__ idx, const unsigned short* __restrict__ table,
                                             const unsigned short* __restrict__ w_p, const unsigned short* __restrict__ w1,
                                             const unsigned short* __restrict__ b1, const unsigned short* __restrict__ w2,
                                             const unsigned short* __restrict__ b2, char* __restrict__ ws, __hip_bfloat16* __restrict__ out) {
  __shared__ __attribute__((aligned(1024))) char smem[163840];
  unsigned tid = threadIdx.x;
  asm volatile(K_ASM
               : "+{v0}"(tid)
               : "{s[0:1]}"(__builtin_amdgcn_kernarg_segment_ptr()), "{s26}"(blockIdx.x), "s"((unsigned)(uintptr_t)smem)
               : "memory", "scc", "vcc", "s3", "s4", "s5", "s6", "s7", "s8", "s9", R10("s", 1), "s20", "s21", "s22", "s23", "s24", "s25", "s27", "s28", "s29", "s30", "s31", "s33", "s34", "s35", "s36", "s37", "s38", "s39", R10("s", 4), R10("s", 5), R10("s", 6), R10("s", 7), R10("s", 8), R10("s", 9),
                 "v1", "v2", "v3", "v4", "v5", "v6", "v7", "v8", "v9", R10("v", 1), R10("v", 2), R10("v", 3), R10("v", 4), R10("v", 5), R10("v", 6), R10("v", 7), R10("v", 8), R10("v", 9),
                 R10("v", 10), R10("v", 11), R10("v", 12), R10("v", 13), R10("v", 14), R10("v", 15), R10("v", 16), R10("v", 17), R10("v", 18), R10("v", 19),
                 R10("v", 20), R10("v", 21), R10("v", 22), R10("v", 23), R10("v", 24), "v250", "v251", "v252", "v253", "v254", "v255");
}

extern "C" void kernel_launch(void* const* d_in, const int* in_sizes, int n_in, void* d_out, int out_size, void* d_ws,
                              size_t ws_size, hipStream_t stream) {
  k1<<<dim3(256), dim3(512), 0, stream>>>((const int*)d_in[0], (const unsigned short*)d_in[1], (const unsigned short*)d_in[2], (const unsigned short*)d_in[3],
                                          (const unsigned short*)d_in[4], (const unsigned short*)d_in[5], (const unsigned short*)d_in[6], (char*)d_ws, (__hip_bfloat16*)d_out);
}
```

```cpp
#include <hip/hip_runtime.h>
#include <hip/hip_bf16.h>
#include <stdint.h>

#define K_ASM \
  "LZ_first:\n" \
  "s_load_dwordx16 s[8:23], s[0:1], 0x0\n" \
  "v_readfirstlane_b32 s27, v0\n" \
  "v_and_b32 v255, 63, v0\n" \
  "s_and_b32 s29, s26, 7\n" \
  "s_lshr_b32 s30, s26, 3\n" \
  "s_lshr_b32 s27, s27, 6\n" \
  "s_and_b32 s31, s30, 1\n" \
  "s_and_b32 s28, s27, 3\n" \
  "s_lshr_b32 s35, s30, 1\n" \
  "s_lshl3_add_u32 s7, s35, s29\n" \
  "v_lshrrev_b32 v1, 3, v255\n" \
  "v_and_b32 v2, 7, v255\n" \
  "v_lshlrev_b32 v1, 8, v1\n" \
  "v_lshl_or_b32 v254, v2, 4, v1\n" \
  "s_min_u32 s36, s7, 122\n" \
  "s_add_u32 s36, s36, s28\n" \
  "s_cmp_ge_u32 s27, 4\n" \
  "s_cbranch_scc1 LB_start\n" \
  "s_setprio 3\n" \
  "v_and_b32 v252, 15, v255\n" \
  "v_lshrrev_b32 v253, 4, v255\n" \
  "v_mul_u32_u24 v2, 400, v253\n" \
  "v_min_u32 v3, 8, v252\n" \
  "v_lshl_add_u32 v250, v252, 4, v2\n" \
  "v_lshl_add_u32 v251, v3, 4, v2\n" \
  "s_lshl2_add_u32 s36, s7, s28\n" \
  "s_mul_i32 s36, s36, 12800\n" \
  "s_lshl_b32 s35, s28, 10\n" \
  "v_lshl_add_u32 v1, v255, 4, s35\n" \
  "s_add_u32 m0, s35, 138240\n" \
  "s_waitcnt lgkmcnt(0)\n" \
  "s_add_u32 s44, s8, s36\n" \
  "s_addc_u32 s45, s9, 0\n" \
  "global_load_lds_dwordx4 v1, s[12:13]\n" \
  "s_add_u32 m0, m0, 0x1000\n" \
  "v_add_u32 v1, 0x1000, v1\n" \
  "global_load_lds_dwordx4 v1, s[12:13]\n" \
  "s_add_u32 m0, m0, 0x1000\n" \
  "v_add_u32 v1, 0x1000, v1\n" \
  "global_load_lds_dwordx4 v1, s[12:13]\n" \
  "s_add_u32 m0, m0, 0x1000\n" \
  "v_add_u32 v1, 0x1000, v1\n" \
  "global_load_lds_dwordx4 v1, s[12:13]\n" \
  "s_add_u32 m0, m0, 0x1000\n" \
  "v_add_u32 v1, 0x1000, v1\n" \
  "v_min_u32 v1, 19984, v1\n" \
  "global_load_lds_dwordx4 v1, s[12:13]\n" \
  "global_load_dwordx4 v[232:235], v250, s[44:45] offset:0\n" \
  "global_load_dwordx4 v[236:239], v251, s[44:45] offset:256\n" \
  "global_load_dwordx4 v[240:243], v250, s[44:45] offset:1600\n" \
  "global_load_dwordx4 v[244:247], v251, s[44:45] offset:1856\n" \
  "s_setprio 1\n" \
  "v_lshrrev_b32 v1, 2, v252\n" \
  "v_and_b32 v2, 3, v252\n" \
  "v_cmp_eq_u32 vcc, v1, v253\n" \
  "v_mov_b32 v4, 0x3f80\n" \
  "v_and_b32 v13, 1, v253\n" \
  "v_lshlrev_b32 v14, 1, v13\n" \
  "v_xor_b32 v2, v2, v14\n" \
  "v_cndmask_b32 v2, 4, v2, vcc\n" \
  "v_cmp_eq_u32 vcc, 0, v2\n" \
  "s_nop 1\n" \
  "v_cndmask_b32 v216, 0, v4, vcc\n" \
  "v_lshlrev_b32 v220, 16, v216\n" \
  "v_cmp_eq_u32 vcc, 1, v2\n" \
  "s_nop 1\n" \
  "v_cndmask_b32 v217, 0, v4, vcc\n" \
  "v_lshlrev_b32 v221, 16, v217\n" \
  "v_cmp_eq_u32 vcc, 2, v2\n" \
  "s_nop 1\n" \
  "v_cndmask_b32 v218, 0, v4, vcc\n" \
  "v_lshlrev_b32 v222, 16, v218\n" \
  "v_cmp_eq_u32 vcc, 3, v2\n" \
  "s_nop 1\n" \
  "v_cndmask_b32 v219, 0, v4, vcc\n" \
  "v_lshlrev_b32 v223, 16, v219\n" \
  "v_and_b32 v1, 2, v252\n" \
  "v_lshlrev_b32 v1, 3, v1\n" \
  "v_lshlrev_b32 v1, v1, v4\n" \
  "v_and_b32 v2, 0x31, v255\n" \
  "v_mov_b64 v[224:225], 0\n" \
  "v_cmp_eq_u32 vcc, 0, v2\n" \
  "s_nop 1\n" \
  "v_cndmask_b32 v226, 0, v1, vcc\n" \
  "v_cmp_eq_u32 vcc, 1, v2\n" \
  "s_nop 1\n" \
  "v_cndmask_b32 v227, 0, v1, vcc\n" \
  "v_mul_u32_u24 v5, 400, v252\n" \
  "v_add_u32 v5, 138240, v5\n" \
  "v_lshl_add_u32 v6, v253, 4, v5\n" \
  "v_add_u32 v7, 6400, v5\n" \
  "v_add_u32 v8, 6400, v6\n" \
  "v_add_u32 v9, 12800, v5\n" \
  "v_add_u32 v10, 12800, v6\n" \
  "v_bfe_u32 v1, v252, 1, 1\n" \
  "v_and_b32 v2, 1, v252\n" \
  "v_lshl_add_u32 v1, v2, 1, v1\n" \
  "v_mul_u32_u24 v1, 200, v1\n" \
  "v_add_u32 v11, 157440, v1\n" \
  "v_lshl_add_u32 v12, v253, 4, v11\n" \
  "v_lshlrev_b32 v13, 3, v13\n" \
  "v_sub_u32 v16, v5, v13\n" \
  "v_add_u32 v5, v5, v13\n" \
  "v_sub_u32 v17, v6, v13\n" \
  "v_add_u32 v6, v6, v13\n" \
  "v_sub_u32 v18, v7, v13\n" \
  "v_add_u32 v7, v7, v13\n" \
  "v_sub_u32 v19, v8, v13\n" \
  "v_add_u32 v8, v8, v13\n" \
  "v_sub_u32 v20, v9, v13\n" \
  "v_add_u32 v9, v9, v13\n" \
  "v_sub_u32 v21, v10, v13\n" \
  "v_add_u32 v10, v10, v13\n" \
  "v_sub_u32 v22, v11, v13\n" \
  "v_add_u32 v11, v11, v13\n" \
  "v_sub_u32 v23, v12, v13\n" \
  "v_add_u32 v12, v12, v13\n" \
  "v_lshlrev_b32 v188, 4, v253\n" \
  "v_and_b32 v96, 4, v252\n" \
  "v_lshl_add_u32 v96, v253, 5, v96\n" \
  "v_mov_b32 v191, 0x40000000\n" \
  "v_cmp_gt_u32 vcc, 8, v252\n" \
  "v_mov_b32 v193, 0x4444\n" \
  "v_mov_b32 v98, 0xeeee\n" \
  "v_cndmask_b32 v189, v191, v96, vcc\n" \
  "v_cndmask_b32 v190, v96, v191, vcc\n" \
  "v_and_b32 v99, 4, v252\n" \
  "v_cmp_ne_u32 vcc, 0, v99\n" \
  "s_lshl_b32 s35, s28, 11\n" \
  "s_add_u32 s35, s35, 130048\n" \
  "v_cndmask_b32 v193, v193, v98, vcc\n" \
  "v_lshl_add_u32 v194, v252, 4, s35\n" \
  "v_and_b32 v96, 7, v252\n" \
  "v_lshlrev_b32 v96, 5, v96\n" \
  "v_lshl_add_u32 v96, v253, 8, v96\n" \
  "v_lshrrev_b32 v97, 3, v252\n" \
  "v_lshl_add_u32 v96, v97, 2, v96\n" \
  "v_add_u32 v195, s35, v96\n" \
  "v_and_b32 v97, v97, v252\n" \
  "v_lshlrev_b32 v192, 4, v97\n" \
  "s_waitcnt vmcnt(4)\n" \
  "s_barrier\n" \
  "ds_read_b64 v[200:201], v12 offset:128\n" \
  "ds_read_b64 v[202:203], v23 offset:136\n" \
  "ds_read_b64 v[184:185], v9 offset:184\n" \
  "ds_read_b64 v[186:187], v20 offset:192\n" \
  "ds_read_b64 v[212:213], v9 offset:384\n" \
  "ds_read_b64 v[214:215], v20 offset:392\n" \
  "ds_read_b64 v[196:197], v12 offset:64\n" \
  "ds_read_b64 v[198:199], v23 offset:72\n" \
  "ds_read_b64 v[176:177], v7 offset:184\n" \
  "ds_read_b64 v[178:179], v18 offset:192\n" \
  "ds_read_b64 v[180:181], v7 offset:384\n" \
  "ds_read_b64 v[182:183], v18 offset:392\n" \
  "ds_read_b64 v[228:229], v12 offset:0\n" \
  "ds_read_b64 v[230:231], v23 offset:8\n" \
  "s_waitcnt lgkmcnt(12)\n" \
  "v_mfma_f32_16x16x32_bf16 v[44:47], v[200:203], v[216:219], 0\n" \
  "ds_read_b64 v[168:169], v5 offset:184\n" \
  "ds_read_b64 v[170:171], v16 offset:192\n" \
  "s_waitcnt lgkmcnt(12)\n" \
  "v_mfma_f32_16x16x32_bf16 v[44:47], v[224:227], v[184:187], v[44:47]\n" \
  "ds_read_b64 v[172:173], v5 offset:384\n" \
  "ds_read_b64 v[174:175], v16 offset:392\n" \
  "v_mfma_f32_16x16x32_bf16 v[208:211], v[200:203], v[220:223], 0\n" \
  "ds_read_b64 v[96:97], v6 offset:0\n" \
  "ds_read_b64 v[98:99], v17 offset:8\n" \
  "s_waitcnt lgkmcnt(14)\n" \
  "v_mfma_f32_16x16x32_bf16 v[208:211], v[224:227], v[212:215], v[208:211]\n" \
  "ds_read_b64 v[124:125], v8 offset:64\n" \
  "ds_read_b64 v[126:127], v19 offset:72\n" \
  "s_waitcnt lgkmcnt(14)\n" \
  "v_mfma_f32_16x16x32_bf16 v[60:63], v[196:199], v[216:219], 0\n" \
  "ds_read_b64 v[136:137], v8 offset:264\n" \
  "ds_read_b64 v[138:139], v19 offset:272\n" \
  "s_waitcnt lgkmcnt(14)\n" \
  "v_mfma_f32_16x16x32_bf16 v[60:63], v[224:227], v[176:179], v[60:63]\n" \
  "ds_read_b64 v[140:141], v8 offset:328\n" \
  "ds_read_b64 v[142:143], v19 offset:336\n" \
  "v_mfma_f32_16x16x32_bf16 v[184:187], v[196:199], v[220:223], 0\n" \
  "ds_read_b64 v[160:161], v10 offset:264\n" \
  "ds_read_b64 v[162:163], v21 offset:272\n" \
  "s_waitcnt lgkmcnt(15)\n" \
  "v_mfma_f32_16x16x32_bf16 v[184:187], v[224:227], v[180:183], v[184:187]\n" \
  "ds_read_b64 v[116:117], v6 offset:328\n" \
  "ds_read_b64 v[118:119], v17 offset:336\n" \
  "s_waitcnt lgkmcnt(15)\n" \
  "v_mfma_f32_16x16x32_bf16 v[68:71], v[228:231], v[216:219], 0\n" \
  "ds_read_b64 v[144:145], v10 offset:0\n" \
  "ds_read_b64 v[146:147], v21 offset:8\n" \
  "s_waitcnt lgkmcnt(15)\n" \
  "v_mfma_f32_16x16x32_bf16 v[68:71], v[224:227], v[168:171], v[68:71]\n" \
  "ds_read_b64 v[148:149], v10 offset:64\n" \
  "ds_read_b64 v[150:151], v21 offset:72\n" \
  "v_mfma_f32_16x16x32_bf16 v[212:215], v[228:231], v[220:223], 0\n" \
  "ds_read_b64 v[104:105], v6 offset:128\n" \
  "ds_read_b64 v[106:107], v17 offset:136\n" \
  "s_waitcnt lgkmcnt(15)\n" \
  "v_mfma_f32_16x16x32_bf16 v[212:215], v[224:227], v[172:175], v[212:215]\n" \
  "ds_read_b64 v[100:101], v6 offset:64\n" \
  "ds_read_b64 v[102:103], v17 offset:72\n" \
  "v_mfma_f32_16x16x32_bf16 v[92:95], v[96:99], v[216:219], 0\n" \
  "ds_read_b64 v[120:121], v8 offset:0\n" \
  "ds_read_b64 v[122:123], v19 offset:8\n" \
  "s_waitcnt lgkmcnt(15)\n" \
  "v_mfma_f32_16x16x32_bf16 v[24:27], v[124:127], v[220:223], 0\n" \
  "ds_read_b64 v[112:113], v6 offset:264\n" \
  "ds_read_b64 v[114:115], v17 offset:272\n" \
  "v_mfma_f32_16x16x32_bf16 v[24:27], v[216:219], v[136:139], v[24:27]\n" \
  "ds_read_b64 v[156:157], v10 offset:200\n" \
  "ds_read_b64 v[158:159], v21 offset:208\n" \
  "s_waitcnt lgkmcnt(15)\n" \
  "v_mfma_f32_16x16x32_bf16 v[28:31], v[140:143], v[220:223], 0\n" \
  "ds_read_b64 v[132:133], v8 offset:200\n" \
  "ds_read_b64 v[134:135], v19 offset:208\n" \
  "v_mfma_f32_16x16x32_bf16 v[28:31], v[220:223], v[160:163], v[28:31]\n" \
  "ds_read_b64 v[108:109], v6 offset:200\n" \
  "ds_read_b64 v[110:111], v17 offset:208\n" \
  "s_waitcnt lgkmcnt(15)\n" \
  "v_mfma_f32_16x16x32_bf16 v[32:35], v[116:119], v[216:219], 0\n" \
  "ds_read_b64 v[152:153], v10 offset:128\n" \
  "ds_read_b64 v[154:155], v21 offset:136\n" \
  "v_mfma_f32_16x16x32_bf16 v[32:35], v[220:223], v[144:147], v[32:35]\n" \
  "ds_read_b64 v[164:165], v10 offset:328\n" \
  "ds_read_b64 v[166:167], v21 offset:336\n" \
  "v_mfma_f32_16x16x32_bf16 v[36:39], v[140:143], v[216:219], 0\n" \
  "ds_read_b64 v[128:129], v8 offset:128\n" \
  "ds_read_b64 v[130:131], v19 offset:136\n" \
  "s_waitcnt lgkmcnt(15)\n" \
  "v_mfma_f32_16x16x32_bf16 v[36:39], v[220:223], v[148:151], v[36:39]\n" \
  "ds_read_b64 v[204:205], v11 offset:184\n" \
  "ds_read_b64 v[206:207], v22 offset:192\n" \
  "v_mfma_f32_16x16x32_bf16 v[40:43], v[104:107], v[216:219], 0\n" \
  "v_mfma_f32_16x16x32_bf16 v[40:43], v[216:219], v[144:147], v[40:43]\n" \
  "v_mfma_f32_16x16x32_bf16 v[48:51], v[100:103], v[216:219], 0\n" \
  "s_waitcnt lgkmcnt(15)\n" \
  "v_mfma_f32_16x16x32_bf16 v[48:51], v[216:219], v[120:123], v[48:51]\n" \
  "s_waitcnt lgkmcnt(14)\n" \
  "v_mfma_f32_16x16x32_bf16 v[52:55], v[112:115], v[216:219], 0\n" \
  "v_mfma_f32_16x16x32_bf16 v[52:55], v[220:223], v[120:123], v[52:55]\n" \
  "v_mfma_f32_16x16x32_bf16 v[56:59], v[116:119], v[220:223], 0\n" \
  "s_waitcnt lgkmcnt(12)\n" \
  "v_mfma_f32_16x16x32_bf16 v[56:59], v[220:223], v[156:159], v[56:59]\n" \
  "s_waitcnt vmcnt(0) lgkmcnt(0)\n" \
  "v_mfma_f32_16x16x32_bf16 v[64:67], v[104:107], v[220:223], 0\n" \
  "v_cvt_pk_u16_u32 v232, v232, v233\n" \
  "v_cvt_pk_u16_u32 v234, v234, v235\n" \
  "v_mfma_f32_16x16x32_bf16 v[64:67], v[216:219], v[156:159], v[64:67]\n" \
  "v_cvt_pk_u16_u32 v236, v236, v237\n" \
  "v_cvt_pk_u16_u32 v238, v238, v239\n" \
  "v_mfma_f32_16x16x32_bf16 v[72:75], v[136:139], v[220:223], 0\n" \
  "v_pk_lshrrev_b16 v233, 3, v232 op_sel_hi:[0,1]\n" \
  "v_pk_lshrrev_b16 v235, 3, v234 op_sel_hi:[0,1]\n" \
  "v_mfma_f32_16x16x32_bf16 v[76:79], v[112:115], v[220:223], 0\n" \
  "v_pk_lshrrev_b16 v237, 3, v236 op_sel_hi:[0,1]\n" \
  "v_pk_lshrrev_b16 v239, 3, v238 op_sel_hi:[0,1]\n" \
  "v_mfma_f32_16x16x32_bf16 v[76:79], v[220:223], v[132:135], v[76:79]\n" \
  "v_pk_mad_u16 v180, v232, 8, v233 op_sel_hi:[1,0,1]\n" \
  "v_pk_mad_u16 v181, v234, 8, v235 op_sel_hi:[1,0,1]\n" \
  "v_mfma_f32_16x16x32_bf16 v[80:83], v[100:103], v[220:223], 0\n" \
  "v_pk_mad_u16 v182, v236, 8, v237 op_sel_hi:[1,0,1]\n" \
  "v_pk_mad_u16 v183, v238, 8, v239 op_sel_hi:[1,0,1]\n" \
  "v_mfma_f32_16x16x32_bf16 v[80:83], v[216:219], v[132:135], v[80:83]\n" \
  "v_alignbit_b32 v182, v182, v182, v192\n" \
  "v_alignbit_b32 v183, v183, v183, v192\n" \
  "v_mfma_f32_16x16x32_bf16 v[84:87], v[96:99], v[220:223], 0\n" \
  "v_cvt_pk_u16_u32 v240, v240, v241\n" \
  "v_cvt_pk_u16_u32 v242, v242, v243\n" \
  "v_mfma_f32_16x16x32_bf16 v[84:87], v[216:219], v[108:111], v[84:87]\n" \
  "v_cvt_pk_u16_u32 v244, v244, v245\n" \
  "v_cvt_pk_u16_u32 v246, v246, v247\n" \
  "v_mfma_f32_16x16x32_bf16 v[88:91], v[108:111], v[220:223], 0\n" \
  "v_pk_lshrrev_b16 v241, 3, v240 op_sel_hi:[0,1]\n" \
  "v_pk_lshrrev_b16 v243, 3, v242 op_sel_hi:[0,1]\n" \
  "v_mfma_f32_16x16x32_bf16 v[0:3], v[124:127], v[216:219], 0\n" \
  "v_pk_lshrrev_b16 v245, 3, v244 op_sel_hi:[0,1]\n" \
  "v_pk_lshrrev_b16 v247, 3, v246 op_sel_hi:[0,1]\n" \
  "v_mfma_f32_16x16x32_bf16 v[4:7], v[152:155], v[216:219], 0\n" \
  "v_pk_mad_u16 v196, v240, 8, v241 op_sel_hi:[1,0,1]\n" \
  "v_pk_mad_u16 v197, v242, 8, v243 op_sel_hi:[1,0,1]\n" \
  "v_mfma_f32_16x16x32_bf16 v[8:11], v[152:155], v[220:223], 0\n" \
  "v_pk_mad_u16 v198, v244, 8, v245 op_sel_hi:[1,0,1]\n" \
  "v_pk_mad_u16 v199, v246, 8, v247 op_sel_hi:[1,0,1]\n" \
  "v_mfma_f32_16x16x32_bf16 v[8:11], v[216:219], v[164:167], v[8:11]\n" \
  "v_alignbit_b32 v198, v198, v198, v192\n" \
  "v_alignbit_b32 v199, v199, v199, v192\n" \
  "v_mfma_f32_16x16x32_bf16 v[12:15], v[164:167], v[220:223], 0\n" \
  "v_add_u32 v200, 0x400, v195\n" \
  "v_mfma_f32_16x16x32_bf16 v[16:19], v[128:131], v[220:223], 0\n" \
  "v_mfma_f32_16x16x32_bf16 v[16:19], v[216:219], v[160:163], v[16:19]\n" \
  "v_mfma_f32_16x16x32_bf16 v[20:23], v[128:131], v[216:219], 0\n" \
  "v_mfma_f32_16x16x32_bf16 v[20:23], v[216:219], v[148:151], v[20:23]\n" \
  "v_mfma_f32_16x16x32_bf16 v[168:171], v[216:219], v[96:99], 0\n" \
  "v_mfma_f32_16x16x32_bf16 v[172:175], v[204:207], v[224:227], 0\n" \
  "v_mfma_f32_16x16x32_bf16 v[176:179], v[224:227], v[204:207], 0\n" \
  "s_mov_b32 exec_lo, 0x0\n" \
  "s_mov_b32 exec_hi, 0xffffffff\n" \
  "v_mov_b64 v[44:45], v[208:209]\n" \
  "v_mov_b64 v[46:47], v[210:211]\n" \
  "v_mov_b64 v[60:61], v[184:185]\n" \
  "v_mov_b64 v[62:63], v[186:187]\n" \
  "v_mov_b64 v[68:69], v[212:213]\n" \
  "v_mov_b64 v[70:71], v[214:215]\n" \
  "s_mov_b32 exec_lo, 0xff00ff00\n" \
  "s_mov_b32 exec_hi, 0x0\n" \
  "v_add_f32 v92, v92, v168\n" \
  "v_add_f32 v93, v93, v169\n" \
  "v_add_f32 v94, v94, v170\n" \
  "v_add_f32 v95, v95, v171\n" \
  "s_mov_b32 exec_lo, 0xf0000f00\n" \
  "s_mov_b32 exec_hi, 0x0\n" \
  "v_sub_f32 v92, v92, v176\n" \
  "v_sub_f32 v93, v93, v177\n" \
  "v_sub_f32 v94, v94, v178\n" \
  "v_sub_f32 v95, v95, v179\n" \
  "s_mov_b32 exec_lo, 0x0\n" \
  "s_mov_b32 exec_hi, 0xff00ff\n" \
  "v_mov_b64 v[92:93], 0\n" \
  "v_mov_b64 v[94:95], 0\n" \
  "s_mov_b32 exec_lo, 0x0\n" \
  "s_mov_b32 exec_hi, 0xf0000f\n" \
  "v_mov_b64 v[92:93], v[172:173]\n" \
  "v_mov_b64 v[94:95], v[174:175]\n" \
  "s_mov_b64 exec, -1\n" \
  "v_add_u32 v96, 6400, v250\n" \
  "v_add_u32 v97, 6400, v251\n" \
  "v_mov_b32 v98, 163328\n" \
  "v_mov_b32 v99, 1\n" \
  "v_lshlrev_b32 v179, 4, v253\n" \
  "s_lshr_b32 s35, s30, 4\n" \
  "s_lshl1_add_u32 s35, s29, s35\n" \
  "s_lshl_b32 s35, s35, 6\n" \
  "s_bfe_u32 s37, s30, 0x20002\n" \
  "s_lshl3_add_u32 s35, s37, s35\n" \
  "v_and_b32 v179, 16, v179\n" \
  "v_lshl_add_u32 v179, v179, 1, s35\n" \
  "s_mov_b64 exec, 1\n" \
  "ds_add_u32 v98, v99 offset:16\n" \
  "s_mov_b64 exec, -1\n" \
  "ds_write2_b32 v195, v180, v181 offset0:0 offset1:4\n" \
  "ds_write2_b32 v195, v182, v183 offset0:2 offset1:6\n" \
  "ds_write2_b32 v200, v196, v197 offset0:0 offset1:4\n" \
  "ds_write2_b32 v200, v198, v199 offset0:2 offset1:6\n" \
  "global_load_dwordx2 v[204:205], v179, s[16:17]\n" \
  "global_load_dwordx2 v[206:207], v179, s[18:19]\n" \
  "v_mov_b32 v179, 0\n" \
  "global_load_ushort v179, v179, s[20:21]\n" \
  "s_add_u32 s46, s44, 4800\n" \
  "s_addc_u32 s47, s45, 0\n" \
  "global_load_dwordx4 v[208:211], v96, s[44:45] offset:-3200\n" \
  "global_load_dwordx4 v[212:215], v97, s[44:45] offset:-2944\n" \
  "global_load_dwordx4 v[216:219], v96, s[44:45] offset:-1600\n" \
  "global_load_dwordx4 v[220:223], v97, s[44:45] offset:-1344\n" \
  "global_load_dwordx4 v[224:227], v96, s[44:45] offset:0\n" \
  "global_load_dwordx4 v[228:231], v97, s[44:45] offset:256\n" \
  "global_load_dwordx4 v[232:235], v96, s[44:45] offset:1600\n" \
  "global_load_dwordx4 v[236:239], v97, s[44:45] offset:1856\n" \
  "global_load_dwordx4 v[240:243], v96, s[44:45] offset:3200\n" \
  "global_load_dwordx4 v[244:247], v97, s[44:45] offset:3456\n" \
  "global_load_dwordx4 v[248:251], v96, s[46:47] offset:0\n" \
  "global_load_dwordx4 v[252:255], v97, s[46:47] offset:256\n" \
  "ds_read_b128 v[180:183], v194\n" \
  "v_add_u32 v194, 0x100, v194\n" \
  "L1_:\n" \
  "ds_read_b32 v99, v98 offset:12\n" \
  "s_waitcnt lgkmcnt(0)\n" \
  "v_readfirstlane_b32 s35, v99\n" \
  "s_cmp_lt_u32 s35, 4\n" \
  "s_cbranch_scc1 L1_\n" \
  "s_setprio 1\n" \
  "v_mad_u32_u16 v184, v182, 16, v188\n" \
  "ds_read_b128 v[128:131], v184 offset:0\n" \
  "ds_read_b128 v[132:135], v184 offset:64\n" \
  "ds_read_b128 v[172:175], v191\n" \
  "v_mad_u32_u16 v185, v181, 16, v188\n" \
  "v_mad_u32_u16 v186, v182, 16, v188 op_sel:[1,0,0,0]\n" \
  "ds_read_b128 v[112:115], v185 offset:0\n" \
  "ds_read_b128 v[116:119], v185 offset:64\n" \
  "ds_read_b128 v[136:139], v186 offset:0\n" \
  "ds_read_b128 v[140:143], v186 offset:64\n" \
  "ds_read_b128 v[168:171], v191\n" \
  "v_mad_u32_u16 v187, v181, 16, v188 op_sel:[1,0,0,0]\n" \
  "v_mad_u32_u16 v184, v180, 16, v188 op_sel:[1,0,0,0]\n" \
  "ds_read_b128 v[120:123], v187 offset:0\n" \
  "v_mad_u32_u16 v185, v180, 16, v188\n" \
  "ds_read_b128 v[124:127], v187 offset:64\n" \
  "ds_read_b128 v[104:107], v184 offset:0\n" \
  "v_mad_u32_u16 v186, v183, 16, v189\n" \
  "ds_read_b128 v[108:111], v184 offset:64\n" \
  "ds_read_b128 v[96:99], v185 offset:0\n" \
  "s_waitcnt lgkmcnt(9)\n" \
  "s_mov_b32 s51, 3\n" \
  "s_mov_b32 s50, 8\n" \
  "s_call_b64 s[52:53], LA_loop\n" \
  "LA_ev0:\n" \
  "s_waitcnt vmcnt(10)\n" \
  "v_cvt_pk_u16_u32 v208, v208, v209\n" \
  "v_cvt_pk_u16_u32 v210, v210, v211\n" \
  "v_cvt_pk_u16_u32 v212, v212, v213\n" \
  "v_cvt_pk_u16_u32 v214, v214, v215\n" \
  "v_pk_lshrrev_b16 v209, 3, v208 op_sel_hi:[0,1]\n" \
  "v_pk_lshrrev_b16 v211, 3, v210 op_sel_hi:[0,1]\n" \
  "v_pk_lshrrev_b16 v213, 3, v212 op_sel_hi:[0,1]\n" \
  "v_pk_lshrrev_b16 v215, 3, v214 op_sel_hi:[0,1]\n" \
  "v_pk_mad_u16 v208, v208, 8, v209 op_sel_hi:[1,0,1]\n" \
  "v_pk_mad_u16 v210, v210, 8, v211 op_sel_hi:[1,0,1]\n" \
  "v_pk_mad_u16 v212, v212, 8, v213 op_sel_hi:[1,0,1]\n" \
  "v_pk_mad_u16 v214, v214, 8, v215 op_sel_hi:[1,0,1]\n" \
  "v_alignbit_b32 v212, v212, v212, v192\n" \
  "v_alignbit_b32 v214, v214, v214, v192\n" \
  "ds_write2_b32 v195, v208, v210 offset0:0 offset1:4\n" \
  "ds_write2_b32 v195, v212, v214 offset0:2 offset1:6\n" \
  "v_mov_b32 v196, v178\n" \
  "v_readfirstlane_b32 s6, v179\n" \
  "v_mov_b32 v208, 163328\n" \
  "s_mov_b32 s51, 4\n" \
  "s_mov_b32 s50, 1\n" \
  "s_mov_b32 s38, 0x800\n" \
  "s_call_b64 s[52:53], LA_loop\n" \
  "LA_ev6:\n" \
  "v_mov_b32 v202, v178\n" \
  "v_readfirstlane_b32 s35, v209\n" \
  "s_cmp_ge_u32 s35, 128\n" \
  "s_cbranch_scc1 LA_pl\n" \
  "L2_:\n" \
  "ds_read_b32 v209, v208 offset:4\n" \
  "s_waitcnt lgkmcnt(0)\n" \
  "v_readfirstlane_b32 s35, v209\n" \
  "s_cmp_lt_u32 s35, 128\n" \
  "s_cbranch_scc1 L2_\n" \
  "LA_pl:\n" \
  "s_and_b32 s4, s35, 15\n" \
  "s_sub_u32 s4, s4, 4\n" \
  "s_bfe_u32 s5, s35, 0x30004\n" \
  "s_sub_u32 s5, s5, s29\n" \
  "s_cmp_le_i32 s4, 0\n" \
  "s_cbranch_scc1 LA_nt\n" \
  "s_mul_i32 s35, s5, 51200\n" \
  "s_ashr_i32 s36, s35, 31\n" \
  "s_add_u32 s46, s46, s35\n" \
  "s_addc_u32 s47, s47, s36\n" \
  "v_lshrrev_b32 v216, 4, v188\n" \
  "v_mul_u32_u24 v216, 400, v216\n" \
  "v_add_u32 v216, 6400, v216\n" \
  "v_bfe_u32 v218, v194, 4, 4\n" \
  "v_min_u32 v217, 8, v218\n" \
  "v_lshl_add_u32 v217, v217, 4, v216\n" \
  "v_lshl_add_u32 v216, v218, 4, v216\n" \
  "global_load_dwordx4 v[208:211], v216, s[46:47] offset:0\n" \
  "global_load_dwordx4 v[212:215], v217, s[46:47] offset:256\n" \
  "LA_nt:\n" \
  "s_lshl_b32 s5, s5, 7\n" \
  "s_sub_u32 s5, s5, s4\n" \
  "s_min_i32 s51, s4, 0\n" \
  "s_add_u32 s51, s51, 4\n" \
  "s_cmp_eq_u32 s51, 0\n" \
  "s_cbranch_scc1 LA_ev7\n" \
  "s_call_b64 s[52:53], LA_loop\n" \
  "LA_ev7:\n" \
  "v_mov_b32 v203, v178\n" \
  "s_cmp_le_i32 s4, 0\n" \
  "s_cbranch_scc1 LA_ev8\n" \
  "s_waitcnt vmcnt(0)\n" \
  "v_cvt_pk_u16_u32 v208, v208, v209\n" \
  "v_cvt_pk_u16_u32 v210, v210, v211\n" \
  "v_cvt_pk_u16_u32 v212, v212, v213\n" \
  "v_cvt_pk_u16_u32 v214, v214, v215\n" \
  "v_pk_lshrrev_b16 v209, 3, v208 op_sel_hi:[0,1]\n" \
  "v_pk_lshrrev_b16 v211, 3, v210 op_sel_hi:[0,1]\n" \
  "v_pk_lshrrev_b16 v213, 3, v212 op_sel_hi:[0,1]\n" \
  "v_pk_lshrrev_b16 v215, 3, v214 op_sel_hi:[0,1]\n" \
  "v_pk_mad_u16 v208, v208, 8, v209 op_sel_hi:[1,0,1]\n" \
  "v_pk_mad_u16 v210, v210, 8, v211 op_sel_hi:[1,0,1]\n" \
  "v_pk_mad_u16 v212, v212, 8, v213 op_sel_hi:[1,0,1]\n" \
  "v_pk_mad_u16 v214, v214, 8, v215 op_sel_hi:[1,0,1]\n" \
  "v_alignbit_b32 v212, v212, v212, v192\n" \
  "v_alignbit_b32 v214, v214, v214, v192\n" \
  "ds_write2_b32 v195, v208, v210 offset0:0 offset1:4\n" \
  "ds_write2_b32 v195, v212, v214 offset0:2 offset1:6\n" \
  "s_lshl_b32 s35, s4, 8\n" \
  "s_add_u32 s35, s35, 0x400\n" \
  "v_subrev_u32 v194, s35, v194\n" \
  "s_mov_b32 s51, s4\n" \
  "s_call_b64 s[52:53], LA_loop\n" \
  "LA_ev8:\n" \
  "v_mov_b32 v179, v178\n" \
  "ds_read_b128 v[100:103], v185 offset:64\n" \
  "ds_read2_b32 v[144:145], v186 offset0:0 offset1:2\n" \
  "ds_read2_b32 v[146:147], v186 offset0:4 offset1:6\n" \
  "v_mad_u32_u16 v187, v183, 16, v190\n" \
  "ds_read2_b32 v[148:149], v187 offset0:0 offset1:2\n" \
  "ds_read2_b32 v[150:151], v187 offset0:4 offset1:6\n" \
  "ds_read_b128 v[164:167], v191\n" \
  "s_lshr_b32 s35, s30, 2\n" \
  "s_lshl3_add_u32 s33, s29, s35\n" \
  "s_and_b32 s34, s30, 3\n" \
  "s_lshl_b32 s59, s27, 11\n" \
  "s_mul_i32 s35, s33, 1600000\n" \
  "s_add_u32 s54, s22, s35\n" \
  "s_addc_u32 s55, s23, 0\n" \
  "s_mul_i32 s35, s34, 819200\n" \
  "s_lshl1_add_u32 s35, s59, s35\n" \
  "s_add_u32 s35, s35, 102400000\n" \
  "s_add_u32 s56, s22, s35\n" \
  "s_addc_u32 s57, s23, 0\n" \
  "v_mbcnt_lo_u32_b32 v189, -1, 0\n" \
  "v_mbcnt_hi_u32_b32 v189, -1, v189\n" \
  "v_lshlrev_b32 v188, 4, v189\n" \
  "s_waitcnt lgkmcnt(6)\n" \
  "v_mfma_f32_16x16x32_bf16 v[152:155], v[112:115], v[112:115], 0\n" \
  "v_fmac_f32 v176, v156, v88\n" \
  "v_fmac_f32 v176, v157, v89\n" \
  "v_mfma_f32_16x16x32_bf16 v[152:155], v[116:119], v[116:119], v[152:155]\n" \
  "v_fmac_f32 v176, v158, v90\n" \
  "v_fmac_f32 v176, v159, v91\n" \
  "v_mfma_f32_16x16x32_bf16 v[156:159], v[128:131], v[128:131], 0\n" \
  "v_fmac_f32 v176, v160, v92\n" \
  "v_fmac_f32 v176, v161, v93\n" \
  "v_mfma_f32_16x16x32_bf16 v[156:159], v[132:135], v[132:135], v[156:159]\n" \
  "v_fmac_f32 v176, v162, v94\n" \
  "v_fma_f32 v177, v163, v95, v176\n" \
  "v_mfma_f32_16x16x32_bf16 v[160:163], v[128:131], v[136:139], 0\n" \
  "v_mul_f32 v176, v152, v0\n" \
  "v_fmac_f32 v176, v153, v1\n" \
  "v_mfma_f32_16x16x32_bf16 v[160:163], v[132:135], v[140:143], v[160:163]\n" \
  "v_fmac_f32 v176, v154, v2\n" \
  "v_fmac_f32 v176, v155, v3\n" \
  "v_mfma_f32_16x16x32_bf16 v[152:155], v[136:139], v[136:139], 0\n" \
  "v_fmac_f32 v176, v156, v4\n" \
  "v_fmac_f32 v176, v157, v5\n" \
  "v_mfma_f32_16x16x32_bf16 v[152:155], v[140:143], v[140:143], v[152:155]\n" \
  "v_fmac_f32 v176, v158, v6\n" \
  "v_fmac_f32 v176, v159, v7\n" \
  "s_waitcnt lgkmcnt(0)\n" \
  "s_setprio 3\n" \
  "s_barrier\n" \
  "global_load_dwordx4 v[208:211], v188, s[56:57] offset:0 sc1\n" \
  "global_load_dwordx4 v[212:215], v188, s[56:57] offset:1024 sc1\n" \
  "global_load_dwordx4 v[216:219], v188, s[56:57] offset:2048 sc1\n" \
  "global_load_dwordx4 v[220:223], v188, s[56:57] offset:3072 sc1\n" \
  "v_mfma_f32_16x16x32_bf16 v[156:159], v[112:115], v[136:139], 0\n" \
  "v_fmac_f32 v176, v160, v8\n" \
  "v_fmac_f32 v176, v161, v9\n" \
  "v_lshrrev_b32 v180, 1, v189\n" \
  "v_mfma_f32_16x16x32_bf16 v[156:159], v[116:119], v[140:143], v[156:159]\n" \
  "v_fmac_f32 v176, v162, v10\n" \
  "v_fmac_f32 v176, v163, v11\n" \
  "v_xor_b32 v180, v180, v189\n" \
  "v_mfma_f32_16x16x32_bf16 v[160:163], v[112:115], v[128:131], 0\n" \
  "v_fmac_f32 v176, v152, v12\n" \
  "v_fmac_f32 v176, v153, v13\n" \
  "v_add_f32_dpp v177, v177, v177 row_ror:8 row_mask:0xf bank_mask:0xf\n" \
  "v_and_b32 v180, 8, v180\n" \
  "v_mfma_f32_16x16x32_bf16 v[160:163], v[116:119], v[132:135], v[160:163]\n" \
  "v_fmac_f32 v176, v154, v14\n" \
  "v_fmac_f32 v176, v155, v15\n" \
  "v_bfe_u32 v181, v189, 2, 1\n" \
  "v_mfma_f32_16x16x32_bf16 v[152:155], v[112:115], v[120:123], 0\n" \
  "v_fmac_f32 v176, v156, v16\n" \
  "v_fmac_f32 v176, v157, v17\n" \
  "v_lshlrev_b32_e64 v181, v181, 5\n" \
  "v_mfma_f32_16x16x32_bf16 v[152:155], v[116:119], v[124:127], v[152:155]\n" \
  "v_fmac_f32 v176, v158, v18\n" \
  "v_fmac_f32 v176, v159, v19\n" \
  "v_mov_b32_dpp v178, v178 row_ror:4 row_mask:0xf bank_mask:0xf\n" \
  "v_lshlrev_b32 v180, v180, v181\n" \
  "v_mfma_f32_16x16x32_bf16 v[156:159], v[120:123], v[136:139], 0\n" \
  "v_fmac_f32 v176, v160, v20\n" \
  "v_fmac_f32 v176, v161, v21\n" \
  "v_bfe_u32 v181, v189, 2, 2\n" \
  "v_mfma_f32_16x16x32_bf16 v[156:159], v[124:127], v[140:143], v[156:159]\n" \
  "v_fmac_f32 v176, v162, v22\n" \
  "v_fmac_f32 v176, v163, v23\n" \
  "v_lshlrev_b32_e64 v181, v181, 1\n" \
  "v_mfma_f32_16x16x32_bf16 v[160:163], v[104:107], v[128:131], 0\n" \
  "v_fmac_f32 v176, v152, v24\n" \
  "v_fmac_f32 v176, v153, v25\n" \
  "v_cmp_gt_u32 vcc, 32, v189\n" \
  "v_mfma_f32_16x16x32_bf16 v[160:163], v[108:111], v[132:135], v[160:163]\n" \
  "v_fmac_f32 v176, v154, v26\n" \
  "v_fmac_f32 v176, v155, v27\n" \
  "v_mov_b32 v191, 0x3f80\n" \
  "v_mfma_f32_16x16x32_bf16 v[152:155], v[120:123], v[128:131], 0\n" \
  "v_fmac_f32 v176, v156, v28\n" \
  "v_fmac_f32 v176, v157, v29\n" \
  "v_add_u32 v190, 32000, v188\n" \
  "v_mfma_f32_16x16x32_bf16 v[152:155], v[124:127], v[132:135], v[152:155]\n" \
  "v_fmac_f32 v176, v158, v30\n" \
  "v_fmac_f32 v176, v159, v31\n" \
  "v_add_f32_dpp v178, v177, v177 row_ror:4 row_mask:0xf bank_mask:0x1\n" \
  "v_cndmask_b32 v192, v181, v180, vcc\n" \
  "v_mfma_f32_16x16x32_bf16 v[156:159], v[96:99], v[128:131], 0\n" \
  "v_fmac_f32 v176, v160, v32\n" \
  "v_fmac_f32 v176, v161, v33\n" \
  "v_bfe_i32 v180, v192, 0, 1\n" \
  "v_mfma_f32_16x16x32_bf16 v[156:159], v[100:103], v[132:135], v[156:159]\n" \
  "v_fmac_f32 v176, v162, v34\n" \
  "v_fmac_f32 v176, v163, v35\n" \
  "v_and_b32 v180, v180, v191\n" \
  "v_smfmac_f32_16x16x64_bf16 v[172:175], v[144:147], v[128:135], v193\n" \
  "v_fmac_f32 v176, v152, v36\n" \
  "v_fmac_f32 v176, v153, v37\n" \
  "v_bfe_i32 v181, v192, 1, 1\n" \
  "v_smfmac_f32_16x16x64_bf16 v[172:175], v[148:151], v[136:143], v193\n" \
  "v_fmac_f32 v176, v154, v38\n" \
  "v_fmac_f32 v176, v155, v39\n" \
  "v_and_b32 v181, v181, v191\n" \
  "v_mfma_f32_16x16x32_bf16 v[160:163], v[96:99], v[112:115], 0\n" \
  "v_fmac_f32 v176, v156, v40\n" \
  "v_fmac_f32 v176, v157, v41\n" \
  "v_bfe_i32 v182, v192, 2, 1\n" \
  "v_mfma_f32_16x16x32_bf16 v[160:163], v[100:103], v[116:119], v[160:163]\n" \
  "v_fmac_f32 v176, v158, v42\n" \
  "v_fmac_f32 v176, v159, v43\n" \
  "v_and_b32 v182, v182, v191\n" \
  "v_mfma_f32_16x16x32_bf16 v[152:155], v[104:107], v[112:115], 0\n" \
  "v_fmac_f32 v176, v172, v44\n" \
  "v_fmac_f32 v176, v173, v45\n" \
  "v_bfe_i32 v183, v192, 3, 1\n" \
  "v_mfma_f32_16x16x32_bf16 v[152:155], v[108:111], v[116:119], v[152:155]\n" \
  "v_fmac_f32 v176, v174, v46\n" \
  "v_fmac_f32 v176, v175, v47\n" \
  "v_and_b32 v183, v183, v191\n" \
  "v_mfma_f32_16x16x32_bf16 v[156:159], v[104:107], v[136:139], 0\n" \
  "v_fmac_f32 v176, v160, v48\n" \
  "v_fmac_f32 v176, v161, v49\n" \
  "v_and_b32 v192, 3, v189\n" \
  "v_mfma_f32_16x16x32_bf16 v[156:159], v[108:111], v[140:143], v[156:159]\n" \
  "v_fmac_f32 v176, v162, v50\n" \
  "v_fmac_f32 v176, v163, v51\n" \
  "v_mul_u32_u24 v192, 0x5555, v192\n" \
  "v_smfmac_f32_16x16x64_bf16 v[168:171], v[144:147], v[112:119], v193\n" \
  "v_fmac_f32 v176, v152, v52\n" \
  "v_fmac_f32 v176, v153, v53\n" \
  "v_add_u32 v189, 16000, v188\n" \
  "v_smfmac_f32_16x16x64_bf16 v[168:171], v[148:151], v[120:127], v193\n" \
  "v_fmac_f32 v176, v154, v54\n" \
  "v_fmac_f32 v176, v155, v55\n" \
  "v_add_u32 v191, 48000, v188\n" \
  "v_mfma_f32_16x16x32_bf16 v[160:163], v[96:99], v[136:139], 0\n" \
  "v_fmac_f32 v176, v156, v56\n" \
  "v_fmac_f32 v176, v157, v57\n" \
  "v_mfma_f32_16x16x32_bf16 v[160:163], v[100:103], v[140:143], v[160:163]\n" \
  "v_fmac_f32 v176, v158, v58\n" \
  "v_fmac_f32 v176, v159, v59\n" \
  "v_smfmac_f32_16x16x64_bf16 v[164:167], v[148:151], v[104:111], v193\n" \
  "v_fmac_f32 v176, v168, v60\n" \
  "v_fmac_f32 v176, v169, v61\n" \
  "v_smfmac_f32_16x16x64_bf16 v[164:167], v[144:147], v[96:103], v193\n" \
  "v_fmac_f32 v176, v170, v62\n" \
  "v_fmac_f32 v176, v171, v63\n" \
  "v_mfma_f32_16x16x32_bf16 v[152:155], v[120:123], v[120:123], 0\n" \
  "v_fmac_f32 v176, v160, v64\n" \
  "v_fmac_f32 v176, v161, v65\n" \
  "v_mfma_f32_16x16x32_bf16 v[152:155], v[124:127], v[124:127], v[152:155]\n" \
  "v_fmac_f32 v176, v162, v66\n" \
  "v_fmac_f32 v176, v163, v67\n" \
  "v_mfma_f32_16x16x32_bf16 v[156:159], v[104:107], v[120:123], 0\n" \
  "v_fmac_f32 v176, v164, v68\n" \
  "v_fmac_f32 v176, v165, v69\n" \
  "v_mfma_f32_16x16x32_bf16 v[156:159], v[108:111], v[124:127], v[156:159]\n" \
  "v_fmac_f32 v176, v166, v70\n" \
  "v_fmac_f32 v176, v167, v71\n" \
  "v_mfma_f32_16x16x32_bf16 v[160:163], v[96:99], v[120:123], 0\n" \
  "v_fmac_f32 v176, v152, v72\n" \
  "v_fmac_f32 v176, v153, v73\n" \
  "v_mfma_f32_16x16x32_bf16 v[160:163], v[100:103], v[124:127], v[160:163]\n" \
  "v_fmac_f32 v176, v154, v74\n" \
  "v_fmac_f32 v176, v155, v75\n" \
  "v_mfma_f32_16x16x32_bf16 v[152:155], v[96:99], v[104:107], 0\n" \
  "v_fmac_f32 v176, v156, v76\n" \
  "v_fmac_f32 v176, v157, v77\n" \
  "v_mfma_f32_16x16x32_bf16 v[152:155], v[100:103], v[108:111], v[152:155]\n" \
  "v_fmac_f32 v176, v158, v78\n" \
  "v_fmac_f32 v176, v159, v79\n" \
  "v_mfma_f32_16x16x32_bf16 v[156:159], v[104:107], v[104:107], 0\n" \
  "v_fmac_f32 v176, v160, v80\n" \
  "v_fmac_f32 v176, v161, v81\n" \
  "v_mfma_f32_16x16x32_bf16 v[156:159], v[108:111], v[108:111], v[156:159]\n" \
  "v_fmac_f32 v176, v162, v82\n" \
  "v_fmac_f32 v176, v163, v83\n" \
  "s_add_u32 s35, s59, 64000\n" \
  "s_add_u32 s54, s54, s35\n" \
  "s_addc_u32 s55, s55, 0\n" \
  "s_add_u32 s58, s59, 0x10000\n" \
  "s_mov_b32 m0, s58\n" \
  "s_add_u32 s35, s58, 0x8000\n" \
  "global_load_lds_dwordx4 v188, s[54:55] sc1\n" \
  "global_load_lds_dwordx4 v188, s[54:55] offset:1024 sc1\n" \
  "v_mfma_f32_16x16x32_bf16 v[160:163], v[148:151], v[144:147], 0\n" \
  "v_fmac_f32 v176, v152, v84\n" \
  "v_fmac_f32 v176, v153, v85\n" \
  "v_mfma_f32_16x16x32_bf16 v[160:163], v[96:99], v[96:99], v[160:163]\n" \
  "v_fmac_f32 v176, v154, v86\n" \
  "v_fmac_f32 v176, v155, v87\n" \
  "s_add_u32 m0, s58, 0x4000\n" \
  "s_cmp_lt_u32 s35, 0x28000\n" \
  "global_load_lds_dwordx4 v189, s[54:55] sc1\n" \
  "global_load_lds_dwordx4 v189, s[54:55] offset:1024 sc1\n" \
  "s_cselect_b32 s58, s35, s59\n" \
  "s_add_u32 s54, s54, 32000\n" \
  "s_addc_u32 s55, s55, 0\n" \
  "v_mfma_f32_16x16x32_bf16 v[160:163], v[100:103], v[100:103], v[160:163]\n" \
  "s_mov_b32 s51, 7\n" \
  "v_fmac_f32 v176, v156, v88\n" \
  "v_fmac_f32 v176, v157, v89\n" \
  "v_fmac_f32 v176, v158, v90\n" \
  "v_fmac_f32 v176, v159, v91\n" \
  "s_nop 2\n" \
  "v_fmac_f32 v176, v160, v92\n" \
  "v_fmac_f32 v176, v161, v93\n" \
  "v_fmac_f32 v176, v162, v94\n" \
  "v_fma_f32 v177, v163, v95, v176\n" \
  "s_nop 1\n" \
  "v_add_f32_dpp v177, v177, v177 row_ror:8 row_mask:0xf bank_mask:0xf\n" \
  "v_mov_b32_dpp v178, v178 row_ror:4 row_mask:0xf bank_mask:0xf\n" \
  "s_nop 1\n" \
  "v_add_f32_dpp v178, v177, v177 row_ror:4 row_mask:0xf bank_mask:0x1\n" \
  "v_mov_b32 v99, v179\n" \
  "s_nop 0\n" \
  "v_mov_b32_dpp v196, v178 row_ror:8 row_mask:0xf bank_mask:0xc\n" \
  "s_nop 1\n" \
  "v_permlane16_swap_b32 v196, v197\n" \
  "v_permlane16_swap_b32 v198, v199\n" \
  "v_permlane16_swap_b32 v200, v201\n" \
  "v_permlane16_swap_b32 v202, v203\n" \
  "v_permlane16_swap_b32 v179, v99\n" \
  "v_add_f32 v196, v196, v197\n" \
  "v_add_f32 v198, v198, v199\n" \
  "v_add_f32 v200, v200, v201\n" \
  "v_add_f32 v202, v202, v203\n" \
  "v_add_f32 v179, v179, v99\n" \
  "v_lshrrev_b32 v96, 4, v188\n" \
  "v_and_b32 v97, 1, v96\n" \
  "v_mov_b32 v99, v179\n" \
  "v_lshrrev_b32 v98, 4, v96\n" \
  "v_permlane32_swap_b32 v196, v198\n" \
  "v_permlane32_swap_b32 v200, v202\n" \
  "v_permlane32_swap_b32 v179, v99\n" \
  "v_add_f32 v196, v196, v198\n" \
  "v_add_f32 v200, v200, v202\n" \
  "v_add_f32 v179, v179, v99\n" \
  "v_cmp_eq_u32 vcc, 1, v97\n" \
  "v_lshlrev_b32 v97, 2, v97\n" \
  "v_add_lshl_u32 v97, v97, v98, 2\n" \
  "v_bfe_u32 v98, v96, 2, 2\n" \
  "v_add_f32_dpp v196, v196, v196 quad_perm:[1,0,3,2] row_mask:0xf bank_mask:0xf\n" \
  "v_add_f32_dpp v200, v200, v200 quad_perm:[1,0,3,2] row_mask:0xf bank_mask:0xf\n" \
  "v_add_f32_dpp v179, v179, v179 quad_perm:[1,0,3,2] row_mask:0xf bank_mask:0xf\n" \
  "v_sub_u32 v97, v97, v98\n" \
  "v_cndmask_b32 v196, v196, v200, vcc\n" \
  "v_add_u32 v97, 1, v97\n" \
  "s_min_i32 s35, s4, 0\n" \
  "s_add_u32 s36, s4, 32\n" \
  "s_add_u32 s37, s4, 29\n" \
  "v_add_f32_dpp v179, v179, v179 quad_perm:[2,3,0,1] row_mask:0xf bank_mask:0xf\n" \
  "v_add_f32_dpp v196, v196, v196 quad_perm:[2,3,0,1] row_mask:0xf bank_mask:0xf\n" \
  "s_mov_b32 exec_lo, 0x0\n" \
  "s_mov_b32 exec_hi, 0xaaaa0000\n" \
  "v_add_u32 v97, s35, v97\n" \
  "s_mov_b32 exec_lo, 0x5500\n" \
  "s_mov_b32 exec_hi, 0x0\n" \
  "v_add_u32 v97, s36, v97\n" \
  "s_mov_b32 exec_lo, 0x4444\n" \
  "s_mov_b32 exec_hi, 0x0\n" \
  "v_sub_u32 v97, s37, v98\n" \
  "v_mov_b32 v196, v179\n" \
  "s_mov_b64 exec, -1\n" \
  "v_cmp_lt_u32 vcc, 31, v97\n" \
  "v_add_u32 v98, s5, v97\n" \
  "s_lshl2_add_u32 s35, s7, s28\n" \
  "s_lshl_b32 s35, s35, 5\n" \
  "v_cndmask_b32 v97, v97, v98, vcc\n" \
  "v_add_lshl_u32 v97, v97, s35, 2\n" \
  "s_lshl_b32 s35, s31, 16\n" \
  "s_add_u32 s35, s35, 109871104\n" \
  "s_add_u32 s40, s22, s35\n" \
  "s_addc_u32 s41, s23, 0\n" \
  "v_min_u32 v196, -2, v196\n" \
  "v_lshlrev_b32 v184, 16, v204\n" \
  "v_and_b32 v185, 0xffff0000, v204\n" \
  "v_lshlrev_b32 v186, 16, v205\n" \
  "v_and_b32 v187, 0xffff0000, v205\n" \
  "s_barrier\n" \
  "s_add_u32 s56, s56, 32768\n" \
  "s_addc_u32 s57, s57, 0\n" \
  "global_load_dwordx4 v[224:227], v188, s[56:57] sc1\n" \
  "global_load_dwordx4 v[228:231], v188, s[56:57] offset:1024 sc1\n" \
  "global_load_dwordx4 v[232:235], v188, s[56:57] offset:2048 sc1\n" \
  "global_load_dwordx4 v[236:239], v188, s[56:57] offset:3072 sc1\n" \
  "s_add_u32 s56, s56, 32768\n" \
  "s_addc_u32 s57, s57, 0\n" \
  "s_mov_b32 exec_lo, 0x33337777\n" \
  "s_mov_b32 exec_hi, 0x33333333\n" \
  "global_store_dword v97, v196, s[40:41] sc1\n" \
  "s_mov_b64 exec, -1\n" \
  "s_waitcnt vmcnt(9)\n" \
  "s_setprio 0\n" \
  "s_branch LG_start\n" \
  ".p2align 6\n" \
  "LA_loop:\n" \
  "LA_mid:\n" \
  "v_mfma_f32_16x16x32_bf16 v[152:155], v[112:115], v[112:115], 0\n" \
  "s_waitcnt lgkmcnt(8)\n" \
  "ds_read_b128 v[100:103], v185 offset:64\n" \
  "v_fmac_f32 v176, v156, v88\n" \
  "v_fmac_f32 v176, v157, v89\n" \
  "v_mfma_f32_16x16x32_bf16 v[152:155], v[116:119], v[116:119], v[152:155]\n" \
  "ds_read2_b32 v[144:145], v186 offset0:0 offset1:2\n" \
  "v_fmac_f32 v176, v158, v90\n" \
  "v_fmac_f32 v176, v159, v91\n" \
  "v_mfma_f32_16x16x32_bf16 v[156:159], v[128:131], v[128:131], 0\n" \
  "ds_read2_b32 v[146:147], v186 offset0:4 offset1:6\n" \
  "v_fmac_f32 v176, v160, v92\n" \
  "v_fmac_f32 v176, v161, v93\n" \
  "v_mad_u32_u16 v187, v183, 16, v190\n" \
  "v_mfma_f32_16x16x32_bf16 v[156:159], v[132:135], v[132:135], v[156:159]\n" \
  "s_waitcnt lgkmcnt(10)\n" \
  "ds_read2_b32 v[148:149], v187 offset0:0 offset1:2\n" \
  "v_fmac_f32 v176, v162, v94\n" \
  "v_fma_f32 v177, v163, v95, v176\n" \
  "v_mfma_f32_16x16x32_bf16 v[160:163], v[128:131], v[136:139], 0\n" \
  "s_waitcnt lgkmcnt(10)\n" \
  "ds_read2_b32 v[150:151], v187 offset0:4 offset1:6\n" \
  "v_mul_f32 v176, v152, v0\n" \
  "v_fmac_f32 v176, v153, v1\n" \
  "v_mfma_f32_16x16x32_bf16 v[160:163], v[132:135], v[140:143], v[160:163]\n" \
  "ds_read_b128 v[180:183], v194\n" \
  "v_fmac_f32 v176, v154, v2\n" \
  "v_fmac_f32 v176, v155, v3\n" \
  "v_mfma_f32_16x16x32_bf16 v[152:155], v[136:139], v[136:139], 0\n" \
  "v_fmac_f32 v176, v156, v4\n" \
  "v_fmac_f32 v176, v157, v5\n" \
  "v_mfma_f32_16x16x32_bf16 v[152:155], v[140:143], v[140:143], v[152:155]\n" \
  "ds_read_b128 v[164:167], v191\n" \
  "v_fmac_f32 v176, v158, v6\n" \
  "v_fmac_f32 v176, v159, v7\n" \
  "v_mfma_f32_16x16x32_bf16 v[156:159], v[112:115], v[136:139], 0\n" \
  "v_fmac_f32 v176, v160, v8\n" \
  "v_fmac_f32 v176, v161, v9\n" \
  "v_mfma_f32_16x16x32_bf16 v[156:159], v[116:119], v[140:143], v[156:159]\n" \
  "v_fmac_f32 v176, v162, v10\n" \
  "v_fmac_f32 v176, v163, v11\n" \
  "v_mfma_f32_16x16x32_bf16 v[160:163], v[112:115], v[128:131], 0\n" \
  "v_fmac_f32 v176, v152, v12\n" \
  "v_fmac_f32 v176, v153, v13\n" \
  "v_add_f32_dpp v177, v177, v177 row_ror:8 row_mask:0xf bank_mask:0xf\n" \
  "v_mfma_f32_16x16x32_bf16 v[160:163], v[116:119], v[132:135], v[160:163]\n" \
  "s_waitcnt lgkmcnt(11)\n" \
  "v_fmac_f32 v176, v154, v14\n" \
  "v_fmac_f32 v176, v155, v15\n" \
  "v_mfma_f32_16x16x32_bf16 v[152:155], v[112:115], v[120:123], 0\n" \
  "s_waitcnt lgkmcnt(10)\n" \
  "v_fmac_f32 v176, v156, v16\n" \
  "v_fmac_f32 v176, v157, v17\n" \
  "v_mfma_f32_16x16x32_bf16 v[152:155], v[116:119], v[124:127], v[152:155]\n" \
  "v_fmac_f32 v176, v158, v18\n" \
  "v_fmac_f32 v176, v159, v19\n" \
  "v_mov_b32_dpp v178, v178 row_ror:4 row_mask:0xf bank_mask:0xf\n" \
  "v_mfma_f32_16x16x32_bf16 v[156:159], v[120:123], v[136:139], 0\n" \
  "v_fmac_f32 v176, v160, v20\n" \
  "v_fmac_f32 v176, v161, v21\n" \
  "v_mfma_f32_16x16x32_bf16 v[156:159], v[124:127], v[140:143], v[156:159]\n" \
  "s_waitcnt lgkmcnt(9)\n" \
  "v_fmac_f32 v176, v162, v22\n" \
  "v_fmac_f32 v176, v163, v23\n" \
  "v_mfma_f32_16x16x32_bf16 v[160:163], v[104:107], v[128:131], 0\n" \
  "s_waitcnt lgkmcnt(1)\n" \
  "v_fmac_f32 v176, v152, v24\n" \
  "v_fmac_f32 v176, v153, v25\n" \
  "v_mad_u32_u16 v184, v182, 16, v188\n" \
  "v_mfma_f32_16x16x32_bf16 v[160:163], v[108:111], v[132:135], v[160:163]\n" \
  "v_fmac_f32 v176, v154, v26\n" \
  "v_fmac_f32 v176, v155, v27\n" \
  "v_mfma_f32_16x16x32_bf16 v[152:155], v[120:123], v[128:131], 0\n" \
  "v_fmac_f32 v176, v156, v28\n" \
  "v_fmac_f32 v176, v157, v29\n" \
  "v_mfma_f32_16x16x32_bf16 v[152:155], v[124:127], v[132:135], v[152:155]\n" \
  "v_fmac_f32 v176, v158, v30\n" \
  "v_fmac_f32 v176, v159, v31\n" \
  "v_add_f32_dpp v178, v177, v177 row_ror:4 row_mask:0xf bank_mask:0x1\n" \
  "v_mfma_f32_16x16x32_bf16 v[156:159], v[96:99], v[128:131], 0\n" \
  "v_fmac_f32 v176, v160, v32\n" \
  "v_fmac_f32 v176, v161, v33\n" \
  "v_mfma_f32_16x16x32_bf16 v[156:159], v[100:103], v[132:135], v[156:159]\n" \
  "v_fmac_f32 v176, v162, v34\n" \
  "v_fmac_f32 v176, v163, v35\n" \
  "v_smfmac_f32_16x16x64_bf16 v[172:175], v[144:147], v[128:135], v193\n" \
  "ds_read_b128 v[128:131], v184 offset:0\n" \
  "v_fmac_f32 v176, v152, v36\n" \
  "v_fmac_f32 v176, v153, v37\n" \
  "v_smfmac_f32_16x16x64_bf16 v[172:175], v[148:151], v[136:143], v193\n" \
  "ds_read_b128 v[132:135], v184 offset:64\n" \
  "v_fmac_f32 v176, v154, v38\n" \
  "v_fmac_f32 v176, v155, v39\n" \
  "v_mfma_f32_16x16x32_bf16 v[160:163], v[96:99], v[112:115], 0\n" \
  "v_fmac_f32 v176, v156, v40\n" \
  "v_fmac_f32 v176, v157, v41\n" \
  "v_mfma_f32_16x16x32_bf16 v[160:163], v[100:103], v[116:119], v[160:163]\n" \
  "v_fmac_f32 v176, v158, v42\n" \
  "v_fmac_f32 v176, v159, v43\n" \
  "v_add_u32 v194, 0x100, v194\n" \
  "v_mfma_f32_16x16x32_bf16 v[152:155], v[104:107], v[112:115], 0\n" \
  "v_fmac_f32 v176, v172, v44\n" \
  "v_fmac_f32 v176, v173, v45\n" \
  "v_mfma_f32_16x16x32_bf16 v[152:155], v[108:111], v[116:119], v[152:155]\n" \
  "v_fmac_f32 v176, v174, v46\n" \
  "v_fmac_f32 v176, v175, v47\n" \
  "v_mfma_f32_16x16x32_bf16 v[156:159], v[104:107], v[136:139], 0\n" \
  "ds_read_b128 v[172:175], v191\n" \
  "v_fmac_f32 v176, v160, v48\n" \
  "v_fmac_f32 v176, v161, v49\n" \
  "v_mad_u32_u16 v185, v181, 16, v188\n" \
  "v_mfma_f32_16x16x32_bf16 v[156:159], v[108:111], v[140:143], v[156:159]\n" \
  "v_fmac_f32 v176, v162, v50\n" \
  "v_fmac_f32 v176, v163, v51\n" \
  "v_mad_u32_u16 v186, v182, 16, v188 op_sel:[1,0,0,0]\n" \
  "v_smfmac_f32_16x16x64_bf16 v[168:171], v[144:147], v[112:119], v193\n" \
  "ds_read_b128 v[112:115], v185 offset:0\n" \
  "v_fmac_f32 v176, v152, v52\n" \
  "v_fmac_f32 v176, v153, v53\n" \
  "v_smfmac_f32_16x16x64_bf16 v[168:171], v[148:151], v[120:127], v193\n" \
  "ds_read_b128 v[116:119], v185 offset:64\n" \
  "v_fmac_f32 v176, v154, v54\n" \
  "v_fmac_f32 v176, v155, v55\n" \
  "v_mfma_f32_16x16x32_bf16 v[160:163], v[96:99], v[136:139], 0\n" \
  "v_fmac_f32 v176, v156, v56\n" \
  "v_fmac_f32 v176, v157, v57\n" \
  "v_mfma_f32_16x16x32_bf16 v[160:163], v[100:103], v[140:143], v[160:163]\n" \
  "s_waitcnt lgkmcnt(5)\n" \
  "v_fmac_f32 v176, v158, v58\n" \
  "v_fmac_f32 v176, v159, v59\n" \
  "v_smfmac_f32_16x16x64_bf16 v[164:167], v[148:151], v[104:111], v193\n" \
  "ds_read_b128 v[136:139], v186 offset:0\n" \
  "v_fmac_f32 v176, v168, v60\n" \
  "v_fmac_f32 v176, v169, v61\n" \
  "v_smfmac_f32_16x16x64_bf16 v[164:167], v[144:147], v[96:103], v193\n" \
  "ds_read_b128 v[140:143], v186 offset:64\n" \
  "v_fmac_f32 v176, v170, v62\n" \
  "v_fmac_f32 v176, v171, v63\n" \
  "v_mfma_f32_16x16x32_bf16 v[152:155], v[120:123], v[120:123], 0\n" \
  "v_fmac_f32 v176, v160, v64\n" \
  "v_fmac_f32 v176, v161, v65\n" \
  "v_mfma_f32_16x16x32_bf16 v[152:155], v[124:127], v[124:127], v[152:155]\n" \
  "ds_read_b128 v[168:171], v191\n" \
  "v_fmac_f32 v176, v162, v66\n" \
  "v_fmac_f32 v176, v163, v67\n" \
  "v_mad_u32_u16 v187, v181, 16, v188 op_sel:[1,0,0,0]\n" \
  "v_mfma_f32_16x16x32_bf16 v[156:159], v[104:107], v[120:123], 0\n" \
  "v_fmac_f32 v176, v164, v68\n" \
  "v_fmac_f32 v176, v165, v69\n" \
  "v_mad_u32_u16 v184, v180, 16, v188 op_sel:[1,0,0,0]\n" \
  "v_mfma_f32_16x16x32_bf16 v[156:159], v[108:111], v[124:127], v[156:159]\n" \
  "v_fmac_f32 v176, v166, v70\n" \
  "v_fmac_f32 v176, v167, v71\n" \
  "v_mfma_f32_16x16x32_bf16 v[160:163], v[96:99], v[120:123], 0\n" \
  "ds_read_b128 v[120:123], v187 offset:0\n" \
  "v_fmac_f32 v176, v152, v72\n" \
  "v_fmac_f32 v176, v153, v73\n" \
  "v_mfma_f32_16x16x32_bf16 v[160:163], v[100:103], v[124:127], v[160:163]\n" \
  "v_fmac_f32 v176, v154, v74\n" \
  "v_fmac_f32 v176, v155, v75\n" \
  "v_mad_u32_u16 v185, v180, 16, v188\n" \
  "v_mfma_f32_16x16x32_bf16 v[152:155], v[96:99], v[104:107], 0\n" \
  "ds_read_b128 v[124:127], v187 offset:64\n" \
  "v_fmac_f32 v176, v156, v76\n" \
  "v_fmac_f32 v176, v157, v77\n" \
  "v_mfma_f32_16x16x32_bf16 v[152:155], v[100:103], v[108:111], v[152:155]\n" \
  "v_fmac_f32 v176, v158, v78\n" \
  "v_fmac_f32 v176, v159, v79\n" \
  "v_mfma_f32_16x16x32_bf16 v[156:159], v[104:107], v[104:107], 0\n" \
  "ds_read_b128 v[104:107], v184 offset:0\n" \
  "v_fmac_f32 v176, v160, v80\n" \
  "v_fmac_f32 v176, v161, v81\n" \
  "v_mfma_f32_16x16x32_bf16 v[156:159], v[108:111], v[108:111], v[156:159]\n" \
  "s_sub_u32 s51, s51, 1\n" \
  "v_fmac_f32 v176, v162, v82\n" \
  "v_fmac_f32 v176, v163, v83\n" \
  "v_mad_u32_u16 v186, v183, 16, v189\n" \
  "v_mfma_f32_16x16x32_bf16 v[160:163], v[148:151], v[144:147], 0\n" \
  "s_cmp_lg_u32 s51, 0\n" \
  "ds_read_b128 v[108:111], v184 offset:64\n" \
  "v_fmac_f32 v176, v152, v84\n" \
  "v_fmac_f32 v176, v153, v85\n" \
  "v_mfma_f32_16x16x32_bf16 v[160:163], v[96:99], v[96:99], v[160:163]\n" \
  "s_waitcnt lgkmcnt(8)\n" \
  "ds_read_b128 v[96:99], v185 offset:0\n" \
  "v_fmac_f32 v176, v154, v86\n" \
  "v_fmac_f32 v176, v155, v87\n" \
  "v_mfma_f32_16x16x32_bf16 v[160:163], v[100:103], v[100:103], v[160:163]\n" \
  "s_cbranch_scc1 LA_loop\n" \
  "s_cmp_ge_u32 s50, 6\n" \
  "s_cbranch_scc1 LA_ret\n" \
  "s_set_gpr_idx_on s50, gpr_idx(DST)\n" \
  "v_mov_b32 v196, v178\n" \
  "s_lshl_b32 s35, s50, 3\n" \
  "s_waitcnt vmcnt(0)\n" \
  "s_set_gpr_idx_on s35, gpr_idx(SRC0,SRC1)\n" \
  "v_cvt_pk_u16_u32 v210, v208, v209\n" \
  "v_cvt_pk_u16_u32 v211, v210, v211\n" \
  "v_cvt_pk_u16_u32 v212, v212, v213\n" \
  "v_cvt_pk_u16_u32 v213, v214, v215\n" \
  "s_set_gpr_idx_off\n" \
  "v_pk_lshrrev_b16 v214, 3, v210 op_sel_hi:[0,1]\n" \
  "v_pk_mad_u16 v210, v210, 8, v214 op_sel_hi:[1,0,1]\n" \
  "v_pk_lshrrev_b16 v215, 3, v211 op_sel_hi:[0,1]\n" \
  "v_pk_mad_u16 v211, v211, 8, v215 op_sel_hi:[1,0,1]\n" \
  "v_pk_lshrrev_b16 v214, 3, v212 op_sel_hi:[0,1]\n" \
  "v_pk_mad_u16 v212, v212, 8, v214 op_sel_hi:[1,0,1]\n" \
  "v_pk_lshrrev_b16 v215, 3, v213 op_sel_hi:[0,1]\n" \
  "v_pk_mad_u16 v213, v213, 8, v215 op_sel_hi:[1,0,1]\n" \
  "s_lshr_b32 s35, s38, 1\n" \
  "v_add_u32 v214, s35, v195\n" \
  "v_alignbit_b32 v212, v212, v212, v192\n" \
  "v_alignbit_b32 v213, v213, v213, v192\n" \
  "ds_write2_b32 v214, v210, v211 offset0:0 offset1:4\n" \
  "ds_write2_b32 v214, v212, v213 offset0:2 offset1:6\n" \
  "ds_read_b32 v209, v208 offset:4\n" \
  "v_subrev_u32 v194, s38, v194\n" \
  "s_xor_b32 s38, s38, 0x800\n" \
  "s_add_u32 s50, s50, 1\n" \
  "s_mov_b32 s51, 4\n" \
  "s_branch LA_loop\n" \
  "LA_ret:\n" \
  "s_setpc_b64 s[52:53]\n" \
  "LB_start:\n" \
  "s_setprio 2\n" \
  "s_lshl_b32 s35, s31, 7\n" \
  "s_waitcnt lgkmcnt(0)\n" \
  "s_add_u32 s40, s10, s35\n" \
  "s_addc_u32 s41, s11, 0\n" \
  "s_add_u32 s37, s36, 32\n" \
  "L3_:\n" \
  "s_sub_u32 s35, s36, 125\n" \
  "s_min_u32 s35, s35, s36\n" \
  "s_mul_i32 m0, s35, 1040\n" \
  "s_lshl_b32 s35, s35, 11\n" \
  "s_add_u32 s42, s40, s35\n" \
  "s_addc_u32 s43, s41, 0\n" \
  "global_load_lds_dwordx4 v254, s[42:43]\n" \
  "s_add_u32 s36, s36, 4\n" \
  "s_sub_u32 s35, s36, 125\n" \
  "s_min_u32 s35, s35, s36\n" \
  "s_mul_i32 m0, s35, 1040\n" \
  "s_lshl_b32 s35, s35, 11\n" \
  "s_add_u32 s42, s40, s35\n" \
  "s_addc_u32 s43, s41, 0\n" \
  "global_load_lds_dwordx4 v254, s[42:43]\n" \
  "s_add_u32 s36, s36, 4\n" \
  "s_cmp_lg_u32 s37, s36\n" \
  "s_cbranch_scc1 L3_\n" \
  "v_mov_b32 v190, 163328\n" \
  "v_mov_b64 v[2:3], 0\n" \
  "ds_write_b64 v190, v[2:3]\n" \
  "ds_write_b64 v190, v[2:3] offset:8\n" \
  "ds_write_b64 v190, v[2:3] offset:16\n" \
  "s_waitcnt lgkmcnt(0)\n" \
  "s_barrier\n" \
  "v_lshlrev_b32 v247, 7, v255\n" \
  "s_add_u32 s37, s36, 96\n" \
  "L4_:\n" \
  "s_sub_u32 s35, s36, 125\n" \
  "s_min_u32 s35, s35, s36\n" \
  "s_mul_i32 m0, s35, 1040\n" \
  "s_lshl_b32 s35, s35, 11\n" \
  "s_add_u32 s42, s40, s35\n" \
  "s_addc_u32 s43, s41, 0\n" \
  "global_load_lds_dwordx4 v254, s[42:43]\n" \
  "s_add_u32 s36, s36, 4\n" \
  "s_sub_u32 s35, s36, 125\n" \
  "s_min_u32 s35, s35, s36\n" \
  "s_mul_i32 m0, s35, 1040\n" \
  "s_lshl_b32 s35, s35, 11\n" \
  "s_add_u32 s42, s40, s35\n" \
  "s_addc_u32 s43, s41, 0\n" \
  "global_load_lds_dwordx4 v254, s[42:43]\n" \
  "s_add_u32 s36, s36, 4\n" \
  "s_cmp_lg_u32 s37, s36\n" \
  "s_cbranch_scc1 L4_\n" \
  "s_add_u32 s48, s22, 110002176\n" \
  "s_addc_u32 s49, s23, 0\n" \
  "s_lshl_b32 s35, s26, 2\n" \
  "v_mov_b32 v193, s35\n" \
  "global_load_dword v193, v193, s[48:49] offset:1024 sc1\n" \
  "v_mov_b32 v191, 1\n" \
  "v_and_b32 v252, 15, v255\n" \
  "v_lshrrev_b32 v253, 4, v255\n" \
  "s_lshl2_add_u32 s35, s31, s28\n" \
  "s_lshl_b32 s35, s35, 1\n" \
  "v_bfe_u32 v1, v255, 2, 1\n" \
  "v_add_u32 v1, s35, v1\n" \
  "v_and_b32 v2, 3, v255\n" \
  "v_lshlrev_b32 v3, 5, v1\n" \
  "v_lshl_add_u32 v3, v2, 3, v3\n" \
  "v_lshlrev_b32 v4, 2, v2\n" \
  "v_add_u32 v4, 380, v4\n" \
  "v_cmp_gt_u32 vcc, 12, v1\n" \
  "v_lshrrev_b32 v5, 3, v255\n" \
  "v_mul_u32_u24 v5, 6400, v5\n" \
  "v_cndmask_b32 v3, v4, v3, vcc\n" \
  "v_add_u32 v5, 3200, v5\n" \
  "v_add_u32 v227, v5, v3\n" \
  "v_lshl_add_u32 v3, v1, 2, v2\n" \
  "v_add_u32 v4, 48, v3\n" \
  "v_mul_u32_u24 v5, 205, v3\n" \
  "v_lshrrev_b32 v5, 11, v5\n" \
  "v_mul_u32_u24 v5, 10, v5\n" \
  "v_sub_u32 v5, v3, v5\n" \
  "v_lshlrev_b32 v236, 26, v5\n" \
  "v_mul_u32_u24 v5, 205, v4\n" \
  "v_lshrrev_b32 v5, 11, v5\n" \
  "v_mul_u32_u24 v5, 10, v5\n" \
  "v_sub_u32 v5, v4, v5\n" \
  "v_lshlrev_b32 v237, 26, v5\n" \
  "s_lshl2_add_u32 s4, s31, s28\n" \
  "s_lshl_b32 s4, s4, 7\n" \
  "s_add_u32 s36, s4, 96\n" \
  "s_min_u32 s36, s36, 984\n" \
  "s_add_u32 s37, s4, 112\n" \
  "s_min_u32 s37, s37, 984\n" \
  "v_add_u32 v186, s4, v252\n" \
  "v_add_u32 v187, s36, v252\n" \
  "v_add_u32 v188, s37, v252\n" \
  "s_mul_i32 s38, s7, 51200\n" \
  "s_add_u32 s42, s8, s38\n" \
  "s_addc_u32 s43, s9, 0\n" \
  "s_xor_b32 s38, s31, 1\n" \
  "s_lshl_b32 s38, s38, 7\n" \
  "s_add_u32 s94, s10, s38\n" \
  "s_addc_u32 s95, s11, 0\n" \
  "s_mul_i32 s5, s28, 4224\n" \
  "s_add_u32 s5, s5, 146432\n" \
  "v_lshrrev_b32 v2, 3, v186\n" \
  "v_lshl_add_u32 v2, v186, 3, v2\n" \
  "v_add_lshl_u32 v249, v2, v253, 4\n" \
  "v_lshrrev_b32 v2, 3, v187\n" \
  "v_lshl_add_u32 v2, v187, 3, v2\n" \
  "v_add_lshl_u32 v250, v2, v253, 4\n" \
  "v_lshrrev_b32 v2, 3, v188\n" \
  "v_lshl_add_u32 v2, v188, 3, v2\n" \
  "v_add_lshl_u32 v251, v2, v253, 4\n" \
  "v_mul_u32_u24 v1, 1600000, v253\n" \
  "v_lshl_add_u32 v186, v186, 4, v1\n" \
  "v_lshl_add_u32 v187, v187, 4, v1\n" \
  "v_lshl_add_u32 v188, v188, 4, v1\n" \
  "v_lshlrev_b32 v1, 4, v253\n" \
  "v_xor_b32 v244, v1, v254\n" \
  "v_lshl_add_u32 v245, v255, 4, s5\n" \
  "v_lshrrev_b32 v1, 1, v252\n" \
  "s_waitcnt vmcnt(1)\n" \
  "s_mov_b64 exec, 1\n" \
  "ds_add_u32 v190, v191 offset:12\n" \
  "s_mov_b64 exec, -1\n" \
  "s_memtime s[88:89]\n" \
  "v_xor_b32 v1, v1, v253\n" \
  "v_lshlrev_b32 v1, 4, v1\n" \
  "v_lshl_add_u32 v1, v252, 7, v1\n" \
  "v_add_u32 v246, s5, v1\n" \
  "v_xor_b32 v1, 64, v1\n" \
  "v_add_u32 v242, s5, v1\n" \
  "v_lshlrev_b32 v1, 4, v255\n" \
  "v_xor_b32 v1, 64, v1\n" \
  "v_add_u32 v243, s5, v1\n" \
  "v_lshrrev_b32 v1, 2, v255\n" \
  "s_lshl_b32 s35, s28, 5\n" \
  "s_lshl_b32 s36, s31, 6\n" \
  "s_add_u32 s35, s35, s36\n" \
  "s_and_b32 s35, s35, 127\n" \
  "v_add_u32 v1, s35, v1\n" \
  "v_lshrrev_b32 v2, 4, v255\n" \
  "v_and_b32 v2, 2, v2\n" \
  "v_xor_b32 v2, v2, v255\n" \
  "v_and_b32 v2, 3, v2\n" \
  "v_lshlrev_b32 v2, 4, v2\n" \
  "v_lshl_add_u32 v189, v1, 10, v2\n" \
  "v_add_u32 v194, 15360, v189\n" \
  "v_lshrrev_b32 v1, 2, v252\n" \
  "v_lshlrev_b32 v1, 6, v1\n" \
  "v_lshl_add_u32 v1, v253, 9, v1\n" \
  "v_and_b32 v2, 3, v252\n" \
  "v_lshl_add_u32 v1, v2, 3, v1\n" \
  "v_and_b32 v2, 1, v253\n" \
  "v_lshl_add_u32 v1, v2, 5, v1\n" \
  "v_add_u32 v184, 138240, v1\n" \
  "v_xor_b32 v1, 32, v1\n" \
  "v_add_u32 v185, 138240, v1\n" \
  "s_bfe_u32 s35, s30, 0x10001\n" \
  "s_lshl1_add_u32 s35, s29, s35\n" \
  "s_lshr_b32 s6, s30, 2\n" \
  "s_lshl_b32 s6, s6, 18\n" \
  "s_lshl_b32 s38, s35, 6\n" \
  "s_add_u32 s92, s14, s38\n" \
  "s_addc_u32 s93, s15, 0\n" \
  "s_lshr_b32 s37, s30, 3\n" \
  "s_add_u32 s37, s37, 96\n" \
  "s_lshl_b32 s34, s37, 17\n" \
  "s_mul_i32 s35, s35, 400\n" \
  "s_add_u32 s37, s37, s35\n" \
  "s_mul_i32 s37, s37, 16000\n" \
  "s_add_u32 s46, s22, s37\n" \
  "s_addc_u32 s47, s23, 0\n" \
  "s_lshr_b32 s36, s30, 2\n" \
  "s_add_u32 s36, s36, s35\n" \
  "s_mul_i32 s36, s36, 16000\n" \
  "s_add_u32 s44, s22, s36\n" \
  "s_addc_u32 s45, s23, 0\n" \
  "s_lshl_b32 s96, s28, 11\n" \
  "s_add_u32 s96, s96, 138240\n" \
  "v_lshlrev_b32 v238, 4, v255\n" \
  "v_mov_b32 v234, 0\n" \
  "s_lshl2_add_u32 s33, s26, s28\n" \
  "s_mov_b32 s52, 0x10002\n" \
  "s_mov_b32 s53, 0xc0c0400\n" \
  "s_mov_b32 s54, 0x4000c0c\n" \
  "s_mov_b32 s55, 0x3030303\n" \
  "s_mov_b32 s56, 0x55555555\n" \
  "s_mov_b32 s38, 0x0\n" \
  "s_mov_b32 s98, 0x0\n" \
  "s_mov_b32 s99, 0x8\n" \
  "s_mov_b32 s97, 0x2000\n" \
  "v_xor_b32 v245, 64, v244\n" \
  "v_add_u32 v245, 0x400, v245\n" \
  "s_setprio 0\n" \
  "L5_:\n" \
  "ds_read_b32 v192, v190 offset:16\n" \
  "s_waitcnt lgkmcnt(0)\n" \
  "v_readfirstlane_b32 s35, v192\n" \
  "s_cmp_lt_u32 s35, 4\n" \
  "s_cbranch_scc1 L5_\n" \
  "s_add_u32 s35, s4, 0\n" \
  "s_min_u32 s35, s35, 984\n" \
  "s_lshl_b32 s35, s35, 8\n" \
  "s_add_u32 m0, s5, 0\n" \
  "s_add_u32 s58, s94, s35\n" \
  "s_addc_u32 s59, s95, 0\n" \
  "global_load_lds_dwordx4 v244, s[58:59]\n" \
  "global_load_lds_dwordx4 v245, s[58:59] offset:1024\n" \
  "s_add_u32 s35, s4, 16\n" \
  "s_min_u32 s35, s35, 984\n" \
  "s_lshl_b32 s35, s35, 8\n" \
  "s_add_u32 m0, s5, 2048\n" \
  "s_add_u32 s58, s94, s35\n" \
  "s_addc_u32 s59, s95, 0\n" \
  "global_load_lds_dwordx4 v244, s[58:59]\n" \
  "global_load_lds_dwordx4 v245, s[58:59] offset:1024\n" \
  "L6_:\n" \
  "ds_read_b32 v192, v190 offset:12\n" \
  "s_waitcnt lgkmcnt(0)\n" \
  "v_readfirstlane_b32 s35, v192\n" \
  "s_cmp_lt_u32 s35, 4\n" \
  "s_cbranch_scc1 L6_\n" \
  "s_waitcnt vmcnt(0)\n" \
  "ds_read_b128 v[8:11], v246 offset:0\n" \
  "ds_read_b128 v[12:15], v242 offset:0\n" \
  "ds_read_b128 v[24:27], v246 offset:2048\n" \
  "ds_read_b128 v[28:31], v242 offset:2048\n" \
  "ds_read_b128 v[0:3], v249 offset:0\n" \
  "ds_read_b128 v[4:7], v249 offset:64\n" \
  "ds_read_b128 v[16:19], v249 offset:2080\n" \
  "ds_read_b128 v[20:23], v249 offset:2144\n" \
  "s_waitcnt lgkmcnt(4)\n" \
  "s_lshl2_add_u32 s35, s26, s28\n" \
  "s_mul_i32 s35, s35, 3200\n" \
  "s_add_u32 s35, s35, 0\n" \
  "s_add_u32 s58, s14, s35\n" \
  "s_addc_u32 s59, s15, 0\n" \
  "s_mov_b32 exec_lo, 0x1ffffff\n" \
  "s_mov_b32 exec_hi, 0x0\n" \
  "global_load_dword v248, v247, s[58:59]\n" \
  "s_mov_b64 exec, -1\n" \
  "s_add_u32 s35, s4, 32\n" \
  "s_min_u32 s35, s35, 984\n" \
  "s_lshl_b32 s35, s35, 8\n" \
  "s_add_u32 m0, s5, 0\n" \
  "s_add_u32 s58, s94, s35\n" \
  "s_addc_u32 s59, s95, 0\n" \
  "global_load_lds_dwordx4 v244, s[58:59]\n" \
  "global_load_lds_dwordx4 v245, s[58:59] offset:1024\n" \
  "s_add_u32 s35, s4, 48\n" \
  "s_min_u32 s35, s35, 984\n" \
  "s_lshl_b32 s35, s35, 8\n" \
  "s_add_u32 m0, s5, 2048\n" \
  "s_add_u32 s58, s94, s35\n" \
  "s_addc_u32 s59, s95, 0\n" \
  "global_load_lds_dwordx4 v244, s[58:59]\n" \
  "global_load_lds_dwordx4 v245, s[58:59] offset:1024\n" \
  "s_waitcnt vmcnt(0)\n" \
  "ds_read_b128 v[40:43], v246 offset:0\n" \
  "ds_read_b128 v[44:47], v242 offset:0\n" \
  "ds_read_b128 v[56:59], v246 offset:2048\n" \
  "ds_read_b128 v[60:63], v242 offset:2048\n" \
  "ds_read_b128 v[32:35], v249 offset:4160\n" \
  "ds_read_b128 v[36:39], v249 offset:4224\n" \
  "ds_read_b128 v[48:51], v249 offset:6240\n" \
  "ds_read_b128 v[52:55], v249 offset:6304\n" \
  "s_waitcnt lgkmcnt(4)\n" \
  "s_sleep 8\n" \
  "s_lshl2_add_u32 s35, s26, s28\n" \
  "s_mul_i32 s35, s35, 3200\n" \
  "s_add_u32 s35, s35, 3276800\n" \
  "s_add_u32 s58, s14, s35\n" \
  "s_addc_u32 s59, s15, 0\n" \
  "s_mov_b32 exec_lo, 0x1ffffff\n" \
  "s_mov_b32 exec_hi, 0x0\n" \
  "global_load_dword v248, v247, s[58:59]\n" \
  "s_mov_b64 exec, -1\n" \
  "s_add_u32 s35, s4, 64\n" \
  "s_min_u32 s35, s35, 984\n" \
  "s_lshl_b32 s35, s35, 8\n" \
  "s_add_u32 m0, s5, 0\n" \
  "s_add_u32 s58, s94, s35\n" \
  "s_addc_u32 s59, s95, 0\n" \
  "global_load_lds_dwordx4 v244, s[58:59]\n" \
  "global_load_lds_dwordx4 v245, s[58:59] offset:1024\n" \
  "s_add_u32 s35, s4, 80\n" \
  "s_min_u32 s35, s35, 984\n" \
  "s_lshl_b32 s35, s35, 8\n" \
  "s_add_u32 m0, s5, 2048\n" \
  "s_add_u32 s58, s94, s35\n" \
  "s_addc_u32 s59, s95, 0\n" \
  "global_load_lds_dwordx4 v244, s[58:59]\n" \
  "global_load_lds_dwordx4 v245, s[58:59] offset:1024\n" \
  "s_waitcnt vmcnt(0)\n" \
  "ds_read_b128 v[72:75], v246 offset:0\n" \
  "ds_read_b128 v[76:79], v242 offset:0\n" \
  "ds_read_b128 v[88:91], v246 offset:2048\n" \
  "ds_read_b128 v[92:95], v242 offset:2048\n" \
  "ds_read_b128 v[64:67], v249 offset:8320\n" \
  "ds_read_b128 v[68:71], v249 offset:8384\n" \
  "ds_read_b128 v[80:83], v249 offset:10400\n" \
  "ds_read_b128 v[84:87], v249 offset:10464\n" \
  "s_waitcnt lgkmcnt(4)\n" \
  "s_sleep 8\n" \
  "s_lshl2_add_u32 s35, s26, s28\n" \
  "s_mul_i32 s35, s35, 3200\n" \
  "s_add_u32 s35, s35, 6553600\n" \
  "s_add_u32 s58, s14, s35\n" \
  "s_addc_u32 s59, s15, 0\n" \
  "s_mov_b32 exec_lo, 0x1ffffff\n" \
  "s_mov_b32 exec_hi, 0x0\n" \
  "global_load_dword v248, v247, s[58:59]\n" \
  "s_mov_b64 exec, -1\n" \
  "s_add_u32 s35, s4, 96\n" \
  "s_min_u32 s35, s35, 984\n" \
  "s_lshl_b32 s35, s35, 8\n" \
  "s_add_u32 m0, s5, 0\n" \
  "s_add_u32 s58, s94, s35\n" \
  "s_addc_u32 s59, s95, 0\n" \
  "global_load_lds_dwordx4 v244, s[58:59]\n" \
  "global_load_lds_dwordx4 v245, s[58:59] offset:1024\n" \
  "s_add_u32 s35, s4, 112\n" \
  "s_min_u32 s35, s35, 984\n" \
  "s_lshl_b32 s35, s35, 8\n" \
  "s_add_u32 m0, s5, 2048\n" \
  "s_add_u32 s58, s94, s35\n" \
  "s_addc_u32 s59, s95, 0\n" \
  "global_load_lds_dwordx4 v244, s[58:59]\n" \
  "global_load_lds_dwordx4 v245, s[58:59] offset:1024\n" \
  "s_waitcnt vmcnt(0)\n" \
  "ds_read_b128 v[104:107], v246 offset:0\n" \
  "ds_read_b128 v[108:111], v242 offset:0\n" \
  "ds_read_b128 v[120:123], v246 offset:2048\n" \
  "ds_read_b128 v[124:127], v242 offset:2048\n" \
  "ds_read_b128 v[96:99], v250 offset:0\n" \
  "ds_read_b128 v[100:103], v250 offset:64\n" \
  "ds_read_b128 v[112:115], v251 offset:0\n" \
  "ds_read_b128 v[116:119], v251 offset:64\n" \
  "s_waitcnt lgkmcnt(4)\n" \
  "s_sleep 8\n" \
  "s_lshl2_add_u32 s35, s26, s28\n" \
  "s_mul_i32 s35, s35, 3200\n" \
  "s_add_u32 s35, s35, 9830400\n" \
  "s_add_u32 s58, s14, s35\n" \
  "s_addc_u32 s59, s15, 0\n" \
  "s_mov_b32 exec_lo, 0x1ffffff\n" \
  "s_mov_b32 exec_hi, 0x0\n" \
  "global_load_dword v248, v247, s[58:59]\n" \
  "s_mov_b64 exec, -1\n" \
  "s_add_u32 s40, s92, s6\n" \
  "s_addc_u32 s41, s93, 0\n" \
  "s_add_u32 s6, s6, 2097152\n" \
  "s_mov_b32 m0, s96\n" \
  "s_nop 0\n" \
  "global_load_lds_dwordx4 v189, s[40:41]\n" \
  "global_load_lds_dwordx4 v194, s[40:41] offset:1024\n" \
  "s_getpc_b64 s[58:59]\n" \
  "LB_pc:\n" \
  "s_sub_u32 s58, s58, LB_pc-LZ_first\n" \
  "s_subb_u32 s59, s59, 0\n" \
  "s_mul_i32 s35, s28, 7168\n" \
  "v_lshl_add_u32 v195, v255, 7, s35\n" \
  "v_min_u32 v195, LZ_end-LZ_first, v195\n" \
  "s_mov_b32 exec_hi, 0xffffff\n" \
  "global_load_dword v248, v195, s[58:59]\n" \
  "s_mov_b64 exec, -1\n" \
  "global_load_dwordx2 v[196:197], v227, s[42:43] offset:-3200\n" \
  "global_load_dwordx2 v[198:199], v227, s[42:43] offset:-2800\n" \
  "global_load_dwordx2 v[200:201], v227, s[42:43] offset:-2400\n" \
  "global_load_dwordx2 v[202:203], v227, s[42:43] offset:-1600\n" \
  "global_load_dwordx2 v[204:205], v227, s[42:43] offset:-1200\n" \
  "global_load_dwordx2 v[206:207], v227, s[42:43] offset:-800\n" \
  "global_load_dwordx2 v[208:209], v227, s[42:43] offset:-400\n" \
  "global_load_dwordx2 v[210:211], v227, s[42:43] offset:0\n" \
  "global_load_dwordx2 v[212:213], v227, s[42:43] offset:400\n" \
  "global_load_dwordx2 v[214:215], v227, s[42:43] offset:800\n" \
  "global_load_dwordx2 v[216:217], v227, s[42:43] offset:1200\n" \
  "global_load_dwordx2 v[218:219], v227, s[42:43] offset:-2000\n" \
  "global_load_dwordx2 v[220:221], v227, s[42:43] offset:1600\n" \
  "global_load_dwordx2 v[222:223], v227, s[42:43] offset:2000\n" \
  "global_load_dwordx2 v[224:225], v227, s[42:43] offset:2400\n" \
  "global_load_dwordx2 v[226:227], v227, s[42:43] offset:2800\n" \
  "s_setprio 2\n" \
  "s_mul_i32 s36, s28, 2176\n" \
  "s_add_u32 s36, s36, 154624\n" \
  "v_bfe_u32 v228, v255, 3, 1\n" \
  "v_and_b32 v229, 1, v255\n" \
  "v_lshl_add_u32 v230, v228, 1, v229\n" \
  "v_lshl_add_u32 v231, v229, 1, v228\n" \
  "v_xor_b32 v229, 1, v230\n" \
  "s_mov_b32 vcc_lo, 0xcccccccc\n" \
  "s_mov_b32 vcc_hi, vcc_lo\n" \
  "v_cndmask_b32 v228, v230, v231, vcc\n" \
  "v_cndmask_b32 v229, v229, v231, vcc\n" \
  "v_and_b32 v230, 4, v255\n" \
  "v_mul_u32_u24 v230, 272, v230\n" \
  "v_lshl_add_u32 v230, v253, 2, v230\n" \
  "v_and_or_b32 v230, v255, 2, v230\n" \
  "v_add_u32 v230, s36, v230\n" \
  "s_movk_i32 s35, 272\n" \
  "v_mad_u32_u24 v195, v228, s35, v230\n" \
  "v_mad_u32_u24 v235, v229, s35, v230\n" \
  "v_lshl_add_u32 v230, v253, 4, v238\n" \
  "v_add_u32 v254, s36, v230\n" \
  "v_readfirstlane_b32 s39, v193\n" \
  "s_lshl_b32 s35, s26, 2\n" \
  "s_add_u32 s39, s39, 1\n" \
  "v_mov_b32 v193, s35\n" \
  "v_mov_b64 v[240:241], -1\n" \
  "v_mov_b64 v[242:243], -1\n" \
  "s_waitcnt vmcnt(0)\n" \
  "s_waitcnt lgkmcnt(0)\n" \
  "s_mov_b64 exec, 1\n" \
  "ds_add_u32 v190, v191 offset:0\n" \
  "s_mov_b64 exec, -1\n" \
  "L7_:\n" \
  "s_sleep 2\n" \
  "ds_read_b32 v192, v190 offset:0\n" \
  "s_waitcnt lgkmcnt(0)\n" \
  "v_readfirstlane_b32 s35, v192\n" \
  "s_cmp_lt_u32 s35, 4\n" \
  "s_cbranch_scc1 L7_\n" \
  "s_add_u32 s96, s96, 8192\n" \
  "s_add_u32 s40, s92, s6\n" \
  "s_addc_u32 s41, s93, 0\n" \
  "s_add_u32 s6, s6, 2097152\n" \
  "s_mov_b32 m0, s96\n" \
  "s_nop 0\n" \
  "global_load_lds_dwordx4 v189, s[40:41]\n" \
  "global_load_lds_dwordx4 v194, s[40:41] offset:1024\n" \
  "s_sub_u32 s96, s96, 8192\n" \
  "ds_read_b64_tr_b16 v[128:129], v184 offset:0\n" \
  "ds_read_b64_tr_b16 v[130:131], v184 offset:256\n" \
  "ds_read_b64_tr_b16 v[132:133], v184 offset:2048\n" \
  "ds_read_b64_tr_b16 v[134:135], v184 offset:2304\n" \
  "ds_read_b64_tr_b16 v[136:137], v184 offset:4096\n" \
  "ds_read_b64_tr_b16 v[138:139], v184 offset:4352\n" \
  "ds_read_b64_tr_b16 v[140:141], v184 offset:6144\n" \
  "ds_read_b64_tr_b16 v[142:143], v184 offset:6400\n" \
  "ds_read_b64_tr_b16 v[156:157], v185 offset:6144\n" \
  "ds_read_b64_tr_b16 v[158:159], v185 offset:6400\n" \
  "ds_read_b64_tr_b16 v[152:153], v185 offset:4096\n" \
  "ds_read_b64_tr_b16 v[154:155], v185 offset:4352\n" \
  "ds_read_b64_tr_b16 v[148:149], v185 offset:2048\n" \
  "ds_read_b64_tr_b16 v[150:151], v185 offset:2304\n" \
  "ds_read_b64_tr_b16 v[144:145], v185 offset:0\n" \
  "ds_read_b64_tr_b16 v[146:147], v185 offset:256\n" \
  "v_mov_b32 v192, s39\n" \
  "s_cmp_lg_u32 s28, 0\n" \
  "s_cbranch_scc1 LB_nc\n" \
  "s_mov_b64 exec, 1\n" \
  "global_store_dword v193, v192, s[48:49] offset:0 sc1\n" \
  "s_mov_b64 exec, -1\n" \
  "LB_nc:\n" \
  "s_lshl2_add_u32 s35, s7, s28\n" \
  "s_lshl_b32 s35, s35, 7\n" \
  "s_lshl_b32 s36, s31, 16\n" \
  "s_add_u32 s35, s35, s36\n" \
  "s_add_u32 s35, s35, 109871104\n" \
  "s_add_u32 s58, s22, s35\n" \
  "s_addc_u32 s59, s23, 0\n" \
  "s_mov_b64 exec, 0xff\n" \
  "global_store_dwordx4 v238, v[240:243], s[58:59] sc1\n" \
  "s_mov_b64 exec, -1\n" \
  "s_cmp_lg_u32 s33, 1021\n" \
  "s_cbranch_scc1 LB_nm\n" \
  "v_mov_b64 v[240:241], 0\n" \
  "v_mov_b64 v[242:243], 0\n" \
  "global_store_dwordx4 v238, v[240:243], s[48:49] offset:2048 sc1\n" \
  "s_nop 1\n" \
  "v_lshlrev_b32 v242, 8, v255\n" \
  "global_store_dwordx2 v242, v[240:241], s[48:49] offset:3072 sc1\n" \
  "v_add_u32 v242, 0x4000, v242\n" \
  "global_store_dwordx2 v242, v[240:241], s[48:49] offset:3072 sc1\n" \
  "v_add_u32 v242, 0x4000, v242\n" \
  "global_store_dwordx2 v242, v[240:241], s[48:49] offset:3072 sc1\n" \
  "v_add_u32 v242, 0x4000, v242\n" \
  "global_store_dwordx2 v242, v[240:241], s[48:49] offset:3072 sc1\n" \
  "v_add_u32 v242, 0x4000, v242\n" \
  "LB_nm:\n" \
  "s_mov_b32 s86, 0x78000b0\n" \
  "s_mov_b32 s87, 0x0\n" \
  "s_or_b32 s86, s86, s87\n" \
  "s_setprio 2\n" \
  "s_memtime s[90:91]\n" \
  "s_memrealtime s[42:43]\n" \
  "LT:\n" \
  "s_cmp_eq_u32 s38, 6\n" \
  "s_cselect_b32 s78, 0x33333333, -1\n" \
  "s_cmp_eq_u32 s38, 7\n" \
  "s_cselect_b32 s78, 0x33333333, s78\n" \
  "s_cmp_eq_u32 s38, 18\n" \
  "s_cselect_b32 s80, 0x33333333, -1\n" \
  "s_cmp_eq_u32 s38, 19\n" \
  "s_cselect_b32 s80, 0x33333333, s80\n" \
  "s_mov_b32 s79, s78\n" \
  "s_mov_b32 s81, s80\n" \
  "s_mov_b32 s37, s38\n" \
  "s_add_u32 s57, s38, 2\n" \
  "s_add_u32 s3, s38, 4\n" \
  "s_add_u32 s38, s38, 6\n" \
  "s_cmp_lt_u32 s57, 32\n" \
  "s_cselect_b32 s57, s57, 37\n" \
  "s_cselect_b32 s3, s3, 37\n" \
  "s_waitcnt lgkmcnt(12)\n" \
  "v_mfma_f32_16x16x32_bf16 v[160:163], v[128:131], v[0:3], 0\n" \
  "s_mov_b64 vcc, s[78:79]\n" \
  "v_cndmask_b32_dpp v234, v234, v234, vcc row_ror:8 row_mask:0xf bank_mask:0xf\n" \
  "v_mfma_f32_16x16x32_bf16 v[160:163], v[132:135], v[4:7], v[160:163]\n" \
  "s_waitcnt lgkmcnt(8)\n" \
  "v_mfma_f32_16x16x32_bf16 v[160:163], v[136:139], v[8:11], v[160:163]\n" \
  "s_set_gpr_idx_on s37, gpr_idx(SRC0)\n" \
  "v_mad_u32_u24 v232, v196, s52, v236\n" \
  "s_set_gpr_idx_off\n" \
  "v_mfma_f32_16x16x32_bf16 v[160:163], v[140:143], v[12:15], v[160:163]\n" \
  "s_waitcnt lgkmcnt(0)\n" \
  "v_lshrrev_b32 v228, v232, v234\n" \
  "v_lshrrev_b32_dpp v229, v232, v234 quad_perm:[2,3,0,1] row_mask:0xf bank_mask:0xf\n" \
  "v_mfma_f32_16x16x32_bf16 v[164:167], v[156:159], v[12:15], 0\n" \
  "v_lshrrev_b32_dpp v230, v232, v234 quad_perm:[1,2,3,0] row_mask:0xf bank_mask:0xf\n" \
  "v_lshrrev_b32_dpp v231, v232, v234 quad_perm:[3,0,1,2] row_mask:0xf bank_mask:0xf\n" \
  "v_mfma_f32_16x16x32_bf16 v[164:167], v[152:155], v[8:11], v[164:167]\n" \
  "v_perm_b32 v228, v229, v228, s53\n" \
  "v_perm_b32 v230, v231, v230, s54\n" \
  "v_mfma_f32_16x16x32_bf16 v[164:167], v[148:151], v[4:7], v[164:167]\n" \
  "v_bitop3_b32 v228, v228, v230, s55 bitop3:0xa8\n" \
  "v_lshl_or_b32 v229, v234, 1, s56\n" \
  "v_mfma_f32_16x16x32_bf16 v[164:167], v[144:147], v[0:3], v[164:167]\n" \
  "v_add_u32_dpp v228, v228, v228 quad_perm:[2,3,0,1] row_mask:0xf bank_mask:0xf\n" \
  "v_min_u32_sdwa v230, v228, v228 dst_sel:DWORD dst_unused:UNUSED_PAD src0_sel:BYTE_0 src1_sel:BYTE_1\n" \
  "v_mfma_f32_16x16x32_bf16 v[172:175], v[144:147], v[16:19], 0\n" \
  "v_min_u32_sdwa v231, v228, v228 dst_sel:DWORD dst_unused:UNUSED_PAD src0_sel:BYTE_2 src1_sel:BYTE_3\n" \
  "v_sub_u32 v230, v230, v231\n" \
  "v_mfma_f32_16x16x32_bf16 v[172:175], v[148:151], v[20:23], v[172:175]\n" \
  "v_cvt_pk_bf16_f32 v176, v160, v161\n" \
  "v_add_u32_dpp v230, v230, v230 quad_perm:[1,0,3,2] row_mask:0xf bank_mask:0xf\n" \
  "v_mfma_f32_16x16x32_bf16 v[172:175], v[152:155], v[24:27], v[172:175]\n" \
  "v_cmp_ge_i32 vcc, 0, v230\n" \
  "v_cvt_pk_bf16_f32 v177, v162, v163\n" \
  "v_mfma_f32_16x16x32_bf16 v[172:175], v[156:159], v[28:31], v[172:175]\n" \
  "v_cndmask_b32_sdwa v228, v228, v228, vcc dst_sel:DWORD dst_unused:UNUSED_PAD src0_sel:WORD_1 src1_sel:WORD_0\n" \
  "v_cvt_pk_bf16_f32 v178, v164, v165\n" \
  "v_mfma_f32_16x16x32_bf16 v[168:171], v[140:143], v[28:31], 0\n" \
  "s_mov_b64 s[50:51], vcc\n" \
  "v_cmp_le_u32_sdwa vcc, v228, v228 src0_sel:BYTE_0 src1_sel:BYTE_1\n" \
  "v_cvt_pk_bf16_f32 v179, v166, v167\n" \
  "v_mfma_f32_16x16x32_bf16 v[168:171], v[136:139], v[24:27], v[168:171]\n" \
  "v_cndmask_b32_dpp v232, v232, v232, vcc quad_perm:[2,3,0,1] row_mask:0xf bank_mask:0xf\n" \
  "v_mfma_f32_16x16x32_bf16 v[168:171], v[132:135], v[20:23], v[168:171]\n" \
  "s_mov_b64 vcc, s[50:51]\n" \
  "v_cndmask_b32_dpp v232, v232, v232, vcc quad_perm:[3,0,1,2] row_mask:0xf bank_mask:0xf\n" \
  "v_lshlrev_b32_e64 v230, v232, 3\n" \
  "v_mfma_f32_16x16x32_bf16 v[168:171], v[128:131], v[16:19], v[168:171]\n" \
  "v_and_or_b32 v234, v229, v230, v234\n" \
  "s_cmp_lg_u32 s33, 1021\n" \
  "s_cbranch_scc0 LT_e\n" \
  "LT_r:\n" \
  "s_waitcnt vmcnt(5)\n" \
  "global_store_dwordx4 v186, v[176:179], s[44:45] offset:0 sc1\n" \
  "v_mfma_f32_16x16x32_bf16 v[160:163], v[128:131], v[32:35], 0\n" \
  "s_set_gpr_idx_on s37, gpr_idx(DST)\n" \
  "v_mov_b32 v196, v232\n" \
  "s_set_gpr_idx_off\n" \
  "v_mfma_f32_16x16x32_bf16 v[160:163], v[132:135], v[36:39], v[160:163]\n" \
  "s_set_gpr_idx_on s57, gpr_idx(SRC0)\n" \
  "v_mad_u32_u24 v232, v196, s52, v236\n" \
  "s_set_gpr_idx_off\n" \
  "v_mfma_f32_16x16x32_bf16 v[160:163], v[136:139], v[40:43], v[160:163]\n" \
  "v_lshrrev_b32 v228, v232, v234\n" \
  "v_lshrrev_b32_dpp v229, v232, v234 quad_perm:[2,3,0,1] row_mask:0xf bank_mask:0xf\n" \
  "v_mfma_f32_16x16x32_bf16 v[160:163], v[140:143], v[44:47], v[160:163]\n" \
  "v_lshrrev_b32_dpp v230, v232, v234 quad_perm:[1,2,3,0] row_mask:0xf bank_mask:0xf\n" \
  "v_lshrrev_b32_dpp v231, v232, v234 quad_perm:[3,0,1,2] row_mask:0xf bank_mask:0xf\n" \
  "v_mfma_f32_16x16x32_bf16 v[164:167], v[156:159], v[44:47], 0\n" \
  "v_perm_b32 v228, v229, v228, s53\n" \
  "v_perm_b32 v230, v231, v230, s54\n" \
  "v_mfma_f32_16x16x32_bf16 v[164:167], v[152:155], v[40:43], v[164:167]\n" \
  "v_bitop3_b32 v228, v228, v230, s55 bitop3:0xa8\n" \
  "v_lshl_or_b32 v229, v234, 1, s56\n" \
  "v_mfma_f32_16x16x32_bf16 v[164:167], v[148:151], v[36:39], v[164:167]\n" \
  "v_cvt_pk_bf16_f32 v182, v172, v173\n" \
  "v_cvt_pk_bf16_f32 v183, v174, v175\n" \
  "v_mfma_f32_16x16x32_bf16 v[164:167], v[144:147], v[32:35], v[164:167]\n" \
  "v_cvt_pk_bf16_f32 v180, v168, v169\n" \
  "v_cvt_pk_bf16_f32 v181, v170, v171\n" \
  "s_waitcnt vmcnt(5)\n" \
  "global_store_dwordx4 v186, v[180:183], s[44:45] offset:256 sc1\n" \
  "v_mfma_f32_16x16x32_bf16 v[172:175], v[144:147], v[48:51], 0\n" \
  "v_add_u32_dpp v228, v228, v228 quad_perm:[2,3,0,1] row_mask:0xf bank_mask:0xf\n" \
  "v_min_u32_sdwa v230, v228, v228 dst_sel:DWORD dst_unused:UNUSED_PAD src0_sel:BYTE_0 src1_sel:BYTE_1\n" \
  "v_mfma_f32_16x16x32_bf16 v[172:175], v[148:151], v[52:55], v[172:175]\n" \
  "v_min_u32_sdwa v231, v228, v228 dst_sel:DWORD dst_unused:UNUSED_PAD src0_sel:BYTE_2 src1_sel:BYTE_3\n" \
  "v_sub_u32 v230, v230, v231\n" \
  "v_mfma_f32_16x16x32_bf16 v[172:175], v[152:155], v[56:59], v[172:175]\n" \
  "v_cvt_pk_bf16_f32 v176, v160, v161\n" \
  "v_add_u32_dpp v230, v230, v230 quad_perm:[1,0,3,2] row_mask:0xf bank_mask:0xf\n" \
  "v_mfma_f32_16x16x32_bf16 v[172:175], v[156:159], v[60:63], v[172:175]\n" \
  "v_cmp_ge_i32 vcc, 0, v230\n" \
  "v_cvt_pk_bf16_f32 v177, v162, v163\n" \
  "v_mfma_f32_16x16x32_bf16 v[168:171], v[140:143], v[60:63], 0\n" \
  "v_cndmask_b32_sdwa v228, v228, v228, vcc dst_sel:DWORD dst_unused:UNUSED_PAD src0_sel:WORD_1 src1_sel:WORD_0\n" \
  "v_cvt_pk_bf16_f32 v178, v164, v165\n" \
  "v_mfma_f32_16x16x32_bf16 v[168:171], v[136:139], v[56:59], v[168:171]\n" \
  "s_mov_b64 s[50:51], vcc\n" \
  "v_cmp_le_u32_sdwa vcc, v228, v228 src0_sel:BYTE_0 src1_sel:BYTE_1\n" \
  "v_cvt_pk_bf16_f32 v179, v166, v167\n" \
  "v_mfma_f32_16x16x32_bf16 v[168:171], v[132:135], v[52:55], v[168:171]\n" \
  "v_cndmask_b32_dpp v232, v232, v232, vcc quad_perm:[2,3,0,1] row_mask:0xf bank_mask:0xf\n" \
  "v_mfma_f32_16x16x32_bf16 v[168:171], v[128:131], v[48:51], v[168:171]\n" \
  "s_mov_b64 vcc, s[50:51]\n" \
  "v_cndmask_b32_dpp v232, v232, v232, vcc quad_perm:[3,0,1,2] row_mask:0xf bank_mask:0xf\n" \
  "v_lshlrev_b32_e64 v230, v232, 3\n" \
  "s_waitcnt vmcnt(5)\n" \
  "global_store_dwordx4 v186, v[176:179], s[44:45] offset:512 sc1\n" \
  "v_mfma_f32_16x16x32_bf16 v[160:163], v[128:131], v[64:67], 0\n" \
  "v_and_or_b32 v234, v229, v230, v234\n" \
  "v_cvt_pk_bf16_f32 v182, v172, v173\n" \
  "v_mfma_f32_16x16x32_bf16 v[160:163], v[132:135], v[68:71], v[160:163]\n" \
  "s_set_gpr_idx_on s57, gpr_idx(DST)\n" \
  "v_mov_b32 v196, v232\n" \
  "s_set_gpr_idx_off\n" \
  "v_mfma_f32_16x16x32_bf16 v[160:163], v[136:139], v[72:75], v[160:163]\n" \
  "s_mov_b64 vcc, s[80:81]\n" \
  "v_cndmask_b32_dpp v234, v234, v234, vcc row_ror:8 row_mask:0xf bank_mask:0xf\n" \
  "v_cvt_pk_bf16_f32 v183, v174, v175\n" \
  "v_mfma_f32_16x16x32_bf16 v[160:163], v[140:143], v[76:79], v[160:163]\n" \
  "s_set_gpr_idx_on s3, gpr_idx(SRC0)\n" \
  "v_mad_u32_u24 v232, v196, s52, v236\n" \
  "s_set_gpr_idx_off\n" \
  "v_mfma_f32_16x16x32_bf16 v[164:167], v[156:159], v[76:79], 0\n" \
  "v_lshrrev_b32 v228, v232, v234\n" \
  "v_lshrrev_b32_dpp v229, v232, v234 quad_perm:[2,3,0,1] row_mask:0xf bank_mask:0xf\n" \
  "v_mfma_f32_16x16x32_bf16 v[164:167], v[152:155], v[72:75], v[164:167]\n" \
  "v_lshrrev_b32_dpp v230, v232, v234 quad_perm:[1,2,3,0] row_mask:0xf bank_mask:0xf\n" \
  "v_lshrrev_b32_dpp v231, v232, v234 quad_perm:[3,0,1,2] row_mask:0xf bank_mask:0xf\n" \
  "v_mfma_f32_16x16x32_bf16 v[164:167], v[148:151], v[68:71], v[164:167]\n" \
  "v_perm_b32 v228, v229, v228, s53\n" \
  "v_perm_b32 v230, v231, v230, s54\n" \
  "v_mfma_f32_16x16x32_bf16 v[164:167], v[144:147], v[64:67], v[164:167]\n" \
  "v_cvt_pk_bf16_f32 v180, v168, v169\n" \
  "v_cvt_pk_bf16_f32 v181, v170, v171\n" \
  "s_waitcnt vmcnt(5)\n" \
  "global_store_dwordx4 v186, v[180:183], s[44:45] offset:768 sc1\n" \
  "v_mfma_f32_16x16x32_bf16 v[172:175], v[144:147], v[80:83], 0\n" \
  "s_waitcnt vmcnt(5)\n" \
  "s_mov_b64 exec, 1\n" \
  "ds_add_u32 v190, v191 offset:0\n" \
  "s_mov_b64 exec, -1\n" \
  "v_mfma_f32_16x16x32_bf16 v[172:175], v[148:151], v[84:87], v[172:175]\n" \
  "v_bitop3_b32 v228, v228, v230, s55 bitop3:0xa8\n" \
  "v_lshl_or_b32 v229, v234, 1, s56\n" \
  "v_mfma_f32_16x16x32_bf16 v[172:175], v[152:155], v[88:91], v[172:175]\n" \
  "v_add_u32_dpp v228, v228, v228 quad_perm:[2,3,0,1] row_mask:0xf bank_mask:0xf\n" \
  "v_min_u32_sdwa v230, v228, v228 dst_sel:DWORD dst_unused:UNUSED_PAD src0_sel:BYTE_0 src1_sel:BYTE_1\n" \
  "v_mfma_f32_16x16x32_bf16 v[172:175], v[156:159], v[92:95], v[172:175]\n" \
  "v_min_u32_sdwa v231, v228, v228 dst_sel:DWORD dst_unused:UNUSED_PAD src0_sel:BYTE_2 src1_sel:BYTE_3\n" \
  "v_sub_u32 v230, v230, v231\n" \
  "v_mfma_f32_16x16x32_bf16 v[168:171], v[140:143], v[92:95], 0\n" \
  "v_cvt_pk_bf16_f32 v176, v160, v161\n" \
  "v_add_u32_dpp v230, v230, v230 quad_perm:[1,0,3,2] row_mask:0xf bank_mask:0xf\n" \
  "v_mfma_f32_16x16x32_bf16 v[168:171], v[136:139], v[88:91], v[168:171]\n" \
  "v_cmp_ge_i32 vcc, 0, v230\n" \
  "v_cvt_pk_bf16_f32 v177, v162, v163\n" \
  "v_mfma_f32_16x16x32_bf16 v[168:171], v[132:135], v[84:87], v[168:171]\n" \
  "v_cndmask_b32_sdwa v228, v228, v228, vcc dst_sel:DWORD dst_unused:UNUSED_PAD src0_sel:WORD_1 src1_sel:WORD_0\n" \
  "v_cvt_pk_bf16_f32 v178, v164, v165\n" \
  "v_mfma_f32_16x16x32_bf16 v[168:171], v[128:131], v[80:83], v[168:171]\n" \
  "s_mov_b64 s[50:51], vcc\n" \
  "v_cmp_le_u32_sdwa vcc, v228, v228 src0_sel:BYTE_0 src1_sel:BYTE_1\n" \
  "v_cvt_pk_bf16_f32 v179, v166, v167\n" \
  "s_cmp_eq_u32 s98, 4\n" \
  "s_cselect_b32 s36, 12451840, 0\n" \
  "s_sub_u32 s6, s6, s36\n" \
  "s_cmp_lt_u32 s98, 10\n" \
  "s_cselect_b32 s6, s6, s34\n" \
  "s_add_u32 s40, s92, s6\n" \
  "s_addc_u32 s41, s93, 0\n" \
  "s_add_u32 s6, s6, 2097152\n" \
  "s_waitcnt vmcnt(5)\n" \
  "global_store_dwordx4 v186, v[176:179], s[44:45] offset:1024 sc1\n" \
  "v_mfma_f32_16x16x32_bf16 v[160:163], v[128:131], v[96:99], 0\n" \
  "ds_read_b32 v192, v190\n" \
  "v_add_u32 v184, s97, v184\n" \
  "v_mfma_f32_16x16x32_bf16 v[160:163], v[132:135], v[100:103], v[160:163]\n" \
  "v_add_u32 v185, s97, v185\n" \
  "v_cndmask_b32_dpp v232, v232, v232, vcc quad_perm:[2,3,0,1] row_mask:0xf bank_mask:0xf\n" \
  "v_mfma_f32_16x16x32_bf16 v[160:163], v[136:139], v[104:107], v[160:163]\n" \
  "s_mov_b64 vcc, s[50:51]\n" \
  "v_cndmask_b32_dpp v232, v232, v232, vcc quad_perm:[3,0,1,2] row_mask:0xf bank_mask:0xf\n" \
  "v_lshlrev_b32_e64 v230, v232, 3\n" \
  "v_mfma_f32_16x16x32_bf16 v[160:163], v[140:143], v[108:111], v[160:163]\n" \
  "v_and_or_b32 v234, v229, v230, v234\n" \
  "v_cvt_pk_bf16_f32 v182, v172, v173\n" \
  "v_mfma_f32_16x16x32_bf16 v[164:167], v[156:159], v[108:111], 0\n" \
  "s_set_gpr_idx_on s3, gpr_idx(DST)\n" \
  "v_mov_b32 v196, v232\n" \
  "s_set_gpr_idx_off\n" \
  "v_mfma_f32_16x16x32_bf16 v[164:167], v[152:155], v[104:107], v[164:167]\n" \
  "s_waitcnt lgkmcnt(0)\n" \
  "v_readfirstlane_b32 s35, v192\n" \
  "v_cvt_pk_bf16_f32 v183, v174, v175\n" \
  "v_mfma_f32_16x16x32_bf16 v[164:167], v[148:151], v[100:103], v[164:167]\n" \
  "v_cvt_pk_bf16_f32 v180, v168, v169\n" \
  "v_cvt_pk_bf16_f32 v181, v170, v171\n" \
  "v_mfma_f32_16x16x32_bf16 v[164:167], v[144:147], v[96:99], v[164:167]\n" \
  "s_cmp_lt_u32 s35, s99\n" \
  "s_cbranch_scc1 LT_w\n" \
  "LT_c:\n" \
  "s_mov_b32 m0, s96\n" \
  "s_nop 0\n" \
  "global_load_lds_dwordx4 v189, s[40:41]\n" \
  "global_load_lds_dwordx4 v194, s[40:41] offset:1024\n" \
  "s_add_u32 s96, s96, s97\n" \
  "s_sub_u32 s97, 0, s97\n" \
  "s_add_u32 s99, s99, 4\n" \
  "s_lshl4_add_u32 s35, s28, s98\n" \
  "s_bitcmp1_b64 s[86:87], s35\n" \
  "s_cbranch_scc1 LT_a\n" \
  "LT_s:\n" \
  "s_waitcnt vmcnt(5)\n" \
  "global_store_dwordx4 v186, v[180:183], s[44:45] offset:1280 sc1\n" \
  "v_mfma_f32_16x16x32_bf16 v[168:171], v[128:131], v[112:115], 0\n" \
  "ds_read_b64_tr_b16 v[128:129], v184 offset:0\n" \
  "ds_read_b64_tr_b16 v[130:131], v184 offset:256\n" \
  "v_mfma_f32_16x16x32_bf16 v[168:171], v[132:135], v[116:119], v[168:171]\n" \
  "ds_read_b64_tr_b16 v[132:133], v184 offset:2048\n" \
  "ds_read_b64_tr_b16 v[134:135], v184 offset:2304\n" \
  "v_mfma_f32_16x16x32_bf16 v[168:171], v[136:139], v[120:123], v[168:171]\n" \
  "ds_read_b64_tr_b16 v[136:137], v184 offset:4096\n" \
  "v_cvt_pk_bf16_f32 v176, v160, v161\n" \
  "v_mfma_f32_16x16x32_bf16 v[168:171], v[140:143], v[124:127], v[168:171]\n" \
  "ds_read_b64_tr_b16 v[138:139], v184 offset:4352\n" \
  "v_cvt_pk_bf16_f32 v177, v162, v163\n" \
  "v_mfma_f32_16x16x32_bf16 v[172:175], v[156:159], v[124:127], 0\n" \
  "ds_read_b64_tr_b16 v[140:141], v184 offset:6144\n" \
  "v_cvt_pk_bf16_f32 v178, v164, v165\n" \
  "v_mfma_f32_16x16x32_bf16 v[172:175], v[152:155], v[120:123], v[172:175]\n" \
  "ds_read_b64_tr_b16 v[142:143], v184 offset:6400\n" \
  "v_cvt_pk_bf16_f32 v179, v166, v167\n" \
  "v_mfma_f32_16x16x32_bf16 v[172:175], v[148:151], v[116:119], v[172:175]\n" \
  "ds_read_b64_tr_b16 v[156:157], v185 offset:6144\n" \
  "ds_read_b64_tr_b16 v[158:159], v185 offset:6400\n" \
  "v_mfma_f32_16x16x32_bf16 v[172:175], v[144:147], v[112:115], v[172:175]\n" \
  "ds_read_b64_tr_b16 v[152:153], v185 offset:4096\n" \
  "ds_read_b64_tr_b16 v[154:155], v185 offset:4352\n" \
  "ds_read_b64_tr_b16 v[148:149], v185 offset:2048\n" \
  "ds_read_b64_tr_b16 v[150:151], v185 offset:2304\n" \
  "ds_read_b64_tr_b16 v[144:145], v185 offset:0\n" \
  "ds_read_b64_tr_b16 v[146:147], v185 offset:256\n" \
  "s_waitcnt vmcnt(5)\n" \
  "global_store_dwordx4 v187, v[176:179], s[44:45] offset:0 sc1\n" \
  "s_add_u32 s98, s98, 1\n" \
  "v_cvt_pk_bf16_f32 v180, v168, v169\n" \
  "v_cvt_pk_bf16_f32 v181, v170, v171\n" \
  "v_cvt_pk_bf16_f32 v182, v172, v173\n" \
  "v_cvt_pk_bf16_f32 v183, v174, v175\n" \
  "s_waitcnt vmcnt(5)\n" \
  "global_store_dwordx4 v188, v[180:183], s[44:45] offset:0 sc1\n" \
  "s_add_u32 s44, s44, 128000\n" \
  "s_addc_u32 s45, s45, 0\n" \
  "s_bitcmp0_b32 4160, s98\n" \
  "s_cbranch_scc1 LT\n" \
  "s_cmp_eq_u32 s98, 6\n" \
  "s_cbranch_scc1 LT_m\n" \
  "s_lshr_b32 s35, s30, 2\n" \
  "s_lshl3_add_u32 s35, s29, s35\n" \
  "s_mul_i32 s35, s35, 1600000\n" \
  "s_lshl_b32 s36, s28, 12\n" \
  "s_add_u32 s35, s35, s36\n" \
  "s_add_u32 s40, s22, s35\n" \
  "s_addc_u32 s41, s23, 0\n" \
  "s_bitcmp1_b32 s30, 2\n" \
  "s_cbranch_scc1 LB_l_h\n" \
  "global_load_dwordx4 v[64:67], v238, s[40:41] offset:0 sc1\n" \
  "global_load_dwordx4 v[68:71], v238, s[40:41] offset:1024 sc1\n" \
  "global_load_dwordx4 v[72:75], v238, s[40:41] offset:2048 sc1\n" \
  "global_load_dwordx4 v[76:79], v238, s[40:41] offset:3072 sc1\n" \
  "s_add_u32 s40, s40, 16000\n" \
  "s_addc_u32 s41, s41, 0\n" \
  "global_load_dwordx4 v[80:83], v238, s[40:41] offset:0 sc1\n" \
  "global_load_dwordx4 v[84:87], v238, s[40:41] offset:1024 sc1\n" \
  "global_load_dwordx4 v[88:91], v238, s[40:41] offset:2048 sc1\n" \
  "global_load_dwordx4 v[92:95], v238, s[40:41] offset:3072 sc1\n" \
  "s_add_u32 s40, s40, 16000\n" \
  "s_addc_u32 s41, s41, 0\n" \
  "global_load_dwordx4 v[96:99], v238, s[40:41] offset:0 sc1\n" \
  "global_load_dwordx4 v[100:103], v238, s[40:41] offset:1024 sc1\n" \
  "global_load_dwordx4 v[104:107], v238, s[40:41] offset:2048 sc1\n" \
  "global_load_dwordx4 v[108:111], v238, s[40:41] offset:3072 sc1\n" \
  "s_add_u32 s40, s40, 16000\n" \
  "s_addc_u32 s41, s41, 0\n" \
  "global_load_dwordx4 v[112:115], v238, s[40:41] offset:0 sc1\n" \
  "global_load_dwordx4 v[116:119], v238, s[40:41] offset:1024 sc1\n" \
  "global_load_dwordx4 v[120:123], v238, s[40:41] offset:2048 sc1\n" \
  "global_load_dwordx4 v[124:127], v238, s[40:41] offset:3072 sc1\n" \
  "s_add_u32 s40, s40, 16000\n" \
  "s_addc_u32 s41, s41, 0\n" \
  "s_branch LB_l_e\n" \
  "LB_l_h:\n" \
  "global_load_dwordx4 v[0:3], v238, s[40:41] offset:0 sc1\n" \
  "global_load_dwordx4 v[4:7], v238, s[40:41] offset:1024 sc1\n" \
  "global_load_dwordx4 v[8:11], v238, s[40:41] offset:2048 sc1\n" \
  "global_load_dwordx4 v[12:15], v238, s[40:41] offset:3072 sc1\n" \
  "s_add_u32 s40, s40, 16000\n" \
  "s_addc_u32 s41, s41, 0\n" \
  "global_load_dwordx4 v[16:19], v238, s[40:41] offset:0 sc1\n" \
  "global_load_dwordx4 v[20:23], v238, s[40:41] offset:1024 sc1\n" \
  "global_load_dwordx4 v[24:27], v238, s[40:41] offset:2048 sc1\n" \
  "global_load_dwordx4 v[28:31], v238, s[40:41] offset:3072 sc1\n" \
  "s_add_u32 s40, s40, 16000\n" \
  "s_addc_u32 s41, s41, 0\n" \
  "global_load_dwordx4 v[32:35], v238, s[40:41] offset:0 sc1\n" \
  "global_load_dwordx4 v[36:39], v238, s[40:41] offset:1024 sc1\n" \
  "global_load_dwordx4 v[40:43], v238, s[40:41] offset:2048 sc1\n" \
  "global_load_dwordx4 v[44:47], v238, s[40:41] offset:3072 sc1\n" \
  "s_add_u32 s40, s40, 16000\n" \
  "s_addc_u32 s41, s41, 0\n" \
  "global_load_dwordx4 v[48:51], v238, s[40:41] offset:0 sc1\n" \
  "global_load_dwordx4 v[52:55], v238, s[40:41] offset:1024 sc1\n" \
  "global_load_dwordx4 v[56:59], v238, s[40:41] offset:2048 sc1\n" \
  "global_load_dwordx4 v[60:63], v238, s[40:41] offset:3072 sc1\n" \
  "s_add_u32 s40, s40, 16000\n" \
  "s_addc_u32 s41, s41, 0\n" \
  "LB_l_e:\n" \
  "ds_write_b16_d16_hi v195, v197 offset:0\n" \
  "ds_write_b16_d16_hi v195, v199 offset:16\n" \
  "ds_write_b16_d16_hi v195, v201 offset:32\n" \
  "ds_write_b16_d16_hi v235, v203 offset:64\n" \
  "ds_write_b16_d16_hi v235, v205 offset:80\n" \
  "ds_write_b16_d16_hi v235, v207 offset:96\n" \
  "ds_write_b16_d16_hi v235, v209 offset:112\n" \
  "ds_write_b16_d16_hi v235, v211 offset:128\n" \
  "ds_write_b16_d16_hi v235, v213 offset:144\n" \
  "ds_write_b16_d16_hi v235, v215 offset:160\n" \
  "ds_write_b16_d16_hi v235, v217 offset:176\n" \
  "ds_write_b16_d16_hi v195, v219 offset:48\n" \
  "ds_write_b16_d16_hi v195, v221 offset:192\n" \
  "ds_write_b16_d16_hi v195, v223 offset:208\n" \
  "ds_write_b16_d16_hi v195, v225 offset:224\n" \
  "ds_write_b16_d16_hi v195, v227 offset:240\n" \
  "ds_read_b128 v[244:247], v254 offset:0\n" \
  "ds_read_b128 v[248:251], v254 offset:1088\n" \
  "s_lshr_b32 s35, s7, 5\n" \
  "s_mul_i32 s35, s35, 819200\n" \
  "s_and_b32 s36, s7, 31\n" \
  "s_lshl_b32 s36, s36, 10\n" \
  "s_add_u32 s35, s35, s36\n" \
  "s_lshl_b32 s36, s4, 9\n" \
  "s_add_u32 s35, s35, s36\n" \
  "s_add_u32 s35, s35, 102793216\n" \
  "s_add_u32 s58, s22, s35\n" \
  "s_addc_u32 s59, s23, 0\n" \
  "s_waitcnt lgkmcnt(0)\n" \
  "s_cmp_ge_u32 s4, 896\n" \
  "s_cbranch_scc1 L17_\n" \
  "global_store_dwordx4 v238, v[244:247], s[58:59] sc1\n" \
  "s_cmp_ge_u32 s4, 768\n" \
  "s_cbranch_scc1 L17_\n" \
  "s_add_u32 s58, s58, 32768\n" \
  "s_addc_u32 s59, s59, 0\n" \
  "global_store_dwordx4 v238, v[248:251], s[58:59] sc1\n" \
  "L17_:\n" \
  "s_lshr_b32 s35, s30, 2\n" \
  "s_lshl3_add_u32 s33, s29, s35\n" \
  "s_and_b32 s34, s30, 3\n" \
  "s_lshl_b32 s59, s27, 11\n" \
  "s_mul_i32 s35, s33, 1600000\n" \
  "s_add_u32 s54, s22, s35\n" \
  "s_addc_u32 s55, s23, 0\n" \
  "s_mul_i32 s35, s34, 819200\n" \
  "s_lshl1_add_u32 s35, s59, s35\n" \
  "s_add_u32 s35, s35, 102400000\n" \
  "s_add_u32 s56, s22, s35\n" \
  "s_addc_u32 s57, s23, 0\n" \
  "s_lshr_b32 s35, s30, 4\n" \
  "s_lshl1_add_u32 s35, s29, s35\n" \
  "s_lshl_b32 s35, s35, 6\n" \
  "s_bfe_u32 s37, s30, 0x20002\n" \
  "s_lshl3_add_u32 s35, s37, s35\n" \
  "v_and_b32 v202, 16, v255\n" \
  "v_lshl_add_u32 v202, v202, 1, s35\n" \
  "global_load_dwordx2 v[204:205], v202, s[16:17]\n" \
  "global_load_dwordx2 v[206:207], v202, s[18:19]\n" \
  "v_mov_b32 v202, v238\n" \
  "global_load_dwordx4 v[208:211], v238, s[56:57] offset:0 sc1\n" \
  "global_load_dwordx4 v[212:215], v238, s[56:57] offset:1024 sc1\n" \
  "global_load_dwordx4 v[216:219], v238, s[56:57] offset:2048 sc1\n" \
  "global_load_dwordx4 v[220:223], v238, s[56:57] offset:3072 sc1\n" \
  "s_add_u32 s56, s56, 32768\n" \
  "s_addc_u32 s57, s57, 0\n" \
  "global_load_dwordx4 v[224:227], v238, s[56:57] sc1\n" \
  "global_load_dwordx4 v[228:231], v238, s[56:57] offset:1024 sc1\n" \
  "global_load_dwordx4 v[232:235], v238, s[56:57] offset:2048 sc1\n" \
  "global_load_dwordx4 v[236:239], v238, s[56:57] offset:3072 sc1\n" \
  "s_add_u32 s56, s56, 32768\n" \
  "s_addc_u32 s57, s57, 0\n" \
  "s_bitcmp1_b32 s30, 2\n" \
  "s_cbranch_scc1 LB_t_h\n" \
  "s_waitcnt lgkmcnt(12)\n" \
  "v_mfma_f32_16x16x32_bf16 v[160:163], v[128:131], v[0:3], 0\n" \
  "v_mfma_f32_16x16x32_bf16 v[160:163], v[132:135], v[4:7], v[160:163]\n" \
  "s_waitcnt lgkmcnt(8)\n" \
  "v_mfma_f32_16x16x32_bf16 v[160:163], v[136:139], v[8:11], v[160:163]\n" \
  "v_mfma_f32_16x16x32_bf16 v[160:163], v[140:143], v[12:15], v[160:163]\n" \
  "s_waitcnt lgkmcnt(0)\n" \
  "v_mfma_f32_16x16x32_bf16 v[164:167], v[156:159], v[12:15], 0\n" \
  "v_mfma_f32_16x16x32_bf16 v[164:167], v[152:155], v[8:11], v[164:167]\n" \
  "v_mfma_f32_16x16x32_bf16 v[164:167], v[148:151], v[4:7], v[164:167]\n" \
  "v_mfma_f32_16x16x32_bf16 v[164:167], v[144:147], v[0:3], v[164:167]\n" \
  "v_mfma_f32_16x16x32_bf16 v[172:175], v[144:147], v[16:19], 0\n" \
  "v_mfma_f32_16x16x32_bf16 v[172:175], v[148:151], v[20:23], v[172:175]\n" \
  "v_mfma_f32_16x16x32_bf16 v[172:175], v[152:155], v[24:27], v[172:175]\n" \
  "v_cvt_pk_bf16_f32 v176, v160, v161\n" \
  "v_cvt_pk_bf16_f32 v177, v162, v163\n" \
  "v_mfma_f32_16x16x32_bf16 v[172:175], v[156:159], v[28:31], v[172:175]\n" \
  "v_mfma_f32_16x16x32_bf16 v[168:171], v[140:143], v[28:31], 0\n" \
  "v_mfma_f32_16x16x32_bf16 v[168:171], v[136:139], v[24:27], v[168:171]\n" \
  "v_cvt_pk_bf16_f32 v178, v164, v165\n" \
  "v_cvt_pk_bf16_f32 v179, v166, v167\n" \
  "v_mfma_f32_16x16x32_bf16 v[168:171], v[132:135], v[20:23], v[168:171]\n" \
  "v_mfma_f32_16x16x32_bf16 v[168:171], v[128:131], v[16:19], v[168:171]\n" \
  "s_waitcnt vmcnt(63)\n" \
  "global_store_dwordx4 v186, v[176:179], s[46:47] offset:0 sc1\n" \
  "v_mfma_f32_16x16x32_bf16 v[160:163], v[128:131], v[32:35], 0\n" \
  "v_cvt_pk_bf16_f32 v182, v172, v173\n" \
  "v_cvt_pk_bf16_f32 v183, v174, v175\n" \
  "v_mfma_f32_16x16x32_bf16 v[160:163], v[132:135], v[36:39], v[160:163]\n" \
  "v_mfma_f32_16x16x32_bf16 v[160:163], v[136:139], v[40:43], v[160:163]\n" \
  "v_mfma_f32_16x16x32_bf16 v[160:163], v[140:143], v[44:47], v[160:163]\n" \
  "v_cvt_pk_bf16_f32 v180, v168, v169\n" \
  "v_cvt_pk_bf16_f32 v181, v170, v171\n" \
  "v_mfma_f32_16x16x32_bf16 v[164:167], v[156:159], v[44:47], 0\n" \
  "v_mfma_f32_16x16x32_bf16 v[164:167], v[152:155], v[40:43], v[164:167]\n" \
  "v_mfma_f32_16x16x32_bf16 v[164:167], v[148:151], v[36:39], v[164:167]\n" \
  "v_mfma_f32_16x16x32_bf16 v[164:167], v[144:147], v[32:35], v[164:167]\n" \
  "s_waitcnt vmcnt(63)\n" \
  "global_store_dwordx4 v186, v[180:183], s[46:47] offset:256 sc1\n" \
  "v_mfma_f32_16x16x32_bf16 v[172:175], v[144:147], v[48:51], 0\n" \
  "v_cvt_pk_bf16_f32 v176, v160, v161\n" \
  "v_cvt_pk_bf16_f32 v177, v162, v163\n" \
  "v_mfma_f32_16x16x32_bf16 v[172:175], v[148:151], v[52:55], v[172:175]\n" \
  "v_mfma_f32_16x16x32_bf16 v[172:175], v[152:155], v[56:59], v[172:175]\n" \
  "v_mfma_f32_16x16x32_bf16 v[172:175], v[156:159], v[60:63], v[172:175]\n" \
  "v_cvt_pk_bf16_f32 v178, v164, v165\n" \
  "v_cvt_pk_bf16_f32 v179, v166, v167\n" \
  "v_mfma_f32_16x16x32_bf16 v[168:171], v[140:143], v[60:63], 0\n" \
  "v_mfma_f32_16x16x32_bf16 v[168:171], v[136:139], v[56:59], v[168:171]\n" \
  "v_mfma_f32_16x16x32_bf16 v[168:171], v[132:135], v[52:55], v[168:171]\n" \
  "v_mfma_f32_16x16x32_bf16 v[168:171], v[128:131], v[48:51], v[168:171]\n" \
  "s_waitcnt vmcnt(63)\n" \
  "global_store_dwordx4 v186, v[176:179], s[46:47] offset:512 sc1\n" \
  "v_cvt_pk_bf16_f32 v182, v172, v173\n" \
  "v_cvt_pk_bf16_f32 v183, v174, v175\n" \
  "s_nop 3\n" \
  "v_cvt_pk_bf16_f32 v180, v168, v169\n" \
  "v_cvt_pk_bf16_f32 v181, v170, v171\n" \
  "s_waitcnt vmcnt(63)\n" \
  "global_store_dwordx4 v186, v[180:183], s[46:47] offset:768 sc1\n" \
  "s_branch LB_t_e\n" \
  "LT_w:\n" \
  "L8_:\n" \
  "s_sleep 1\n" \
  "ds_read_b32 v192, v190 offset:0\n" \
  "s_waitcnt lgkmcnt(0)\n" \
  "v_readfirstlane_b32 s35, v192\n" \
  "s_cmp_lt_u32 s35, s99\n" \
  "s_cbranch_scc1 L8_\n" \
  "s_branch LT_c\n" \
  "LT_e:\n" \
  "s_bitcmp1_b32 10, s98\n" \
  "s_cbranch_scc0 LT_r\n" \
  "global_load_dwordx4 v[240:243], v238, s[48:49] offset:0 sc1\n" \
  "s_waitcnt vmcnt(0)\n" \
  "global_store_dwordx4 v238, v[240:243], s[48:49] offset:1024 sc1\n" \
  "s_branch LT_r\n" \
  "LT_a:\n" \
  "s_cmp_lg_u32 s35, 4\n" \
  "s_cbranch_scc1 L9_\n" \
  "global_load_dword v193, v193, s[48:49] offset:1024 sc1\n" \
  "s_branch LT_s\n" \
  "L9_:\n" \
  "s_cmp_lg_u32 s35, 5\n" \
  "s_cbranch_scc1 L10_\n" \
  "s_memtime s[58:59]\n" \
  "s_memrealtime s[40:41]\n" \
  "s_mov_b32 s5, 0x10000\n" \
  "LB_ec:\n" \
  "v_readfirstlane_b32 s35, v193\n" \
  "s_cmp_eq_u32 s35, s39\n" \
  "s_cbranch_scc1 LB_ok\n" \
  "s_lshl_b32 s35, s26, 2\n" \
  "v_mov_b32 v193, s35\n" \
  "s_sleep 8\n" \
  "global_load_dword v193, v193, s[48:49] offset:1024 sc1\n" \
  "s_waitcnt vmcnt(0)\n" \
  "s_sub_u32 s5, s5, 1\n" \
  "s_cbranch_scc0 LB_ec\n" \
  "LB_ok:\n" \
  "s_lshl_b32 s35, s26, 2\n" \
  "v_mov_b32 v244, s35\n" \
  "s_waitcnt lgkmcnt(0)\n" \
  "s_sub_u32 s88, s58, s88\n" \
  "s_sub_u32 s88, 106000, s88\n" \
  "s_sub_u32 s90, s58, s90\n" \
  "s_sub_u32 s42, s40, s42\n" \
  "s_lshl_b32 s40, s40, 4\n" \
  "v_cvt_f32_u32 v245, s90\n" \
  "v_cvt_f32_u32 v246, s42\n" \
  "v_rcp_f32 v245, v245\n" \
  "v_cvt_f32_i32 v247, s88\n" \
  "v_mul_f32 v246, 0x4111eb85, v246\n" \
  "v_mul_f32 v245, v245, v246\n" \
  "v_mul_f32 v245, v245, v247\n" \
  "v_cvt_i32_f32 v245, v245\n" \
  "v_add_u32 v245, s40, v245\n" \
  "v_or_b32 v245, 1, v245\n" \
  "s_mov_b64 exec, 1\n" \
  "global_store_dword v244, v245, s[48:49] offset:2048 sc1\n" \
  "s_mov_b64 exec, -1\n" \
  "s_branch LT_s\n" \
  "L10_:\n" \
  "s_cmp_lg_u32 s35, 7\n" \
  "s_cbranch_scc1 L11_\n" \
  "s_mov_b32 exec_hi, 0\n" \
  "global_atomic_add v196, v191, s[48:49] offset:3072 sc1\n" \
  "s_mov_b64 exec, -1\n" \
  "s_branch LT_s\n" \
  "L11_:\n" \
  "s_cmp_lg_u32 s35, 23\n" \
  "s_cbranch_scc1 L12_\n" \
  "s_lshl_b32 s35, s30, 5\n" \
  "v_lshrrev_b32 v206, 3, v255\n" \
  "v_and_b32 v208, 7, v255\n" \
  "v_lshl_add_u32 v210, v206, 2, s35\n" \
  "v_lshl_add_u32 v212, v208, 2, s35\n" \
  "global_load_dword v202, v210, s[48:49] offset:2048 sc1\n" \
  "global_load_dword v204, v212, s[48:49] offset:2048 sc1\n" \
  "s_branch LT_s\n" \
  "L12_:\n" \
  "s_cmp_lg_u32 s35, 24\n" \
  "s_cbranch_scc1 L13_\n" \
  "s_mov_b32 s5, 0x10000\n" \
  "LB_z:\n" \
  "v_min_u32 v214, v202, v204\n" \
  "v_cmp_eq_u32 s[58:59], 0, v214\n" \
  "s_sub_u32 s5, s5, 1\n" \
  "s_cbranch_scc1 LB_zx\n" \
  "s_cmp_eq_u64 s[58:59], 0\n" \
  "s_cbranch_scc1 LB_zx\n" \
  "s_cmp_lg_u32 s33, 1021\n" \
  "s_cbranch_scc1 LB_n1\n" \
  "global_load_dwordx4 v[240:243], v238, s[48:49] offset:0 sc1\n" \
  "s_waitcnt vmcnt(0)\n" \
  "global_store_dwordx4 v238, v[240:243], s[48:49] offset:1024 sc1\n" \
  "LB_n1:\n" \
  "s_sleep 4\n" \
  "global_load_dword v202, v210, s[48:49] offset:2048 sc1\n" \
  "global_load_dword v204, v212, s[48:49] offset:2048 sc1\n" \
  "s_waitcnt vmcnt(0)\n" \
  "s_branch LB_z\n" \
  "LB_zx:\n" \
  "v_and_or_b32 v202, v202, -8, v206\n" \
  "v_and_or_b32 v204, v204, -8, v208\n" \
  "v_cmp_lt_u32 s[58:59], v204, v202\n" \
  "v_sub_u32 v202, v204, v202\n" \
  "s_lshl_b32 s35, s29, 3\n" \
  "s_lshr_b64 s[40:41], s[58:59], s35\n" \
  "s_and_b32 s40, s40, 0xff\n" \
  "s_bcnt1_i32_b32 s40, s40\n" \
  "s_sub_u32 s40, 7, s40\n" \
  "s_mov_b32 s36, s29\n" \
  "s_bfe_u32 s41, s58, 0x80000\n" \
  "s_bcnt1_i32_b32 s41, s41\n" \
  "s_cmp_eq_u32 s41, s40\n" \
  "s_cselect_b32 s36, 0, s36\n" \
  "s_bfe_u32 s41, s58, 0x80008\n" \
  "s_bcnt1_i32_b32 s41, s41\n" \
  "s_cmp_eq_u32 s41, s40\n" \
  "s_cselect_b32 s36, 1, s36\n" \
  "s_bfe_u32 s41, s58, 0x80010\n" \
  "s_bcnt1_i32_b32 s41, s41\n" \
  "s_cmp_eq_u32 s41, s40\n" \
  "s_cselect_b32 s36, 2, s36\n" \
  "s_bfe_u32 s41, s58, 0x80018\n" \
  "s_bcnt1_i32_b32 s41, s41\n" \
  "s_cmp_eq_u32 s41, s40\n" \
  "s_cselect_b32 s36, 3, s36\n" \
  "s_bfe_u32 s41, s59, 0x80000\n" \
  "s_bcnt1_i32_b32 s41, s41\n" \
  "s_cmp_eq_u32 s41, s40\n" \
  "s_cselect_b32 s36, 4, s36\n" \
  "s_bfe_u32 s41, s59, 0x80008\n" \
  "s_bcnt1_i32_b32 s41, s41\n" \
  "s_cmp_eq_u32 s41, s40\n" \
  "s_cselect_b32 s36, 5, s36\n" \
  "s_bfe_u32 s41, s59, 0x80010\n" \
  "s_bcnt1_i32_b32 s41, s41\n" \
  "s_cmp_eq_u32 s41, s40\n" \
  "s_cselect_b32 s36, 6, s36\n" \
  "s_bfe_u32 s41, s59, 0x80018\n" \
  "s_bcnt1_i32_b32 s41, s41\n" \
  "s_cmp_eq_u32 s41, s40\n" \
  "s_cselect_b32 s36, 7, s36\n" \
  "s_add_u32 s35, s35, s36\n" \
  "v_readlane_b32 s41, v202, s35\n" \
  "s_sub_u32 s35, 0, s41\n" \
  "s_cmp_lt_u32 s40, 4\n" \
  "s_cselect_b32 s41, s35, s41\n" \
  "s_min_u32 s41, s41, 0xffff\n" \
  "s_mul_i32 s41, s41, 39\n" \
  "s_add_u32 s41, s41, 28672\n" \
  "s_lshr_b32 s41, s41, 16\n" \
  "s_min_u32 s41, s41, 4\n" \
  "s_sub_u32 s35, 0, s41\n" \
  "s_cmp_lt_u32 s40, 4\n" \
  "s_cselect_b32 s35, s35, s41\n" \
  "s_lshl4_add_u32 s35, s36, s35\n" \
  "s_add_u32 s35, s35, 132\n" \
  "v_mov_b32 v204, s35\n" \
  "ds_write_b32 v190, v204 offset:4\n" \
  "s_branch LT_s\n" \
  "L13_:\n" \
  "s_cmp_lg_u32 s35, 25\n" \
  "s_cbranch_scc1 L14_\n" \
  "s_mov_b64 exec, 0xff\n" \
  "global_load_dword v200, v198, s[48:49] offset:3072 sc1\n" \
  "s_mov_b64 exec, -1\n" \
  "s_branch LT_s\n" \
  "L14_:\n" \
  "s_cmp_lg_u32 s35, 26\n" \
  "s_cbranch_scc1 L15_\n" \
  "s_mov_b64 s[58:59], vcc\n" \
  "s_mov_b32 s5, 0x10000\n" \
  "LB_p:\n" \
  "s_mov_b64 exec, 0xff\n" \
  "v_cmp_le_u32 vcc, 32, v200\n" \
  "s_mov_b64 exec, -1\n" \
  "s_sub_u32 s5, s5, 1\n" \
  "s_cselect_b32 s35, 0xff, vcc_lo\n" \
  "s_cmp_eq_u32 s35, 0xff\n" \
  "s_cbranch_scc1 LB_p_x\n" \
  "s_cmp_lg_u32 s33, 1021\n" \
  "s_cbranch_scc1 LB_n2\n" \
  "global_load_dwordx4 v[240:243], v238, s[48:49] offset:0 sc1\n" \
  "s_waitcnt vmcnt(0)\n" \
  "global_store_dwordx4 v238, v[240:243], s[48:49] offset:1024 sc1\n" \
  "LB_n2:\n" \
  "s_sleep 2\n" \
  "s_mov_b64 exec, 0xff\n" \
  "global_load_dword v200, v198, s[48:49] offset:3072 sc1\n" \
  "s_mov_b64 exec, -1\n" \
  "s_waitcnt vmcnt(0)\n" \
  "s_branch LB_p\n" \
  "LB_p_x:\n" \
  "s_mov_b64 vcc, s[58:59]\n" \
  "s_branch LT_s\n" \
  "L15_:\n" \
  "s_branch LT_s\n" \
  "LT_m:\n" \
  "ds_write_b16_d16_hi v195, v196 offset:0\n" \
  "ds_write_b16_d16_hi v195, v198 offset:16\n" \
  "ds_write_b16_d16_hi v195, v200 offset:32\n" \
  "ds_write_b16_d16_hi v235, v202 offset:64\n" \
  "ds_write_b16_d16_hi v235, v204 offset:80\n" \
  "ds_write_b16_d16_hi v235, v206 offset:96\n" \
  "ds_write_b16_d16_hi v235, v208 offset:112\n" \
  "ds_write_b16_d16_hi v235, v210 offset:128\n" \
  "ds_write_b16_d16_hi v235, v212 offset:144\n" \
  "ds_write_b16_d16_hi v235, v214 offset:160\n" \
  "ds_write_b16_d16_hi v235, v216 offset:176\n" \
  "ds_write_b16_d16_hi v195, v218 offset:48\n" \
  "ds_write_b16_d16_hi v195, v220 offset:192\n" \
  "ds_write_b16_d16_hi v195, v222 offset:208\n" \
  "ds_write_b16_d16_hi v195, v224 offset:224\n" \
  "ds_write_b16_d16_hi v195, v226 offset:240\n" \
  "ds_read_b128 v[244:247], v254 offset:0\n" \
  "ds_read_b128 v[248:251], v254 offset:1088\n" \
  "s_lshr_b32 s35, s7, 5\n" \
  "s_mul_i32 s35, s35, 819200\n" \
  "s_and_b32 s36, s7, 31\n" \
  "s_lshl_b32 s36, s36, 10\n" \
  "s_add_u32 s35, s35, s36\n" \
  "s_lshl_b32 s36, s4, 9\n" \
  "s_add_u32 s35, s35, s36\n" \
  "s_add_u32 s35, s35, 102400000\n" \
  "s_add_u32 s58, s22, s35\n" \
  "s_addc_u32 s59, s23, 0\n" \
  "s_waitcnt lgkmcnt(0)\n" \
  "s_cmp_ge_u32 s4, 768\n" \
  "s_cbranch_scc1 L16_\n" \
  "global_store_dwordx4 v238, v[244:247], s[58:59] sc1\n" \
  "s_cmp_ge_u32 s4, 768\n" \
  "s_cbranch_scc1 L16_\n" \
  "s_add_u32 s58, s58, 32768\n" \
  "s_addc_u32 s59, s59, 0\n" \
  "global_store_dwordx4 v238, v[248:251], s[58:59] sc1\n" \
  "L16_:\n" \
  "v_mov_b32 v234, 0\n" \
  "v_mov_b32 v236, v237\n" \
  "s_mov_b32 s38, 1\n" \
  "s_lshl_b32 s35, s29, 8\n" \
  "v_lshl_add_u32 v196, v255, 11, s35\n" \
  "s_lshl_b32 s35, s30, 11\n" \
  "v_lshl_add_u32 v198, v255, 8, s35\n" \
  "s_branch LT\n" \
  "LB_t_h:\n" \
  "s_waitcnt lgkmcnt(12)\n" \
  "v_mfma_f32_16x16x32_bf16 v[160:163], v[128:131], v[64:67], 0\n" \
  "v_mfma_f32_16x16x32_bf16 v[160:163], v[132:135], v[68:71], v[160:163]\n" \
  "s_waitcnt lgkmcnt(8)\n" \
  "v_mfma_f32_16x16x32_bf16 v[160:163], v[136:139], v[72:75], v[160:163]\n" \
  "v_mfma_f32_16x16x32_bf16 v[160:163], v[140:143], v[76:79], v[160:163]\n" \
  "s_waitcnt lgkmcnt(0)\n" \
  "v_mfma_f32_16x16x32_bf16 v[164:167], v[156:159], v[76:79], 0\n" \
  "v_mfma_f32_16x16x32_bf16 v[164:167], v[152:155], v[72:75], v[164:167]\n" \
  "v_mfma_f32_16x16x32_bf16 v[164:167], v[148:151], v[68:71], v[164:167]\n" \
  "v_mfma_f32_16x16x32_bf16 v[164:167], v[144:147], v[64:67], v[164:167]\n" \
  "v_mfma_f32_16x16x32_bf16 v[172:175], v[144:147], v[80:83], 0\n" \
  "v_mfma_f32_16x16x32_bf16 v[172:175], v[148:151], v[84:87], v[172:175]\n" \
  "v_mfma_f32_16x16x32_bf16 v[172:175], v[152:155], v[88:91], v[172:175]\n" \
  "v_cvt_pk_bf16_f32 v176, v160, v161\n" \
  "v_cvt_pk_bf16_f32 v177, v162, v163\n" \
  "v_mfma_f32_16x16x32_bf16 v[172:175], v[156:159], v[92:95], v[172:175]\n" \
  "v_mfma_f32_16x16x32_bf16 v[168:171], v[140:143], v[92:95], 0\n" \
  "v_mfma_f32_16x16x32_bf16 v[168:171], v[136:139], v[88:91], v[168:171]\n" \
  "v_cvt_pk_bf16_f32 v178, v164, v165\n" \
  "v_cvt_pk_bf16_f32 v179, v166, v167\n" \
  "v_mfma_f32_16x16x32_bf16 v[168:171], v[132:135], v[84:87], v[168:171]\n" \
  "v_mfma_f32_16x16x32_bf16 v[168:171], v[128:131], v[80:83], v[168:171]\n" \
  "s_waitcnt vmcnt(63)\n" \
  "global_store_dwordx4 v186, v[176:179], s[46:47] offset:1024 sc1\n" \
  "v_mfma_f32_16x16x32_bf16 v[160:163], v[128:131], v[96:99], 0\n" \
  "v_cvt_pk_bf16_f32 v182, v172, v173\n" \
  "v_cvt_pk_bf16_f32 v183, v174, v175\n" \
  "v_mfma_f32_16x16x32_bf16 v[160:163], v[132:135], v[100:103], v[160:163]\n" \
  "v_mfma_f32_16x16x32_bf16 v[160:163], v[136:139], v[104:107], v[160:163]\n" \
  "v_mfma_f32_16x16x32_bf16 v[160:163], v[140:143], v[108:111], v[160:163]\n" \
  "v_cvt_pk_bf16_f32 v180, v168, v169\n" \
  "v_cvt_pk_bf16_f32 v181, v170, v171\n" \
  "v_mfma_f32_16x16x32_bf16 v[164:167], v[156:159], v[108:111], 0\n" \
  "v_mfma_f32_16x16x32_bf16 v[164:167], v[152:155], v[104:107], v[164:167]\n" \
  "v_mfma_f32_16x16x32_bf16 v[164:167], v[148:151], v[100:103], v[164:167]\n" \
  "v_mfma_f32_16x16x32_bf16 v[164:167], v[144:147], v[96:99], v[164:167]\n" \
  "s_waitcnt vmcnt(63)\n" \
  "global_store_dwordx4 v186, v[180:183], s[46:47] offset:1280 sc1\n" \
  "v_mfma_f32_16x16x32_bf16 v[168:171], v[128:131], v[112:115], 0\n" \
  "v_cvt_pk_bf16_f32 v176, v160, v161\n" \
  "v_cvt_pk_bf16_f32 v177, v162, v163\n" \
  "v_mfma_f32_16x16x32_bf16 v[168:171], v[132:135], v[116:119], v[168:171]\n" \
  "v_mfma_f32_16x16x32_bf16 v[168:171], v[136:139], v[120:123], v[168:171]\n" \
  "v_mfma_f32_16x16x32_bf16 v[168:171], v[140:143], v[124:127], v[168:171]\n" \
  "v_cvt_pk_bf16_f32 v178, v164, v165\n" \
  "v_cvt_pk_bf16_f32 v179, v166, v167\n" \
  "v_mfma_f32_16x16x32_bf16 v[172:175], v[156:159], v[124:127], 0\n" \
  "v_mfma_f32_16x16x32_bf16 v[172:175], v[152:155], v[120:123], v[172:175]\n" \
  "v_mfma_f32_16x16x32_bf16 v[172:175], v[148:151], v[116:119], v[172:175]\n" \
  "v_mfma_f32_16x16x32_bf16 v[172:175], v[144:147], v[112:115], v[172:175]\n" \
  "s_waitcnt vmcnt(63)\n" \
  "global_store_dwordx4 v187, v[176:179], s[46:47] offset:0 sc1\n" \
  "v_cvt_pk_bf16_f32 v180, v168, v169\n" \
  "v_cvt_pk_bf16_f32 v181, v170, v171\n" \
  "s_nop 3\n" \
  "v_cvt_pk_bf16_f32 v182, v172, v173\n" \
  "v_cvt_pk_bf16_f32 v183, v174, v175\n" \
  "s_waitcnt vmcnt(63)\n" \
  "global_store_dwordx4 v188, v[180:183], s[46:47] offset:0 sc1\n" \
  "LB_t_e:\n" \
  "s_setprio 2\n" \
  "v_mov_b64 v[240:241], -1\n" \
  "v_mov_b64 v[242:243], -1\n" \
  "s_lshl2_add_u32 s35, s33, s34\n" \
  "s_lshl2_add_u32 s35, s35, s28\n" \
  "s_lshl_b32 s35, s35, 12\n" \
  "s_add_u32 s35, s35, 105676800\n" \
  "s_add_u32 s40, s22, s35\n" \
  "s_addc_u32 s41, s23, 0\n" \
  "v_mbcnt_lo_u32_b32 v189, -1, 0\n" \
  "v_mbcnt_hi_u32_b32 v189, -1, v189\n" \
  "v_lshlrev_b32 v188, 4, v189\n" \
  "s_lshl_b32 s35, s28, 12\n" \
  "v_add_u32 v128, s35, v188\n" \
  "v_mov_b32 v199, 1\n" \
  "v_lshrrev_b32 v180, 1, v189\n" \
  "v_xor_b32 v180, v180, v189\n" \
  "v_and_b32 v180, 8, v180\n" \
  "v_bfe_u32 v181, v189, 2, 1\n" \
  "v_lshlrev_b32_e64 v181, v181, 5\n" \
  "v_lshlrev_b32 v180, v180, v181\n" \
  "v_bfe_u32 v181, v189, 2, 2\n" \
  "v_lshlrev_b32_e64 v181, v181, 1\n" \
  "v_cmp_gt_u32 vcc, 32, v189\n" \
  "v_mov_b32 v191, 0x3f80\n" \
  "v_add_u32 v190, 32000, v188\n" \
  "v_cndmask_b32 v192, v181, v180, vcc\n" \
  "v_bfe_i32 v180, v192, 0, 1\n" \
  "v_and_b32 v180, v180, v191\n" \
  "v_bfe_i32 v181, v192, 1, 1\n" \
  "v_and_b32 v181, v181, v191\n" \
  "v_bfe_i32 v182, v192, 2, 1\n" \
  "v_and_b32 v182, v182, v191\n" \
  "v_bfe_i32 v183, v192, 3, 1\n" \
  "v_and_b32 v183, v183, v191\n" \
  "v_and_b32 v192, 3, v189\n" \
  "v_mul_u32_u24 v192, 0x5555, v192\n" \
  "v_add_u32 v189, 16000, v188\n" \
  "v_add_u32 v191, 48000, v188\n" \
  "s_waitcnt vmcnt(4)\n" \
  "v_lshlrev_b32 v184, 16, v204\n" \
  "v_and_b32 v185, 0xffff0000, v204\n" \
  "v_lshlrev_b32 v186, 16, v205\n" \
  "v_and_b32 v187, 0xffff0000, v205\n" \
  "global_store_dwordx4 v202, v[240:243], s[40:41] offset:0 sc1\n" \
  "global_store_dwordx4 v202, v[240:243], s[40:41] offset:1024 sc1\n" \
  "global_store_dwordx4 v202, v[240:243], s[40:41] offset:2048 sc1\n" \
  "global_store_dwordx4 v202, v[240:243], s[40:41] offset:3072 sc1\n" \
  "s_load_dwordx2 s[24:25], s[0:1], 0x40\n" \
  "s_setprio 0\n" \
  "s_barrier\n" \
  "s_bitcmp1_b32 s30, 2\n" \
  "s_cbranch_scc1 LB_w_h\n" \
  "ds_write_b128 v128, v[64:67] offset:0\n" \
  "ds_write_b128 v128, v[68:71] offset:1024\n" \
  "ds_write_b128 v128, v[72:75] offset:2048\n" \
  "ds_write_b128 v128, v[76:79] offset:3072\n" \
  "ds_write_b128 v128, v[80:83] offset:16384\n" \
  "ds_write_b128 v128, v[84:87] offset:17408\n" \
  "ds_write_b128 v128, v[88:91] offset:18432\n" \
  "ds_write_b128 v128, v[92:95] offset:19456\n" \
  "ds_write_b128 v128, v[96:99] offset:32768\n" \
  "ds_write_b128 v128, v[100:103] offset:33792\n" \
  "ds_write_b128 v128, v[104:107] offset:34816\n" \
  "ds_write_b128 v128, v[108:111] offset:35840\n" \
  "ds_write_b128 v128, v[112:115] offset:49152\n" \
  "ds_write_b128 v128, v[116:119] offset:50176\n" \
  "ds_write_b128 v128, v[120:123] offset:51200\n" \
  "ds_write_b128 v128, v[124:127] offset:52224\n" \
  "s_add_u32 s35, s59, 64000\n" \
  "s_add_u32 s54, s54, s35\n" \
  "s_addc_u32 s55, s55, 0\n" \
  "s_add_u32 s58, s59, 0x10000\n" \
  "s_mov_b32 m0, s58\n" \
  "s_add_u32 s35, s58, 0x8000\n" \
  "global_load_lds_dwordx4 v188, s[54:55] sc1\n" \
  "global_load_lds_dwordx4 v188, s[54:55] offset:1024 sc1\n" \
  "s_add_u32 m0, s58, 0x4000\n" \
  "s_cmp_lt_u32 s35, 0x28000\n" \
  "global_load_lds_dwordx4 v189, s[54:55] sc1\n" \
  "global_load_lds_dwordx4 v189, s[54:55] offset:1024 sc1\n" \
  "s_cselect_b32 s58, s35, s59\n" \
  "s_add_u32 s54, s54, 32000\n" \
  "s_addc_u32 s55, s55, 0\n" \
  "s_branch LB_w_e\n" \
  "LB_w_h:\n" \
  "ds_write_b128 v128, v[0:3] offset:0\n" \
  "ds_write_b128 v128, v[4:7] offset:1024\n" \
  "ds_write_b128 v128, v[8:11] offset:2048\n" \
  "ds_write_b128 v128, v[12:15] offset:3072\n" \
  "ds_write_b128 v128, v[16:19] offset:16384\n" \
  "ds_write_b128 v128, v[20:23] offset:17408\n" \
  "ds_write_b128 v128, v[24:27] offset:18432\n" \
  "ds_write_b128 v128, v[28:31] offset:19456\n" \
  "ds_write_b128 v128, v[32:35] offset:32768\n" \
  "ds_write_b128 v128, v[36:39] offset:33792\n" \
  "ds_write_b128 v128, v[40:43] offset:34816\n" \
  "ds_write_b128 v128, v[44:47] offset:35840\n" \
  "ds_write_b128 v128, v[48:51] offset:49152\n" \
  "ds_write_b128 v128, v[52:55] offset:50176\n" \
  "ds_write_b128 v128, v[56:59] offset:51200\n" \
  "ds_write_b128 v128, v[60:63] offset:52224\n" \
  "s_add_u32 s35, s59, 64000\n" \
  "s_add_u32 s54, s54, s35\n" \
  "s_addc_u32 s55, s55, 0\n" \
  "s_add_u32 s58, s59, 0x10000\n" \
  "s_mov_b32 m0, s58\n" \
  "s_add_u32 s35, s58, 0x8000\n" \
  "global_load_lds_dwordx4 v188, s[54:55] sc1\n" \
  "global_load_lds_dwordx4 v188, s[54:55] offset:1024 sc1\n" \
  "s_add_u32 m0, s58, 0x4000\n" \
  "s_cmp_lt_u32 s35, 0x28000\n" \
  "global_load_lds_dwordx4 v189, s[54:55] sc1\n" \
  "global_load_lds_dwordx4 v189, s[54:55] offset:1024 sc1\n" \
  "s_cselect_b32 s58, s35, s59\n" \
  "s_add_u32 s54, s54, 32000\n" \
  "s_addc_u32 s55, s55, 0\n" \
  "LB_w_e:\n" \
  "s_waitcnt lgkmcnt(0)\n" \
  "s_mov_b32 s51, 7\n" \
  "s_setprio 0\n" \
  "s_barrier\n" \
  "LG_start:\n" \
  "s_sub_u32 s35, 14960, s59\n" \
  "v_min_u32 v156, s35, v188\n" \
  "v_add_u32 v157, 16000, v156\n" \
  "v_add_u32 v158, 32000, v156\n" \
  "v_add_u32 v159, 48000, v156\n" \
  "v_lshlrev_b32_sdwa v64, 4, v208 dst_sel:DWORD dst_unused:UNUSED_PAD src0_sel:DWORD src1_sel:WORD_0\n" \
  "ds_read_b128 v[68:71], v64\n" \
  "v_mov_b64 v[0:1], v[184:185]\n" \
  "v_lshlrev_b32_sdwa v65, 4, v208 dst_sel:DWORD dst_unused:UNUSED_PAD src0_sel:DWORD src1_sel:WORD_1\n" \
  "ds_read_b128 v[72:75], v65\n" \
  "v_mov_b64 v[2:3], v[186:187]\n" \
  "v_lshlrev_b32_sdwa v66, 4, v209 dst_sel:DWORD dst_unused:UNUSED_PAD src0_sel:DWORD src1_sel:WORD_0\n" \
  "ds_read_b128 v[76:79], v66\n" \
  "v_mov_b64 v[4:5], v[184:185]\n" \
  "v_lshlrev_b32_sdwa v67, 4, v209 dst_sel:DWORD dst_unused:UNUSED_PAD src0_sel:DWORD src1_sel:WORD_1\n" \
  "ds_read_b128 v[80:83], v67\n" \
  "v_mov_b64 v[6:7], v[186:187]\n" \
  "s_mov_b32 m0, s58\n" \
  "s_add_u32 s35, s58, 0x8000\n" \
  "global_load_lds_dwordx4 v188, s[54:55] sc1\n" \
  "global_load_lds_dwordx4 v156, s[54:55] offset:1024 sc1\n" \
  "v_lshlrev_b32_sdwa v64, 4, v210 dst_sel:DWORD dst_unused:UNUSED_PAD src0_sel:DWORD src1_sel:WORD_0\n" \
  "ds_read_b128 v[84:87], v64\n" \
  "v_mov_b64 v[8:9], v[184:185]\n" \
  "v_lshlrev_b32_sdwa v65, 4, v210 dst_sel:DWORD dst_unused:UNUSED_PAD src0_sel:DWORD src1_sel:WORD_1\n" \
  "ds_read_b128 v[88:91], v65\n" \
  "v_mov_b64 v[10:11], v[186:187]\n" \
  "v_lshlrev_b32_sdwa v66, 4, v211 dst_sel:DWORD dst_unused:UNUSED_PAD src0_sel:DWORD src1_sel:WORD_0\n" \
  "ds_read_b128 v[92:95], v66\n" \
  "v_mov_b64 v[12:13], v[184:185]\n" \
  "v_lshlrev_b32_sdwa v67, 4, v211 dst_sel:DWORD dst_unused:UNUSED_PAD src0_sel:DWORD src1_sel:WORD_1\n" \
  "ds_read_b128 v[96:99], v67\n" \
  "v_mov_b64 v[14:15], v[186:187]\n" \
  "s_add_u32 m0, s58, 0x4000\n" \
  "s_cmp_lt_u32 s35, 0x28000\n" \
  "global_load_lds_dwordx4 v189, s[54:55] sc1\n" \
  "global_load_lds_dwordx4 v157, s[54:55] offset:1024 sc1\n" \
  "s_cselect_b32 s58, s35, s59\n" \
  "v_lshlrev_b32_sdwa v64, 4, v212 dst_sel:DWORD dst_unused:UNUSED_PAD src0_sel:DWORD src1_sel:WORD_0\n" \
  "ds_read_b128 v[100:103], v64\n" \
  "v_mov_b64 v[16:17], v[184:185]\n" \
  "v_lshlrev_b32_sdwa v65, 4, v212 dst_sel:DWORD dst_unused:UNUSED_PAD src0_sel:DWORD src1_sel:WORD_1\n" \
  "ds_read_b128 v[104:107], v65\n" \
  "v_mov_b64 v[18:19], v[186:187]\n" \
  "v_lshlrev_b32_sdwa v66, 4, v213 dst_sel:DWORD dst_unused:UNUSED_PAD src0_sel:DWORD src1_sel:WORD_0\n" \
  "ds_read_b128 v[108:111], v66\n" \
  "v_mov_b64 v[20:21], v[184:185]\n" \
  "v_lshlrev_b32_sdwa v67, 4, v213 dst_sel:DWORD dst_unused:UNUSED_PAD src0_sel:DWORD src1_sel:WORD_1\n" \
  "ds_read_b128 v[112:115], v67\n" \
  "v_mov_b64 v[22:23], v[186:187]\n" \
  "s_waitcnt lgkmcnt(10)\n" \
  "v_smfmac_f32_16x16x64_bf16 v[0:3], v[180:183], v[68:75], v192\n" \
  "v_lshlrev_b32_sdwa v64, 4, v214 dst_sel:DWORD dst_unused:UNUSED_PAD src0_sel:DWORD src1_sel:WORD_0\n" \
  "ds_read_b128 v[68:71], v64\n" \
  "v_mov_b64 v[24:25], v[184:185]\n" \
  "v_lshlrev_b32_sdwa v65, 4, v214 dst_sel:DWORD dst_unused:UNUSED_PAD src0_sel:DWORD src1_sel:WORD_1\n" \
  "ds_read_b128 v[72:75], v65\n" \
  "v_mov_b64 v[26:27], v[186:187]\n" \
  "s_mov_b32 m0, s58\n" \
  "s_add_u32 s35, s58, 0x8000\n" \
  "global_load_lds_dwordx4 v190, s[54:55] sc1\n" \
  "global_load_lds_dwordx4 v158, s[54:55] offset:1024 sc1\n" \
  "s_waitcnt lgkmcnt(10)\n" \
  "v_smfmac_f32_16x16x64_bf16 v[4:7], v[180:183], v[76:83], v192\n" \
  "v_lshlrev_b32_sdwa v66, 4, v215 dst_sel:DWORD dst_unused:UNUSED_PAD src0_sel:DWORD src1_sel:WORD_0\n" \
  "ds_read_b128 v[76:79], v66\n" \
  "v_mov_b64 v[28:29], v[184:185]\n" \
  "v_lshlrev_b32_sdwa v67, 4, v215 dst_sel:DWORD dst_unused:UNUSED_PAD src0_sel:DWORD src1_sel:WORD_1\n" \
  "ds_read_b128 v[80:83], v67\n" \
  "v_mov_b64 v[30:31], v[186:187]\n" \
  "s_waitcnt lgkmcnt(10)\n" \
  "v_smfmac_f32_16x16x64_bf16 v[8:11], v[180:183], v[84:91], v192\n" \
  "v_lshlrev_b32_sdwa v64, 4, v216 dst_sel:DWORD dst_unused:UNUSED_PAD src0_sel:DWORD src1_sel:WORD_0\n" \
  "ds_read_b128 v[84:87], v64\n" \
  "v_mov_b64 v[32:33], v[184:185]\n" \
  "v_lshlrev_b32_sdwa v65, 4, v216 dst_sel:DWORD dst_unused:UNUSED_PAD src0_sel:DWORD src1_sel:WORD_1\n" \
  "ds_read_b128 v[88:91], v65\n" \
  "v_mov_b64 v[34:35], v[186:187]\n" \
  "s_add_u32 m0, s58, 0x4000\n" \
  "s_cmp_lt_u32 s35, 0x28000\n" \
  "global_load_lds_dwordx4 v191, s[54:55] sc1\n" \
  "global_load_lds_dwordx4 v159, s[54:55] offset:1024 sc1\n" \
  "s_cselect_b32 s58, s35, s59\n" \
  "s_waitcnt lgkmcnt(10)\n" \
  "v_smfmac_f32_16x16x64_bf16 v[12:15], v[180:183], v[92:99], v192\n" \
  "v_lshlrev_b32_sdwa v66, 4, v217 dst_sel:DWORD dst_unused:UNUSED_PAD src0_sel:DWORD src1_sel:WORD_0\n" \
  "ds_read_b128 v[92:95], v66\n" \
  "v_mov_b64 v[36:37], v[184:185]\n" \
  "v_lshlrev_b32_sdwa v67, 4, v217 dst_sel:DWORD dst_unused:UNUSED_PAD src0_sel:DWORD src1_sel:WORD_1\n" \
  "ds_read_b128 v[96:99], v67\n" \
  "v_mov_b64 v[38:39], v[186:187]\n" \
  "s_waitcnt lgkmcnt(10)\n" \
  "v_smfmac_f32_16x16x64_bf16 v[16:19], v[180:183], v[100:107], v192\n" \
  "v_lshlrev_b32_sdwa v64, 4, v218 dst_sel:DWORD dst_unused:UNUSED_PAD src0_sel:DWORD src1_sel:WORD_0\n" \
  "ds_read_b128 v[100:103], v64\n" \
  "v_mov_b64 v[40:41], v[184:185]\n" \
  "v_lshlrev_b32_sdwa v65, 4, v218 dst_sel:DWORD dst_unused:UNUSED_PAD src0_sel:DWORD src1_sel:WORD_1\n" \
  "ds_read_b128 v[104:107], v65\n" \
  "v_mov_b64 v[42:43], v[186:187]\n" \
  "global_load_dwordx4 v[240:243], v188, s[56:57] sc1\n" \
  "global_load_dwordx4 v[244:247], v188, s[56:57] offset:1024 sc1\n" \
  "s_waitcnt lgkmcnt(10)\n" \
  "v_smfmac_f32_16x16x64_bf16 v[20:23], v[180:183], v[108:115], v192\n" \
  "v_lshlrev_b32_sdwa v66, 4, v219 dst_sel:DWORD dst_unused:UNUSED_PAD src0_sel:DWORD src1_sel:WORD_0\n" \
  "ds_read_b128 v[108:111], v66\n" \
  "v_mov_b64 v[44:45], v[184:185]\n" \
  "v_lshlrev_b32_sdwa v67, 4, v219 dst_sel:DWORD dst_unused:UNUSED_PAD src0_sel:DWORD src1_sel:WORD_1\n" \
  "ds_read_b128 v[112:115], v67\n" \
  "v_mov_b64 v[46:47], v[186:187]\n" \
  "s_waitcnt lgkmcnt(10)\n" \
  "v_smfmac_f32_16x16x64_bf16 v[24:27], v[180:183], v[68:75], v192\n" \
  "v_lshlrev_b32_sdwa v64, 4, v220 dst_sel:DWORD dst_unused:UNUSED_PAD src0_sel:DWORD src1_sel:WORD_0\n" \
  "ds_read_b128 v[68:71], v64\n" \
  "v_mov_b64 v[48:49], v[184:185]\n" \
  "v_lshlrev_b32_sdwa v65, 4, v220 dst_sel:DWORD dst_unused:UNUSED_PAD src0_sel:DWORD src1_sel:WORD_1\n" \
  "ds_read_b128 v[72:75], v65\n" \
  "v_mov_b64 v[50:51], v[186:187]\n" \
  "global_load_dwordx4 v[248:251], v188, s[56:57] offset:2048 sc1\n" \
  "global_load_dwordx4 v[252:255], v188, s[56:57] offset:3072 sc1\n" \
  "s_waitcnt lgkmcnt(10)\n" \
  "v_smfmac_f32_16x16x64_bf16 v[28:31], v[180:183], v[76:83], v192\n" \
  "v_lshlrev_b32_sdwa v66, 4, v221 dst_sel:DWORD dst_unused:UNUSED_PAD src0_sel:DWORD src1_sel:WORD_0\n" \
  "ds_read_b128 v[76:79], v66\n" \
  "v_mov_b64 v[52:53], v[184:185]\n" \
  "v_lshlrev_b32_sdwa v67, 4, v221 dst_sel:DWORD dst_unused:UNUSED_PAD src0_sel:DWORD src1_sel:WORD_1\n" \
  "ds_read_b128 v[80:83], v67\n" \
  "v_mov_b64 v[54:55], v[186:187]\n" \
  "s_waitcnt lgkmcnt(10)\n" \
  "v_smfmac_f32_16x16x64_bf16 v[32:35], v[180:183], v[84:91], v192\n" \
  "v_lshlrev_b32_sdwa v64, 4, v222 dst_sel:DWORD dst_unused:UNUSED_PAD src0_sel:DWORD src1_sel:WORD_0\n" \
  "ds_read_b128 v[84:87], v64\n" \
  "v_mov_b64 v[56:57], v[184:185]\n" \
  "v_lshlrev_b32_sdwa v65, 4, v222 dst_sel:DWORD dst_unused:UNUSED_PAD src0_sel:DWORD src1_sel:WORD_1\n" \
  "ds_read_b128 v[88:91], v65\n" \
  "v_mov_b64 v[58:59], v[186:187]\n" \
  "s_waitcnt lgkmcnt(10)\n" \
  "v_smfmac_f32_16x16x64_bf16 v[36:39], v[180:183], v[92:99], v192\n" \
  "v_lshlrev_b32_sdwa v66, 4, v223 dst_sel:DWORD dst_unused:UNUSED_PAD src0_sel:DWORD src1_sel:WORD_0\n" \
  "ds_read_b128 v[92:95], v66\n" \
  "v_mov_b64 v[60:61], v[184:185]\n" \
  "v_lshlrev_b32_sdwa v67, 4, v223 dst_sel:DWORD dst_unused:UNUSED_PAD src0_sel:DWORD src1_sel:WORD_1\n" \
  "ds_read_b128 v[96:99], v67\n" \
  "v_mov_b64 v[62:63], v[186:187]\n" \
  "s_add_u32 s56, s56, 32768\n" \
  "s_addc_u32 s57, s57, 0\n" \
  "s_add_u32 s54, s54, 64000\n" \
  "s_addc_u32 s55, s55, 0\n" \
  "s_cmp_eq_u32 s27, 4\n" \
  "s_cselect_b32 s37, 0x90, 0\n" \
  ".p2align 6\n" \
  "LG_loop:\n" \
  "s_bitcmp1_b32 s37, s51\n" \
  "s_waitcnt lgkmcnt(10)\n" \
  "v_smfmac_f32_16x16x64_bf16 v[40:43], v[180:183], v[100:107], v192\n" \
  "s_waitcnt lgkmcnt(8)\n" \
  "v_smfmac_f32_16x16x64_bf16 v[44:47], v[180:183], v[108:115], v192\n" \
  "s_waitcnt vmcnt(8)\n" \
  "s_cbranch_scc1 LG_h\n" \
  "LG_ret:\n" \
  "s_barrier\n" \
  "LG_aft:\n" \
  "v_lshlrev_b32_sdwa v64, 4, v224 dst_sel:DWORD dst_unused:UNUSED_PAD src0_sel:DWORD src1_sel:WORD_0\n" \
  "ds_read_b128 v[100:103], v64\n" \
  "v_lshlrev_b32_sdwa v65, 4, v224 dst_sel:DWORD dst_unused:UNUSED_PAD src0_sel:DWORD src1_sel:WORD_1\n" \
  "ds_read_b128 v[104:107], v65\n" \
  "v_lshlrev_b32_sdwa v66, 4, v225 dst_sel:DWORD dst_unused:UNUSED_PAD src0_sel:DWORD src1_sel:WORD_0\n" \
  "ds_read_b128 v[108:111], v66\n" \
  "v_lshlrev_b32_sdwa v67, 4, v225 dst_sel:DWORD dst_unused:UNUSED_PAD src0_sel:DWORD src1_sel:WORD_1\n" \
  "ds_read_b128 v[112:115], v67\n" \
  "s_waitcnt lgkmcnt(10)\n" \
  "v_smfmac_f32_16x16x64_bf16 v[48:51], v[180:183], v[68:75], v192\n" \
  "s_waitcnt lgkmcnt(8)\n" \
  "v_smfmac_f32_16x16x64_bf16 v[52:55], v[180:183], v[76:83], v192\n" \
  "s_waitcnt lgkmcnt(6)\n" \
  "v_smfmac_f32_16x16x64_bf16 v[56:59], v[180:183], v[84:91], v192\n" \
  "s_waitcnt lgkmcnt(4)\n" \
  "v_smfmac_f32_16x16x64_bf16 v[60:63], v[180:183], v[92:99], v192\n" \
  "s_barrier\n" \
  "s_mov_b32 m0, s58\n" \
  "s_add_u32 s35, s58, 0x8000\n" \
  "global_load_lds_dwordx4 v188, s[54:55] sc1\n" \
  "global_load_lds_dwordx4 v156, s[54:55] offset:1024 sc1\n" \
  "v_lshlrev_b32_sdwa v64, 4, v226 dst_sel:DWORD dst_unused:UNUSED_PAD src0_sel:DWORD src1_sel:WORD_0\n" \
  "ds_read_b128 v[68:71], v64\n" \
  "v_lshlrev_b32_sdwa v65, 4, v226 dst_sel:DWORD dst_unused:UNUSED_PAD src0_sel:DWORD src1_sel:WORD_1\n" \
  "ds_read_b128 v[72:75], v65\n" \
  "v_lshlrev_b32_sdwa v66, 4, v227 dst_sel:DWORD dst_unused:UNUSED_PAD src0_sel:DWORD src1_sel:WORD_0\n" \
  "ds_read_b128 v[76:79], v66\n" \
  "v_lshlrev_b32_sdwa v67, 4, v227 dst_sel:DWORD dst_unused:UNUSED_PAD src0_sel:DWORD src1_sel:WORD_1\n" \
  "ds_read_b128 v[80:83], v67\n" \
  "s_add_u32 m0, s58, 0x4000\n" \
  "s_cmp_lt_u32 s35, 0x28000\n" \
  "global_load_lds_dwordx4 v189, s[54:55] sc1\n" \
  "global_load_lds_dwordx4 v157, s[54:55] offset:1024 sc1\n" \
  "s_cselect_b32 s58, s35, s59\n" \
  "v_lshlrev_b32_sdwa v64, 4, v228 dst_sel:DWORD dst_unused:UNUSED_PAD src0_sel:DWORD src1_sel:WORD_0\n" \
  "ds_read_b128 v[84:87], v64\n" \
  "v_lshlrev_b32_sdwa v65, 4, v228 dst_sel:DWORD dst_unused:UNUSED_PAD src0_sel:DWORD src1_sel:WORD_1\n" \
  "ds_read_b128 v[88:91], v65\n" \
  "v_lshlrev_b32_sdwa v66, 4, v229 dst_sel:DWORD dst_unused:UNUSED_PAD src0_sel:DWORD src1_sel:WORD_0\n" \
  "ds_read_b128 v[92:95], v66\n" \
  "v_lshlrev_b32_sdwa v67, 4, v229 dst_sel:DWORD dst_unused:UNUSED_PAD src0_sel:DWORD src1_sel:WORD_1\n" \
  "ds_read_b128 v[96:99], v67\n" \
  "s_waitcnt lgkmcnt(10)\n" \
  "v_smfmac_f32_16x16x64_bf16 v[0:3], v[180:183], v[100:107], v192\n" \
  "v_lshlrev_b32_sdwa v64, 4, v230 dst_sel:DWORD dst_unused:UNUSED_PAD src0_sel:DWORD src1_sel:WORD_0\n" \
  "ds_read_b128 v[100:103], v64\n" \
  "v_lshlrev_b32_sdwa v65, 4, v230 dst_sel:DWORD dst_unused:UNUSED_PAD src0_sel:DWORD src1_sel:WORD_1\n" \
  "ds_read_b128 v[104:107], v65\n" \
  "s_mov_b32 m0, s58\n" \
  "s_add_u32 s35, s58, 0x8000\n" \
  "global_load_lds_dwordx4 v190, s[54:55] sc1\n" \
  "global_load_lds_dwordx4 v158, s[54:55] offset:1024 sc1\n" \
  "s_waitcnt lgkmcnt(10)\n" \
  "v_smfmac_f32_16x16x64_bf16 v[4:7], v[180:183], v[108:115], v192\n" \
  "v_lshlrev_b32_sdwa v66, 4, v231 dst_sel:DWORD dst_unused:UNUSED_PAD src0_sel:DWORD src1_sel:WORD_0\n" \
  "ds_read_b128 v[108:111], v66\n" \
  "v_lshlrev_b32_sdwa v67, 4, v231 dst_sel:DWORD dst_unused:UNUSED_PAD src0_sel:DWORD src1_sel:WORD_1\n" \
  "ds_read_b128 v[112:115], v67\n" \
  "s_waitcnt lgkmcnt(10)\n" \
  "v_smfmac_f32_16x16x64_bf16 v[8:11], v[180:183], v[68:75], v192\n" \
  "v_lshlrev_b32_sdwa v64, 4, v232 dst_sel:DWORD dst_unused:UNUSED_PAD src0_sel:DWORD src1_sel:WORD_0\n" \
  "ds_read_b128 v[68:71], v64\n" \
  "v_lshlrev_b32_sdwa v65, 4, v232 dst_sel:DWORD dst_unused:UNUSED_PAD src0_sel:DWORD src1_sel:WORD_1\n" \
  "ds_read_b128 v[72:75], v65\n" \
  "s_add_u32 m0, s58, 0x4000\n" \
  "s_cmp_lt_u32 s35, 0x28000\n" \
  "global_load_lds_dwordx4 v191, s[54:55] sc1\n" \
  "global_load_lds_dwordx4 v159, s[54:55] offset:1024 sc1\n" \
  "s_cselect_b32 s58, s35, s59\n" \
  "s_waitcnt lgkmcnt(10)\n" \
  "v_smfmac_f32_16x16x64_bf16 v[12:15], v[180:183], v[76:83], v192\n" \
  "v_lshlrev_b32_sdwa v66, 4, v233 dst_sel:DWORD dst_unused:UNUSED_PAD src0_sel:DWORD src1_sel:WORD_0\n" \
  "ds_read_b128 v[76:79], v66\n" \
  "v_lshlrev_b32_sdwa v67, 4, v233 dst_sel:DWORD dst_unused:UNUSED_PAD src0_sel:DWORD src1_sel:WORD_1\n" \
  "ds_read_b128 v[80:83], v67\n" \
  "s_waitcnt lgkmcnt(10)\n" \
  "v_smfmac_f32_16x16x64_bf16 v[16:19], v[180:183], v[84:91], v192\n" \
  "v_lshlrev_b32_sdwa v64, 4, v234 dst_sel:DWORD dst_unused:UNUSED_PAD src0_sel:DWORD src1_sel:WORD_0\n" \
  "ds_read_b128 v[84:87], v64\n" \
  "v_lshlrev_b32_sdwa v65, 4, v234 dst_sel:DWORD dst_unused:UNUSED_PAD src0_sel:DWORD src1_sel:WORD_1\n" \
  "ds_read_b128 v[88:91], v65\n" \
  "global_load_dwordx4 v[208:211], v188, s[56:57] sc1\n" \
  "global_load_dwordx4 v[212:215], v188, s[56:57] offset:1024 sc1\n" \
  "s_waitcnt lgkmcnt(10)\n" \
  "v_smfmac_f32_16x16x64_bf16 v[20:23], v[180:183], v[92:99], v192\n" \
  "v_lshlrev_b32_sdwa v66, 4, v235 dst_sel:DWORD dst_unused:UNUSED_PAD src0_sel:DWORD src1_sel:WORD_0\n" \
  "ds_read_b128 v[92:95], v66\n" \
  "v_lshlrev_b32_sdwa v67, 4, v235 dst_sel:DWORD dst_unused:UNUSED_PAD src0_sel:DWORD src1_sel:WORD_1\n" \
  "ds_read_b128 v[96:99], v67\n" \
  "s_waitcnt lgkmcnt(10)\n" \
  "v_smfmac_f32_16x16x64_bf16 v[24:27], v[180:183], v[100:107], v192\n" \
  "v_lshlrev_b32_sdwa v64, 4, v236 dst_sel:DWORD dst_unused:UNUSED_PAD src0_sel:DWORD src1_sel:WORD_0\n" \
  "ds_read_b128 v[100:103], v64\n" \
  "v_lshlrev_b32_sdwa v65, 4, v236 dst_sel:DWORD dst_unused:UNUSED_PAD src0_sel:DWORD src1_sel:WORD_1\n" \
  "ds_read_b128 v[104:107], v65\n" \
  "global_load_dwordx4 v[216:219], v188, s[56:57] offset:2048 sc1\n" \
  "global_load_dwordx4 v[220:223], v188, s[56:57] offset:3072 sc1\n" \
  "s_waitcnt lgkmcnt(10)\n" \
  "v_smfmac_f32_16x16x64_bf16 v[28:31], v[180:183], v[108:115], v192\n" \
  "v_lshlrev_b32_sdwa v66, 4, v237 dst_sel:DWORD dst_unused:UNUSED_PAD src0_sel:DWORD src1_sel:WORD_0\n" \
  "ds_read_b128 v[108:111], v66\n" \
  "v_lshlrev_b32_sdwa v67, 4, v237 dst_sel:DWORD dst_unused:UNUSED_PAD src0_sel:DWORD src1_sel:WORD_1\n" \
  "ds_read_b128 v[112:115], v67\n" \
  "s_waitcnt lgkmcnt(10)\n" \
  "v_smfmac_f32_16x16x64_bf16 v[32:35], v[180:183], v[68:75], v192\n" \
  "v_lshlrev_b32_sdwa v64, 4, v238 dst_sel:DWORD dst_unused:UNUSED_PAD src0_sel:DWORD src1_sel:WORD_0\n" \
  "ds_read_b128 v[68:71], v64\n" \
  "v_lshlrev_b32_sdwa v65, 4, v238 dst_sel:DWORD dst_unused:UNUSED_PAD src0_sel:DWORD src1_sel:WORD_1\n" \
  "ds_read_b128 v[72:75], v65\n" \
  "s_waitcnt lgkmcnt(10)\n" \
  "v_smfmac_f32_16x16x64_bf16 v[36:39], v[180:183], v[76:83], v192\n" \
  "v_lshlrev_b32_sdwa v66, 4, v239 dst_sel:DWORD dst_unused:UNUSED_PAD src0_sel:DWORD src1_sel:WORD_0\n" \
  "ds_read_b128 v[76:79], v66\n" \
  "v_lshlrev_b32_sdwa v67, 4, v239 dst_sel:DWORD dst_unused:UNUSED_PAD src0_sel:DWORD src1_sel:WORD_1\n" \
  "ds_read_b128 v[80:83], v67\n" \
  "s_add_u32 s56, s56, 32768\n" \
  "s_addc_u32 s57, s57, 0\n" \
  "s_add_u32 s54, s54, 64000\n" \
  "s_addc_u32 s55, s55, 0\n" \
  "s_waitcnt lgkmcnt(10)\n" \
  "v_smfmac_f32_16x16x64_bf16 v[40:43], v[180:183], v[84:91], v192\n" \
  "s_waitcnt lgkmcnt(8)\n" \
  "v_smfmac_f32_16x16x64_bf16 v[44:47], v[180:183], v[92:99], v192\n" \
  "s_waitcnt vmcnt(8)\n" \
  "s_barrier\n" \
  "v_lshlrev_b32_sdwa v64, 4, v240 dst_sel:DWORD dst_unused:UNUSED_PAD src0_sel:DWORD src1_sel:WORD_0\n" \
  "ds_read_b128 v[84:87], v64\n" \
  "v_lshlrev_b32_sdwa v65, 4, v240 dst_sel:DWORD dst_unused:UNUSED_PAD src0_sel:DWORD src1_sel:WORD_1\n" \
  "ds_read_b128 v[88:91], v65\n" \
  "v_lshlrev_b32_sdwa v66, 4, v241 dst_sel:DWORD dst_unused:UNUSED_PAD src0_sel:DWORD src1_sel:WORD_0\n" \
  "ds_read_b128 v[92:95], v66\n" \
  "v_lshlrev_b32_sdwa v67, 4, v241 dst_sel:DWORD dst_unused:UNUSED_PAD src0_sel:DWORD src1_sel:WORD_1\n" \
  "ds_read_b128 v[96:99], v67\n" \
  "s_waitcnt lgkmcnt(10)\n" \
  "v_smfmac_f32_16x16x64_bf16 v[48:51], v[180:183], v[100:107], v192\n" \
  "s_waitcnt lgkmcnt(8)\n" \
  "v_smfmac_f32_16x16x64_bf16 v[52:55], v[180:183], v[108:115], v192\n" \
  "s_waitcnt lgkmcnt(6)\n" \
  "v_smfmac_f32_16x16x64_bf16 v[56:59], v[180:183], v[68:75], v192\n" \
  "s_waitcnt lgkmcnt(4)\n" \
  "v_smfmac_f32_16x16x64_bf16 v[60:63], v[180:183], v[76:83], v192\n" \
  "s_barrier\n" \
  "s_mov_b32 m0, s58\n" \
  "s_add_u32 s35, s58, 0x8000\n" \
  "global_load_lds_dwordx4 v188, s[54:55] sc1\n" \
  "global_load_lds_dwordx4 v156, s[54:55] offset:1024 sc1\n" \
  "v_lshlrev_b32_sdwa v64, 4, v242 dst_sel:DWORD dst_unused:UNUSED_PAD src0_sel:DWORD src1_sel:WORD_0\n" \
  "ds_read_b128 v[100:103], v64\n" \
  "v_lshlrev_b32_sdwa v65, 4, v242 dst_sel:DWORD dst_unused:UNUSED_PAD src0_sel:DWORD src1_sel:WORD_1\n" \
  "ds_read_b128 v[104:107], v65\n" \
  "v_lshlrev_b32_sdwa v66, 4, v243 dst_sel:DWORD dst_unused:UNUSED_PAD src0_sel:DWORD src1_sel:WORD_0\n" \
  "ds_read_b128 v[108:111], v66\n" \
  "v_lshlrev_b32_sdwa v67, 4, v243 dst_sel:DWORD dst_unused:UNUSED_PAD src0_sel:DWORD src1_sel:WORD_1\n" \
  "ds_read_b128 v[112:115], v67\n" \
  "s_add_u32 m0, s58, 0x4000\n" \
  "s_cmp_lt_u32 s35, 0x28000\n" \
  "global_load_lds_dwordx4 v189, s[54:55] sc1\n" \
  "global_load_lds_dwordx4 v157, s[54:55] offset:1024 sc1\n" \
  "s_cselect_b32 s58, s35, s59\n" \
  "v_lshlrev_b32_sdwa v64, 4, v244 dst_sel:DWORD dst_unused:UNUSED_PAD src0_sel:DWORD src1_sel:WORD_0\n" \
  "ds_read_b128 v[68:71], v64\n" \
  "v_lshlrev_b32_sdwa v65, 4, v244 dst_sel:DWORD dst_unused:UNUSED_PAD src0_sel:DWORD src1_sel:WORD_1\n" \
  "ds_read_b128 v[72:75], v65\n" \
  "v_lshlrev_b32_sdwa v66, 4, v245 dst_sel:DWORD dst_unused:UNUSED_PAD src0_sel:DWORD src1_sel:WORD_0\n" \
  "ds_read_b128 v[76:79], v66\n" \
  "v_lshlrev_b32_sdwa v67, 4, v245 dst_sel:DWORD dst_unused:UNUSED_PAD src0_sel:DWORD src1_sel:WORD_1\n" \
  "ds_read_b128 v[80:83], v67\n" \
  "s_waitcnt lgkmcnt(10)\n" \
  "v_smfmac_f32_16x16x64_bf16 v[0:3], v[180:183], v[84:91], v192\n" \
  "v_lshlrev_b32_sdwa v64, 4, v246 dst_sel:DWORD dst_unused:UNUSED_PAD src0_sel:DWORD src1_sel:WORD_0\n" \
  "ds_read_b128 v[84:87], v64\n" \
  "v_lshlrev_b32_sdwa v65, 4, v246 dst_sel:DWORD dst_unused:UNUSED_PAD src0_sel:DWORD src1_sel:WORD_1\n" \
  "ds_read_b128 v[88:91], v65\n" \
  "s_mov_b32 m0, s58\n" \
  "s_add_u32 s35, s58, 0x8000\n" \
  "global_load_lds_dwordx4 v190, s[54:55] sc1\n" \
  "global_load_lds_dwordx4 v158, s[54:55] offset:1024 sc1\n" \
  "s_waitcnt lgkmcnt(10)\n" \
  "v_smfmac_f32_16x16x64_bf16 v[4:7], v[180:183], v[92:99], v192\n" \
  "v_lshlrev_b32_sdwa v66, 4, v247 dst_sel:DWORD dst_unused:UNUSED_PAD src0_sel:DWORD src1_sel:WORD_0\n" \
  "ds_read_b128 v[92:95], v66\n" \
  "v_lshlrev_b32_sdwa v67, 4, v247 dst_sel:DWORD dst_unused:UNUSED_PAD src0_sel:DWORD src1_sel:WORD_1\n" \
  "ds_read_b128 v[96:99], v67\n" \
  "s_waitcnt lgkmcnt(10)\n" \
  "v_smfmac_f32_16x16x64_bf16 v[8:11], v[180:183], v[100:107], v192\n" \
  "v_lshlrev_b32_sdwa v64, 4, v248 dst_sel:DWORD dst_unused:UNUSED_PAD src0_sel:DWORD src1_sel:WORD_0\n" \
  "ds_read_b128 v[100:103], v64\n" \
  "v_lshlrev_b32_sdwa v65, 4, v248 dst_sel:DWORD dst_unused:UNUSED_PAD src0_sel:DWORD src1_sel:WORD_1\n" \
  "ds_read_b128 v[104:107], v65\n" \
  "s_add_u32 m0, s58, 0x4000\n" \
  "s_cmp_lt_u32 s35, 0x28000\n" \
  "global_load_lds_dwordx4 v191, s[54:55] sc1\n" \
  "global_load_lds_dwordx4 v159, s[54:55] offset:1024 sc1\n" \
  "s_cselect_b32 s58, s35, s59\n" \
  "s_waitcnt lgkmcnt(10)\n" \
  "v_smfmac_f32_16x16x64_bf16 v[12:15], v[180:183], v[108:115], v192\n" \
  "v_lshlrev_b32_sdwa v66, 4, v249 dst_sel:DWORD dst_unused:UNUSED_PAD src0_sel:DWORD src1_sel:WORD_0\n" \
  "ds_read_b128 v[108:111], v66\n" \
  "v_lshlrev_b32_sdwa v67, 4, v249 dst_sel:DWORD dst_unused:UNUSED_PAD src0_sel:DWORD src1_sel:WORD_1\n" \
  "ds_read_b128 v[112:115], v67\n" \
  "s_waitcnt lgkmcnt(10)\n" \
  "v_smfmac_f32_16x16x64_bf16 v[16:19], v[180:183], v[68:75], v192\n" \
  "v_lshlrev_b32_sdwa v64, 4, v250 dst_sel:DWORD dst_unused:UNUSED_PAD src0_sel:DWORD src1_sel:WORD_0\n" \
  "ds_read_b128 v[68:71], v64\n" \
  "v_lshlrev_b32_sdwa v65, 4, v250 dst_sel:DWORD dst_unused:UNUSED_PAD src0_sel:DWORD src1_sel:WORD_1\n" \
  "ds_read_b128 v[72:75], v65\n" \
  "global_load_dwordx4 v[224:227], v188, s[56:57] sc1\n" \
  "global_load_dwordx4 v[228:231], v188, s[56:57] offset:1024 sc1\n" \
  "s_waitcnt lgkmcnt(10)\n" \
  "v_smfmac_f32_16x16x64_bf16 v[20:23], v[180:183], v[76:83], v192\n" \
  "v_lshlrev_b32_sdwa v66, 4, v251 dst_sel:DWORD dst_unused:UNUSED_PAD src0_sel:DWORD src1_sel:WORD_0\n" \
  "ds_read_b128 v[76:79], v66\n" \
  "v_lshlrev_b32_sdwa v67, 4, v251 dst_sel:DWORD dst_unused:UNUSED_PAD src0_sel:DWORD src1_sel:WORD_1\n" \
  "ds_read_b128 v[80:83], v67\n" \
  "s_waitcnt lgkmcnt(10)\n" \
  "v_smfmac_f32_16x16x64_bf16 v[24:27], v[180:183], v[84:91], v192\n" \
  "v_lshlrev_b32_sdwa v64, 4, v252 dst_sel:DWORD dst_unused:UNUSED_PAD src0_sel:DWORD src1_sel:WORD_0\n" \
  "ds_read_b128 v[84:87], v64\n" \
  "v_lshlrev_b32_sdwa v65, 4, v252 dst_sel:DWORD dst_unused:UNUSED_PAD src0_sel:DWORD src1_sel:WORD_1\n" \
  "ds_read_b128 v[88:91], v65\n" \
  "global_load_dwordx4 v[232:235], v188, s[56:57] offset:2048 sc1\n" \
  "global_load_dwordx4 v[236:239], v188, s[56:57] offset:3072 sc1\n" \
  "s_waitcnt lgkmcnt(10)\n" \
  "v_smfmac_f32_16x16x64_bf16 v[28:31], v[180:183], v[92:99], v192\n" \
  "v_lshlrev_b32_sdwa v66, 4, v253 dst_sel:DWORD dst_unused:UNUSED_PAD src0_sel:DWORD src1_sel:WORD_0\n" \
  "ds_read_b128 v[92:95], v66\n" \
  "v_lshlrev_b32_sdwa v67, 4, v253 dst_sel:DWORD dst_unused:UNUSED_PAD src0_sel:DWORD src1_sel:WORD_1\n" \
  "ds_read_b128 v[96:99], v67\n" \
  "s_waitcnt lgkmcnt(10)\n" \
  "v_smfmac_f32_16x16x64_bf16 v[32:35], v[180:183], v[100:107], v192\n" \
  "v_lshlrev_b32_sdwa v64, 4, v254 dst_sel:DWORD dst_unused:UNUSED_PAD src0_sel:DWORD src1_sel:WORD_0\n" \
  "ds_read_b128 v[100:103], v64\n" \
  "v_lshlrev_b32_sdwa v65, 4, v254 dst_sel:DWORD dst_unused:UNUSED_PAD src0_sel:DWORD src1_sel:WORD_1\n" \
  "ds_read_b128 v[104:107], v65\n" \
  "s_waitcnt lgkmcnt(10)\n" \
  "v_smfmac_f32_16x16x64_bf16 v[36:39], v[180:183], v[108:115], v192\n" \
  "v_lshlrev_b32_sdwa v66, 4, v255 dst_sel:DWORD dst_unused:UNUSED_PAD src0_sel:DWORD src1_sel:WORD_0\n" \
  "ds_read_b128 v[108:111], v66\n" \
  "v_lshlrev_b32_sdwa v67, 4, v255 dst_sel:DWORD dst_unused:UNUSED_PAD src0_sel:DWORD src1_sel:WORD_1\n" \
  "ds_read_b128 v[112:115], v67\n" \
  "s_add_u32 s56, s56, 32768\n" \
  "s_addc_u32 s57, s57, 0\n" \
  "s_add_u32 s54, s54, 64000\n" \
  "s_addc_u32 s55, s55, 0\n" \
  "s_lshl3_add_u32 s36, s27, s51\n" \
  "s_cmp_eq_u32 s36, 37\n" \
  "s_waitcnt lgkmcnt(10)\n" \
  "v_smfmac_f32_16x16x64_bf16 v[40:43], v[180:183], v[68:75], v192\n" \
  "s_waitcnt lgkmcnt(8)\n" \
  "v_smfmac_f32_16x16x64_bf16 v[44:47], v[180:183], v[76:83], v192\n" \
  "s_waitcnt vmcnt(8)\n" \
  "s_cbranch_scc1 LG_pol\n" \
  "LG_rt2:\n" \
  "s_barrier\n" \
  "v_lshlrev_b32_sdwa v64, 4, v208 dst_sel:DWORD dst_unused:UNUSED_PAD src0_sel:DWORD src1_sel:WORD_0\n" \
  "ds_read_b128 v[68:71], v64\n" \
  "v_lshlrev_b32_sdwa v65, 4, v208 dst_sel:DWORD dst_unused:UNUSED_PAD src0_sel:DWORD src1_sel:WORD_1\n" \
  "ds_read_b128 v[72:75], v65\n" \
  "v_lshlrev_b32_sdwa v66, 4, v209 dst_sel:DWORD dst_unused:UNUSED_PAD src0_sel:DWORD src1_sel:WORD_0\n" \
  "ds_read_b128 v[76:79], v66\n" \
  "v_lshlrev_b32_sdwa v67, 4, v209 dst_sel:DWORD dst_unused:UNUSED_PAD src0_sel:DWORD src1_sel:WORD_1\n" \
  "ds_read_b128 v[80:83], v67\n" \
  "s_waitcnt lgkmcnt(10)\n" \
  "v_smfmac_f32_16x16x64_bf16 v[48:51], v[180:183], v[84:91], v192\n" \
  "s_waitcnt lgkmcnt(8)\n" \
  "v_smfmac_f32_16x16x64_bf16 v[52:55], v[180:183], v[92:99], v192\n" \
  "s_waitcnt lgkmcnt(6)\n" \
  "v_smfmac_f32_16x16x64_bf16 v[56:59], v[180:183], v[100:107], v192\n" \
  "s_waitcnt lgkmcnt(4)\n" \
  "v_smfmac_f32_16x16x64_bf16 v[60:63], v[180:183], v[108:115], v192\n" \
  "s_barrier\n" \
  "s_mov_b32 m0, s58\n" \
  "s_add_u32 s35, s58, 0x8000\n" \
  "global_load_lds_dwordx4 v188, s[54:55] sc1\n" \
  "global_load_lds_dwordx4 v156, s[54:55] offset:1024 sc1\n" \
  "v_lshlrev_b32_sdwa v64, 4, v210 dst_sel:DWORD dst_unused:UNUSED_PAD src0_sel:DWORD src1_sel:WORD_0\n" \
  "ds_read_b128 v[84:87], v64\n" \
  "v_lshlrev_b32_sdwa v65, 4, v210 dst_sel:DWORD dst_unused:UNUSED_PAD src0_sel:DWORD src1_sel:WORD_1\n" \
  "ds_read_b128 v[88:91], v65\n" \
  "v_lshlrev_b32_sdwa v66, 4, v211 dst_sel:DWORD dst_unused:UNUSED_PAD src0_sel:DWORD src1_sel:WORD_0\n" \
  "ds_read_b128 v[92:95], v66\n" \
  "v_lshlrev_b32_sdwa v67, 4, v211 dst_sel:DWORD dst_unused:UNUSED_PAD src0_sel:DWORD src1_sel:WORD_1\n" \
  "ds_read_b128 v[96:99], v67\n" \
  "s_add_u32 m0, s58, 0x4000\n" \
  "s_cmp_lt_u32 s35, 0x28000\n" \
  "global_load_lds_dwordx4 v189, s[54:55] sc1\n" \
  "global_load_lds_dwordx4 v157, s[54:55] offset:1024 sc1\n" \
  "s_cselect_b32 s58, s35, s59\n" \
  "v_lshlrev_b32_sdwa v64, 4, v212 dst_sel:DWORD dst_unused:UNUSED_PAD src0_sel:DWORD src1_sel:WORD_0\n" \
  "ds_read_b128 v[100:103], v64\n" \
  "v_lshlrev_b32_sdwa v65, 4, v212 dst_sel:DWORD dst_unused:UNUSED_PAD src0_sel:DWORD src1_sel:WORD_1\n" \
  "ds_read_b128 v[104:107], v65\n" \
  "v_lshlrev_b32_sdwa v66, 4, v213 dst_sel:DWORD dst_unused:UNUSED_PAD src0_sel:DWORD src1_sel:WORD_0\n" \
  "ds_read_b128 v[108:111], v66\n" \
  "v_lshlrev_b32_sdwa v67, 4, v213 dst_sel:DWORD dst_unused:UNUSED_PAD src0_sel:DWORD src1_sel:WORD_1\n" \
  "ds_read_b128 v[112:115], v67\n" \
  "s_waitcnt lgkmcnt(10)\n" \
  "v_smfmac_f32_16x16x64_bf16 v[0:3], v[180:183], v[68:75], v192\n" \
  "v_lshlrev_b32_sdwa v64, 4, v214 dst_sel:DWORD dst_unused:UNUSED_PAD src0_sel:DWORD src1_sel:WORD_0\n" \
  "ds_read_b128 v[68:71], v64\n" \
  "v_lshlrev_b32_sdwa v65, 4, v214 dst_sel:DWORD dst_unused:UNUSED_PAD src0_sel:DWORD src1_sel:WORD_1\n" \
  "ds_read_b128 v[72:75], v65\n" \
  "s_mov_b32 m0, s58\n" \
  "s_add_u32 s35, s58, 0x8000\n" \
  "global_load_lds_dwordx4 v190, s[54:55] sc1\n" \
  "global_load_lds_dwordx4 v158, s[54:55] offset:1024 sc1\n" \
  "s_waitcnt lgkmcnt(10)\n" \
  "v_smfmac_f32_16x16x64_bf16 v[4:7], v[180:183], v[76:83], v192\n" \
  "v_lshlrev_b32_sdwa v66, 4, v215 dst_sel:DWORD dst_unused:UNUSED_PAD src0_sel:DWORD src1_sel:WORD_0\n" \
  "ds_read_b128 v[76:79], v66\n" \
  "v_lshlrev_b32_sdwa v67, 4, v215 dst_sel:DWORD dst_unused:UNUSED_PAD src0_sel:DWORD src1_sel:WORD_1\n" \
  "ds_read_b128 v[80:83], v67\n" \
  "s_waitcnt lgkmcnt(10)\n" \
  "v_smfmac_f32_16x16x64_bf16 v[8:11], v[180:183], v[84:91], v192\n" \
  "v_lshlrev_b32_sdwa v64, 4, v216 dst_sel:DWORD dst_unused:UNUSED_PAD src0_sel:DWORD src1_sel:WORD_0\n" \
  "ds_read_b128 v[84:87], v64\n" \
  "v_lshlrev_b32_sdwa v65, 4, v216 dst_sel:DWORD dst_unused:UNUSED_PAD src0_sel:DWORD src1_sel:WORD_1\n" \
  "ds_read_b128 v[88:91], v65\n" \
  "s_add_u32 m0, s58, 0x4000\n" \
  "s_cmp_lt_u32 s35, 0x28000\n" \
  "global_load_lds_dwordx4 v191, s[54:55] sc1\n" \
  "global_load_lds_dwordx4 v159, s[54:55] offset:1024 sc1\n" \
  "s_cselect_b32 s58, s35, s59\n" \
  "s_waitcnt lgkmcnt(10)\n" \
  "v_smfmac_f32_16x16x64_bf16 v[12:15], v[180:183], v[92:99], v192\n" \
  "v_lshlrev_b32_sdwa v66, 4, v217 dst_sel:DWORD dst_unused:UNUSED_PAD src0_sel:DWORD src1_sel:WORD_0\n" \
  "ds_read_b128 v[92:95], v66\n" \
  "v_lshlrev_b32_sdwa v67, 4, v217 dst_sel:DWORD dst_unused:UNUSED_PAD src0_sel:DWORD src1_sel:WORD_1\n" \
  "ds_read_b128 v[96:99], v67\n" \
  "s_waitcnt lgkmcnt(10)\n" \
  "v_smfmac_f32_16x16x64_bf16 v[16:19], v[180:183], v[100:107], v192\n" \
  "v_lshlrev_b32_sdwa v64, 4, v218 dst_sel:DWORD dst_unused:UNUSED_PAD src0_sel:DWORD src1_sel:WORD_0\n" \
  "ds_read_b128 v[100:103], v64\n" \
  "v_lshlrev_b32_sdwa v65, 4, v218 dst_sel:DWORD dst_unused:UNUSED_PAD src0_sel:DWORD src1_sel:WORD_1\n" \
  "ds_read_b128 v[104:107], v65\n" \
  "global_load_dwordx4 v[240:243], v188, s[56:57] sc1\n" \
  "global_load_dwordx4 v[244:247], v188, s[56:57] offset:1024 sc1\n" \
  "s_waitcnt lgkmcnt(10)\n" \
  "v_smfmac_f32_16x16x64_bf16 v[20:23], v[180:183], v[108:115], v192\n" \
  "v_lshlrev_b32_sdwa v66, 4, v219 dst_sel:DWORD dst_unused:UNUSED_PAD src0_sel:DWORD src1_sel:WORD_0\n" \
  "ds_read_b128 v[108:111], v66\n" \
  "v_lshlrev_b32_sdwa v67, 4, v219 dst_sel:DWORD dst_unused:UNUSED_PAD src0_sel:DWORD src1_sel:WORD_1\n" \
  "ds_read_b128 v[112:115], v67\n" \
  "s_waitcnt lgkmcnt(10)\n" \
  "v_smfmac_f32_16x16x64_bf16 v[24:27], v[180:183], v[68:75], v192\n" \
  "v_lshlrev_b32_sdwa v64, 4, v220 dst_sel:DWORD dst_unused:UNUSED_PAD src0_sel:DWORD src1_sel:WORD_0\n" \
  "ds_read_b128 v[68:71], v64\n" \
  "v_lshlrev_b32_sdwa v65, 4, v220 dst_sel:DWORD dst_unused:UNUSED_PAD src0_sel:DWORD src1_sel:WORD_1\n" \
  "ds_read_b128 v[72:75], v65\n" \
  "global_load_dwordx4 v[248:251], v188, s[56:57] offset:2048 sc1\n" \
  "global_load_dwordx4 v[252:255], v188, s[56:57] offset:3072 sc1\n" \
  "s_waitcnt lgkmcnt(10)\n" \
  "v_smfmac_f32_16x16x64_bf16 v[28:31], v[180:183], v[76:83], v192\n" \
  "v_lshlrev_b32_sdwa v66, 4, v221 dst_sel:DWORD dst_unused:UNUSED_PAD src0_sel:DWORD src1_sel:WORD_0\n" \
  "ds_read_b128 v[76:79], v66\n" \
  "v_lshlrev_b32_sdwa v67, 4, v221 dst_sel:DWORD dst_unused:UNUSED_PAD src0_sel:DWORD src1_sel:WORD_1\n" \
  "ds_read_b128 v[80:83], v67\n" \
  "s_waitcnt lgkmcnt(10)\n" \
  "v_smfmac_f32_16x16x64_bf16 v[32:35], v[180:183], v[84:91], v192\n" \
  "v_lshlrev_b32_sdwa v64, 4, v222 dst_sel:DWORD dst_unused:UNUSED_PAD src0_sel:DWORD src1_sel:WORD_0\n" \
  "ds_read_b128 v[84:87], v64\n" \
  "v_lshlrev_b32_sdwa v65, 4, v222 dst_sel:DWORD dst_unused:UNUSED_PAD src0_sel:DWORD src1_sel:WORD_1\n" \
  "ds_read_b128 v[88:91], v65\n" \
  "s_waitcnt lgkmcnt(10)\n" \
  "v_smfmac_f32_16x16x64_bf16 v[36:39], v[180:183], v[92:99], v192\n" \
  "v_lshlrev_b32_sdwa v66, 4, v223 dst_sel:DWORD dst_unused:UNUSED_PAD src0_sel:DWORD src1_sel:WORD_0\n" \
  "ds_read_b128 v[92:95], v66\n" \
  "v_lshlrev_b32_sdwa v67, 4, v223 dst_sel:DWORD dst_unused:UNUSED_PAD src0_sel:DWORD src1_sel:WORD_1\n" \
  "ds_read_b128 v[96:99], v67\n" \
  "s_add_u32 s56, s56, 32768\n" \
  "s_addc_u32 s57, s57, 0\n" \
  "s_add_u32 s54, s54, 64000\n" \
  "s_addc_u32 s55, s55, 0\n" \
  "s_sub_u32 s51, s51, 1\n" \
  "s_cmp_lg_u32 s51, 0\n" \
  "s_cbranch_scc1 LG_loop\n" \
  "s_waitcnt lgkmcnt(10)\n" \
  "v_smfmac_f32_16x16x64_bf16 v[40:43], v[180:183], v[100:107], v192\n" \
  "s_waitcnt lgkmcnt(8)\n" \
  "v_smfmac_f32_16x16x64_bf16 v[44:47], v[180:183], v[108:115], v192\n" \
  "s_waitcnt vmcnt(8)\n" \
  "s_barrier\n" \
  "v_lshlrev_b32_sdwa v64, 4, v224 dst_sel:DWORD dst_unused:UNUSED_PAD src0_sel:DWORD src1_sel:WORD_0\n" \
  "ds_read_b128 v[100:103], v64\n" \
  "v_lshlrev_b32_sdwa v65, 4, v224 dst_sel:DWORD dst_unused:UNUSED_PAD src0_sel:DWORD src1_sel:WORD_1\n" \
  "ds_read_b128 v[104:107], v65\n" \
  "v_lshlrev_b32_sdwa v66, 4, v225 dst_sel:DWORD dst_unused:UNUSED_PAD src0_sel:DWORD src1_sel:WORD_0\n" \
  "ds_read_b128 v[108:111], v66\n" \
  "v_lshlrev_b32_sdwa v67, 4, v225 dst_sel:DWORD dst_unused:UNUSED_PAD src0_sel:DWORD src1_sel:WORD_1\n" \
  "ds_read_b128 v[112:115], v67\n" \
  "s_waitcnt lgkmcnt(10)\n" \
  "v_smfmac_f32_16x16x64_bf16 v[48:51], v[180:183], v[68:75], v192\n" \
  "s_waitcnt lgkmcnt(8)\n" \
  "v_smfmac_f32_16x16x64_bf16 v[52:55], v[180:183], v[76:83], v192\n" \
  "s_waitcnt lgkmcnt(6)\n" \
  "v_smfmac_f32_16x16x64_bf16 v[56:59], v[180:183], v[84:91], v192\n" \
  "s_waitcnt lgkmcnt(4)\n" \
  "v_smfmac_f32_16x16x64_bf16 v[60:63], v[180:183], v[92:99], v192\n" \
  "s_barrier\n" \
  "s_mov_b32 m0, s58\n" \
  "s_add_u32 s35, s58, 0x8000\n" \
  "global_load_lds_dwordx4 v188, s[54:55] sc1\n" \
  "global_load_lds_dwordx4 v156, s[54:55] offset:1024 sc1\n" \
  "v_lshlrev_b32_sdwa v64, 4, v226 dst_sel:DWORD dst_unused:UNUSED_PAD src0_sel:DWORD src1_sel:WORD_0\n" \
  "ds_read_b128 v[68:71], v64\n" \
  "v_lshlrev_b32_sdwa v65, 4, v226 dst_sel:DWORD dst_unused:UNUSED_PAD src0_sel:DWORD src1_sel:WORD_1\n" \
  "ds_read_b128 v[72:75], v65\n" \
  "v_lshlrev_b32_sdwa v66, 4, v227 dst_sel:DWORD dst_unused:UNUSED_PAD src0_sel:DWORD src1_sel:WORD_0\n" \
  "ds_read_b128 v[76:79], v66\n" \
  "v_lshlrev_b32_sdwa v67, 4, v227 dst_sel:DWORD dst_unused:UNUSED_PAD src0_sel:DWORD src1_sel:WORD_1\n" \
  "ds_read_b128 v[80:83], v67\n" \
  "s_add_u32 m0, s58, 0x4000\n" \
  "s_cmp_lt_u32 s35, 0x28000\n" \
  "global_load_lds_dwordx4 v189, s[54:55] sc1\n" \
  "global_load_lds_dwordx4 v157, s[54:55] offset:1024 sc1\n" \
  "s_cselect_b32 s58, s35, s59\n" \
  "v_lshlrev_b32_sdwa v64, 4, v228 dst_sel:DWORD dst_unused:UNUSED_PAD src0_sel:DWORD src1_sel:WORD_0\n" \
  "ds_read_b128 v[84:87], v64\n" \
  "v_lshlrev_b32_sdwa v65, 4, v228 dst_sel:DWORD dst_unused:UNUSED_PAD src0_sel:DWORD src1_sel:WORD_1\n" \
  "ds_read_b128 v[88:91], v65\n" \
  "v_lshlrev_b32_sdwa v66, 4, v229 dst_sel:DWORD dst_unused:UNUSED_PAD src0_sel:DWORD src1_sel:WORD_0\n" \
  "ds_read_b128 v[92:95], v66\n" \
  "v_lshlrev_b32 v120, 16, v206\n" \
  "v_and_b32 v121, 0xffff0000, v206\n" \
  "v_lshlrev_b32 v122, 16, v207\n" \
  "v_and_b32 v123, 0xffff0000, v207\n" \
  "v_rcp_f32 v120, v120\n" \
  "v_rcp_f32 v121, v121\n" \
  "v_rcp_f32 v122, v122\n" \
  "v_rcp_f32 v123, v123\n" \
  "s_mov_b32 s52, 0x7f000000\n" \
  "v_med3_f32 v120, v120, s52, -s52\n" \
  "v_med3_f32 v121, v121, s52, -s52\n" \
  "v_med3_f32 v122, v122, s52, -s52\n" \
  "v_med3_f32 v123, v123, s52, -s52\n" \
  "v_mov_b32 v124, 0xc0135761\n" \
  "v_mov_b32 v125, 0xc0135761\n" \
  "s_mov_b32 s52, 0xbdd2d3e8\n" \
  "s_mov_b32 s53, s52\n" \
  "s_lshl2_add_u32 s35, s33, s34\n" \
  "s_lshl_b32 s35, s35, 14\n" \
  "s_add_u32 s35, s35, s59\n" \
  "s_add_u32 s35, s35, 105676800\n" \
  "s_add_u32 s40, s22, s35\n" \
  "s_addc_u32 s41, s23, 0\n" \
  "v_lshlrev_b32_sdwa v67, 4, v229 dst_sel:DWORD dst_unused:UNUSED_PAD src0_sel:DWORD src1_sel:WORD_1\n" \
  "ds_read_b128 v[96:99], v67\n" \
  "s_waitcnt lgkmcnt(10)\n" \
  "v_smfmac_f32_16x16x64_bf16 v[0:3], v[180:183], v[100:107], v192\n" \
  "v_lshlrev_b32_sdwa v64, 4, v230 dst_sel:DWORD dst_unused:UNUSED_PAD src0_sel:DWORD src1_sel:WORD_0\n" \
  "ds_read_b128 v[100:103], v64\n" \
  "v_lshlrev_b32_sdwa v65, 4, v230 dst_sel:DWORD dst_unused:UNUSED_PAD src0_sel:DWORD src1_sel:WORD_1\n" \
  "ds_read_b128 v[104:107], v65\n" \
  "s_mov_b32 m0, s58\n" \
  "s_add_u32 s35, s58, 0x8000\n" \
  "global_load_lds_dwordx4 v190, s[54:55] sc1\n" \
  "global_load_lds_dwordx4 v158, s[54:55] offset:1024 sc1\n" \
  "s_waitcnt lgkmcnt(10)\n" \
  "v_smfmac_f32_16x16x64_bf16 v[4:7], v[180:183], v[108:115], v192\n" \
  "v_lshlrev_b32_sdwa v66, 4, v231 dst_sel:DWORD dst_unused:UNUSED_PAD src0_sel:DWORD src1_sel:WORD_0\n" \
  "ds_read_b128 v[108:111], v66\n" \
  "v_lshlrev_b32_sdwa v67, 4, v231 dst_sel:DWORD dst_unused:UNUSED_PAD src0_sel:DWORD src1_sel:WORD_1\n" \
  "ds_read_b128 v[112:115], v67\n" \
  "s_waitcnt lgkmcnt(10)\n" \
  "v_smfmac_f32_16x16x64_bf16 v[8:11], v[180:183], v[68:75], v192\n" \
  "v_lshlrev_b32_sdwa v64, 4, v232 dst_sel:DWORD dst_unused:UNUSED_PAD src0_sel:DWORD src1_sel:WORD_0\n" \
  "ds_read_b128 v[68:71], v64\n" \
  "v_lshlrev_b32_sdwa v65, 4, v232 dst_sel:DWORD dst_unused:UNUSED_PAD src0_sel:DWORD src1_sel:WORD_1\n" \
  "ds_read_b128 v[72:75], v65\n" \
  "s_add_u32 m0, s58, 0x4000\n" \
  "s_cmp_lt_u32 s35, 0x28000\n" \
  "global_load_lds_dwordx4 v191, s[54:55] sc1\n" \
  "global_load_lds_dwordx4 v159, s[54:55] offset:1024 sc1\n" \
  "s_cselect_b32 s58, s35, s59\n" \
  "s_waitcnt lgkmcnt(10)\n" \
  "v_smfmac_f32_16x16x64_bf16 v[12:15], v[180:183], v[76:83], v192\n" \
  "v_lshlrev_b32_sdwa v66, 4, v233 dst_sel:DWORD dst_unused:UNUSED_PAD src0_sel:DWORD src1_sel:WORD_0\n" \
  "ds_read_b128 v[76:79], v66\n" \
  "v_lshlrev_b32_sdwa v67, 4, v233 dst_sel:DWORD dst_unused:UNUSED_PAD src0_sel:DWORD src1_sel:WORD_1\n" \
  "ds_read_b128 v[80:83], v67\n" \
  "s_waitcnt lgkmcnt(10)\n" \
  "v_smfmac_f32_16x16x64_bf16 v[16:19], v[180:183], v[84:91], v192\n" \
  "v_lshlrev_b32_sdwa v64, 4, v234 dst_sel:DWORD dst_unused:UNUSED_PAD src0_sel:DWORD src1_sel:WORD_0\n" \
  "ds_read_b128 v[84:87], v64\n" \
  "v_lshlrev_b32_sdwa v65, 4, v234 dst_sel:DWORD dst_unused:UNUSED_PAD src0_sel:DWORD src1_sel:WORD_1\n" \
  "ds_read_b128 v[88:91], v65\n" \
  "global_load_dwordx4 v[208:211], v188, s[56:57] sc1\n" \
  "global_load_dwordx4 v[212:215], v188, s[56:57] offset:1024 sc1\n" \
  "s_waitcnt lgkmcnt(10)\n" \
  "v_smfmac_f32_16x16x64_bf16 v[20:23], v[180:183], v[92:99], v192\n" \
  "v_lshlrev_b32_sdwa v66, 4, v235 dst_sel:DWORD dst_unused:UNUSED_PAD src0_sel:DWORD src1_sel:WORD_0\n" \
  "ds_read_b128 v[92:95], v66\n" \
  "v_lshlrev_b32_sdwa v67, 4, v235 dst_sel:DWORD dst_unused:UNUSED_PAD src0_sel:DWORD src1_sel:WORD_1\n" \
  "ds_read_b128 v[96:99], v67\n" \
  "s_waitcnt lgkmcnt(10)\n" \
  "v_smfmac_f32_16x16x64_bf16 v[24:27], v[180:183], v[100:107], v192\n" \
  "v_lshlrev_b32_sdwa v64, 4, v236 dst_sel:DWORD dst_unused:UNUSED_PAD src0_sel:DWORD src1_sel:WORD_0\n" \
  "ds_read_b128 v[100:103], v64\n" \
  "v_lshlrev_b32_sdwa v65, 4, v236 dst_sel:DWORD dst_unused:UNUSED_PAD src0_sel:DWORD src1_sel:WORD_1\n" \
  "ds_read_b128 v[104:107], v65\n" \
  "global_load_dwordx4 v[216:219], v188, s[56:57] offset:2048 sc1\n" \
  "global_load_dwordx4 v[220:223], v188, s[56:57] offset:3072 sc1\n" \
  "s_waitcnt lgkmcnt(10)\n" \
  "v_smfmac_f32_16x16x64_bf16 v[28:31], v[180:183], v[108:115], v192\n" \
  "v_lshlrev_b32_sdwa v66, 4, v237 dst_sel:DWORD dst_unused:UNUSED_PAD src0_sel:DWORD src1_sel:WORD_0\n" \
  "ds_read_b128 v[108:111], v66\n" \
  "v_lshlrev_b32_sdwa v67, 4, v237 dst_sel:DWORD dst_unused:UNUSED_PAD src0_sel:DWORD src1_sel:WORD_1\n" \
  "ds_read_b128 v[112:115], v67\n" \
  "s_waitcnt lgkmcnt(10)\n" \
  "v_smfmac_f32_16x16x64_bf16 v[32:35], v[180:183], v[68:75], v192\n" \
  "v_lshlrev_b32_sdwa v64, 4, v238 dst_sel:DWORD dst_unused:UNUSED_PAD src0_sel:DWORD src1_sel:WORD_0\n" \
  "ds_read_b128 v[68:71], v64\n" \
  "v_lshlrev_b32_sdwa v65, 4, v238 dst_sel:DWORD dst_unused:UNUSED_PAD src0_sel:DWORD src1_sel:WORD_1\n" \
  "ds_read_b128 v[72:75], v65\n" \
  "s_waitcnt lgkmcnt(10)\n" \
  "v_smfmac_f32_16x16x64_bf16 v[36:39], v[180:183], v[76:83], v192\n" \
  "v_lshlrev_b32_sdwa v66, 4, v239 dst_sel:DWORD dst_unused:UNUSED_PAD src0_sel:DWORD src1_sel:WORD_0\n" \
  "ds_read_b128 v[76:79], v66\n" \
  "v_lshlrev_b32_sdwa v67, 4, v239 dst_sel:DWORD dst_unused:UNUSED_PAD src0_sel:DWORD src1_sel:WORD_1\n" \
  "ds_read_b128 v[80:83], v67\n" \
  "s_add_u32 s56, s56, 32768\n" \
  "s_addc_u32 s57, s57, 0\n" \
  "s_add_u32 s54, s54, 64000\n" \
  "s_addc_u32 s55, s55, 0\n" \
  "s_waitcnt lgkmcnt(10)\n" \
  "v_smfmac_f32_16x16x64_bf16 v[40:43], v[180:183], v[84:91], v192\n" \
  "s_waitcnt lgkmcnt(8)\n" \
  "v_smfmac_f32_16x16x64_bf16 v[44:47], v[180:183], v[92:99], v192\n" \
  "s_waitcnt vmcnt(8)\n" \
  "s_barrier\n" \
  "v_lshlrev_b32_sdwa v64, 4, v240 dst_sel:DWORD dst_unused:UNUSED_PAD src0_sel:DWORD src1_sel:WORD_0\n" \
  "ds_read_b128 v[84:87], v64\n" \
  "v_lshlrev_b32_sdwa v65, 4, v240 dst_sel:DWORD dst_unused:UNUSED_PAD src0_sel:DWORD src1_sel:WORD_1\n" \
  "ds_read_b128 v[88:91], v65\n" \
  "v_lshlrev_b32_sdwa v66, 4, v241 dst_sel:DWORD dst_unused:UNUSED_PAD src0_sel:DWORD src1_sel:WORD_0\n" \
  "ds_read_b128 v[92:95], v66\n" \
  "v_lshlrev_b32_sdwa v67, 4, v241 dst_sel:DWORD dst_unused:UNUSED_PAD src0_sel:DWORD src1_sel:WORD_1\n" \
  "ds_read_b128 v[96:99], v67\n" \
  "s_waitcnt lgkmcnt(10)\n" \
  "v_smfmac_f32_16x16x64_bf16 v[48:51], v[180:183], v[100:107], v192\n" \
  "s_waitcnt lgkmcnt(8)\n" \
  "v_smfmac_f32_16x16x64_bf16 v[52:55], v[180:183], v[108:115], v192\n" \
  "s_waitcnt lgkmcnt(6)\n" \
  "v_smfmac_f32_16x16x64_bf16 v[56:59], v[180:183], v[68:75], v192\n" \
  "s_waitcnt lgkmcnt(4)\n" \
  "v_smfmac_f32_16x16x64_bf16 v[60:63], v[180:183], v[76:83], v192\n" \
  "s_barrier\n" \
  "s_mov_b32 m0, s58\n" \
  "s_add_u32 s35, s58, 0x8000\n" \
  "global_load_lds_dwordx4 v188, s[54:55] sc1\n" \
  "global_load_lds_dwordx4 v156, s[54:55] offset:1024 sc1\n" \
  "s_add_u32 m0, s58, 0x4000\n" \
  "s_cmp_lt_u32 s35, 0x28000\n" \
  "global_load_lds_dwordx4 v189, s[54:55] sc1\n" \
  "global_load_lds_dwordx4 v157, s[54:55] offset:1024 sc1\n" \
  "s_cselect_b32 s58, s35, s59\n" \
  "v_lshlrev_b32_sdwa v64, 4, v242 dst_sel:DWORD dst_unused:UNUSED_PAD src0_sel:DWORD src1_sel:WORD_0\n" \
  "ds_read_b128 v[100:103], v64\n" \
  "v_lshlrev_b32_sdwa v65, 4, v242 dst_sel:DWORD dst_unused:UNUSED_PAD src0_sel:DWORD src1_sel:WORD_1\n" \
  "ds_read_b128 v[104:107], v65\n" \
  "v_lshlrev_b32_sdwa v66, 4, v243 dst_sel:DWORD dst_unused:UNUSED_PAD src0_sel:DWORD src1_sel:WORD_0\n" \
  "ds_read_b128 v[108:111], v66\n" \
  "v_lshlrev_b32_sdwa v67, 4, v243 dst_sel:DWORD dst_unused:UNUSED_PAD src0_sel:DWORD src1_sel:WORD_1\n" \
  "ds_read_b128 v[112:115], v67\n" \
  "v_lshlrev_b32_sdwa v64, 4, v244 dst_sel:DWORD dst_unused:UNUSED_PAD src0_sel:DWORD src1_sel:WORD_0\n" \
  "ds_read_b128 v[68:71], v64\n" \
  "v_lshlrev_b32_sdwa v65, 4, v244 dst_sel:DWORD dst_unused:UNUSED_PAD src0_sel:DWORD src1_sel:WORD_1\n" \
  "ds_read_b128 v[72:75], v65\n" \
  "v_lshlrev_b32_sdwa v66, 4, v245 dst_sel:DWORD dst_unused:UNUSED_PAD src0_sel:DWORD src1_sel:WORD_0\n" \
  "ds_read_b128 v[76:79], v66\n" \
  "v_lshlrev_b32_sdwa v67, 4, v245 dst_sel:DWORD dst_unused:UNUSED_PAD src0_sel:DWORD src1_sel:WORD_1\n" \
  "ds_read_b128 v[80:83], v67\n" \
  "s_waitcnt lgkmcnt(10)\n" \
  "v_smfmac_f32_16x16x64_bf16 v[0:3], v[180:183], v[84:91], v192\n" \
  "v_lshlrev_b32_sdwa v64, 4, v246 dst_sel:DWORD dst_unused:UNUSED_PAD src0_sel:DWORD src1_sel:WORD_0\n" \
  "ds_read_b128 v[84:87], v64\n" \
  "v_lshlrev_b32_sdwa v65, 4, v246 dst_sel:DWORD dst_unused:UNUSED_PAD src0_sel:DWORD src1_sel:WORD_1\n" \
  "ds_read_b128 v[88:91], v65\n" \
  "s_waitcnt lgkmcnt(10)\n" \
  "v_smfmac_f32_16x16x64_bf16 v[4:7], v[180:183], v[92:99], v192\n" \
  "v_lshlrev_b32_sdwa v66, 4, v247 dst_sel:DWORD dst_unused:UNUSED_PAD src0_sel:DWORD src1_sel:WORD_0\n" \
  "ds_read_b128 v[92:95], v66\n" \
  "v_lshlrev_b32_sdwa v67, 4, v247 dst_sel:DWORD dst_unused:UNUSED_PAD src0_sel:DWORD src1_sel:WORD_1\n" \
  "ds_read_b128 v[96:99], v67\n" \
  "s_waitcnt lgkmcnt(10)\n" \
  "v_smfmac_f32_16x16x64_bf16 v[8:11], v[180:183], v[100:107], v192\n" \
  "v_lshlrev_b32_sdwa v64, 4, v248 dst_sel:DWORD dst_unused:UNUSED_PAD src0_sel:DWORD src1_sel:WORD_0\n" \
  "ds_read_b128 v[100:103], v64\n" \
  "v_lshlrev_b32_sdwa v65, 4, v248 dst_sel:DWORD dst_unused:UNUSED_PAD src0_sel:DWORD src1_sel:WORD_1\n" \
  "ds_read_b128 v[104:107], v65\n" \
  "s_waitcnt lgkmcnt(10)\n" \
  "v_smfmac_f32_16x16x64_bf16 v[12:15], v[180:183], v[108:115], v192\n" \
  "v_lshlrev_b32_sdwa v66, 4, v249 dst_sel:DWORD dst_unused:UNUSED_PAD src0_sel:DWORD src1_sel:WORD_0\n" \
  "ds_read_b128 v[108:111], v66\n" \
  "v_lshlrev_b32_sdwa v67, 4, v249 dst_sel:DWORD dst_unused:UNUSED_PAD src0_sel:DWORD src1_sel:WORD_1\n" \
  "ds_read_b128 v[112:115], v67\n" \
  "s_waitcnt vmcnt(0)\n" \
  "s_barrier\n" \
  "s_cmp_lt_u32 s27, 4\n" \
  "s_cbranch_scc1 LG_tp\n" \
  "s_setprio 1\n" \
  "LG_tp:\n" \
  "s_waitcnt lgkmcnt(10)\n" \
  "v_smfmac_f32_16x16x64_bf16 v[16:19], v[180:183], v[68:75], v192\n" \
  "s_waitcnt lgkmcnt(8)\n" \
  "v_smfmac_f32_16x16x64_bf16 v[20:23], v[180:183], v[76:83], v192\n" \
  "s_waitcnt lgkmcnt(6)\n" \
  "v_smfmac_f32_16x16x64_bf16 v[24:27], v[180:183], v[84:91], v192\n" \
  "s_waitcnt lgkmcnt(4)\n" \
  "v_smfmac_f32_16x16x64_bf16 v[28:31], v[180:183], v[92:99], v192\n" \
  "s_waitcnt lgkmcnt(2)\n" \
  "v_smfmac_f32_16x16x64_bf16 v[32:35], v[180:183], v[100:107], v192\n" \
  "v_lshlrev_b32_sdwa v64, 4, v208 dst_sel:DWORD dst_unused:UNUSED_PAD src0_sel:DWORD src1_sel:WORD_0\n" \
  "ds_read_b128 v[68:71], v64\n" \
  "v_lshlrev_b32_sdwa v65, 4, v208 dst_sel:DWORD dst_unused:UNUSED_PAD src0_sel:DWORD src1_sel:WORD_1\n" \
  "ds_read_b128 v[72:75], v65\n" \
  "s_waitcnt lgkmcnt(2)\n" \
  "v_smfmac_f32_16x16x64_bf16 v[36:39], v[180:183], v[108:115], v192\n" \
  "v_lshlrev_b32_sdwa v66, 4, v209 dst_sel:DWORD dst_unused:UNUSED_PAD src0_sel:DWORD src1_sel:WORD_0\n" \
  "ds_read_b128 v[76:79], v66\n" \
  "v_lshlrev_b32_sdwa v67, 4, v209 dst_sel:DWORD dst_unused:UNUSED_PAD src0_sel:DWORD src1_sel:WORD_1\n" \
  "ds_read_b128 v[80:83], v67\n" \
  "s_waitcnt lgkmcnt(2)\n" \
  "v_smfmac_f32_16x16x64_bf16 v[0:3], v[180:183], v[68:75], v192\n" \
  "v_lshlrev_b32_sdwa v64, 4, v210 dst_sel:DWORD dst_unused:UNUSED_PAD src0_sel:DWORD src1_sel:WORD_0\n" \
  "ds_read_b128 v[84:87], v64\n" \
  "v_lshlrev_b32_sdwa v65, 4, v210 dst_sel:DWORD dst_unused:UNUSED_PAD src0_sel:DWORD src1_sel:WORD_1\n" \
  "ds_read_b128 v[88:91], v65\n" \
  "s_waitcnt lgkmcnt(2)\n" \
  "v_smfmac_f32_16x16x64_bf16 v[4:7], v[180:183], v[76:83], v192\n" \
  "v_lshlrev_b32_sdwa v66, 4, v211 dst_sel:DWORD dst_unused:UNUSED_PAD src0_sel:DWORD src1_sel:WORD_0\n" \
  "ds_read_b128 v[92:95], v66\n" \
  "v_lshlrev_b32_sdwa v67, 4, v211 dst_sel:DWORD dst_unused:UNUSED_PAD src0_sel:DWORD src1_sel:WORD_1\n" \
  "ds_read_b128 v[96:99], v67\n" \
  "v_pk_mul_f32 v[128:129], v[0:1], v[0:1]\n" \
  "v_pk_mul_f32 v[130:131], v[2:3], v[2:3]\n" \
  "v_pk_fma_f32 v[128:129], v[128:129], s[52:53], v[124:125]\n" \
  "v_pk_fma_f32 v[130:131], v[130:131], s[52:53], v[124:125]\n" \
  "v_pk_mul_f32 v[128:129], v[128:129], v[0:1]\n" \
  "v_pk_mul_f32 v[130:131], v[130:131], v[2:3]\n" \
  "v_exp_f32 v128, v128\n" \
  "v_exp_f32 v129, v129\n" \
  "v_exp_f32 v130, v130\n" \
  "v_exp_f32 v131, v131\n" \
  "s_waitcnt lgkmcnt(2)\n" \
  "v_smfmac_f32_16x16x64_bf16 v[8:11], v[180:183], v[84:91], v192\n" \
  "v_lshlrev_b32_sdwa v64, 4, v212 dst_sel:DWORD dst_unused:UNUSED_PAD src0_sel:DWORD src1_sel:WORD_0\n" \
  "ds_read_b128 v[100:103], v64\n" \
  "v_lshlrev_b32_sdwa v65, 4, v212 dst_sel:DWORD dst_unused:UNUSED_PAD src0_sel:DWORD src1_sel:WORD_1\n" \
  "ds_read_b128 v[104:107], v65\n" \
  "v_pk_fma_f32 v[128:129], v[128:129], v[120:121], v[120:121]\n" \
  "v_pk_fma_f32 v[130:131], v[130:131], v[122:123], v[122:123]\n" \
  "v_rcp_f32 v128, v128\n" \
  "v_rcp_f32 v129, v129\n" \
  "v_rcp_f32 v130, v130\n" \
  "v_rcp_f32 v131, v131\n" \
  "v_pk_mul_f32 v[0:1], v[0:1], v[128:129]\n" \
  "v_pk_fma_f32 v[0:1], v[2:3], v[130:131], v[0:1]\n" \
  "v_add_f32 v0, v0, v1\n" \
  "v_pk_mul_f32 v[132:133], v[4:5], v[4:5]\n" \
  "s_waitcnt lgkmcnt(2)\n" \
  "v_smfmac_f32_16x16x64_bf16 v[12:15], v[180:183], v[92:99], v192\n" \
  "v_lshlrev_b32_sdwa v66, 4, v213 dst_sel:DWORD dst_unused:UNUSED_PAD src0_sel:DWORD src1_sel:WORD_0\n" \
  "ds_read_b128 v[108:111], v66\n" \
  "v_lshlrev_b32_sdwa v67, 4, v213 dst_sel:DWORD dst_unused:UNUSED_PAD src0_sel:DWORD src1_sel:WORD_1\n" \
  "ds_read_b128 v[112:115], v67\n" \
  "v_pk_mul_f32 v[134:135], v[6:7], v[6:7]\n" \
  "v_pk_fma_f32 v[132:133], v[132:133], s[52:53], v[124:125]\n" \
  "v_pk_fma_f32 v[134:135], v[134:135], s[52:53], v[124:125]\n" \
  "v_pk_mul_f32 v[132:133], v[132:133], v[4:5]\n" \
  "v_pk_mul_f32 v[134:135], v[134:135], v[6:7]\n" \
  "v_exp_f32 v132, v132\n" \
  "v_exp_f32 v133, v133\n" \
  "v_exp_f32 v134, v134\n" \
  "v_exp_f32 v135, v135\n" \
  "v_pk_fma_f32 v[132:133], v[132:133], v[120:121], v[120:121]\n" \
  "s_waitcnt lgkmcnt(2)\n" \
  "v_smfmac_f32_16x16x64_bf16 v[16:19], v[180:183], v[100:107], v192\n" \
  "v_lshlrev_b32_sdwa v64, 4, v214 dst_sel:DWORD dst_unused:UNUSED_PAD src0_sel:DWORD src1_sel:WORD_0\n" \
  "ds_read_b128 v[68:71], v64\n" \
  "v_lshlrev_b32_sdwa v65, 4, v214 dst_sel:DWORD dst_unused:UNUSED_PAD src0_sel:DWORD src1_sel:WORD_1\n" \
  "ds_read_b128 v[72:75], v65\n" \
  "v_pk_fma_f32 v[134:135], v[134:135], v[122:123], v[122:123]\n" \
  "v_rcp_f32 v132, v132\n" \
  "v_rcp_f32 v133, v133\n" \
  "v_rcp_f32 v134, v134\n" \
  "v_rcp_f32 v135, v135\n" \
  "v_pk_mul_f32 v[4:5], v[4:5], v[132:133]\n" \
  "v_pk_fma_f32 v[4:5], v[6:7], v[134:135], v[4:5]\n" \
  "v_add_f32 v4, v4, v5\n" \
  "v_pk_mul_f32 v[136:137], v[8:9], v[8:9]\n" \
  "v_pk_mul_f32 v[138:139], v[10:11], v[10:11]\n" \
  "s_waitcnt lgkmcnt(2)\n" \
  "v_smfmac_f32_16x16x64_bf16 v[20:23], v[180:183], v[108:115], v192\n" \
  "v_lshlrev_b32_sdwa v66, 4, v215 dst_sel:DWORD dst_unused:UNUSED_PAD src0_sel:DWORD src1_sel:WORD_0\n" \
  "ds_read_b128 v[76:79], v66\n" \
  "v_lshlrev_b32_sdwa v67, 4, v215 dst_sel:DWORD dst_unused:UNUSED_PAD src0_sel:DWORD src1_sel:WORD_1\n" \
  "ds_read_b128 v[80:83], v67\n" \
  "v_pk_fma_f32 v[136:137], v[136:137], s[52:53], v[124:125]\n" \
  "v_pk_fma_f32 v[138:139], v[138:139], s[52:53], v[124:125]\n" \
  "v_pk_mul_f32 v[136:137], v[136:137], v[8:9]\n" \
  "v_pk_mul_f32 v[138:139], v[138:139], v[10:11]\n" \
  "v_exp_f32 v136, v136\n" \
  "v_exp_f32 v137, v137\n" \
  "v_exp_f32 v138, v138\n" \
  "v_exp_f32 v139, v139\n" \
  "v_pk_fma_f32 v[136:137], v[136:137], v[120:121], v[120:121]\n" \
  "v_pk_fma_f32 v[138:139], v[138:139], v[122:123], v[122:123]\n" \
  "s_waitcnt lgkmcnt(2)\n" \
  "v_smfmac_f32_16x16x64_bf16 v[24:27], v[180:183], v[68:75], v192\n" \
  "v_lshlrev_b32_sdwa v64, 4, v216 dst_sel:DWORD dst_unused:UNUSED_PAD src0_sel:DWORD src1_sel:WORD_0\n" \
  "ds_read_b128 v[84:87], v64\n" \
  "v_lshlrev_b32_sdwa v65, 4, v216 dst_sel:DWORD dst_unused:UNUSED_PAD src0_sel:DWORD src1_sel:WORD_1\n" \
  "ds_read_b128 v[88:91], v65\n" \
  "v_rcp_f32 v136, v136\n" \
  "v_rcp_f32 v137, v137\n" \
  "v_rcp_f32 v138, v138\n" \
  "v_rcp_f32 v139, v139\n" \
  "v_pk_mul_f32 v[8:9], v[8:9], v[136:137]\n" \
  "v_pk_fma_f32 v[8:9], v[10:11], v[138:139], v[8:9]\n" \
  "v_add_f32 v8, v8, v9\n" \
  "v_pk_mul_f32 v[140:141], v[12:13], v[12:13]\n" \
  "v_pk_mul_f32 v[142:143], v[14:15], v[14:15]\n" \
  "v_pk_fma_f32 v[140:141], v[140:141], s[52:53], v[124:125]\n" \
  "s_waitcnt lgkmcnt(2)\n" \
  "v_smfmac_f32_16x16x64_bf16 v[28:31], v[180:183], v[76:83], v192\n" \
  "v_lshlrev_b32_sdwa v66, 4, v217 dst_sel:DWORD dst_unused:UNUSED_PAD src0_sel:DWORD src1_sel:WORD_0\n" \
  "ds_read_b128 v[92:95], v66\n" \
  "v_lshlrev_b32_sdwa v67, 4, v217 dst_sel:DWORD dst_unused:UNUSED_PAD src0_sel:DWORD src1_sel:WORD_1\n" \
  "ds_read_b128 v[96:99], v67\n" \
  "v_pk_fma_f32 v[142:143], v[142:143], s[52:53], v[124:125]\n" \
  "v_pk_mul_f32 v[140:141], v[140:141], v[12:13]\n" \
  "v_pk_mul_f32 v[142:143], v[142:143], v[14:15]\n" \
  "v_exp_f32 v140, v140\n" \
  "v_exp_f32 v141, v141\n" \
  "v_exp_f32 v142, v142\n" \
  "v_exp_f32 v143, v143\n" \
  "v_pk_fma_f32 v[140:141], v[140:141], v[120:121], v[120:121]\n" \
  "v_pk_fma_f32 v[142:143], v[142:143], v[122:123], v[122:123]\n" \
  "v_rcp_f32 v140, v140\n" \
  "s_waitcnt lgkmcnt(2)\n" \
  "v_smfmac_f32_16x16x64_bf16 v[32:35], v[180:183], v[84:91], v192\n" \
  "v_lshlrev_b32_sdwa v64, 4, v250 dst_sel:DWORD dst_unused:UNUSED_PAD src0_sel:DWORD src1_sel:WORD_0\n" \
  "ds_read_b128 v[100:103], v64\n" \
  "v_lshlrev_b32_sdwa v65, 4, v250 dst_sel:DWORD dst_unused:UNUSED_PAD src0_sel:DWORD src1_sel:WORD_1\n" \
  "ds_read_b128 v[104:107], v65\n" \
  "v_rcp_f32 v141, v141\n" \
  "v_rcp_f32 v142, v142\n" \
  "v_rcp_f32 v143, v143\n" \
  "v_pk_mul_f32 v[12:13], v[12:13], v[140:141]\n" \
  "v_pk_fma_f32 v[12:13], v[14:15], v[142:143], v[12:13]\n" \
  "v_add_f32 v12, v12, v13\n" \
  "v_pk_mul_f32 v[128:129], v[16:17], v[16:17]\n" \
  "v_pk_mul_f32 v[130:131], v[18:19], v[18:19]\n" \
  "v_pk_fma_f32 v[128:129], v[128:129], s[52:53], v[124:125]\n" \
  "v_pk_fma_f32 v[130:131], v[130:131], s[52:53], v[124:125]\n" \
  "s_waitcnt lgkmcnt(2)\n" \
  "v_smfmac_f32_16x16x64_bf16 v[36:39], v[180:183], v[92:99], v192\n" \
  "v_lshlrev_b32_sdwa v66, 4, v218 dst_sel:DWORD dst_unused:UNUSED_PAD src0_sel:DWORD src1_sel:WORD_0\n" \
  "ds_read_b128 v[108:111], v66\n" \
  "v_lshlrev_b32_sdwa v67, 4, v218 dst_sel:DWORD dst_unused:UNUSED_PAD src0_sel:DWORD src1_sel:WORD_1\n" \
  "ds_read_b128 v[112:115], v67\n" \
  "v_pk_mul_f32 v[128:129], v[128:129], v[16:17]\n" \
  "v_pk_mul_f32 v[130:131], v[130:131], v[18:19]\n" \
  "v_exp_f32 v128, v128\n" \
  "v_exp_f32 v129, v129\n" \
  "v_exp_f32 v130, v130\n" \
  "v_exp_f32 v131, v131\n" \
  "v_pk_fma_f32 v[128:129], v[128:129], v[120:121], v[120:121]\n" \
  "v_pk_fma_f32 v[130:131], v[130:131], v[122:123], v[122:123]\n" \
  "v_rcp_f32 v128, v128\n" \
  "v_rcp_f32 v129, v129\n" \
  "s_waitcnt lgkmcnt(2)\n" \
  "v_smfmac_f32_16x16x64_bf16 v[40:43], v[180:183], v[100:107], v192\n" \
  "v_lshlrev_b32_sdwa v64, 4, v251 dst_sel:DWORD dst_unused:UNUSED_PAD src0_sel:DWORD src1_sel:WORD_0\n" \
  "ds_read_b128 v[68:71], v64\n" \
  "v_lshlrev_b32_sdwa v65, 4, v251 dst_sel:DWORD dst_unused:UNUSED_PAD src0_sel:DWORD src1_sel:WORD_1\n" \
  "ds_read_b128 v[72:75], v65\n" \
  "v_rcp_f32 v130, v130\n" \
  "v_rcp_f32 v131, v131\n" \
  "v_pk_mul_f32 v[16:17], v[16:17], v[128:129]\n" \
  "v_pk_fma_f32 v[16:17], v[18:19], v[130:131], v[16:17]\n" \
  "v_add_f32 v16, v16, v17\n" \
  "v_pk_mul_f32 v[132:133], v[20:21], v[20:21]\n" \
  "v_pk_mul_f32 v[134:135], v[22:23], v[22:23]\n" \
  "v_pk_fma_f32 v[132:133], v[132:133], s[52:53], v[124:125]\n" \
  "v_pk_fma_f32 v[134:135], v[134:135], s[52:53], v[124:125]\n" \
  "v_pk_mul_f32 v[132:133], v[132:133], v[20:21]\n" \
  "s_waitcnt lgkmcnt(2)\n" \
  "v_smfmac_f32_16x16x64_bf16 v[40:43], v[180:183], v[108:115], v192\n" \
  "v_lshlrev_b32_sdwa v66, 4, v219 dst_sel:DWORD dst_unused:UNUSED_PAD src0_sel:DWORD src1_sel:WORD_0\n" \
  "ds_read_b128 v[76:79], v66\n" \
  "v_lshlrev_b32_sdwa v67, 4, v219 dst_sel:DWORD dst_unused:UNUSED_PAD src0_sel:DWORD src1_sel:WORD_1\n" \
  "ds_read_b128 v[80:83], v67\n" \
  "v_pk_mul_f32 v[134:135], v[134:135], v[22:23]\n" \
  "v_exp_f32 v132, v132\n" \
  "v_exp_f32 v133, v133\n" \
  "v_exp_f32 v134, v134\n" \
  "v_exp_f32 v135, v135\n" \
  "v_pk_fma_f32 v[132:133], v[132:133], v[120:121], v[120:121]\n" \
  "v_pk_fma_f32 v[134:135], v[134:135], v[122:123], v[122:123]\n" \
  "v_rcp_f32 v132, v132\n" \
  "v_rcp_f32 v133, v133\n" \
  "v_rcp_f32 v134, v134\n" \
  "s_waitcnt lgkmcnt(2)\n" \
  "v_smfmac_f32_16x16x64_bf16 v[44:47], v[180:183], v[68:75], v192\n" \
  "v_lshlrev_b32_sdwa v64, 4, v252 dst_sel:DWORD dst_unused:UNUSED_PAD src0_sel:DWORD src1_sel:WORD_0\n" \
  "ds_read_b128 v[84:87], v64\n" \
  "v_lshlrev_b32_sdwa v65, 4, v252 dst_sel:DWORD dst_unused:UNUSED_PAD src0_sel:DWORD src1_sel:WORD_1\n" \
  "ds_read_b128 v[88:91], v65\n" \
  "v_rcp_f32 v135, v135\n" \
  "v_pk_mul_f32 v[20:21], v[20:21], v[132:133]\n" \
  "v_pk_fma_f32 v[20:21], v[22:23], v[134:135], v[20:21]\n" \
  "v_add_f32 v20, v20, v21\n" \
  "v_pk_mul_f32 v[136:137], v[24:25], v[24:25]\n" \
  "v_pk_mul_f32 v[138:139], v[26:27], v[26:27]\n" \
  "v_pk_fma_f32 v[136:137], v[136:137], s[52:53], v[124:125]\n" \
  "v_pk_fma_f32 v[138:139], v[138:139], s[52:53], v[124:125]\n" \
  "v_pk_mul_f32 v[136:137], v[136:137], v[24:25]\n" \
  "v_pk_mul_f32 v[138:139], v[138:139], v[26:27]\n" \
  "s_waitcnt lgkmcnt(2)\n" \
  "v_smfmac_f32_16x16x64_bf16 v[44:47], v[180:183], v[76:83], v192\n" \
  "v_lshlrev_b32_sdwa v66, 4, v220 dst_sel:DWORD dst_unused:UNUSED_PAD src0_sel:DWORD src1_sel:WORD_0\n" \
  "ds_read_b128 v[92:95], v66\n" \
  "v_lshlrev_b32_sdwa v67, 4, v220 dst_sel:DWORD dst_unused:UNUSED_PAD src0_sel:DWORD src1_sel:WORD_1\n" \
  "ds_read_b128 v[96:99], v67\n" \
  "v_exp_f32 v136, v136\n" \
  "v_exp_f32 v137, v137\n" \
  "v_exp_f32 v138, v138\n" \
  "v_exp_f32 v139, v139\n" \
  "v_pk_fma_f32 v[136:137], v[136:137], v[120:121], v[120:121]\n" \
  "v_pk_fma_f32 v[138:139], v[138:139], v[122:123], v[122:123]\n" \
  "v_rcp_f32 v136, v136\n" \
  "v_rcp_f32 v137, v137\n" \
  "v_rcp_f32 v138, v138\n" \
  "v_rcp_f32 v139, v139\n" \
  "s_waitcnt lgkmcnt(2)\n" \
  "v_smfmac_f32_16x16x64_bf16 v[48:51], v[180:183], v[84:91], v192\n" \
  "v_lshlrev_b32_sdwa v64, 4, v253 dst_sel:DWORD dst_unused:UNUSED_PAD src0_sel:DWORD src1_sel:WORD_0\n" \
  "ds_read_b128 v[100:103], v64\n" \
  "v_lshlrev_b32_sdwa v65, 4, v253 dst_sel:DWORD dst_unused:UNUSED_PAD src0_sel:DWORD src1_sel:WORD_1\n" \
  "ds_read_b128 v[104:107], v65\n" \
  "v_pk_mul_f32 v[24:25], v[24:25], v[136:137]\n" \
  "v_pk_fma_f32 v[24:25], v[26:27], v[138:139], v[24:25]\n" \
  "v_add_f32 v24, v24, v25\n" \
  "v_pk_mul_f32 v[140:141], v[28:29], v[28:29]\n" \
  "v_pk_mul_f32 v[142:143], v[30:31], v[30:31]\n" \
  "v_pk_fma_f32 v[140:141], v[140:141], s[52:53], v[124:125]\n" \
  "v_pk_fma_f32 v[142:143], v[142:143], s[52:53], v[124:125]\n" \
  "v_pk_mul_f32 v[140:141], v[140:141], v[28:29]\n" \
  "v_pk_mul_f32 v[142:143], v[142:143], v[30:31]\n" \
  "v_exp_f32 v140, v140\n" \
  "s_waitcnt lgkmcnt(2)\n" \
  "v_smfmac_f32_16x16x64_bf16 v[48:51], v[180:183], v[92:99], v192\n" \
  "v_lshlrev_b32_sdwa v66, 4, v221 dst_sel:DWORD dst_unused:UNUSED_PAD src0_sel:DWORD src1_sel:WORD_0\n" \
  "ds_read_b128 v[108:111], v66\n" \
  "v_lshlrev_b32_sdwa v67, 4, v221 dst_sel:DWORD dst_unused:UNUSED_PAD src0_sel:DWORD src1_sel:WORD_1\n" \
  "ds_read_b128 v[112:115], v67\n" \
  "v_exp_f32 v141, v141\n" \
  "v_exp_f32 v142, v142\n" \
  "v_exp_f32 v143, v143\n" \
  "v_pk_fma_f32 v[140:141], v[140:141], v[120:121], v[120:121]\n" \
  "v_pk_fma_f32 v[142:143], v[142:143], v[122:123], v[122:123]\n" \
  "v_rcp_f32 v140, v140\n" \
  "v_rcp_f32 v141, v141\n" \
  "v_rcp_f32 v142, v142\n" \
  "v_rcp_f32 v143, v143\n" \
  "v_pk_mul_f32 v[28:29], v[28:29], v[140:141]\n" \
  "s_waitcnt lgkmcnt(2)\n" \
  "v_smfmac_f32_16x16x64_bf16 v[52:55], v[180:183], v[100:107], v192\n" \
  "v_lshlrev_b32_sdwa v64, 4, v254 dst_sel:DWORD dst_unused:UNUSED_PAD src0_sel:DWORD src1_sel:WORD_0\n" \
  "ds_read_b128 v[68:71], v64\n" \
  "v_lshlrev_b32_sdwa v65, 4, v254 dst_sel:DWORD dst_unused:UNUSED_PAD src0_sel:DWORD src1_sel:WORD_1\n" \
  "ds_read_b128 v[72:75], v65\n" \
  "v_pk_fma_f32 v[28:29], v[30:31], v[142:143], v[28:29]\n" \
  "v_add_f32 v28, v28, v29\n" \
  "v_permlane16_swap_b32 v0, v16\n" \
  "v_permlane16_swap_b32 v4, v20\n" \
  "v_permlane16_swap_b32 v8, v24\n" \
  "v_permlane16_swap_b32 v12, v28\n" \
  "v_add_f32 v144, v0, v16\n" \
  "v_add_f32 v145, v4, v20\n" \
  "v_add_f32 v146, v8, v24\n" \
  "v_add_f32 v147, v12, v28\n" \
  "s_waitcnt lgkmcnt(2)\n" \
  "v_smfmac_f32_16x16x64_bf16 v[52:55], v[180:183], v[108:115], v192\n" \
  "v_lshlrev_b32_sdwa v66, 4, v222 dst_sel:DWORD dst_unused:UNUSED_PAD src0_sel:DWORD src1_sel:WORD_0\n" \
  "ds_read_b128 v[76:79], v66\n" \
  "v_lshlrev_b32_sdwa v67, 4, v222 dst_sel:DWORD dst_unused:UNUSED_PAD src0_sel:DWORD src1_sel:WORD_1\n" \
  "ds_read_b128 v[80:83], v67\n" \
  "v_min_u32 v144, -2, v144\n" \
  "v_min_u32 v145, -2, v145\n" \
  "v_min_u32 v146, -2, v146\n" \
  "v_min_u32 v147, -2, v147\n" \
  "global_store_dwordx4 v188, v[144:147], s[40:41] offset:0 sc1\n" \
  "v_pk_mul_f32 v[128:129], v[32:33], v[32:33]\n" \
  "v_pk_mul_f32 v[130:131], v[34:35], v[34:35]\n" \
  "v_pk_fma_f32 v[128:129], v[128:129], s[52:53], v[124:125]\n" \
  "v_pk_fma_f32 v[130:131], v[130:131], s[52:53], v[124:125]\n" \
  "v_pk_mul_f32 v[128:129], v[128:129], v[32:33]\n" \
  "s_waitcnt lgkmcnt(2)\n" \
  "v_smfmac_f32_16x16x64_bf16 v[56:59], v[180:183], v[68:75], v192\n" \
  "v_lshlrev_b32_sdwa v64, 4, v255 dst_sel:DWORD dst_unused:UNUSED_PAD src0_sel:DWORD src1_sel:WORD_0\n" \
  "ds_read_b128 v[84:87], v64\n" \
  "v_lshlrev_b32_sdwa v65, 4, v255 dst_sel:DWORD dst_unused:UNUSED_PAD src0_sel:DWORD src1_sel:WORD_1\n" \
  "ds_read_b128 v[88:91], v65\n" \
  "v_pk_mul_f32 v[130:131], v[130:131], v[34:35]\n" \
  "v_exp_f32 v128, v128\n" \
  "v_exp_f32 v129, v129\n" \
  "v_exp_f32 v130, v130\n" \
  "v_exp_f32 v131, v131\n" \
  "v_pk_fma_f32 v[128:129], v[128:129], v[120:121], v[120:121]\n" \
  "v_pk_fma_f32 v[130:131], v[130:131], v[122:123], v[122:123]\n" \
  "v_rcp_f32 v128, v128\n" \
  "v_rcp_f32 v129, v129\n" \
  "v_rcp_f32 v130, v130\n" \
  "s_waitcnt lgkmcnt(2)\n" \
  "v_smfmac_f32_16x16x64_bf16 v[56:59], v[180:183], v[76:83], v192\n" \
  "v_lshlrev_b32_sdwa v66, 4, v223 dst_sel:DWORD dst_unused:UNUSED_PAD src0_sel:DWORD src1_sel:WORD_0\n" \
  "ds_read_b128 v[92:95], v66\n" \
  "v_lshlrev_b32_sdwa v67, 4, v223 dst_sel:DWORD dst_unused:UNUSED_PAD src0_sel:DWORD src1_sel:WORD_1\n" \
  "ds_read_b128 v[96:99], v67\n" \
  "v_rcp_f32 v131, v131\n" \
  "v_pk_mul_f32 v[32:33], v[32:33], v[128:129]\n" \
  "v_pk_fma_f32 v[32:33], v[34:35], v[130:131], v[32:33]\n" \
  "v_add_f32 v32, v32, v33\n" \
  "v_pk_mul_f32 v[132:133], v[36:37], v[36:37]\n" \
  "v_pk_mul_f32 v[134:135], v[38:39], v[38:39]\n" \
  "v_pk_fma_f32 v[132:133], v[132:133], s[52:53], v[124:125]\n" \
  "v_pk_fma_f32 v[134:135], v[134:135], s[52:53], v[124:125]\n" \
  "v_pk_mul_f32 v[132:133], v[132:133], v[36:37]\n" \
  "v_pk_mul_f32 v[134:135], v[134:135], v[38:39]\n" \
  "s_setprio 0\n" \
  "s_waitcnt lgkmcnt(2)\n" \
  "v_smfmac_f32_16x16x64_bf16 v[60:63], v[180:183], v[84:91], v192\n" \
  "v_exp_f32 v132, v132\n" \
  "v_exp_f32 v133, v133\n" \
  "v_exp_f32 v134, v134\n" \
  "v_exp_f32 v135, v135\n" \
  "v_pk_fma_f32 v[132:133], v[132:133], v[120:121], v[120:121]\n" \
  "v_pk_fma_f32 v[134:135], v[134:135], v[122:123], v[122:123]\n" \
  "v_rcp_f32 v132, v132\n" \
  "v_rcp_f32 v133, v133\n" \
  "v_rcp_f32 v134, v134\n" \
  "v_rcp_f32 v135, v135\n" \
  "v_pk_mul_f32 v[36:37], v[36:37], v[132:133]\n" \
  "v_pk_fma_f32 v[36:37], v[38:39], v[134:135], v[36:37]\n" \
  "v_add_f32 v36, v36, v37\n" \
  "v_pk_mul_f32 v[136:137], v[40:41], v[40:41]\n" \
  "v_pk_mul_f32 v[138:139], v[42:43], v[42:43]\n" \
  "v_pk_fma_f32 v[136:137], v[136:137], s[52:53], v[124:125]\n" \
  "v_pk_fma_f32 v[138:139], v[138:139], s[52:53], v[124:125]\n" \
  "v_pk_mul_f32 v[136:137], v[136:137], v[40:41]\n" \
  "v_pk_mul_f32 v[138:139], v[138:139], v[42:43]\n" \
  "v_exp_f32 v136, v136\n" \
  "v_exp_f32 v137, v137\n" \
  "v_exp_f32 v138, v138\n" \
  "v_exp_f32 v139, v139\n" \
  "v_pk_fma_f32 v[136:137], v[136:137], v[120:121], v[120:121]\n" \
  "v_pk_fma_f32 v[138:139], v[138:139], v[122:123], v[122:123]\n" \
  "v_rcp_f32 v136, v136\n" \
  "v_rcp_f32 v137, v137\n" \
  "v_rcp_f32 v138, v138\n" \
  "v_rcp_f32 v139, v139\n" \
  "v_pk_mul_f32 v[40:41], v[40:41], v[136:137]\n" \
  "v_pk_fma_f32 v[40:41], v[42:43], v[138:139], v[40:41]\n" \
  "v_add_f32 v40, v40, v41\n" \
  "s_waitcnt lgkmcnt(0)\n" \
  "v_smfmac_f32_16x16x64_bf16 v[60:63], v[180:183], v[92:99], v192\n" \
  "v_pk_mul_f32 v[140:141], v[44:45], v[44:45]\n" \
  "v_pk_mul_f32 v[142:143], v[46:47], v[46:47]\n" \
  "v_pk_fma_f32 v[140:141], v[140:141], s[52:53], v[124:125]\n" \
  "v_pk_fma_f32 v[142:143], v[142:143], s[52:53], v[124:125]\n" \
  "v_pk_mul_f32 v[140:141], v[140:141], v[44:45]\n" \
  "v_pk_mul_f32 v[142:143], v[142:143], v[46:47]\n" \
  "v_exp_f32 v140, v140\n" \
  "v_exp_f32 v141, v141\n" \
  "v_exp_f32 v142, v142\n" \
  "v_exp_f32 v143, v143\n" \
  "v_pk_fma_f32 v[140:141], v[140:141], v[120:121], v[120:121]\n" \
  "v_pk_fma_f32 v[142:143], v[142:143], v[122:123], v[122:123]\n" \
  "v_rcp_f32 v140, v140\n" \
  "v_rcp_f32 v141, v141\n" \
  "v_rcp_f32 v142, v142\n" \
  "v_rcp_f32 v143, v143\n" \
  "v_pk_mul_f32 v[44:45], v[44:45], v[140:141]\n" \
  "v_pk_fma_f32 v[44:45], v[46:47], v[142:143], v[44:45]\n" \
  "v_add_f32 v44, v44, v45\n" \
  "v_pk_mul_f32 v[128:129], v[48:49], v[48:49]\n" \
  "v_pk_mul_f32 v[130:131], v[50:51], v[50:51]\n" \
  "v_pk_fma_f32 v[128:129], v[128:129], s[52:53], v[124:125]\n" \
  "v_pk_fma_f32 v[130:131], v[130:131], s[52:53], v[124:125]\n" \
  "v_pk_mul_f32 v[128:129], v[128:129], v[48:49]\n" \
  "v_pk_mul_f32 v[130:131], v[130:131], v[50:51]\n" \
  "v_exp_f32 v128, v128\n" \
  "v_exp_f32 v129, v129\n" \
  "v_exp_f32 v130, v130\n" \
  "v_exp_f32 v131, v131\n" \
  "v_pk_fma_f32 v[128:129], v[128:129], v[120:121], v[120:121]\n" \
  "v_pk_fma_f32 v[130:131], v[130:131], v[122:123], v[122:123]\n" \
  "v_rcp_f32 v128, v128\n" \
  "v_rcp_f32 v129, v129\n" \
  "v_rcp_f32 v130, v130\n" \
  "v_rcp_f32 v131, v131\n" \
  "v_pk_mul_f32 v[48:49], v[48:49], v[128:129]\n" \
  "v_pk_fma_f32 v[48:49], v[50:51], v[130:131], v[48:49]\n" \
  "v_add_f32 v48, v48, v49\n" \
  "v_pk_mul_f32 v[132:133], v[52:53], v[52:53]\n" \
  "v_pk_mul_f32 v[134:135], v[54:55], v[54:55]\n" \
  "v_pk_fma_f32 v[132:133], v[132:133], s[52:53], v[124:125]\n" \
  "v_pk_fma_f32 v[134:135], v[134:135], s[52:53], v[124:125]\n" \
  "v_pk_mul_f32 v[132:133], v[132:133], v[52:53]\n" \
  "v_pk_mul_f32 v[134:135], v[134:135], v[54:55]\n" \
  "v_exp_f32 v132, v132\n" \
  "v_exp_f32 v133, v133\n" \
  "v_exp_f32 v134, v134\n" \
  "v_exp_f32 v135, v135\n" \
  "v_pk_fma_f32 v[132:133], v[132:133], v[120:121], v[120:121]\n" \
  "v_pk_fma_f32 v[134:135], v[134:135], v[122:123], v[122:123]\n" \
  "v_rcp_f32 v132, v132\n" \
  "v_rcp_f32 v133, v133\n" \
  "v_rcp_f32 v134, v134\n" \
  "v_rcp_f32 v135, v135\n" \
  "v_pk_mul_f32 v[52:53], v[52:53], v[132:133]\n" \
  "v_pk_fma_f32 v[52:53], v[54:55], v[134:135], v[52:53]\n" \
  "v_add_f32 v52, v52, v53\n" \
  "v_pk_mul_f32 v[136:137], v[56:57], v[56:57]\n" \
  "v_pk_mul_f32 v[138:139], v[58:59], v[58:59]\n" \
  "v_pk_fma_f32 v[136:137], v[136:137], s[52:53], v[124:125]\n" \
  "v_pk_fma_f32 v[138:139], v[138:139], s[52:53], v[124:125]\n" \
  "v_pk_mul_f32 v[136:137], v[136:137], v[56:57]\n" \
  "v_pk_mul_f32 v[138:139], v[138:139], v[58:59]\n" \
  "v_exp_f32 v136, v136\n" \
  "v_exp_f32 v137, v137\n" \
  "v_exp_f32 v138, v138\n" \
  "v_exp_f32 v139, v139\n" \
  "v_pk_fma_f32 v[136:137], v[136:137], v[120:121], v[120:121]\n" \
  "v_pk_fma_f32 v[138:139], v[138:139], v[122:123], v[122:123]\n" \
  "v_rcp_f32 v136, v136\n" \
  "v_rcp_f32 v137, v137\n" \
  "v_rcp_f32 v138, v138\n" \
  "v_rcp_f32 v139, v139\n" \
  "v_pk_mul_f32 v[56:57], v[56:57], v[136:137]\n" \
  "v_pk_fma_f32 v[56:57], v[58:59], v[138:139], v[56:57]\n" \
  "v_add_f32 v56, v56, v57\n" \
  "v_pk_mul_f32 v[140:141], v[60:61], v[60:61]\n" \
  "v_pk_mul_f32 v[142:143], v[62:63], v[62:63]\n" \
  "v_pk_fma_f32 v[140:141], v[140:141], s[52:53], v[124:125]\n" \
  "v_pk_fma_f32 v[142:143], v[142:143], s[52:53], v[124:125]\n" \
  "v_pk_mul_f32 v[140:141], v[140:141], v[60:61]\n" \
  "v_pk_mul_f32 v[142:143], v[142:143], v[62:63]\n" \
  "v_exp_f32 v140, v140\n" \
  "v_exp_f32 v141, v141\n" \
  "v_exp_f32 v142, v142\n" \
  "v_exp_f32 v143, v143\n" \
  "v_pk_fma_f32 v[140:141], v[140:141], v[120:121], v[120:121]\n" \
  "v_pk_fma_f32 v[142:143], v[142:143], v[122:123], v[122:123]\n" \
  "v_rcp_f32 v140, v140\n" \
  "v_rcp_f32 v141, v141\n" \
  "v_rcp_f32 v142, v142\n" \
  "v_rcp_f32 v143, v143\n" \
  "v_pk_mul_f32 v[60:61], v[60:61], v[140:141]\n" \
  "v_pk_fma_f32 v[60:61], v[62:63], v[142:143], v[60:61]\n" \
  "v_add_f32 v60, v60, v61\n" \
  "v_permlane16_swap_b32 v32, v48\n" \
  "v_permlane16_swap_b32 v36, v52\n" \
  "v_permlane16_swap_b32 v40, v56\n" \
  "v_permlane16_swap_b32 v44, v60\n" \
  "v_add_f32 v148, v32, v48\n" \
  "v_add_f32 v149, v36, v52\n" \
  "v_add_f32 v150, v40, v56\n" \
  "v_add_f32 v151, v44, v60\n" \
  "v_min_u32 v148, -2, v148\n" \
  "v_min_u32 v149, -2, v149\n" \
  "v_min_u32 v150, -2, v150\n" \
  "v_min_u32 v151, -2, v151\n" \
  "global_store_dwordx4 v188, v[148:151], s[40:41] offset:1024 sc1\n" \
  "s_cmp_ge_u32 s27, 4\n" \
  "s_cbranch_scc1 LF_b\n" \
  "s_load_dwordx2 s[24:25], s[0:1], 0x40\n" \
  "v_lshrrev_b32 v0, 4, v188\n" \
  "v_lshrrev_b32 v1, 4, v0\n" \
  "v_and_b32 v2, 15, v0\n" \
  "v_lshlrev_b32 v2, 4, v2\n" \
  "v_lshl_add_u32 v1, v1, 18, v2\n" \
  "s_lshl_b32 s35, s27, 20\n" \
  "s_lshl_b32 s36, s26, 8\n" \
  "s_add_u32 s35, s35, s36\n" \
  "s_add_u32 s35, s35, 105676800\n" \
  "s_add_u32 s40, s22, s35\n" \
  "s_addc_u32 s41, s23, 0\n" \
  "s_add_u32 s42, s40, 65536\n" \
  "s_addc_u32 s43, s41, 0\n" \
  "s_add_u32 s54, s42, 65536\n" \
  "s_addc_u32 s55, s43, 0\n" \
  "s_add_u32 s56, s54, 65536\n" \
  "s_addc_u32 s57, s55, 0\n" \
  "s_add_u32 s44, s22, 109871104\n" \
  "s_addc_u32 s45, s23, 0\n" \
  "s_add_u32 s46, s44, 65536\n" \
  "s_addc_u32 s47, s45, 0\n" \
  "s_lshr_b32 s35, s26, 2\n" \
  "s_lshl_b32 s35, s35, 8\n" \
  "s_and_b32 s36, s26, 1\n" \
  "s_lshl_b32 s36, s36, 7\n" \
  "s_add_u32 s35, s35, s36\n" \
  "s_and_b32 s36, s26, 2\n" \
  "s_lshl3_add_u32 s35, s36, s35\n" \
  "v_and_b32 v2, 3, v0\n" \
  "v_lshrrev_b32 v3, 2, v0\n" \
  "v_lshl_add_u32 v2, v2, 5, v3\n" \
  "v_add_u32 v2, s35, v2\n" \
  "v_lshlrev_b32 v3, 2, v2\n" \
  "s_cmp_eq_u32 s27, 0\n" \
  "s_cselect_b32 s35, -1, 0\n" \
  "v_and_b32 v3, s35, v3\n" \
  "s_lshl_b32 s6, s6, 16\n" \
  "v_and_b32 v6, 15, v0\n" \
  "v_lshrrev_b32 v7, 4, v0\n" \
  "v_lshl_add_u32 v6, v6, 2, v7\n" \
  "s_lshl_b32 s35, s27, 8\n" \
  "v_lshl_add_u32 v6, v6, 2, s35\n" \
  "v_lshlrev_b32 v7, 2, v0\n" \
  "v_lshlrev_b32 v2, 1, v2\n" \
  "s_mov_b32 s5, 0x10000\n" \
  "s_barrier\n" \
  "s_sleep 12\n" \
  "LF_poll:\n" \
  "global_load_dwordx4 v[8:11], v1, s[40:41] sc1\n" \
  "global_load_dwordx4 v[12:15], v1, s[42:43] sc1\n" \
  "global_load_dwordx4 v[16:19], v1, s[54:55] sc1\n" \
  "global_load_dwordx4 v[20:23], v1, s[56:57] sc1\n" \
  "global_load_dword v24, v3, s[44:45] sc1\n" \
  "global_load_dword v25, v3, s[46:47] sc1\n" \
  "s_waitcnt vmcnt(0)\n" \
  "v_max3_u32 v4, v8, v9, v10\n" \
  "v_max3_u32 v5, v11, v12, v13\n" \
  "v_max3_u32 v4, v4, v14, v15\n" \
  "v_max3_u32 v5, v5, v16, v17\n" \
  "v_max3_u32 v4, v4, v18, v19\n" \
  "v_max3_u32 v5, v5, v20, v21\n" \
  "v_max3_u32 v4, v4, v22, v23\n" \
  "v_max3_u32 v5, v5, v24, v25\n" \
  "v_max_u32 v5, v5, v4\n" \
  "v_cmp_eq_u32 vcc, -1, v5\n" \
  "s_sub_u32 s5, s5, 1\n" \
  "s_cbranch_scc1 LF_go\n" \
  "s_nop 2\n" \
  "s_cbranch_vccz LF_go\n" \
  "s_mov_b64 exec, vcc\n" \
  "s_sleep 1\n" \
  "s_branch LF_poll\n" \
  "LF_go:\n" \
  "s_mov_b64 exec, -1\n" \
  "v_pk_add_f32 v[8:9], v[8:9], v[12:13]\n" \
  "v_pk_add_f32 v[10:11], v[10:11], v[14:15]\n" \
  "v_pk_add_f32 v[16:17], v[16:17], v[20:21]\n" \
  "v_pk_add_f32 v[18:19], v[18:19], v[22:23]\n" \
  "v_pk_add_f32 v[8:9], v[8:9], v[16:17]\n" \
  "v_pk_add_f32 v[10:11], v[10:11], v[18:19]\n" \
  "v_add_f32 v24, v24, v25\n" \
  "v_permlane16_swap_b32 v8, v9\n" \
  "v_permlane16_swap_b32 v10, v11\n" \
  "v_add_f32 v8, v8, v9\n" \
  "v_add_f32 v10, v10, v11\n" \
  "v_cvt_pk_bf16_f32 v24, v24, v24\n" \
  "s_cmp_lg_u32 s27, 0\n" \
  "v_permlane32_swap_b32 v8, v10\n" \
  "v_add_f32 v8, v8, v10\n" \
  "ds_write_b32 v6, v8\n" \
  "s_waitcnt lgkmcnt(0)\n" \
  "s_barrier\n" \
  "s_cbranch_scc1 LF_end\n" \
  "ds_read2st64_b32 v[8:9], v7 offset0:0 offset1:1\n" \
  "ds_read2st64_b32 v[10:11], v7 offset0:2 offset1:3\n" \
  "v_lshlrev_b32 v24, 16, v24\n" \
  "s_waitcnt lgkmcnt(0)\n" \
  "v_pk_add_f32 v[8:9], v[8:9], v[10:11]\n" \
  "v_add_f32 v8, v8, v9\n" \
  "v_cvt_pk_bf16_f32 v8, v8, v8\n" \
  "v_lshlrev_b32 v8, 16, v8\n" \
  "v_add_f32 v8, s6, v8\n" \
  "v_cvt_pk_bf16_f32 v8, v8, v8\n" \
  "v_lshlrev_b32 v8, 16, v8\n" \
  "v_add_f32 v8, v8, v24\n" \
  "v_cvt_pk_bf16_f32 v8, v8, v8\n" \
  "global_store_short v2, v8, s[24:25]\n" \
  "LF_end:\n" \
  "s_endpgm\n" \
  "LF_b:\n" \
  "s_barrier\n" \
  "s_endpgm\n" \
  "LG_h:\n" \
  "s_cmp_eq_u32 s51, 4\n" \
  "s_cbranch_scc1 LG_chk\n" \
  "s_cmp_lg_u32 s51, 7\n" \
  "s_cbranch_scc1 LG_ret\n" \
  "s_barrier\n" \
  "s_mov_b32 exec_hi, 0\n" \
  "global_atomic_add v196, v199, s[48:49] offset:3076 sc1\n" \
  "s_mov_b64 exec, -1\n" \
  "s_branch LG_aft\n" \
  "LG_pol:\n" \
  "s_mov_b64 exec, 0xff\n" \
  "global_load_dword v200, v198, s[48:49] offset:3076 sc1\n" \
  "s_mov_b64 exec, -1\n" \
  "s_branch LG_rt2\n" \
  "LG_chk:\n" \
  "s_mov_b32 s5, 0x10000\n" \
  "LG_p:\n" \
  "s_mov_b64 exec, 0xff\n" \
  "v_cmp_le_u32 vcc, 32, v200\n" \
  "s_mov_b64 exec, -1\n" \
  "s_sub_u32 s5, s5, 1\n" \
  "s_cselect_b32 s35, 0xff, vcc_lo\n" \
  "s_cmp_eq_u32 s35, 0xff\n" \
  "s_cbranch_scc1 LG_p_x\n" \
  "s_sleep 2\n" \
  "s_mov_b64 exec, 0xff\n" \
  "global_load_dword v200, v198, s[48:49] offset:3076 sc1\n" \
  "s_mov_b64 exec, -1\n" \
  "s_waitcnt vmcnt(0)\n" \
  "s_branch LG_p\n" \
  "LG_p_x:\n" \
  "s_branch LG_ret\n" \
  "LZ_end:\n"

#define R10(p, b) p #b "0", p #b "1", p #b "2", p #b "3", p #b "4", p #b "5", p #b "6", p #b "7", p #b "8", p #b "9"
__global__ __launch_bounds__(512, 2) void k1(const int* __restrict__ idx, const unsigned short* __restrict__ table,
                                             const unsigned short* __restrict__ w_p, const unsigned short* __restrict__ w1,
                                             const unsigned short* __restrict__ b1, const unsigned short* __restrict__ w2,
                                             const unsigned short* __restrict__ b2, char* __restrict__ ws, __hip_bfloat16* __restrict__ out) {
  __shared__ __attribute__((aligned(1024))) char smem[163840];
  unsigned tid = threadIdx.x;
  asm volatile(K_ASM
               : "+{v0}"(tid)
               : "{s[0:1]}"(__builtin_amdgcn_kernarg_segment_ptr()), "{s26}"(blockIdx.x), "s"((unsigned)(uintptr_t)smem)
               : "memory", "scc", "vcc", "s3", "s4", "s5", "s6", "s7", "s8", "s9", R10("s", 1), "s20", "s21", "s22", "s23", "s24", "s25", "s27", "s28", "s29", "s30", "s31", "s33", "s34", "s35", "s36", "s37", "s38", "s39", R10("s", 4), R10("s", 5), R10("s", 6), R10("s", 7), R10("s", 8), R10("s", 9),
                 "v1", "v2", "v3", "v4", "v5", "v6", "v7", "v8", "v9", R10("v", 1), R10("v", 2), R10("v", 3), R10("v", 4), R10("v", 5), R10("v", 6), R10("v", 7), R10("v", 8), R10("v", 9),
                 R10("v", 10), R10("v", 11), R10("v", 12), R10("v", 13), R10("v", 14), R10("v", 15), R10("v", 16), R10("v", 17), R10("v", 18), R10("v", 19),
                 R10("v", 20), R10("v", 21), R10("v", 22), R10("v", 23), R10("v", 24), "v250", "v251", "v252", "v253", "v254", "v255");
}

extern "C" void kernel_launch(void* const* d_in, const int* in_sizes, int n_in, void* d_out, int out_size, void* d_ws,
                              size_t ws_size, hipStream_t stream) {
  k1<<<dim3(256), dim3(512), 0, stream>>>((const int*)d_in[0], (const unsigned short*)d_in[1], (const unsigned short*)d_in[2], (const unsigned short*)d_in[3],
                                          (const unsigned short*)d_in[4], (const unsigned short*)d_in[5], (const unsigned short*)d_in[6], (char*)d_ws, (__hip_bfloat16*)d_out);
}
```

```cpp
#include <hip/hip_runtime.h>
#include <hip/hip_bf16.h>
#include <stdint.h>

#define K_ASM \
  "LZ_first:\n" \
  "s_load_dwordx16 s[8:23], s[0:1], 0x0\n" \
  "v_readfirstlane_b32 s27, v0\n" \
  "v_and_b32 v255, 63, v0\n" \
  "s_and_b32 s29, s26, 7\n" \
  "s_lshr_b32 s30, s26, 3\n" \
  "s_lshr_b32 s27, s27, 6\n" \
  "s_and_b32 s31, s30, 1\n" \
  "s_and_b32 s28, s27, 3\n" \
  "s_lshr_b32 s35, s30, 1\n" \
  "s_lshl3_add_u32 s7, s35, s29\n" \
  "v_lshrrev_b32 v1, 3, v255\n" \
  "v_and_b32 v2, 7, v255\n" \
  "v_lshlrev_b32 v1, 8, v1\n" \
  "v_lshl_or_b32 v254, v2, 4, v1\n" \
  "s_min_u32 s36, s7, 122\n" \
  "s_add_u32 s36, s36, s28\n" \
  "s_cmp_ge_u32 s27, 4\n" \
  "s_cbranch_scc1 LB_start\n" \
  "s_setprio 3\n" \
  "v_and_b32 v252, 15, v255\n" \
  "v_lshrrev_b32 v253, 4, v255\n" \
  "v_mul_u32_u24 v2, 400, v253\n" \
  "v_min_u32 v3, 8, v252\n" \
  "v_lshl_add_u32 v250, v252, 4, v2\n" \
  "v_lshl_add_u32 v251, v3, 4, v2\n" \
  "s_lshl2_add_u32 s36, s7, s28\n" \
  "s_mul_i32 s36, s36, 12800\n" \
  "s_lshl_b32 s35, s28, 10\n" \
  "v_lshl_add_u32 v1, v255, 4, s35\n" \
  "s_add_u32 m0, s35, 138240\n" \
  "s_waitcnt lgkmcnt(0)\n" \
  "s_add_u32 s44, s8, s36\n" \
  "s_addc_u32 s45, s9, 0\n" \
  "global_load_lds_dwordx4 v1, s[12:13]\n" \
  "s_add_u32 m0, m0, 0x1000\n" \
  "v_add_u32 v1, 0x1000, v1\n" \
  "global_load_lds_dwordx4 v1, s[12:13]\n" \
  "s_add_u32 m0, m0, 0x1000\n" \
  "v_add_u32 v1, 0x1000, v1\n" \
  "global_load_lds_dwordx4 v1, s[12:13]\n" \
  "s_add_u32 m0, m0, 0x1000\n" \
  "v_add_u32 v1, 0x1000, v1\n" \
  "global_load_lds_dwordx4 v1, s[12:13]\n" \
  "s_add_u32 m0, m0, 0x1000\n" \
  "v_add_u32 v1, 0x1000, v1\n" \
  "v_min_u32 v1, 19984, v1\n" \
  "global_load_lds_dwordx4 v1, s[12:13]\n" \
  "global_load_dwordx4 v[232:235], v250, s[44:45] offset:0\n" \
  "global_load_dwordx4 v[236:239], v251, s[44:45] offset:256\n" \
  "global_load_dwordx4 v[240:243], v250, s[44:45] offset:1600\n" \
  "global_load_dwordx4 v[244:247], v251, s[44:45] offset:1856\n" \
  "s_setprio 1\n" \
  "v_lshrrev_b32 v1, 2, v252\n" \
  "v_and_b32 v2, 3, v252\n" \
  "v_cmp_eq_u32 vcc, v1, v253\n" \
  "v_mov_b32 v4, 0x3f80\n" \
  "v_and_b32 v13, 1, v253\n" \
  "v_lshlrev_b32 v14, 1, v13\n" \
  "v_xor_b32 v2, v2, v14\n" \
  "v_cndmask_b32 v2, 4, v2, vcc\n" \
  "v_cmp_eq_u32 vcc, 0, v2\n" \
  "s_nop 1\n" \
  "v_cndmask_b32 v216, 0, v4, vcc\n" \
  "v_lshlrev_b32 v220, 16, v216\n" \
  "v_cmp_eq_u32 vcc, 1, v2\n" \
  "s_nop 1\n" \
  "v_cndmask_b32 v217, 0, v4, vcc\n" \
  "v_lshlrev_b32 v221, 16, v217\n" \
  "v_cmp_eq_u32 vcc, 2, v2\n" \
  "s_nop 1\n" \
  "v_cndmask_b32 v218, 0, v4, vcc\n" \
  "v_lshlrev_b32 v222, 16, v218\n" \
  "v_cmp_eq_u32 vcc, 3, v2\n" \
  "s_nop 1\n" \
  "v_cndmask_b32 v219, 0, v4, vcc\n" \
  "v_lshlrev_b32 v223, 16, v219\n" \
  "v_and_b32 v1, 2, v252\n" \
  "v_lshlrev_b32 v1, 3, v1\n" \
  "v_lshlrev_b32 v1, v1, v4\n" \
  "v_and_b32 v2, 0x31, v255\n" \
  "v_mov_b64 v[224:225], 0\n" \
  "v_cmp_eq_u32 vcc, 0, v2\n" \
  "s_nop 1\n" \
  "v_cndmask_b32 v226, 0, v1, vcc\n" \
  "v_cmp_eq_u32 vcc, 1, v2\n" \
  "s_nop 1\n" \
  "v_cndmask_b32 v227, 0, v1, vcc\n" \
  "v_mul_u32_u24 v5, 400, v252\n" \
  "v_add_u32 v5, 138240, v5\n" \
  "v_lshl_add_u32 v6, v253, 4, v5\n" \
  "v_add_u32 v7, 6400, v5\n" \
  "v_add_u32 v8, 6400, v6\n" \
  "v_add_u32 v9, 12800, v5\n" \
  "v_add_u32 v10, 12800, v6\n" \
  "v_bfe_u32 v1, v252, 1, 1\n" \
  "v_and_b32 v2, 1, v252\n" \
  "v_lshl_add_u32 v1, v2, 1, v1\n" \
  "v_mul_u32_u24 v1, 200, v1\n" \
  "v_add_u32 v11, 157440, v1\n" \
  "v_lshl_add_u32 v12, v253, 4, v11\n" \
  "v_lshlrev_b32 v13, 3, v13\n" \
  "v_sub_u32 v16, v5, v13\n" \
  "v_add_u32 v5, v5, v13\n" \
  "v_sub_u32 v17, v6, v13\n" \
  "v_add_u32 v6, v6, v13\n" \
  "v_sub_u32 v18, v7, v13\n" \
  "v_add_u32 v7, v7, v13\n" \
  "v_sub_u32 v19, v8, v13\n" \
  "v_add_u32 v8, v8, v13\n" \
  "v_sub_u32 v20, v9, v13\n" \
  "v_add_u32 v9, v9, v13\n" \
  "v_sub_u32 v21, v10, v13\n" \
  "v_add_u32 v10, v10, v13\n" \
  "v_sub_u32 v22, v11, v13\n" \
  "v_add_u32 v11, v11, v13\n" \
  "v_sub_u32 v23, v12, v13\n" \
  "v_add_u32 v12, v12, v13\n" \
  "v_lshlrev_b32 v188, 4, v253\n" \
  "v_and_b32 v96, 4, v252\n" \
  "v_lshl_add_u32 v96, v253, 5, v96\n" \
  "v_mov_b32 v191, 0x40000000\n" \
  "v_cmp_gt_u32 vcc, 8, v252\n" \
  "v_mov_b32 v193, 0x4444\n" \
  "v_mov_b32 v98, 0xeeee\n" \
  "v_cndmask_b32 v189, v191, v96, vcc\n" \
  "v_cndmask_b32 v190, v96, v191, vcc\n" \
  "v_and_b32 v99, 4, v252\n" \
  "v_cmp_ne_u32 vcc, 0, v99\n" \
  "s_lshl_b32 s35, s28, 11\n" \
  "s_add_u32 s35, s35, 130048\n" \
  "v_cndmask_b32 v193, v193, v98, vcc\n" \
  "v_lshl_add_u32 v194, v252, 4, s35\n" \
  "v_and_b32 v96, 7, v252\n" \
  "v_lshlrev_b32 v96, 5, v96\n" \
  "v_lshl_add_u32 v96, v253, 8, v96\n" \
  "v_lshrrev_b32 v97, 3, v252\n" \
  "v_lshl_add_u32 v96, v97, 2, v96\n" \
  "v_add_u32 v195, s35, v96\n" \
  "v_and_b32 v97, v97, v252\n" \
  "v_lshlrev_b32 v192, 4, v97\n" \
  "s_waitcnt vmcnt(4)\n" \
  "s_barrier\n" \
  "ds_read_b64 v[200:201], v12 offset:128\n" \
  "ds_read_b64 v[202:203], v23 offset:136\n" \
  "ds_read_b64 v[184:185], v9 offset:184\n" \
  "ds_read_b64 v[186:187], v20 offset:192\n" \
  "ds_read_b64 v[212:213], v9 offset:384\n" \
  "ds_read_b64 v[214:215], v20 offset:392\n" \
  "ds_read_b64 v[196:197], v12 offset:64\n" \
  "ds_read_b64 v[198:199], v23 offset:72\n" \
  "ds_read_b64 v[176:177], v7 offset:184\n" \
  "ds_read_b64 v[178:179], v18 offset:192\n" \
  "ds_read_b64 v[180:181], v7 offset:384\n" \
  "ds_read_b64 v[182:183], v18 offset:392\n" \
  "ds_read_b64 v[228:229], v12 offset:0\n" \
  "ds_read_b64 v[230:231], v23 offset:8\n" \
  "s_waitcnt lgkmcnt(12)\n" \
  "v_mfma_f32_16x16x32_bf16 v[44:47], v[200:203], v[216:219], 0\n" \
  "ds_read_b64 v[168:169], v5 offset:184\n" \
  "ds_read_b64 v[170:171], v16 offset:192\n" \
  "s_waitcnt lgkmcnt(12)\n" \
  "v_mfma_f32_16x16x32_bf16 v[44:47], v[224:227], v[184:187], v[44:47]\n" \
  "ds_read_b64 v[172:173], v5 offset:384\n" \
  "ds_read_b64 v[174:175], v16 offset:392\n" \
  "v_mfma_f32_16x16x32_bf16 v[208:211], v[200:203], v[220:223], 0\n" \
  "ds_read_b64 v[96:97], v6 offset:0\n" \
  "ds_read_b64 v[98:99], v17 offset:8\n" \
  "s_waitcnt lgkmcnt(14)\n" \
  "v_mfma_f32_16x16x32_bf16 v[208:211], v[224:227], v[212:215], v[208:211]\n" \
  "ds_read_b64 v[124:125], v8 offset:64\n" \
  "ds_read_b64 v[126:127], v19 offset:72\n" \
  "s_waitcnt lgkmcnt(14)\n" \
  "v_mfma_f32_16x16x32_bf16 v[60:63], v[196:199], v[216:219], 0\n" \
  "ds_read_b64 v[136:137], v8 offset:264\n" \
  "ds_read_b64 v[138:139], v19 offset:272\n" \
  "s_waitcnt lgkmcnt(14)\n" \
  "v_mfma_f32_16x16x32_bf16 v[60:63], v[224:227], v[176:179], v[60:63]\n" \
  "ds_read_b64 v[140:141], v8 offset:328\n" \
  "ds_read_b64 v[142:143], v19 offset:336\n" \
  "v_mfma_f32_16x16x32_bf16 v[184:187], v[196:199], v[220:223], 0\n" \
  "ds_read_b64 v[160:161], v10 offset:264\n" \
  "ds_read_b64 v[162:163], v21 offset:272\n" \
  "s_waitcnt lgkmcnt(15)\n" \
  "v_mfma_f32_16x16x32_bf16 v[184:187], v[224:227], v[180:183], v[184:187]\n" \
  "ds_read_b64 v[116:117], v6 offset:328\n" \
  "ds_read_b64 v[118:119], v17 offset:336\n" \
  "s_waitcnt lgkmcnt(15)\n" \
  "v_mfma_f32_16x16x32_bf16 v[68:71], v[228:231], v[216:219], 0\n" \
  "ds_read_b64 v[144:145], v10 offset:0\n" \
  "ds_read_b64 v[146:147], v21 offset:8\n" \
  "s_waitcnt lgkmcnt(15)\n" \
  "v_mfma_f32_16x16x32_bf16 v[68:71], v[224:227], v[168:171], v[68:71]\n" \
  "ds_read_b64 v[148:149], v10 offset:64\n" \
  "ds_read_b64 v[150:151], v21 offset:72\n" \
  "v_mfma_f32_16x16x32_bf16 v[212:215], v[228:231], v[220:223], 0\n" \
  "ds_read_b64 v[104:105], v6 offset:128\n" \
  "ds_read_b64 v[106:107], v17 offset:136\n" \
  "s_waitcnt lgkmcnt(15)\n" \
  "v_mfma_f32_16x16x32_bf16 v[212:215], v[224:227], v[172:175], v[212:215]\n" \
  "ds_read_b64 v[100:101], v6 offset:64\n" \
  "ds_read_b64 v[102:103], v17 offset:72\n" \
  "v_mfma_f32_16x16x32_bf16 v[92:95], v[96:99], v[216:219], 0\n" \
  "ds_read_b64 v[120:121], v8 offset:0\n" \
  "ds_read_b64 v[122:123], v19 offset:8\n" \
  "s_waitcnt lgkmcnt(15)\n" \
  "v_mfma_f32_16x16x32_bf16 v[24:27], v[124:127], v[220:223], 0\n" \
  "ds_read_b64 v[112:113], v6 offset:264\n" \
  "ds_read_b64 v[114:115], v17 offset:272\n" \
  "v_mfma_f32_16x16x32_bf16 v[24:27], v[216:219], v[136:139], v[24:27]\n" \
  "ds_read_b64 v[156:157], v10 offset:200\n" \
  "ds_read_b64 v[158:159], v21 offset:208\n" \
  "s_waitcnt lgkmcnt(15)\n" \
  "v_mfma_f32_16x16x32_bf16 v[28:31], v[140:143], v[220:223], 0\n" \
  "ds_read_b64 v[132:133], v8 offset:200\n" \
  "ds_read_b64 v[134:135], v19 offset:208\n" \
  "v_mfma_f32_16x16x32_bf16 v[28:31], v[220:223], v[160:163], v[28:31]\n" \
  "ds_read_b64 v[108:109], v6 offset:200\n" \
  "ds_read_b64 v[110:111], v17 offset:208\n" \
  "s_waitcnt lgkmcnt(15)\n" \
  "v_mfma_f32_16x16x32_bf16 v[32:35], v[116:119], v[216:219], 0\n" \
  "ds_read_b64 v[152:153], v10 offset:128\n" \
  "ds_read_b64 v[154:155], v21 offset:136\n" \
  "v_mfma_f32_16x16x32_bf16 v[32:35], v[220:223], v[144:147], v[32:35]\n" \
  "ds_read_b64 v[164:165], v10 offset:328\n" \
  "ds_read_b64 v[166:167], v21 offset:336\n" \
  "v_mfma_f32_16x16x32_bf16 v[36:39], v[140:143], v[216:219], 0\n" \
  "ds_read_b64 v[128:129], v8 offset:128\n" \
  "ds_read_b64 v[130:131], v19 offset:136\n" \
  "s_waitcnt lgkmcnt(15)\n" \
  "v_mfma_f32_16x16x32_bf16 v[36:39], v[220:223], v[148:151], v[36:39]\n" \
  "ds_read_b64 v[204:205], v11 offset:184\n" \
  "ds_read_b64 v[206:207], v22 offset:192\n" \
  "v_mfma_f32_16x16x32_bf16 v[40:43], v[104:107], v[216:219], 0\n" \
  "v_mfma_f32_16x16x32_bf16 v[40:43], v[216:219], v[144:147], v[40:43]\n" \
  "v_mfma_f32_16x16x32_bf16 v[48:51], v[100:103], v[216:219], 0\n" \
  "s_waitcnt lgkmcnt(15)\n" \
  "v_mfma_f32_16x16x32_bf16 v[48:51], v[216:219], v[120:123], v[48:51]\n" \
  "s_waitcnt lgkmcnt(14)\n" \
  "v_mfma_f32_16x16x32_bf16 v[52:55], v[112:115], v[216:219], 0\n" \
  "v_mfma_f32_16x16x32_bf16 v[52:55], v[220:223], v[120:123], v[52:55]\n" \
  "v_mfma_f32_16x16x32_bf16 v[56:59], v[116:119], v[220:223], 0\n" \
  "s_waitcnt lgkmcnt(12)\n" \
  "v_mfma_f32_16x16x32_bf16 v[56:59], v[220:223], v[156:159], v[56:59]\n" \
  "s_waitcnt vmcnt(0) lgkmcnt(0)\n" \
  "v_mfma_f32_16x16x32_bf16 v[64:67], v[104:107], v[220:223], 0\n" \
  "v_cvt_pk_u16_u32 v232, v232, v233\n" \
  "v_cvt_pk_u16_u32 v234, v234, v235\n" \
  "v_mfma_f32_16x16x32_bf16 v[64:67], v[216:219], v[156:159], v[64:67]\n" \
  "v_cvt_pk_u16_u32 v236, v236, v237\n" \
  "v_cvt_pk_u16_u32 v238, v238, v239\n" \
  "v_mfma_f32_16x16x32_bf16 v[72:75], v[136:139], v[220:223], 0\n" \
  "v_pk_lshrrev_b16 v233, 3, v232 op_sel_hi:[0,1]\n" \
  "v_pk_lshrrev_b16 v235, 3, v234 op_sel_hi:[0,1]\n" \
  "v_mfma_f32_16x16x32_bf16 v[76:79], v[112:115], v[220:223], 0\n" \
  "v_pk_lshrrev_b16 v237, 3, v236 op_sel_hi:[0,1]\n" \
  "v_pk_lshrrev_b16 v239, 3, v238 op_sel_hi:[0,1]\n" \
  "v_mfma_f32_16x16x32_bf16 v[76:79], v[220:223], v[132:135], v[76:79]\n" \
  "v_pk_mad_u16 v180, v232, 8, v233 op_sel_hi:[1,0,1]\n" \
  "v_pk_mad_u16 v181, v234, 8, v235 op_sel_hi:[1,0,1]\n" \
  "v_mfma_f32_16x16x32_bf16 v[80:83], v[100:103], v[220:223], 0\n" \
  "v_pk_mad_u16 v182, v236, 8, v237 op_sel_hi:[1,0,1]\n" \
  "v_pk_mad_u16 v183, v238, 8, v239 op_sel_hi:[1,0,1]\n" \
  "v_mfma_f32_16x16x32_bf16 v[80:83], v[216:219], v[132:135], v[80:83]\n" \
  "v_alignbit_b32 v182, v182, v182, v192\n" \
  "v_alignbit_b32 v183, v183, v183, v192\n" \
  "v_mfma_f32_16x16x32_bf16 v[84:87], v[96:99], v[220:223], 0\n" \
  "v_cvt_pk_u16_u32 v240, v240, v241\n" \
  "v_cvt_pk_u16_u32 v242, v242, v243\n" \
  "v_mfma_f32_16x16x32_bf16 v[84:87], v[216:219], v[108:111], v[84:87]\n" \
  "v_cvt_pk_u16_u32 v244, v244, v245\n" \
  "v_cvt_pk_u16_u32 v246, v246, v247\n" \
  "v_mfma_f32_16x16x32_bf16 v[88:91], v[108:111], v[220:223], 0\n" \
  "v_pk_lshrrev_b16 v241, 3, v240 op_sel_hi:[0,1]\n" \
  "v_pk_lshrrev_b16 v243, 3, v242 op_sel_hi:[0,1]\n" \
  "v_mfma_f32_16x16x32_bf16 v[0:3], v[124:127], v[216:219], 0\n" \
  "v_pk_lshrrev_b16 v245, 3, v244 op_sel_hi:[0,1]\n" \
  "v_pk_lshrrev_b16 v247, 3, v246 op_sel_hi:[0,1]\n" \
  "v_mfma_f32_16x16x32_bf16 v[4:7], v[152:155], v[216:219], 0\n" \
  "v_pk_mad_u16 v196, v240, 8, v241 op_sel_hi:[1,0,1]\n" \
  "v_pk_mad_u16 v197, v242, 8, v243 op_sel_hi:[1,0,1]\n" \
  "v_mfma_f32_16x16x32_bf16 v[8:11], v[152:155], v[220:223], 0\n" \
  "v_pk_mad_u16 v198, v244, 8, v245 op_sel_hi:[1,0,1]\n" \
  "v_pk_mad_u16 v199, v246, 8, v247 op_sel_hi:[1,0,1]\n" \
  "v_mfma_f32_16x16x32_bf16 v[8:11], v[216:219], v[164:167], v[8:11]\n" \
  "v_alignbit_b32 v198, v198, v198, v192\n" \
  "v_alignbit_b32 v199, v199, v199, v192\n" \
  "v_mfma_f32_16x16x32_bf16 v[12:15], v[164:167], v[220:223], 0\n" \
  "v_add_u32 v200, 0x400, v195\n" \
  "v_mfma_f32_16x16x32_bf16 v[16:19], v[128:131], v[220:223], 0\n" \
  "v_mfma_f32_16x16x32_bf16 v[16:19], v[216:219], v[160:163], v[16:19]\n" \
  "v_mfma_f32_16x16x32_bf16 v[20:23], v[128:131], v[216:219], 0\n" \
  "v_mfma_f32_16x16x32_bf16 v[20:23], v[216:219], v[148:151], v[20:23]\n" \
  "v_mfma_f32_16x16x32_bf16 v[168:171], v[216:219], v[96:99], 0\n" \
  "v_mfma_f32_16x16x32_bf16 v[172:175], v[204:207], v[224:227], 0\n" \
  "v_mfma_f32_16x16x32_bf16 v[176:179], v[224:227], v[204:207], 0\n" \
  "s_mov_b32 exec_lo, 0x0\n" \
  "s_mov_b32 exec_hi, 0xffffffff\n" \
  "v_mov_b64 v[44:45], v[208:209]\n" \
  "v_mov_b64 v[46:47], v[210:211]\n" \
  "v_mov_b64 v[60:61], v[184:185]\n" \
  "v_mov_b64 v[62:63], v[186:187]\n" \
  "v_mov_b64 v[68:69], v[212:213]\n" \
  "v_mov_b64 v[70:71], v[214:215]\n" \
  "s_mov_b32 exec_lo, 0xff00ff00\n" \
  "s_mov_b32 exec_hi, 0x0\n" \
  "v_add_f32 v92, v92, v168\n" \
  "v_add_f32 v93, v93, v169\n" \
  "v_add_f32 v94, v94, v170\n" \
  "v_add_f32 v95, v95, v171\n" \
  "s_mov_b32 exec_lo, 0xf0000f00\n" \
  "s_mov_b32 exec_hi, 0x0\n" \
  "v_sub_f32 v92, v92, v176\n" \
  "v_sub_f32 v93, v93, v177\n" \
  "v_sub_f32 v94, v94, v178\n" \
  "v_sub_f32 v95, v95, v179\n" \
  "s_mov_b32 exec_lo, 0x0\n" \
  "s_mov_b32 exec_hi, 0xff00ff\n" \
  "v_mov_b64 v[92:93], 0\n" \
  "v_mov_b64 v[94:95], 0\n" \
  "s_mov_b32 exec_lo, 0x0\n" \
  "s_mov_b32 exec_hi, 0xf0000f\n" \
  "v_mov_b64 v[92:93], v[172:173]\n" \
  "v_mov_b64 v[94:95], v[174:175]\n" \
  "s_mov_b64 exec, -1\n" \
  "v_add_u32 v96, 6400, v250\n" \
  "v_add_u32 v97, 6400, v251\n" \
  "v_mov_b32 v98, 163328\n" \
  "v_mov_b32 v99, 1\n" \
  "v_lshlrev_b32 v203, 4, v253\n" \
  "s_lshr_b32 s35, s30, 4\n" \
  "s_lshl1_add_u32 s35, s29, s35\n" \
  "s_lshl_b32 s35, s35, 6\n" \
  "s_bfe_u32 s37, s30, 0x20002\n" \
  "s_lshl3_add_u32 s35, s37, s35\n" \
  "v_and_b32 v203, 16, v203\n" \
  "v_lshl_add_u32 v203, v203, 1, s35\n" \
  "s_mov_b64 exec, 1\n" \
  "ds_add_u32 v98, v99 offset:16\n" \
  "s_mov_b64 exec, -1\n" \
  "ds_write2_b32 v195, v180, v181 offset0:0 offset1:4\n" \
  "ds_write2_b32 v195, v182, v183 offset0:2 offset1:6\n" \
  "ds_write2_b32 v200, v196, v197 offset0:0 offset1:4\n" \
  "ds_write2_b32 v200, v198, v199 offset0:2 offset1:6\n" \
  "global_load_dwordx2 v[204:205], v203, s[16:17]\n" \
  "global_load_dwordx2 v[206:207], v203, s[18:19]\n" \
  "v_mov_b32 v203, 0\n" \
  "global_load_ushort v203, v203, s[20:21]\n" \
  "s_add_u32 s46, s44, 4800\n" \
  "s_addc_u32 s47, s45, 0\n" \
  "global_load_dwordx4 v[208:211], v96, s[44:45] offset:-3200\n" \
  "global_load_dwordx4 v[212:215], v97, s[44:45] offset:-2944\n" \
  "global_load_dwordx4 v[216:219], v96, s[44:45] offset:-1600\n" \
  "global_load_dwordx4 v[220:223], v97, s[44:45] offset:-1344\n" \
  "global_load_dwordx4 v[224:227], v96, s[44:45] offset:0\n" \
  "global_load_dwordx4 v[228:231], v97, s[44:45] offset:256\n" \
  "global_load_dwordx4 v[232:235], v96, s[44:45] offset:1600\n" \
  "global_load_dwordx4 v[236:239], v97, s[44:45] offset:1856\n" \
  "global_load_dwordx4 v[240:243], v96, s[44:45] offset:3200\n" \
  "global_load_dwordx4 v[244:247], v97, s[44:45] offset:3456\n" \
  "global_load_dwordx4 v[248:251], v96, s[46:47] offset:0\n" \
  "global_load_dwordx4 v[252:255], v97, s[46:47] offset:256\n" \
  "ds_read_b128 v[180:183], v194\n" \
  "v_add_u32 v194, 0x100, v194\n" \
  "L1_:\n" \
  "ds_read_b32 v99, v98 offset:12\n" \
  "s_waitcnt lgkmcnt(0)\n" \
  "v_readfirstlane_b32 s35, v99\n" \
  "s_cmp_lt_u32 s35, 4\n" \
  "s_cbranch_scc1 L1_\n" \
  "s_setprio 1\n" \
  "v_mad_u32_u16 v184, v182, 16, v188\n" \
  "ds_read_b128 v[128:131], v184 offset:0\n" \
  "ds_read_b128 v[132:135], v184 offset:64\n" \
  "v_mad_u32_u16 v185, v181, 16, v188\n" \
  "v_mad_u32_u16 v186, v182, 16, v188 op_sel:[1,0,0,0]\n" \
  "ds_read_b128 v[112:115], v185 offset:0\n" \
  "ds_read_b128 v[116:119], v185 offset:64\n" \
  "ds_read_b128 v[136:139], v186 offset:0\n" \
  "ds_read_b128 v[140:143], v186 offset:64\n" \
  "v_mad_u32_u16 v187, v181, 16, v188 op_sel:[1,0,0,0]\n" \
  "v_mad_u32_u16 v184, v180, 16, v188 op_sel:[1,0,0,0]\n" \
  "ds_read_b128 v[120:123], v187 offset:0\n" \
  "v_mad_u32_u16 v185, v180, 16, v188\n" \
  "ds_read_b128 v[124:127], v187 offset:64\n" \
  "ds_read_b128 v[104:107], v184 offset:0\n" \
  "v_mad_u32_u16 v186, v183, 16, v189\n" \
  "ds_read_b128 v[108:111], v184 offset:64\n" \
  "ds_read_b128 v[96:99], v185 offset:0\n" \
  "v_mad_u32_u16 v187, v183, 16, v190\n" \
  "s_waitcnt lgkmcnt(8)\n" \
  "s_mov_b32 s51, 3\n" \
  "s_mov_b32 s50, 8\n" \
  "s_call_b64 s[52:53], LA_loop\n" \
  "LA_ev0:\n" \
  "s_waitcnt vmcnt(10)\n" \
  "v_cvt_pk_u16_u32 v208, v208, v209\n" \
  "v_cvt_pk_u16_u32 v210, v210, v211\n" \
  "v_cvt_pk_u16_u32 v212, v212, v213\n" \
  "v_cvt_pk_u16_u32 v214, v214, v215\n" \
  "v_pk_lshrrev_b16 v209, 3, v208 op_sel_hi:[0,1]\n" \
  "v_pk_lshrrev_b16 v211, 3, v210 op_sel_hi:[0,1]\n" \
  "v_pk_lshrrev_b16 v213, 3, v212 op_sel_hi:[0,1]\n" \
  "v_pk_lshrrev_b16 v215, 3, v214 op_sel_hi:[0,1]\n" \
  "v_pk_mad_u16 v208, v208, 8, v209 op_sel_hi:[1,0,1]\n" \
  "v_pk_mad_u16 v210, v210, 8, v211 op_sel_hi:[1,0,1]\n" \
  "v_pk_mad_u16 v212, v212, 8, v213 op_sel_hi:[1,0,1]\n" \
  "v_pk_mad_u16 v214, v214, 8, v215 op_sel_hi:[1,0,1]\n" \
  "v_alignbit_b32 v212, v212, v212, v192\n" \
  "v_alignbit_b32 v214, v214, v214, v192\n" \
  "ds_write2_b32 v195, v208, v210 offset0:0 offset1:4\n" \
  "ds_write2_b32 v195, v212, v214 offset0:2 offset1:6\n" \
  "v_mov_b32 v196, v179\n" \
  "v_readfirstlane_b32 s6, v203\n" \
  "v_mov_b32 v208, 163328\n" \
  "s_mov_b32 s51, 4\n" \
  "s_mov_b32 s50, 1\n" \
  "s_mov_b32 s38, 0x800\n" \
  "s_call_b64 s[52:53], LA_loop\n" \
  "LA_ev6:\n" \
  "v_mov_b32 v202, v179\n" \
  "v_readfirstlane_b32 s35, v209\n" \
  "s_cmp_ge_u32 s35, 128\n" \
  "s_cbranch_scc1 LA_pl\n" \
  "L2_:\n" \
  "ds_read_b32 v209, v208 offset:4\n" \
  "s_waitcnt lgkmcnt(0)\n" \
  "v_readfirstlane_b32 s35, v209\n" \
  "s_cmp_lt_u32 s35, 128\n" \
  "s_cbranch_scc1 L2_\n" \
  "LA_pl:\n" \
  "s_and_b32 s4, s35, 15\n" \
  "s_sub_u32 s4, s4, 4\n" \
  "s_bfe_u32 s5, s35, 0x30004\n" \
  "s_sub_u32 s5, s5, s29\n" \
  "s_cmp_le_i32 s4, 0\n" \
  "s_cbranch_scc1 LA_nt\n" \
  "s_mul_i32 s35, s5, 51200\n" \
  "s_ashr_i32 s36, s35, 31\n" \
  "s_add_u32 s46, s46, s35\n" \
  "s_addc_u32 s47, s47, s36\n" \
  "v_lshrrev_b32 v216, 4, v188\n" \
  "v_mul_u32_u24 v216, 400, v216\n" \
  "v_add_u32 v216, 6400, v216\n" \
  "v_bfe_u32 v218, v194, 4, 4\n" \
  "v_min_u32 v217, 8, v218\n" \
  "v_lshl_add_u32 v217, v217, 4, v216\n" \
  "v_lshl_add_u32 v216, v218, 4, v216\n" \
  "global_load_dwordx4 v[208:211], v216, s[46:47] offset:0\n" \
  "global_load_dwordx4 v[212:215], v217, s[46:47] offset:256\n" \
  "LA_nt:\n" \
  "s_lshl_b32 s5, s5, 7\n" \
  "s_sub_u32 s5, s5, s4\n" \
  "s_min_i32 s51, s4, 0\n" \
  "s_add_u32 s51, s51, 4\n" \
  "s_cmp_eq_u32 s51, 0\n" \
  "s_cbranch_scc1 LA_ev7\n" \
  "s_call_b64 s[52:53], LA_loop\n" \
  "LA_ev7:\n" \
  "v_mov_b32 v203, v179\n" \
  "s_cmp_le_i32 s4, 0\n" \
  "s_cbranch_scc1 LA_ev8\n" \
  "s_waitcnt vmcnt(0)\n" \
  "v_cvt_pk_u16_u32 v208, v208, v209\n" \
  "v_cvt_pk_u16_u32 v210, v210, v211\n" \
  "v_cvt_pk_u16_u32 v212, v212, v213\n" \
  "v_cvt_pk_u16_u32 v214, v214, v215\n" \
  "v_pk_lshrrev_b16 v209, 3, v208 op_sel_hi:[0,1]\n" \
  "v_pk_lshrrev_b16 v211, 3, v210 op_sel_hi:[0,1]\n" \
  "v_pk_lshrrev_b16 v213, 3, v212 op_sel_hi:[0,1]\n" \
  "v_pk_lshrrev_b16 v215, 3, v214 op_sel_hi:[0,1]\n" \
  "v_pk_mad_u16 v208, v208, 8, v209 op_sel_hi:[1,0,1]\n" \
  "v_pk_mad_u16 v210, v210, 8, v211 op_sel_hi:[1,0,1]\n" \
  "v_pk_mad_u16 v212, v212, 8, v213 op_sel_hi:[1,0,1]\n" \
  "v_pk_mad_u16 v214, v214, 8, v215 op_sel_hi:[1,0,1]\n" \
  "v_alignbit_b32 v212, v212, v212, v192\n" \
  "v_alignbit_b32 v214, v214, v214, v192\n" \
  "ds_write2_b32 v195, v208, v210 offset0:0 offset1:4\n" \
  "ds_write2_b32 v195, v212, v214 offset0:2 offset1:6\n" \
  "s_lshl_b32 s35, s4, 8\n" \
  "s_add_u32 s35, s35, 0x400\n" \
  "v_subrev_u32 v194, s35, v194\n" \
  "s_mov_b32 s51, s4\n" \
  "s_call_b64 s[52:53], LA_loop\n" \
  "LA_ev8:\n" \
  "v_mov_b32 v195, v179\n" \
  "ds_read_b128 v[100:103], v185 offset:64\n" \
  "ds_read2_b32 v[144:145], v186 offset0:0 offset1:2\n" \
  "ds_read2_b32 v[146:147], v186 offset0:4 offset1:6\n" \
  "ds_read2_b32 v[148:149], v187 offset0:0 offset1:2\n" \
  "ds_read2_b32 v[150:151], v187 offset0:4 offset1:6\n" \
  "s_lshr_b32 s35, s30, 2\n" \
  "s_lshl3_add_u32 s33, s29, s35\n" \
  "s_and_b32 s34, s30, 3\n" \
  "s_lshl_b32 s59, s27, 11\n" \
  "s_mul_i32 s35, s33, 1600000\n" \
  "s_add_u32 s54, s22, s35\n" \
  "s_addc_u32 s55, s23, 0\n" \
  "s_mul_i32 s35, s34, 819200\n" \
  "s_lshl1_add_u32 s35, s59, s35\n" \
  "s_add_u32 s35, s35, 102400000\n" \
  "s_add_u32 s56, s22, s35\n" \
  "s_addc_u32 s57, s23, 0\n" \
  "v_mbcnt_lo_u32_b32 v189, -1, 0\n" \
  "v_mbcnt_hi_u32_b32 v189, -1, v189\n" \
  "v_lshlrev_b32 v188, 4, v189\n" \
  "s_waitcnt lgkmcnt(5)\n" \
  "v_fmac_f32 v176, v153, v73\n" \
  "v_pk_fma_f32 v[176:177], v[154:155], v[74:75], v[176:177]\n" \
  "v_pk_fma_f32 v[176:177], v[156:157], v[76:77], v[176:177]\n" \
  "v_pk_fma_f32 v[176:177], v[158:159], v[78:79], v[176:177]\n" \
  "v_pk_fma_f32 v[176:177], v[160:161], v[80:81], v[176:177]\n" \
  "v_pk_fma_f32 v[176:177], v[162:163], v[82:83], v[176:177]\n" \
  "v_pk_fma_f32 v[176:177], v[164:165], v[84:85], v[176:177]\n" \
  "v_pk_fma_f32 v[176:177], v[166:167], v[86:87], v[176:177]\n" \
  "v_pk_fma_f32 v[176:177], v[168:169], v[88:89], v[176:177]\n" \
  "v_pk_fma_f32 v[176:177], v[170:171], v[90:91], v[176:177]\n" \
  "v_mfma_f32_16x16x32_bf16 v[152:155], v[112:115], v[112:115], 0\n" \
  "v_fmac_f32 v176, v172, v92\n" \
  "v_mfma_f32_16x16x32_bf16 v[152:155], v[116:119], v[116:119], v[152:155]\n" \
  "v_fmac_f32 v176, v173, v93\n" \
  "v_mfma_f32_16x16x32_bf16 v[156:159], v[128:131], v[128:131], 0\n" \
  "v_fmac_f32 v176, v174, v94\n" \
  "v_mfma_f32_16x16x32_bf16 v[156:159], v[132:135], v[132:135], v[156:159]\n" \
  "v_fma_f32 v178, v175, v95, v176\n" \
  "v_mfma_f32_16x16x32_bf16 v[160:163], v[128:131], v[136:139], 0\n" \
  "v_add_f32 v178, v178, v177\n" \
  "v_mfma_f32_16x16x32_bf16 v[160:163], v[132:135], v[140:143], v[160:163]\n" \
  "v_mul_f32 v176, v152, v0\n" \
  "v_mfma_f32_16x16x32_bf16 v[164:167], v[136:139], v[136:139], 0\n" \
  "v_mul_f32 v177, v153, v1\n" \
  "v_fmac_f32 v176, v154, v2\n" \
  "v_mfma_f32_16x16x32_bf16 v[164:167], v[140:143], v[140:143], v[164:167]\n" \
  "v_fmac_f32 v176, v155, v3\n" \
  "v_fmac_f32 v176, v156, v4\n" \
  "s_waitcnt lgkmcnt(0)\n" \
  "s_setprio 3\n" \
  "s_barrier\n" \
  "global_load_dwordx4 v[208:211], v188, s[56:57] offset:0 sc1\n" \
  "global_load_dwordx4 v[212:215], v188, s[56:57] offset:1024 sc1\n" \
  "global_load_dwordx4 v[216:219], v188, s[56:57] offset:2048 sc1\n" \
  "global_load_dwordx4 v[220:223], v188, s[56:57] offset:3072 sc1\n" \
  "v_mfma_f32_16x16x32_bf16 v[168:171], v[112:115], v[136:139], 0\n" \
  "v_fmac_f32 v176, v157, v5\n" \
  "v_mov_b32_dpp v179, v179 row_ror:4 row_mask:0xf bank_mask:0xf\n" \
  "v_lshrrev_b32 v180, 1, v189\n" \
  "v_mfma_f32_16x16x32_bf16 v[168:171], v[116:119], v[140:143], v[168:171]\n" \
  "v_fmac_f32 v176, v158, v6\n" \
  "v_fmac_f32 v176, v159, v7\n" \
  "v_xor_b32 v180, v180, v189\n" \
  "v_mfma_f32_16x16x32_bf16 v[172:175], v[112:115], v[128:131], 0\n" \
  "v_fmac_f32 v176, v160, v8\n" \
  "v_fmac_f32 v176, v161, v9\n" \
  "v_and_b32 v180, 8, v180\n" \
  "v_mfma_f32_16x16x32_bf16 v[172:175], v[116:119], v[132:135], v[172:175]\n" \
  "v_fmac_f32 v176, v162, v10\n" \
  "v_fmac_f32 v176, v163, v11\n" \
  "v_bfe_u32 v181, v189, 2, 1\n" \
  "v_mfma_f32_16x16x32_bf16 v[152:155], v[112:115], v[120:123], 0\n" \
  "v_fmac_f32 v176, v164, v12\n" \
  "v_add_f32_dpp v178, v178, v178 row_ror:8 row_mask:0xf bank_mask:0xf\n" \
  "v_lshlrev_b32_e64 v181, v181, 5\n" \
  "v_mfma_f32_16x16x32_bf16 v[152:155], v[116:119], v[124:127], v[152:155]\n" \
  "v_fmac_f32 v176, v165, v13\n" \
  "v_fmac_f32 v176, v166, v14\n" \
  "v_lshlrev_b32 v180, v180, v181\n" \
  "v_mfma_f32_16x16x32_bf16 v[156:159], v[120:123], v[136:139], 0\n" \
  "v_fmac_f32 v176, v167, v15\n" \
  "v_fmac_f32 v176, v168, v16\n" \
  "v_bfe_u32 v181, v189, 2, 2\n" \
  "v_mfma_f32_16x16x32_bf16 v[156:159], v[124:127], v[140:143], v[156:159]\n" \
  "v_fmac_f32 v176, v169, v17\n" \
  "v_fmac_f32 v176, v170, v18\n" \
  "v_lshlrev_b32_e64 v181, v181, 1\n" \
  "v_mfma_f32_16x16x32_bf16 v[160:163], v[104:107], v[128:131], 0\n" \
  "v_fmac_f32 v176, v171, v19\n" \
  "v_cmp_gt_u32 vcc, 32, v189\n" \
  "v_mfma_f32_16x16x32_bf16 v[160:163], v[108:111], v[132:135], v[160:163]\n" \
  "v_fmac_f32 v176, v172, v20\n" \
  "v_fmac_f32 v176, v173, v21\n" \
  "v_mov_b32 v191, 0x3f80\n" \
  "v_mfma_f32_16x16x32_bf16 v[164:167], v[120:123], v[128:131], 0\n" \
  "v_fmac_f32 v176, v174, v22\n" \
  "v_fmac_f32 v176, v175, v23\n" \
  "v_add_u32 v190, 32000, v188\n" \
  "v_mfma_f32_16x16x32_bf16 v[164:167], v[124:127], v[132:135], v[164:167]\n" \
  "v_fmac_f32 v176, v152, v24\n" \
  "v_fmac_f32 v176, v153, v25\n" \
  "v_cndmask_b32 v192, v181, v180, vcc\n" \
  "v_mfma_f32_16x16x32_bf16 v[168:171], v[96:99], v[128:131], 0\n" \
  "v_mov_b64 v[172:173], 0\n" \
  "v_mov_b64 v[174:175], 0\n" \
  "v_bfe_i32 v180, v192, 0, 1\n" \
  "v_mfma_f32_16x16x32_bf16 v[168:171], v[100:103], v[132:135], v[168:171]\n" \
  "v_fmac_f32 v176, v154, v26\n" \
  "v_fmac_f32 v176, v155, v27\n" \
  "v_and_b32 v180, v180, v191\n" \
  "v_smfmac_f32_16x16x64_bf16 v[172:175], v[144:147], v[128:135], v193\n" \
  "v_fmac_f32 v176, v156, v28\n" \
  "v_bfe_i32 v181, v192, 1, 1\n" \
  "v_smfmac_f32_16x16x64_bf16 v[172:175], v[148:151], v[136:143], v193\n" \
  "v_fmac_f32 v176, v157, v29\n" \
  "v_and_b32 v181, v181, v191\n" \
  "v_mfma_f32_16x16x32_bf16 v[152:155], v[96:99], v[112:115], 0\n" \
  "v_fmac_f32 v176, v158, v30\n" \
  "v_fmac_f32 v176, v159, v31\n" \
  "v_bfe_i32 v182, v192, 2, 1\n" \
  "v_mfma_f32_16x16x32_bf16 v[152:155], v[100:103], v[116:119], v[152:155]\n" \
  "v_fmac_f32 v176, v160, v32\n" \
  "v_fmac_f32 v176, v161, v33\n" \
  "v_and_b32 v182, v182, v191\n" \
  "v_mfma_f32_16x16x32_bf16 v[156:159], v[104:107], v[112:115], 0\n" \
  "v_fmac_f32 v176, v162, v34\n" \
  "v_fmac_f32 v176, v163, v35\n" \
  "v_bfe_i32 v183, v192, 3, 1\n" \
  "v_mfma_f32_16x16x32_bf16 v[156:159], v[108:111], v[116:119], v[156:159]\n" \
  "v_fmac_f32 v176, v164, v36\n" \
  "v_fmac_f32 v176, v165, v37\n" \
  "v_add_f32_dpp v179, v178, v178 row_ror:4 row_mask:0xf bank_mask:0x1\n" \
  "v_pk_fma_f32 v[176:177], v[166:167], v[38:39], v[176:177]\n" \
  "v_pk_fma_f32 v[176:177], v[168:169], v[40:41], v[176:177]\n" \
  "v_pk_fma_f32 v[176:177], v[170:171], v[42:43], v[176:177]\n" \
  "v_pk_fma_f32 v[176:177], v[172:173], v[44:45], v[176:177]\n" \
  "v_pk_fma_f32 v[176:177], v[174:175], v[46:47], v[176:177]\n" \
  "v_pk_fma_f32 v[176:177], v[152:153], v[48:49], v[176:177]\n" \
  "v_pk_fma_f32 v[176:177], v[154:155], v[50:51], v[176:177]\n" \
  "v_and_b32 v183, v183, v191\n" \
  "v_mfma_f32_16x16x32_bf16 v[160:163], v[104:107], v[136:139], 0\n" \
  "v_mov_b64 v[164:165], 0\n" \
  "v_mov_b64 v[166:167], 0\n" \
  "v_and_b32 v192, 3, v189\n" \
  "v_mfma_f32_16x16x32_bf16 v[160:163], v[108:111], v[140:143], v[160:163]\n" \
  "v_fmac_f32 v176, v156, v52\n" \
  "v_mul_u32_u24 v192, 0x5555, v192\n" \
  "v_smfmac_f32_16x16x64_bf16 v[164:167], v[144:147], v[112:119], v193\n" \
  "v_fmac_f32 v176, v157, v53\n" \
  "v_add_u32 v189, 16000, v188\n" \
  "v_smfmac_f32_16x16x64_bf16 v[164:167], v[148:151], v[120:127], v193\n" \
  "v_fmac_f32 v176, v158, v54\n" \
  "v_add_u32 v191, 48000, v188\n" \
  "v_mfma_f32_16x16x32_bf16 v[168:171], v[96:99], v[136:139], 0\n" \
  "v_mov_b64 v[172:173], 0\n" \
  "v_mov_b64 v[174:175], 0\n" \
  "v_mfma_f32_16x16x32_bf16 v[168:171], v[100:103], v[140:143], v[168:171]\n" \
  "v_fmac_f32 v176, v159, v55\n" \
  "v_fmac_f32 v176, v160, v56\n" \
  "v_smfmac_f32_16x16x64_bf16 v[172:175], v[148:151], v[104:111], v193\n" \
  "v_fmac_f32 v176, v161, v57\n" \
  "v_smfmac_f32_16x16x64_bf16 v[172:175], v[144:147], v[96:103], v193\n" \
  "v_fmac_f32 v176, v162, v58\n" \
  "v_mfma_f32_16x16x32_bf16 v[152:155], v[120:123], v[120:123], 0\n" \
  "v_fmac_f32 v176, v163, v59\n" \
  "v_fmac_f32 v176, v164, v60\n" \
  "v_mfma_f32_16x16x32_bf16 v[152:155], v[124:127], v[124:127], v[152:155]\n" \
  "v_fmac_f32 v176, v165, v61\n" \
  "v_mfma_f32_16x16x32_bf16 v[156:159], v[104:107], v[120:123], 0\n" \
  "v_fmac_f32 v176, v166, v62\n" \
  "v_mfma_f32_16x16x32_bf16 v[156:159], v[108:111], v[124:127], v[156:159]\n" \
  "v_fmac_f32 v176, v167, v63\n" \
  "v_fmac_f32 v176, v168, v64\n" \
  "v_mfma_f32_16x16x32_bf16 v[160:163], v[96:99], v[120:123], 0\n" \
  "v_fmac_f32 v176, v169, v65\n" \
  "v_mfma_f32_16x16x32_bf16 v[160:163], v[100:103], v[124:127], v[160:163]\n" \
  "v_fmac_f32 v176, v170, v66\n" \
  "v_mfma_f32_16x16x32_bf16 v[164:167], v[96:99], v[104:107], 0\n" \
  "v_fmac_f32 v176, v171, v67\n" \
  "v_mfma_f32_16x16x32_bf16 v[164:167], v[100:103], v[108:111], v[164:167]\n" \
  "v_fmac_f32 v176, v172, v68\n" \
  "v_fmac_f32 v176, v173, v69\n" \
  "v_mfma_f32_16x16x32_bf16 v[168:171], v[104:107], v[104:107], 0\n" \
  "v_fmac_f32 v176, v174, v70\n" \
  "v_mfma_f32_16x16x32_bf16 v[168:171], v[108:111], v[108:111], v[168:171]\n" \
  "v_fmac_f32 v176, v175, v71\n" \
  "s_add_u32 s35, s59, 64000\n" \
  "s_add_u32 s54, s54, s35\n" \
  "s_addc_u32 s55, s55, 0\n" \
  "s_add_u32 s58, s59, 0x10000\n" \
  "s_mov_b32 m0, s58\n" \
  "s_add_u32 s35, s58, 0x8000\n" \
  "global_load_lds_dwordx4 v188, s[54:55] sc1\n" \
  "global_load_lds_dwordx4 v188, s[54:55] offset:1024 sc1\n" \
  "v_mfma_f32_16x16x32_bf16 v[172:175], v[148:151], v[144:147], 0\n" \
  "v_fmac_f32 v176, v152, v72\n" \
  "v_mfma_f32_16x16x32_bf16 v[172:175], v[96:99], v[96:99], v[172:175]\n" \
  "s_add_u32 m0, s58, 0x4000\n" \
  "s_cmp_lt_u32 s35, 0x28000\n" \
  "global_load_lds_dwordx4 v189, s[54:55] sc1\n" \
  "global_load_lds_dwordx4 v189, s[54:55] offset:1024 sc1\n" \
  "s_cselect_b32 s58, s35, s59\n" \
  "s_add_u32 s54, s54, 32000\n" \
  "s_addc_u32 s55, s55, 0\n" \
  "v_mfma_f32_16x16x32_bf16 v[172:175], v[100:103], v[100:103], v[172:175]\n" \
  "s_mov_b32 s51, 7\n" \
  "v_fmac_f32 v176, v153, v73\n" \
  "v_pk_fma_f32 v[176:177], v[154:155], v[74:75], v[176:177]\n" \
  "v_pk_fma_f32 v[176:177], v[156:157], v[76:77], v[176:177]\n" \
  "v_pk_fma_f32 v[176:177], v[158:159], v[78:79], v[176:177]\n" \
  "v_pk_fma_f32 v[176:177], v[160:161], v[80:81], v[176:177]\n" \
  "v_pk_fma_f32 v[176:177], v[162:163], v[82:83], v[176:177]\n" \
  "v_pk_fma_f32 v[176:177], v[164:165], v[84:85], v[176:177]\n" \
  "v_pk_fma_f32 v[176:177], v[166:167], v[86:87], v[176:177]\n" \
  "v_pk_fma_f32 v[176:177], v[168:169], v[88:89], v[176:177]\n" \
  "v_pk_fma_f32 v[176:177], v[170:171], v[90:91], v[176:177]\n" \
  "v_fmac_f32 v176, v172, v92\n" \
  "v_fmac_f32 v176, v173, v93\n" \
  "v_fmac_f32 v176, v174, v94\n" \
  "v_fma_f32 v178, v175, v95, v176\n" \
  "v_add_f32 v178, v178, v177\n" \
  "s_nop 1\n" \
  "v_add_f32_dpp v178, v178, v178 row_ror:8 row_mask:0xf bank_mask:0xf\n" \
  "v_mov_b32_dpp v179, v179 row_ror:4 row_mask:0xf bank_mask:0xf\n" \
  "s_nop 1\n" \
  "v_add_f32_dpp v179, v178, v178 row_ror:4 row_mask:0xf bank_mask:0x1\n" \
  "v_mov_b32 v99, v195\n" \
  "s_nop 0\n" \
  "v_mov_b32_dpp v196, v179 row_ror:8 row_mask:0xf bank_mask:0xc\n" \
  "s_nop 1\n" \
  "v_permlane16_swap_b32 v196, v197\n" \
  "v_permlane16_swap_b32 v198, v199\n" \
  "v_permlane16_swap_b32 v200, v201\n" \
  "v_permlane16_swap_b32 v202, v203\n" \
  "v_permlane16_swap_b32 v195, v99\n" \
  "v_add_f32 v196, v196, v197\n" \
  "v_add_f32 v198, v198, v199\n" \
  "v_add_f32 v200, v200, v201\n" \
  "v_add_f32 v202, v202, v203\n" \
  "v_add_f32 v195, v195, v99\n" \
  "v_lshrrev_b32 v96, 4, v188\n" \
  "v_and_b32 v97, 1, v96\n" \
  "v_mov_b32 v99, v195\n" \
  "v_lshrrev_b32 v98, 4, v96\n" \
  "v_permlane32_swap_b32 v196, v198\n" \
  "v_permlane32_swap_b32 v200, v202\n" \
  "v_permlane32_swap_b32 v195, v99\n" \
  "v_add_f32 v196, v196, v198\n" \
  "v_add_f32 v200, v200, v202\n" \
  "v_add_f32 v195, v195, v99\n" \
  "v_cmp_eq_u32 vcc, 1, v97\n" \
  "v_lshlrev_b32 v97, 2, v97\n" \
  "v_add_lshl_u32 v97, v97, v98, 2\n" \
  "v_bfe_u32 v98, v96, 2, 2\n" \
  "v_add_f32_dpp v196, v196, v196 quad_perm:[1,0,3,2] row_mask:0xf bank_mask:0xf\n" \
  "v_add_f32_dpp v200, v200, v200 quad_perm:[1,0,3,2] row_mask:0xf bank_mask:0xf\n" \
  "v_add_f32_dpp v195, v195, v195 quad_perm:[1,0,3,2] row_mask:0xf bank_mask:0xf\n" \
  "v_sub_u32 v97, v97, v98\n" \
  "v_cndmask_b32 v196, v196, v200, vcc\n" \
  "v_add_u32 v97, 1, v97\n" \
  "s_min_i32 s35, s4, 0\n" \
  "s_add_u32 s36, s4, 32\n" \
  "s_add_u32 s37, s4, 29\n" \
  "v_add_f32_dpp v195, v195, v195 quad_perm:[2,3,0,1] row_mask:0xf bank_mask:0xf\n" \
  "v_add_f32_dpp v196, v196, v196 quad_perm:[2,3,0,1] row_mask:0xf bank_mask:0xf\n" \
  "s_mov_b32 exec_lo, 0x0\n" \
  "s_mov_b32 exec_hi, 0xaaaa0000\n" \
  "v_add_u32 v97, s35, v97\n" \
  "s_mov_b32 exec_lo, 0x5500\n" \
  "s_mov_b32 exec_hi, 0x0\n" \
  "v_add_u32 v97, s36, v97\n" \
  "s_mov_b32 exec_lo, 0x4444\n" \
  "s_mov_b32 exec_hi, 0x0\n" \
  "v_sub_u32 v97, s37, v98\n" \
  "v_mov_b32 v196, v195\n" \
  "s_mov_b64 exec, -1\n" \
  "v_cmp_lt_u32 vcc, 31, v97\n" \
  "v_add_u32 v98, s5, v97\n" \
  "s_lshl2_add_u32 s35, s7, s28\n" \
  "s_lshl_b32 s35, s35, 5\n" \
  "v_cndmask_b32 v97, v97, v98, vcc\n" \
  "v_add_lshl_u32 v97, v97, s35, 2\n" \
  "s_lshl_b32 s35, s31, 16\n" \
  "s_add_u32 s35, s35, 109871104\n" \
  "s_add_u32 s40, s22, s35\n" \
  "s_addc_u32 s41, s23, 0\n" \
  "v_min_u32 v196, -2, v196\n" \
  "v_lshlrev_b32 v184, 16, v204\n" \
  "v_and_b32 v185, 0xffff0000, v204\n" \
  "v_lshlrev_b32 v186, 16, v205\n" \
  "v_and_b32 v187, 0xffff0000, v205\n" \
  "s_barrier\n" \
  "s_add_u32 s56, s56, 32768\n" \
  "s_addc_u32 s57, s57, 0\n" \
  "global_load_dwordx4 v[224:227], v188, s[56:57] sc1\n" \
  "global_load_dwordx4 v[228:231], v188, s[56:57] offset:1024 sc1\n" \
  "global_load_dwordx4 v[232:235], v188, s[56:57] offset:2048 sc1\n" \
  "global_load_dwordx4 v[236:239], v188, s[56:57] offset:3072 sc1\n" \
  "s_add_u32 s56, s56, 32768\n" \
  "s_addc_u32 s57, s57, 0\n" \
  "s_mov_b32 exec_lo, 0x33337777\n" \
  "s_mov_b32 exec_hi, 0x33333333\n" \
  "global_store_dword v97, v196, s[40:41] sc1\n" \
  "s_mov_b64 exec, -1\n" \
  "s_waitcnt vmcnt(9)\n" \
  "s_setprio 0\n" \
  "s_branch LG_start\n" \
  ".p2align 6\n" \
  "LA_loop:\n" \
  "LA_mid:\n" \
  "v_fmac_f32 v176, v153, v73\n" \
  "v_pk_fma_f32 v[176:177], v[154:155], v[74:75], v[176:177]\n" \
  "v_pk_fma_f32 v[176:177], v[156:157], v[76:77], v[176:177]\n" \
  "v_pk_fma_f32 v[176:177], v[158:159], v[78:79], v[176:177]\n" \
  "v_pk_fma_f32 v[176:177], v[160:161], v[80:81], v[176:177]\n" \
  "v_pk_fma_f32 v[176:177], v[162:163], v[82:83], v[176:177]\n" \
  "v_pk_fma_f32 v[176:177], v[164:165], v[84:85], v[176:177]\n" \
  "v_pk_fma_f32 v[176:177], v[166:167], v[86:87], v[176:177]\n" \
  "v_pk_fma_f32 v[176:177], v[168:169], v[88:89], v[176:177]\n" \
  "v_pk_fma_f32 v[176:177], v[170:171], v[90:91], v[176:177]\n" \
  "v_mfma_f32_16x16x32_bf16 v[152:155], v[112:115], v[112:115], 0\n" \
  "s_waitcnt lgkmcnt(7)\n" \
  "ds_read_b128 v[100:103], v185 offset:64\n" \
  "v_fmac_f32 v176, v172, v92\n" \
  "v_mfma_f32_16x16x32_bf16 v[152:155], v[116:119], v[116:119], v[152:155]\n" \
  "ds_read2_b32 v[144:145], v186 offset0:0 offset1:2\n" \
  "v_fmac_f32 v176, v173, v93\n" \
  "v_mfma_f32_16x16x32_bf16 v[156:159], v[128:131], v[128:131], 0\n" \
  "ds_read2_b32 v[146:147], v186 offset0:4 offset1:6\n" \
  "v_fmac_f32 v176, v174, v94\n" \
  "v_mfma_f32_16x16x32_bf16 v[156:159], v[132:135], v[132:135], v[156:159]\n" \
  "s_waitcnt lgkmcnt(9)\n" \
  "ds_read2_b32 v[148:149], v187 offset0:0 offset1:2\n" \
  "v_fma_f32 v178, v175, v95, v176\n" \
  "v_mfma_f32_16x16x32_bf16 v[160:163], v[128:131], v[136:139], 0\n" \
  "s_waitcnt lgkmcnt(9)\n" \
  "ds_read2_b32 v[150:151], v187 offset0:4 offset1:6\n" \
  "v_add_f32 v178, v178, v177\n" \
  "v_mfma_f32_16x16x32_bf16 v[160:163], v[132:135], v[140:143], v[160:163]\n" \
  "ds_read_b128 v[180:183], v194\n" \
  "v_mul_f32 v176, v152, v0\n" \
  "v_mfma_f32_16x16x32_bf16 v[164:167], v[136:139], v[136:139], 0\n" \
  "v_mul_f32 v177, v153, v1\n" \
  "v_fmac_f32 v176, v154, v2\n" \
  "v_mfma_f32_16x16x32_bf16 v[164:167], v[140:143], v[140:143], v[164:167]\n" \
  "v_fmac_f32 v176, v155, v3\n" \
  "v_fmac_f32 v176, v156, v4\n" \
  "v_mfma_f32_16x16x32_bf16 v[168:171], v[112:115], v[136:139], 0\n" \
  "v_fmac_f32 v176, v157, v5\n" \
  "v_mov_b32_dpp v179, v179 row_ror:4 row_mask:0xf bank_mask:0xf\n" \
  "v_mfma_f32_16x16x32_bf16 v[168:171], v[116:119], v[140:143], v[168:171]\n" \
  "v_fmac_f32 v176, v158, v6\n" \
  "v_fmac_f32 v176, v159, v7\n" \
  "v_mfma_f32_16x16x32_bf16 v[172:175], v[112:115], v[128:131], 0\n" \
  "v_fmac_f32 v176, v160, v8\n" \
  "v_fmac_f32 v176, v161, v9\n" \
  "v_mfma_f32_16x16x32_bf16 v[172:175], v[116:119], v[132:135], v[172:175]\n" \
  "s_waitcnt lgkmcnt(10)\n" \
  "v_fmac_f32 v176, v162, v10\n" \
  "v_fmac_f32 v176, v163, v11\n" \
  "v_mfma_f32_16x16x32_bf16 v[152:155], v[112:115], v[120:123], 0\n" \
  "s_waitcnt lgkmcnt(9)\n" \
  "v_fmac_f32 v176, v164, v12\n" \
  "v_add_f32_dpp v178, v178, v178 row_ror:8 row_mask:0xf bank_mask:0xf\n" \
  "v_mfma_f32_16x16x32_bf16 v[152:155], v[116:119], v[124:127], v[152:155]\n" \
  "v_fmac_f32 v176, v165, v13\n" \
  "v_fmac_f32 v176, v166, v14\n" \
  "v_mfma_f32_16x16x32_bf16 v[156:159], v[120:123], v[136:139], 0\n" \
  "v_fmac_f32 v176, v167, v15\n" \
  "v_fmac_f32 v176, v168, v16\n" \
  "v_mfma_f32_16x16x32_bf16 v[156:159], v[124:127], v[140:143], v[156:159]\n" \
  "s_waitcnt lgkmcnt(8)\n" \
  "v_fmac_f32 v176, v169, v17\n" \
  "v_fmac_f32 v176, v170, v18\n" \
  "v_mfma_f32_16x16x32_bf16 v[160:163], v[104:107], v[128:131], 0\n" \
  "s_waitcnt lgkmcnt(0)\n" \
  "v_fmac_f32 v176, v171, v19\n" \
  "v_mad_u32_u16 v184, v182, 16, v188\n" \
  "v_mfma_f32_16x16x32_bf16 v[160:163], v[108:111], v[132:135], v[160:163]\n" \
  "v_fmac_f32 v176, v172, v20\n" \
  "v_fmac_f32 v176, v173, v21\n" \
  "v_mfma_f32_16x16x32_bf16 v[164:167], v[120:123], v[128:131], 0\n" \
  "v_fmac_f32 v176, v174, v22\n" \
  "v_fmac_f32 v176, v175, v23\n" \
  "v_mfma_f32_16x16x32_bf16 v[164:167], v[124:127], v[132:135], v[164:167]\n" \
  "v_fmac_f32 v176, v152, v24\n" \
  "v_fmac_f32 v176, v153, v25\n" \
  "v_mfma_f32_16x16x32_bf16 v[168:171], v[96:99], v[128:131], 0\n" \
  "v_mov_b64 v[172:173], 0\n" \
  "v_mov_b64 v[174:175], 0\n" \
  "v_mfma_f32_16x16x32_bf16 v[168:171], v[100:103], v[132:135], v[168:171]\n" \
  "v_fmac_f32 v176, v154, v26\n" \
  "v_fmac_f32 v176, v155, v27\n" \
  "v_smfmac_f32_16x16x64_bf16 v[172:175], v[144:147], v[128:135], v193\n" \
  "ds_read_b128 v[128:131], v184 offset:0\n" \
  "v_fmac_f32 v176, v156, v28\n" \
  "v_smfmac_f32_16x16x64_bf16 v[172:175], v[148:151], v[136:143], v193\n" \
  "ds_read_b128 v[132:135], v184 offset:64\n" \
  "v_fmac_f32 v176, v157, v29\n" \
  "v_mfma_f32_16x16x32_bf16 v[152:155], v[96:99], v[112:115], 0\n" \
  "v_fmac_f32 v176, v158, v30\n" \
  "v_fmac_f32 v176, v159, v31\n" \
  "v_mfma_f32_16x16x32_bf16 v[152:155], v[100:103], v[116:119], v[152:155]\n" \
  "v_fmac_f32 v176, v160, v32\n" \
  "v_fmac_f32 v176, v161, v33\n" \
  "v_mfma_f32_16x16x32_bf16 v[156:159], v[104:107], v[112:115], 0\n" \
  "v_fmac_f32 v176, v162, v34\n" \
  "v_fmac_f32 v176, v163, v35\n" \
  "v_mfma_f32_16x16x32_bf16 v[156:159], v[108:111], v[116:119], v[156:159]\n" \
  "v_fmac_f32 v176, v164, v36\n" \
  "v_fmac_f32 v176, v165, v37\n" \
  "v_mad_u32_u16 v185, v181, 16, v188\n" \
  "v_add_f32_dpp v179, v178, v178 row_ror:4 row_mask:0xf bank_mask:0x1\n" \
  "v_add_u32 v194, 0x100, v194\n" \
  "v_pk_fma_f32 v[176:177], v[166:167], v[38:39], v[176:177]\n" \
  "v_pk_fma_f32 v[176:177], v[168:169], v[40:41], v[176:177]\n" \
  "v_pk_fma_f32 v[176:177], v[170:171], v[42:43], v[176:177]\n" \
  "v_pk_fma_f32 v[176:177], v[172:173], v[44:45], v[176:177]\n" \
  "v_pk_fma_f32 v[176:177], v[174:175], v[46:47], v[176:177]\n" \
  "v_pk_fma_f32 v[176:177], v[152:153], v[48:49], v[176:177]\n" \
  "v_pk_fma_f32 v[176:177], v[154:155], v[50:51], v[176:177]\n" \
  "v_mfma_f32_16x16x32_bf16 v[160:163], v[104:107], v[136:139], 0\n" \
  "v_mov_b64 v[164:165], 0\n" \
  "v_mov_b64 v[166:167], 0\n" \
  "v_mfma_f32_16x16x32_bf16 v[160:163], v[108:111], v[140:143], v[160:163]\n" \
  "v_fmac_f32 v176, v156, v52\n" \
  "v_mad_u32_u16 v186, v182, 16, v188 op_sel:[1,0,0,0]\n" \
  "v_smfmac_f32_16x16x64_bf16 v[164:167], v[144:147], v[112:119], v193\n" \
  "ds_read_b128 v[112:115], v185 offset:0\n" \
  "v_fmac_f32 v176, v157, v53\n" \
  "v_smfmac_f32_16x16x64_bf16 v[164:167], v[148:151], v[120:127], v193\n" \
  "ds_read_b128 v[116:119], v185 offset:64\n" \
  "v_fmac_f32 v176, v158, v54\n" \
  "v_mfma_f32_16x16x32_bf16 v[168:171], v[96:99], v[136:139], 0\n" \
  "v_mov_b64 v[172:173], 0\n" \
  "v_mov_b64 v[174:175], 0\n" \
  "v_mfma_f32_16x16x32_bf16 v[168:171], v[100:103], v[140:143], v[168:171]\n" \
  "v_fmac_f32 v176, v159, v55\n" \
  "v_fmac_f32 v176, v160, v56\n" \
  "v_smfmac_f32_16x16x64_bf16 v[172:175], v[148:151], v[104:111], v193\n" \
  "ds_read_b128 v[136:139], v186 offset:0\n" \
  "v_fmac_f32 v176, v161, v57\n" \
  "v_smfmac_f32_16x16x64_bf16 v[172:175], v[144:147], v[96:103], v193\n" \
  "ds_read_b128 v[140:143], v186 offset:64\n" \
  "v_fmac_f32 v176, v162, v58\n" \
  "v_mfma_f32_16x16x32_bf16 v[152:155], v[120:123], v[120:123], 0\n" \
  "v_fmac_f32 v176, v163, v59\n" \
  "v_fmac_f32 v176, v164, v60\n" \
  "v_mfma_f32_16x16x32_bf16 v[152:155], v[124:127], v[124:127], v[152:155]\n" \
  "v_fmac_f32 v176, v165, v61\n" \
  "v_mad_u32_u16 v187, v181, 16, v188 op_sel:[1,0,0,0]\n" \
  "v_mfma_f32_16x16x32_bf16 v[156:159], v[104:107], v[120:123], 0\n" \
  "v_fmac_f32 v176, v166, v62\n" \
  "v_mad_u32_u16 v184, v180, 16, v188 op_sel:[1,0,0,0]\n" \
  "v_mfma_f32_16x16x32_bf16 v[156:159], v[108:111], v[124:127], v[156:159]\n" \
  "v_fmac_f32 v176, v167, v63\n" \
  "v_fmac_f32 v176, v168, v64\n" \
  "v_mfma_f32_16x16x32_bf16 v[160:163], v[96:99], v[120:123], 0\n" \
  "ds_read_b128 v[120:123], v187 offset:0\n" \
  "v_fmac_f32 v176, v169, v65\n" \
  "v_mfma_f32_16x16x32_bf16 v[160:163], v[100:103], v[124:127], v[160:163]\n" \
  "v_fmac_f32 v176, v170, v66\n" \
  "v_mad_u32_u16 v185, v180, 16, v188\n" \
  "v_mfma_f32_16x16x32_bf16 v[164:167], v[96:99], v[104:107], 0\n" \
  "ds_read_b128 v[124:127], v187 offset:64\n" \
  "v_fmac_f32 v176, v171, v67\n" \
  "v_mfma_f32_16x16x32_bf16 v[164:167], v[100:103], v[108:111], v[164:167]\n" \
  "v_fmac_f32 v176, v172, v68\n" \
  "v_fmac_f32 v176, v173, v69\n" \
  "v_mfma_f32_16x16x32_bf16 v[168:171], v[104:107], v[104:107], 0\n" \
  "ds_read_b128 v[104:107], v184 offset:0\n" \
  "v_fmac_f32 v176, v174, v70\n" \
  "v_mfma_f32_16x16x32_bf16 v[168:171], v[108:111], v[108:111], v[168:171]\n" \
  "s_sub_u32 s51, s51, 1\n" \
  "v_fmac_f32 v176, v175, v71\n" \
  "v_mad_u32_u16 v186, v183, 16, v189\n" \
  "v_mfma_f32_16x16x32_bf16 v[172:175], v[148:151], v[144:147], 0\n" \
  "s_cmp_lg_u32 s51, 0\n" \
  "ds_read_b128 v[108:111], v184 offset:64\n" \
  "v_fmac_f32 v176, v152, v72\n" \
  "v_mfma_f32_16x16x32_bf16 v[172:175], v[96:99], v[96:99], v[172:175]\n" \
  "s_waitcnt lgkmcnt(7)\n" \
  "ds_read_b128 v[96:99], v185 offset:0\n" \
  "v_mad_u32_u16 v187, v183, 16, v190\n" \
  "v_mfma_f32_16x16x32_bf16 v[172:175], v[100:103], v[100:103], v[172:175]\n" \
  "s_cbranch_scc1 LA_loop\n" \
  "s_cmp_ge_u32 s50, 6\n" \
  "s_cbranch_scc1 LA_ret\n" \
  "s_set_gpr_idx_on s50, gpr_idx(DST)\n" \
  "v_mov_b32 v196, v179\n" \
  "s_lshl_b32 s35, s50, 3\n" \
  "s_waitcnt vmcnt(0)\n" \
  "s_set_gpr_idx_on s35, gpr_idx(SRC0,SRC1)\n" \
  "v_cvt_pk_u16_u32 v210, v208, v209\n" \
  "v_cvt_pk_u16_u32 v211, v210, v211\n" \
  "v_cvt_pk_u16_u32 v212, v212, v213\n" \
  "v_cvt_pk_u16_u32 v213, v214, v215\n" \
  "s_set_gpr_idx_off\n" \
  "v_pk_lshrrev_b16 v214, 3, v210 op_sel_hi:[0,1]\n" \
  "v_pk_mad_u16 v210, v210, 8, v214 op_sel_hi:[1,0,1]\n" \
  "v_pk_lshrrev_b16 v215, 3, v211 op_sel_hi:[0,1]\n" \
  "v_pk_mad_u16 v211, v211, 8, v215 op_sel_hi:[1,0,1]\n" \
  "v_pk_lshrrev_b16 v214, 3, v212 op_sel_hi:[0,1]\n" \
  "v_pk_mad_u16 v212, v212, 8, v214 op_sel_hi:[1,0,1]\n" \
  "v_pk_lshrrev_b16 v215, 3, v213 op_sel_hi:[0,1]\n" \
  "v_pk_mad_u16 v213, v213, 8, v215 op_sel_hi:[1,0,1]\n" \
  "s_lshr_b32 s35, s38, 1\n" \
  "v_add_u32 v214, s35, v195\n" \
  "v_alignbit_b32 v212, v212, v212, v192\n" \
  "v_alignbit_b32 v213, v213, v213, v192\n" \
  "ds_write2_b32 v214, v210, v211 offset0:0 offset1:4\n" \
  "ds_write2_b32 v214, v212, v213 offset0:2 offset1:6\n" \
  "ds_read_b32 v209, v208 offset:4\n" \
  "v_subrev_u32 v194, s38, v194\n" \
  "s_xor_b32 s38, s38, 0x800\n" \
  "s_add_u32 s50, s50, 1\n" \
  "s_mov_b32 s51, 4\n" \
  "s_branch LA_loop\n" \
  "LA_ret:\n" \
  "s_setpc_b64 s[52:53]\n" \
  "LB_start:\n" \
  "s_setprio 2\n" \
  "s_lshl_b32 s35, s31, 7\n" \
  "s_waitcnt lgkmcnt(0)\n" \
  "s_add_u32 s40, s10, s35\n" \
  "s_addc_u32 s41, s11, 0\n" \
  "s_add_u32 s37, s36, 32\n" \
  "L3_:\n" \
  "s_sub_u32 s35, s36, 125\n" \
  "s_min_u32 s35, s35, s36\n" \
  "s_mul_i32 m0, s35, 1040\n" \
  "s_lshl_b32 s35, s35, 11\n" \
  "s_add_u32 s42, s40, s35\n" \
  "s_addc_u32 s43, s41, 0\n" \
  "global_load_lds_dwordx4 v254, s[42:43]\n" \
  "s_add_u32 s36, s36, 4\n" \
  "s_sub_u32 s35, s36, 125\n" \
  "s_min_u32 s35, s35, s36\n" \
  "s_mul_i32 m0, s35, 1040\n" \
  "s_lshl_b32 s35, s35, 11\n" \
  "s_add_u32 s42, s40, s35\n" \
  "s_addc_u32 s43, s41, 0\n" \
  "global_load_lds_dwordx4 v254, s[42:43]\n" \
  "s_add_u32 s36, s36, 4\n" \
  "s_cmp_lg_u32 s37, s36\n" \
  "s_cbranch_scc1 L3_\n" \
  "v_mov_b32 v190, 163328\n" \
  "v_mov_b64 v[2:3], 0\n" \
  "ds_write_b64 v190, v[2:3]\n" \
  "ds_write_b64 v190, v[2:3] offset:8\n" \
  "ds_write_b64 v190, v[2:3] offset:16\n" \
  "s_waitcnt lgkmcnt(0)\n" \
  "s_barrier\n" \
  "v_lshlrev_b32 v247, 7, v255\n" \
  "s_add_u32 s37, s36, 96\n" \
  "L4_:\n" \
  "s_sub_u32 s35, s36, 125\n" \
  "s_min_u32 s35, s35, s36\n" \
  "s_mul_i32 m0, s35, 1040\n" \
  "s_lshl_b32 s35, s35, 11\n" \
  "s_add_u32 s42, s40, s35\n" \
  "s_addc_u32 s43, s41, 0\n" \
  "global_load_lds_dwordx4 v254, s[42:43]\n" \
  "s_add_u32 s36, s36, 4\n" \
  "s_sub_u32 s35, s36, 125\n" \
  "s_min_u32 s35, s35, s36\n" \
  "s_mul_i32 m0, s35, 1040\n" \
  "s_lshl_b32 s35, s35, 11\n" \
  "s_add_u32 s42, s40, s35\n" \
  "s_addc_u32 s43, s41, 0\n" \
  "global_load_lds_dwordx4 v254, s[42:43]\n" \
  "s_add_u32 s36, s36, 4\n" \
  "s_cmp_lg_u32 s37, s36\n" \
  "s_cbranch_scc1 L4_\n" \
  "s_add_u32 s48, s22, 110002176\n" \
  "s_addc_u32 s49, s23, 0\n" \
  "s_lshl_b32 s35, s26, 2\n" \
  "v_mov_b32 v193, s35\n" \
  "global_load_dword v193, v193, s[48:49] offset:1024 sc1\n" \
  "v_mov_b32 v191, 1\n" \
  "v_and_b32 v252, 15, v255\n" \
  "v_lshrrev_b32 v253, 4, v255\n" \
  "s_lshl2_add_u32 s35, s31, s28\n" \
  "s_lshl_b32 s35, s35, 1\n" \
  "v_bfe_u32 v1, v255, 2, 1\n" \
  "v_add_u32 v1, s35, v1\n" \
  "v_and_b32 v2, 3, v255\n" \
  "v_lshlrev_b32 v3, 5, v1\n" \
  "v_lshl_add_u32 v3, v2, 3, v3\n" \
  "v_lshlrev_b32 v4, 2, v2\n" \
  "v_add_u32 v4, 380, v4\n" \
  "v_cmp_gt_u32 vcc, 12, v1\n" \
  "v_lshrrev_b32 v5, 3, v255\n" \
  "v_mul_u32_u24 v5, 6400, v5\n" \
  "v_cndmask_b32 v3, v4, v3, vcc\n" \
  "v_add_u32 v5, 3200, v5\n" \
  "v_add_u32 v227, v5, v3\n" \
  "v_lshl_add_u32 v3, v1, 2, v2\n" \
  "v_add_u32 v4, 48, v3\n" \
  "v_mul_u32_u24 v5, 205, v3\n" \
  "v_lshrrev_b32 v5, 11, v5\n" \
  "v_mul_u32_u24 v5, 10, v5\n" \
  "v_sub_u32 v5, v3, v5\n" \
  "v_lshlrev_b32 v236, 26, v5\n" \
  "v_mul_u32_u24 v5, 205, v4\n" \
  "v_lshrrev_b32 v5, 11, v5\n" \
  "v_mul_u32_u24 v5, 10, v5\n" \
  "v_sub_u32 v5, v4, v5\n" \
  "v_lshlrev_b32 v237, 26, v5\n" \
  "s_lshl2_add_u32 s4, s31, s28\n" \
  "s_lshl_b32 s4, s4, 7\n" \
  "s_add_u32 s36, s4, 96\n" \
  "s_min_u32 s36, s36, 984\n" \
  "s_add_u32 s37, s4, 112\n" \
  "s_min_u32 s37, s37, 984\n" \
  "v_add_u32 v186, s4, v252\n" \
  "v_add_u32 v187, s36, v252\n" \
  "v_add_u32 v188, s37, v252\n" \
  "s_mul_i32 s38, s7, 51200\n" \
  "s_add_u32 s42, s8, s38\n" \
  "s_addc_u32 s43, s9, 0\n" \
  "s_xor_b32 s38, s31, 1\n" \
  "s_lshl_b32 s38, s38, 7\n" \
  "s_add_u32 s94, s10, s38\n" \
  "s_addc_u32 s95, s11, 0\n" \
  "s_mul_i32 s5, s28, 4224\n" \
  "s_add_u32 s5, s5, 146432\n" \
  "v_lshrrev_b32 v2, 3, v186\n" \
  "v_lshl_add_u32 v2, v186, 3, v2\n" \
  "v_add_lshl_u32 v249, v2, v253, 4\n" \
  "v_lshrrev_b32 v2, 3, v187\n" \
  "v_lshl_add_u32 v2, v187, 3, v2\n" \
  "v_add_lshl_u32 v250, v2, v253, 4\n" \
  "v_lshrrev_b32 v2, 3, v188\n" \
  "v_lshl_add_u32 v2, v188, 3, v2\n" \
  "v_add_lshl_u32 v251, v2, v253, 4\n" \
  "v_mul_u32_u24 v1, 1600000, v253\n" \
  "v_lshl_add_u32 v186, v186, 4, v1\n" \
  "v_lshl_add_u32 v187, v187, 4, v1\n" \
  "v_lshl_add_u32 v188, v188, 4, v1\n" \
  "v_lshlrev_b32 v1, 4, v253\n" \
  "v_xor_b32 v244, v1, v254\n" \
  "v_lshl_add_u32 v245, v255, 4, s5\n" \
  "v_lshrrev_b32 v1, 1, v252\n" \
  "s_waitcnt vmcnt(1)\n" \
  "s_mov_b64 exec, 1\n" \
  "ds_add_u32 v190, v191 offset:12\n" \
  "s_mov_b64 exec, -1\n" \
  "s_memtime s[88:89]\n" \
  "v_xor_b32 v1, v1, v253\n" \
  "v_lshlrev_b32 v1, 4, v1\n" \
  "v_lshl_add_u32 v1, v252, 7, v1\n" \
  "v_add_u32 v246, s5, v1\n" \
  "v_xor_b32 v1, 64, v1\n" \
  "v_add_u32 v242, s5, v1\n" \
  "v_lshlrev_b32 v1, 4, v255\n" \
  "v_xor_b32 v1, 64, v1\n" \
  "v_add_u32 v243, s5, v1\n" \
  "v_lshrrev_b32 v1, 2, v255\n" \
  "s_lshl_b32 s35, s28, 5\n" \
  "s_lshl_b32 s36, s31, 6\n" \
  "s_add_u32 s35, s35, s36\n" \
  "s_and_b32 s35, s35, 127\n" \
  "v_add_u32 v1, s35, v1\n" \
  "v_lshrrev_b32 v2, 4, v255\n" \
  "v_and_b32 v2, 2, v2\n" \
  "v_xor_b32 v2, v2, v255\n" \
  "v_and_b32 v2, 3, v2\n" \
  "v_lshlrev_b32 v2, 4, v2\n" \
  "v_lshl_add_u32 v189, v1, 10, v2\n" \
  "v_add_u32 v194, 15360, v189\n" \
  "v_lshrrev_b32 v1, 2, v252\n" \
  "v_lshlrev_b32 v1, 6, v1\n" \
  "v_lshl_add_u32 v1, v253, 9, v1\n" \
  "v_and_b32 v2, 3, v252\n" \
  "v_lshl_add_u32 v1, v2, 3, v1\n" \
  "v_and_b32 v2, 1, v253\n" \
  "v_lshl_add_u32 v1, v2, 5, v1\n" \
  "v_add_u32 v184, 138240, v1\n" \
  "v_xor_b32 v1, 32, v1\n" \
  "v_add_u32 v185, 138240, v1\n" \
  "s_bfe_u32 s35, s30, 0x10001\n" \
  "s_lshl1_add_u32 s35, s29, s35\n" \
  "s_lshr_b32 s6, s30, 2\n" \
  "s_lshl_b32 s6, s6, 18\n" \
  "s_lshl_b32 s38, s35, 6\n" \
  "s_add_u32 s92, s14, s38\n" \
  "s_addc_u32 s93, s15, 0\n" \
  "s_lshr_b32 s37, s30, 3\n" \
  "s_add_u32 s37, s37, 96\n" \
  "s_lshl_b32 s34, s37, 17\n" \
  "s_mul_i32 s35, s35, 400\n" \
  "s_add_u32 s37, s37, s35\n" \
  "s_mul_i32 s37, s37, 16000\n" \
  "s_add_u32 s46, s22, s37\n" \
  "s_addc_u32 s47, s23, 0\n" \
  "s_lshr_b32 s36, s30, 2\n" \
  "s_add_u32 s36, s36, s35\n" \
  "s_mul_i32 s36, s36, 16000\n" \
  "s_add_u32 s44, s22, s36\n" \
  "s_addc_u32 s45, s23, 0\n" \
  "s_lshl_b32 s96, s28, 11\n" \
  "s_add_u32 s96, s96, 138240\n" \
  "v_lshlrev_b32 v238, 4, v255\n" \
  "v_mov_b32 v234, 0\n" \
  "s_lshl2_add_u32 s33, s26, s28\n" \
  "s_mov_b32 s52, 0x10002\n" \
  "s_mov_b32 s53, 0xc0c0400\n" \
  "s_mov_b32 s54, 0x4000c0c\n" \
  "s_mov_b32 s55, 0x3030303\n" \
  "s_mov_b32 s56, 0x55555555\n" \
  "s_mov_b32 s38, 0x0\n" \
  "s_mov_b32 s98, 0x0\n" \
  "s_mov_b32 s99, 0x8\n" \
  "s_mov_b32 s97, 0x2000\n" \
  "v_xor_b32 v245, 64, v244\n" \
  "v_add_u32 v245, 0x400, v245\n" \
  "s_setprio 0\n" \
  "L5_:\n" \
  "ds_read_b32 v192, v190 offset:16\n" \
  "s_waitcnt lgkmcnt(0)\n" \
  "v_readfirstlane_b32 s35, v192\n" \
  "s_cmp_lt_u32 s35, 4\n" \
  "s_cbranch_scc1 L5_\n" \
  "s_add_u32 s35, s4, 0\n" \
  "s_min_u32 s35, s35, 984\n" \
  "s_lshl_b32 s35, s35, 8\n" \
  "s_add_u32 m0, s5, 0\n" \
  "s_add_u32 s58, s94, s35\n" \
  "s_addc_u32 s59, s95, 0\n" \
  "global_load_lds_dwordx4 v244, s[58:59]\n" \
  "global_load_lds_dwordx4 v245, s[58:59] offset:1024\n" \
  "s_add_u32 s35, s4, 16\n" \
  "s_min_u32 s35, s35, 984\n" \
  "s_lshl_b32 s35, s35, 8\n" \
  "s_add_u32 m0, s5, 2048\n" \
  "s_add_u32 s58, s94, s35\n" \
  "s_addc_u32 s59, s95, 0\n" \
  "global_load_lds_dwordx4 v244, s[58:59]\n" \
  "global_load_lds_dwordx4 v245, s[58:59] offset:1024\n" \
  "L6_:\n" \
  "ds_read_b32 v192, v190 offset:12\n" \
  "s_waitcnt lgkmcnt(0)\n" \
  "v_readfirstlane_b32 s35, v192\n" \
  "s_cmp_lt_u32 s35, 4\n" \
  "s_cbranch_scc1 L6_\n" \
  "s_waitcnt vmcnt(0)\n" \
  "ds_read_b128 v[8:11], v246 offset:0\n" \
  "ds_read_b128 v[12:15], v242 offset:0\n" \
  "ds_read_b128 v[24:27], v246 offset:2048\n" \
  "ds_read_b128 v[28:31], v242 offset:2048\n" \
  "ds_read_b128 v[0:3], v249 offset:0\n" \
  "ds_read_b128 v[4:7], v249 offset:64\n" \
  "ds_read_b128 v[16:19], v249 offset:2080\n" \
  "ds_read_b128 v[20:23], v249 offset:2144\n" \
  "s_waitcnt lgkmcnt(4)\n" \
  "s_lshl2_add_u32 s35, s26, s28\n" \
  "s_mul_i32 s35, s35, 3200\n" \
  "s_add_u32 s35, s35, 0\n" \
  "s_add_u32 s58, s14, s35\n" \
  "s_addc_u32 s59, s15, 0\n" \
  "s_mov_b32 exec_lo, 0x1ffffff\n" \
  "s_mov_b32 exec_hi, 0x0\n" \
  "global_load_dword v248, v247, s[58:59]\n" \
  "s_mov_b64 exec, -1\n" \
  "s_add_u32 s35, s4, 32\n" \
  "s_min_u32 s35, s35, 984\n" \
  "s_lshl_b32 s35, s35, 8\n" \
  "s_add_u32 m0, s5, 0\n" \
  "s_add_u32 s58, s94, s35\n" \
  "s_addc_u32 s59, s95, 0\n" \
  "global_load_lds_dwordx4 v244, s[58:59]\n" \
  "global_load_lds_dwordx4 v245, s[58:59] offset:1024\n" \
  "s_add_u32 s35, s4, 48\n" \
  "s_min_u32 s35, s35, 984\n" \
  "s_lshl_b32 s35, s35, 8\n" \
  "s_add_u32 m0, s5, 2048\n" \
  "s_add_u32 s58, s94, s35\n" \
  "s_addc_u32 s59, s95, 0\n" \
  "global_load_lds_dwordx4 v244, s[58:59]\n" \
  "global_load_lds_dwordx4 v245, s[58:59] offset:1024\n" \
  "s_waitcnt vmcnt(0)\n" \
  "ds_read_b128 v[40:43], v246 offset:0\n" \
  "ds_read_b128 v[44:47], v242 offset:0\n" \
  "ds_read_b128 v[56:59], v246 offset:2048\n" \
  "ds_read_b128 v[60:63], v242 offset:2048\n" \
  "ds_read_b128 v[32:35], v249 offset:4160\n" \
  "ds_read_b128 v[36:39], v249 offset:4224\n" \
  "ds_read_b128 v[48:51], v249 offset:6240\n" \
  "ds_read_b128 v[52:55], v249 offset:6304\n" \
  "s_waitcnt lgkmcnt(4)\n" \
  "s_sleep 8\n" \
  "s_lshl2_add_u32 s35, s26, s28\n" \
  "s_mul_i32 s35, s35, 3200\n" \
  "s_add_u32 s35, s35, 3276800\n" \
  "s_add_u32 s58, s14, s35\n" \
  "s_addc_u32 s59, s15, 0\n" \
  "s_mov_b32 exec_lo, 0x1ffffff\n" \
  "s_mov_b32 exec_hi, 0x0\n" \
  "global_load_dword v248, v247, s[58:59]\n" \
  "s_mov_b64 exec, -1\n" \
  "s_add_u32 s35, s4, 64\n" \
  "s_min_u32 s35, s35, 984\n" \
  "s_lshl_b32 s35, s35, 8\n" \
  "s_add_u32 m0, s5, 0\n" \
  "s_add_u32 s58, s94, s35\n" \
  "s_addc_u32 s59, s95, 0\n" \
  "global_load_lds_dwordx4 v244, s[58:59]\n" \
  "global_load_lds_dwordx4 v245, s[58:59] offset:1024\n" \
  "s_add_u32 s35, s4, 80\n" \
  "s_min_u32 s35, s35, 984\n" \
  "s_lshl_b32 s35, s35, 8\n" \
  "s_add_u32 m0, s5, 2048\n" \
  "s_add_u32 s58, s94, s35\n" \
  "s_addc_u32 s59, s95, 0\n" \
  "global_load_lds_dwordx4 v244, s[58:59]\n" \
  "global_load_lds_dwordx4 v245, s[58:59] offset:1024\n" \
  "s_waitcnt vmcnt(0)\n" \
  "ds_read_b128 v[72:75], v246 offset:0\n" \
  "ds_read_b128 v[76:79], v242 offset:0\n" \
  "ds_read_b128 v[88:91], v246 offset:2048\n" \
  "ds_read_b128 v[92:95], v242 offset:2048\n" \
  "ds_read_b128 v[64:67], v249 offset:8320\n" \
  "ds_read_b128 v[68:71], v249 offset:8384\n" \
  "ds_read_b128 v[80:83], v249 offset:10400\n" \
  "ds_read_b128 v[84:87], v249 offset:10464\n" \
  "s_waitcnt lgkmcnt(4)\n" \
  "s_sleep 8\n" \
  "s_lshl2_add_u32 s35, s26, s28\n" \
  "s_mul_i32 s35, s35, 3200\n" \
  "s_add_u32 s35, s35, 6553600\n" \
  "s_add_u32 s58, s14, s35\n" \
  "s_addc_u32 s59, s15, 0\n" \
  "s_mov_b32 exec_lo, 0x1ffffff\n" \
  "s_mov_b32 exec_hi, 0x0\n" \
  "global_load_dword v248, v247, s[58:59]\n" \
  "s_mov_b64 exec, -1\n" \
  "s_add_u32 s35, s4, 96\n" \
  "s_min_u32 s35, s35, 984\n" \
  "s_lshl_b32 s35, s35, 8\n" \
  "s_add_u32 m0, s5, 0\n" \
  "s_add_u32 s58, s94, s35\n" \
  "s_addc_u32 s59, s95, 0\n" \
  "global_load_lds_dwordx4 v244, s[58:59]\n" \
  "global_load_lds_dwordx4 v245, s[58:59] offset:1024\n" \
  "s_add_u32 s35, s4, 112\n" \
  "s_min_u32 s35, s35, 984\n" \
  "s_lshl_b32 s35, s35, 8\n" \
  "s_add_u32 m0, s5, 2048\n" \
  "s_add_u32 s58, s94, s35\n" \
  "s_addc_u32 s59, s95, 0\n" \
  "global_load_lds_dwordx4 v244, s[58:59]\n" \
  "global_load_lds_dwordx4 v245, s[58:59] offset:1024\n" \
  "s_waitcnt vmcnt(0)\n" \
  "ds_read_b128 v[104:107], v246 offset:0\n" \
  "ds_read_b128 v[108:111], v242 offset:0\n" \
  "ds_read_b128 v[120:123], v246 offset:2048\n" \
  "ds_read_b128 v[124:127], v242 offset:2048\n" \
  "ds_read_b128 v[96:99], v250 offset:0\n" \
  "ds_read_b128 v[100:103], v250 offset:64\n" \
  "ds_read_b128 v[112:115], v251 offset:0\n" \
  "ds_read_b128 v[116:119], v251 offset:64\n" \
  "s_waitcnt lgkmcnt(4)\n" \
  "s_sleep 8\n" \
  "s_lshl2_add_u32 s35, s26, s28\n" \
  "s_mul_i32 s35, s35, 3200\n" \
  "s_add_u32 s35, s35, 9830400\n" \
  "s_add_u32 s58, s14, s35\n" \
  "s_addc_u32 s59, s15, 0\n" \
  "s_mov_b32 exec_lo, 0x1ffffff\n" \
  "s_mov_b32 exec_hi, 0x0\n" \
  "global_load_dword v248, v247, s[58:59]\n" \
  "s_mov_b64 exec, -1\n" \
  "s_add_u32 s40, s92, s6\n" \
  "s_addc_u32 s41, s93, 0\n" \
  "s_add_u32 s6, s6, 2097152\n" \
  "s_mov_b32 m0, s96\n" \
  "s_nop 0\n" \
  "global_load_lds_dwordx4 v189, s[40:41]\n" \
  "global_load_lds_dwordx4 v194, s[40:41] offset:1024\n" \
  "s_getpc_b64 s[58:59]\n" \
  "LB_pc:\n" \
  "s_sub_u32 s58, s58, LB_pc-LZ_first\n" \
  "s_subb_u32 s59, s59, 0\n" \
  "s_mul_i32 s35, s28, 7168\n" \
  "v_lshl_add_u32 v195, v255, 7, s35\n" \
  "v_min_u32 v195, LZ_end-LZ_first, v195\n" \
  "s_mov_b32 exec_hi, 0xffffff\n" \
  "global_load_dword v248, v195, s[58:59]\n" \
  "s_mov_b64 exec, -1\n" \
  "global_load_dwordx2 v[196:197], v227, s[42:43] offset:-3200\n" \
  "global_load_dwordx2 v[198:199], v227, s[42:43] offset:-2800\n" \
  "global_load_dwordx2 v[200:201], v227, s[42:43] offset:-2400\n" \
  "global_load_dwordx2 v[202:203], v227, s[42:43] offset:-1600\n" \
  "global_load_dwordx2 v[204:205], v227, s[42:43] offset:-1200\n" \
  "global_load_dwordx2 v[206:207], v227, s[42:43] offset:-800\n" \
  "global_load_dwordx2 v[208:209], v227, s[42:43] offset:-400\n" \
  "global_load_dwordx2 v[210:211], v227, s[42:43] offset:0\n" \
  "global_load_dwordx2 v[212:213], v227, s[42:43] offset:400\n" \
  "global_load_dwordx2 v[214:215], v227, s[42:43] offset:800\n" \
  "global_load_dwordx2 v[216:217], v227, s[42:43] offset:1200\n" \
  "global_load_dwordx2 v[218:219], v227, s[42:43] offset:-2000\n" \
  "global_load_dwordx2 v[220:221], v227, s[42:43] offset:1600\n" \
  "global_load_dwordx2 v[222:223], v227, s[42:43] offset:2000\n" \
  "global_load_dwordx2 v[224:225], v227, s[42:43] offset:2400\n" \
  "global_load_dwordx2 v[226:227], v227, s[42:43] offset:2800\n" \
  "s_setprio 2\n" \
  "s_mul_i32 s36, s28, 2176\n" \
  "s_add_u32 s36, s36, 154624\n" \
  "v_bfe_u32 v228, v255, 3, 1\n" \
  "v_and_b32 v229, 1, v255\n" \
  "v_lshl_add_u32 v230, v228, 1, v229\n" \
  "v_lshl_add_u32 v231, v229, 1, v228\n" \
  "v_xor_b32 v229, 1, v230\n" \
  "s_mov_b32 vcc_lo, 0xcccccccc\n" \
  "s_mov_b32 vcc_hi, vcc_lo\n" \
  "v_cndmask_b32 v228, v230, v231, vcc\n" \
  "v_cndmask_b32 v229, v229, v231, vcc\n" \
  "v_and_b32 v230, 4, v255\n" \
  "v_mul_u32_u24 v230, 272, v230\n" \
  "v_lshl_add_u32 v230, v253, 2, v230\n" \
  "v_and_or_b32 v230, v255, 2, v230\n" \
  "v_add_u32 v230, s36, v230\n" \
  "s_movk_i32 s35, 272\n" \
  "v_mad_u32_u24 v195, v228, s35, v230\n" \
  "v_mad_u32_u24 v235, v229, s35, v230\n" \
  "v_lshl_add_u32 v230, v253, 4, v238\n" \
  "v_add_u32 v254, s36, v230\n" \
  "v_readfirstlane_b32 s39, v193\n" \
  "s_lshl_b32 s35, s26, 2\n" \
  "s_add_u32 s39, s39, 1\n" \
  "v_mov_b32 v193, s35\n" \
  "v_mov_b64 v[240:241], -1\n" \
  "v_mov_b64 v[242:243], -1\n" \
  "s_waitcnt vmcnt(0)\n" \
  "s_waitcnt lgkmcnt(0)\n" \
  "s_mov_b64 exec, 1\n" \
  "ds_add_u32 v190, v191 offset:0\n" \
  "s_mov_b64 exec, -1\n" \
  "L7_:\n" \
  "s_sleep 2\n" \
  "ds_read_b32 v192, v190 offset:0\n" \
  "s_waitcnt lgkmcnt(0)\n" \
  "v_readfirstlane_b32 s35, v192\n" \
  "s_cmp_lt_u32 s35, 4\n" \
  "s_cbranch_scc1 L7_\n" \
  "s_add_u32 s96, s96, 8192\n" \
  "s_add_u32 s40, s92, s6\n" \
  "s_addc_u32 s41, s93, 0\n" \
  "s_add_u32 s6, s6, 2097152\n" \
  "s_mov_b32 m0, s96\n" \
  "s_nop 0\n" \
  "global_load_lds_dwordx4 v189, s[40:41]\n" \
  "global_load_lds_dwordx4 v194, s[40:41] offset:1024\n" \
  "s_sub_u32 s96, s96, 8192\n" \
  "ds_read_b64_tr_b16 v[128:129], v184 offset:0\n" \
  "ds_read_b64_tr_b16 v[130:131], v184 offset:256\n" \
  "ds_read_b64_tr_b16 v[132:133], v184 offset:2048\n" \
  "ds_read_b64_tr_b16 v[134:135], v184 offset:2304\n" \
  "ds_read_b64_tr_b16 v[136:137], v184 offset:4096\n" \
  "ds_read_b64_tr_b16 v[138:139], v184 offset:4352\n" \
  "ds_read_b64_tr_b16 v[140:141], v184 offset:6144\n" \
  "ds_read_b64_tr_b16 v[142:143], v184 offset:6400\n" \
  "ds_read_b64_tr_b16 v[156:157], v185 offset:6144\n" \
  "ds_read_b64_tr_b16 v[158:159], v185 offset:6400\n" \
  "ds_read_b64_tr_b16 v[152:153], v185 offset:4096\n" \
  "ds_read_b64_tr_b16 v[154:155], v185 offset:4352\n" \
  "ds_read_b64_tr_b16 v[148:149], v185 offset:2048\n" \
  "ds_read_b64_tr_b16 v[150:151], v185 offset:2304\n" \
  "ds_read_b64_tr_b16 v[144:145], v185 offset:0\n" \
  "ds_read_b64_tr_b16 v[146:147], v185 offset:256\n" \
  "v_mov_b32 v192, s39\n" \
  "s_cmp_lg_u32 s28, 0\n" \
  "s_cbranch_scc1 LB_nc\n" \
  "s_mov_b64 exec, 1\n" \
  "global_store_dword v193, v192, s[48:49] offset:0 sc1\n" \
  "s_mov_b64 exec, -1\n" \
  "LB_nc:\n" \
  "s_lshl2_add_u32 s35, s7, s28\n" \
  "s_lshl_b32 s35, s35, 7\n" \
  "s_lshl_b32 s36, s31, 16\n" \
  "s_add_u32 s35, s35, s36\n" \
  "s_add_u32 s35, s35, 109871104\n" \
  "s_add_u32 s58, s22, s35\n" \
  "s_addc_u32 s59, s23, 0\n" \
  "s_mov_b64 exec, 0xff\n" \
  "global_store_dwordx4 v238, v[240:243], s[58:59] sc1\n" \
  "s_mov_b64 exec, -1\n" \
  "s_cmp_lg_u32 s33, 1021\n" \
  "s_cbranch_scc1 LB_nm\n" \
  "v_mov_b64 v[240:241], 0\n" \
  "v_mov_b64 v[242:243], 0\n" \
  "global_store_dwordx4 v238, v[240:243], s[48:49] offset:2048 sc1\n" \
  "s_nop 1\n" \
  "v_lshlrev_b32 v242, 8, v255\n" \
  "global_store_dwordx2 v242, v[240:241], s[48:49] offset:3072 sc1\n" \
  "v_add_u32 v242, 0x4000, v242\n" \
  "global_store_dwordx2 v242, v[240:241], s[48:49] offset:3072 sc1\n" \
  "v_add_u32 v242, 0x4000, v242\n" \
  "global_store_dwordx2 v242, v[240:241], s[48:49] offset:3072 sc1\n" \
  "v_add_u32 v242, 0x4000, v242\n" \
  "global_store_dwordx2 v242, v[240:241], s[48:49] offset:3072 sc1\n" \
  "v_add_u32 v242, 0x4000, v242\n" \
  "LB_nm:\n" \
  "s_mov_b32 s86, 0x78000b0\n" \
  "s_mov_b32 s87, 0x0\n" \
  "s_or_b32 s86, s86, s87\n" \
  "s_setprio 2\n" \
  "s_memtime s[90:91]\n" \
  "s_memrealtime s[42:43]\n" \
  "LT:\n" \
  "s_cmp_eq_u32 s38, 6\n" \
  "s_cselect_b32 s78, 0x33333333, -1\n" \
  "s_cmp_eq_u32 s38, 7\n" \
  "s_cselect_b32 s78, 0x33333333, s78\n" \
  "s_cmp_eq_u32 s38, 18\n" \
  "s_cselect_b32 s80, 0x33333333, -1\n" \
  "s_cmp_eq_u32 s38, 19\n" \
  "s_cselect_b32 s80, 0x33333333, s80\n" \
  "s_mov_b32 s79, s78\n" \
  "s_mov_b32 s81, s80\n" \
  "s_mov_b32 s37, s38\n" \
  "s_add_u32 s57, s38, 2\n" \
  "s_add_u32 s3, s38, 4\n" \
  "s_add_u32 s38, s38, 6\n" \
  "s_cmp_lt_u32 s57, 32\n" \
  "s_cselect_b32 s57, s57, 37\n" \
  "s_cselect_b32 s3, s3, 37\n" \
  "s_waitcnt lgkmcnt(12)\n" \
  "v_mfma_f32_16x16x32_bf16 v[160:163], v[128:131], v[0:3], 0\n" \
  "s_mov_b64 vcc, s[78:79]\n" \
  "v_cndmask_b32_dpp v234, v234, v234, vcc row_ror:8 row_mask:0xf bank_mask:0xf\n" \
  "v_mfma_f32_16x16x32_bf16 v[160:163], v[132:135], v[4:7], v[160:163]\n" \
  "s_waitcnt lgkmcnt(8)\n" \
  "v_mfma_f32_16x16x32_bf16 v[160:163], v[136:139], v[8:11], v[160:163]\n" \
  "s_set_gpr_idx_on s37, gpr_idx(SRC0)\n" \
  "v_mad_u32_u24 v232, v196, s52, v236\n" \
  "s_set_gpr_idx_off\n" \
  "v_mfma_f32_16x16x32_bf16 v[160:163], v[140:143], v[12:15], v[160:163]\n" \
  "s_waitcnt lgkmcnt(0)\n" \
  "v_lshrrev_b32 v228, v232, v234\n" \
  "v_lshrrev_b32_dpp v229, v232, v234 quad_perm:[2,3,0,1] row_mask:0xf bank_mask:0xf\n" \
  "v_mfma_f32_16x16x32_bf16 v[164:167], v[156:159], v[12:15], 0\n" \
  "v_lshrrev_b32_dpp v230, v232, v234 quad_perm:[1,2,3,0] row_mask:0xf bank_mask:0xf\n" \
  "v_lshrrev_b32_dpp v231, v232, v234 quad_perm:[3,0,1,2] row_mask:0xf bank_mask:0xf\n" \
  "v_mfma_f32_16x16x32_bf16 v[164:167], v[152:155], v[8:11], v[164:167]\n" \
  "v_perm_b32 v228, v229, v228, s53\n" \
  "v_perm_b32 v230, v231, v230, s54\n" \
  "v_mfma_f32_16x16x32_bf16 v[164:167], v[148:151], v[4:7], v[164:167]\n" \
  "v_bitop3_b32 v228, v228, v230, s55 bitop3:0xa8\n" \
  "v_lshl_or_b32 v229, v234, 1, s56\n" \
  "v_mfma_f32_16x16x32_bf16 v[164:167], v[144:147], v[0:3], v[164:167]\n" \
  "v_add_u32_dpp v228, v228, v228 quad_perm:[2,3,0,1] row_mask:0xf bank_mask:0xf\n" \
  "v_min_u32_sdwa v230, v228, v228 dst_sel:DWORD dst_unused:UNUSED_PAD src0_sel:BYTE_0 src1_sel:BYTE_1\n" \
  "v_mfma_f32_16x16x32_bf16 v[172:175], v[144:147], v[16:19], 0\n" \
  "v_min_u32_sdwa v231, v228, v228 dst_sel:DWORD dst_unused:UNUSED_PAD src0_sel:BYTE_2 src1_sel:BYTE_3\n" \
  "v_sub_u32 v230, v230, v231\n" \
  "v_mfma_f32_16x16x32_bf16 v[172:175], v[148:151], v[20:23], v[172:175]\n" \
  "v_cvt_pk_bf16_f32 v176, v160, v161\n" \
  "v_add_u32_dpp v230, v230, v230 quad_perm:[1,0,3,2] row_mask:0xf bank_mask:0xf\n" \
  "v_mfma_f32_16x16x32_bf16 v[172:175], v[152:155], v[24:27], v[172:175]\n" \
  "v_cmp_ge_i32 vcc, 0, v230\n" \
  "v_cvt_pk_bf16_f32 v177, v162, v163\n" \
  "v_mfma_f32_16x16x32_bf16 v[172:175], v[156:159], v[28:31], v[172:175]\n" \
  "v_cndmask_b32_sdwa v228, v228, v228, vcc dst_sel:DWORD dst_unused:UNUSED_PAD src0_sel:WORD_1 src1_sel:WORD_0\n" \
  "v_cvt_pk_bf16_f32 v178, v164, v165\n" \
  "v_mfma_f32_16x16x32_bf16 v[168:171], v[140:143], v[28:31], 0\n" \
  "s_mov_b64 s[50:51], vcc\n" \
  "v_cmp_le_u32_sdwa vcc, v228, v228 src0_sel:BYTE_0 src1_sel:BYTE_1\n" \
  "v_cvt_pk_bf16_f32 v179, v166, v167\n" \
  "v_mfma_f32_16x16x32_bf16 v[168:171], v[136:139], v[24:27], v[168:171]\n" \
  "v_cndmask_b32_dpp v232, v232, v232, vcc quad_perm:[2,3,0,1] row_mask:0xf bank_mask:0xf\n" \
  "v_mfma_f32_16x16x32_bf16 v[168:171], v[132:135], v[20:23], v[168:171]\n" \
  "s_mov_b64 vcc, s[50:51]\n" \
  "v_cndmask_b32_dpp v232, v232, v232, vcc quad_perm:[3,0,1,2] row_mask:0xf bank_mask:0xf\n" \
  "v_lshlrev_b32_e64 v230, v232, 3\n" \
  "v_mfma_f32_16x16x32_bf16 v[168:171], v[128:131], v[16:19], v[168:171]\n" \
  "v_and_or_b32 v234, v229, v230, v234\n" \
  "s_cmp_lg_u32 s33, 1021\n" \
  "s_cbranch_scc0 LT_e\n" \
  "LT_r:\n" \
  "s_waitcnt vmcnt(5)\n" \
  "global_store_dwordx4 v186, v[176:179], s[44:45] offset:0 sc1\n" \
  "v_mfma_f32_16x16x32_bf16 v[160:163], v[128:131], v[32:35], 0\n" \
  "s_set_gpr_idx_on s37, gpr_idx(DST)\n" \
  "v_mov_b32 v196, v232\n" \
  "s_set_gpr_idx_off\n" \
  "v_mfma_f32_16x16x32_bf16 v[160:163], v[132:135], v[36:39], v[160:163]\n" \
  "s_set_gpr_idx_on s57, gpr_idx(SRC0)\n" \
  "v_mad_u32_u24 v232, v196, s52, v236\n" \
  "s_set_gpr_idx_off\n" \
  "v_mfma_f32_16x16x32_bf16 v[160:163], v[136:139], v[40:43], v[160:163]\n" \
  "v_lshrrev_b32 v228, v232, v234\n" \
  "v_lshrrev_b32_dpp v229, v232, v234 quad_perm:[2,3,0,1] row_mask:0xf bank_mask:0xf\n" \
  "v_mfma_f32_16x16x32_bf16 v[160:163], v[140:143], v[44:47], v[160:163]\n" \
  "v_lshrrev_b32_dpp v230, v232, v234 quad_perm:[1,2,3,0] row_mask:0xf bank_mask:0xf\n" \
  "v_lshrrev_b32_dpp v231, v232, v234 quad_perm:[3,0,1,2] row_mask:0xf bank_mask:0xf\n" \
  "v_mfma_f32_16x16x32_bf16 v[164:167], v[156:159], v[44:47], 0\n" \
  "v_perm_b32 v228, v229, v228, s53\n" \
  "v_perm_b32 v230, v231, v230, s54\n" \
  "v_mfma_f32_16x16x32_bf16 v[164:167], v[152:155], v[40:43], v[164:167]\n" \
  "v_bitop3_b32 v228, v228, v230, s55 bitop3:0xa8\n" \
  "v_lshl_or_b32 v229, v234, 1, s56\n" \
  "v_mfma_f32_16x16x32_bf16 v[164:167], v[148:151], v[36:39], v[164:167]\n" \
  "v_cvt_pk_bf16_f32 v182, v172, v173\n" \
  "v_cvt_pk_bf16_f32 v183, v174, v175\n" \
  "v_mfma_f32_16x16x32_bf16 v[164:167], v[144:147], v[32:35], v[164:167]\n" \
  "v_cvt_pk_bf16_f32 v180, v168, v169\n" \
  "v_cvt_pk_bf16_f32 v181, v170, v171\n" \
  "s_waitcnt vmcnt(5)\n" \
  "global_store_dwordx4 v186, v[180:183], s[44:45] offset:256 sc1\n" \
  "v_mfma_f32_16x16x32_bf16 v[172:175], v[144:147], v[48:51], 0\n" \
  "v_add_u32_dpp v228, v228, v228 quad_perm:[2,3,0,1] row_mask:0xf bank_mask:0xf\n" \
  "v_min_u32_sdwa v230, v228, v228 dst_sel:DWORD dst_unused:UNUSED_PAD src0_sel:BYTE_0 src1_sel:BYTE_1\n" \
  "v_mfma_f32_16x16x32_bf16 v[172:175], v[148:151], v[52:55], v[172:175]\n" \
  "v_min_u32_sdwa v231, v228, v228 dst_sel:DWORD dst_unused:UNUSED_PAD src0_sel:BYTE_2 src1_sel:BYTE_3\n" \
  "v_sub_u32 v230, v230, v231\n" \
  "v_mfma_f32_16x16x32_bf16 v[172:175], v[152:155], v[56:59], v[172:175]\n" \
  "v_cvt_pk_bf16_f32 v176, v160, v161\n" \
  "v_add_u32_dpp v230, v230, v230 quad_perm:[1,0,3,2] row_mask:0xf bank_mask:0xf\n" \
  "v_mfma_f32_16x16x32_bf16 v[172:175], v[156:159], v[60:63], v[172:175]\n" \
  "v_cmp_ge_i32 vcc, 0, v230\n" \
  "v_cvt_pk_bf16_f32 v177, v162, v163\n" \
  "v_mfma_f32_16x16x32_bf16 v[168:171], v[140:143], v[60:63], 0\n" \
  "v_cndmask_b32_sdwa v228, v228, v228, vcc dst_sel:DWORD dst_unused:UNUSED_PAD src0_sel:WORD_1 src1_sel:WORD_0\n" \
  "v_cvt_pk_bf16_f32 v178, v164, v165\n" \
  "v_mfma_f32_16x16x32_bf16 v[168:171], v[136:139], v[56:59], v[168:171]\n" \
  "s_mov_b64 s[50:51], vcc\n" \
  "v_cmp_le_u32_sdwa vcc, v228, v228 src0_sel:BYTE_0 src1_sel:BYTE_1\n" \
  "v_cvt_pk_bf16_f32 v179, v166, v167\n" \
  "v_mfma_f32_16x16x32_bf16 v[168:171], v[132:135], v[52:55], v[168:171]\n" \
  "v_cndmask_b32_dpp v232, v232, v232, vcc quad_perm:[2,3,0,1] row_mask:0xf bank_mask:0xf\n" \
  "v_mfma_f32_16x16x32_bf16 v[168:171], v[128:131], v[48:51], v[168:171]\n" \
  "s_mov_b64 vcc, s[50:51]\n" \
  "v_cndmask_b32_dpp v232, v232, v232, vcc quad_perm:[3,0,1,2] row_mask:0xf bank_mask:0xf\n" \
  "v_lshlrev_b32_e64 v230, v232, 3\n" \
  "s_waitcnt vmcnt(5)\n" \
  "global_store_dwordx4 v186, v[176:179], s[44:45] offset:512 sc1\n" \
  "v_mfma_f32_16x16x32_bf16 v[160:163], v[128:131], v[64:67], 0\n" \
  "v_and_or_b32 v234, v229, v230, v234\n" \
  "v_cvt_pk_bf16_f32 v182, v172, v173\n" \
  "v_mfma_f32_16x16x32_bf16 v[160:163], v[132:135], v[68:71], v[160:163]\n" \
  "s_set_gpr_idx_on s57, gpr_idx(DST)\n" \
  "v_mov_b32 v196, v232\n" \
  "s_set_gpr_idx_off\n" \
  "v_mfma_f32_16x16x32_bf16 v[160:163], v[136:139], v[72:75], v[160:163]\n" \
  "s_mov_b64 vcc, s[80:81]\n" \
  "v_cndmask_b32_dpp v234, v234, v234, vcc row_ror:8 row_mask:0xf bank_mask:0xf\n" \
  "v_cvt_pk_bf16_f32 v183, v174, v175\n" \
  "v_mfma_f32_16x16x32_bf16 v[160:163], v[140:143], v[76:79], v[160:163]\n" \
  "s_set_gpr_idx_on s3, gpr_idx(SRC0)\n" \
  "v_mad_u32_u24 v232, v196, s52, v236\n" \
  "s_set_gpr_idx_off\n" \
  "v_mfma_f32_16x16x32_bf16 v[164:167], v[156:159], v[76:79], 0\n" \
  "v_lshrrev_b32 v228, v232, v234\n" \
  "v_lshrrev_b32_dpp v229, v232, v234 quad_perm:[2,3,0,1] row_mask:0xf bank_mask:0xf\n" \
  "v_mfma_f32_16x16x32_bf16 v[164:167], v[152:155], v[72:75], v[164:167]\n" \
  "v_lshrrev_b32_dpp v230, v232, v234 quad_perm:[1,2,3,0] row_mask:0xf bank_mask:0xf\n" \
  "v_lshrrev_b32_dpp v231, v232, v234 quad_perm:[3,0,1,2] row_mask:0xf bank_mask:0xf\n" \
  "v_mfma_f32_16x16x32_bf16 v[164:167], v[148:151], v[68:71], v[164:167]\n" \
  "v_perm_b32 v228, v229, v228, s53\n" \
  "v_perm_b32 v230, v231, v230, s54\n" \
  "v_mfma_f32_16x16x32_bf16 v[164:167], v[144:147], v[64:67], v[164:167]\n" \
  "v_cvt_pk_bf16_f32 v180, v168, v169\n" \
  "v_cvt_pk_bf16_f32 v181, v170, v171\n" \
  "s_waitcnt vmcnt(5)\n" \
  "global_store_dwordx4 v186, v[180:183], s[44:45] offset:768 sc1\n" \
  "v_mfma_f32_16x16x32_bf16 v[172:175], v[144:147], v[80:83], 0\n" \
  "s_waitcnt vmcnt(5)\n" \
  "s_mov_b64 exec, 1\n" \
  "ds_add_u32 v190, v191 offset:0\n" \
  "s_mov_b64 exec, -1\n" \
  "v_mfma_f32_16x16x32_bf16 v[172:175], v[148:151], v[84:87], v[172:175]\n" \
  "v_bitop3_b32 v228, v228, v230, s55 bitop3:0xa8\n" \
  "v_lshl_or_b32 v229, v234, 1, s56\n" \
  "v_mfma_f32_16x16x32_bf16 v[172:175], v[152:155], v[88:91], v[172:175]\n" \
  "v_add_u32_dpp v228, v228, v228 quad_perm:[2,3,0,1] row_mask:0xf bank_mask:0xf\n" \
  "v_min_u32_sdwa v230, v228, v228 dst_sel:DWORD dst_unused:UNUSED_PAD src0_sel:BYTE_0 src1_sel:BYTE_1\n" \
  "v_mfma_f32_16x16x32_bf16 v[172:175], v[156:159], v[92:95], v[172:175]\n" \
  "v_min_u32_sdwa v231, v228, v228 dst_sel:DWORD dst_unused:UNUSED_PAD src0_sel:BYTE_2 src1_sel:BYTE_3\n" \
  "v_sub_u32 v230, v230, v231\n" \
  "v_mfma_f32_16x16x32_bf16 v[168:171], v[140:143], v[92:95], 0\n" \
  "v_cvt_pk_bf16_f32 v176, v160, v161\n" \
  "v_add_u32_dpp v230, v230, v230 quad_perm:[1,0,3,2] row_mask:0xf bank_mask:0xf\n" \
  "v_mfma_f32_16x16x32_bf16 v[168:171], v[136:139], v[88:91], v[168:171]\n" \
  "v_cmp_ge_i32 vcc, 0, v230\n" \
  "v_cvt_pk_bf16_f32 v177, v162, v163\n" \
  "v_mfma_f32_16x16x32_bf16 v[168:171], v[132:135], v[84:87], v[168:171]\n" \
  "v_cndmask_b32_sdwa v228, v228, v228, vcc dst_sel:DWORD dst_unused:UNUSED_PAD src0_sel:WORD_1 src1_sel:WORD_0\n" \
  "v_cvt_pk_bf16_f32 v178, v164, v165\n" \
  "v_mfma_f32_16x16x32_bf16 v[168:171], v[128:131], v[80:83], v[168:171]\n" \
  "s_mov_b64 s[50:51], vcc\n" \
  "v_cmp_le_u32_sdwa vcc, v228, v228 src0_sel:BYTE_0 src1_sel:BYTE_1\n" \
  "v_cvt_pk_bf16_f32 v179, v166, v167\n" \
  "s_cmp_eq_u32 s98, 4\n" \
  "s_cselect_b32 s36, 12451840, 0\n" \
  "s_sub_u32 s6, s6, s36\n" \
  "s_cmp_lt_u32 s98, 10\n" \
  "s_cselect_b32 s6, s6, s34\n" \
  "s_add_u32 s40, s92, s6\n" \
  "s_addc_u32 s41, s93, 0\n" \
  "s_add_u32 s6, s6, 2097152\n" \
  "s_waitcnt vmcnt(5)\n" \
  "global_store_dwordx4 v186, v[176:179], s[44:45] offset:1024 sc1\n" \
  "v_mfma_f32_16x16x32_bf16 v[160:163], v[128:131], v[96:99], 0\n" \
  "ds_read_b32 v192, v190\n" \
  "v_add_u32 v184, s97, v184\n" \
  "v_mfma_f32_16x16x32_bf16 v[160:163], v[132:135], v[100:103], v[160:163]\n" \
  "v_add_u32 v185, s97, v185\n" \
  "v_cndmask_b32_dpp v232, v232, v232, vcc quad_perm:[2,3,0,1] row_mask:0xf bank_mask:0xf\n" \
  "v_mfma_f32_16x16x32_bf16 v[160:163], v[136:139], v[104:107], v[160:163]\n" \
  "s_mov_b64 vcc, s[50:51]\n" \
  "v_cndmask_b32_dpp v232, v232, v232, vcc quad_perm:[3,0,1,2] row_mask:0xf bank_mask:0xf\n" \
  "v_lshlrev_b32_e64 v230, v232, 3\n" \
  "v_mfma_f32_16x16x32_bf16 v[160:163], v[140:143], v[108:111], v[160:163]\n" \
  "v_and_or_b32 v234, v229, v230, v234\n" \
  "v_cvt_pk_bf16_f32 v182, v172, v173\n" \
  "v_mfma_f32_16x16x32_bf16 v[164:167], v[156:159], v[108:111], 0\n" \
  "s_set_gpr_idx_on s3, gpr_idx(DST)\n" \
  "v_mov_b32 v196, v232\n" \
  "s_set_gpr_idx_off\n" \
  "v_mfma_f32_16x16x32_bf16 v[164:167], v[152:155], v[104:107], v[164:167]\n" \
  "s_waitcnt lgkmcnt(0)\n" \
  "v_readfirstlane_b32 s35, v192\n" \
  "v_cvt_pk_bf16_f32 v183, v174, v175\n" \
  "v_mfma_f32_16x16x32_bf16 v[164:167], v[148:151], v[100:103], v[164:167]\n" \
  "v_cvt_pk_bf16_f32 v180, v168, v169\n" \
  "v_cvt_pk_bf16_f32 v181, v170, v171\n" \
  "v_mfma_f32_16x16x32_bf16 v[164:167], v[144:147], v[96:99], v[164:167]\n" \
  "s_cmp_lt_u32 s35, s99\n" \
  "s_cbranch_scc1 LT_w\n" \
  "LT_c:\n" \
  "s_mov_b32 m0, s96\n" \
  "s_nop 0\n" \
  "global_load_lds_dwordx4 v189, s[40:41]\n" \
  "global_load_lds_dwordx4 v194, s[40:41] offset:1024\n" \
  "s_add_u32 s96, s96, s97\n" \
  "s_sub_u32 s97, 0, s97\n" \
  "s_add_u32 s99, s99, 4\n" \
  "s_lshl4_add_u32 s35, s28, s98\n" \
  "s_bitcmp1_b64 s[86:87], s35\n" \
  "s_cbranch_scc1 LT_a\n" \
  "LT_s:\n" \
  "s_waitcnt vmcnt(5)\n" \
  "global_store_dwordx4 v186, v[180:183], s[44:45] offset:1280 sc1\n" \
  "v_mfma_f32_16x16x32_bf16 v[168:171], v[128:131], v[112:115], 0\n" \
  "ds_read_b64_tr_b16 v[128:129], v184 offset:0\n" \
  "ds_read_b64_tr_b16 v[130:131], v184 offset:256\n" \
  "v_mfma_f32_16x16x32_bf16 v[168:171], v[132:135], v[116:119], v[168:171]\n" \
  "ds_read_b64_tr_b16 v[132:133], v184 offset:2048\n" \
  "ds_read_b64_tr_b16 v[134:135], v184 offset:2304\n" \
  "v_mfma_f32_16x16x32_bf16 v[168:171], v[136:139], v[120:123], v[168:171]\n" \
  "ds_read_b64_tr_b16 v[136:137], v184 offset:4096\n" \
  "v_cvt_pk_bf16_f32 v176, v160, v161\n" \
  "v_mfma_f32_16x16x32_bf16 v[168:171], v[140:143], v[124:127], v[168:171]\n" \
  "ds_read_b64_tr_b16 v[138:139], v184 offset:4352\n" \
  "v_cvt_pk_bf16_f32 v177, v162, v163\n" \
  "v_mfma_f32_16x16x32_bf16 v[172:175], v[156:159], v[124:127], 0\n" \
  "ds_read_b64_tr_b16 v[140:141], v184 offset:6144\n" \
  "v_cvt_pk_bf16_f32 v178, v164, v165\n" \
  "v_mfma_f32_16x16x32_bf16 v[172:175], v[152:155], v[120:123], v[172:175]\n" \
  "ds_read_b64_tr_b16 v[142:143], v184 offset:6400\n" \
  "v_cvt_pk_bf16_f32 v179, v166, v167\n" \
  "v_mfma_f32_16x16x32_bf16 v[172:175], v[148:151], v[116:119], v[172:175]\n" \
  "ds_read_b64_tr_b16 v[156:157], v185 offset:6144\n" \
  "ds_read_b64_tr_b16 v[158:159], v185 offset:6400\n" \
  "v_mfma_f32_16x16x32_bf16 v[172:175], v[144:147], v[112:115], v[172:175]\n" \
  "ds_read_b64_tr_b16 v[152:153], v185 offset:4096\n" \
  "ds_read_b64_tr_b16 v[154:155], v185 offset:4352\n" \
  "ds_read_b64_tr_b16 v[148:149], v185 offset:2048\n" \
  "ds_read_b64_tr_b16 v[150:151], v185 offset:2304\n" \
  "ds_read_b64_tr_b16 v[144:145], v185 offset:0\n" \
  "ds_read_b64_tr_b16 v[146:147], v185 offset:256\n" \
  "s_waitcnt vmcnt(5)\n" \
  "global_store_dwordx4 v187, v[176:179], s[44:45] offset:0 sc1\n" \
  "s_add_u32 s98, s98, 1\n" \
  "v_cvt_pk_bf16_f32 v180, v168, v169\n" \
  "v_cvt_pk_bf16_f32 v181, v170, v171\n" \
  "v_cvt_pk_bf16_f32 v182, v172, v173\n" \
  "v_cvt_pk_bf16_f32 v183, v174, v175\n" \
  "s_waitcnt vmcnt(5)\n" \
  "global_store_dwordx4 v188, v[180:183], s[44:45] offset:0 sc1\n" \
  "s_add_u32 s44, s44, 128000\n" \
  "s_addc_u32 s45, s45, 0\n" \
  "s_bitcmp0_b32 4160, s98\n" \
  "s_cbranch_scc1 LT\n" \
  "s_cmp_eq_u32 s98, 6\n" \
  "s_cbranch_scc1 LT_m\n" \
  "s_lshr_b32 s35, s30, 2\n" \
  "s_lshl3_add_u32 s35, s29, s35\n" \
  "s_mul_i32 s35, s35, 1600000\n" \
  "s_lshl_b32 s36, s28, 12\n" \
  "s_add_u32 s35, s35, s36\n" \
  "s_add_u32 s40, s22, s35\n" \
  "s_addc_u32 s41, s23, 0\n" \
  "s_bitcmp1_b32 s30, 2\n" \
  "s_cbranch_scc1 LB_l_h\n" \
  "global_load_dwordx4 v[64:67], v238, s[40:41] offset:0 sc1\n" \
  "global_load_dwordx4 v[68:71], v238, s[40:41] offset:1024 sc1\n" \
  "global_load_dwordx4 v[72:75], v238, s[40:41] offset:2048 sc1\n" \
  "global_load_dwordx4 v[76:79], v238, s[40:41] offset:3072 sc1\n" \
  "s_add_u32 s40, s40, 16000\n" \
  "s_addc_u32 s41, s41, 0\n" \
  "global_load_dwordx4 v[80:83], v238, s[40:41] offset:0 sc1\n" \
  "global_load_dwordx4 v[84:87], v238, s[40:41] offset:1024 sc1\n" \
  "global_load_dwordx4 v[88:91], v238, s[40:41] offset:2048 sc1\n" \
  "global_load_dwordx4 v[92:95], v238, s[40:41] offset:3072 sc1\n" \
  "s_add_u32 s40, s40, 16000\n" \
  "s_addc_u32 s41, s41, 0\n" \
  "global_load_dwordx4 v[96:99], v238, s[40:41] offset:0 sc1\n" \
  "global_load_dwordx4 v[100:103], v238, s[40:41] offset:1024 sc1\n" \
  "global_load_dwordx4 v[104:107], v238, s[40:41] offset:2048 sc1\n" \
  "global_load_dwordx4 v[108:111], v238, s[40:41] offset:3072 sc1\n" \
  "s_add_u32 s40, s40, 16000\n" \
  "s_addc_u32 s41, s41, 0\n" \
  "global_load_dwordx4 v[112:115], v238, s[40:41] offset:0 sc1\n" \
  "global_load_dwordx4 v[116:119], v238, s[40:41] offset:1024 sc1\n" \
  "global_load_dwordx4 v[120:123], v238, s[40:41] offset:2048 sc1\n" \
  "global_load_dwordx4 v[124:127], v238, s[40:41] offset:3072 sc1\n" \
  "s_add_u32 s40, s40, 16000\n" \
  "s_addc_u32 s41, s41, 0\n" \
  "s_branch LB_l_e\n" \
  "LB_l_h:\n" \
  "global_load_dwordx4 v[0:3], v238, s[40:41] offset:0 sc1\n" \
  "global_load_dwordx4 v[4:7], v238, s[40:41] offset:1024 sc1\n" \
  "global_load_dwordx4 v[8:11], v238, s[40:41] offset:2048 sc1\n" \
  "global_load_dwordx4 v[12:15], v238, s[40:41] offset:3072 sc1\n" \
  "s_add_u32 s40, s40, 16000\n" \
  "s_addc_u32 s41, s41, 0\n" \
  "global_load_dwordx4 v[16:19], v238, s[40:41] offset:0 sc1\n" \
  "global_load_dwordx4 v[20:23], v238, s[40:41] offset:1024 sc1\n" \
  "global_load_dwordx4 v[24:27], v238, s[40:41] offset:2048 sc1\n" \
  "global_load_dwordx4 v[28:31], v238, s[40:41] offset:3072 sc1\n" \
  "s_add_u32 s40, s40, 16000\n" \
  "s_addc_u32 s41, s41, 0\n" \
  "global_load_dwordx4 v[32:35], v238, s[40:41] offset:0 sc1\n" \
  "global_load_dwordx4 v[36:39], v238, s[40:41] offset:1024 sc1\n" \
  "global_load_dwordx4 v[40:43], v238, s[40:41] offset:2048 sc1\n" \
  "global_load_dwordx4 v[44:47], v238, s[40:41] offset:3072 sc1\n" \
  "s_add_u32 s40, s40, 16000\n" \
  "s_addc_u32 s41, s41, 0\n" \
  "global_load_dwordx4 v[48:51], v238, s[40:41] offset:0 sc1\n" \
  "global_load_dwordx4 v[52:55], v238, s[40:41] offset:1024 sc1\n" \
  "global_load_dwordx4 v[56:59], v238, s[40:41] offset:2048 sc1\n" \
  "global_load_dwordx4 v[60:63], v238, s[40:41] offset:3072 sc1\n" \
  "s_add_u32 s40, s40, 16000\n" \
  "s_addc_u32 s41, s41, 0\n" \
  "LB_l_e:\n" \
  "ds_write_b16_d16_hi v195, v197 offset:0\n" \
  "ds_write_b16_d16_hi v195, v199 offset:16\n" \
  "ds_write_b16_d16_hi v195, v201 offset:32\n" \
  "ds_write_b16_d16_hi v235, v203 offset:64\n" \
  "ds_write_b16_d16_hi v235, v205 offset:80\n" \
  "ds_write_b16_d16_hi v235, v207 offset:96\n" \
  "ds_write_b16_d16_hi v235, v209 offset:112\n" \
  "ds_write_b16_d16_hi v235, v211 offset:128\n" \
  "ds_write_b16_d16_hi v235, v213 offset:144\n" \
  "ds_write_b16_d16_hi v235, v215 offset:160\n" \
  "ds_write_b16_d16_hi v235, v217 offset:176\n" \
  "ds_write_b16_d16_hi v195, v219 offset:48\n" \
  "ds_write_b16_d16_hi v195, v221 offset:192\n" \
  "ds_write_b16_d16_hi v195, v223 offset:208\n" \
  "ds_write_b16_d16_hi v195, v225 offset:224\n" \
  "ds_write_b16_d16_hi v195, v227 offset:240\n" \
  "ds_read_b128 v[244:247], v254 offset:0\n" \
  "ds_read_b128 v[248:251], v254 offset:1088\n" \
  "s_lshr_b32 s35, s7, 5\n" \
  "s_mul_i32 s35, s35, 819200\n" \
  "s_and_b32 s36, s7, 31\n" \
  "s_lshl_b32 s36, s36, 10\n" \
  "s_add_u32 s35, s35, s36\n" \
  "s_lshl_b32 s36, s4, 9\n" \
  "s_add_u32 s35, s35, s36\n" \
  "s_add_u32 s35, s35, 102793216\n" \
  "s_add_u32 s58, s22, s35\n" \
  "s_addc_u32 s59, s23, 0\n" \
  "s_waitcnt lgkmcnt(0)\n" \
  "s_cmp_ge_u32 s4, 896\n" \
  "s_cbranch_scc1 L17_\n" \
  "global_store_dwordx4 v238, v[244:247], s[58:59] sc1\n" \
  "s_cmp_ge_u32 s4, 768\n" \
  "s_cbranch_scc1 L17_\n" \
  "s_add_u32 s58, s58, 32768\n" \
  "s_addc_u32 s59, s59, 0\n" \
  "global_store_dwordx4 v238, v[248:251], s[58:59] sc1\n" \
  "L17_:\n" \
  "s_lshr_b32 s35, s30, 2\n" \
  "s_lshl3_add_u32 s33, s29, s35\n" \
  "s_and_b32 s34, s30, 3\n" \
  "s_lshl_b32 s59, s27, 11\n" \
  "s_mul_i32 s35, s33, 1600000\n" \
  "s_add_u32 s54, s22, s35\n" \
  "s_addc_u32 s55, s23, 0\n" \
  "s_mul_i32 s35, s34, 819200\n" \
  "s_lshl1_add_u32 s35, s59, s35\n" \
  "s_add_u32 s35, s35, 102400000\n" \
  "s_add_u32 s56, s22, s35\n" \
  "s_addc_u32 s57, s23, 0\n" \
  "s_lshr_b32 s35, s30, 4\n" \
  "s_lshl1_add_u32 s35, s29, s35\n" \
  "s_lshl_b32 s35, s35, 6\n" \
  "s_bfe_u32 s37, s30, 0x20002\n" \
  "s_lshl3_add_u32 s35, s37, s35\n" \
  "v_and_b32 v202, 16, v255\n" \
  "v_lshl_add_u32 v202, v202, 1, s35\n" \
  "global_load_dwordx2 v[204:205], v202, s[16:17]\n" \
  "global_load_dwordx2 v[206:207], v202, s[18:19]\n" \
  "v_mov_b32 v202, v238\n" \
  "global_load_dwordx4 v[208:211], v238, s[56:57] offset:0 sc1\n" \
  "global_load_dwordx4 v[212:215], v238, s[56:57] offset:1024 sc1\n" \
  "global_load_dwordx4 v[216:219], v238, s[56:57] offset:2048 sc1\n" \
  "global_load_dwordx4 v[220:223], v238, s[56:57] offset:3072 sc1\n" \
  "s_add_u32 s56, s56, 32768\n" \
  "s_addc_u32 s57, s57, 0\n" \
  "global_load_dwordx4 v[224:227], v238, s[56:57] sc1\n" \
  "global_load_dwordx4 v[228:231], v238, s[56:57] offset:1024 sc1\n" \
  "global_load_dwordx4 v[232:235], v238, s[56:57] offset:2048 sc1\n" \
  "global_load_dwordx4 v[236:239], v238, s[56:57] offset:3072 sc1\n" \
  "s_add_u32 s56, s56, 32768\n" \
  "s_addc_u32 s57, s57, 0\n" \
  "s_bitcmp1_b32 s30, 2\n" \
  "s_cbranch_scc1 LB_t_h\n" \
  "s_waitcnt lgkmcnt(12)\n" \
  "v_mfma_f32_16x16x32_bf16 v[160:163], v[128:131], v[0:3], 0\n" \
  "v_mfma_f32_16x16x32_bf16 v[160:163], v[132:135], v[4:7], v[160:163]\n" \
  "s_waitcnt lgkmcnt(8)\n" \
  "v_mfma_f32_16x16x32_bf16 v[160:163], v[136:139], v[8:11], v[160:163]\n" \
  "v_mfma_f32_16x16x32_bf16 v[160:163], v[140:143], v[12:15], v[160:163]\n" \
  "s_waitcnt lgkmcnt(0)\n" \
  "v_mfma_f32_16x16x32_bf16 v[164:167], v[156:159], v[12:15], 0\n" \
  "v_mfma_f32_16x16x32_bf16 v[164:167], v[152:155], v[8:11], v[164:167]\n" \
  "v_mfma_f32_16x16x32_bf16 v[164:167], v[148:151], v[4:7], v[164:167]\n" \
  "v_mfma_f32_16x16x32_bf16 v[164:167], v[144:147], v[0:3], v[164:167]\n" \
  "v_mfma_f32_16x16x32_bf16 v[172:175], v[144:147], v[16:19], 0\n" \
  "v_mfma_f32_16x16x32_bf16 v[172:175], v[148:151], v[20:23], v[172:175]\n" \
  "v_mfma_f32_16x16x32_bf16 v[172:175], v[152:155], v[24:27], v[172:175]\n" \
  "v_cvt_pk_bf16_f32 v176, v160, v161\n" \
  "v_cvt_pk_bf16_f32 v177, v162, v163\n" \
  "v_mfma_f32_16x16x32_bf16 v[172:175], v[156:159], v[28:31], v[172:175]\n" \
  "v_mfma_f32_16x16x32_bf16 v[168:171], v[140:143], v[28:31], 0\n" \
  "v_mfma_f32_16x16x32_bf16 v[168:171], v[136:139], v[24:27], v[168:171]\n" \
  "v_cvt_pk_bf16_f32 v178, v164, v165\n" \
  "v_cvt_pk_bf16_f32 v179, v166, v167\n" \
  "v_mfma_f32_16x16x32_bf16 v[168:171], v[132:135], v[20:23], v[168:171]\n" \
  "v_mfma_f32_16x16x32_bf16 v[168:171], v[128:131], v[16:19], v[168:171]\n" \
  "s_waitcnt vmcnt(63)\n" \
  "global_store_dwordx4 v186, v[176:179], s[46:47] offset:0 sc1\n" \
  "v_mfma_f32_16x16x32_bf16 v[160:163], v[128:131], v[32:35], 0\n" \
  "v_cvt_pk_bf16_f32 v182, v172, v173\n" \
  "v_cvt_pk_bf16_f32 v183, v174, v175\n" \
  "v_mfma_f32_16x16x32_bf16 v[160:163], v[132:135], v[36:39], v[160:163]\n" \
  "v_mfma_f32_16x16x32_bf16 v[160:163], v[136:139], v[40:43], v[160:163]\n" \
  "v_mfma_f32_16x16x32_bf16 v[160:163], v[140:143], v[44:47], v[160:163]\n" \
  "v_cvt_pk_bf16_f32 v180, v168, v169\n" \
  "v_cvt_pk_bf16_f32 v181, v170, v171\n" \
  "v_mfma_f32_16x16x32_bf16 v[164:167], v[156:159], v[44:47], 0\n" \
  "v_mfma_f32_16x16x32_bf16 v[164:167], v[152:155], v[40:43], v[164:167]\n" \
  "v_mfma_f32_16x16x32_bf16 v[164:167], v[148:151], v[36:39], v[164:167]\n" \
  "v_mfma_f32_16x16x32_bf16 v[164:167], v[144:147], v[32:35], v[164:167]\n" \
  "s_waitcnt vmcnt(63)\n" \
  "global_store_dwordx4 v186, v[180:183], s[46:47] offset:256 sc1\n" \
  "v_mfma_f32_16x16x32_bf16 v[172:175], v[144:147], v[48:51], 0\n" \
  "v_cvt_pk_bf16_f32 v176, v160, v161\n" \
  "v_cvt_pk_bf16_f32 v177, v162, v163\n" \
  "v_mfma_f32_16x16x32_bf16 v[172:175], v[148:151], v[52:55], v[172:175]\n" \
  "v_mfma_f32_16x16x32_bf16 v[172:175], v[152:155], v[56:59], v[172:175]\n" \
  "v_mfma_f32_16x16x32_bf16 v[172:175], v[156:159], v[60:63], v[172:175]\n" \
  "v_cvt_pk_bf16_f32 v178, v164, v165\n" \
  "v_cvt_pk_bf16_f32 v179, v166, v167\n" \
  "v_mfma_f32_16x16x32_bf16 v[168:171], v[140:143], v[60:63], 0\n" \
  "v_mfma_f32_16x16x32_bf16 v[168:171], v[136:139], v[56:59], v[168:171]\n" \
  "v_mfma_f32_16x16x32_bf16 v[168:171], v[132:135], v[52:55], v[168:171]\n" \
  "v_mfma_f32_16x16x32_bf16 v[168:171], v[128:131], v[48:51], v[168:171]\n" \
  "s_waitcnt vmcnt(63)\n" \
  "global_store_dwordx4 v186, v[176:179], s[46:47] offset:512 sc1\n" \
  "v_cvt_pk_bf16_f32 v182, v172, v173\n" \
  "v_cvt_pk_bf16_f32 v183, v174, v175\n" \
  "s_nop 3\n" \
  "v_cvt_pk_bf16_f32 v180, v168, v169\n" \
  "v_cvt_pk_bf16_f32 v181, v170, v171\n" \
  "s_waitcnt vmcnt(63)\n" \
  "global_store_dwordx4 v186, v[180:183], s[46:47] offset:768 sc1\n" \
  "s_branch LB_t_e\n" \
  "LT_w:\n" \
  "L8_:\n" \
  "s_sleep 1\n" \
  "ds_read_b32 v192, v190 offset:0\n" \
  "s_waitcnt lgkmcnt(0)\n" \
  "v_readfirstlane_b32 s35, v192\n" \
  "s_cmp_lt_u32 s35, s99\n" \
  "s_cbranch_scc1 L8_\n" \
  "s_branch LT_c\n" \
  "LT_e:\n" \
  "s_bitcmp1_b32 10, s98\n" \
  "s_cbranch_scc0 LT_r\n" \
  "global_load_dwordx4 v[240:243], v238, s[48:49] offset:0 sc1\n" \
  "s_waitcnt vmcnt(0)\n" \
  "global_store_dwordx4 v238, v[240:243], s[48:49] offset:1024 sc1\n" \
  "s_branch LT_r\n" \
  "LT_a:\n" \
  "s_cmp_lg_u32 s35, 4\n" \
  "s_cbranch_scc1 L9_\n" \
  "global_load_dword v193, v193, s[48:49] offset:1024 sc1\n" \
  "s_branch LT_s\n" \
  "L9_:\n" \
  "s_cmp_lg_u32 s35, 5\n" \
  "s_cbranch_scc1 L10_\n" \
  "s_memtime s[58:59]\n" \
  "s_memrealtime s[40:41]\n" \
  "s_mov_b32 s5, 0x10000\n" \
  "LB_ec:\n" \
  "v_readfirstlane_b32 s35, v193\n" \
  "s_cmp_eq_u32 s35, s39\n" \
  "s_cbranch_scc1 LB_ok\n" \
  "s_lshl_b32 s35, s26, 2\n" \
  "v_mov_b32 v193, s35\n" \
  "s_sleep 8\n" \
  "global_load_dword v193, v193, s[48:49] offset:1024 sc1\n" \
  "s_waitcnt vmcnt(0)\n" \
  "s_sub_u32 s5, s5, 1\n" \
  "s_cbranch_scc0 LB_ec\n" \
  "LB_ok:\n" \
  "s_lshl_b32 s35, s26, 2\n" \
  "v_mov_b32 v244, s35\n" \
  "s_waitcnt lgkmcnt(0)\n" \
  "s_sub_u32 s88, s58, s88\n" \
  "s_sub_u32 s88, 106000, s88\n" \
  "s_sub_u32 s90, s58, s90\n" \
  "s_sub_u32 s42, s40, s42\n" \
  "s_lshl_b32 s40, s40, 4\n" \
  "v_cvt_f32_u32 v245, s90\n" \
  "v_cvt_f32_u32 v246, s42\n" \
  "v_rcp_f32 v245, v245\n" \
  "v_cvt_f32_i32 v247, s88\n" \
  "v_mul_f32 v246, 0x4111eb85, v246\n" \
  "v_mul_f32 v245, v245, v246\n" \
  "v_mul_f32 v245, v245, v247\n" \
  "v_cvt_i32_f32 v245, v245\n" \
  "v_add_u32 v245, s40, v245\n" \
  "v_or_b32 v245, 1, v245\n" \
  "s_mov_b64 exec, 1\n" \
  "global_store_dword v244, v245, s[48:49] offset:2048 sc1\n" \
  "s_mov_b64 exec, -1\n" \
  "s_branch LT_s\n" \
  "L10_:\n" \
  "s_cmp_lg_u32 s35, 7\n" \
  "s_cbranch_scc1 L11_\n" \
  "s_mov_b32 exec_hi, 0\n" \
  "global_atomic_add v196, v191, s[48:49] offset:3072 sc1\n" \
  "s_mov_b64 exec, -1\n" \
  "s_branch LT_s\n" \
  "L11_:\n" \
  "s_cmp_lg_u32 s35, 23\n" \
  "s_cbranch_scc1 L12_\n" \
  "s_lshl_b32 s35, s30, 5\n" \
  "v_lshrrev_b32 v206, 3, v255\n" \
  "v_and_b32 v208, 7, v255\n" \
  "v_lshl_add_u32 v210, v206, 2, s35\n" \
  "v_lshl_add_u32 v212, v208, 2, s35\n" \
  "global_load_dword v202, v210, s[48:49] offset:2048 sc1\n" \
  "global_load_dword v204, v212, s[48:49] offset:2048 sc1\n" \
  "s_branch LT_s\n" \
  "L12_:\n" \
  "s_cmp_lg_u32 s35, 24\n" \
  "s_cbranch_scc1 L13_\n" \
  "s_mov_b32 s5, 0x10000\n" \
  "LB_z:\n" \
  "v_min_u32 v214, v202, v204\n" \
  "v_cmp_eq_u32 s[58:59], 0, v214\n" \
  "s_sub_u32 s5, s5, 1\n" \
  "s_cbranch_scc1 LB_zx\n" \
  "s_cmp_eq_u64 s[58:59], 0\n" \
  "s_cbranch_scc1 LB_zx\n" \
  "s_cmp_lg_u32 s33, 1021\n" \
  "s_cbranch_scc1 LB_n1\n" \
  "global_load_dwordx4 v[240:243], v238, s[48:49] offset:0 sc1\n" \
  "s_waitcnt vmcnt(0)\n" \
  "global_store_dwordx4 v238, v[240:243], s[48:49] offset:1024 sc1\n" \
  "LB_n1:\n" \
  "s_sleep 4\n" \
  "global_load_dword v202, v210, s[48:49] offset:2048 sc1\n" \
  "global_load_dword v204, v212, s[48:49] offset:2048 sc1\n" \
  "s_waitcnt vmcnt(0)\n" \
  "s_branch LB_z\n" \
  "LB_zx:\n" \
  "v_and_or_b32 v202, v202, -8, v206\n" \
  "v_and_or_b32 v204, v204, -8, v208\n" \
  "v_cmp_lt_u32 s[58:59], v204, v202\n" \
  "v_sub_u32 v202, v204, v202\n" \
  "s_lshl_b32 s35, s29, 3\n" \
  "s_lshr_b64 s[40:41], s[58:59], s35\n" \
  "s_and_b32 s40, s40, 0xff\n" \
  "s_bcnt1_i32_b32 s40, s40\n" \
  "s_sub_u32 s40, 7, s40\n" \
  "s_mov_b32 s36, s29\n" \
  "s_bfe_u32 s41, s58, 0x80000\n" \
  "s_bcnt1_i32_b32 s41, s41\n" \
  "s_cmp_eq_u32 s41, s40\n" \
  "s_cselect_b32 s36, 0, s36\n" \
  "s_bfe_u32 s41, s58, 0x80008\n" \
  "s_bcnt1_i32_b32 s41, s41\n" \
  "s_cmp_eq_u32 s41, s40\n" \
  "s_cselect_b32 s36, 1, s36\n" \
  "s_bfe_u32 s41, s58, 0x80010\n" \
  "s_bcnt1_i32_b32 s41, s41\n" \
  "s_cmp_eq_u32 s41, s40\n" \
  "s_cselect_b32 s36, 2, s36\n" \
  "s_bfe_u32 s41, s58, 0x80018\n" \
  "s_bcnt1_i32_b32 s41, s41\n" \
  "s_cmp_eq_u32 s41, s40\n" \
  "s_cselect_b32 s36, 3, s36\n" \
  "s_bfe_u32 s41, s59, 0x80000\n" \
  "s_bcnt1_i32_b32 s41, s41\n" \
  "s_cmp_eq_u32 s41, s40\n" \
  "s_cselect_b32 s36, 4, s36\n" \
  "s_bfe_u32 s41, s59, 0x80008\n" \
  "s_bcnt1_i32_b32 s41, s41\n" \
  "s_cmp_eq_u32 s41, s40\n" \
  "s_cselect_b32 s36, 5, s36\n" \
  "s_bfe_u32 s41, s59, 0x80010\n" \
  "s_bcnt1_i32_b32 s41, s41\n" \
  "s_cmp_eq_u32 s41, s40\n" \
  "s_cselect_b32 s36, 6, s36\n" \
  "s_bfe_u32 s41, s59, 0x80018\n" \
  "s_bcnt1_i32_b32 s41, s41\n" \
  "s_cmp_eq_u32 s41, s40\n" \
  "s_cselect_b32 s36, 7, s36\n" \
  "s_add_u32 s35, s35, s36\n" \
  "v_readlane_b32 s41, v202, s35\n" \
  "s_sub_u32 s35, 0, s41\n" \
  "s_cmp_lt_u32 s40, 4\n" \
  "s_cselect_b32 s41, s35, s41\n" \
  "s_min_u32 s41, s41, 0xffff\n" \
  "s_mul_i32 s41, s41, 39\n" \
  "s_add_u32 s41, s41, 28672\n" \
  "s_lshr_b32 s41, s41, 16\n" \
  "s_min_u32 s41, s41, 4\n" \
  "s_sub_u32 s35, 0, s41\n" \
  "s_cmp_lt_u32 s40, 4\n" \
  "s_cselect_b32 s35, s35, s41\n" \
  "s_lshl4_add_u32 s35, s36, s35\n" \
  "s_add_u32 s35, s35, 132\n" \
  "v_mov_b32 v204, s35\n" \
  "ds_write_b32 v190, v204 offset:4\n" \
  "s_branch LT_s\n" \
  "L13_:\n" \
  "s_cmp_lg_u32 s35, 25\n" \
  "s_cbranch_scc1 L14_\n" \
  "s_mov_b64 exec, 0xff\n" \
  "global_load_dword v200, v198, s[48:49] offset:3072 sc1\n" \
  "s_mov_b64 exec, -1\n" \
  "s_branch LT_s\n" \
  "L14_:\n" \
  "s_cmp_lg_u32 s35, 26\n" \
  "s_cbranch_scc1 L15_\n" \
  "s_mov_b64 s[58:59], vcc\n" \
  "s_mov_b32 s5, 0x10000\n" \
  "LB_p:\n" \
  "s_mov_b64 exec, 0xff\n" \
  "v_cmp_le_u32 vcc, 32, v200\n" \
  "s_mov_b64 exec, -1\n" \
  "s_sub_u32 s5, s5, 1\n" \
  "s_cselect_b32 s35, 0xff, vcc_lo\n" \
  "s_cmp_eq_u32 s35, 0xff\n" \
  "s_cbranch_scc1 LB_p_x\n" \
  "s_cmp_lg_u32 s33, 1021\n" \
  "s_cbranch_scc1 LB_n2\n" \
  "global_load_dwordx4 v[240:243], v238, s[48:49] offset:0 sc1\n" \
  "s_waitcnt vmcnt(0)\n" \
  "global_store_dwordx4 v238, v[240:243], s[48:49] offset:1024 sc1\n" \
  "LB_n2:\n" \
  "s_sleep 2\n" \
  "s_mov_b64 exec, 0xff\n" \
  "global_load_dword v200, v198, s[48:49] offset:3072 sc1\n" \
  "s_mov_b64 exec, -1\n" \
  "s_waitcnt vmcnt(0)\n" \
  "s_branch LB_p\n" \
  "LB_p_x:\n" \
  "s_mov_b64 vcc, s[58:59]\n" \
  "s_branch LT_s\n" \
  "L15_:\n" \
  "s_branch LT_s\n" \
  "LT_m:\n" \
  "ds_write_b16_d16_hi v195, v196 offset:0\n" \
  "ds_write_b16_d16_hi v195, v198 offset:16\n" \
  "ds_write_b16_d16_hi v195, v200 offset:32\n" \
  "ds_write_b16_d16_hi v235, v202 offset:64\n" \
  "ds_write_b16_d16_hi v235, v204 offset:80\n" \
  "ds_write_b16_d16_hi v235, v206 offset:96\n" \
  "ds_write_b16_d16_hi v235, v208 offset:112\n" \
  "ds_write_b16_d16_hi v235, v210 offset:128\n" \
  "ds_write_b16_d16_hi v235, v212 offset:144\n" \
  "ds_write_b16_d16_hi v235, v214 offset:160\n" \
  "ds_write_b16_d16_hi v235, v216 offset:176\n" \
  "ds_write_b16_d16_hi v195, v218 offset:48\n" \
  "ds_write_b16_d16_hi v195, v220 offset:192\n" \
  "ds_write_b16_d16_hi v195, v222 offset:208\n" \
  "ds_write_b16_d16_hi v195, v224 offset:224\n" \
  "ds_write_b16_d16_hi v195, v226 offset:240\n" \
  "ds_read_b128 v[244:247], v254 offset:0\n" \
  "ds_read_b128 v[248:251], v254 offset:1088\n" \
  "s_lshr_b32 s35, s7, 5\n" \
  "s_mul_i32 s35, s35, 819200\n" \
  "s_and_b32 s36, s7, 31\n" \
  "s_lshl_b32 s36, s36, 10\n" \
  "s_add_u32 s35, s35, s36\n" \
  "s_lshl_b32 s36, s4, 9\n" \
  "s_add_u32 s35, s35, s36\n" \
  "s_add_u32 s35, s35, 102400000\n" \
  "s_add_u32 s58, s22, s35\n" \
  "s_addc_u32 s59, s23, 0\n" \
  "s_waitcnt lgkmcnt(0)\n" \
  "s_cmp_ge_u32 s4, 768\n" \
  "s_cbranch_scc1 L16_\n" \
  "global_store_dwordx4 v238, v[244:247], s[58:59] sc1\n" \
  "s_cmp_ge_u32 s4, 768\n" \
  "s_cbranch_scc1 L16_\n" \
  "s_add_u32 s58, s58, 32768\n" \
  "s_addc_u32 s59, s59, 0\n" \
  "global_store_dwordx4 v238, v[248:251], s[58:59] sc1\n" \
  "L16_:\n" \
  "v_mov_b32 v234, 0\n" \
  "v_mov_b32 v236, v237\n" \
  "s_mov_b32 s38, 1\n" \
  "s_lshl_b32 s35, s29, 8\n" \
  "v_lshl_add_u32 v196, v255, 11, s35\n" \
  "s_lshl_b32 s35, s30, 11\n" \
  "v_lshl_add_u32 v198, v255, 8, s35\n" \
  "s_branch LT\n" \
  "LB_t_h:\n" \
  "s_waitcnt lgkmcnt(12)\n" \
  "v_mfma_f32_16x16x32_bf16 v[160:163], v[128:131], v[64:67], 0\n" \
  "v_mfma_f32_16x16x32_bf16 v[160:163], v[132:135], v[68:71], v[160:163]\n" \
  "s_waitcnt lgkmcnt(8)\n" \
  "v_mfma_f32_16x16x32_bf16 v[160:163], v[136:139], v[72:75], v[160:163]\n" \
  "v_mfma_f32_16x16x32_bf16 v[160:163], v[140:143], v[76:79], v[160:163]\n" \
  "s_waitcnt lgkmcnt(0)\n" \
  "v_mfma_f32_16x16x32_bf16 v[164:167], v[156:159], v[76:79], 0\n" \
  "v_mfma_f32_16x16x32_bf16 v[164:167], v[152:155], v[72:75], v[164:167]\n" \
  "v_mfma_f32_16x16x32_bf16 v[164:167], v[148:151], v[68:71], v[164:167]\n" \
  "v_mfma_f32_16x16x32_bf16 v[164:167], v[144:147], v[64:67], v[164:167]\n" \
  "v_mfma_f32_16x16x32_bf16 v[172:175], v[144:147], v[80:83], 0\n" \
  "v_mfma_f32_16x16x32_bf16 v[172:175], v[148:151], v[84:87], v[172:175]\n" \
  "v_mfma_f32_16x16x32_bf16 v[172:175], v[152:155], v[88:91], v[172:175]\n" \
  "v_cvt_pk_bf16_f32 v176, v160, v161\n" \
  "v_cvt_pk_bf16_f32 v177, v162, v163\n" \
  "v_mfma_f32_16x16x32_bf16 v[172:175], v[156:159], v[92:95], v[172:175]\n" \
  "v_mfma_f32_16x16x32_bf16 v[168:171], v[140:143], v[92:95], 0\n" \
  "v_mfma_f32_16x16x32_bf16 v[168:171], v[136:139], v[88:91], v[168:171]\n" \
  "v_cvt_pk_bf16_f32 v178, v164, v165\n" \
  "v_cvt_pk_bf16_f32 v179, v166, v167\n" \
  "v_mfma_f32_16x16x32_bf16 v[168:171], v[132:135], v[84:87], v[168:171]\n" \
  "v_mfma_f32_16x16x32_bf16 v[168:171], v[128:131], v[80:83], v[168:171]\n" \
  "s_waitcnt vmcnt(63)\n" \
  "global_store_dwordx4 v186, v[176:179], s[46:47] offset:1024 sc1\n" \
  "v_mfma_f32_16x16x32_bf16 v[160:163], v[128:131], v[96:99], 0\n" \
  "v_cvt_pk_bf16_f32 v182, v172, v173\n" \
  "v_cvt_pk_bf16_f32 v183, v174, v175\n" \
  "v_mfma_f32_16x16x32_bf16 v[160:163], v[132:135], v[100:103], v[160:163]\n" \
  "v_mfma_f32_16x16x32_bf16 v[160:163], v[136:139], v[104:107], v[160:163]\n" \
  "v_mfma_f32_16x16x32_bf16 v[160:163], v[140:143], v[108:111], v[160:163]\n" \
  "v_cvt_pk_bf16_f32 v180, v168, v169\n" \
  "v_cvt_pk_bf16_f32 v181, v170, v171\n" \
  "v_mfma_f32_16x16x32_bf16 v[164:167], v[156:159], v[108:111], 0\n" \
  "v_mfma_f32_16x16x32_bf16 v[164:167], v[152:155], v[104:107], v[164:167]\n" \
  "v_mfma_f32_16x16x32_bf16 v[164:167], v[148:151], v[100:103], v[164:167]\n" \
  "v_mfma_f32_16x16x32_bf16 v[164:167], v[144:147], v[96:99], v[164:167]\n" \
  "s_waitcnt vmcnt(63)\n" \
  "global_store_dwordx4 v186, v[180:183], s[46:47] offset:1280 sc1\n" \
  "v_mfma_f32_16x16x32_bf16 v[168:171], v[128:131], v[112:115], 0\n" \
  "v_cvt_pk_bf16_f32 v176, v160, v161\n" \
  "v_cvt_pk_bf16_f32 v177, v162, v163\n" \
  "v_mfma_f32_16x16x32_bf16 v[168:171], v[132:135], v[116:119], v[168:171]\n" \
  "v_mfma_f32_16x16x32_bf16 v[168:171], v[136:139], v[120:123], v[168:171]\n" \
  "v_mfma_f32_16x16x32_bf16 v[168:171], v[140:143], v[124:127], v[168:171]\n" \
  "v_cvt_pk_bf16_f32 v178, v164, v165\n" \
  "v_cvt_pk_bf16_f32 v179, v166, v167\n" \
  "v_mfma_f32_16x16x32_bf16 v[172:175], v[156:159], v[124:127], 0\n" \
  "v_mfma_f32_16x16x32_bf16 v[172:175], v[152:155], v[120:123], v[172:175]\n" \
  "v_mfma_f32_16x16x32_bf16 v[172:175], v[148:151], v[116:119], v[172:175]\n" \
  "v_mfma_f32_16x16x32_bf16 v[172:175], v[144:147], v[112:115], v[172:175]\n" \
  "s_waitcnt vmcnt(63)\n" \
  "global_store_dwordx4 v187, v[176:179], s[46:47] offset:0 sc1\n" \
  "v_cvt_pk_bf16_f32 v180, v168, v169\n" \
  "v_cvt_pk_bf16_f32 v181, v170, v171\n" \
  "s_nop 3\n" \
  "v_cvt_pk_bf16_f32 v182, v172, v173\n" \
  "v_cvt_pk_bf16_f32 v183, v174, v175\n" \
  "s_waitcnt vmcnt(63)\n" \
  "global_store_dwordx4 v188, v[180:183], s[46:47] offset:0 sc1\n" \
  "LB_t_e:\n" \
  "s_setprio 2\n" \
  "v_mov_b64 v[240:241], -1\n" \
  "v_mov_b64 v[242:243], -1\n" \
  "s_lshl2_add_u32 s35, s33, s34\n" \
  "s_lshl2_add_u32 s35, s35, s28\n" \
  "s_lshl_b32 s35, s35, 12\n" \
  "s_add_u32 s35, s35, 105676800\n" \
  "s_add_u32 s40, s22, s35\n" \
  "s_addc_u32 s41, s23, 0\n" \
  "v_mbcnt_lo_u32_b32 v189, -1, 0\n" \
  "v_mbcnt_hi_u32_b32 v189, -1, v189\n" \
  "v_lshlrev_b32 v188, 4, v189\n" \
  "s_lshl_b32 s35, s28, 12\n" \
  "v_add_u32 v128, s35, v188\n" \
  "v_mov_b32 v199, 1\n" \
  "v_lshrrev_b32 v180, 1, v189\n" \
  "v_xor_b32 v180, v180, v189\n" \
  "v_and_b32 v180, 8, v180\n" \
  "v_bfe_u32 v181, v189, 2, 1\n" \
  "v_lshlrev_b32_e64 v181, v181, 5\n" \
  "v_lshlrev_b32 v180, v180, v181\n" \
  "v_bfe_u32 v181, v189, 2, 2\n" \
  "v_lshlrev_b32_e64 v181, v181, 1\n" \
  "v_cmp_gt_u32 vcc, 32, v189\n" \
  "v_mov_b32 v191, 0x3f80\n" \
  "v_add_u32 v190, 32000, v188\n" \
  "v_cndmask_b32 v192, v181, v180, vcc\n" \
  "v_bfe_i32 v180, v192, 0, 1\n" \
  "v_and_b32 v180, v180, v191\n" \
  "v_bfe_i32 v181, v192, 1, 1\n" \
  "v_and_b32 v181, v181, v191\n" \
  "v_bfe_i32 v182, v192, 2, 1\n" \
  "v_and_b32 v182, v182, v191\n" \
  "v_bfe_i32 v183, v192, 3, 1\n" \
  "v_and_b32 v183, v183, v191\n" \
  "v_and_b32 v192, 3, v189\n" \
  "v_mul_u32_u24 v192, 0x5555, v192\n" \
  "v_add_u32 v189, 16000, v188\n" \
  "v_add_u32 v191, 48000, v188\n" \
  "s_waitcnt vmcnt(4)\n" \
  "v_lshlrev_b32 v184, 16, v204\n" \
  "v_and_b32 v185, 0xffff0000, v204\n" \
  "v_lshlrev_b32 v186, 16, v205\n" \
  "v_and_b32 v187, 0xffff0000, v205\n" \
  "global_store_dwordx4 v202, v[240:243], s[40:41] offset:0 sc1\n" \
  "global_store_dwordx4 v202, v[240:243], s[40:41] offset:1024 sc1\n" \
  "global_store_dwordx4 v202, v[240:243], s[40:41] offset:2048 sc1\n" \
  "global_store_dwordx4 v202, v[240:243], s[40:41] offset:3072 sc1\n" \
  "s_load_dwordx2 s[24:25], s[0:1], 0x40\n" \
  "s_setprio 0\n" \
  "s_barrier\n" \
  "s_bitcmp1_b32 s30, 2\n" \
  "s_cbranch_scc1 LB_w_h\n" \
  "ds_write_b128 v128, v[64:67] offset:0\n" \
  "ds_write_b128 v128, v[68:71] offset:1024\n" \
  "ds_write_b128 v128, v[72:75] offset:2048\n" \
  "ds_write_b128 v128, v[76:79] offset:3072\n" \
  "ds_write_b128 v128, v[80:83] offset:16384\n" \
  "ds_write_b128 v128, v[84:87] offset:17408\n" \
  "ds_write_b128 v128, v[88:91] offset:18432\n" \
  "ds_write_b128 v128, v[92:95] offset:19456\n" \
  "ds_write_b128 v128, v[96:99] offset:32768\n" \
  "ds_write_b128 v128, v[100:103] offset:33792\n" \
  "ds_write_b128 v128, v[104:107] offset:34816\n" \
  "ds_write_b128 v128, v[108:111] offset:35840\n" \
  "ds_write_b128 v128, v[112:115] offset:49152\n" \
  "ds_write_b128 v128, v[116:119] offset:50176\n" \
  "ds_write_b128 v128, v[120:123] offset:51200\n" \
  "ds_write_b128 v128, v[124:127] offset:52224\n" \
  "s_add_u32 s35, s59, 64000\n" \
  "s_add_u32 s54, s54, s35\n" \
  "s_addc_u32 s55, s55, 0\n" \
  "s_add_u32 s58, s59, 0x10000\n" \
  "s_mov_b32 m0, s58\n" \
  "s_add_u32 s35, s58, 0x8000\n" \
  "global_load_lds_dwordx4 v188, s[54:55] sc1\n" \
  "global_load_lds_dwordx4 v188, s[54:55] offset:1024 sc1\n" \
  "s_add_u32 m0, s58, 0x4000\n" \
  "s_cmp_lt_u32 s35, 0x28000\n" \
  "global_load_lds_dwordx4 v189, s[54:55] sc1\n" \
  "global_load_lds_dwordx4 v189, s[54:55] offset:1024 sc1\n" \
  "s_cselect_b32 s58, s35, s59\n" \
  "s_add_u32 s54, s54, 32000\n" \
  "s_addc_u32 s55, s55, 0\n" \
  "s_branch LB_w_e\n" \
  "LB_w_h:\n" \
  "ds_write_b128 v128, v[0:3] offset:0\n" \
  "ds_write_b128 v128, v[4:7] offset:1024\n" \
  "ds_write_b128 v128, v[8:11] offset:2048\n" \
  "ds_write_b128 v128, v[12:15] offset:3072\n" \
  "ds_write_b128 v128, v[16:19] offset:16384\n" \
  "ds_write_b128 v128, v[20:23] offset:17408\n" \
  "ds_write_b128 v128, v[24:27] offset:18432\n" \
  "ds_write_b128 v128, v[28:31] offset:19456\n" \
  "ds_write_b128 v128, v[32:35] offset:32768\n" \
  "ds_write_b128 v128, v[36:39] offset:33792\n" \
  "ds_write_b128 v128, v[40:43] offset:34816\n" \
  "ds_write_b128 v128, v[44:47] offset:35840\n" \
  "ds_write_b128 v128, v[48:51] offset:49152\n" \
  "ds_write_b128 v128, v[52:55] offset:50176\n" \
  "ds_write_b128 v128, v[56:59] offset:51200\n" \
  "ds_write_b128 v128, v[60:63] offset:52224\n" \
  "s_add_u32 s35, s59, 64000\n" \
  "s_add_u32 s54, s54, s35\n" \
  "s_addc_u32 s55, s55, 0\n" \
  "s_add_u32 s58, s59, 0x10000\n" \
  "s_mov_b32 m0, s58\n" \
  "s_add_u32 s35, s58, 0x8000\n" \
  "global_load_lds_dwordx4 v188, s[54:55] sc1\n" \
  "global_load_lds_dwordx4 v188, s[54:55] offset:1024 sc1\n" \
  "s_add_u32 m0, s58, 0x4000\n" \
  "s_cmp_lt_u32 s35, 0x28000\n" \
  "global_load_lds_dwordx4 v189, s[54:55] sc1\n" \
  "global_load_lds_dwordx4 v189, s[54:55] offset:1024 sc1\n" \
  "s_cselect_b32 s58, s35, s59\n" \
  "s_add_u32 s54, s54, 32000\n" \
  "s_addc_u32 s55, s55, 0\n" \
  "LB_w_e:\n" \
  "s_waitcnt lgkmcnt(0)\n" \
  "s_mov_b32 s51, 7\n" \
  "s_setprio 0\n" \
  "s_barrier\n" \
  "LG_start:\n" \
  "s_sub_u32 s35, 14960, s59\n" \
  "v_min_u32 v156, s35, v188\n" \
  "v_add_u32 v157, 16000, v156\n" \
  "v_add_u32 v158, 32000, v156\n" \
  "v_add_u32 v159, 48000, v156\n" \
  "v_lshlrev_b32_sdwa v64, 4, v208 dst_sel:DWORD dst_unused:UNUSED_PAD src0_sel:DWORD src1_sel:WORD_0\n" \
  "ds_read_b128 v[68:71], v64\n" \
  "v_mov_b64 v[0:1], v[184:185]\n" \
  "v_lshlrev_b32_sdwa v65, 4, v208 dst_sel:DWORD dst_unused:UNUSED_PAD src0_sel:DWORD src1_sel:WORD_1\n" \
  "ds_read_b128 v[72:75], v65\n" \
  "v_mov_b64 v[2:3], v[186:187]\n" \
  "v_lshlrev_b32_sdwa v66, 4, v209 dst_sel:DWORD dst_unused:UNUSED_PAD src0_sel:DWORD src1_sel:WORD_0\n" \
  "ds_read_b128 v[76:79], v66\n" \
  "v_mov_b64 v[4:5], v[184:185]\n" \
  "v_lshlrev_b32_sdwa v67, 4, v209 dst_sel:DWORD dst_unused:UNUSED_PAD src0_sel:DWORD src1_sel:WORD_1\n" \
  "ds_read_b128 v[80:83], v67\n" \
  "v_mov_b64 v[6:7], v[186:187]\n" \
  "s_mov_b32 m0, s58\n" \
  "s_add_u32 s35, s58, 0x8000\n" \
  "global_load_lds_dwordx4 v188, s[54:55] sc1\n" \
  "global_load_lds_dwordx4 v156, s[54:55] offset:1024 sc1\n" \
  "v_lshlrev_b32_sdwa v64, 4, v210 dst_sel:DWORD dst_unused:UNUSED_PAD src0_sel:DWORD src1_sel:WORD_0\n" \
  "ds_read_b128 v[84:87], v64\n" \
  "v_mov_b64 v[8:9], v[184:185]\n" \
  "v_lshlrev_b32_sdwa v65, 4, v210 dst_sel:DWORD dst_unused:UNUSED_PAD src0_sel:DWORD src1_sel:WORD_1\n" \
  "ds_read_b128 v[88:91], v65\n" \
  "v_mov_b64 v[10:11], v[186:187]\n" \
  "v_lshlrev_b32_sdwa v66, 4, v211 dst_sel:DWORD dst_unused:UNUSED_PAD src0_sel:DWORD src1_sel:WORD_0\n" \
  "ds_read_b128 v[92:95], v66\n" \
  "v_mov_b64 v[12:13], v[184:185]\n" \
  "v_lshlrev_b32_sdwa v67, 4, v211 dst_sel:DWORD dst_unused:UNUSED_PAD src0_sel:DWORD src1_sel:WORD_1\n" \
  "ds_read_b128 v[96:99], v67\n" \
  "v_mov_b64 v[14:15], v[186:187]\n" \
  "s_add_u32 m0, s58, 0x4000\n" \
  "s_cmp_lt_u32 s35, 0x28000\n" \
  "global_load_lds_dwordx4 v189, s[54:55] sc1\n" \
  "global_load_lds_dwordx4 v157, s[54:55] offset:1024 sc1\n" \
  "s_cselect_b32 s58, s35, s59\n" \
  "v_lshlrev_b32_sdwa v64, 4, v212 dst_sel:DWORD dst_unused:UNUSED_PAD src0_sel:DWORD src1_sel:WORD_0\n" \
  "ds_read_b128 v[100:103], v64\n" \
  "v_mov_b64 v[16:17], v[184:185]\n" \
  "v_lshlrev_b32_sdwa v65, 4, v212 dst_sel:DWORD dst_unused:UNUSED_PAD src0_sel:DWORD src1_sel:WORD_1\n" \
  "ds_read_b128 v[104:107], v65\n" \
  "v_mov_b64 v[18:19], v[186:187]\n" \
  "v_lshlrev_b32_sdwa v66, 4, v213 dst_sel:DWORD dst_unused:UNUSED_PAD src0_sel:DWORD src1_sel:WORD_0\n" \
  "ds_read_b128 v[108:111], v66\n" \
  "v_mov_b64 v[20:21], v[184:185]\n" \
  "v_lshlrev_b32_sdwa v67, 4, v213 dst_sel:DWORD dst_unused:UNUSED_PAD src0_sel:DWORD src1_sel:WORD_1\n" \
  "ds_read_b128 v[112:115], v67\n" \
  "v_mov_b64 v[22:23], v[186:187]\n" \
  "s_waitcnt lgkmcnt(10)\n" \
  "v_smfmac_f32_16x16x64_bf16 v[0:3], v[180:183], v[68:75], v192\n" \
  "v_lshlrev_b32_sdwa v64, 4, v214 dst_sel:DWORD dst_unused:UNUSED_PAD src0_sel:DWORD src1_sel:WORD_0\n" \
  "ds_read_b128 v[68:71], v64\n" \
  "v_mov_b64 v[24:25], v[184:185]\n" \
  "v_lshlrev_b32_sdwa v65, 4, v214 dst_sel:DWORD dst_unused:UNUSED_PAD src0_sel:DWORD src1_sel:WORD_1\n" \
  "ds_read_b128 v[72:75], v65\n" \
  "v_mov_b64 v[26:27], v[186:187]\n" \
  "s_mov_b32 m0, s58\n" \
  "s_add_u32 s35, s58, 0x8000\n" \
  "global_load_lds_dwordx4 v190, s[54:55] sc1\n" \
  "global_load_lds_dwordx4 v158, s[54:55] offset:1024 sc1\n" \
  "s_waitcnt lgkmcnt(10)\n" \
  "v_smfmac_f32_16x16x64_bf16 v[4:7], v[180:183], v[76:83], v192\n" \
  "v_lshlrev_b32_sdwa v66, 4, v215 dst_sel:DWORD dst_unused:UNUSED_PAD src0_sel:DWORD src1_sel:WORD_0\n" \
  "ds_read_b128 v[76:79], v66\n" \
  "v_mov_b64 v[28:29], v[184:185]\n" \
  "v_lshlrev_b32_sdwa v67, 4, v215 dst_sel:DWORD dst_unused:UNUSED_PAD src0_sel:DWORD src1_sel:WORD_1\n" \
  "ds_read_b128 v[80:83], v67\n" \
  "v_mov_b64 v[30:31], v[186:187]\n" \
  "s_waitcnt lgkmcnt(10)\n" \
  "v_smfmac_f32_16x16x64_bf16 v[8:11], v[180:183], v[84:91], v192\n" \
  "v_lshlrev_b32_sdwa v64, 4, v216 dst_sel:DWORD dst_unused:UNUSED_PAD src0_sel:DWORD src1_sel:WORD_0\n" \
  "ds_read_b128 v[84:87], v64\n" \
  "v_mov_b64 v[32:33], v[184:185]\n" \
  "v_lshlrev_b32_sdwa v65, 4, v216 dst_sel:DWORD dst_unused:UNUSED_PAD src0_sel:DWORD src1_sel:WORD_1\n" \
  "ds_read_b128 v[88:91], v65\n" \
  "v_mov_b64 v[34:35], v[186:187]\n" \
  "s_add_u32 m0, s58, 0x4000\n" \
  "s_cmp_lt_u32 s35, 0x28000\n" \
  "global_load_lds_dwordx4 v191, s[54:55] sc1\n" \
  "global_load_lds_dwordx4 v159, s[54:55] offset:1024 sc1\n" \
  "s_cselect_b32 s58, s35, s59\n" \
  "s_waitcnt lgkmcnt(10)\n" \
  "v_smfmac_f32_16x16x64_bf16 v[12:15], v[180:183], v[92:99], v192\n" \
  "v_lshlrev_b32_sdwa v66, 4, v217 dst_sel:DWORD dst_unused:UNUSED_PAD src0_sel:DWORD src1_sel:WORD_0\n" \
  "ds_read_b128 v[92:95], v66\n" \
  "v_mov_b64 v[36:37], v[184:185]\n" \
  "v_lshlrev_b32_sdwa v67, 4, v217 dst_sel:DWORD dst_unused:UNUSED_PAD src0_sel:DWORD src1_sel:WORD_1\n" \
  "ds_read_b128 v[96:99], v67\n" \
  "v_mov_b64 v[38:39], v[186:187]\n" \
  "s_waitcnt lgkmcnt(10)\n" \
  "v_smfmac_f32_16x16x64_bf16 v[16:19], v[180:183], v[100:107], v192\n" \
  "v_lshlrev_b32_sdwa v64, 4, v218 dst_sel:DWORD dst_unused:UNUSED_PAD src0_sel:DWORD src1_sel:WORD_0\n" \
  "ds_read_b128 v[100:103], v64\n" \
  "v_mov_b64 v[40:41], v[184:185]\n" \
  "v_lshlrev_b32_sdwa v65, 4, v218 dst_sel:DWORD dst_unused:UNUSED_PAD src0_sel:DWORD src1_sel:WORD_1\n" \
  "ds_read_b128 v[104:107], v65\n" \
  "v_mov_b64 v[42:43], v[186:187]\n" \
  "global_load_dwordx4 v[240:243], v188, s[56:57] sc1\n" \
  "global_load_dwordx4 v[244:247], v188, s[56:57] offset:1024 sc1\n" \
  "s_waitcnt lgkmcnt(10)\n" \
  "v_smfmac_f32_16x16x64_bf16 v[20:23], v[180:183], v[108:115], v192\n" \
  "v_lshlrev_b32_sdwa v66, 4, v219 dst_sel:DWORD dst_unused:UNUSED_PAD src0_sel:DWORD src1_sel:WORD_0\n" \
  "ds_read_b128 v[108:111], v66\n" \
  "v_mov_b64 v[44:45], v[184:185]\n" \
  "v_lshlrev_b32_sdwa v67, 4, v219 dst_sel:DWORD dst_unused:UNUSED_PAD src0_sel:DWORD src1_sel:WORD_1\n" \
  "ds_read_b128 v[112:115], v67\n" \
  "v_mov_b64 v[46:47], v[186:187]\n" \
  "s_waitcnt lgkmcnt(10)\n" \
  "v_smfmac_f32_16x16x64_bf16 v[24:27], v[180:183], v[68:75], v192\n" \
  "v_lshlrev_b32_sdwa v64, 4, v220 dst_sel:DWORD dst_unused:UNUSED_PAD src0_sel:DWORD src1_sel:WORD_0\n" \
  "ds_read_b128 v[68:71], v64\n" \
  "v_mov_b64 v[48:49], v[184:185]\n" \
  "v_lshlrev_b32_sdwa v65, 4, v220 dst_sel:DWORD dst_unused:UNUSED_PAD src0_sel:DWORD src1_sel:WORD_1\n" \
  "ds_read_b128 v[72:75], v65\n" \
  "v_mov_b64 v[50:51], v[186:187]\n" \
  "global_load_dwordx4 v[248:251], v188, s[56:57] offset:2048 sc1\n" \
  "global_load_dwordx4 v[252:255], v188, s[56:57] offset:3072 sc1\n" \
  "s_waitcnt lgkmcnt(10)\n" \
  "v_smfmac_f32_16x16x64_bf16 v[28:31], v[180:183], v[76:83], v192\n" \
  "v_lshlrev_b32_sdwa v66, 4, v221 dst_sel:DWORD dst_unused:UNUSED_PAD src0_sel:DWORD src1_sel:WORD_0\n" \
  "ds_read_b128 v[76:79], v66\n" \
  "v_mov_b64 v[52:53], v[184:185]\n" \
  "v_lshlrev_b32_sdwa v67, 4, v221 dst_sel:DWORD dst_unused:UNUSED_PAD src0_sel:DWORD src1_sel:WORD_1\n" \
  "ds_read_b128 v[80:83], v67\n" \
  "v_mov_b64 v[54:55], v[186:187]\n" \
  "s_waitcnt lgkmcnt(10)\n" \
  "v_smfmac_f32_16x16x64_bf16 v[32:35], v[180:183], v[84:91], v192\n" \
  "v_lshlrev_b32_sdwa v64, 4, v222 dst_sel:DWORD dst_unused:UNUSED_PAD src0_sel:DWORD src1_sel:WORD_0\n" \
  "ds_read_b128 v[84:87], v64\n" \
  "v_mov_b64 v[56:57], v[184:185]\n" \
  "v_lshlrev_b32_sdwa v65, 4, v222 dst_sel:DWORD dst_unused:UNUSED_PAD src0_sel:DWORD src1_sel:WORD_1\n" \
  "ds_read_b128 v[88:91], v65\n" \
  "v_mov_b64 v[58:59], v[186:187]\n" \
  "s_waitcnt lgkmcnt(10)\n" \
  "v_smfmac_f32_16x16x64_bf16 v[36:39], v[180:183], v[92:99], v192\n" \
  "v_lshlrev_b32_sdwa v66, 4, v223 dst_sel:DWORD dst_unused:UNUSED_PAD src0_sel:DWORD src1_sel:WORD_0\n" \
  "ds_read_b128 v[92:95], v66\n" \
  "v_mov_b64 v[60:61], v[184:185]\n" \
  "v_lshlrev_b32_sdwa v67, 4, v223 dst_sel:DWORD dst_unused:UNUSED_PAD src0_sel:DWORD src1_sel:WORD_1\n" \
  "ds_read_b128 v[96:99], v67\n" \
  "v_mov_b64 v[62:63], v[186:187]\n" \
  "s_add_u32 s56, s56, 32768\n" \
  "s_addc_u32 s57, s57, 0\n" \
  "s_add_u32 s54, s54, 64000\n" \
  "s_addc_u32 s55, s55, 0\n" \
  "s_cmp_eq_u32 s27, 4\n" \
  "s_cselect_b32 s37, 0x90, 0\n" \
  ".p2align 6\n" \
  "LG_loop:\n" \
  "s_bitcmp1_b32 s37, s51\n" \
  "s_waitcnt lgkmcnt(10)\n" \
  "v_smfmac_f32_16x16x64_bf16 v[40:43], v[180:183], v[100:107], v192\n" \
  "s_waitcnt lgkmcnt(8)\n" \
  "v_smfmac_f32_16x16x64_bf16 v[44:47], v[180:183], v[108:115], v192\n" \
  "s_waitcnt vmcnt(8)\n" \
  "s_cbranch_scc1 LG_h\n" \
  "LG_ret:\n" \
  "s_barrier\n" \
  "LG_aft:\n" \
  "v_lshlrev_b32_sdwa v64, 4, v224 dst_sel:DWORD dst_unused:UNUSED_PAD src0_sel:DWORD src1_sel:WORD_0\n" \
  "ds_read_b128 v[100:103], v64\n" \
  "v_lshlrev_b32_sdwa v65, 4, v224 dst_sel:DWORD dst_unused:UNUSED_PAD src0_sel:DWORD src1_sel:WORD_1\n" \
  "ds_read_b128 v[104:107], v65\n" \
  "v_lshlrev_b32_sdwa v66, 4, v225 dst_sel:DWORD dst_unused:UNUSED_PAD src0_sel:DWORD src1_sel:WORD_0\n" \
  "ds_read_b128 v[108:111], v66\n" \
  "v_lshlrev_b32_sdwa v67, 4, v225 dst_sel:DWORD dst_unused:UNUSED_PAD src0_sel:DWORD src1_sel:WORD_1\n" \
  "ds_read_b128 v[112:115], v67\n" \
  "s_waitcnt lgkmcnt(10)\n" \
  "v_smfmac_f32_16x16x64_bf16 v[48:51], v[180:183], v[68:75], v192\n" \
  "s_waitcnt lgkmcnt(8)\n" \
  "v_smfmac_f32_16x16x64_bf16 v[52:55], v[180:183], v[76:83], v192\n" \
  "s_waitcnt lgkmcnt(6)\n" \
  "v_smfmac_f32_16x16x64_bf16 v[56:59], v[180:183], v[84:91], v192\n" \
  "s_waitcnt lgkmcnt(4)\n" \
  "v_smfmac_f32_16x16x64_bf16 v[60:63], v[180:183], v[92:99], v192\n" \
  "s_barrier\n" \
  "s_mov_b32 m0, s58\n" \
  "s_add_u32 s35, s58, 0x8000\n" \
  "global_load_lds_dwordx4 v188, s[54:55] sc1\n" \
  "global_load_lds_dwordx4 v156, s[54:55] offset:1024 sc1\n" \
  "v_lshlrev_b32_sdwa v64, 4, v226 dst_sel:DWORD dst_unused:UNUSED_PAD src0_sel:DWORD src1_sel:WORD_0\n" \
  "ds_read_b128 v[68:71], v64\n" \
  "v_lshlrev_b32_sdwa v65, 4, v226 dst_sel:DWORD dst_unused:UNUSED_PAD src0_sel:DWORD src1_sel:WORD_1\n" \
  "ds_read_b128 v[72:75], v65\n" \
  "v_lshlrev_b32_sdwa v66, 4, v227 dst_sel:DWORD dst_unused:UNUSED_PAD src0_sel:DWORD src1_sel:WORD_0\n" \
  "ds_read_b128 v[76:79], v66\n" \
  "v_lshlrev_b32_sdwa v67, 4, v227 dst_sel:DWORD dst_unused:UNUSED_PAD src0_sel:DWORD src1_sel:WORD_1\n" \
  "ds_read_b128 v[80:83], v67\n" \
  "s_add_u32 m0, s58, 0x4000\n" \
  "s_cmp_lt_u32 s35, 0x28000\n" \
  "global_load_lds_dwordx4 v189, s[54:55] sc1\n" \
  "global_load_lds_dwordx4 v157, s[54:55] offset:1024 sc1\n" \
  "s_cselect_b32 s58, s35, s59\n" \
  "v_lshlrev_b32_sdwa v64, 4, v228 dst_sel:DWORD dst_unused:UNUSED_PAD src0_sel:DWORD src1_sel:WORD_0\n" \
  "ds_read_b128 v[84:87], v64\n" \
  "v_lshlrev_b32_sdwa v65, 4, v228 dst_sel:DWORD dst_unused:UNUSED_PAD src0_sel:DWORD src1_sel:WORD_1\n" \
  "ds_read_b128 v[88:91], v65\n" \
  "v_lshlrev_b32_sdwa v66, 4, v229 dst_sel:DWORD dst_unused:UNUSED_PAD src0_sel:DWORD src1_sel:WORD_0\n" \
  "ds_read_b128 v[92:95], v66\n" \
  "v_lshlrev_b32_sdwa v67, 4, v229 dst_sel:DWORD dst_unused:UNUSED_PAD src0_sel:DWORD src1_sel:WORD_1\n" \
  "ds_read_b128 v[96:99], v67\n" \
  "s_waitcnt lgkmcnt(10)\n" \
  "v_smfmac_f32_16x16x64_bf16 v[0:3], v[180:183], v[100:107], v192\n" \
  "v_lshlrev_b32_sdwa v64, 4, v230 dst_sel:DWORD dst_unused:UNUSED_PAD src0_sel:DWORD src1_sel:WORD_0\n" \
  "ds_read_b128 v[100:103], v64\n" \
  "v_lshlrev_b32_sdwa v65, 4, v230 dst_sel:DWORD dst_unused:UNUSED_PAD src0_sel:DWORD src1_sel:WORD_1\n" \
  "ds_read_b128 v[104:107], v65\n" \
  "s_mov_b32 m0, s58\n" \
  "s_add_u32 s35, s58, 0x8000\n" \
  "global_load_lds_dwordx4 v190, s[54:55] sc1\n" \
  "global_load_lds_dwordx4 v158, s[54:55] offset:1024 sc1\n" \
  "s_waitcnt lgkmcnt(10)\n" \
  "v_smfmac_f32_16x16x64_bf16 v[4:7], v[180:183], v[108:115], v192\n" \
  "v_lshlrev_b32_sdwa v66, 4, v231 dst_sel:DWORD dst_unused:UNUSED_PAD src0_sel:DWORD src1_sel:WORD_0\n" \
  "ds_read_b128 v[108:111], v66\n" \
  "v_lshlrev_b32_sdwa v67, 4, v231 dst_sel:DWORD dst_unused:UNUSED_PAD src0_sel:DWORD src1_sel:WORD_1\n" \
  "ds_read_b128 v[112:115], v67\n" \
  "s_waitcnt lgkmcnt(10)\n" \
  "v_smfmac_f32_16x16x64_bf16 v[8:11], v[180:183], v[68:75], v192\n" \
  "v_lshlrev_b32_sdwa v64, 4, v232 dst_sel:DWORD dst_unused:UNUSED_PAD src0_sel:DWORD src1_sel:WORD_0\n" \
  "ds_read_b128 v[68:71], v64\n" \
  "v_lshlrev_b32_sdwa v65, 4, v232 dst_sel:DWORD dst_unused:UNUSED_PAD src0_sel:DWORD src1_sel:WORD_1\n" \
  "ds_read_b128 v[72:75], v65\n" \
  "s_add_u32 m0, s58, 0x4000\n" \
  "s_cmp_lt_u32 s35, 0x28000\n" \
  "global_load_lds_dwordx4 v191, s[54:55] sc1\n" \
  "global_load_lds_dwordx4 v159, s[54:55] offset:1024 sc1\n" \
  "s_cselect_b32 s58, s35, s59\n" \
  "s_waitcnt lgkmcnt(10)\n" \
  "v_smfmac_f32_16x16x64_bf16 v[12:15], v[180:183], v[76:83], v192\n" \
  "v_lshlrev_b32_sdwa v66, 4, v233 dst_sel:DWORD dst_unused:UNUSED_PAD src0_sel:DWORD src1_sel:WORD_0\n" \
  "ds_read_b128 v[76:79], v66\n" \
  "v_lshlrev_b32_sdwa v67, 4, v233 dst_sel:DWORD dst_unused:UNUSED_PAD src0_sel:DWORD src1_sel:WORD_1\n" \
  "ds_read_b128 v[80:83], v67\n" \
  "s_waitcnt lgkmcnt(10)\n" \
  "v_smfmac_f32_16x16x64_bf16 v[16:19], v[180:183], v[84:91], v192\n" \
  "v_lshlrev_b32_sdwa v64, 4, v234 dst_sel:DWORD dst_unused:UNUSED_PAD src0_sel:DWORD src1_sel:WORD_0\n" \
  "ds_read_b128 v[84:87], v64\n" \
  "v_lshlrev_b32_sdwa v65, 4, v234 dst_sel:DWORD dst_unused:UNUSED_PAD src0_sel:DWORD src1_sel:WORD_1\n" \
  "ds_read_b128 v[88:91], v65\n" \
  "global_load_dwordx4 v[208:211], v188, s[56:57] sc1\n" \
  "global_load_dwordx4 v[212:215], v188, s[56:57] offset:1024 sc1\n" \
  "s_waitcnt lgkmcnt(10)\n" \
  "v_smfmac_f32_16x16x64_bf16 v[20:23], v[180:183], v[92:99], v192\n" \
  "v_lshlrev_b32_sdwa v66, 4, v235 dst_sel:DWORD dst_unused:UNUSED_PAD src0_sel:DWORD src1_sel:WORD_0\n" \
  "ds_read_b128 v[92:95], v66\n" \
  "v_lshlrev_b32_sdwa v67, 4, v235 dst_sel:DWORD dst_unused:UNUSED_PAD src0_sel:DWORD src1_sel:WORD_1\n" \
  "ds_read_b128 v[96:99], v67\n" \
  "s_waitcnt lgkmcnt(10)\n" \
  "v_smfmac_f32_16x16x64_bf16 v[24:27], v[180:183], v[100:107], v192\n" \
  "v_lshlrev_b32_sdwa v64, 4, v236 dst_sel:DWORD dst_unused:UNUSED_PAD src0_sel:DWORD src1_sel:WORD_0\n" \
  "ds_read_b128 v[100:103], v64\n" \
  "v_lshlrev_b32_sdwa v65, 4, v236 dst_sel:DWORD dst_unused:UNUSED_PAD src0_sel:DWORD src1_sel:WORD_1\n" \
  "ds_read_b128 v[104:107], v65\n" \
  "global_load_dwordx4 v[216:219], v188, s[56:57] offset:2048 sc1\n" \
  "global_load_dwordx4 v[220:223], v188, s[56:57] offset:3072 sc1\n" \
  "s_waitcnt lgkmcnt(10)\n" \
  "v_smfmac_f32_16x16x64_bf16 v[28:31], v[180:183], v[108:115], v192\n" \
  "v_lshlrev_b32_sdwa v66, 4, v237 dst_sel:DWORD dst_unused:UNUSED_PAD src0_sel:DWORD src1_sel:WORD_0\n" \
  "ds_read_b128 v[108:111], v66\n" \
  "v_lshlrev_b32_sdwa v67, 4, v237 dst_sel:DWORD dst_unused:UNUSED_PAD src0_sel:DWORD src1_sel:WORD_1\n" \
  "ds_read_b128 v[112:115], v67\n" \
  "s_waitcnt lgkmcnt(10)\n" \
  "v_smfmac_f32_16x16x64_bf16 v[32:35], v[180:183], v[68:75], v192\n" \
  "v_lshlrev_b32_sdwa v64, 4, v238 dst_sel:DWORD dst_unused:UNUSED_PAD src0_sel:DWORD src1_sel:WORD_0\n" \
  "ds_read_b128 v[68:71], v64\n" \
  "v_lshlrev_b32_sdwa v65, 4, v238 dst_sel:DWORD dst_unused:UNUSED_PAD src0_sel:DWORD src1_sel:WORD_1\n" \
  "ds_read_b128 v[72:75], v65\n" \
  "s_waitcnt lgkmcnt(10)\n" \
  "v_smfmac_f32_16x16x64_bf16 v[36:39], v[180:183], v[76:83], v192\n" \
  "v_lshlrev_b32_sdwa v66, 4, v239 dst_sel:DWORD dst_unused:UNUSED_PAD src0_sel:DWORD src1_sel:WORD_0\n" \
  "ds_read_b128 v[76:79], v66\n" \
  "v_lshlrev_b32_sdwa v67, 4, v239 dst_sel:DWORD dst_unused:UNUSED_PAD src0_sel:DWORD src1_sel:WORD_1\n" \
  "ds_read_b128 v[80:83], v67\n" \
  "s_add_u32 s56, s56, 32768\n" \
  "s_addc_u32 s57, s57, 0\n" \
  "s_add_u32 s54, s54, 64000\n" \
  "s_addc_u32 s55, s55, 0\n" \
  "s_waitcnt lgkmcnt(10)\n" \
  "v_smfmac_f32_16x16x64_bf16 v[40:43], v[180:183], v[84:91], v192\n" \
  "s_waitcnt lgkmcnt(8)\n" \
  "v_smfmac_f32_16x16x64_bf16 v[44:47], v[180:183], v[92:99], v192\n" \
  "s_waitcnt vmcnt(8)\n" \
  "s_barrier\n" \
  "v_lshlrev_b32_sdwa v64, 4, v240 dst_sel:DWORD dst_unused:UNUSED_PAD src0_sel:DWORD src1_sel:WORD_0\n" \
  "ds_read_b128 v[84:87], v64\n" \
  "v_lshlrev_b32_sdwa v65, 4, v240 dst_sel:DWORD dst_unused:UNUSED_PAD src0_sel:DWORD src1_sel:WORD_1\n" \
  "ds_read_b128 v[88:91], v65\n" \
  "v_lshlrev_b32_sdwa v66, 4, v241 dst_sel:DWORD dst_unused:UNUSED_PAD src0_sel:DWORD src1_sel:WORD_0\n" \
  "ds_read_b128 v[92:95], v66\n" \
  "v_lshlrev_b32_sdwa v67, 4, v241 dst_sel:DWORD dst_unused:UNUSED_PAD src0_sel:DWORD src1_sel:WORD_1\n" \
  "ds_read_b128 v[96:99], v67\n" \
  "s_waitcnt lgkmcnt(10)\n" \
  "v_smfmac_f32_16x16x64_bf16 v[48:51], v[180:183], v[100:107], v192\n" \
  "s_waitcnt lgkmcnt(8)\n" \
  "v_smfmac_f32_16x16x64_bf16 v[52:55], v[180:183], v[108:115], v192\n" \
  "s_waitcnt lgkmcnt(6)\n" \
  "v_smfmac_f32_16x16x64_bf16 v[56:59], v[180:183], v[68:75], v192\n" \
  "s_waitcnt lgkmcnt(4)\n" \
  "v_smfmac_f32_16x16x64_bf16 v[60:63], v[180:183], v[76:83], v192\n" \
  "s_barrier\n" \
  "s_mov_b32 m0, s58\n" \
  "s_add_u32 s35, s58, 0x8000\n" \
  "global_load_lds_dwordx4 v188, s[54:55] sc1\n" \
  "global_load_lds_dwordx4 v156, s[54:55] offset:1024 sc1\n" \
  "v_lshlrev_b32_sdwa v64, 4, v242 dst_sel:DWORD dst_unused:UNUSED_PAD src0_sel:DWORD src1_sel:WORD_0\n" \
  "ds_read_b128 v[100:103], v64\n" \
  "v_lshlrev_b32_sdwa v65, 4, v242 dst_sel:DWORD dst_unused:UNUSED_PAD src0_sel:DWORD src1_sel:WORD_1\n" \
  "ds_read_b128 v[104:107], v65\n" \
  "v_lshlrev_b32_sdwa v66, 4, v243 dst_sel:DWORD dst_unused:UNUSED_PAD src0_sel:DWORD src1_sel:WORD_0\n" \
  "ds_read_b128 v[108:111], v66\n" \
  "v_lshlrev_b32_sdwa v67, 4, v243 dst_sel:DWORD dst_unused:UNUSED_PAD src0_sel:DWORD src1_sel:WORD_1\n" \
  "ds_read_b128 v[112:115], v67\n" \
  "s_add_u32 m0, s58, 0x4000\n" \
  "s_cmp_lt_u32 s35, 0x28000\n" \
  "global_load_lds_dwordx4 v189, s[54:55] sc1\n" \
  "global_load_lds_dwordx4 v157, s[54:55] offset:1024 sc1\n" \
  "s_cselect_b32 s58, s35, s59\n" \
  "v_lshlrev_b32_sdwa v64, 4, v244 dst_sel:DWORD dst_unused:UNUSED_PAD src0_sel:DWORD src1_sel:WORD_0\n" \
  "ds_read_b128 v[68:71], v64\n" \
  "v_lshlrev_b32_sdwa v65, 4, v244 dst_sel:DWORD dst_unused:UNUSED_PAD src0_sel:DWORD src1_sel:WORD_1\n" \
  "ds_read_b128 v[72:75], v65\n" \
  "v_lshlrev_b32_sdwa v66, 4, v245 dst_sel:DWORD dst_unused:UNUSED_PAD src0_sel:DWORD src1_sel:WORD_0\n" \
  "ds_read_b128 v[76:79], v66\n" \
  "v_lshlrev_b32_sdwa v67, 4, v245 dst_sel:DWORD dst_unused:UNUSED_PAD src0_sel:DWORD src1_sel:WORD_1\n" \
  "ds_read_b128 v[80:83], v67\n" \
  "s_waitcnt lgkmcnt(10)\n" \
  "v_smfmac_f32_16x16x64_bf16 v[0:3], v[180:183], v[84:91], v192\n" \
  "v_lshlrev_b32_sdwa v64, 4, v246 dst_sel:DWORD dst_unused:UNUSED_PAD src0_sel:DWORD src1_sel:WORD_0\n" \
  "ds_read_b128 v[84:87], v64\n" \
  "v_lshlrev_b32_sdwa v65, 4, v246 dst_sel:DWORD dst_unused:UNUSED_PAD src0_sel:DWORD src1_sel:WORD_1\n" \
  "ds_read_b128 v[88:91], v65\n" \
  "s_mov_b32 m0, s58\n" \
  "s_add_u32 s35, s58, 0x8000\n" \
  "global_load_lds_dwordx4 v190, s[54:55] sc1\n" \
  "global_load_lds_dwordx4 v158, s[54:55] offset:1024 sc1\n" \
  "s_waitcnt lgkmcnt(10)\n" \
  "v_smfmac_f32_16x16x64_bf16 v[4:7], v[180:183], v[92:99], v192\n" \
  "v_lshlrev_b32_sdwa v66, 4, v247 dst_sel:DWORD dst_unused:UNUSED_PAD src0_sel:DWORD src1_sel:WORD_0\n" \
  "ds_read_b128 v[92:95], v66\n" \
  "v_lshlrev_b32_sdwa v67, 4, v247 dst_sel:DWORD dst_unused:UNUSED_PAD src0_sel:DWORD src1_sel:WORD_1\n" \
  "ds_read_b128 v[96:99], v67\n" \
  "s_waitcnt lgkmcnt(10)\n" \
  "v_smfmac_f32_16x16x64_bf16 v[8:11], v[180:183], v[100:107], v192\n" \
  "v_lshlrev_b32_sdwa v64, 4, v248 dst_sel:DWORD dst_unused:UNUSED_PAD src0_sel:DWORD src1_sel:WORD_0\n" \
  "ds_read_b128 v[100:103], v64\n" \
  "v_lshlrev_b32_sdwa v65, 4, v248 dst_sel:DWORD dst_unused:UNUSED_PAD src0_sel:DWORD src1_sel:WORD_1\n" \
  "ds_read_b128 v[104:107], v65\n" \
  "s_add_u32 m0, s58, 0x4000\n" \
  "s_cmp_lt_u32 s35, 0x28000\n" \
  "global_load_lds_dwordx4 v191, s[54:55] sc1\n" \
  "global_load_lds_dwordx4 v159, s[54:55] offset:1024 sc1\n" \
  "s_cselect_b32 s58, s35, s59\n" \
  "s_waitcnt lgkmcnt(10)\n" \
  "v_smfmac_f32_16x16x64_bf16 v[12:15], v[180:183], v[108:115], v192\n" \
  "v_lshlrev_b32_sdwa v66, 4, v249 dst_sel:DWORD dst_unused:UNUSED_PAD src0_sel:DWORD src1_sel:WORD_0\n" \
  "ds_read_b128 v[108:111], v66\n" \
  "v_lshlrev_b32_sdwa v67, 4, v249 dst_sel:DWORD dst_unused:UNUSED_PAD src0_sel:DWORD src1_sel:WORD_1\n" \
  "ds_read_b128 v[112:115], v67\n" \
  "s_waitcnt lgkmcnt(10)\n" \
  "v_smfmac_f32_16x16x64_bf16 v[16:19], v[180:183], v[68:75], v192\n" \
  "v_lshlrev_b32_sdwa v64, 4, v250 dst_sel:DWORD dst_unused:UNUSED_PAD src0_sel:DWORD src1_sel:WORD_0\n" \
  "ds_read_b128 v[68:71], v64\n" \
  "v_lshlrev_b32_sdwa v65, 4, v250 dst_sel:DWORD dst_unused:UNUSED_PAD src0_sel:DWORD src1_sel:WORD_1\n" \
  "ds_read_b128 v[72:75], v65\n" \
  "global_load_dwordx4 v[224:227], v188, s[56:57] sc1\n" \
  "global_load_dwordx4 v[228:231], v188, s[56:57] offset:1024 sc1\n" \
  "s_waitcnt lgkmcnt(10)\n" \
  "v_smfmac_f32_16x16x64_bf16 v[20:23], v[180:183], v[76:83], v192\n" \
  "v_lshlrev_b32_sdwa v66, 4, v251 dst_sel:DWORD dst_unused:UNUSED_PAD src0_sel:DWORD src1_sel:WORD_0\n" \
  "ds_read_b128 v[76:79], v66\n" \
  "v_lshlrev_b32_sdwa v67, 4, v251 dst_sel:DWORD dst_unused:UNUSED_PAD src0_sel:DWORD src1_sel:WORD_1\n" \
  "ds_read_b128 v[80:83], v67\n" \
  "s_waitcnt lgkmcnt(10)\n" \
  "v_smfmac_f32_16x16x64_bf16 v[24:27], v[180:183], v[84:91], v192\n" \
  "v_lshlrev_b32_sdwa v64, 4, v252 dst_sel:DWORD dst_unused:UNUSED_PAD src0_sel:DWORD src1_sel:WORD_0\n" \
  "ds_read_b128 v[84:87], v64\n" \
  "v_lshlrev_b32_sdwa v65, 4, v252 dst_sel:DWORD dst_unused:UNUSED_PAD src0_sel:DWORD src1_sel:WORD_1\n" \
  "ds_read_b128 v[88:91], v65\n" \
  "global_load_dwordx4 v[232:235], v188, s[56:57] offset:2048 sc1\n" \
  "global_load_dwordx4 v[236:239], v188, s[56:57] offset:3072 sc1\n" \
  "s_waitcnt lgkmcnt(10)\n" \
  "v_smfmac_f32_16x16x64_bf16 v[28:31], v[180:183], v[92:99], v192\n" \
  "v_lshlrev_b32_sdwa v66, 4, v253 dst_sel:DWORD dst_unused:UNUSED_PAD src0_sel:DWORD src1_sel:WORD_0\n" \
  "ds_read_b128 v[92:95], v66\n" \
  "v_lshlrev_b32_sdwa v67, 4, v253 dst_sel:DWORD dst_unused:UNUSED_PAD src0_sel:DWORD src1_sel:WORD_1\n" \
  "ds_read_b128 v[96:99], v67\n" \
  "s_waitcnt lgkmcnt(10)\n" \
  "v_smfmac_f32_16x16x64_bf16 v[32:35], v[180:183], v[100:107], v192\n" \
  "v_lshlrev_b32_sdwa v64, 4, v254 dst_sel:DWORD dst_unused:UNUSED_PAD src0_sel:DWORD src1_sel:WORD_0\n" \
  "ds_read_b128 v[100:103], v64\n" \
  "v_lshlrev_b32_sdwa v65, 4, v254 dst_sel:DWORD dst_unused:UNUSED_PAD src0_sel:DWORD src1_sel:WORD_1\n" \
  "ds_read_b128 v[104:107], v65\n" \
  "s_waitcnt lgkmcnt(10)\n" \
  "v_smfmac_f32_16x16x64_bf16 v[36:39], v[180:183], v[108:115], v192\n" \
  "v_lshlrev_b32_sdwa v66, 4, v255 dst_sel:DWORD dst_unused:UNUSED_PAD src0_sel:DWORD src1_sel:WORD_0\n" \
  "ds_read_b128 v[108:111], v66\n" \
  "v_lshlrev_b32_sdwa v67, 4, v255 dst_sel:DWORD dst_unused:UNUSED_PAD src0_sel:DWORD src1_sel:WORD_1\n" \
  "ds_read_b128 v[112:115], v67\n" \
  "s_add_u32 s56, s56, 32768\n" \
  "s_addc_u32 s57, s57, 0\n" \
  "s_add_u32 s54, s54, 64000\n" \
  "s_addc_u32 s55, s55, 0\n" \
  "s_lshl3_add_u32 s36, s27, s51\n" \
  "s_cmp_eq_u32 s36, 37\n" \
  "s_waitcnt lgkmcnt(10)\n" \
  "v_smfmac_f32_16x16x64_bf16 v[40:43], v[180:183], v[68:75], v192\n" \
  "s_waitcnt lgkmcnt(8)\n" \
  "v_smfmac_f32_16x16x64_bf16 v[44:47], v[180:183], v[76:83], v192\n" \
  "s_waitcnt vmcnt(8)\n" \
  "s_cbranch_scc1 LG_pol\n" \
  "LG_rt2:\n" \
  "s_barrier\n" \
  "v_lshlrev_b32_sdwa v64, 4, v208 dst_sel:DWORD dst_unused:UNUSED_PAD src0_sel:DWORD src1_sel:WORD_0\n" \
  "ds_read_b128 v[68:71], v64\n" \
  "v_lshlrev_b32_sdwa v65, 4, v208 dst_sel:DWORD dst_unused:UNUSED_PAD src0_sel:DWORD src1_sel:WORD_1\n" \
  "ds_read_b128 v[72:75], v65\n" \
  "v_lshlrev_b32_sdwa v66, 4, v209 dst_sel:DWORD dst_unused:UNUSED_PAD src0_sel:DWORD src1_sel:WORD_0\n" \
  "ds_read_b128 v[76:79], v66\n" \
  "v_lshlrev_b32_sdwa v67, 4, v209 dst_sel:DWORD dst_unused:UNUSED_PAD src0_sel:DWORD src1_sel:WORD_1\n" \
  "ds_read_b128 v[80:83], v67\n" \
  "s_waitcnt lgkmcnt(10)\n" \
  "v_smfmac_f32_16x16x64_bf16 v[48:51], v[180:183], v[84:91], v192\n" \
  "s_waitcnt lgkmcnt(8)\n" \
  "v_smfmac_f32_16x16x64_bf16 v[52:55], v[180:183], v[92:99], v192\n" \
  "s_waitcnt lgkmcnt(6)\n" \
  "v_smfmac_f32_16x16x64_bf16 v[56:59], v[180:183], v[100:107], v192\n" \
  "s_waitcnt lgkmcnt(4)\n" \
  "v_smfmac_f32_16x16x64_bf16 v[60:63], v[180:183], v[108:115], v192\n" \
  "s_barrier\n" \
  "s_mov_b32 m0, s58\n" \
  "s_add_u32 s35, s58, 0x8000\n" \
  "global_load_lds_dwordx4 v188, s[54:55] sc1\n" \
  "global_load_lds_dwordx4 v156, s[54:55] offset:1024 sc1\n" \
  "v_lshlrev_b32_sdwa v64, 4, v210 dst_sel:DWORD dst_unused:UNUSED_PAD src0_sel:DWORD src1_sel:WORD_0\n" \
  "ds_read_b128 v[84:87], v64\n" \
  "v_lshlrev_b32_sdwa v65, 4, v210 dst_sel:DWORD dst_unused:UNUSED_PAD src0_sel:DWORD src1_sel:WORD_1\n" \
  "ds_read_b128 v[88:91], v65\n" \
  "v_lshlrev_b32_sdwa v66, 4, v211 dst_sel:DWORD dst_unused:UNUSED_PAD src0_sel:DWORD src1_sel:WORD_0\n" \
  "ds_read_b128 v[92:95], v66\n" \
  "v_lshlrev_b32_sdwa v67, 4, v211 dst_sel:DWORD dst_unused:UNUSED_PAD src0_sel:DWORD src1_sel:WORD_1\n" \
  "ds_read_b128 v[96:99], v67\n" \
  "s_add_u32 m0, s58, 0x4000\n" \
  "s_cmp_lt_u32 s35, 0x28000\n" \
  "global_load_lds_dwordx4 v189, s[54:55] sc1\n" \
  "global_load_lds_dwordx4 v157, s[54:55] offset:1024 sc1\n" \
  "s_cselect_b32 s58, s35, s59\n" \
  "v_lshlrev_b32_sdwa v64, 4, v212 dst_sel:DWORD dst_unused:UNUSED_PAD src0_sel:DWORD src1_sel:WORD_0\n" \
  "ds_read_b128 v[100:103], v64\n" \
  "v_lshlrev_b32_sdwa v65, 4, v212 dst_sel:DWORD dst_unused:UNUSED_PAD src0_sel:DWORD src1_sel:WORD_1\n" \
  "ds_read_b128 v[104:107], v65\n" \
  "v_lshlrev_b32_sdwa v66, 4, v213 dst_sel:DWORD dst_unused:UNUSED_PAD src0_sel:DWORD src1_sel:WORD_0\n" \
  "ds_read_b128 v[108:111], v66\n" \
  "v_lshlrev_b32_sdwa v67, 4, v213 dst_sel:DWORD dst_unused:UNUSED_PAD src0_sel:DWORD src1_sel:WORD_1\n" \
  "ds_read_b128 v[112:115], v67\n" \
  "s_waitcnt lgkmcnt(10)\n" \
  "v_smfmac_f32_16x16x64_bf16 v[0:3], v[180:183], v[68:75], v192\n" \
  "v_lshlrev_b32_sdwa v64, 4, v214 dst_sel:DWORD dst_unused:UNUSED_PAD src0_sel:DWORD src1_sel:WORD_0\n" \
  "ds_read_b128 v[68:71], v64\n" \
  "v_lshlrev_b32_sdwa v65, 4, v214 dst_sel:DWORD dst_unused:UNUSED_PAD src0_sel:DWORD src1_sel:WORD_1\n" \
  "ds_read_b128 v[72:75], v65\n" \
  "s_mov_b32 m0, s58\n" \
  "s_add_u32 s35, s58, 0x8000\n" \
  "global_load_lds_dwordx4 v190, s[54:55] sc1\n" \
  "global_load_lds_dwordx4 v158, s[54:55] offset:1024 sc1\n" \
  "s_waitcnt lgkmcnt(10)\n" \
  "v_smfmac_f32_16x16x64_bf16 v[4:7], v[180:183], v[76:83], v192\n" \
  "v_lshlrev_b32_sdwa v66, 4, v215 dst_sel:DWORD dst_unused:UNUSED_PAD src0_sel:DWORD src1_sel:WORD_0\n" \
  "ds_read_b128 v[76:79], v66\n" \
  "v_lshlrev_b32_sdwa v67, 4, v215 dst_sel:DWORD dst_unused:UNUSED_PAD src0_sel:DWORD src1_sel:WORD_1\n" \
  "ds_read_b128 v[80:83], v67\n" \
  "s_waitcnt lgkmcnt(10)\n" \
  "v_smfmac_f32_16x16x64_bf16 v[8:11], v[180:183], v[84:91], v192\n" \
  "v_lshlrev_b32_sdwa v64, 4, v216 dst_sel:DWORD dst_unused:UNUSED_PAD src0_sel:DWORD src1_sel:WORD_0\n" \
  "ds_read_b128 v[84:87], v64\n" \
  "v_lshlrev_b32_sdwa v65, 4, v216 dst_sel:DWORD dst_unused:UNUSED_PAD src0_sel:DWORD src1_sel:WORD_1\n" \
  "ds_read_b128 v[88:91], v65\n" \
  "s_add_u32 m0, s58, 0x4000\n" \
  "s_cmp_lt_u32 s35, 0x28000\n" \
  "global_load_lds_dwordx4 v191, s[54:55] sc1\n" \
  "global_load_lds_dwordx4 v159, s[54:55] offset:1024 sc1\n" \
  "s_cselect_b32 s58, s35, s59\n" \
  "s_waitcnt lgkmcnt(10)\n" \
  "v_smfmac_f32_16x16x64_bf16 v[12:15], v[180:183], v[92:99], v192\n" \
  "v_lshlrev_b32_sdwa v66, 4, v217 dst_sel:DWORD dst_unused:UNUSED_PAD src0_sel:DWORD src1_sel:WORD_0\n" \
  "ds_read_b128 v[92:95], v66\n" \
  "v_lshlrev_b32_sdwa v67, 4, v217 dst_sel:DWORD dst_unused:UNUSED_PAD src0_sel:DWORD src1_sel:WORD_1\n" \
  "ds_read_b128 v[96:99], v67\n" \
  "s_waitcnt lgkmcnt(10)\n" \
  "v_smfmac_f32_16x16x64_bf16 v[16:19], v[180:183], v[100:107], v192\n" \
  "v_lshlrev_b32_sdwa v64, 4, v218 dst_sel:DWORD dst_unused:UNUSED_PAD src0_sel:DWORD src1_sel:WORD_0\n" \
  "ds_read_b128 v[100:103], v64\n" \
  "v_lshlrev_b32_sdwa v65, 4, v218 dst_sel:DWORD dst_unused:UNUSED_PAD src0_sel:DWORD src1_sel:WORD_1\n" \
  "ds_read_b128 v[104:107], v65\n" \
  "global_load_dwordx4 v[240:243], v188, s[56:57] sc1\n" \
  "global_load_dwordx4 v[244:247], v188, s[56:57] offset:1024 sc1\n" \
  "s_waitcnt lgkmcnt(10)\n" \
  "v_smfmac_f32_16x16x64_bf16 v[20:23], v[180:183], v[108:115], v192\n" \
  "v_lshlrev_b32_sdwa v66, 4, v219 dst_sel:DWORD dst_unused:UNUSED_PAD src0_sel:DWORD src1_sel:WORD_0\n" \
  "ds_read_b128 v[108:111], v66\n" \
  "v_lshlrev_b32_sdwa v67, 4, v219 dst_sel:DWORD dst_unused:UNUSED_PAD src0_sel:DWORD src1_sel:WORD_1\n" \
  "ds_read_b128 v[112:115], v67\n" \
  "s_waitcnt lgkmcnt(10)\n" \
  "v_smfmac_f32_16x16x64_bf16 v[24:27], v[180:183], v[68:75], v192\n" \
  "v_lshlrev_b32_sdwa v64, 4, v220 dst_sel:DWORD dst_unused:UNUSED_PAD src0_sel:DWORD src1_sel:WORD_0\n" \
  "ds_read_b128 v[68:71], v64\n" \
  "v_lshlrev_b32_sdwa v65, 4, v220 dst_sel:DWORD dst_unused:UNUSED_PAD src0_sel:DWORD src1_sel:WORD_1\n" \
  "ds_read_b128 v[72:75], v65\n" \
  "global_load_dwordx4 v[248:251], v188, s[56:57] offset:2048 sc1\n" \
  "global_load_dwordx4 v[252:255], v188, s[56:57] offset:3072 sc1\n" \
  "s_waitcnt lgkmcnt(10)\n" \
  "v_smfmac_f32_16x16x64_bf16 v[28:31], v[180:183], v[76:83], v192\n" \
  "v_lshlrev_b32_sdwa v66, 4, v221 dst_sel:DWORD dst_unused:UNUSED_PAD src0_sel:DWORD src1_sel:WORD_0\n" \
  "ds_read_b128 v[76:79], v66\n" \
  "v_lshlrev_b32_sdwa v67, 4, v221 dst_sel:DWORD dst_unused:UNUSED_PAD src0_sel:DWORD src1_sel:WORD_1\n" \
  "ds_read_b128 v[80:83], v67\n" \
  "s_waitcnt lgkmcnt(10)\n" \
  "v_smfmac_f32_16x16x64_bf16 v[32:35], v[180:183], v[84:91], v192\n" \
  "v_lshlrev_b32_sdwa v64, 4, v222 dst_sel:DWORD dst_unused:UNUSED_PAD src0_sel:DWORD src1_sel:WORD_0\n" \
  "ds_read_b128 v[84:87], v64\n" \
  "v_lshlrev_b32_sdwa v65, 4, v222 dst_sel:DWORD dst_unused:UNUSED_PAD src0_sel:DWORD src1_sel:WORD_1\n" \
  "ds_read_b128 v[88:91], v65\n" \
  "s_waitcnt lgkmcnt(10)\n" \
  "v_smfmac_f32_16x16x64_bf16 v[36:39], v[180:183], v[92:99], v192\n" \
  "v_lshlrev_b32_sdwa v66, 4, v223 dst_sel:DWORD dst_unused:UNUSED_PAD src0_sel:DWORD src1_sel:WORD_0\n" \
  "ds_read_b128 v[92:95], v66\n" \
  "v_lshlrev_b32_sdwa v67, 4, v223 dst_sel:DWORD dst_unused:UNUSED_PAD src0_sel:DWORD src1_sel:WORD_1\n" \
  "ds_read_b128 v[96:99], v67\n" \
  "s_add_u32 s56, s56, 32768\n" \
  "s_addc_u32 s57, s57, 0\n" \
  "s_add_u32 s54, s54, 64000\n" \
  "s_addc_u32 s55, s55, 0\n" \
  "s_sub_u32 s51, s51, 1\n" \
  "s_cmp_lg_u32 s51, 0\n" \
  "s_cbranch_scc1 LG_loop\n" \
  "s_waitcnt lgkmcnt(10)\n" \
  "v_smfmac_f32_16x16x64_bf16 v[40:43], v[180:183], v[100:107], v192\n" \
  "s_waitcnt lgkmcnt(8)\n" \
  "v_smfmac_f32_16x16x64_bf16 v[44:47], v[180:183], v[108:115], v192\n" \
  "s_waitcnt vmcnt(8)\n" \
  "s_barrier\n" \
  "v_lshlrev_b32_sdwa v64, 4, v224 dst_sel:DWORD dst_unused:UNUSED_PAD src0_sel:DWORD src1_sel:WORD_0\n" \
  "ds_read_b128 v[100:103], v64\n" \
  "v_lshlrev_b32_sdwa v65, 4, v224 dst_sel:DWORD dst_unused:UNUSED_PAD src0_sel:DWORD src1_sel:WORD_1\n" \
  "ds_read_b128 v[104:107], v65\n" \
  "v_lshlrev_b32_sdwa v66, 4, v225 dst_sel:DWORD dst_unused:UNUSED_PAD src0_sel:DWORD src1_sel:WORD_0\n" \
  "ds_read_b128 v[108:111], v66\n" \
  "v_lshlrev_b32_sdwa v67, 4, v225 dst_sel:DWORD dst_unused:UNUSED_PAD src0_sel:DWORD src1_sel:WORD_1\n" \
  "ds_read_b128 v[112:115], v67\n" \
  "s_waitcnt lgkmcnt(10)\n" \
  "v_smfmac_f32_16x16x64_bf16 v[48:51], v[180:183], v[68:75], v192\n" \
  "s_waitcnt lgkmcnt(8)\n" \
  "v_smfmac_f32_16x16x64_bf16 v[52:55], v[180:183], v[76:83], v192\n" \
  "s_waitcnt lgkmcnt(6)\n" \
  "v_smfmac_f32_16x16x64_bf16 v[56:59], v[180:183], v[84:91], v192\n" \
  "s_waitcnt lgkmcnt(4)\n" \
  "v_smfmac_f32_16x16x64_bf16 v[60:63], v[180:183], v[92:99], v192\n" \
  "s_barrier\n" \
  "s_mov_b32 m0, s58\n" \
  "s_add_u32 s35, s58, 0x8000\n" \
  "global_load_lds_dwordx4 v188, s[54:55] sc1\n" \
  "global_load_lds_dwordx4 v156, s[54:55] offset:1024 sc1\n" \
  "v_lshlrev_b32_sdwa v64, 4, v226 dst_sel:DWORD dst_unused:UNUSED_PAD src0_sel:DWORD src1_sel:WORD_0\n" \
  "ds_read_b128 v[68:71], v64\n" \
  "v_lshlrev_b32_sdwa v65, 4, v226 dst_sel:DWORD dst_unused:UNUSED_PAD src0_sel:DWORD src1_sel:WORD_1\n" \
  "ds_read_b128 v[72:75], v65\n" \
  "v_lshlrev_b32_sdwa v66, 4, v227 dst_sel:DWORD dst_unused:UNUSED_PAD src0_sel:DWORD src1_sel:WORD_0\n" \
  "ds_read_b128 v[76:79], v66\n" \
  "v_lshlrev_b32_sdwa v67, 4, v227 dst_sel:DWORD dst_unused:UNUSED_PAD src0_sel:DWORD src1_sel:WORD_1\n" \
  "ds_read_b128 v[80:83], v67\n" \
  "s_add_u32 m0, s58, 0x4000\n" \
  "s_cmp_lt_u32 s35, 0x28000\n" \
  "global_load_lds_dwordx4 v189, s[54:55] sc1\n" \
  "global_load_lds_dwordx4 v157, s[54:55] offset:1024 sc1\n" \
  "s_cselect_b32 s58, s35, s59\n" \
  "v_lshlrev_b32_sdwa v64, 4, v228 dst_sel:DWORD dst_unused:UNUSED_PAD src0_sel:DWORD src1_sel:WORD_0\n" \
  "ds_read_b128 v[84:87], v64\n" \
  "v_lshlrev_b32_sdwa v65, 4, v228 dst_sel:DWORD dst_unused:UNUSED_PAD src0_sel:DWORD src1_sel:WORD_1\n" \
  "ds_read_b128 v[88:91], v65\n" \
  "v_lshlrev_b32_sdwa v66, 4, v229 dst_sel:DWORD dst_unused:UNUSED_PAD src0_sel:DWORD src1_sel:WORD_0\n" \
  "ds_read_b128 v[92:95], v66\n" \
  "v_lshlrev_b32 v120, 16, v206\n" \
  "v_and_b32 v121, 0xffff0000, v206\n" \
  "v_lshlrev_b32 v122, 16, v207\n" \
  "v_and_b32 v123, 0xffff0000, v207\n" \
  "v_rcp_f32 v120, v120\n" \
  "v_rcp_f32 v121, v121\n" \
  "v_rcp_f32 v122, v122\n" \
  "v_rcp_f32 v123, v123\n" \
  "s_mov_b32 s52, 0x7f000000\n" \
  "v_med3_f32 v120, v120, s52, -s52\n" \
  "v_med3_f32 v121, v121, s52, -s52\n" \
  "v_med3_f32 v122, v122, s52, -s52\n" \
  "v_med3_f32 v123, v123, s52, -s52\n" \
  "v_mov_b32 v124, 0xc0135761\n" \
  "v_mov_b32 v125, 0xc0135761\n" \
  "s_mov_b32 s52, 0xbdd2d3e8\n" \
  "s_mov_b32 s53, s52\n" \
  "s_lshl2_add_u32 s35, s33, s34\n" \
  "s_lshl_b32 s35, s35, 14\n" \
  "s_add_u32 s35, s35, s59\n" \
  "s_add_u32 s35, s35, 105676800\n" \
  "s_add_u32 s40, s22, s35\n" \
  "s_addc_u32 s41, s23, 0\n" \
  "v_lshlrev_b32_sdwa v67, 4, v229 dst_sel:DWORD dst_unused:UNUSED_PAD src0_sel:DWORD src1_sel:WORD_1\n" \
  "ds_read_b128 v[96:99], v67\n" \
  "s_waitcnt lgkmcnt(10)\n" \
  "v_smfmac_f32_16x16x64_bf16 v[0:3], v[180:183], v[100:107], v192\n" \
  "v_lshlrev_b32_sdwa v64, 4, v230 dst_sel:DWORD dst_unused:UNUSED_PAD src0_sel:DWORD src1_sel:WORD_0\n" \
  "ds_read_b128 v[100:103], v64\n" \
  "v_lshlrev_b32_sdwa v65, 4, v230 dst_sel:DWORD dst_unused:UNUSED_PAD src0_sel:DWORD src1_sel:WORD_1\n" \
  "ds_read_b128 v[104:107], v65\n" \
  "s_mov_b32 m0, s58\n" \
  "s_add_u32 s35, s58, 0x8000\n" \
  "global_load_lds_dwordx4 v190, s[54:55] sc1\n" \
  "global_load_lds_dwordx4 v158, s[54:55] offset:1024 sc1\n" \
  "s_waitcnt lgkmcnt(10)\n" \
  "v_smfmac_f32_16x16x64_bf16 v[4:7], v[180:183], v[108:115], v192\n" \
  "v_lshlrev_b32_sdwa v66, 4, v231 dst_sel:DWORD dst_unused:UNUSED_PAD src0_sel:DWORD src1_sel:WORD_0\n" \
  "ds_read_b128 v[108:111], v66\n" \
  "v_lshlrev_b32_sdwa v67, 4, v231 dst_sel:DWORD dst_unused:UNUSED_PAD src0_sel:DWORD src1_sel:WORD_1\n" \
  "ds_read_b128 v[112:115], v67\n" \
  "s_waitcnt lgkmcnt(10)\n" \
  "v_smfmac_f32_16x16x64_bf16 v[8:11], v[180:183], v[68:75], v192\n" \
  "v_lshlrev_b32_sdwa v64, 4, v232 dst_sel:DWORD dst_unused:UNUSED_PAD src0_sel:DWORD src1_sel:WORD_0\n" \
  "ds_read_b128 v[68:71], v64\n" \
  "v_lshlrev_b32_sdwa v65, 4, v232 dst_sel:DWORD dst_unused:UNUSED_PAD src0_sel:DWORD src1_sel:WORD_1\n" \
  "ds_read_b128 v[72:75], v65\n" \
  "s_add_u32 m0, s58, 0x4000\n" \
  "s_cmp_lt_u32 s35, 0x28000\n" \
  "global_load_lds_dwordx4 v191, s[54:55] sc1\n" \
  "global_load_lds_dwordx4 v159, s[54:55] offset:1024 sc1\n" \
  "s_cselect_b32 s58, s35, s59\n" \
  "s_waitcnt lgkmcnt(10)\n" \
  "v_smfmac_f32_16x16x64_bf16 v[12:15], v[180:183], v[76:83], v192\n" \
  "v_lshlrev_b32_sdwa v66, 4, v233 dst_sel:DWORD dst_unused:UNUSED_PAD src0_sel:DWORD src1_sel:WORD_0\n" \
  "ds_read_b128 v[76:79], v66\n" \
  "v_lshlrev_b32_sdwa v67, 4, v233 dst_sel:DWORD dst_unused:UNUSED_PAD src0_sel:DWORD src1_sel:WORD_1\n" \
  "ds_read_b128 v[80:83], v67\n" \
  "s_waitcnt lgkmcnt(10)\n" \
  "v_smfmac_f32_16x16x64_bf16 v[16:19], v[180:183], v[84:91], v192\n" \
  "v_lshlrev_b32_sdwa v64, 4, v234 dst_sel:DWORD dst_unused:UNUSED_PAD src0_sel:DWORD src1_sel:WORD_0\n" \
  "ds_read_b128 v[84:87], v64\n" \
  "v_lshlrev_b32_sdwa v65, 4, v234 dst_sel:DWORD dst_unused:UNUSED_PAD src0_sel:DWORD src1_sel:WORD_1\n" \
  "ds_read_b128 v[88:91], v65\n" \
  "global_load_dwordx4 v[208:211], v188, s[56:57] sc1\n" \
  "global_load_dwordx4 v[212:215], v188, s[56:57] offset:1024 sc1\n" \
  "s_waitcnt lgkmcnt(10)\n" \
  "v_smfmac_f32_16x16x64_bf16 v[20:23], v[180:183], v[92:99], v192\n" \
  "v_lshlrev_b32_sdwa v66, 4, v235 dst_sel:DWORD dst_unused:UNUSED_PAD src0_sel:DWORD src1_sel:WORD_0\n" \
  "ds_read_b128 v[92:95], v66\n" \
  "v_lshlrev_b32_sdwa v67, 4, v235 dst_sel:DWORD dst_unused:UNUSED_PAD src0_sel:DWORD src1_sel:WORD_1\n" \
  "ds_read_b128 v[96:99], v67\n" \
  "s_waitcnt lgkmcnt(10)\n" \
  "v_smfmac_f32_16x16x64_bf16 v[24:27], v[180:183], v[100:107], v192\n" \
  "v_lshlrev_b32_sdwa v64, 4, v236 dst_sel:DWORD dst_unused:UNUSED_PAD src0_sel:DWORD src1_sel:WORD_0\n" \
  "ds_read_b128 v[100:103], v64\n" \
  "v_lshlrev_b32_sdwa v65, 4, v236 dst_sel:DWORD dst_unused:UNUSED_PAD src0_sel:DWORD src1_sel:WORD_1\n" \
  "ds_read_b128 v[104:107], v65\n" \
  "global_load_dwordx4 v[216:219], v188, s[56:57] offset:2048 sc1\n" \
  "global_load_dwordx4 v[220:223], v188, s[56:57] offset:3072 sc1\n" \
  "s_waitcnt lgkmcnt(10)\n" \
  "v_smfmac_f32_16x16x64_bf16 v[28:31], v[180:183], v[108:115], v192\n" \
  "v_lshlrev_b32_sdwa v66, 4, v237 dst_sel:DWORD dst_unused:UNUSED_PAD src0_sel:DWORD src1_sel:WORD_0\n" \
  "ds_read_b128 v[108:111], v66\n" \
  "v_lshlrev_b32_sdwa v67, 4, v237 dst_sel:DWORD dst_unused:UNUSED_PAD src0_sel:DWORD src1_sel:WORD_1\n" \
  "ds_read_b128 v[112:115], v67\n" \
  "s_waitcnt lgkmcnt(10)\n" \
  "v_smfmac_f32_16x16x64_bf16 v[32:35], v[180:183], v[68:75], v192\n" \
  "v_lshlrev_b32_sdwa v64, 4, v238 dst_sel:DWORD dst_unused:UNUSED_PAD src0_sel:DWORD src1_sel:WORD_0\n" \
  "ds_read_b128 v[68:71], v64\n" \
  "v_lshlrev_b32_sdwa v65, 4, v238 dst_sel:DWORD dst_unused:UNUSED_PAD src0_sel:DWORD src1_sel:WORD_1\n" \
  "ds_read_b128 v[72:75], v65\n" \
  "s_waitcnt lgkmcnt(10)\n" \
  "v_smfmac_f32_16x16x64_bf16 v[36:39], v[180:183], v[76:83], v192\n" \
  "v_lshlrev_b32_sdwa v66, 4, v239 dst_sel:DWORD dst_unused:UNUSED_PAD src0_sel:DWORD src1_sel:WORD_0\n" \
  "ds_read_b128 v[76:79], v66\n" \
  "v_lshlrev_b32_sdwa v67, 4, v239 dst_sel:DWORD dst_unused:UNUSED_PAD src0_sel:DWORD src1_sel:WORD_1\n" \
  "ds_read_b128 v[80:83], v67\n" \
  "s_add_u32 s56, s56, 32768\n" \
  "s_addc_u32 s57, s57, 0\n" \
  "s_add_u32 s54, s54, 64000\n" \
  "s_addc_u32 s55, s55, 0\n" \
  "s_waitcnt lgkmcnt(10)\n" \
  "v_smfmac_f32_16x16x64_bf16 v[40:43], v[180:183], v[84:91], v192\n" \
  "s_waitcnt lgkmcnt(8)\n" \
  "v_smfmac_f32_16x16x64_bf16 v[44:47], v[180:183], v[92:99], v192\n" \
  "s_waitcnt vmcnt(8)\n" \
  "s_barrier\n" \
  "v_lshlrev_b32_sdwa v64, 4, v240 dst_sel:DWORD dst_unused:UNUSED_PAD src0_sel:DWORD src1_sel:WORD_0\n" \
  "ds_read_b128 v[84:87], v64\n" \
  "v_lshlrev_b32_sdwa v65, 4, v240 dst_sel:DWORD dst_unused:UNUSED_PAD src0_sel:DWORD src1_sel:WORD_1\n" \
  "ds_read_b128 v[88:91], v65\n" \
  "v_lshlrev_b32_sdwa v66, 4, v241 dst_sel:DWORD dst_unused:UNUSED_PAD src0_sel:DWORD src1_sel:WORD_0\n" \
  "ds_read_b128 v[92:95], v66\n" \
  "v_lshlrev_b32_sdwa v67, 4, v241 dst_sel:DWORD dst_unused:UNUSED_PAD src0_sel:DWORD src1_sel:WORD_1\n" \
  "ds_read_b128 v[96:99], v67\n" \
  "s_waitcnt lgkmcnt(10)\n" \
  "v_smfmac_f32_16x16x64_bf16 v[48:51], v[180:183], v[100:107], v192\n" \
  "s_waitcnt lgkmcnt(8)\n" \
  "v_smfmac_f32_16x16x64_bf16 v[52:55], v[180:183], v[108:115], v192\n" \
  "s_waitcnt lgkmcnt(6)\n" \
  "v_smfmac_f32_16x16x64_bf16 v[56:59], v[180:183], v[68:75], v192\n" \
  "s_waitcnt lgkmcnt(4)\n" \
  "v_smfmac_f32_16x16x64_bf16 v[60:63], v[180:183], v[76:83], v192\n" \
  "s_barrier\n" \
  "s_mov_b32 m0, s58\n" \
  "s_add_u32 s35, s58, 0x8000\n" \
  "global_load_lds_dwordx4 v188, s[54:55] sc1\n" \
  "global_load_lds_dwordx4 v156, s[54:55] offset:1024 sc1\n" \
  "s_add_u32 m0, s58, 0x4000\n" \
  "s_cmp_lt_u32 s35, 0x28000\n" \
  "global_load_lds_dwordx4 v189, s[54:55] sc1\n" \
  "global_load_lds_dwordx4 v157, s[54:55] offset:1024 sc1\n" \
  "s_cselect_b32 s58, s35, s59\n" \
  "v_lshlrev_b32_sdwa v64, 4, v242 dst_sel:DWORD dst_unused:UNUSED_PAD src0_sel:DWORD src1_sel:WORD_0\n" \
  "ds_read_b128 v[100:103], v64\n" \
  "v_lshlrev_b32_sdwa v65, 4, v242 dst_sel:DWORD dst_unused:UNUSED_PAD src0_sel:DWORD src1_sel:WORD_1\n" \
  "ds_read_b128 v[104:107], v65\n" \
  "v_lshlrev_b32_sdwa v66, 4, v243 dst_sel:DWORD dst_unused:UNUSED_PAD src0_sel:DWORD src1_sel:WORD_0\n" \
  "ds_read_b128 v[108:111], v66\n" \
  "v_lshlrev_b32_sdwa v67, 4, v243 dst_sel:DWORD dst_unused:UNUSED_PAD src0_sel:DWORD src1_sel:WORD_1\n" \
  "ds_read_b128 v[112:115], v67\n" \
  "v_lshlrev_b32_sdwa v64, 4, v244 dst_sel:DWORD dst_unused:UNUSED_PAD src0_sel:DWORD src1_sel:WORD_0\n" \
  "ds_read_b128 v[68:71], v64\n" \
  "v_lshlrev_b32_sdwa v65, 4, v244 dst_sel:DWORD dst_unused:UNUSED_PAD src0_sel:DWORD src1_sel:WORD_1\n" \
  "ds_read_b128 v[72:75], v65\n" \
  "v_lshlrev_b32_sdwa v66, 4, v245 dst_sel:DWORD dst_unused:UNUSED_PAD src0_sel:DWORD src1_sel:WORD_0\n" \
  "ds_read_b128 v[76:79], v66\n" \
  "v_lshlrev_b32_sdwa v67, 4, v245 dst_sel:DWORD dst_unused:UNUSED_PAD src0_sel:DWORD src1_sel:WORD_1\n" \
  "ds_read_b128 v[80:83], v67\n" \
  "s_waitcnt lgkmcnt(10)\n" \
  "v_smfmac_f32_16x16x64_bf16 v[0:3], v[180:183], v[84:91], v192\n" \
  "v_lshlrev_b32_sdwa v64, 4, v246 dst_sel:DWORD dst_unused:UNUSED_PAD src0_sel:DWORD src1_sel:WORD_0\n" \
  "ds_read_b128 v[84:87], v64\n" \
  "v_lshlrev_b32_sdwa v65, 4, v246 dst_sel:DWORD dst_unused:UNUSED_PAD src0_sel:DWORD src1_sel:WORD_1\n" \
  "ds_read_b128 v[88:91], v65\n" \
  "s_waitcnt lgkmcnt(10)\n" \
  "v_smfmac_f32_16x16x64_bf16 v[4:7], v[180:183], v[92:99], v192\n" \
  "v_lshlrev_b32_sdwa v66, 4, v247 dst_sel:DWORD dst_unused:UNUSED_PAD src0_sel:DWORD src1_sel:WORD_0\n" \
  "ds_read_b128 v[92:95], v66\n" \
  "v_lshlrev_b32_sdwa v67, 4, v247 dst_sel:DWORD dst_unused:UNUSED_PAD src0_sel:DWORD src1_sel:WORD_1\n" \
  "ds_read_b128 v[96:99], v67\n" \
  "s_waitcnt lgkmcnt(10)\n" \
  "v_smfmac_f32_16x16x64_bf16 v[8:11], v[180:183], v[100:107], v192\n" \
  "v_lshlrev_b32_sdwa v64, 4, v248 dst_sel:DWORD dst_unused:UNUSED_PAD src0_sel:DWORD src1_sel:WORD_0\n" \
  "ds_read_b128 v[100:103], v64\n" \
  "v_lshlrev_b32_sdwa v65, 4, v248 dst_sel:DWORD dst_unused:UNUSED_PAD src0_sel:DWORD src1_sel:WORD_1\n" \
  "ds_read_b128 v[104:107], v65\n" \
  "s_waitcnt lgkmcnt(10)\n" \
  "v_smfmac_f32_16x16x64_bf16 v[12:15], v[180:183], v[108:115], v192\n" \
  "v_lshlrev_b32_sdwa v66, 4, v249 dst_sel:DWORD dst_unused:UNUSED_PAD src0_sel:DWORD src1_sel:WORD_0\n" \
  "ds_read_b128 v[108:111], v66\n" \
  "v_lshlrev_b32_sdwa v67, 4, v249 dst_sel:DWORD dst_unused:UNUSED_PAD src0_sel:DWORD src1_sel:WORD_1\n" \
  "ds_read_b128 v[112:115], v67\n" \
  "s_waitcnt vmcnt(0)\n" \
  "s_barrier\n" \
  "s_cmp_lt_u32 s27, 4\n" \
  "s_cbranch_scc1 LG_tp\n" \
  "s_setprio 1\n" \
  "LG_tp:\n" \
  "s_waitcnt lgkmcnt(10)\n" \
  "v_smfmac_f32_16x16x64_bf16 v[16:19], v[180:183], v[68:75], v192\n" \
  "s_waitcnt lgkmcnt(8)\n" \
  "v_smfmac_f32_16x16x64_bf16 v[20:23], v[180:183], v[76:83], v192\n" \
  "s_waitcnt lgkmcnt(6)\n" \
  "v_smfmac_f32_16x16x64_bf16 v[24:27], v[180:183], v[84:91], v192\n" \
  "s_waitcnt lgkmcnt(4)\n" \
  "v_smfmac_f32_16x16x64_bf16 v[28:31], v[180:183], v[92:99], v192\n" \
  "s_waitcnt lgkmcnt(2)\n" \
  "v_smfmac_f32_16x16x64_bf16 v[32:35], v[180:183], v[100:107], v192\n" \
  "v_lshlrev_b32_sdwa v64, 4, v208 dst_sel:DWORD dst_unused:UNUSED_PAD src0_sel:DWORD src1_sel:WORD_0\n" \
  "ds_read_b128 v[68:71], v64\n" \
  "v_lshlrev_b32_sdwa v65, 4, v208 dst_sel:DWORD dst_unused:UNUSED_PAD src0_sel:DWORD src1_sel:WORD_1\n" \
  "ds_read_b128 v[72:75], v65\n" \
  "s_waitcnt lgkmcnt(2)\n" \
  "v_smfmac_f32_16x16x64_bf16 v[36:39], v[180:183], v[108:115], v192\n" \
  "v_lshlrev_b32_sdwa v66, 4, v209 dst_sel:DWORD dst_unused:UNUSED_PAD src0_sel:DWORD src1_sel:WORD_0\n" \
  "ds_read_b128 v[76:79], v66\n" \
  "v_lshlrev_b32_sdwa v67, 4, v209 dst_sel:DWORD dst_unused:UNUSED_PAD src0_sel:DWORD src1_sel:WORD_1\n" \
  "ds_read_b128 v[80:83], v67\n" \
  "s_waitcnt lgkmcnt(2)\n" \
  "v_smfmac_f32_16x16x64_bf16 v[0:3], v[180:183], v[68:75], v192\n" \
  "v_lshlrev_b32_sdwa v64, 4, v210 dst_sel:DWORD dst_unused:UNUSED_PAD src0_sel:DWORD src1_sel:WORD_0\n" \
  "ds_read_b128 v[84:87], v64\n" \
  "v_lshlrev_b32_sdwa v65, 4, v210 dst_sel:DWORD dst_unused:UNUSED_PAD src0_sel:DWORD src1_sel:WORD_1\n" \
  "ds_read_b128 v[88:91], v65\n" \
  "s_waitcnt lgkmcnt(2)\n" \
  "v_smfmac_f32_16x16x64_bf16 v[4:7], v[180:183], v[76:83], v192\n" \
  "v_lshlrev_b32_sdwa v66, 4, v211 dst_sel:DWORD dst_unused:UNUSED_PAD src0_sel:DWORD src1_sel:WORD_0\n" \
  "ds_read_b128 v[92:95], v66\n" \
  "v_lshlrev_b32_sdwa v67, 4, v211 dst_sel:DWORD dst_unused:UNUSED_PAD src0_sel:DWORD src1_sel:WORD_1\n" \
  "ds_read_b128 v[96:99], v67\n" \
  "v_pk_mul_f32 v[128:129], v[0:1], v[0:1]\n" \
  "v_pk_mul_f32 v[130:131], v[2:3], v[2:3]\n" \
  "v_pk_fma_f32 v[128:129], v[128:129], s[52:53], v[124:125]\n" \
  "v_pk_fma_f32 v[130:131], v[130:131], s[52:53], v[124:125]\n" \
  "v_pk_mul_f32 v[128:129], v[128:129], v[0:1]\n" \
  "v_pk_mul_f32 v[130:131], v[130:131], v[2:3]\n" \
  "v_exp_f32 v128, v128\n" \
  "v_exp_f32 v129, v129\n" \
  "v_exp_f32 v130, v130\n" \
  "v_exp_f32 v131, v131\n" \
  "s_waitcnt lgkmcnt(2)\n" \
  "v_smfmac_f32_16x16x64_bf16 v[8:11], v[180:183], v[84:91], v192\n" \
  "v_lshlrev_b32_sdwa v64, 4, v212 dst_sel:DWORD dst_unused:UNUSED_PAD src0_sel:DWORD src1_sel:WORD_0\n" \
  "ds_read_b128 v[100:103], v64\n" \
  "v_lshlrev_b32_sdwa v65, 4, v212 dst_sel:DWORD dst_unused:UNUSED_PAD src0_sel:DWORD src1_sel:WORD_1\n" \
  "ds_read_b128 v[104:107], v65\n" \
  "v_pk_fma_f32 v[128:129], v[128:129], v[120:121], v[120:121]\n" \
  "v_pk_fma_f32 v[130:131], v[130:131], v[122:123], v[122:123]\n" \
  "v_rcp_f32 v128, v128\n" \
  "v_rcp_f32 v129, v129\n" \
  "v_rcp_f32 v130, v130\n" \
  "v_rcp_f32 v131, v131\n" \
  "v_pk_mul_f32 v[0:1], v[0:1], v[128:129]\n" \
  "v_pk_fma_f32 v[0:1], v[2:3], v[130:131], v[0:1]\n" \
  "v_add_f32 v0, v0, v1\n" \
  "v_pk_mul_f32 v[132:133], v[4:5], v[4:5]\n" \
  "s_waitcnt lgkmcnt(2)\n" \
  "v_smfmac_f32_16x16x64_bf16 v[12:15], v[180:183], v[92:99], v192\n" \
  "v_lshlrev_b32_sdwa v66, 4, v213 dst_sel:DWORD dst_unused:UNUSED_PAD src0_sel:DWORD src1_sel:WORD_0\n" \
  "ds_read_b128 v[108:111], v66\n" \
  "v_lshlrev_b32_sdwa v67, 4, v213 dst_sel:DWORD dst_unused:UNUSED_PAD src0_sel:DWORD src1_sel:WORD_1\n" \
  "ds_read_b128 v[112:115], v67\n" \
  "v_pk_mul_f32 v[134:135], v[6:7], v[6:7]\n" \
  "v_pk_fma_f32 v[132:133], v[132:133], s[52:53], v[124:125]\n" \
  "v_pk_fma_f32 v[134:135], v[134:135], s[52:53], v[124:125]\n" \
  "v_pk_mul_f32 v[132:133], v[132:133], v[4:5]\n" \
  "v_pk_mul_f32 v[134:135], v[134:135], v[6:7]\n" \
  "v_exp_f32 v132, v132\n" \
  "v_exp_f32 v133, v133\n" \
  "v_exp_f32 v134, v134\n" \
  "v_exp_f32 v135, v135\n" \
  "v_pk_fma_f32 v[132:133], v[132:133], v[120:121], v[120:121]\n" \
  "s_waitcnt lgkmcnt(2)\n" \
  "v_smfmac_f32_16x16x64_bf16 v[16:19], v[180:183], v[100:107], v192\n" \
  "v_lshlrev_b32_sdwa v64, 4, v214 dst_sel:DWORD dst_unused:UNUSED_PAD src0_sel:DWORD src1_sel:WORD_0\n" \
  "ds_read_b128 v[68:71], v64\n" \
  "v_lshlrev_b32_sdwa v65, 4, v214 dst_sel:DWORD dst_unused:UNUSED_PAD src0_sel:DWORD src1_sel:WORD_1\n" \
  "ds_read_b128 v[72:75], v65\n" \
  "v_pk_fma_f32 v[134:135], v[134:135], v[122:123], v[122:123]\n" \
  "v_rcp_f32 v132, v132\n" \
  "v_rcp_f32 v133, v133\n" \
  "v_rcp_f32 v134, v134\n" \
  "v_rcp_f32 v135, v135\n" \
  "v_pk_mul_f32 v[4:5], v[4:5], v[132:133]\n" \
  "v_pk_fma_f32 v[4:5], v[6:7], v[134:135], v[4:5]\n" \
  "v_add_f32 v4, v4, v5\n" \
  "v_pk_mul_f32 v[136:137], v[8:9], v[8:9]\n" \
  "v_pk_mul_f32 v[138:139], v[10:11], v[10:11]\n" \
  "s_waitcnt lgkmcnt(2)\n" \
  "v_smfmac_f32_16x16x64_bf16 v[20:23], v[180:183], v[108:115], v192\n" \
  "v_lshlrev_b32_sdwa v66, 4, v215 dst_sel:DWORD dst_unused:UNUSED_PAD src0_sel:DWORD src1_sel:WORD_0\n" \
  "ds_read_b128 v[76:79], v66\n" \
  "v_lshlrev_b32_sdwa v67, 4, v215 dst_sel:DWORD dst_unused:UNUSED_PAD src0_sel:DWORD src1_sel:WORD_1\n" \
  "ds_read_b128 v[80:83], v67\n" \
  "v_pk_fma_f32 v[136:137], v[136:137], s[52:53], v[124:125]\n" \
  "v_pk_fma_f32 v[138:139], v[138:139], s[52:53], v[124:125]\n" \
  "v_pk_mul_f32 v[136:137], v[136:137], v[8:9]\n" \
  "v_pk_mul_f32 v[138:139], v[138:139], v[10:11]\n" \
  "v_exp_f32 v136, v136\n" \
  "v_exp_f32 v137, v137\n" \
  "v_exp_f32 v138, v138\n" \
  "v_exp_f32 v139, v139\n" \
  "v_pk_fma_f32 v[136:137], v[136:137], v[120:121], v[120:121]\n" \
  "v_pk_fma_f32 v[138:139], v[138:139], v[122:123], v[122:123]\n" \
  "s_waitcnt lgkmcnt(2)\n" \
  "v_smfmac_f32_16x16x64_bf16 v[24:27], v[180:183], v[68:75], v192\n" \
  "v_lshlrev_b32_sdwa v64, 4, v216 dst_sel:DWORD dst_unused:UNUSED_PAD src0_sel:DWORD src1_sel:WORD_0\n" \
  "ds_read_b128 v[84:87], v64\n" \
  "v_lshlrev_b32_sdwa v65, 4, v216 dst_sel:DWORD dst_unused:UNUSED_PAD src0_sel:DWORD src1_sel:WORD_1\n" \
  "ds_read_b128 v[88:91], v65\n" \
  "v_rcp_f32 v136, v136\n" \
  "v_rcp_f32 v137, v137\n" \
  "v_rcp_f32 v138, v138\n" \
  "v_rcp_f32 v139, v139\n" \
  "v_pk_mul_f32 v[8:9], v[8:9], v[136:137]\n" \
  "v_pk_fma_f32 v[8:9], v[10:11], v[138:139], v[8:9]\n" \
  "v_add_f32 v8, v8, v9\n" \
  "v_pk_mul_f32 v[140:141], v[12:13], v[12:13]\n" \
  "v_pk_mul_f32 v[142:143], v[14:15], v[14:15]\n" \
  "v_pk_fma_f32 v[140:141], v[140:141], s[52:53], v[124:125]\n" \
  "s_waitcnt lgkmcnt(2)\n" \
  "v_smfmac_f32_16x16x64_bf16 v[28:31], v[180:183], v[76:83], v192\n" \
  "v_lshlrev_b32_sdwa v66, 4, v217 dst_sel:DWORD dst_unused:UNUSED_PAD src0_sel:DWORD src1_sel:WORD_0\n" \
  "ds_read_b128 v[92:95], v66\n" \
  "v_lshlrev_b32_sdwa v67, 4, v217 dst_sel:DWORD dst_unused:UNUSED_PAD src0_sel:DWORD src1_sel:WORD_1\n" \
  "ds_read_b128 v[96:99], v67\n" \
  "v_pk_fma_f32 v[142:143], v[142:143], s[52:53], v[124:125]\n" \
  "v_pk_mul_f32 v[140:141], v[140:141], v[12:13]\n" \
  "v_pk_mul_f32 v[142:143], v[142:143], v[14:15]\n" \
  "v_exp_f32 v140, v140\n" \
  "v_exp_f32 v141, v141\n" \
  "v_exp_f32 v142, v142\n" \
  "v_exp_f32 v143, v143\n" \
  "v_pk_fma_f32 v[140:141], v[140:141], v[120:121], v[120:121]\n" \
  "v_pk_fma_f32 v[142:143], v[142:143], v[122:123], v[122:123]\n" \
  "v_rcp_f32 v140, v140\n" \
  "s_waitcnt lgkmcnt(2)\n" \
  "v_smfmac_f32_16x16x64_bf16 v[32:35], v[180:183], v[84:91], v192\n" \
  "v_lshlrev_b32_sdwa v64, 4, v250 dst_sel:DWORD dst_unused:UNUSED_PAD src0_sel:DWORD src1_sel:WORD_0\n" \
  "ds_read_b128 v[100:103], v64\n" \
  "v_lshlrev_b32_sdwa v65, 4, v250 dst_sel:DWORD dst_unused:UNUSED_PAD src0_sel:DWORD src1_sel:WORD_1\n" \
  "ds_read_b128 v[104:107], v65\n" \
  "v_rcp_f32 v141, v141\n" \
  "v_rcp_f32 v142, v142\n" \
  "v_rcp_f32 v143, v143\n" \
  "v_pk_mul_f32 v[12:13], v[12:13], v[140:141]\n" \
  "v_pk_fma_f32 v[12:13], v[14:15], v[142:143], v[12:13]\n" \
  "v_add_f32 v12, v12, v13\n" \
  "v_pk_mul_f32 v[128:129], v[16:17], v[16:17]\n" \
  "v_pk_mul_f32 v[130:131], v[18:19], v[18:19]\n" \
  "v_pk_fma_f32 v[128:129], v[128:129], s[52:53], v[124:125]\n" \
  "v_pk_fma_f32 v[130:131], v[130:131], s[52:53], v[124:125]\n" \
  "s_waitcnt lgkmcnt(2)\n" \
  "v_smfmac_f32_16x16x64_bf16 v[36:39], v[180:183], v[92:99], v192\n" \
  "v_lshlrev_b32_sdwa v66, 4, v218 dst_sel:DWORD dst_unused:UNUSED_PAD src0_sel:DWORD src1_sel:WORD_0\n" \
  "ds_read_b128 v[108:111], v66\n" \
  "v_lshlrev_b32_sdwa v67, 4, v218 dst_sel:DWORD dst_unused:UNUSED_PAD src0_sel:DWORD src1_sel:WORD_1\n" \
  "ds_read_b128 v[112:115], v67\n" \
  "v_pk_mul_f32 v[128:129], v[128:129], v[16:17]\n" \
  "v_pk_mul_f32 v[130:131], v[130:131], v[18:19]\n" \
  "v_exp_f32 v128, v128\n" \
  "v_exp_f32 v129, v129\n" \
  "v_exp_f32 v130, v130\n" \
  "v_exp_f32 v131, v131\n" \
  "v_pk_fma_f32 v[128:129], v[128:129], v[120:121], v[120:121]\n" \
  "v_pk_fma_f32 v[130:131], v[130:131], v[122:123], v[122:123]\n" \
  "v_rcp_f32 v128, v128\n" \
  "v_rcp_f32 v129, v129\n" \
  "s_waitcnt lgkmcnt(2)\n" \
  "v_smfmac_f32_16x16x64_bf16 v[40:43], v[180:183], v[100:107], v192\n" \
  "v_lshlrev_b32_sdwa v64, 4, v251 dst_sel:DWORD dst_unused:UNUSED_PAD src0_sel:DWORD src1_sel:WORD_0\n" \
  "ds_read_b128 v[68:71], v64\n" \
  "v_lshlrev_b32_sdwa v65, 4, v251 dst_sel:DWORD dst_unused:UNUSED_PAD src0_sel:DWORD src1_sel:WORD_1\n" \
  "ds_read_b128 v[72:75], v65\n" \
  "v_rcp_f32 v130, v130\n" \
  "v_rcp_f32 v131, v131\n" \
  "v_pk_mul_f32 v[16:17], v[16:17], v[128:129]\n" \
  "v_pk_fma_f32 v[16:17], v[18:19], v[130:131], v[16:17]\n" \
  "v_add_f32 v16, v16, v17\n" \
  "v_pk_mul_f32 v[132:133], v[20:21], v[20:21]\n" \
  "v_pk_mul_f32 v[134:135], v[22:23], v[22:23]\n" \
  "v_pk_fma_f32 v[132:133], v[132:133], s[52:53], v[124:125]\n" \
  "v_pk_fma_f32 v[134:135], v[134:135], s[52:53], v[124:125]\n" \
  "v_pk_mul_f32 v[132:133], v[132:133], v[20:21]\n" \
  "s_waitcnt lgkmcnt(2)\n" \
  "v_smfmac_f32_16x16x64_bf16 v[40:43], v[180:183], v[108:115], v192\n" \
  "v_lshlrev_b32_sdwa v66, 4, v219 dst_sel:DWORD dst_unused:UNUSED_PAD src0_sel:DWORD src1_sel:WORD_0\n" \
  "ds_read_b128 v[76:79], v66\n" \
  "v_lshlrev_b32_sdwa v67, 4, v219 dst_sel:DWORD dst_unused:UNUSED_PAD src0_sel:DWORD src1_sel:WORD_1\n" \
  "ds_read_b128 v[80:83], v67\n" \
  "v_pk_mul_f32 v[134:135], v[134:135], v[22:23]\n" \
  "v_exp_f32 v132, v132\n" \
  "v_exp_f32 v133, v133\n" \
  "v_exp_f32 v134, v134\n" \
  "v_exp_f32 v135, v135\n" \
  "v_pk_fma_f32 v[132:133], v[132:133], v[120:121], v[120:121]\n" \
  "v_pk_fma_f32 v[134:135], v[134:135], v[122:123], v[122:123]\n" \
  "v_rcp_f32 v132, v132\n" \
  "v_rcp_f32 v133, v133\n" \
  "v_rcp_f32 v134, v134\n" \
  "s_waitcnt lgkmcnt(2)\n" \
  "v_smfmac_f32_16x16x64_bf16 v[44:47], v[180:183], v[68:75], v192\n" \
  "v_lshlrev_b32_sdwa v64, 4, v252 dst_sel:DWORD dst_unused:UNUSED_PAD src0_sel:DWORD src1_sel:WORD_0\n" \
  "ds_read_b128 v[84:87], v64\n" \
  "v_lshlrev_b32_sdwa v65, 4, v252 dst_sel:DWORD dst_unused:UNUSED_PAD src0_sel:DWORD src1_sel:WORD_1\n" \
  "ds_read_b128 v[88:91], v65\n" \
  "v_rcp_f32 v135, v135\n" \
  "v_pk_mul_f32 v[20:21], v[20:21], v[132:133]\n" \
  "v_pk_fma_f32 v[20:21], v[22:23], v[134:135], v[20:21]\n" \
  "v_add_f32 v20, v20, v21\n" \
  "v_pk_mul_f32 v[136:137], v[24:25], v[24:25]\n" \
  "v_pk_mul_f32 v[138:139], v[26:27], v[26:27]\n" \
  "v_pk_fma_f32 v[136:137], v[136:137], s[52:53], v[124:125]\n" \
  "v_pk_fma_f32 v[138:139], v[138:139], s[52:53], v[124:125]\n" \
  "v_pk_mul_f32 v[136:137], v[136:137], v[24:25]\n" \
  "v_pk_mul_f32 v[138:139], v[138:139], v[26:27]\n" \
  "s_waitcnt lgkmcnt(2)\n" \
  "v_smfmac_f32_16x16x64_bf16 v[44:47], v[180:183], v[76:83], v192\n" \
  "v_lshlrev_b32_sdwa v66, 4, v220 dst_sel:DWORD dst_unused:UNUSED_PAD src0_sel:DWORD src1_sel:WORD_0\n" \
  "ds_read_b128 v[92:95], v66\n" \
  "v_lshlrev_b32_sdwa v67, 4, v220 dst_sel:DWORD dst_unused:UNUSED_PAD src0_sel:DWORD src1_sel:WORD_1\n" \
  "ds_read_b128 v[96:99], v67\n" \
  "v_exp_f32 v136, v136\n" \
  "v_exp_f32 v137, v137\n" \
  "v_exp_f32 v138, v138\n" \
  "v_exp_f32 v139, v139\n" \
  "v_pk_fma_f32 v[136:137], v[136:137], v[120:121], v[120:121]\n" \
  "v_pk_fma_f32 v[138:139], v[138:139], v[122:123], v[122:123]\n" \
  "v_rcp_f32 v136, v136\n" \
  "v_rcp_f32 v137, v137\n" \
  "v_rcp_f32 v138, v138\n" \
  "v_rcp_f32 v139, v139\n" \
  "s_waitcnt lgkmcnt(2)\n" \
  "v_smfmac_f32_16x16x64_bf16 v[48:51], v[180:183], v[84:91], v192\n" \
  "v_lshlrev_b32_sdwa v64, 4, v253 dst_sel:DWORD dst_unused:UNUSED_PAD src0_sel:DWORD src1_sel:WORD_0\n" \
  "ds_read_b128 v[100:103], v64\n" \
  "v_lshlrev_b32_sdwa v65, 4, v253 dst_sel:DWORD dst_unused:UNUSED_PAD src0_sel:DWORD src1_sel:WORD_1\n" \
  "ds_read_b128 v[104:107], v65\n" \
  "v_pk_mul_f32 v[24:25], v[24:25], v[136:137]\n" \
  "v_pk_fma_f32 v[24:25], v[26:27], v[138:139], v[24:25]\n" \
  "v_add_f32 v24, v24, v25\n" \
  "v_pk_mul_f32 v[140:141], v[28:29], v[28:29]\n" \
  "v_pk_mul_f32 v[142:143], v[30:31], v[30:31]\n" \
  "v_pk_fma_f32 v[140:141], v[140:141], s[52:53], v[124:125]\n" \
  "v_pk_fma_f32 v[142:143], v[142:143], s[52:53], v[124:125]\n" \
  "v_pk_mul_f32 v[140:141], v[140:141], v[28:29]\n" \
  "v_pk_mul_f32 v[142:143], v[142:143], v[30:31]\n" \
  "v_exp_f32 v140, v140\n" \
  "s_waitcnt lgkmcnt(2)\n" \
  "v_smfmac_f32_16x16x64_bf16 v[48:51], v[180:183], v[92:99], v192\n" \
  "v_lshlrev_b32_sdwa v66, 4, v221 dst_sel:DWORD dst_unused:UNUSED_PAD src0_sel:DWORD src1_sel:WORD_0\n" \
  "ds_read_b128 v[108:111], v66\n" \
  "v_lshlrev_b32_sdwa v67, 4, v221 dst_sel:DWORD dst_unused:UNUSED_PAD src0_sel:DWORD src1_sel:WORD_1\n" \
  "ds_read_b128 v[112:115], v67\n" \
  "v_exp_f32 v141, v141\n" \
  "v_exp_f32 v142, v142\n" \
  "v_exp_f32 v143, v143\n" \
  "v_pk_fma_f32 v[140:141], v[140:141], v[120:121], v[120:121]\n" \
  "v_pk_fma_f32 v[142:143], v[142:143], v[122:123], v[122:123]\n" \
  "v_rcp_f32 v140, v140\n" \
  "v_rcp_f32 v141, v141\n" \
  "v_rcp_f32 v142, v142\n" \
  "v_rcp_f32 v143, v143\n" \
  "v_pk_mul_f32 v[28:29], v[28:29], v[140:141]\n" \
  "s_waitcnt lgkmcnt(2)\n" \
  "v_smfmac_f32_16x16x64_bf16 v[52:55], v[180:183], v[100:107], v192\n" \
  "v_lshlrev_b32_sdwa v64, 4, v254 dst_sel:DWORD dst_unused:UNUSED_PAD src0_sel:DWORD src1_sel:WORD_0\n" \
  "ds_read_b128 v[68:71], v64\n" \
  "v_lshlrev_b32_sdwa v65, 4, v254 dst_sel:DWORD dst_unused:UNUSED_PAD src0_sel:DWORD src1_sel:WORD_1\n" \
  "ds_read_b128 v[72:75], v65\n" \
  "v_pk_fma_f32 v[28:29], v[30:31], v[142:143], v[28:29]\n" \
  "v_add_f32 v28, v28, v29\n" \
  "v_permlane16_swap_b32 v0, v16\n" \
  "v_permlane16_swap_b32 v4, v20\n" \
  "v_permlane16_swap_b32 v8, v24\n" \
  "v_permlane16_swap_b32 v12, v28\n" \
  "v_add_f32 v144, v0, v16\n" \
  "v_add_f32 v145, v4, v20\n" \
  "v_add_f32 v146, v8, v24\n" \
  "v_add_f32 v147, v12, v28\n" \
  "s_waitcnt lgkmcnt(2)\n" \
  "v_smfmac_f32_16x16x64_bf16 v[52:55], v[180:183], v[108:115], v192\n" \
  "v_lshlrev_b32_sdwa v66, 4, v222 dst_sel:DWORD dst_unused:UNUSED_PAD src0_sel:DWORD src1_sel:WORD_0\n" \
  "ds_read_b128 v[76:79], v66\n" \
  "v_lshlrev_b32_sdwa v67, 4, v222 dst_sel:DWORD dst_unused:UNUSED_PAD src0_sel:DWORD src1_sel:WORD_1\n" \
  "ds_read_b128 v[80:83], v67\n" \
  "v_min_u32 v144, -2, v144\n" \
  "v_min_u32 v145, -2, v145\n" \
  "v_min_u32 v146, -2, v146\n" \
  "v_min_u32 v147, -2, v147\n" \
  "global_store_dwordx4 v188, v[144:147], s[40:41] offset:0 sc1\n" \
  "v_pk_mul_f32 v[128:129], v[32:33], v[32:33]\n" \
  "v_pk_mul_f32 v[130:131], v[34:35], v[34:35]\n" \
  "v_pk_fma_f32 v[128:129], v[128:129], s[52:53], v[124:125]\n" \
  "v_pk_fma_f32 v[130:131], v[130:131], s[52:53], v[124:125]\n" \
  "v_pk_mul_f32 v[128:129], v[128:129], v[32:33]\n" \
  "s_waitcnt lgkmcnt(2)\n" \
  "v_smfmac_f32_16x16x64_bf16 v[56:59], v[180:183], v[68:75], v192\n" \
  "v_lshlrev_b32_sdwa v64, 4, v255 dst_sel:DWORD dst_unused:UNUSED_PAD src0_sel:DWORD src1_sel:WORD_0\n" \
  "ds_read_b128 v[84:87], v64\n" \
  "v_lshlrev_b32_sdwa v65, 4, v255 dst_sel:DWORD dst_unused:UNUSED_PAD src0_sel:DWORD src1_sel:WORD_1\n" \
  "ds_read_b128 v[88:91], v65\n" \
  "v_pk_mul_f32 v[130:131], v[130:131], v[34:35]\n" \
  "v_exp_f32 v128, v128\n" \
  "v_exp_f32 v129, v129\n" \
  "v_exp_f32 v130, v130\n" \
  "v_exp_f32 v131, v131\n" \
  "v_pk_fma_f32 v[128:129], v[128:129], v[120:121], v[120:121]\n" \
  "v_pk_fma_f32 v[130:131], v[130:131], v[122:123], v[122:123]\n" \
  "v_rcp_f32 v128, v128\n" \
  "v_rcp_f32 v129, v129\n" \
  "v_rcp_f32 v130, v130\n" \
  "s_waitcnt lgkmcnt(2)\n" \
  "v_smfmac_f32_16x16x64_bf16 v[56:59], v[180:183], v[76:83], v192\n" \
  "v_lshlrev_b32_sdwa v66, 4, v223 dst_sel:DWORD dst_unused:UNUSED_PAD src0_sel:DWORD src1_sel:WORD_0\n" \
  "ds_read_b128 v[92:95], v66\n" \
  "v_lshlrev_b32_sdwa v67, 4, v223 dst_sel:DWORD dst_unused:UNUSED_PAD src0_sel:DWORD src1_sel:WORD_1\n" \
  "ds_read_b128 v[96:99], v67\n" \
  "v_rcp_f32 v131, v131\n" \
  "v_pk_mul_f32 v[32:33], v[32:33], v[128:129]\n" \
  "v_pk_fma_f32 v[32:33], v[34:35], v[130:131], v[32:33]\n" \
  "v_add_f32 v32, v32, v33\n" \
  "v_pk_mul_f32 v[132:133], v[36:37], v[36:37]\n" \
  "v_pk_mul_f32 v[134:135], v[38:39], v[38:39]\n" \
  "v_pk_fma_f32 v[132:133], v[132:133], s[52:53], v[124:125]\n" \
  "v_pk_fma_f32 v[134:135], v[134:135], s[52:53], v[124:125]\n" \
  "v_pk_mul_f32 v[132:133], v[132:133], v[36:37]\n" \
  "v_pk_mul_f32 v[134:135], v[134:135], v[38:39]\n" \
  "s_setprio 0\n" \
  "s_waitcnt lgkmcnt(2)\n" \
  "v_smfmac_f32_16x16x64_bf16 v[60:63], v[180:183], v[84:91], v192\n" \
  "v_exp_f32 v132, v132\n" \
  "v_exp_f32 v133, v133\n" \
  "v_exp_f32 v134, v134\n" \
  "v_exp_f32 v135, v135\n" \
  "v_pk_fma_f32 v[132:133], v[132:133], v[120:121], v[120:121]\n" \
  "v_pk_fma_f32 v[134:135], v[134:135], v[122:123], v[122:123]\n" \
  "v_rcp_f32 v132, v132\n" \
  "v_rcp_f32 v133, v133\n" \
  "v_rcp_f32 v134, v134\n" \
  "v_rcp_f32 v135, v135\n" \
  "v_pk_mul_f32 v[36:37], v[36:37], v[132:133]\n" \
  "v_pk_fma_f32 v[36:37], v[38:39], v[134:135], v[36:37]\n" \
  "v_add_f32 v36, v36, v37\n" \
  "v_pk_mul_f32 v[136:137], v[40:41], v[40:41]\n" \
  "v_pk_mul_f32 v[138:139], v[42:43], v[42:43]\n" \
  "v_pk_fma_f32 v[136:137], v[136:137], s[52:53], v[124:125]\n" \
  "v_pk_fma_f32 v[138:139], v[138:139], s[52:53], v[124:125]\n" \
  "v_pk_mul_f32 v[136:137], v[136:137], v[40:41]\n" \
  "v_pk_mul_f32 v[138:139], v[138:139], v[42:43]\n" \
  "v_exp_f32 v136, v136\n" \
  "v_exp_f32 v137, v137\n" \
  "v_exp_f32 v138, v138\n" \
  "v_exp_f32 v139, v139\n" \
  "v_pk_fma_f32 v[136:137], v[136:137], v[120:121], v[120:121]\n" \
  "v_pk_fma_f32 v[138:139], v[138:139], v[122:123], v[122:123]\n" \
  "v_rcp_f32 v136, v136\n" \
  "v_rcp_f32 v137, v137\n" \
  "v_rcp_f32 v138, v138\n" \
  "v_rcp_f32 v139, v139\n" \
  "v_pk_mul_f32 v[40:41], v[40:41], v[136:137]\n" \
  "v_pk_fma_f32 v[40:41], v[42:43], v[138:139], v[40:41]\n" \
  "v_add_f32 v40, v40, v41\n" \
  "s_waitcnt lgkmcnt(0)\n" \
  "v_smfmac_f32_16x16x64_bf16 v[60:63], v[180:183], v[92:99], v192\n" \
  "v_pk_mul_f32 v[140:141], v[44:45], v[44:45]\n" \
  "v_pk_mul_f32 v[142:143], v[46:47], v[46:47]\n" \
  "v_pk_fma_f32 v[140:141], v[140:141], s[52:53], v[124:125]\n" \
  "v_pk_fma_f32 v[142:143], v[142:143], s[52:53], v[124:125]\n" \
  "v_pk_mul_f32 v[140:141], v[140:141], v[44:45]\n" \
  "v_pk_mul_f32 v[142:143], v[142:143], v[46:47]\n" \
  "v_exp_f32 v140, v140\n" \
  "v_exp_f32 v141, v141\n" \
  "v_exp_f32 v142, v142\n" \
  "v_exp_f32 v143, v143\n" \
  "v_pk_fma_f32 v[140:141], v[140:141], v[120:121], v[120:121]\n" \
  "v_pk_fma_f32 v[142:143], v[142:143], v[122:123], v[122:123]\n" \
  "v_rcp_f32 v140, v140\n" \
  "v_rcp_f32 v141, v141\n" \
  "v_rcp_f32 v142, v142\n" \
  "v_rcp_f32 v143, v143\n" \
  "v_pk_mul_f32 v[44:45], v[44:45], v[140:141]\n" \
  "v_pk_fma_f32 v[44:45], v[46:47], v[142:143], v[44:45]\n" \
  "v_add_f32 v44, v44, v45\n" \
  "v_pk_mul_f32 v[128:129], v[48:49], v[48:49]\n" \
  "v_pk_mul_f32 v[130:131], v[50:51], v[50:51]\n" \
  "v_pk_fma_f32 v[128:129], v[128:129], s[52:53], v[124:125]\n" \
  "v_pk_fma_f32 v[130:131], v[130:131], s[52:53], v[124:125]\n" \
  "v_pk_mul_f32 v[128:129], v[128:129], v[48:49]\n" \
  "v_pk_mul_f32 v[130:131], v[130:131], v[50:51]\n" \
  "v_exp_f32 v128, v128\n" \
  "v_exp_f32 v129, v129\n" \
  "v_exp_f32 v130, v130\n" \
  "v_exp_f32 v131, v131\n" \
  "v_pk_fma_f32 v[128:129], v[128:129], v[120:121], v[120:121]\n" \
  "v_pk_fma_f32 v[130:131], v[130:131], v[122:123], v[122:123]\n" \
  "v_rcp_f32 v128, v128\n" \
  "v_rcp_f32 v129, v129\n" \
  "v_rcp_f32 v130, v130\n" \
  "v_rcp_f32 v131, v131\n" \
  "v_pk_mul_f32 v[48:49], v[48:49], v[128:129]\n" \
  "v_pk_fma_f32 v[48:49], v[50:51], v[130:131], v[48:49]\n" \
  "v_add_f32 v48, v48, v49\n" \
  "v_pk_mul_f32 v[132:133], v[52:53], v[52:53]\n" \
  "v_pk_mul_f32 v[134:135], v[54:55], v[54:55]\n" \
  "v_pk_fma_f32 v[132:133], v[132:133], s[52:53], v[124:125]\n" \
  "v_pk_fma_f32 v[134:135], v[134:135], s[52:53], v[124:125]\n" \
  "v_pk_mul_f32 v[132:133], v[132:133], v[52:53]\n" \
  "v_pk_mul_f32 v[134:135], v[134:135], v[54:55]\n" \
  "v_exp_f32 v132, v132\n" \
  "v_exp_f32 v133, v133\n" \
  "v_exp_f32 v134, v134\n" \
  "v_exp_f32 v135, v135\n" \
  "v_pk_fma_f32 v[132:133], v[132:133], v[120:121], v[120:121]\n" \
  "v_pk_fma_f32 v[134:135], v[134:135], v[122:123], v[122:123]\n" \
  "v_rcp_f32 v132, v132\n" \
  "v_rcp_f32 v133, v133\n" \
  "v_rcp_f32 v134, v134\n" \
  "v_rcp_f32 v135, v135\n" \
  "v_pk_mul_f32 v[52:53], v[52:53], v[132:133]\n" \
  "v_pk_fma_f32 v[52:53], v[54:55], v[134:135], v[52:53]\n" \
  "v_add_f32 v52, v52, v53\n" \
  "v_pk_mul_f32 v[136:137], v[56:57], v[56:57]\n" \
  "v_pk_mul_f32 v[138:139], v[58:59], v[58:59]\n" \
  "v_pk_fma_f32 v[136:137], v[136:137], s[52:53], v[124:125]\n" \
  "v_pk_fma_f32 v[138:139], v[138:139], s[52:53], v[124:125]\n" \
  "v_pk_mul_f32 v[136:137], v[136:137], v[56:57]\n" \
  "v_pk_mul_f32 v[138:139], v[138:139], v[58:59]\n" \
  "v_exp_f32 v136, v136\n" \
  "v_exp_f32 v137, v137\n" \
  "v_exp_f32 v138, v138\n" \
  "v_exp_f32 v139, v139\n" \
  "v_pk_fma_f32 v[136:137], v[136:137], v[120:121], v[120:121]\n" \
  "v_pk_fma_f32 v[138:139], v[138:139], v[122:123], v[122:123]\n" \
  "v_rcp_f32 v136, v136\n" \
  "v_rcp_f32 v137, v137\n" \
  "v_rcp_f32 v138, v138\n" \
  "v_rcp_f32 v139, v139\n" \
  "v_pk_mul_f32 v[56:57], v[56:57], v[136:137]\n" \
  "v_pk_fma_f32 v[56:57], v[58:59], v[138:139], v[56:57]\n" \
  "v_add_f32 v56, v56, v57\n" \
  "v_pk_mul_f32 v[140:141], v[60:61], v[60:61]\n" \
  "v_pk_mul_f32 v[142:143], v[62:63], v[62:63]\n" \
  "v_pk_fma_f32 v[140:141], v[140:141], s[52:53], v[124:125]\n" \
  "v_pk_fma_f32 v[142:143], v[142:143], s[52:53], v[124:125]\n" \
  "v_pk_mul_f32 v[140:141], v[140:141], v[60:61]\n" \
  "v_pk_mul_f32 v[142:143], v[142:143], v[62:63]\n" \
  "v_exp_f32 v140, v140\n" \
  "v_exp_f32 v141, v141\n" \
  "v_exp_f32 v142, v142\n" \
  "v_exp_f32 v143, v143\n" \
  "v_pk_fma_f32 v[140:141], v[140:141], v[120:121], v[120:121]\n" \
  "v_pk_fma_f32 v[142:143], v[142:143], v[122:123], v[122:123]\n" \
  "v_rcp_f32 v140, v140\n" \
  "v_rcp_f32 v141, v141\n" \
  "v_rcp_f32 v142, v142\n" \
  "v_rcp_f32 v143, v143\n" \
  "v_pk_mul_f32 v[60:61], v[60:61], v[140:141]\n" \
  "v_pk_fma_f32 v[60:61], v[62:63], v[142:143], v[60:61]\n" \
  "v_add_f32 v60, v60, v61\n" \
  "v_permlane16_swap_b32 v32, v48\n" \
  "v_permlane16_swap_b32 v36, v52\n" \
  "v_permlane16_swap_b32 v40, v56\n" \
  "v_permlane16_swap_b32 v44, v60\n" \
  "v_add_f32 v148, v32, v48\n" \
  "v_add_f32 v149, v36, v52\n" \
  "v_add_f32 v150, v40, v56\n" \
  "v_add_f32 v151, v44, v60\n" \
  "v_min_u32 v148, -2, v148\n" \
  "v_min_u32 v149, -2, v149\n" \
  "v_min_u32 v150, -2, v150\n" \
  "v_min_u32 v151, -2, v151\n" \
  "global_store_dwordx4 v188, v[148:151], s[40:41] offset:1024 sc1\n" \
  "s_cmp_ge_u32 s27, 4\n" \
  "s_cbranch_scc1 LF_b\n" \
  "s_load_dwordx2 s[24:25], s[0:1], 0x40\n" \
  "v_lshrrev_b32 v0, 4, v188\n" \
  "v_lshrrev_b32 v1, 4, v0\n" \
  "v_and_b32 v2, 15, v0\n" \
  "v_lshlrev_b32 v2, 4, v2\n" \
  "v_lshl_add_u32 v1, v1, 18, v2\n" \
  "s_lshl_b32 s35, s27, 20\n" \
  "s_lshl_b32 s36, s26, 8\n" \
  "s_add_u32 s35, s35, s36\n" \
  "s_add_u32 s35, s35, 105676800\n" \
  "s_add_u32 s40, s22, s35\n" \
  "s_addc_u32 s41, s23, 0\n" \
  "s_add_u32 s42, s40, 65536\n" \
  "s_addc_u32 s43, s41, 0\n" \
  "s_add_u32 s54, s42, 65536\n" \
  "s_addc_u32 s55, s43, 0\n" \
  "s_add_u32 s56, s54, 65536\n" \
  "s_addc_u32 s57, s55, 0\n" \
  "s_add_u32 s44, s22, 109871104\n" \
  "s_addc_u32 s45, s23, 0\n" \
  "s_add_u32 s46, s44, 65536\n" \
  "s_addc_u32 s47, s45, 0\n" \
  "s_lshr_b32 s35, s26, 2\n" \
  "s_lshl_b32 s35, s35, 8\n" \
  "s_and_b32 s36, s26, 1\n" \
  "s_lshl_b32 s36, s36, 7\n" \
  "s_add_u32 s35, s35, s36\n" \
  "s_and_b32 s36, s26, 2\n" \
  "s_lshl3_add_u32 s35, s36, s35\n" \
  "v_and_b32 v2, 3, v0\n" \
  "v_lshrrev_b32 v3, 2, v0\n" \
  "v_lshl_add_u32 v2, v2, 5, v3\n" \
  "v_add_u32 v2, s35, v2\n" \
  "v_lshlrev_b32 v3, 2, v2\n" \
  "s_cmp_eq_u32 s27, 0\n" \
  "s_cselect_b32 s35, -1, 0\n" \
  "v_and_b32 v3, s35, v3\n" \
  "s_lshl_b32 s6, s6, 16\n" \
  "v_and_b32 v6, 15, v0\n" \
  "v_lshrrev_b32 v7, 4, v0\n" \
  "v_lshl_add_u32 v6, v6, 2, v7\n" \
  "s_lshl_b32 s35, s27, 8\n" \
  "v_lshl_add_u32 v6, v6, 2, s35\n" \
  "v_lshlrev_b32 v7, 2, v0\n" \
  "v_lshlrev_b32 v2, 1, v2\n" \
  "s_mov_b32 s5, 0x10000\n" \
  "s_barrier\n" \
  "s_sleep 12\n" \
  "LF_poll:\n" \
  "global_load_dwordx4 v[8:11], v1, s[40:41] sc1\n" \
  "global_load_dwordx4 v[12:15], v1, s[42:43] sc1\n" \
  "global_load_dwordx4 v[16:19], v1, s[54:55] sc1\n" \
  "global_load_dwordx4 v[20:23], v1, s[56:57] sc1\n" \
  "global_load_dword v24, v3, s[44:45] sc1\n" \
  "global_load_dword v25, v3, s[46:47] sc1\n" \
  "s_waitcnt vmcnt(0)\n" \
  "v_max3_u32 v4, v8, v9, v10\n" \
  "v_max3_u32 v5, v11, v12, v13\n" \
  "v_max3_u32 v4, v4, v14, v15\n" \
  "v_max3_u32 v5, v5, v16, v17\n" \
  "v_max3_u32 v4, v4, v18, v19\n" \
  "v_max3_u32 v5, v5, v20, v21\n" \
  "v_max3_u32 v4, v4, v22, v23\n" \
  "v_max3_u32 v5, v5, v24, v25\n" \
  "v_max_u32 v5, v5, v4\n" \
  "v_cmp_eq_u32 vcc, -1, v5\n" \
  "s_sub_u32 s5, s5, 1\n" \
  "s_cbranch_scc1 LF_go\n" \
  "s_nop 2\n" \
  "s_cbranch_vccz LF_go\n" \
  "s_mov_b64 exec, vcc\n" \
  "s_sleep 1\n" \
  "s_branch LF_poll\n" \
  "LF_go:\n" \
  "s_mov_b64 exec, -1\n" \
  "v_pk_add_f32 v[8:9], v[8:9], v[12:13]\n" \
  "v_pk_add_f32 v[10:11], v[10:11], v[14:15]\n" \
  "v_pk_add_f32 v[16:17], v[16:17], v[20:21]\n" \
  "v_pk_add_f32 v[18:19], v[18:19], v[22:23]\n" \
  "v_pk_add_f32 v[8:9], v[8:9], v[16:17]\n" \
  "v_pk_add_f32 v[10:11], v[10:11], v[18:19]\n" \
  "v_add_f32 v24, v24, v25\n" \
  "v_permlane16_swap_b32 v8, v9\n" \
  "v_permlane16_swap_b32 v10, v11\n" \
  "v_add_f32 v8, v8, v9\n" \
  "v_add_f32 v10, v10, v11\n" \
  "v_cvt_pk_bf16_f32 v24, v24, v24\n" \
  "s_cmp_lg_u32 s27, 0\n" \
  "v_permlane32_swap_b32 v8, v10\n" \
  "v_add_f32 v8, v8, v10\n" \
  "ds_write_b32 v6, v8\n" \
  "s_waitcnt lgkmcnt(0)\n" \
  "s_barrier\n" \
  "s_cbranch_scc1 LF_end\n" \
  "ds_read2st64_b32 v[8:9], v7 offset0:0 offset1:1\n" \
  "ds_read2st64_b32 v[10:11], v7 offset0:2 offset1:3\n" \
  "v_lshlrev_b32 v24, 16, v24\n" \
  "s_waitcnt lgkmcnt(0)\n" \
  "v_pk_add_f32 v[8:9], v[8:9], v[10:11]\n" \
  "v_add_f32 v8, v8, v9\n" \
  "v_cvt_pk_bf16_f32 v8, v8, v8\n" \
  "v_lshlrev_b32 v8, 16, v8\n" \
  "v_add_f32 v8, s6, v8\n" \
  "v_cvt_pk_bf16_f32 v8, v8, v8\n" \
  "v_lshlrev_b32 v8, 16, v8\n" \
  "v_add_f32 v8, v8, v24\n" \
  "v_cvt_pk_bf16_f32 v8, v8, v8\n" \
  "global_store_short v2, v8, s[24:25]\n" \
  "LF_end:\n" \
  "s_endpgm\n" \
  "LF_b:\n" \
  "s_barrier\n" \
  "s_endpgm\n" \
  "LG_h:\n" \
  "s_cmp_eq_u32 s51, 4\n" \
  "s_cbranch_scc1 LG_chk\n" \
  "s_cmp_lg_u32 s51, 7\n" \
  "s_cbranch_scc1 LG_ret\n" \
  "s_barrier\n" \
  "s_mov_b32 exec_hi, 0\n" \
  "global_atomic_add v196, v199, s[48:49] offset:3076 sc1\n" \
  "s_mov_b64 exec, -1\n" \
  "s_branch LG_aft\n" \
  "LG_pol:\n" \
  "s_mov_b64 exec, 0xff\n" \
  "global_load_dword v200, v198, s[48:49] offset:3076 sc1\n" \
  "s_mov_b64 exec, -1\n" \
  "s_branch LG_rt2\n" \
  "LG_chk:\n" \
  "s_mov_b32 s5, 0x10000\n" \
  "LG_p:\n" \
  "s_mov_b64 exec, 0xff\n" \
  "v_cmp_le_u32 vcc, 32, v200\n" \
  "s_mov_b64 exec, -1\n" \
  "s_sub_u32 s5, s5, 1\n" \
  "s_cselect_b32 s35, 0xff, vcc_lo\n" \
  "s_cmp_eq_u32 s35, 0xff\n" \
  "s_cbranch_scc1 LG_p_x\n" \
  "s_sleep 2\n" \
  "s_mov_b64 exec, 0xff\n" \
  "global_load_dword v200, v198, s[48:49] offset:3076 sc1\n" \
  "s_mov_b64 exec, -1\n" \
  "s_waitcnt vmcnt(0)\n" \
  "s_branch LG_p\n" \
  "LG_p_x:\n" \
  "s_branch LG_ret\n" \
  "LZ_end:\n"

#define R10(p, b) p #b "0", p #b "1", p #b "2", p #b "3", p #b "4", p #b "5", p #b "6", p #b "7", p #b "8", p #b "9"
__global__ __launch_bounds__(512, 2) void k1(const int* __restrict__ idx, const unsigned short* __restrict__ table,
                                             const unsigned short* __restrict__ w_p, const unsigned short* __restrict__ w1,
                                             const unsigned short* __restrict__ b1, const unsigned short* __restrict__ w2,
                                             const unsigned short* __restrict__ b2, char* __restrict__ ws, __hip_bfloat16* __restrict__ out) {
  __shared__ __attribute__((aligned(1024))) char smem[163840];
  unsigned tid = threadIdx.x;
  asm volatile(K_ASM
               : "+{v0}"(tid)
               : "{s[0:1]}"(__builtin_amdgcn_kernarg_segment_ptr()), "{s26}"(blockIdx.x), "s"((unsigned)(uintptr_t)smem)
               : "memory", "scc", "vcc", "s3", "s4", "s5", "s6", "s7", "s8", "s9", R10("s", 1), "s20", "s21", "s22", "s23", "s24", "s25", "s27", "s28", "s29", "s30", "s31", "s33", "s34", "s35", "s36", "s37", "s38", "s39", R10("s", 4), R10("s", 5), R10("s", 6), R10("s", 7), R10("s", 8), R10("s", 9),
                 "v1", "v2", "v3", "v4", "v5", "v6", "v7", "v8", "v9", R10("v", 1), R10("v", 2), R10("v", 3), R10("v", 4), R10("v", 5), R10("v", 6), R10("v", 7), R10("v", 8), R10("v", 9),
                 R10("v", 10), R10("v", 11), R10("v", 12), R10("v", 13), R10("v", 14), R10("v", 15), R10("v", 16), R10("v", 17), R10("v", 18), R10("v", 19),
                 R10("v", 20), R10("v", 21), R10("v", 22), R10("v", 23), R10("v", 24), "v250", "v251", "v252", "v253", "v254", "v255");
}

extern "C" void kernel_launch(void* const* d_in, const int* in_sizes, int n_in, void* d_out, int out_size, void* d_ws,
                              size_t ws_size, hipStream_t stream) {
  k1<<<dim3(256), dim3(512), 0, stream>>>((const int*)d_in[0], (const unsigned short*)d_in[1], (const unsigned short*)d_in[2], (const unsigned short*)d_in[3],
                                          (const unsigned short*)d_in[4], (const unsigned short*)d_in[5], (const unsigned short*)d_in[6], (char*)d_ws, (__hip_bfloat16*)d_out);
}
```

```cpp
#include <hip/hip_runtime.h>
#include <hip/hip_bf16.h>
#include <stdint.h>

#define K_ASM \
  "LZ_first:\n" \
  "s_load_dwordx16 s[8:23], s[0:1], 0x0\n" \
  "v_readfirstlane_b32 s27, v0\n" \
  "v_and_b32 v255, 63, v0\n" \
  "s_and_b32 s29, s26, 7\n" \
  "s_lshr_b32 s30, s26, 3\n" \
  "s_lshr_b32 s27, s27, 6\n" \
  "s_and_b32 s31, s30, 1\n" \
  "s_and_b32 s28, s27, 3\n" \
  "s_lshr_b32 s35, s30, 1\n" \
  "s_lshl3_add_u32 s7, s35, s29\n" \
  "v_lshrrev_b32 v1, 3, v255\n" \
  "v_and_b32 v2, 7, v255\n" \
  "v_lshlrev_b32 v1, 8, v1\n" \
  "v_lshl_or_b32 v254, v2, 4, v1\n" \
  "s_min_u32 s36, s7, 122\n" \
  "s_add_u32 s36, s36, s28\n" \
  "s_cmp_ge_u32 s27, 4\n" \
  "s_cbranch_scc1 LB_start\n" \
  "s_setprio 3\n" \
  "v_and_b32 v252, 15, v255\n" \
  "v_lshrrev_b32 v253, 4, v255\n" \
  "v_mul_u32_u24 v2, 400, v253\n" \
  "v_min_u32 v3, 8, v252\n" \
  "v_lshl_add_u32 v250, v252, 4, v2\n" \
  "v_lshl_add_u32 v251, v3, 4, v2\n" \
  "s_lshl2_add_u32 s36, s7, s28\n" \
  "s_mul_i32 s36, s36, 12800\n" \
  "s_lshl_b32 s35, s28, 10\n" \
  "v_lshl_add_u32 v1, v255, 4, s35\n" \
  "s_add_u32 m0, s35, 138240\n" \
  "s_waitcnt lgkmcnt(0)\n" \
  "s_add_u32 s44, s8, s36\n" \
  "s_addc_u32 s45, s9, 0\n" \
  "global_load_lds_dwordx4 v1, s[12:13]\n" \
  "s_add_u32 m0, m0, 0x1000\n" \
  "v_add_u32 v1, 0x1000, v1\n" \
  "global_load_lds_dwordx4 v1, s[12:13]\n" \
  "s_add_u32 m0, m0, 0x1000\n" \
  "v_add_u32 v1, 0x1000, v1\n" \
  "global_load_lds_dwordx4 v1, s[12:13]\n" \
  "s_add_u32 m0, m0, 0x1000\n" \
  "v_add_u32 v1, 0x1000, v1\n" \
  "global_load_lds_dwordx4 v1, s[12:13]\n" \
  "s_add_u32 m0, m0, 0x1000\n" \
  "v_add_u32 v1, 0x1000, v1\n" \
  "v_min_u32 v1, 19984, v1\n" \
  "global_load_lds_dwordx4 v1, s[12:13]\n" \
  "global_load_dwordx4 v[232:235], v250, s[44:45] offset:0\n" \
  "global_load_dwordx4 v[236:239], v251, s[44:45] offset:256\n" \
  "global_load_dwordx4 v[240:243], v250, s[44:45] offset:1600\n" \
  "global_load_dwordx4 v[244:247], v251, s[44:45] offset:1856\n" \
  "s_setprio 1\n" \
  "v_lshrrev_b32 v1, 2, v252\n" \
  "v_and_b32 v2, 3, v252\n" \
  "v_cmp_eq_u32 vcc, v1, v253\n" \
  "v_mov_b32 v4, 0x3f80\n" \
  "v_and_b32 v13, 1, v253\n" \
  "v_lshlrev_b32 v14, 1, v13\n" \
  "v_xor_b32 v2, v2, v14\n" \
  "v_cndmask_b32 v2, 4, v2, vcc\n" \
  "v_cmp_eq_u32 vcc, 0, v2\n" \
  "s_nop 1\n" \
  "v_cndmask_b32 v216, 0, v4, vcc\n" \
  "v_lshlrev_b32 v220, 16, v216\n" \
  "v_cmp_eq_u32 vcc, 1, v2\n" \
  "s_nop 1\n" \
  "v_cndmask_b32 v217, 0, v4, vcc\n" \
  "v_lshlrev_b32 v221, 16, v217\n" \
  "v_cmp_eq_u32 vcc, 2, v2\n" \
  "s_nop 1\n" \
  "v_cndmask_b32 v218, 0, v4, vcc\n" \
  "v_lshlrev_b32 v222, 16, v218\n" \
  "v_cmp_eq_u32 vcc, 3, v2\n" \
  "s_nop 1\n" \
  "v_cndmask_b32 v219, 0, v4, vcc\n" \
  "v_lshlrev_b32 v223, 16, v219\n" \
  "v_and_b32 v1, 2, v252\n" \
  "v_lshlrev_b32 v1, 3, v1\n" \
  "v_lshlrev_b32 v1, v1, v4\n" \
  "v_and_b32 v2, 0x31, v255\n" \
  "v_mov_b64 v[224:225], 0\n" \
  "v_cmp_eq_u32 vcc, 0, v2\n" \
  "s_nop 1\n" \
  "v_cndmask_b32 v226, 0, v1, vcc\n" \
  "v_cmp_eq_u32 vcc, 1, v2\n" \
  "s_nop 1\n" \
  "v_cndmask_b32 v227, 0, v1, vcc\n" \
  "v_mul_u32_u24 v5, 400, v252\n" \
  "v_add_u32 v5, 138240, v5\n" \
  "v_lshl_add_u32 v6, v253, 4, v5\n" \
  "v_add_u32 v7, 6400, v5\n" \
  "v_add_u32 v8, 6400, v6\n" \
  "v_add_u32 v9, 12800, v5\n" \
  "v_add_u32 v10, 12800, v6\n" \
  "v_bfe_u32 v1, v252, 1, 1\n" \
  "v_and_b32 v2, 1, v252\n" \
  "v_lshl_add_u32 v1, v2, 1, v1\n" \
  "v_mul_u32_u24 v1, 200, v1\n" \
  "v_add_u32 v11, 157440, v1\n" \
  "v_lshl_add_u32 v12, v253, 4, v11\n" \
  "v_lshlrev_b32 v13, 3, v13\n" \
  "v_sub_u32 v16, v5, v13\n" \
  "v_add_u32 v5, v5, v13\n" \
  "v_sub_u32 v17, v6, v13\n" \
  "v_add_u32 v6, v6, v13\n" \
  "v_sub_u32 v18, v7, v13\n" \
  "v_add_u32 v7, v7, v13\n" \
  "v_sub_u32 v19, v8, v13\n" \
  "v_add_u32 v8, v8, v13\n" \
  "v_sub_u32 v20, v9, v13\n" \
  "v_add_u32 v9, v9, v13\n" \
  "v_sub_u32 v21, v10, v13\n" \
  "v_add_u32 v10, v10, v13\n" \
  "v_sub_u32 v22, v11, v13\n" \
  "v_add_u32 v11, v11, v13\n" \
  "v_sub_u32 v23, v12, v13\n" \
  "v_add_u32 v12, v12, v13\n" \
  "v_lshlrev_b32 v188, 4, v253\n" \
  "v_and_b32 v96, 4, v252\n" \
  "v_lshl_add_u32 v96, v253, 5, v96\n" \
  "v_mov_b32 v191, 0x40000000\n" \
  "v_cmp_gt_u32 vcc, 8, v252\n" \
  "v_mov_b32 v193, 0x4444\n" \
  "v_mov_b32 v98, 0xeeee\n" \
  "v_cndmask_b32 v189, v191, v96, vcc\n" \
  "v_cndmask_b32 v190, v96, v191, vcc\n" \
  "v_and_b32 v99, 4, v252\n" \
  "v_cmp_ne_u32 vcc, 0, v99\n" \
  "s_lshl_b32 s35, s28, 11\n" \
  "s_add_u32 s35, s35, 130048\n" \
  "v_cndmask_b32 v193, v193, v98, vcc\n" \
  "v_lshl_add_u32 v194, v252, 4, s35\n" \
  "v_and_b32 v96, 7, v252\n" \
  "v_lshlrev_b32 v96, 5, v96\n" \
  "v_lshl_add_u32 v96, v253, 8, v96\n" \
  "v_lshrrev_b32 v97, 3, v252\n" \
  "v_lshl_add_u32 v96, v97, 2, v96\n" \
  "v_add_u32 v195, s35, v96\n" \
  "v_and_b32 v97, v97, v252\n" \
  "v_lshlrev_b32 v192, 4, v97\n" \
  "s_waitcnt vmcnt(4)\n" \
  "s_barrier\n" \
  "ds_read_b64 v[200:201], v12 offset:128\n" \
  "ds_read_b64 v[202:203], v23 offset:136\n" \
  "ds_read_b64 v[184:185], v9 offset:184\n" \
  "ds_read_b64 v[186:187], v20 offset:192\n" \
  "ds_read_b64 v[212:213], v9 offset:384\n" \
  "ds_read_b64 v[214:215], v20 offset:392\n" \
  "ds_read_b64 v[196:197], v12 offset:64\n" \
  "ds_read_b64 v[198:199], v23 offset:72\n" \
  "ds_read_b64 v[176:177], v7 offset:184\n" \
  "ds_read_b64 v[178:179], v18 offset:192\n" \
  "ds_read_b64 v[180:181], v7 offset:384\n" \
  "ds_read_b64 v[182:183], v18 offset:392\n" \
  "ds_read_b64 v[228:229], v12 offset:0\n" \
  "ds_read_b64 v[230:231], v23 offset:8\n" \
  "s_waitcnt lgkmcnt(12)\n" \
  "v_mfma_f32_16x16x32_bf16 v[44:47], v[200:203], v[216:219], 0\n" \
  "ds_read_b64 v[168:169], v5 offset:184\n" \
  "ds_read_b64 v[170:171], v16 offset:192\n" \
  "s_waitcnt lgkmcnt(12)\n" \
  "v_mfma_f32_16x16x32_bf16 v[44:47], v[224:227], v[184:187], v[44:47]\n" \
  "ds_read_b64 v[172:173], v5 offset:384\n" \
  "ds_read_b64 v[174:175], v16 offset:392\n" \
  "v_mfma_f32_16x16x32_bf16 v[208:211], v[200:203], v[220:223], 0\n" \
  "ds_read_b64 v[96:97], v6 offset:0\n" \
  "ds_read_b64 v[98:99], v17 offset:8\n" \
  "s_waitcnt lgkmcnt(14)\n" \
  "v_mfma_f32_16x16x32_bf16 v[208:211], v[224:227], v[212:215], v[208:211]\n" \
  "ds_read_b64 v[124:125], v8 offset:64\n" \
  "ds_read_b64 v[126:127], v19 offset:72\n" \
  "s_waitcnt lgkmcnt(14)\n" \
  "v_mfma_f32_16x16x32_bf16 v[60:63], v[196:199], v[216:219], 0\n" \
  "ds_read_b64 v[136:137], v8 offset:264\n" \
  "ds_read_b64 v[138:139], v19 offset:272\n" \
  "s_waitcnt lgkmcnt(14)\n" \
  "v_mfma_f32_16x16x32_bf16 v[60:63], v[224:227], v[176:179], v[60:63]\n" \
  "ds_read_b64 v[140:141], v8 offset:328\n" \
  "ds_read_b64 v[142:143], v19 offset:336\n" \
  "v_mfma_f32_16x16x32_bf16 v[184:187], v[196:199], v[220:223], 0\n" \
  "ds_read_b64 v[160:161], v10 offset:264\n" \
  "ds_read_b64 v[162:163], v21 offset:272\n" \
  "s_waitcnt lgkmcnt(15)\n" \
  "v_mfma_f32_16x16x32_bf16 v[184:187], v[224:227], v[180:183], v[184:187]\n" \
  "ds_read_b64 v[116:117], v6 offset:328\n" \
  "ds_read_b64 v[118:119], v17 offset:336\n" \
  "s_waitcnt lgkmcnt(15)\n" \
  "v_mfma_f32_16x16x32_bf16 v[68:71], v[228:231], v[216:219], 0\n" \
  "ds_read_b64 v[144:145], v10 offset:0\n" \
  "ds_read_b64 v[146:147], v21 offset:8\n" \
  "s_waitcnt lgkmcnt(15)\n" \
  "v_mfma_f32_16x16x32_bf16 v[68:71], v[224:227], v[168:171], v[68:71]\n" \
  "ds_read_b64 v[148:149], v10 offset:64\n" \
  "ds_read_b64 v[150:151], v21 offset:72\n" \
  "v_mfma_f32_16x16x32_bf16 v[212:215], v[228:231], v[220:223], 0\n" \
  "ds_read_b64 v[104:105], v6 offset:128\n" \
  "ds_read_b64 v[106:107], v17 offset:136\n" \
  "s_waitcnt lgkmcnt(15)\n" \
  "v_mfma_f32_16x16x32_bf16 v[212:215], v[224:227], v[172:175], v[212:215]\n" \
  "ds_read_b64 v[100:101], v6 offset:64\n" \
  "ds_read_b64 v[102:103], v17 offset:72\n" \
  "v_mfma_f32_16x16x32_bf16 v[92:95], v[96:99], v[216:219], 0\n" \
  "ds_read_b64 v[120:121], v8 offset:0\n" \
  "ds_read_b64 v[122:123], v19 offset:8\n" \
  "s_waitcnt lgkmcnt(15)\n" \
  "v_mfma_f32_16x16x32_bf16 v[24:27], v[124:127], v[220:223], 0\n" \
  "ds_read_b64 v[112:113], v6 offset:264\n" \
  "ds_read_b64 v[114:115], v17 offset:272\n" \
  "v_mfma_f32_16x16x32_bf16 v[24:27], v[216:219], v[136:139], v[24:27]\n" \
  "ds_read_b64 v[156:157], v10 offset:200\n" \
  "ds_read_b64 v[158:159], v21 offset:208\n" \
  "s_waitcnt lgkmcnt(15)\n" \
  "v_mfma_f32_16x16x32_bf16 v[28:31], v[140:143], v[220:223], 0\n" \
  "ds_read_b64 v[132:133], v8 offset:200\n" \
  "ds_read_b64 v[134:135], v19 offset:208\n" \
  "v_mfma_f32_16x16x32_bf16 v[28:31], v[220:223], v[160:163], v[28:31]\n" \
  "ds_read_b64 v[108:109], v6 offset:200\n" \
  "ds_read_b64 v[110:111], v17 offset:208\n" \
  "s_waitcnt lgkmcnt(15)\n" \
  "v_mfma_f32_16x16x32_bf16 v[32:35], v[116:119], v[216:219], 0\n" \
  "ds_read_b64 v[152:153], v10 offset:128\n" \
  "ds_read_b64 v[154:155], v21 offset:136\n" \
  "v_mfma_f32_16x16x32_bf16 v[32:35], v[220:223], v[144:147], v[32:35]\n" \
  "ds_read_b64 v[164:165], v10 offset:328\n" \
  "ds_read_b64 v[166:167], v21 offset:336\n" \
  "v_mfma_f32_16x16x32_bf16 v[36:39], v[140:143], v[216:219], 0\n" \
  "ds_read_b64 v[128:129], v8 offset:128\n" \
  "ds_read_b64 v[130:131], v19 offset:136\n" \
  "s_waitcnt lgkmcnt(15)\n" \
  "v_mfma_f32_16x16x32_bf16 v[36:39], v[220:223], v[148:151], v[36:39]\n" \
  "ds_read_b64 v[204:205], v11 offset:184\n" \
  "ds_read_b64 v[206:207], v22 offset:192\n" \
  "v_mfma_f32_16x16x32_bf16 v[40:43], v[104:107], v[216:219], 0\n" \
  "v_mfma_f32_16x16x32_bf16 v[40:43], v[216:219], v[144:147], v[40:43]\n" \
  "v_mfma_f32_16x16x32_bf16 v[48:51], v[100:103], v[216:219], 0\n" \
  "s_waitcnt lgkmcnt(15)\n" \
  "v_mfma_f32_16x16x32_bf16 v[48:51], v[216:219], v[120:123], v[48:51]\n" \
  "s_waitcnt lgkmcnt(14)\n" \
  "v_mfma_f32_16x16x32_bf16 v[52:55], v[112:115], v[216:219], 0\n" \
  "v_mfma_f32_16x16x32_bf16 v[52:55], v[220:223], v[120:123], v[52:55]\n" \
  "v_mfma_f32_16x16x32_bf16 v[56:59], v[116:119], v[220:223], 0\n" \
  "s_waitcnt lgkmcnt(12)\n" \
  "v_mfma_f32_16x16x32_bf16 v[56:59], v[220:223], v[156:159], v[56:59]\n" \
  "s_waitcnt vmcnt(0) lgkmcnt(0)\n" \
  "v_mfma_f32_16x16x32_bf16 v[64:67], v[104:107], v[220:223], 0\n" \
  "v_cvt_pk_u16_u32 v232, v232, v233\n" \
  "v_cvt_pk_u16_u32 v234, v234, v235\n" \
  "v_mfma_f32_16x16x32_bf16 v[64:67], v[216:219], v[156:159], v[64:67]\n" \
  "v_cvt_pk_u16_u32 v236, v236, v237\n" \
  "v_cvt_pk_u16_u32 v238, v238, v239\n" \
  "v_mfma_f32_16x16x32_bf16 v[72:75], v[136:139], v[220:223], 0\n" \
  "v_pk_lshrrev_b16 v233, 3, v232 op_sel_hi:[0,1]\n" \
  "v_pk_lshrrev_b16 v235, 3, v234 op_sel_hi:[0,1]\n" \
  "v_mfma_f32_16x16x32_bf16 v[76:79], v[112:115], v[220:223], 0\n" \
  "v_pk_lshrrev_b16 v237, 3, v236 op_sel_hi:[0,1]\n" \
  "v_pk_lshrrev_b16 v239, 3, v238 op_sel_hi:[0,1]\n" \
  "v_mfma_f32_16x16x32_bf16 v[76:79], v[220:223], v[132:135], v[76:79]\n" \
  "v_pk_mad_u16 v180, v232, 8, v233 op_sel_hi:[1,0,1]\n" \
  "v_pk_mad_u16 v181, v234, 8, v235 op_sel_hi:[1,0,1]\n" \
  "v_mfma_f32_16x16x32_bf16 v[80:83], v[100:103], v[220:223], 0\n" \
  "v_pk_mad_u16 v182, v236, 8, v237 op_sel_hi:[1,0,1]\n" \
  "v_pk_mad_u16 v183, v238, 8, v239 op_sel_hi:[1,0,1]\n" \
  "v_mfma_f32_16x16x32_bf16 v[80:83], v[216:219], v[132:135], v[80:83]\n" \
  "v_alignbit_b32 v182, v182, v182, v192\n" \
  "v_alignbit_b32 v183, v183, v183, v192\n" \
  "v_mfma_f32_16x16x32_bf16 v[84:87], v[96:99], v[220:223], 0\n" \
  "v_cvt_pk_u16_u32 v240, v240, v241\n" \
  "v_cvt_pk_u16_u32 v242, v242, v243\n" \
  "v_mfma_f32_16x16x32_bf16 v[84:87], v[216:219], v[108:111], v[84:87]\n" \
  "v_cvt_pk_u16_u32 v244, v244, v245\n" \
  "v_cvt_pk_u16_u32 v246, v246, v247\n" \
  "v_mfma_f32_16x16x32_bf16 v[88:91], v[108:111], v[220:223], 0\n" \
  "v_pk_lshrrev_b16 v241, 3, v240 op_sel_hi:[0,1]\n" \
  "v_pk_lshrrev_b16 v243, 3, v242 op_sel_hi:[0,1]\n" \
  "v_mfma_f32_16x16x32_bf16 v[0:3], v[124:127], v[216:219], 0\n" \
  "v_pk_lshrrev_b16 v245, 3, v244 op_sel_hi:[0,1]\n" \
  "v_pk_lshrrev_b16 v247, 3, v246 op_sel_hi:[0,1]\n" \
  "v_mfma_f32_16x16x32_bf16 v[4:7], v[152:155], v[216:219], 0\n" \
  "v_pk_mad_u16 v196, v240, 8, v241 op_sel_hi:[1,0,1]\n" \
  "v_pk_mad_u16 v197, v242, 8, v243 op_sel_hi:[1,0,1]\n" \
  "v_mfma_f32_16x16x32_bf16 v[8:11], v[152:155], v[220:223], 0\n" \
  "v_pk_mad_u16 v198, v244, 8, v245 op_sel_hi:[1,0,1]\n" \
  "v_pk_mad_u16 v199, v246, 8, v247 op_sel_hi:[1,0,1]\n" \
  "v_mfma_f32_16x16x32_bf16 v[8:11], v[216:219], v[164:167], v[8:11]\n" \
  "v_alignbit_b32 v198, v198, v198, v192\n" \
  "v_alignbit_b32 v199, v199, v199, v192\n" \
  "v_mfma_f32_16x16x32_bf16 v[12:15], v[164:167], v[220:223], 0\n" \
  "v_add_u32 v200, 0x400, v195\n" \
  "v_mfma_f32_16x16x32_bf16 v[16:19], v[128:131], v[220:223], 0\n" \
  "v_mfma_f32_16x16x32_bf16 v[16:19], v[216:219], v[160:163], v[16:19]\n" \
  "v_mfma_f32_16x16x32_bf16 v[20:23], v[128:131], v[216:219], 0\n" \
  "v_mfma_f32_16x16x32_bf16 v[20:23], v[216:219], v[148:151], v[20:23]\n" \
  "v_mfma_f32_16x16x32_bf16 v[168:171], v[216:219], v[96:99], 0\n" \
  "v_mfma_f32_16x16x32_bf16 v[172:175], v[204:207], v[224:227], 0\n" \
  "v_mfma_f32_16x16x32_bf16 v[176:179], v[224:227], v[204:207], 0\n" \
  "s_mov_b32 exec_lo, 0x0\n" \
  "s_mov_b32 exec_hi, 0xffffffff\n" \
  "v_mov_b64 v[44:45], v[208:209]\n" \
  "v_mov_b64 v[46:47], v[210:211]\n" \
  "v_mov_b64 v[60:61], v[184:185]\n" \
  "v_mov_b64 v[62:63], v[186:187]\n" \
  "v_mov_b64 v[68:69], v[212:213]\n" \
  "v_mov_b64 v[70:71], v[214:215]\n" \
  "s_mov_b32 exec_lo, 0xff00ff00\n" \
  "s_mov_b32 exec_hi, 0x0\n" \
  "v_add_f32 v92, v92, v168\n" \
  "v_add_f32 v93, v93, v169\n" \
  "v_add_f32 v94, v94, v170\n" \
  "v_add_f32 v95, v95, v171\n" \
  "s_mov_b32 exec_lo, 0xf0000f00\n" \
  "s_mov_b32 exec_hi, 0x0\n" \
  "v_sub_f32 v92, v92, v176\n" \
  "v_sub_f32 v93, v93, v177\n" \
  "v_sub_f32 v94, v94, v178\n" \
  "v_sub_f32 v95, v95, v179\n" \
  "s_mov_b32 exec_lo, 0x0\n" \
  "s_mov_b32 exec_hi, 0xff00ff\n" \
  "v_mov_b64 v[92:93], 0\n" \
  "v_mov_b64 v[94:95], 0\n" \
  "s_mov_b32 exec_lo, 0x0\n" \
  "s_mov_b32 exec_hi, 0xf0000f\n" \
  "v_mov_b64 v[92:93], v[172:173]\n" \
  "v_mov_b64 v[94:95], v[174:175]\n" \
  "s_mov_b64 exec, -1\n" \
  "v_add_u32 v96, 6400, v250\n" \
  "v_add_u32 v97, 6400, v251\n" \
  "v_mov_b32 v98, 163328\n" \
  "v_mov_b32 v99, 1\n" \
  "v_lshlrev_b32 v203, 4, v253\n" \
  "s_lshr_b32 s35, s30, 4\n" \
  "s_lshl1_add_u32 s35, s29, s35\n" \
  "s_lshl_b32 s35, s35, 6\n" \
  "s_bfe_u32 s37, s30, 0x20002\n" \
  "s_lshl3_add_u32 s35, s37, s35\n" \
  "v_and_b32 v203, 16, v203\n" \
  "v_lshl_add_u32 v203, v203, 1, s35\n" \
  "s_mov_b64 exec, 1\n" \
  "ds_add_u32 v98, v99 offset:16\n" \
  "s_mov_b64 exec, -1\n" \
  "ds_write2_b32 v195, v180, v181 offset0:0 offset1:4\n" \
  "ds_write2_b32 v195, v182, v183 offset0:2 offset1:6\n" \
  "ds_write2_b32 v200, v196, v197 offset0:0 offset1:4\n" \
  "ds_write2_b32 v200, v198, v199 offset0:2 offset1:6\n" \
  "global_load_dwordx2 v[204:205], v203, s[16:17]\n" \
  "global_load_dwordx2 v[206:207], v203, s[18:19]\n" \
  "v_mov_b32 v203, 0\n" \
  "global_load_ushort v203, v203, s[20:21]\n" \
  "s_add_u32 s46, s44, 4800\n" \
  "s_addc_u32 s47, s45, 0\n" \
  "global_load_dwordx4 v[208:211], v96, s[44:45] offset:-3200\n" \
  "global_load_dwordx4 v[212:215], v97, s[44:45] offset:-2944\n" \
  "global_load_dwordx4 v[216:219], v96, s[44:45] offset:-1600\n" \
  "global_load_dwordx4 v[220:223], v97, s[44:45] offset:-1344\n" \
  "global_load_dwordx4 v[224:227], v96, s[44:45] offset:0\n" \
  "global_load_dwordx4 v[228:231], v97, s[44:45] offset:256\n" \
  "global_load_dwordx4 v[232:235], v96, s[44:45] offset:1600\n" \
  "global_load_dwordx4 v[236:239], v97, s[44:45] offset:1856\n" \
  "global_load_dwordx4 v[240:243], v96, s[44:45] offset:3200\n" \
  "global_load_dwordx4 v[244:247], v97, s[44:45] offset:3456\n" \
  "global_load_dwordx4 v[248:251], v96, s[46:47] offset:0\n" \
  "global_load_dwordx4 v[252:255], v97, s[46:47] offset:256\n" \
  "ds_read_b128 v[180:183], v194\n" \
  "v_add_u32 v194, 0x100, v194\n" \
  "L1_:\n" \
  "ds_read_b32 v99, v98 offset:12\n" \
  "s_waitcnt lgkmcnt(0)\n" \
  "v_readfirstlane_b32 s35, v99\n" \
  "s_cmp_lt_u32 s35, 4\n" \
  "s_cbranch_scc1 L1_\n" \
  "s_setprio 1\n" \
  "v_mad_u32_u16 v184, v182, 16, v188\n" \
  "ds_read_b128 v[128:131], v184 offset:0\n" \
  "ds_read_b128 v[132:135], v184 offset:64\n" \
  "v_mad_u32_u16 v185, v181, 16, v188\n" \
  "v_mad_u32_u16 v186, v182, 16, v188 op_sel:[1,0,0,0]\n" \
  "ds_read_b128 v[112:115], v185 offset:0\n" \
  "ds_read_b128 v[116:119], v185 offset:64\n" \
  "ds_read_b128 v[136:139], v186 offset:0\n" \
  "ds_read_b128 v[140:143], v186 offset:64\n" \
  "v_mad_u32_u16 v187, v181, 16, v188 op_sel:[1,0,0,0]\n" \
  "v_mad_u32_u16 v184, v180, 16, v188 op_sel:[1,0,0,0]\n" \
  "ds_read_b128 v[120:123], v187 offset:0\n" \
  "v_mad_u32_u16 v185, v180, 16, v188\n" \
  "ds_read_b128 v[124:127], v187 offset:64\n" \
  "ds_read_b128 v[104:107], v184 offset:0\n" \
  "v_mad_u32_u16 v186, v183, 16, v189\n" \
  "ds_read_b128 v[108:111], v184 offset:64\n" \
  "ds_read_b128 v[96:99], v185 offset:0\n" \
  "v_mad_u32_u16 v187, v183, 16, v190\n" \
  "s_waitcnt lgkmcnt(8)\n" \
  "s_mov_b32 s51, 3\n" \
  "s_mov_b32 s50, 8\n" \
  "s_call_b64 s[52:53], LA_loop\n" \
  "LA_ev0:\n" \
  "s_waitcnt vmcnt(10)\n" \
  "v_cvt_pk_u16_u32 v208, v208, v209\n" \
  "v_cvt_pk_u16_u32 v210, v210, v211\n" \
  "v_cvt_pk_u16_u32 v212, v212, v213\n" \
  "v_cvt_pk_u16_u32 v214, v214, v215\n" \
  "v_pk_lshrrev_b16 v209, 3, v208 op_sel_hi:[0,1]\n" \
  "v_pk_lshrrev_b16 v211, 3, v210 op_sel_hi:[0,1]\n" \
  "v_pk_lshrrev_b16 v213, 3, v212 op_sel_hi:[0,1]\n" \
  "v_pk_lshrrev_b16 v215, 3, v214 op_sel_hi:[0,1]\n" \
  "v_pk_mad_u16 v208, v208, 8, v209 op_sel_hi:[1,0,1]\n" \
  "v_pk_mad_u16 v210, v210, 8, v211 op_sel_hi:[1,0,1]\n" \
  "v_pk_mad_u16 v212, v212, 8, v213 op_sel_hi:[1,0,1]\n" \
  "v_pk_mad_u16 v214, v214, 8, v215 op_sel_hi:[1,0,1]\n" \
  "v_alignbit_b32 v212, v212, v212, v192\n" \
  "v_alignbit_b32 v214, v214, v214, v192\n" \
  "ds_write2_b32 v195, v208, v210 offset0:0 offset1:4\n" \
  "ds_write2_b32 v195, v212, v214 offset0:2 offset1:6\n" \
  "v_mov_b32 v196, v179\n" \
  "v_readfirstlane_b32 s6, v203\n" \
  "v_mov_b32 v208, 163328\n" \
  "s_mov_b32 s51, 4\n" \
  "s_mov_b32 s50, 1\n" \
  "s_mov_b32 s38, 0x800\n" \
  "s_call_b64 s[52:53], LA_loop\n" \
  "LA_ev6:\n" \
  "v_mov_b32 v202, v179\n" \
  "v_readfirstlane_b32 s35, v209\n" \
  "s_cmp_ge_u32 s35, 128\n" \
  "s_cbranch_scc1 LA_pl\n" \
  "L2_:\n" \
  "ds_read_b32 v209, v208 offset:4\n" \
  "s_waitcnt lgkmcnt(0)\n" \
  "v_readfirstlane_b32 s35, v209\n" \
  "s_cmp_lt_u32 s35, 128\n" \
  "s_cbranch_scc1 L2_\n" \
  "LA_pl:\n" \
  "s_and_b32 s4, s35, 15\n" \
  "s_sub_u32 s4, s4, 4\n" \
  "s_bfe_u32 s5, s35, 0x30004\n" \
  "s_sub_u32 s5, s5, s29\n" \
  "s_cmp_le_i32 s4, 0\n" \
  "s_cbranch_scc1 LA_nt\n" \
  "s_mul_i32 s35, s5, 51200\n" \
  "s_ashr_i32 s36, s35, 31\n" \
  "s_add_u32 s46, s46, s35\n" \
  "s_addc_u32 s47, s47, s36\n" \
  "v_lshrrev_b32 v216, 4, v188\n" \
  "v_mul_u32_u24 v216, 400, v216\n" \
  "v_add_u32 v216, 6400, v216\n" \
  "v_bfe_u32 v218, v194, 4, 4\n" \
  "v_min_u32 v217, 8, v218\n" \
  "v_lshl_add_u32 v217, v217, 4, v216\n" \
  "v_lshl_add_u32 v216, v218, 4, v216\n" \
  "global_load_dwordx4 v[208:211], v216, s[46:47] offset:0\n" \
  "global_load_dwordx4 v[212:215], v217, s[46:47] offset:256\n" \
  "LA_nt:\n" \
  "s_lshl_b32 s5, s5, 7\n" \
  "s_sub_u32 s5, s5, s4\n" \
  "s_min_i32 s51, s4, 0\n" \
  "s_add_u32 s51, s51, 4\n" \
  "s_cmp_eq_u32 s51, 0\n" \
  "s_cbranch_scc1 LA_ev7\n" \
  "s_call_b64 s[52:53], LA_loop\n" \
  "LA_ev7:\n" \
  "v_mov_b32 v203, v179\n" \
  "s_cmp_le_i32 s4, 0\n" \
  "s_cbranch_scc1 LA_ev8\n" \
  "s_waitcnt vmcnt(0)\n" \
  "v_cvt_pk_u16_u32 v208, v208, v209\n" \
  "v_cvt_pk_u16_u32 v210, v210, v211\n" \
  "v_cvt_pk_u16_u32 v212, v212, v213\n" \
  "v_cvt_pk_u16_u32 v214, v214, v215\n" \
  "v_pk_lshrrev_b16 v209, 3, v208 op_sel_hi:[0,1]\n" \
  "v_pk_lshrrev_b16 v211, 3, v210 op_sel_hi:[0,1]\n" \
  "v_pk_lshrrev_b16 v213, 3, v212 op_sel_hi:[0,1]\n" \
  "v_pk_lshrrev_b16 v215, 3, v214 op_sel_hi:[0,1]\n" \
  "v_pk_mad_u16 v208, v208, 8, v209 op_sel_hi:[1,0,1]\n" \
  "v_pk_mad_u16 v210, v210, 8, v211 op_sel_hi:[1,0,1]\n" \
  "v_pk_mad_u16 v212, v212, 8, v213 op_sel_hi:[1,0,1]\n" \
  "v_pk_mad_u16 v214, v214, 8, v215 op_sel_hi:[1,0,1]\n" \
  "v_alignbit_b32 v212, v212, v212, v192\n" \
  "v_alignbit_b32 v214, v214, v214, v192\n" \
  "ds_write2_b32 v195, v208, v210 offset0:0 offset1:4\n" \
  "ds_write2_b32 v195, v212, v214 offset0:2 offset1:6\n" \
  "s_lshl_b32 s35, s4, 8\n" \
  "s_add_u32 s35, s35, 0x400\n" \
  "v_subrev_u32 v194, s35, v194\n" \
  "s_mov_b32 s51, s4\n" \
  "s_call_b64 s[52:53], LA_loop\n" \
  "LA_ev8:\n" \
  "v_mov_b32 v195, v179\n" \
  "ds_read_b128 v[100:103], v185 offset:64\n" \
  "ds_read2_b32 v[144:145], v186 offset0:0 offset1:2\n" \
  "ds_read2_b32 v[146:147], v186 offset0:4 offset1:6\n" \
  "ds_read2_b32 v[148:149], v187 offset0:0 offset1:2\n" \
  "ds_read2_b32 v[150:151], v187 offset0:4 offset1:6\n" \
  "s_lshr_b32 s35, s30, 2\n" \
  "s_lshl3_add_u32 s33, s29, s35\n" \
  "s_and_b32 s34, s30, 3\n" \
  "s_lshl_b32 s59, s27, 11\n" \
  "s_mul_i32 s35, s33, 1600000\n" \
  "s_add_u32 s54, s22, s35\n" \
  "s_addc_u32 s55, s23, 0\n" \
  "s_mul_i32 s35, s34, 819200\n" \
  "s_lshl1_add_u32 s35, s59, s35\n" \
  "s_add_u32 s35, s35, 102400000\n" \
  "s_add_u32 s56, s22, s35\n" \
  "s_addc_u32 s57, s23, 0\n" \
  "v_mbcnt_lo_u32_b32 v189, -1, 0\n" \
  "v_mbcnt_hi_u32_b32 v189, -1, v189\n" \
  "v_lshlrev_b32 v188, 4, v189\n" \
  "s_waitcnt lgkmcnt(5)\n" \
  "v_fmac_f32 v176, v153, v73\n" \
  "v_pk_fma_f32 v[176:177], v[154:155], v[74:75], v[176:177]\n" \
  "v_pk_fma_f32 v[176:177], v[156:157], v[76:77], v[176:177]\n" \
  "v_pk_fma_f32 v[176:177], v[158:159], v[78:79], v[176:177]\n" \
  "v_pk_fma_f32 v[176:177], v[160:161], v[80:81], v[176:177]\n" \
  "v_pk_fma_f32 v[176:177], v[162:163], v[82:83], v[176:177]\n" \
  "v_pk_fma_f32 v[176:177], v[164:165], v[84:85], v[176:177]\n" \
  "v_pk_fma_f32 v[176:177], v[166:167], v[86:87], v[176:177]\n" \
  "v_pk_fma_f32 v[176:177], v[168:169], v[88:89], v[176:177]\n" \
  "v_pk_fma_f32 v[176:177], v[170:171], v[90:91], v[176:177]\n" \
  "v_mfma_f32_16x16x32_bf16 v[152:155], v[112:115], v[112:115], 0\n" \
  "v_fmac_f32 v176, v172, v92\n" \
  "v_mfma_f32_16x16x32_bf16 v[152:155], v[116:119], v[116:119], v[152:155]\n" \
  "v_fmac_f32 v176, v173, v93\n" \
  "v_mfma_f32_16x16x32_bf16 v[156:159], v[128:131], v[128:131], 0\n" \
  "v_fmac_f32 v176, v174, v94\n" \
  "v_mfma_f32_16x16x32_bf16 v[156:159], v[132:135], v[132:135], v[156:159]\n" \
  "v_fma_f32 v178, v175, v95, v176\n" \
  "v_mfma_f32_16x16x32_bf16 v[160:163], v[128:131], v[136:139], 0\n" \
  "v_add_f32 v178, v178, v177\n" \
  "v_mfma_f32_16x16x32_bf16 v[160:163], v[132:135], v[140:143], v[160:163]\n" \
  "v_mul_f32 v176, v152, v0\n" \
  "v_mfma_f32_16x16x32_bf16 v[164:167], v[136:139], v[136:139], 0\n" \
  "v_mul_f32 v177, v153, v1\n" \
  "v_fmac_f32 v176, v154, v2\n" \
  "v_mfma_f32_16x16x32_bf16 v[164:167], v[140:143], v[140:143], v[164:167]\n" \
  "v_fmac_f32 v176, v155, v3\n" \
  "v_fmac_f32 v176, v156, v4\n" \
  "s_waitcnt lgkmcnt(0)\n" \
  "s_setprio 3\n" \
  "s_barrier\n" \
  "global_load_dwordx4 v[208:211], v188, s[56:57] offset:0 sc1\n" \
  "global_load_dwordx4 v[212:215], v188, s[56:57] offset:1024 sc1\n" \
  "global_load_dwordx4 v[216:219], v188, s[56:57] offset:2048 sc1\n" \
  "global_load_dwordx4 v[220:223], v188, s[56:57] offset:3072 sc1\n" \
  "v_mfma_f32_16x16x32_bf16 v[168:171], v[112:115], v[136:139], 0\n" \
  "v_fmac_f32 v176, v157, v5\n" \
  "v_mov_b32_dpp v179, v179 row_ror:4 row_mask:0xf bank_mask:0xf\n" \
  "v_lshrrev_b32 v180, 1, v189\n" \
  "v_mfma_f32_16x16x32_bf16 v[168:171], v[116:119], v[140:143], v[168:171]\n" \
  "v_fmac_f32 v176, v158, v6\n" \
  "v_fmac_f32 v176, v159, v7\n" \
  "v_xor_b32 v180, v180, v189\n" \
  "v_mfma_f32_16x16x32_bf16 v[172:175], v[112:115], v[128:131], 0\n" \
  "v_fmac_f32 v176, v160, v8\n" \
  "v_fmac_f32 v176, v161, v9\n" \
  "v_and_b32 v180, 8, v180\n" \
  "v_mfma_f32_16x16x32_bf16 v[172:175], v[116:119], v[132:135], v[172:175]\n" \
  "v_fmac_f32 v176, v162, v10\n" \
  "v_fmac_f32 v176, v163, v11\n" \
  "v_bfe_u32 v181, v189, 2, 1\n" \
  "v_mfma_f32_16x16x32_bf16 v[152:155], v[112:115], v[120:123], 0\n" \
  "v_fmac_f32 v176, v164, v12\n" \
  "v_add_f32_dpp v178, v178, v178 row_ror:8 row_mask:0xf bank_mask:0xf\n" \
  "v_lshlrev_b32_e64 v181, v181, 5\n" \
  "v_mfma_f32_16x16x32_bf16 v[152:155], v[116:119], v[124:127], v[152:155]\n" \
  "v_fmac_f32 v176, v165, v13\n" \
  "v_fmac_f32 v176, v166, v14\n" \
  "v_lshlrev_b32 v180, v180, v181\n" \
  "v_mfma_f32_16x16x32_bf16 v[156:159], v[120:123], v[136:139], 0\n" \
  "v_fmac_f32 v176, v167, v15\n" \
  "v_fmac_f32 v176, v168, v16\n" \
  "v_bfe_u32 v181, v189, 2, 2\n" \
  "v_mfma_f32_16x16x32_bf16 v[156:159], v[124:127], v[140:143], v[156:159]\n" \
  "v_fmac_f32 v176, v169, v17\n" \
  "v_fmac_f32 v176, v170, v18\n" \
  "v_lshlrev_b32_e64 v181, v181, 1\n" \
  "v_mfma_f32_16x16x32_bf16 v[160:163], v[104:107], v[128:131], 0\n" \
  "v_fmac_f32 v176, v171, v19\n" \
  "v_cmp_gt_u32 vcc, 32, v189\n" \
  "v_mfma_f32_16x16x32_bf16 v[160:163], v[108:111], v[132:135], v[160:163]\n" \
  "v_fmac_f32 v176, v172, v20\n" \
  "v_fmac_f32 v176, v173, v21\n" \
  "v_mov_b32 v191, 0x3f80\n" \
  "v_mfma_f32_16x16x32_bf16 v[164:167], v[120:123], v[128:131], 0\n" \
  "v_fmac_f32 v176, v174, v22\n" \
  "v_fmac_f32 v176, v175, v23\n" \
  "v_add_u32 v190, 32000, v188\n" \
  "v_mfma_f32_16x16x32_bf16 v[164:167], v[124:127], v[132:135], v[164:167]\n" \
  "v_fmac_f32 v176, v152, v24\n" \
  "v_fmac_f32 v176, v153, v25\n" \
  "v_cndmask_b32 v192, v181, v180, vcc\n" \
  "v_mfma_f32_16x16x32_bf16 v[168:171], v[96:99], v[128:131], 0\n" \
  "v_mov_b64 v[172:173], 0\n" \
  "v_mov_b64 v[174:175], 0\n" \
  "v_bfe_i32 v180, v192, 0, 1\n" \
  "v_mfma_f32_16x16x32_bf16 v[168:171], v[100:103], v[132:135], v[168:171]\n" \
  "v_fmac_f32 v176, v154, v26\n" \
  "v_fmac_f32 v176, v155, v27\n" \
  "v_and_b32 v180, v180, v191\n" \
  "v_smfmac_f32_16x16x64_bf16 v[172:175], v[144:147], v[128:135], v193\n" \
  "v_fmac_f32 v176, v156, v28\n" \
  "v_bfe_i32 v181, v192, 1, 1\n" \
  "v_smfmac_f32_16x16x64_bf16 v[172:175], v[148:151], v[136:143], v193\n" \
  "v_fmac_f32 v176, v157, v29\n" \
  "v_and_b32 v181, v181, v191\n" \
  "v_mfma_f32_16x16x32_bf16 v[152:155], v[96:99], v[112:115], 0\n" \
  "v_fmac_f32 v176, v158, v30\n" \
  "v_fmac_f32 v176, v159, v31\n" \
  "v_bfe_i32 v182, v192, 2, 1\n" \
  "v_mfma_f32_16x16x32_bf16 v[152:155], v[100:103], v[116:119], v[152:155]\n" \
  "v_fmac_f32 v176, v160, v32\n" \
  "v_fmac_f32 v176, v161, v33\n" \
  "v_and_b32 v182, v182, v191\n" \
  "v_mfma_f32_16x16x32_bf16 v[156:159], v[104:107], v[112:115], 0\n" \
  "v_fmac_f32 v176, v162, v34\n" \
  "v_fmac_f32 v176, v163, v35\n" \
  "v_bfe_i32 v183, v192, 3, 1\n" \
  "v_mfma_f32_16x16x32_bf16 v[156:159], v[108:111], v[116:119], v[156:159]\n" \
  "v_fmac_f32 v176, v164, v36\n" \
  "v_fmac_f32 v176, v165, v37\n" \
  "v_add_f32_dpp v179, v178, v178 row_ror:4 row_mask:0xf bank_mask:0x1\n" \
  "v_pk_fma_f32 v[176:177], v[166:167], v[38:39], v[176:177]\n" \
  "v_pk_fma_f32 v[176:177], v[168:169], v[40:41], v[176:177]\n" \
  "v_pk_fma_f32 v[176:177], v[170:171], v[42:43], v[176:177]\n" \
  "v_pk_fma_f32 v[176:177], v[172:173], v[44:45], v[176:177]\n" \
  "v_pk_fma_f32 v[176:177], v[174:175], v[46:47], v[176:177]\n" \
  "v_pk_fma_f32 v[176:177], v[152:153], v[48:49], v[176:177]\n" \
  "v_pk_fma_f32 v[176:177], v[154:155], v[50:51], v[176:177]\n" \
  "v_and_b32 v183, v183, v191\n" \
  "v_mfma_f32_16x16x32_bf16 v[160:163], v[104:107], v[136:139], 0\n" \
  "v_mov_b64 v[164:165], 0\n" \
  "v_mov_b64 v[166:167], 0\n" \
  "v_and_b32 v192, 3, v189\n" \
  "v_mfma_f32_16x16x32_bf16 v[160:163], v[108:111], v[140:143], v[160:163]\n" \
  "v_fmac_f32 v176, v156, v52\n" \
  "v_mul_u32_u24 v192, 0x5555, v192\n" \
  "v_smfmac_f32_16x16x64_bf16 v[164:167], v[144:147], v[112:119], v193\n" \
  "v_fmac_f32 v176, v157, v53\n" \
  "v_add_u32 v189, 16000, v188\n" \
  "v_smfmac_f32_16x16x64_bf16 v[164:167], v[148:151], v[120:127], v193\n" \
  "v_fmac_f32 v176, v158, v54\n" \
  "v_add_u32 v191, 48000, v188\n" \
  "v_mfma_f32_16x16x32_bf16 v[168:171], v[96:99], v[136:139], 0\n" \
  "v_mov_b64 v[172:173], 0\n" \
  "v_mov_b64 v[174:175], 0\n" \
  "v_mfma_f32_16x16x32_bf16 v[168:171], v[100:103], v[140:143], v[168:171]\n" \
  "v_fmac_f32 v176, v159, v55\n" \
  "v_fmac_f32 v176, v160, v56\n" \
  "v_smfmac_f32_16x16x64_bf16 v[172:175], v[148:151], v[104:111], v193\n" \
  "v_fmac_f32 v176, v161, v57\n" \
  "v_smfmac_f32_16x16x64_bf16 v[172:175], v[144:147], v[96:103], v193\n" \
  "v_fmac_f32 v176, v162, v58\n" \
  "v_mfma_f32_16x16x32_bf16 v[152:155], v[120:123], v[120:123], 0\n" \
  "v_fmac_f32 v176, v163, v59\n" \
  "v_fmac_f32 v176, v164, v60\n" \
  "v_mfma_f32_16x16x32_bf16 v[152:155], v[124:127], v[124:127], v[152:155]\n" \
  "v_fmac_f32 v176, v165, v61\n" \
  "v_mfma_f32_16x16x32_bf16 v[156:159], v[104:107], v[120:123], 0\n" \
  "v_fmac_f32 v176, v166, v62\n" \
  "v_mfma_f32_16x16x32_bf16 v[156:159], v[108:111], v[124:127], v[156:159]\n" \
  "v_fmac_f32 v176, v167, v63\n" \
  "v_fmac_f32 v176, v168, v64\n" \
  "v_mfma_f32_16x16x32_bf16 v[160:163], v[96:99], v[120:123], 0\n" \
  "v_fmac_f32 v176, v169, v65\n" \
  "v_mfma_f32_16x16x32_bf16 v[160:163], v[100:103], v[124:127], v[160:163]\n" \
  "v_fmac_f32 v176, v170, v66\n" \
  "v_mfma_f32_16x16x32_bf16 v[164:167], v[96:99], v[104:107], 0\n" \
  "v_fmac_f32 v176, v171, v67\n" \
  "v_mfma_f32_16x16x32_bf16 v[164:167], v[100:103], v[108:111], v[164:167]\n" \
  "v_fmac_f32 v176, v172, v68\n" \
  "v_fmac_f32 v176, v173, v69\n" \
  "v_mfma_f32_16x16x32_bf16 v[168:171], v[104:107], v[104:107], 0\n" \
  "v_fmac_f32 v176, v174, v70\n" \
  "v_mfma_f32_16x16x32_bf16 v[168:171], v[108:111], v[108:111], v[168:171]\n" \
  "v_fmac_f32 v176, v175, v71\n" \
  "s_add_u32 s35, s59, 64000\n" \
  "s_add_u32 s54, s54, s35\n" \
  "s_addc_u32 s55, s55, 0\n" \
  "s_add_u32 s58, s59, 0x10000\n" \
  "s_mov_b32 m0, s58\n" \
  "s_add_u32 s35, s58, 0x8000\n" \
  "global_load_lds_dwordx4 v188, s[54:55] sc1\n" \
  "global_load_lds_dwordx4 v188, s[54:55] offset:1024 sc1\n" \
  "v_mfma_f32_16x16x32_bf16 v[172:175], v[148:151], v[144:147], 0\n" \
  "v_fmac_f32 v176, v152, v72\n" \
  "v_mfma_f32_16x16x32_bf16 v[172:175], v[96:99], v[96:99], v[172:175]\n" \
  "s_add_u32 m0, s58, 0x4000\n" \
  "s_cmp_lt_u32 s35, 0x28000\n" \
  "global_load_lds_dwordx4 v189, s[54:55] sc1\n" \
  "global_load_lds_dwordx4 v189, s[54:55] offset:1024 sc1\n" \
  "s_cselect_b32 s58, s35, s59\n" \
  "s_add_u32 s54, s54, 32000\n" \
  "s_addc_u32 s55, s55, 0\n" \
  "v_mfma_f32_16x16x32_bf16 v[172:175], v[100:103], v[100:103], v[172:175]\n" \
  "s_mov_b32 s51, 7\n" \
  "v_fmac_f32 v176, v153, v73\n" \
  "v_pk_fma_f32 v[176:177], v[154:155], v[74:75], v[176:177]\n" \
  "v_pk_fma_f32 v[176:177], v[156:157], v[76:77], v[176:177]\n" \
  "v_pk_fma_f32 v[176:177], v[158:159], v[78:79], v[176:177]\n" \
  "v_pk_fma_f32 v[176:177], v[160:161], v[80:81], v[176:177]\n" \
  "v_pk_fma_f32 v[176:177], v[162:163], v[82:83], v[176:177]\n" \
  "v_pk_fma_f32 v[176:177], v[164:165], v[84:85], v[176:177]\n" \
  "v_pk_fma_f32 v[176:177], v[166:167], v[86:87], v[176:177]\n" \
  "v_pk_fma_f32 v[176:177], v[168:169], v[88:89], v[176:177]\n" \
  "v_pk_fma_f32 v[176:177], v[170:171], v[90:91], v[176:177]\n" \
  "v_fmac_f32 v176, v172, v92\n" \
  "v_fmac_f32 v176, v173, v93\n" \
  "v_fmac_f32 v176, v174, v94\n" \
  "v_fma_f32 v178, v175, v95, v176\n" \
  "v_add_f32 v178, v178, v177\n" \
  "s_nop 1\n" \
  "v_add_f32_dpp v178, v178, v178 row_ror:8 row_mask:0xf bank_mask:0xf\n" \
  "v_mov_b32_dpp v179, v179 row_ror:4 row_mask:0xf bank_mask:0xf\n" \
  "s_nop 1\n" \
  "v_add_f32_dpp v179, v178, v178 row_ror:4 row_mask:0xf bank_mask:0x1\n" \
  "v_mov_b32 v99, v195\n" \
  "s_nop 0\n" \
  "v_mov_b32_dpp v196, v179 row_ror:8 row_mask:0xf bank_mask:0xc\n" \
  "s_nop 1\n" \
  "v_permlane16_swap_b32 v196, v197\n" \
  "v_permlane16_swap_b32 v198, v199\n" \
  "v_permlane16_swap_b32 v200, v201\n" \
  "v_permlane16_swap_b32 v202, v203\n" \
  "v_permlane16_swap_b32 v195, v99\n" \
  "v_add_f32 v196, v196, v197\n" \
  "v_add_f32 v198, v198, v199\n" \
  "v_add_f32 v200, v200, v201\n" \
  "v_add_f32 v202, v202, v203\n" \
  "v_add_f32 v195, v195, v99\n" \
  "v_lshrrev_b32 v96, 4, v188\n" \
  "v_and_b32 v97, 1, v96\n" \
  "v_mov_b32 v99, v195\n" \
  "v_lshrrev_b32 v98, 4, v96\n" \
  "v_permlane32_swap_b32 v196, v198\n" \
  "v_permlane32_swap_b32 v200, v202\n" \
  "v_permlane32_swap_b32 v195, v99\n" \
  "v_add_f32 v196, v196, v198\n" \
  "v_add_f32 v200, v200, v202\n" \
  "v_add_f32 v195, v195, v99\n" \
  "v_cmp_eq_u32 vcc, 1, v97\n" \
  "v_lshlrev_b32 v97, 2, v97\n" \
  "v_add_lshl_u32 v97, v97, v98, 2\n" \
  "v_bfe_u32 v98, v96, 2, 2\n" \
  "v_add_f32_dpp v196, v196, v196 quad_perm:[1,0,3,2] row_mask:0xf bank_mask:0xf\n" \
  "v_add_f32_dpp v200, v200, v200 quad_perm:[1,0,3,2] row_mask:0xf bank_mask:0xf\n" \
  "v_add_f32_dpp v195, v195, v195 quad_perm:[1,0,3,2] row_mask:0xf bank_mask:0xf\n" \
  "v_sub_u32 v97, v97, v98\n" \
  "v_cndmask_b32 v196, v196, v200, vcc\n" \
  "v_add_u32 v97, 1, v97\n" \
  "s_min_i32 s35, s4, 0\n" \
  "s_add_u32 s36, s4, 32\n" \
  "s_add_u32 s37, s4, 29\n" \
  "v_add_f32_dpp v195, v195, v195 quad_perm:[2,3,0,1] row_mask:0xf bank_mask:0xf\n" \
  "v_add_f32_dpp v196, v196, v196 quad_perm:[2,3,0,1] row_mask:0xf bank_mask:0xf\n" \
  "s_mov_b32 exec_lo, 0x0\n" \
  "s_mov_b32 exec_hi, 0xaaaa0000\n" \
  "v_add_u32 v97, s35, v97\n" \
  "s_mov_b32 exec_lo, 0x5500\n" \
  "s_mov_b32 exec_hi, 0x0\n" \
  "v_add_u32 v97, s36, v97\n" \
  "s_mov_b32 exec_lo, 0x4444\n" \
  "s_mov_b32 exec_hi, 0x0\n" \
  "v_sub_u32 v97, s37, v98\n" \
  "v_mov_b32 v196, v195\n" \
  "s_mov_b64 exec, -1\n" \
  "v_cmp_lt_u32 vcc, 31, v97\n" \
  "v_add_u32 v98, s5, v97\n" \
  "s_lshl2_add_u32 s35, s7, s28\n" \
  "s_lshl_b32 s35, s35, 5\n" \
  "v_cndmask_b32 v97, v97, v98, vcc\n" \
  "v_add_lshl_u32 v97, v97, s35, 2\n" \
  "s_lshl_b32 s35, s31, 16\n" \
  "s_add_u32 s35, s35, 109871104\n" \
  "s_add_u32 s40, s22, s35\n" \
  "s_addc_u32 s41, s23, 0\n" \
  "v_min_u32 v196, -2, v196\n" \
  "v_lshlrev_b32 v184, 16, v204\n" \
  "v_and_b32 v185, 0xffff0000, v204\n" \
  "v_lshlrev_b32 v186, 16, v205\n" \
  "v_and_b32 v187, 0xffff0000, v205\n" \
  "s_barrier\n" \
  "s_add_u32 s56, s56, 32768\n" \
  "s_addc_u32 s57, s57, 0\n" \
  "global_load_dwordx4 v[224:227], v188, s[56:57] sc1\n" \
  "global_load_dwordx4 v[228:231], v188, s[56:57] offset:1024 sc1\n" \
  "global_load_dwordx4 v[232:235], v188, s[56:57] offset:2048 sc1\n" \
  "global_load_dwordx4 v[236:239], v188, s[56:57] offset:3072 sc1\n" \
  "s_add_u32 s56, s56, 32768\n" \
  "s_addc_u32 s57, s57, 0\n" \
  "s_mov_b32 exec_lo, 0x33337777\n" \
  "s_mov_b32 exec_hi, 0x33333333\n" \
  "global_store_dword v97, v196, s[40:41] sc1\n" \
  "s_mov_b64 exec, -1\n" \
  "s_waitcnt vmcnt(9)\n" \
  "s_setprio 0\n" \
  "s_branch LG_start\n" \
  ".p2align 6\n" \
  "LA_loop:\n" \
  "LA_mid:\n" \
  "v_fmac_f32 v176, v153, v73\n" \
  "v_pk_fma_f32 v[176:177], v[154:155], v[74:75], v[176:177]\n" \
  "v_pk_fma_f32 v[176:177], v[156:157], v[76:77], v[176:177]\n" \
  "v_pk_fma_f32 v[176:177], v[158:159], v[78:79], v[176:177]\n" \
  "v_pk_fma_f32 v[176:177], v[160:161], v[80:81], v[176:177]\n" \
  "v_pk_fma_f32 v[176:177], v[162:163], v[82:83], v[176:177]\n" \
  "v_pk_fma_f32 v[176:177], v[164:165], v[84:85], v[176:177]\n" \
  "v_pk_fma_f32 v[176:177], v[166:167], v[86:87], v[176:177]\n" \
  "v_pk_fma_f32 v[176:177], v[168:169], v[88:89], v[176:177]\n" \
  "v_pk_fma_f32 v[176:177], v[170:171], v[90:91], v[176:177]\n" \
  "v_mfma_f32_16x16x32_bf16 v[152:155], v[112:115], v[112:115], 0\n" \
  "s_waitcnt lgkmcnt(7)\n" \
  "ds_read_b128 v[100:103], v185 offset:64\n" \
  "v_fmac_f32 v176, v172, v92\n" \
  "v_mfma_f32_16x16x32_bf16 v[152:155], v[116:119], v[116:119], v[152:155]\n" \
  "ds_read2_b32 v[144:145], v186 offset0:0 offset1:2\n" \
  "v_fmac_f32 v176, v173, v93\n" \
  "v_mfma_f32_16x16x32_bf16 v[156:159], v[128:131], v[128:131], 0\n" \
  "ds_read2_b32 v[146:147], v186 offset0:4 offset1:6\n" \
  "v_fmac_f32 v176, v174, v94\n" \
  "v_mfma_f32_16x16x32_bf16 v[156:159], v[132:135], v[132:135], v[156:159]\n" \
  "s_waitcnt lgkmcnt(9)\n" \
  "ds_read2_b32 v[148:149], v187 offset0:0 offset1:2\n" \
  "v_fma_f32 v178, v175, v95, v176\n" \
  "v_mfma_f32_16x16x32_bf16 v[160:163], v[128:131], v[136:139], 0\n" \
  "s_waitcnt lgkmcnt(9)\n" \
  "ds_read2_b32 v[150:151], v187 offset0:4 offset1:6\n" \
  "v_add_f32 v178, v178, v177\n" \
  "v_mfma_f32_16x16x32_bf16 v[160:163], v[132:135], v[140:143], v[160:163]\n" \
  "ds_read_b128 v[180:183], v194\n" \
  "v_mul_f32 v176, v152, v0\n" \
  "v_mfma_f32_16x16x32_bf16 v[164:167], v[136:139], v[136:139], 0\n" \
  "v_mul_f32 v177, v153, v1\n" \
  "v_fmac_f32 v176, v154, v2\n" \
  "v_mfma_f32_16x16x32_bf16 v[164:167], v[140:143], v[140:143], v[164:167]\n" \
  "v_fmac_f32 v176, v155, v3\n" \
  "v_fmac_f32 v176, v156, v4\n" \
  "v_mfma_f32_16x16x32_bf16 v[168:171], v[112:115], v[136:139], 0\n" \
  "v_fmac_f32 v176, v157, v5\n" \
  "v_mov_b32_dpp v179, v179 row_ror:4 row_mask:0xf bank_mask:0xf\n" \
  "v_mfma_f32_16x16x32_bf16 v[168:171], v[116:119], v[140:143], v[168:171]\n" \
  "v_fmac_f32 v176, v158, v6\n" \
  "v_fmac_f32 v176, v159, v7\n" \
  "v_mfma_f32_16x16x32_bf16 v[172:175], v[112:115], v[128:131], 0\n" \
  "v_fmac_f32 v176, v160, v8\n" \
  "v_fmac_f32 v176, v161, v9\n" \
  "v_mfma_f32_16x16x32_bf16 v[172:175], v[116:119], v[132:135], v[172:175]\n" \
  "s_waitcnt lgkmcnt(10)\n" \
  "v_fmac_f32 v176, v162, v10\n" \
  "v_fmac_f32 v176, v163, v11\n" \
  "v_mfma_f32_16x16x32_bf16 v[152:155], v[112:115], v[120:123], 0\n" \
  "s_waitcnt lgkmcnt(9)\n" \
  "v_fmac_f32 v176, v164, v12\n" \
  "v_add_f32_dpp v178, v178, v178 row_ror:8 row_mask:0xf bank_mask:0xf\n" \
  "v_mfma_f32_16x16x32_bf16 v[152:155], v[116:119], v[124:127], v[152:155]\n" \
  "v_fmac_f32 v176, v165, v13\n" \
  "v_fmac_f32 v176, v166, v14\n" \
  "v_mfma_f32_16x16x32_bf16 v[156:159], v[120:123], v[136:139], 0\n" \
  "v_fmac_f32 v176, v167, v15\n" \
  "v_fmac_f32 v176, v168, v16\n" \
  "v_mfma_f32_16x16x32_bf16 v[156:159], v[124:127], v[140:143], v[156:159]\n" \
  "s_waitcnt lgkmcnt(8)\n" \
  "v_fmac_f32 v176, v169, v17\n" \
  "v_fmac_f32 v176, v170, v18\n" \
  "v_mfma_f32_16x16x32_bf16 v[160:163], v[104:107], v[128:131], 0\n" \
  "s_waitcnt lgkmcnt(0)\n" \
  "v_fmac_f32 v176, v171, v19\n" \
  "v_mad_u32_u16 v184, v182, 16, v188\n" \
  "v_mfma_f32_16x16x32_bf16 v[160:163], v[108:111], v[132:135], v[160:163]\n" \
  "v_fmac_f32 v176, v172, v20\n" \
  "v_fmac_f32 v176, v173, v21\n" \
  "v_mfma_f32_16x16x32_bf16 v[164:167], v[120:123], v[128:131], 0\n" \
  "v_fmac_f32 v176, v174, v22\n" \
  "v_fmac_f32 v176, v175, v23\n" \
  "v_mfma_f32_16x16x32_bf16 v[164:167], v[124:127], v[132:135], v[164:167]\n" \
  "v_fmac_f32 v176, v152, v24\n" \
  "v_fmac_f32 v176, v153, v25\n" \
  "v_mfma_f32_16x16x32_bf16 v[168:171], v[96:99], v[128:131], 0\n" \
  "v_mov_b64 v[172:173], 0\n" \
  "v_mov_b64 v[174:175], 0\n" \
  "v_mfma_f32_16x16x32_bf16 v[168:171], v[100:103], v[132:135], v[168:171]\n" \
  "v_fmac_f32 v176, v154, v26\n" \
  "v_fmac_f32 v176, v155, v27\n" \
  "v_smfmac_f32_16x16x64_bf16 v[172:175], v[144:147], v[128:135], v193\n" \
  "ds_read_b128 v[128:131], v184 offset:0\n" \
  "v_fmac_f32 v176, v156, v28\n" \
  "v_smfmac_f32_16x16x64_bf16 v[172:175], v[148:151], v[136:143], v193\n" \
  "ds_read_b128 v[132:135], v184 offset:64\n" \
  "v_fmac_f32 v176, v157, v29\n" \
  "v_mfma_f32_16x16x32_bf16 v[152:155], v[96:99], v[112:115], 0\n" \
  "v_fmac_f32 v176, v158, v30\n" \
  "v_fmac_f32 v176, v159, v31\n" \
  "v_mfma_f32_16x16x32_bf16 v[152:155], v[100:103], v[116:119], v[152:155]\n" \
  "v_fmac_f32 v176, v160, v32\n" \
  "v_fmac_f32 v176, v161, v33\n" \
  "v_mfma_f32_16x16x32_bf16 v[156:159], v[104:107], v[112:115], 0\n" \
  "v_fmac_f32 v176, v162, v34\n" \
  "v_fmac_f32 v176, v163, v35\n" \
  "v_mfma_f32_16x16x32_bf16 v[156:159], v[108:111], v[116:119], v[156:159]\n" \
  "v_fmac_f32 v176, v164, v36\n" \
  "v_fmac_f32 v176, v165, v37\n" \
  "v_mad_u32_u16 v185, v181, 16, v188\n" \
  "v_add_f32_dpp v179, v178, v178 row_ror:4 row_mask:0xf bank_mask:0x1\n" \
  "v_add_u32 v194, 0x100, v194\n" \
  "v_pk_fma_f32 v[176:177], v[166:167], v[38:39], v[176:177]\n" \
  "v_pk_fma_f32 v[176:177], v[168:169], v[40:41], v[176:177]\n" \
  "v_pk_fma_f32 v[176:177], v[170:171], v[42:43], v[176:177]\n" \
  "v_pk_fma_f32 v[176:177], v[172:173], v[44:45], v[176:177]\n" \
  "v_pk_fma_f32 v[176:177], v[174:175], v[46:47], v[176:177]\n" \
  "v_pk_fma_f32 v[176:177], v[152:153], v[48:49], v[176:177]\n" \
  "v_pk_fma_f32 v[176:177], v[154:155], v[50:51], v[176:177]\n" \
  "v_mfma_f32_16x16x32_bf16 v[160:163], v[104:107], v[136:139], 0\n" \
  "v_mov_b64 v[164:165], 0\n" \
  "v_mov_b64 v[166:167], 0\n" \
  "v_mfma_f32_16x16x32_bf16 v[160:163], v[108:111], v[140:143], v[160:163]\n" \
  "v_fmac_f32 v176, v156, v52\n" \
  "v_mad_u32_u16 v186, v182, 16, v188 op_sel:[1,0,0,0]\n" \
  "v_smfmac_f32_16x16x64_bf16 v[164:167], v[144:147], v[112:119], v193\n" \
  "ds_read_b128 v[112:115], v185 offset:0\n" \
  "v_fmac_f32 v176, v157, v53\n" \
  "v_smfmac_f32_16x16x64_bf16 v[164:167], v[148:151], v[120:127], v193\n" \
  "ds_read_b128 v[116:119], v185 offset:64\n" \
  "v_fmac_f32 v176, v158, v54\n" \
  "v_mfma_f32_16x16x32_bf16 v[168:171], v[96:99], v[136:139], 0\n" \
  "v_mov_b64 v[172:173], 0\n" \
  "v_mov_b64 v[174:175], 0\n" \
  "v_mfma_f32_16x16x32_bf16 v[168:171], v[100:103], v[140:143], v[168:171]\n" \
  "v_fmac_f32 v176, v159, v55\n" \
  "v_fmac_f32 v176, v160, v56\n" \
  "v_smfmac_f32_16x16x64_bf16 v[172:175], v[148:151], v[104:111], v193\n" \
  "ds_read_b128 v[136:139], v186 offset:0\n" \
  "v_fmac_f32 v176, v161, v57\n" \
  "v_smfmac_f32_16x16x64_bf16 v[172:175], v[144:147], v[96:103], v193\n" \
  "ds_read_b128 v[140:143], v186 offset:64\n" \
  "v_fmac_f32 v176, v162, v58\n" \
  "v_mfma_f32_16x16x32_bf16 v[152:155], v[120:123], v[120:123], 0\n" \
  "v_fmac_f32 v176, v163, v59\n" \
  "v_fmac_f32 v176, v164, v60\n" \
  "v_mfma_f32_16x16x32_bf16 v[152:155], v[124:127], v[124:127], v[152:155]\n" \
  "v_fmac_f32 v176, v165, v61\n" \
  "v_mad_u32_u16 v187, v181, 16, v188 op_sel:[1,0,0,0]\n" \
  "v_mfma_f32_16x16x32_bf16 v[156:159], v[104:107], v[120:123], 0\n" \
  "v_fmac_f32 v176, v166, v62\n" \
  "v_mad_u32_u16 v184, v180, 16, v188 op_sel:[1,0,0,0]\n" \
  "v_mfma_f32_16x16x32_bf16 v[156:159], v[108:111], v[124:127], v[156:159]\n" \
  "v_fmac_f32 v176, v167, v63\n" \
  "v_fmac_f32 v176, v168, v64\n" \
  "v_mfma_f32_16x16x32_bf16 v[160:163], v[96:99], v[120:123], 0\n" \
  "ds_read_b128 v[120:123], v187 offset:0\n" \
  "v_fmac_f32 v176, v169, v65\n" \
  "v_mfma_f32_16x16x32_bf16 v[160:163], v[100:103], v[124:127], v[160:163]\n" \
  "v_fmac_f32 v176, v170, v66\n" \
  "v_mad_u32_u16 v185, v180, 16, v188\n" \
  "v_mfma_f32_16x16x32_bf16 v[164:167], v[96:99], v[104:107], 0\n" \
  "ds_read_b128 v[124:127], v187 offset:64\n" \
  "v_fmac_f32 v176, v171, v67\n" \
  "v_mfma_f32_16x16x32_bf16 v[164:167], v[100:103], v[108:111], v[164:167]\n" \
  "v_fmac_f32 v176, v172, v68\n" \
  "v_fmac_f32 v176, v173, v69\n" \
  "v_mfma_f32_16x16x32_bf16 v[168:171], v[104:107], v[104:107], 0\n" \
  "ds_read_b128 v[104:107], v184 offset:0\n" \
  "v_fmac_f32 v176, v174, v70\n" \
  "v_mfma_f32_16x16x32_bf16 v[168:171], v[108:111], v[108:111], v[168:171]\n" \
  "s_sub_u32 s51, s51, 1\n" \
  "v_fmac_f32 v176, v175, v71\n" \
  "v_mad_u32_u16 v186, v183, 16, v189\n" \
  "v_mfma_f32_16x16x32_bf16 v[172:175], v[148:151], v[144:147], 0\n" \
  "s_cmp_lg_u32 s51, 0\n" \
  "ds_read_b128 v[108:111], v184 offset:64\n" \
  "v_fmac_f32 v176, v152, v72\n" \
  "v_mfma_f32_16x16x32_bf16 v[172:175], v[96:99], v[96:99], v[172:175]\n" \
  "s_waitcnt lgkmcnt(7)\n" \
  "ds_read_b128 v[96:99], v185 offset:0\n" \
  "v_mad_u32_u16 v187, v183, 16, v190\n" \
  "v_mfma_f32_16x16x32_bf16 v[172:175], v[100:103], v[100:103], v[172:175]\n" \
  "s_cbranch_scc1 LA_loop\n" \
  "s_cmp_ge_u32 s50, 6\n" \
  "s_cbranch_scc1 LA_ret\n" \
  "s_set_gpr_idx_on s50, gpr_idx(DST)\n" \
  "v_mov_b32 v196, v179\n" \
  "s_lshl_b32 s35, s50, 3\n" \
  "s_waitcnt vmcnt(0)\n" \
  "s_set_gpr_idx_on s35, gpr_idx(SRC0,SRC1)\n" \
  "v_cvt_pk_u16_u32 v210, v208, v209\n" \
  "v_cvt_pk_u16_u32 v211, v210, v211\n" \
  "v_cvt_pk_u16_u32 v212, v212, v213\n" \
  "v_cvt_pk_u16_u32 v213, v214, v215\n" \
  "s_set_gpr_idx_off\n" \
  "v_pk_lshrrev_b16 v214, 3, v210 op_sel_hi:[0,1]\n" \
  "v_pk_mad_u16 v210, v210, 8, v214 op_sel_hi:[1,0,1]\n" \
  "v_pk_lshrrev_b16 v215, 3, v211 op_sel_hi:[0,1]\n" \
  "v_pk_mad_u16 v211, v211, 8, v215 op_sel_hi:[1,0,1]\n" \
  "v_pk_lshrrev_b16 v214, 3, v212 op_sel_hi:[0,1]\n" \
  "v_pk_mad_u16 v212, v212, 8, v214 op_sel_hi:[1,0,1]\n" \
  "v_pk_lshrrev_b16 v215, 3, v213 op_sel_hi:[0,1]\n" \
  "v_pk_mad_u16 v213, v213, 8, v215 op_sel_hi:[1,0,1]\n" \
  "s_lshr_b32 s35, s38, 1\n" \
  "v_add_u32 v214, s35, v195\n" \
  "v_alignbit_b32 v212, v212, v212, v192\n" \
  "v_alignbit_b32 v213, v213, v213, v192\n" \
  "ds_write2_b32 v214, v210, v211 offset0:0 offset1:4\n" \
  "ds_write2_b32 v214, v212, v213 offset0:2 offset1:6\n" \
  "ds_read_b32 v209, v208 offset:4\n" \
  "v_subrev_u32 v194, s38, v194\n" \
  "s_xor_b32 s38, s38, 0x800\n" \
  "s_add_u32 s50, s50, 1\n" \
  "s_mov_b32 s51, 4\n" \
  "s_branch LA_loop\n" \
  "LA_ret:\n" \
  "s_setpc_b64 s[52:53]\n" \
  "LB_start:\n" \
  "s_setprio 2\n" \
  "s_lshl_b32 s35, s31, 7\n" \
  "s_waitcnt lgkmcnt(0)\n" \
  "s_add_u32 s40, s10, s35\n" \
  "s_addc_u32 s41, s11, 0\n" \
  "s_add_u32 s37, s36, 32\n" \
  "L3_:\n" \
  "s_sub_u32 s35, s36, 125\n" \
  "s_min_u32 s35, s35, s36\n" \
  "s_mul_i32 m0, s35, 1040\n" \
  "s_lshl_b32 s35, s35, 11\n" \
  "s_add_u32 s42, s40, s35\n" \
  "s_addc_u32 s43, s41, 0\n" \
  "global_load_lds_dwordx4 v254, s[42:43]\n" \
  "s_add_u32 s36, s36, 4\n" \
  "s_sub_u32 s35, s36, 125\n" \
  "s_min_u32 s35, s35, s36\n" \
  "s_mul_i32 m0, s35, 1040\n" \
  "s_lshl_b32 s35, s35, 11\n" \
  "s_add_u32 s42, s40, s35\n" \
  "s_addc_u32 s43, s41, 0\n" \
  "global_load_lds_dwordx4 v254, s[42:43]\n" \
  "s_add_u32 s36, s36, 4\n" \
  "s_cmp_lg_u32 s37, s36\n" \
  "s_cbranch_scc1 L3_\n" \
  "v_mov_b32 v190, 163328\n" \
  "v_mov_b64 v[2:3], 0\n" \
  "ds_write_b64 v190, v[2:3]\n" \
  "ds_write_b64 v190, v[2:3] offset:8\n" \
  "ds_write_b64 v190, v[2:3] offset:16\n" \
  "s_waitcnt lgkmcnt(0)\n" \
  "s_barrier\n" \
  "v_lshlrev_b32 v247, 7, v255\n" \
  "s_add_u32 s37, s36, 96\n" \
  "L4_:\n" \
  "s_sub_u32 s35, s36, 125\n" \
  "s_min_u32 s35, s35, s36\n" \
  "s_mul_i32 m0, s35, 1040\n" \
  "s_lshl_b32 s35, s35, 11\n" \
  "s_add_u32 s42, s40, s35\n" \
  "s_addc_u32 s43, s41, 0\n" \
  "global_load_lds_dwordx4 v254, s[42:43]\n" \
  "s_add_u32 s36, s36, 4\n" \
  "s_sub_u32 s35, s36, 125\n" \
  "s_min_u32 s35, s35, s36\n" \
  "s_mul_i32 m0, s35, 1040\n" \
  "s_lshl_b32 s35, s35, 11\n" \
  "s_add_u32 s42, s40, s35\n" \
  "s_addc_u32 s43, s41, 0\n" \
  "global_load_lds_dwordx4 v254, s[42:43]\n" \
  "s_add_u32 s36, s36, 4\n" \
  "s_cmp_lg_u32 s37, s36\n" \
  "s_cbranch_scc1 L4_\n" \
  "s_add_u32 s48, s22, 110002176\n" \
  "s_addc_u32 s49, s23, 0\n" \
  "s_lshl_b32 s35, s26, 2\n" \
  "v_mov_b32 v193, s35\n" \
  "global_load_dword v193, v193, s[48:49] offset:1024 sc1\n" \
  "v_mov_b32 v191, 1\n" \
  "v_and_b32 v252, 15, v255\n" \
  "v_lshrrev_b32 v253, 4, v255\n" \
  "s_lshl2_add_u32 s35, s31, s28\n" \
  "s_lshl_b32 s35, s35, 1\n" \
  "v_bfe_u32 v1, v255, 2, 1\n" \
  "v_add_u32 v1, s35, v1\n" \
  "v_and_b32 v2, 3, v255\n" \
  "v_lshlrev_b32 v3, 5, v1\n" \
  "v_lshl_add_u32 v3, v2, 3, v3\n" \
  "v_lshlrev_b32 v4, 2, v2\n" \
  "v_add_u32 v4, 380, v4\n" \
  "v_cmp_gt_u32 vcc, 12, v1\n" \
  "v_lshrrev_b32 v5, 3, v255\n" \
  "v_mul_u32_u24 v5, 6400, v5\n" \
  "v_cndmask_b32 v3, v4, v3, vcc\n" \
  "v_add_u32 v5, 3200, v5\n" \
  "v_add_u32 v227, v5, v3\n" \
  "v_lshl_add_u32 v3, v1, 2, v2\n" \
  "v_add_u32 v4, 48, v3\n" \
  "v_mul_u32_u24 v5, 205, v3\n" \
  "v_lshrrev_b32 v5, 11, v5\n" \
  "v_mul_u32_u24 v5, 10, v5\n" \
  "v_sub_u32 v5, v3, v5\n" \
  "v_lshlrev_b32 v236, 26, v5\n" \
  "v_mul_u32_u24 v5, 205, v4\n" \
  "v_lshrrev_b32 v5, 11, v5\n" \
  "v_mul_u32_u24 v5, 10, v5\n" \
  "v_sub_u32 v5, v4, v5\n" \
  "v_lshlrev_b32 v237, 26, v5\n" \
  "s_lshl2_add_u32 s4, s31, s28\n" \
  "s_lshl_b32 s4, s4, 7\n" \
  "s_lshl_b32 s36, s31, 4\n" \
  "s_sub_u32 s4, s4, s36\n" \
  "s_add_u32 s36, s4, 96\n" \
  "s_min_u32 s36, s36, 984\n" \
  "s_add_u32 s37, s4, 112\n" \
  "s_min_u32 s37, s37, 984\n" \
  "v_add_u32 v186, s4, v252\n" \
  "v_add_u32 v187, s36, v252\n" \
  "v_add_u32 v188, s37, v252\n" \
  "s_mul_i32 s38, s7, 51200\n" \
  "s_add_u32 s42, s8, s38\n" \
  "s_addc_u32 s43, s9, 0\n" \
  "s_xor_b32 s38, s31, 1\n" \
  "s_lshl_b32 s38, s38, 7\n" \
  "s_add_u32 s94, s10, s38\n" \
  "s_addc_u32 s95, s11, 0\n" \
  "s_mul_i32 s5, s28, 4224\n" \
  "s_add_u32 s5, s5, 146432\n" \
  "v_lshrrev_b32 v2, 3, v186\n" \
  "v_lshl_add_u32 v2, v186, 3, v2\n" \
  "v_add_lshl_u32 v249, v2, v253, 4\n" \
  "v_lshrrev_b32 v2, 3, v187\n" \
  "v_lshl_add_u32 v2, v187, 3, v2\n" \
  "v_add_lshl_u32 v250, v2, v253, 4\n" \
  "v_lshrrev_b32 v2, 3, v188\n" \
  "v_lshl_add_u32 v2, v188, 3, v2\n" \
  "v_add_lshl_u32 v251, v2, v253, 4\n" \
  "v_mul_u32_u24 v1, 1600000, v253\n" \
  "v_lshl_add_u32 v186, v186, 4, v1\n" \
  "v_lshl_add_u32 v187, v187, 4, v1\n" \
  "v_lshl_add_u32 v188, v188, 4, v1\n" \
  "v_lshlrev_b32 v1, 4, v253\n" \
  "v_xor_b32 v244, v1, v254\n" \
  "s_waitcnt vmcnt(1)\n" \
  "s_mov_b64 exec, 1\n" \
  "ds_add_u32 v190, v191 offset:12\n" \
  "s_mov_b64 exec, -1\n" \
  "s_memtime s[88:89]\n" \
  "v_lshl_add_u32 v245, v255, 4, s5\n" \
  "v_lshrrev_b32 v1, 1, v252\n" \
  "v_xor_b32 v1, v1, v253\n" \
  "v_lshlrev_b32 v1, 4, v1\n" \
  "v_lshl_add_u32 v1, v252, 7, v1\n" \
  "v_add_u32 v246, s5, v1\n" \
  "v_xor_b32 v1, 64, v1\n" \
  "v_add_u32 v242, s5, v1\n" \
  "v_lshlrev_b32 v1, 4, v255\n" \
  "v_xor_b32 v1, 64, v1\n" \
  "v_add_u32 v243, s5, v1\n" \
  "v_lshrrev_b32 v1, 2, v255\n" \
  "s_lshl_b32 s35, s28, 5\n" \
  "s_lshl_b32 s36, s31, 6\n" \
  "s_add_u32 s35, s35, s36\n" \
  "s_and_b32 s35, s35, 127\n" \
  "v_add_u32 v1, s35, v1\n" \
  "v_lshrrev_b32 v2, 4, v255\n" \
  "v_and_b32 v2, 2, v2\n" \
  "v_xor_b32 v2, v2, v255\n" \
  "v_and_b32 v2, 3, v2\n" \
  "v_lshlrev_b32 v2, 4, v2\n" \
  "v_lshl_add_u32 v189, v1, 10, v2\n" \
  "v_add_u32 v194, 15360, v189\n" \
  "v_lshrrev_b32 v1, 2, v252\n" \
  "v_lshlrev_b32 v1, 6, v1\n" \
  "v_lshl_add_u32 v1, v253, 9, v1\n" \
  "v_and_b32 v2, 3, v252\n" \
  "v_lshl_add_u32 v1, v2, 3, v1\n" \
  "v_and_b32 v2, 1, v253\n" \
  "v_lshl_add_u32 v1, v2, 5, v1\n" \
  "v_add_u32 v184, 138240, v1\n" \
  "v_xor_b32 v1, 32, v1\n" \
  "v_add_u32 v185, 138240, v1\n" \
  "s_bfe_u32 s35, s30, 0x10001\n" \
  "s_lshl1_add_u32 s35, s29, s35\n" \
  "s_lshr_b32 s6, s30, 2\n" \
  "s_lshl_b32 s6, s6, 18\n" \
  "s_lshl_b32 s38, s35, 6\n" \
  "s_add_u32 s92, s14, s38\n" \
  "s_addc_u32 s93, s15, 0\n" \
  "s_lshr_b32 s37, s30, 3\n" \
  "s_add_u32 s37, s37, 96\n" \
  "s_lshl_b32 s34, s37, 17\n" \
  "s_mul_i32 s35, s35, 400\n" \
  "s_add_u32 s37, s37, s35\n" \
  "s_mul_i32 s37, s37, 16000\n" \
  "s_add_u32 s46, s22, s37\n" \
  "s_addc_u32 s47, s23, 0\n" \
  "s_lshr_b32 s36, s30, 2\n" \
  "s_add_u32 s36, s36, s35\n" \
  "s_mul_i32 s36, s36, 16000\n" \
  "s_add_u32 s44, s22, s36\n" \
  "s_addc_u32 s45, s23, 0\n" \
  "s_lshl_b32 s96, s28, 11\n" \
  "s_add_u32 s96, s96, 138240\n" \
  "v_lshlrev_b32 v238, 4, v255\n" \
  "v_mov_b32 v234, 0\n" \
  "s_lshl2_add_u32 s33, s26, s28\n" \
  "s_mov_b32 s52, 0x10002\n" \
  "s_mov_b32 s53, 0xc0c0400\n" \
  "s_mov_b32 s54, 0x4000c0c\n" \
  "s_mov_b32 s55, 0x3030303\n" \
  "s_mov_b32 s56, 0x55555555\n" \
  "s_mov_b32 s38, 0x0\n" \
  "s_mov_b32 s98, 0x0\n" \
  "s_mov_b32 s99, 0x8\n" \
  "s_mov_b32 s97, 0x2000\n" \
  "v_xor_b32 v245, 64, v244\n" \
  "v_add_u32 v245, 0x400, v245\n" \
  "s_setprio 0\n" \
  "L5_:\n" \
  "ds_read_b32 v192, v190 offset:16\n" \
  "s_waitcnt lgkmcnt(0)\n" \
  "v_readfirstlane_b32 s35, v192\n" \
  "s_cmp_lt_u32 s35, 4\n" \
  "s_cbranch_scc1 L5_\n" \
  "s_add_u32 s35, s4, 0\n" \
  "s_min_u32 s35, s35, 984\n" \
  "s_lshl_b32 s35, s35, 8\n" \
  "s_add_u32 m0, s5, 0\n" \
  "s_add_u32 s58, s94, s35\n" \
  "s_addc_u32 s59, s95, 0\n" \
  "global_load_lds_dwordx4 v244, s[58:59]\n" \
  "global_load_lds_dwordx4 v245, s[58:59] offset:1024\n" \
  "s_add_u32 s35, s4, 16\n" \
  "s_min_u32 s35, s35, 984\n" \
  "s_lshl_b32 s35, s35, 8\n" \
  "s_add_u32 m0, s5, 2048\n" \
  "s_add_u32 s58, s94, s35\n" \
  "s_addc_u32 s59, s95, 0\n" \
  "global_load_lds_dwordx4 v244, s[58:59]\n" \
  "global_load_lds_dwordx4 v245, s[58:59] offset:1024\n" \
  "L6_:\n" \
  "ds_read_b32 v192, v190 offset:12\n" \
  "s_waitcnt lgkmcnt(0)\n" \
  "v_readfirstlane_b32 s35, v192\n" \
  "s_cmp_lt_u32 s35, 4\n" \
  "s_cbranch_scc1 L6_\n" \
  "s_waitcnt vmcnt(0)\n" \
  "ds_read_b128 v[8:11], v246 offset:0\n" \
  "ds_read_b128 v[12:15], v242 offset:0\n" \
  "ds_read_b128 v[24:27], v246 offset:2048\n" \
  "ds_read_b128 v[28:31], v242 offset:2048\n" \
  "ds_read_b128 v[0:3], v249 offset:0\n" \
  "ds_read_b128 v[4:7], v249 offset:64\n" \
  "ds_read_b128 v[16:19], v249 offset:2080\n" \
  "ds_read_b128 v[20:23], v249 offset:2144\n" \
  "s_waitcnt lgkmcnt(4)\n" \
  "s_lshl2_add_u32 s35, s26, s28\n" \
  "s_mul_i32 s35, s35, 3200\n" \
  "s_add_u32 s35, s35, 0\n" \
  "s_add_u32 s58, s14, s35\n" \
  "s_addc_u32 s59, s15, 0\n" \
  "s_mov_b32 exec_lo, 0x1ffffff\n" \
  "s_mov_b32 exec_hi, 0x0\n" \
  "global_load_dword v248, v247, s[58:59]\n" \
  "s_mov_b64 exec, -1\n" \
  "s_add_u32 s35, s4, 32\n" \
  "s_min_u32 s35, s35, 984\n" \
  "s_lshl_b32 s35, s35, 8\n" \
  "s_add_u32 m0, s5, 0\n" \
  "s_add_u32 s58, s94, s35\n" \
  "s_addc_u32 s59, s95, 0\n" \
  "global_load_lds_dwordx4 v244, s[58:59]\n" \
  "global_load_lds_dwordx4 v245, s[58:59] offset:1024\n" \
  "s_add_u32 s35, s4, 48\n" \
  "s_min_u32 s35, s35, 984\n" \
  "s_lshl_b32 s35, s35, 8\n" \
  "s_add_u32 m0, s5, 2048\n" \
  "s_add_u32 s58, s94, s35\n" \
  "s_addc_u32 s59, s95, 0\n" \
  "global_load_lds_dwordx4 v244, s[58:59]\n" \
  "global_load_lds_dwordx4 v245, s[58:59] offset:1024\n" \
  "s_waitcnt vmcnt(0)\n" \
  "ds_read_b128 v[40:43], v246 offset:0\n" \
  "ds_read_b128 v[44:47], v242 offset:0\n" \
  "ds_read_b128 v[56:59], v246 offset:2048\n" \
  "ds_read_b128 v[60:63], v242 offset:2048\n" \
  "ds_read_b128 v[32:35], v249 offset:4160\n" \
  "ds_read_b128 v[36:39], v249 offset:4224\n" \
  "ds_read_b128 v[48:51], v249 offset:6240\n" \
  "ds_read_b128 v[52:55], v249 offset:6304\n" \
  "s_waitcnt lgkmcnt(4)\n" \
  "s_sleep 8\n" \
  "s_lshl2_add_u32 s35, s26, s28\n" \
  "s_mul_i32 s35, s35, 3200\n" \
  "s_add_u32 s35, s35, 3276800\n" \
  "s_add_u32 s58, s14, s35\n" \
  "s_addc_u32 s59, s15, 0\n" \
  "s_mov_b32 exec_lo, 0x1ffffff\n" \
  "s_mov_b32 exec_hi, 0x0\n" \
  "global_load_dword v248, v247, s[58:59]\n" \
  "s_mov_b64 exec, -1\n" \
  "s_add_u32 s35, s4, 64\n" \
  "s_min_u32 s35, s35, 984\n" \
  "s_lshl_b32 s35, s35, 8\n" \
  "s_add_u32 m0, s5, 0\n" \
  "s_add_u32 s58, s94, s35\n" \
  "s_addc_u32 s59, s95, 0\n" \
  "global_load_lds_dwordx4 v244, s[58:59]\n" \
  "global_load_lds_dwordx4 v245, s[58:59] offset:1024\n" \
  "s_add_u32 s35, s4, 80\n" \
  "s_min_u32 s35, s35, 984\n" \
  "s_lshl_b32 s35, s35, 8\n" \
  "s_add_u32 m0, s5, 2048\n" \
  "s_add_u32 s58, s94, s35\n" \
  "s_addc_u32 s59, s95, 0\n" \
  "global_load_lds_dwordx4 v244, s[58:59]\n" \
  "global_load_lds_dwordx4 v245, s[58:59] offset:1024\n" \
  "s_waitcnt vmcnt(0)\n" \
  "ds_read_b128 v[72:75], v246 offset:0\n" \
  "ds_read_b128 v[76:79], v242 offset:0\n" \
  "ds_read_b128 v[88:91], v246 offset:2048\n" \
  "ds_read_b128 v[92:95], v242 offset:2048\n" \
  "ds_read_b128 v[64:67], v249 offset:8320\n" \
  "ds_read_b128 v[68:71], v249 offset:8384\n" \
  "ds_read_b128 v[80:83], v249 offset:10400\n" \
  "ds_read_b128 v[84:87], v249 offset:10464\n" \
  "s_waitcnt lgkmcnt(4)\n" \
  "s_sleep 8\n" \
  "s_lshl2_add_u32 s35, s26, s28\n" \
  "s_mul_i32 s35, s35, 3200\n" \
  "s_add_u32 s35, s35, 6553600\n" \
  "s_add_u32 s58, s14, s35\n" \
  "s_addc_u32 s59, s15, 0\n" \
  "s_mov_b32 exec_lo, 0x1ffffff\n" \
  "s_mov_b32 exec_hi, 0x0\n" \
  "global_load_dword v248, v247, s[58:59]\n" \
  "s_mov_b64 exec, -1\n" \
  "s_add_u32 s35, s4, 96\n" \
  "s_min_u32 s35, s35, 984\n" \
  "s_lshl_b32 s35, s35, 8\n" \
  "s_add_u32 m0, s5, 0\n" \
  "s_add_u32 s58, s94, s35\n" \
  "s_addc_u32 s59, s95, 0\n" \
  "global_load_lds_dwordx4 v244, s[58:59]\n" \
  "global_load_lds_dwordx4 v245, s[58:59] offset:1024\n" \
  "s_add_u32 s35, s4, 112\n" \
  "s_min_u32 s35, s35, 984\n" \
  "s_lshl_b32 s35, s35, 8\n" \
  "s_add_u32 m0, s5, 2048\n" \
  "s_add_u32 s58, s94, s35\n" \
  "s_addc_u32 s59, s95, 0\n" \
  "global_load_lds_dwordx4 v244, s[58:59]\n" \
  "global_load_lds_dwordx4 v245, s[58:59] offset:1024\n" \
  "s_waitcnt vmcnt(0)\n" \
  "ds_read_b128 v[104:107], v246 offset:0\n" \
  "ds_read_b128 v[108:111], v242 offset:0\n" \
  "ds_read_b128 v[120:123], v246 offset:2048\n" \
  "ds_read_b128 v[124:127], v242 offset:2048\n" \
  "ds_read_b128 v[96:99], v250 offset:0\n" \
  "ds_read_b128 v[100:103], v250 offset:64\n" \
  "ds_read_b128 v[112:115], v251 offset:0\n" \
  "ds_read_b128 v[116:119], v251 offset:64\n" \
  "s_waitcnt lgkmcnt(4)\n" \
  "s_sleep 8\n" \
  "s_lshl2_add_u32 s35, s26, s28\n" \
  "s_mul_i32 s35, s35, 3200\n" \
  "s_add_u32 s35, s35, 9830400\n" \
  "s_add_u32 s58, s14, s35\n" \
  "s_addc_u32 s59, s15, 0\n" \
  "s_mov_b32 exec_lo, 0x1ffffff\n" \
  "s_mov_b32 exec_hi, 0x0\n" \
  "global_load_dword v248, v247, s[58:59]\n" \
  "s_mov_b64 exec, -1\n" \
  "s_add_u32 s40, s92, s6\n" \
  "s_addc_u32 s41, s93, 0\n" \
  "s_add_u32 s6, s6, 2097152\n" \
  "s_mov_b32 m0, s96\n" \
  "s_nop 0\n" \
  "global_load_lds_dwordx4 v189, s[40:41]\n" \
  "global_load_lds_dwordx4 v194, s[40:41] offset:1024\n" \
  "s_getpc_b64 s[58:59]\n" \
  "LB_pc:\n" \
  "s_sub_u32 s58, s58, LB_pc-LZ_first\n" \
  "s_subb_u32 s59, s59, 0\n" \
  "s_mul_i32 s35, s28, 7168\n" \
  "v_lshl_add_u32 v195, v255, 7, s35\n" \
  "v_min_u32 v195, LZ_end-LZ_first, v195\n" \
  "s_mov_b32 exec_hi, 0xffffff\n" \
  "global_load_dword v248, v195, s[58:59]\n" \
  "s_mov_b64 exec, -1\n" \
  "global_load_dwordx2 v[196:197], v227, s[42:43] offset:-3200\n" \
  "global_load_dwordx2 v[198:199], v227, s[42:43] offset:-2800\n" \
  "global_load_dwordx2 v[200:201], v227, s[42:43] offset:-2400\n" \
  "global_load_dwordx2 v[202:203], v227, s[42:43] offset:-1600\n" \
  "global_load_dwordx2 v[204:205], v227, s[42:43] offset:-1200\n" \
  "global_load_dwordx2 v[206:207], v227, s[42:43] offset:-800\n" \
  "global_load_dwordx2 v[208:209], v227, s[42:43] offset:-400\n" \
  "global_load_dwordx2 v[210:211], v227, s[42:43] offset:0\n" \
  "global_load_dwordx2 v[212:213], v227, s[42:43] offset:400\n" \
  "global_load_dwordx2 v[214:215], v227, s[42:43] offset:800\n" \
  "global_load_dwordx2 v[216:217], v227, s[42:43] offset:1200\n" \
  "global_load_dwordx2 v[218:219], v227, s[42:43] offset:-2000\n" \
  "global_load_dwordx2 v[220:221], v227, s[42:43] offset:1600\n" \
  "global_load_dwordx2 v[222:223], v227, s[42:43] offset:2000\n" \
  "global_load_dwordx2 v[224:225], v227, s[42:43] offset:2400\n" \
  "global_load_dwordx2 v[226:227], v227, s[42:43] offset:2800\n" \
  "s_setprio 2\n" \
  "s_mul_i32 s36, s28, 2176\n" \
  "s_add_u32 s36, s36, 154624\n" \
  "v_bfe_u32 v228, v255, 3, 1\n" \
  "v_and_b32 v229, 1, v255\n" \
  "v_lshl_add_u32 v230, v228, 1, v229\n" \
  "v_lshl_add_u32 v231, v229, 1, v228\n" \
  "v_xor_b32 v229, 1, v230\n" \
  "s_mov_b32 vcc_lo, 0xcccccccc\n" \
  "s_mov_b32 vcc_hi, vcc_lo\n" \
  "v_cndmask_b32 v228, v230, v231, vcc\n" \
  "v_cndmask_b32 v229, v229, v231, vcc\n" \
  "v_and_b32 v230, 4, v255\n" \
  "v_mul_u32_u24 v230, 272, v230\n" \
  "v_lshl_add_u32 v230, v253, 2, v230\n" \
  "v_and_or_b32 v230, v255, 2, v230\n" \
  "v_add_u32 v230, s36, v230\n" \
  "s_movk_i32 s35, 272\n" \
  "v_mad_u32_u24 v195, v228, s35, v230\n" \
  "v_mad_u32_u24 v235, v229, s35, v230\n" \
  "v_lshl_add_u32 v230, v253, 4, v238\n" \
  "v_add_u32 v254, s36, v230\n" \
  "s_lshl2_add_u32 s4, s31, s28\n" \
  "s_lshl_b32 s4, s4, 7\n" \
  "s_mul_i32 s84, s31, 0xff00ff00\n" \
  "s_cmp_eq_u32 s28, 3\n" \
  "s_cselect_b32 s84, s84, -1\n" \
  "s_mov_b32 s85, s84\n" \
  "v_readfirstlane_b32 s39, v193\n" \
  "s_lshl_b32 s35, s26, 2\n" \
  "s_add_u32 s39, s39, 1\n" \
  "v_mov_b32 v193, s35\n" \
  "v_mov_b64 v[240:241], -1\n" \
  "v_mov_b64 v[242:243], -1\n" \
  "s_waitcnt vmcnt(0)\n" \
  "s_waitcnt lgkmcnt(0)\n" \
  "s_mov_b64 exec, 1\n" \
  "ds_add_u32 v190, v191 offset:0\n" \
  "s_mov_b64 exec, -1\n" \
  "L7_:\n" \
  "s_sleep 2\n" \
  "ds_read_b32 v192, v190 offset:0\n" \
  "s_waitcnt lgkmcnt(0)\n" \
  "v_readfirstlane_b32 s35, v192\n" \
  "s_cmp_lt_u32 s35, 4\n" \
  "s_cbranch_scc1 L7_\n" \
  "s_add_u32 s96, s96, 8192\n" \
  "s_add_u32 s40, s92, s6\n" \
  "s_addc_u32 s41, s93, 0\n" \
  "s_add_u32 s6, s6, 2097152\n" \
  "s_mov_b32 m0, s96\n" \
  "s_nop 0\n" \
  "global_load_lds_dwordx4 v189, s[40:41]\n" \
  "global_load_lds_dwordx4 v194, s[40:41] offset:1024\n" \
  "s_sub_u32 s96, s96, 8192\n" \
  "ds_read_b64_tr_b16 v[128:129], v184 offset:0\n" \
  "ds_read_b64_tr_b16 v[130:131], v184 offset:256\n" \
  "ds_read_b64_tr_b16 v[132:133], v184 offset:2048\n" \
  "ds_read_b64_tr_b16 v[134:135], v184 offset:2304\n" \
  "ds_read_b64_tr_b16 v[136:137], v184 offset:4096\n" \
  "ds_read_b64_tr_b16 v[138:139], v184 offset:4352\n" \
  "ds_read_b64_tr_b16 v[140:141], v184 offset:6144\n" \
  "ds_read_b64_tr_b16 v[142:143], v184 offset:6400\n" \
  "ds_read_b64_tr_b16 v[156:157], v185 offset:6144\n" \
  "ds_read_b64_tr_b16 v[158:159], v185 offset:6400\n" \
  "ds_read_b64_tr_b16 v[152:153], v185 offset:4096\n" \
  "ds_read_b64_tr_b16 v[154:155], v185 offset:4352\n" \
  "ds_read_b64_tr_b16 v[148:149], v185 offset:2048\n" \
  "ds_read_b64_tr_b16 v[150:151], v185 offset:2304\n" \
  "ds_read_b64_tr_b16 v[144:145], v185 offset:0\n" \
  "ds_read_b64_tr_b16 v[146:147], v185 offset:256\n" \
  "v_mov_b32 v192, s39\n" \
  "s_cmp_lg_u32 s28, 0\n" \
  "s_cbranch_scc1 LB_nc\n" \
  "s_mov_b64 exec, 1\n" \
  "global_store_dword v193, v192, s[48:49] offset:0 sc1\n" \
  "s_mov_b64 exec, -1\n" \
  "LB_nc:\n" \
  "s_lshl2_add_u32 s35, s7, s28\n" \
  "s_lshl_b32 s35, s35, 7\n" \
  "s_lshl_b32 s36, s31, 16\n" \
  "s_add_u32 s35, s35, s36\n" \
  "s_add_u32 s35, s35, 109871104\n" \
  "s_add_u32 s58, s22, s35\n" \
  "s_addc_u32 s59, s23, 0\n" \
  "s_mov_b64 exec, 0xff\n" \
  "global_store_dwordx4 v238, v[240:243], s[58:59] sc1\n" \
  "s_mov_b64 exec, -1\n" \
  "s_cmp_lg_u32 s33, 1021\n" \
  "s_cbranch_scc1 LB_nm\n" \
  "v_mov_b64 v[240:241], 0\n" \
  "v_mov_b64 v[242:243], 0\n" \
  "global_store_dwordx4 v238, v[240:243], s[48:49] offset:2048 sc1\n" \
  "s_nop 1\n" \
  "v_lshlrev_b32 v242, 8, v255\n" \
  "global_store_dwordx2 v242, v[240:241], s[48:49] offset:3072 sc1\n" \
  "v_add_u32 v242, 0x4000, v242\n" \
  "global_store_dwordx2 v242, v[240:241], s[48:49] offset:3072 sc1\n" \
  "v_add_u32 v242, 0x4000, v242\n" \
  "global_store_dwordx2 v242, v[240:241], s[48:49] offset:3072 sc1\n" \
  "v_add_u32 v242, 0x4000, v242\n" \
  "global_store_dwordx2 v242, v[240:241], s[48:49] offset:3072 sc1\n" \
  "v_add_u32 v242, 0x4000, v242\n" \
  "LB_nm:\n" \
  "s_mov_b32 s86, 0x78000b0\n" \
  "s_mov_b32 s87, 0x0\n" \
  "s_or_b32 s86, s86, s87\n" \
  "s_setprio 2\n" \
  "s_memtime s[90:91]\n" \
  "s_memrealtime s[42:43]\n" \
  "LT:\n" \
  "s_cmp_eq_u32 s38, 6\n" \
  "s_cselect_b32 s78, 0x33333333, -1\n" \
  "s_cmp_eq_u32 s38, 7\n" \
  "s_cselect_b32 s78, 0x33333333, s78\n" \
  "s_cmp_eq_u32 s38, 18\n" \
  "s_cselect_b32 s80, 0x33333333, -1\n" \
  "s_cmp_eq_u32 s38, 19\n" \
  "s_cselect_b32 s80, 0x33333333, s80\n" \
  "s_mov_b32 s79, s78\n" \
  "s_mov_b32 s81, s80\n" \
  "s_mov_b32 s37, s38\n" \
  "s_add_u32 s57, s38, 2\n" \
  "s_add_u32 s3, s38, 4\n" \
  "s_add_u32 s38, s38, 6\n" \
  "s_cmp_lt_u32 s57, 32\n" \
  "s_cselect_b32 s57, s57, 37\n" \
  "s_cselect_b32 s3, s3, 37\n" \
  "s_waitcnt lgkmcnt(12)\n" \
  "v_mfma_f32_16x16x32_bf16 v[160:163], v[128:131], v[0:3], 0\n" \
  "s_mov_b64 vcc, s[78:79]\n" \
  "v_cndmask_b32_dpp v234, v234, v234, vcc row_ror:8 row_mask:0xf bank_mask:0xf\n" \
  "v_mfma_f32_16x16x32_bf16 v[160:163], v[132:135], v[4:7], v[160:163]\n" \
  "s_waitcnt lgkmcnt(8)\n" \
  "v_mfma_f32_16x16x32_bf16 v[160:163], v[136:139], v[8:11], v[160:163]\n" \
  "s_set_gpr_idx_on s37, gpr_idx(SRC0)\n" \
  "v_mad_u32_u24 v232, v196, s52, v236\n" \
  "s_set_gpr_idx_off\n" \
  "v_mfma_f32_16x16x32_bf16 v[160:163], v[140:143], v[12:15], v[160:163]\n" \
  "s_waitcnt lgkmcnt(0)\n" \
  "v_lshrrev_b32 v228, v232, v234\n" \
  "v_lshrrev_b32_dpp v229, v232, v234 quad_perm:[2,3,0,1] row_mask:0xf bank_mask:0xf\n" \
  "v_mfma_f32_16x16x32_bf16 v[164:167], v[156:159], v[12:15], 0\n" \
  "v_lshrrev_b32_dpp v230, v232, v234 quad_perm:[1,2,3,0] row_mask:0xf bank_mask:0xf\n" \
  "v_lshrrev_b32_dpp v231, v232, v234 quad_perm:[3,0,1,2] row_mask:0xf bank_mask:0xf\n" \
  "v_mfma_f32_16x16x32_bf16 v[164:167], v[152:155], v[8:11], v[164:167]\n" \
  "v_perm_b32 v228, v229, v228, s53\n" \
  "v_perm_b32 v230, v231, v230, s54\n" \
  "v_mfma_f32_16x16x32_bf16 v[164:167], v[148:151], v[4:7], v[164:167]\n" \
  "v_bitop3_b32 v228, v228, v230, s55 bitop3:0xa8\n" \
  "v_lshl_or_b32 v229, v234, 1, s56\n" \
  "v_mfma_f32_16x16x32_bf16 v[164:167], v[144:147], v[0:3], v[164:167]\n" \
  "v_add_u32_dpp v228, v228, v228 quad_perm:[2,3,0,1] row_mask:0xf bank_mask:0xf\n" \
  "v_min_u32_sdwa v230, v228, v228 dst_sel:DWORD dst_unused:UNUSED_PAD src0_sel:BYTE_0 src1_sel:BYTE_1\n" \
  "v_mfma_f32_16x16x32_bf16 v[172:175], v[144:147], v[16:19], 0\n" \
  "v_min_u32_sdwa v231, v228, v228 dst_sel:DWORD dst_unused:UNUSED_PAD src0_sel:BYTE_2 src1_sel:BYTE_3\n" \
  "v_sub_u32 v230, v230, v231\n" \
  "v_mfma_f32_16x16x32_bf16 v[172:175], v[148:151], v[20:23], v[172:175]\n" \
  "v_cvt_pk_bf16_f32 v176, v160, v161\n" \
  "v_add_u32_dpp v230, v230, v230 quad_perm:[1,0,3,2] row_mask:0xf bank_mask:0xf\n" \
  "v_mfma_f32_16x16x32_bf16 v[172:175], v[152:155], v[24:27], v[172:175]\n" \
  "v_cmp_ge_i32 vcc, 0, v230\n" \
  "v_cvt_pk_bf16_f32 v177, v162, v163\n" \
  "v_mfma_f32_16x16x32_bf16 v[172:175], v[156:159], v[28:31], v[172:175]\n" \
  "v_cndmask_b32_sdwa v228, v228, v228, vcc dst_sel:DWORD dst_unused:UNUSED_PAD src0_sel:WORD_1 src1_sel:WORD_0\n" \
  "v_cvt_pk_bf16_f32 v178, v164, v165\n" \
  "v_mfma_f32_16x16x32_bf16 v[168:171], v[140:143], v[28:31], 0\n" \
  "s_mov_b64 s[50:51], vcc\n" \
  "v_cmp_le_u32_sdwa vcc, v228, v228 src0_sel:BYTE_0 src1_sel:BYTE_1\n" \
  "v_cvt_pk_bf16_f32 v179, v166, v167\n" \
  "v_mfma_f32_16x16x32_bf16 v[168:171], v[136:139], v[24:27], v[168:171]\n" \
  "v_cndmask_b32_dpp v232, v232, v232, vcc quad_perm:[2,3,0,1] row_mask:0xf bank_mask:0xf\n" \
  "v_mfma_f32_16x16x32_bf16 v[168:171], v[132:135], v[20:23], v[168:171]\n" \
  "s_mov_b64 vcc, s[50:51]\n" \
  "v_cndmask_b32_dpp v232, v232, v232, vcc quad_perm:[3,0,1,2] row_mask:0xf bank_mask:0xf\n" \
  "v_lshlrev_b32_e64 v230, v232, 3\n" \
  "v_mfma_f32_16x16x32_bf16 v[168:171], v[128:131], v[16:19], v[168:171]\n" \
  "v_and_or_b32 v234, v229, v230, v234\n" \
  "s_cmp_lg_u32 s33, 1021\n" \
  "s_cbranch_scc0 LT_e\n" \
  "LT_r:\n" \
  "s_waitcnt vmcnt(5)\n" \
  "global_store_dwordx4 v186, v[176:179], s[44:45] offset:0 sc1\n" \
  "v_mfma_f32_16x16x32_bf16 v[160:163], v[128:131], v[32:35], 0\n" \
  "s_set_gpr_idx_on s37, gpr_idx(DST)\n" \
  "v_mov_b32 v196, v232\n" \
  "s_set_gpr_idx_off\n" \
  "v_mfma_f32_16x16x32_bf16 v[160:163], v[132:135], v[36:39], v[160:163]\n" \
  "s_set_gpr_idx_on s57, gpr_idx(SRC0)\n" \
  "v_mad_u32_u24 v232, v196, s52, v236\n" \
  "s_set_gpr_idx_off\n" \
  "v_mfma_f32_16x16x32_bf16 v[160:163], v[136:139], v[40:43], v[160:163]\n" \
  "v_lshrrev_b32 v228, v232, v234\n" \
  "v_lshrrev_b32_dpp v229, v232, v234 quad_perm:[2,3,0,1] row_mask:0xf bank_mask:0xf\n" \
  "v_mfma_f32_16x16x32_bf16 v[160:163], v[140:143], v[44:47], v[160:163]\n" \
  "v_lshrrev_b32_dpp v230, v232, v234 quad_perm:[1,2,3,0] row_mask:0xf bank_mask:0xf\n" \
  "v_lshrrev_b32_dpp v231, v232, v234 quad_perm:[3,0,1,2] row_mask:0xf bank_mask:0xf\n" \
  "v_mfma_f32_16x16x32_bf16 v[164:167], v[156:159], v[44:47], 0\n" \
  "v_perm_b32 v228, v229, v228, s53\n" \
  "v_perm_b32 v230, v231, v230, s54\n" \
  "v_mfma_f32_16x16x32_bf16 v[164:167], v[152:155], v[40:43], v[164:167]\n" \
  "v_bitop3_b32 v228, v228, v230, s55 bitop3:0xa8\n" \
  "v_lshl_or_b32 v229, v234, 1, s56\n" \
  "v_mfma_f32_16x16x32_bf16 v[164:167], v[148:151], v[36:39], v[164:167]\n" \
  "v_cvt_pk_bf16_f32 v182, v172, v173\n" \
  "v_cvt_pk_bf16_f32 v183, v174, v175\n" \
  "v_mfma_f32_16x16x32_bf16 v[164:167], v[144:147], v[32:35], v[164:167]\n" \
  "v_cvt_pk_bf16_f32 v180, v168, v169\n" \
  "v_cvt_pk_bf16_f32 v181, v170, v171\n" \
  "s_waitcnt vmcnt(5)\n" \
  "global_store_dwordx4 v186, v[180:183], s[44:45] offset:256 sc1\n" \
  "v_mfma_f32_16x16x32_bf16 v[172:175], v[144:147], v[48:51], 0\n" \
  "v_add_u32_dpp v228, v228, v228 quad_perm:[2,3,0,1] row_mask:0xf bank_mask:0xf\n" \
  "v_min_u32_sdwa v230, v228, v228 dst_sel:DWORD dst_unused:UNUSED_PAD src0_sel:BYTE_0 src1_sel:BYTE_1\n" \
  "v_mfma_f32_16x16x32_bf16 v[172:175], v[148:151], v[52:55], v[172:175]\n" \
  "v_min_u32_sdwa v231, v228, v228 dst_sel:DWORD dst_unused:UNUSED_PAD src0_sel:BYTE_2 src1_sel:BYTE_3\n" \
  "v_sub_u32 v230, v230, v231\n" \
  "v_mfma_f32_16x16x32_bf16 v[172:175], v[152:155], v[56:59], v[172:175]\n" \
  "v_cvt_pk_bf16_f32 v176, v160, v161\n" \
  "v_add_u32_dpp v230, v230, v230 quad_perm:[1,0,3,2] row_mask:0xf bank_mask:0xf\n" \
  "v_mfma_f32_16x16x32_bf16 v[172:175], v[156:159], v[60:63], v[172:175]\n" \
  "v_cmp_ge_i32 vcc, 0, v230\n" \
  "v_cvt_pk_bf16_f32 v177, v162, v163\n" \
  "v_mfma_f32_16x16x32_bf16 v[168:171], v[140:143], v[60:63], 0\n" \
  "v_cndmask_b32_sdwa v228, v228, v228, vcc dst_sel:DWORD dst_unused:UNUSED_PAD src0_sel:WORD_1 src1_sel:WORD_0\n" \
  "v_cvt_pk_bf16_f32 v178, v164, v165\n" \
  "v_mfma_f32_16x16x32_bf16 v[168:171], v[136:139], v[56:59], v[168:171]\n" \
  "s_mov_b64 s[50:51], vcc\n" \
  "v_cmp_le_u32_sdwa vcc, v228, v228 src0_sel:BYTE_0 src1_sel:BYTE_1\n" \
  "v_cvt_pk_bf16_f32 v179, v166, v167\n" \
  "v_mfma_f32_16x16x32_bf16 v[168:171], v[132:135], v[52:55], v[168:171]\n" \
  "v_cndmask_b32_dpp v232, v232, v232, vcc quad_perm:[2,3,0,1] row_mask:0xf bank_mask:0xf\n" \
  "v_mfma_f32_16x16x32_bf16 v[168:171], v[128:131], v[48:51], v[168:171]\n" \
  "s_mov_b64 vcc, s[50:51]\n" \
  "v_cndmask_b32_dpp v232, v232, v232, vcc quad_perm:[3,0,1,2] row_mask:0xf bank_mask:0xf\n" \
  "v_lshlrev_b32_e64 v230, v232, 3\n" \
  "s_waitcnt vmcnt(5)\n" \
  "global_store_dwordx4 v186, v[176:179], s[44:45] offset:512 sc1\n" \
  "v_mfma_f32_16x16x32_bf16 v[160:163], v[128:131], v[64:67], 0\n" \
  "v_and_or_b32 v234, v229, v230, v234\n" \
  "v_cvt_pk_bf16_f32 v182, v172, v173\n" \
  "v_mfma_f32_16x16x32_bf16 v[160:163], v[132:135], v[68:71], v[160:163]\n" \
  "s_set_gpr_idx_on s57, gpr_idx(DST)\n" \
  "v_mov_b32 v196, v232\n" \
  "s_set_gpr_idx_off\n" \
  "v_mfma_f32_16x16x32_bf16 v[160:163], v[136:139], v[72:75], v[160:163]\n" \
  "s_mov_b64 vcc, s[80:81]\n" \
  "v_cndmask_b32_dpp v234, v234, v234, vcc row_ror:8 row_mask:0xf bank_mask:0xf\n" \
  "v_cvt_pk_bf16_f32 v183, v174, v175\n" \
  "v_mfma_f32_16x16x32_bf16 v[160:163], v[140:143], v[76:79], v[160:163]\n" \
  "s_set_gpr_idx_on s3, gpr_idx(SRC0)\n" \
  "v_mad_u32_u24 v232, v196, s52, v236\n" \
  "s_set_gpr_idx_off\n" \
  "v_mfma_f32_16x16x32_bf16 v[164:167], v[156:159], v[76:79], 0\n" \
  "v_lshrrev_b32 v228, v232, v234\n" \
  "v_lshrrev_b32_dpp v229, v232, v234 quad_perm:[2,3,0,1] row_mask:0xf bank_mask:0xf\n" \
  "v_mfma_f32_16x16x32_bf16 v[164:167], v[152:155], v[72:75], v[164:167]\n" \
  "v_lshrrev_b32_dpp v230, v232, v234 quad_perm:[1,2,3,0] row_mask:0xf bank_mask:0xf\n" \
  "v_lshrrev_b32_dpp v231, v232, v234 quad_perm:[3,0,1,2] row_mask:0xf bank_mask:0xf\n" \
  "v_mfma_f32_16x16x32_bf16 v[164:167], v[148:151], v[68:71], v[164:167]\n" \
  "v_perm_b32 v228, v229, v228, s53\n" \
  "v_perm_b32 v230, v231, v230, s54\n" \
  "v_mfma_f32_16x16x32_bf16 v[164:167], v[144:147], v[64:67], v[164:167]\n" \
  "v_cvt_pk_bf16_f32 v180, v168, v169\n" \
  "v_cvt_pk_bf16_f32 v181, v170, v171\n" \
  "s_waitcnt vmcnt(5)\n" \
  "global_store_dwordx4 v186, v[180:183], s[44:45] offset:768 sc1\n" \
  "v_mfma_f32_16x16x32_bf16 v[172:175], v[144:147], v[80:83], 0\n" \
  "s_waitcnt vmcnt(5)\n" \
  "s_mov_b64 exec, 1\n" \
  "ds_add_u32 v190, v191 offset:0\n" \
  "s_mov_b64 exec, -1\n" \
  "v_mfma_f32_16x16x32_bf16 v[172:175], v[148:151], v[84:87], v[172:175]\n" \
  "v_bitop3_b32 v228, v228, v230, s55 bitop3:0xa8\n" \
  "v_lshl_or_b32 v229, v234, 1, s56\n" \
  "v_mfma_f32_16x16x32_bf16 v[172:175], v[152:155], v[88:91], v[172:175]\n" \
  "v_add_u32_dpp v228, v228, v228 quad_perm:[2,3,0,1] row_mask:0xf bank_mask:0xf\n" \
  "v_min_u32_sdwa v230, v228, v228 dst_sel:DWORD dst_unused:UNUSED_PAD src0_sel:BYTE_0 src1_sel:BYTE_1\n" \
  "v_mfma_f32_16x16x32_bf16 v[172:175], v[156:159], v[92:95], v[172:175]\n" \
  "v_min_u32_sdwa v231, v228, v228 dst_sel:DWORD dst_unused:UNUSED_PAD src0_sel:BYTE_2 src1_sel:BYTE_3\n" \
  "v_sub_u32 v230, v230, v231\n" \
  "v_mfma_f32_16x16x32_bf16 v[168:171], v[140:143], v[92:95], 0\n" \
  "v_cvt_pk_bf16_f32 v176, v160, v161\n" \
  "v_add_u32_dpp v230, v230, v230 quad_perm:[1,0,3,2] row_mask:0xf bank_mask:0xf\n" \
  "v_mfma_f32_16x16x32_bf16 v[168:171], v[136:139], v[88:91], v[168:171]\n" \
  "v_cmp_ge_i32 vcc, 0, v230\n" \
  "v_cvt_pk_bf16_f32 v177, v162, v163\n" \
  "v_mfma_f32_16x16x32_bf16 v[168:171], v[132:135], v[84:87], v[168:171]\n" \
  "v_cndmask_b32_sdwa v228, v228, v228, vcc dst_sel:DWORD dst_unused:UNUSED_PAD src0_sel:WORD_1 src1_sel:WORD_0\n" \
  "v_cvt_pk_bf16_f32 v178, v164, v165\n" \
  "v_mfma_f32_16x16x32_bf16 v[168:171], v[128:131], v[80:83], v[168:171]\n" \
  "s_mov_b64 s[50:51], vcc\n" \
  "v_cmp_le_u32_sdwa vcc, v228, v228 src0_sel:BYTE_0 src1_sel:BYTE_1\n" \
  "v_cvt_pk_bf16_f32 v179, v166, v167\n" \
  "s_cmp_eq_u32 s98, 4\n" \
  "s_cselect_b32 s36, 12451840, 0\n" \
  "s_sub_u32 s6, s6, s36\n" \
  "s_cmp_lt_u32 s98, 10\n" \
  "s_cselect_b32 s6, s6, s34\n" \
  "s_add_u32 s40, s92, s6\n" \
  "s_addc_u32 s41, s93, 0\n" \
  "s_add_u32 s6, s6, 2097152\n" \
  "s_waitcnt vmcnt(5)\n" \
  "global_store_dwordx4 v186, v[176:179], s[44:45] offset:1024 sc1\n" \
  "v_mfma_f32_16x16x32_bf16 v[160:163], v[128:131], v[96:99], 0\n" \
  "ds_read_b32 v192, v190\n" \
  "v_add_u32 v184, s97, v184\n" \
  "v_mfma_f32_16x16x32_bf16 v[160:163], v[132:135], v[100:103], v[160:163]\n" \
  "v_add_u32 v185, s97, v185\n" \
  "v_cndmask_b32_dpp v232, v232, v232, vcc quad_perm:[2,3,0,1] row_mask:0xf bank_mask:0xf\n" \
  "v_mfma_f32_16x16x32_bf16 v[160:163], v[136:139], v[104:107], v[160:163]\n" \
  "s_mov_b64 vcc, s[50:51]\n" \
  "v_cndmask_b32_dpp v232, v232, v232, vcc quad_perm:[3,0,1,2] row_mask:0xf bank_mask:0xf\n" \
  "v_lshlrev_b32_e64 v230, v232, 3\n" \
  "v_mfma_f32_16x16x32_bf16 v[160:163], v[140:143], v[108:111], v[160:163]\n" \
  "v_and_or_b32 v234, v229, v230, v234\n" \
  "v_cvt_pk_bf16_f32 v182, v172, v173\n" \
  "v_mfma_f32_16x16x32_bf16 v[164:167], v[156:159], v[108:111], 0\n" \
  "s_set_gpr_idx_on s3, gpr_idx(DST)\n" \
  "v_mov_b32 v196, v232\n" \
  "s_set_gpr_idx_off\n" \
  "v_mfma_f32_16x16x32_bf16 v[164:167], v[152:155], v[104:107], v[164:167]\n" \
  "s_waitcnt lgkmcnt(0)\n" \
  "v_readfirstlane_b32 s35, v192\n" \
  "v_cvt_pk_bf16_f32 v183, v174, v175\n" \
  "v_mfma_f32_16x16x32_bf16 v[164:167], v[148:151], v[100:103], v[164:167]\n" \
  "v_cvt_pk_bf16_f32 v180, v168, v169\n" \
  "v_cvt_pk_bf16_f32 v181, v170, v171\n" \
  "v_mfma_f32_16x16x32_bf16 v[164:167], v[144:147], v[96:99], v[164:167]\n" \
  "s_cmp_lt_u32 s35, s99\n" \
  "s_cbranch_scc1 LT_w\n" \
  "LT_c:\n" \
  "s_mov_b32 m0, s96\n" \
  "s_nop 0\n" \
  "global_load_lds_dwordx4 v189, s[40:41]\n" \
  "global_load_lds_dwordx4 v194, s[40:41] offset:1024\n" \
  "s_add_u32 s96, s96, s97\n" \
  "s_sub_u32 s97, 0, s97\n" \
  "s_add_u32 s99, s99, 4\n" \
  "s_lshl4_add_u32 s35, s28, s98\n" \
  "s_bitcmp1_b64 s[86:87], s35\n" \
  "s_cbranch_scc1 LT_a\n" \
  "LT_s:\n" \
  "s_waitcnt vmcnt(5)\n" \
  "global_store_dwordx4 v186, v[180:183], s[44:45] offset:1280 sc1\n" \
  "v_mfma_f32_16x16x32_bf16 v[168:171], v[128:131], v[112:115], 0\n" \
  "ds_read_b64_tr_b16 v[128:129], v184 offset:0\n" \
  "ds_read_b64_tr_b16 v[130:131], v184 offset:256\n" \
  "v_mfma_f32_16x16x32_bf16 v[168:171], v[132:135], v[116:119], v[168:171]\n" \
  "ds_read_b64_tr_b16 v[132:133], v184 offset:2048\n" \
  "ds_read_b64_tr_b16 v[134:135], v184 offset:2304\n" \
  "v_mfma_f32_16x16x32_bf16 v[168:171], v[136:139], v[120:123], v[168:171]\n" \
  "ds_read_b64_tr_b16 v[136:137], v184 offset:4096\n" \
  "v_cvt_pk_bf16_f32 v176, v160, v161\n" \
  "v_mfma_f32_16x16x32_bf16 v[168:171], v[140:143], v[124:127], v[168:171]\n" \
  "ds_read_b64_tr_b16 v[138:139], v184 offset:4352\n" \
  "v_cvt_pk_bf16_f32 v177, v162, v163\n" \
  "v_mfma_f32_16x16x32_bf16 v[172:175], v[156:159], v[124:127], 0\n" \
  "ds_read_b64_tr_b16 v[140:141], v184 offset:6144\n" \
  "v_cvt_pk_bf16_f32 v178, v164, v165\n" \
  "v_mfma_f32_16x16x32_bf16 v[172:175], v[152:155], v[120:123], v[172:175]\n" \
  "ds_read_b64_tr_b16 v[142:143], v184 offset:6400\n" \
  "v_cvt_pk_bf16_f32 v179, v166, v167\n" \
  "v_mfma_f32_16x16x32_bf16 v[172:175], v[148:151], v[116:119], v[172:175]\n" \
  "ds_read_b64_tr_b16 v[156:157], v185 offset:6144\n" \
  "ds_read_b64_tr_b16 v[158:159], v185 offset:6400\n" \
  "v_mfma_f32_16x16x32_bf16 v[172:175], v[144:147], v[112:115], v[172:175]\n" \
  "ds_read_b64_tr_b16 v[152:153], v185 offset:4096\n" \
  "ds_read_b64_tr_b16 v[154:155], v185 offset:4352\n" \
  "ds_read_b64_tr_b16 v[148:149], v185 offset:2048\n" \
  "ds_read_b64_tr_b16 v[150:151], v185 offset:2304\n" \
  "ds_read_b64_tr_b16 v[144:145], v185 offset:0\n" \
  "ds_read_b64_tr_b16 v[146:147], v185 offset:256\n" \
  "s_waitcnt vmcnt(5)\n" \
  "global_store_dwordx4 v187, v[176:179], s[44:45] offset:0 sc1\n" \
  "s_add_u32 s98, s98, 1\n" \
  "v_cvt_pk_bf16_f32 v180, v168, v169\n" \
  "v_cvt_pk_bf16_f32 v181, v170, v171\n" \
  "v_cvt_pk_bf16_f32 v182, v172, v173\n" \
  "v_cvt_pk_bf16_f32 v183, v174, v175\n" \
  "s_mov_b64 exec, s[84:85]\n" \
  "s_cbranch_execz L8_\n" \
  "s_waitcnt vmcnt(5)\n" \
  "global_store_dwordx4 v188, v[180:183], s[44:45] offset:0 sc1\n" \
  "L8_:\n" \
  "s_mov_b64 exec, -1\n" \
  "s_add_u32 s44, s44, 128000\n" \
  "s_addc_u32 s45, s45, 0\n" \
  "s_bitcmp0_b32 4160, s98\n" \
  "s_cbranch_scc1 LT\n" \
  "s_cmp_eq_u32 s98, 6\n" \
  "s_cbranch_scc1 LT_m\n" \
  "s_lshr_b32 s35, s30, 2\n" \
  "s_lshl3_add_u32 s35, s29, s35\n" \
  "s_mul_i32 s35, s35, 1600000\n" \
  "s_lshl_b32 s36, s28, 12\n" \
  "s_add_u32 s35, s35, s36\n" \
  "s_add_u32 s40, s22, s35\n" \
  "s_addc_u32 s41, s23, 0\n" \
  "s_bitcmp1_b32 s30, 2\n" \
  "s_cbranch_scc1 LB_l_h\n" \
  "global_load_dwordx4 v[64:67], v238, s[40:41] offset:0 sc1\n" \
  "global_load_dwordx4 v[68:71], v238, s[40:41] offset:1024 sc1\n" \
  "global_load_dwordx4 v[72:75], v238, s[40:41] offset:2048 sc1\n" \
  "global_load_dwordx4 v[76:79], v238, s[40:41] offset:3072 sc1\n" \
  "s_add_u32 s40, s40, 16000\n" \
  "s_addc_u32 s41, s41, 0\n" \
  "global_load_dwordx4 v[80:83], v238, s[40:41] offset:0 sc1\n" \
  "global_load_dwordx4 v[84:87], v238, s[40:41] offset:1024 sc1\n" \
  "global_load_dwordx4 v[88:91], v238, s[40:41] offset:2048 sc1\n" \
  "global_load_dwordx4 v[92:95], v238, s[40:41] offset:3072 sc1\n" \
  "s_add_u32 s40, s40, 16000\n" \
  "s_addc_u32 s41, s41, 0\n" \
  "global_load_dwordx4 v[96:99], v238, s[40:41] offset:0 sc1\n" \
  "global_load_dwordx4 v[100:103], v238, s[40:41] offset:1024 sc1\n" \
  "global_load_dwordx4 v[104:107], v238, s[40:41] offset:2048 sc1\n" \
  "global_load_dwordx4 v[108:111], v238, s[40:41] offset:3072 sc1\n" \
  "s_add_u32 s40, s40, 16000\n" \
  "s_addc_u32 s41, s41, 0\n" \
  "global_load_dwordx4 v[112:115], v238, s[40:41] offset:0 sc1\n" \
  "global_load_dwordx4 v[116:119], v238, s[40:41] offset:1024 sc1\n" \
  "global_load_dwordx4 v[120:123], v238, s[40:41] offset:2048 sc1\n" \
  "global_load_dwordx4 v[124:127], v238, s[40:41] offset:3072 sc1\n" \
  "s_add_u32 s40, s40, 16000\n" \
  "s_addc_u32 s41, s41, 0\n" \
  "s_branch LB_l_e\n" \
  "LB_l_h:\n" \
  "global_load_dwordx4 v[0:3], v238, s[40:41] offset:0 sc1\n" \
  "global_load_dwordx4 v[4:7], v238, s[40:41] offset:1024 sc1\n" \
  "global_load_dwordx4 v[8:11], v238, s[40:41] offset:2048 sc1\n" \
  "global_load_dwordx4 v[12:15], v238, s[40:41] offset:3072 sc1\n" \
  "s_add_u32 s40, s40, 16000\n" \
  "s_addc_u32 s41, s41, 0\n" \
  "global_load_dwordx4 v[16:19], v238, s[40:41] offset:0 sc1\n" \
  "global_load_dwordx4 v[20:23], v238, s[40:41] offset:1024 sc1\n" \
  "global_load_dwordx4 v[24:27], v238, s[40:41] offset:2048 sc1\n" \
  "global_load_dwordx4 v[28:31], v238, s[40:41] offset:3072 sc1\n" \
  "s_add_u32 s40, s40, 16000\n" \
  "s_addc_u32 s41, s41, 0\n" \
  "global_load_dwordx4 v[32:35], v238, s[40:41] offset:0 sc1\n" \
  "global_load_dwordx4 v[36:39], v238, s[40:41] offset:1024 sc1\n" \
  "global_load_dwordx4 v[40:43], v238, s[40:41] offset:2048 sc1\n" \
  "global_load_dwordx4 v[44:47], v238, s[40:41] offset:3072 sc1\n" \
  "s_add_u32 s40, s40, 16000\n" \
  "s_addc_u32 s41, s41, 0\n" \
  "global_load_dwordx4 v[48:51], v238, s[40:41] offset:0 sc1\n" \
  "global_load_dwordx4 v[52:55], v238, s[40:41] offset:1024 sc1\n" \
  "global_load_dwordx4 v[56:59], v238, s[40:41] offset:2048 sc1\n" \
  "global_load_dwordx4 v[60:63], v238, s[40:41] offset:3072 sc1\n" \
  "s_add_u32 s40, s40, 16000\n" \
  "s_addc_u32 s41, s41, 0\n" \
  "LB_l_e:\n" \
  "ds_write_b16_d16_hi v195, v197 offset:0\n" \
  "ds_write_b16_d16_hi v195, v199 offset:16\n" \
  "ds_write_b16_d16_hi v195, v201 offset:32\n" \
  "ds_write_b16_d16_hi v235, v203 offset:64\n" \
  "ds_write_b16_d16_hi v235, v205 offset:80\n" \
  "ds_write_b16_d16_hi v235, v207 offset:96\n" \
  "ds_write_b16_d16_hi v235, v209 offset:112\n" \
  "ds_write_b16_d16_hi v235, v211 offset:128\n" \
  "ds_write_b16_d16_hi v235, v213 offset:144\n" \
  "ds_write_b16_d16_hi v235, v215 offset:160\n" \
  "ds_write_b16_d16_hi v235, v217 offset:176\n" \
  "ds_write_b16_d16_hi v195, v219 offset:48\n" \
  "ds_write_b16_d16_hi v195, v221 offset:192\n" \
  "ds_write_b16_d16_hi v195, v223 offset:208\n" \
  "ds_write_b16_d16_hi v195, v225 offset:224\n" \
  "ds_write_b16_d16_hi v195, v227 offset:240\n" \
  "ds_read_b128 v[244:247], v254 offset:0\n" \
  "ds_read_b128 v[248:251], v254 offset:1088\n" \
  "s_lshr_b32 s35, s7, 5\n" \
  "s_mul_i32 s35, s35, 819200\n" \
  "s_and_b32 s36, s7, 31\n" \
  "s_lshl_b32 s36, s36, 10\n" \
  "s_add_u32 s35, s35, s36\n" \
  "s_lshl_b32 s36, s4, 9\n" \
  "s_add_u32 s35, s35, s36\n" \
  "s_add_u32 s35, s35, 102793216\n" \
  "s_add_u32 s58, s22, s35\n" \
  "s_addc_u32 s59, s23, 0\n" \
  "s_waitcnt lgkmcnt(0)\n" \
  "s_cmp_ge_u32 s4, 896\n" \
  "s_cbranch_scc1 L18_\n" \
  "global_store_dwordx4 v238, v[244:247], s[58:59] sc1\n" \
  "s_cmp_ge_u32 s4, 768\n" \
  "s_cbranch_scc1 L18_\n" \
  "s_add_u32 s58, s58, 32768\n" \
  "s_addc_u32 s59, s59, 0\n" \
  "global_store_dwordx4 v238, v[248:251], s[58:59] sc1\n" \
  "L18_:\n" \
  "s_lshr_b32 s35, s30, 2\n" \
  "s_lshl3_add_u32 s33, s29, s35\n" \
  "s_and_b32 s34, s30, 3\n" \
  "s_lshl_b32 s59, s27, 11\n" \
  "s_mul_i32 s35, s33, 1600000\n" \
  "s_add_u32 s54, s22, s35\n" \
  "s_addc_u32 s55, s23, 0\n" \
  "s_mul_i32 s35, s34, 819200\n" \
  "s_lshl1_add_u32 s35, s59, s35\n" \
  "s_add_u32 s35, s35, 102400000\n" \
  "s_add_u32 s56, s22, s35\n" \
  "s_addc_u32 s57, s23, 0\n" \
  "s_lshr_b32 s35, s30, 4\n" \
  "s_lshl1_add_u32 s35, s29, s35\n" \
  "s_lshl_b32 s35, s35, 6\n" \
  "s_bfe_u32 s37, s30, 0x20002\n" \
  "s_lshl3_add_u32 s35, s37, s35\n" \
  "v_and_b32 v202, 16, v255\n" \
  "v_lshl_add_u32 v202, v202, 1, s35\n" \
  "global_load_dwordx2 v[204:205], v202, s[16:17]\n" \
  "global_load_dwordx2 v[206:207], v202, s[18:19]\n" \
  "v_mov_b32 v202, v238\n" \
  "global_load_dwordx4 v[208:211], v238, s[56:57] offset:0 sc1\n" \
  "global_load_dwordx4 v[212:215], v238, s[56:57] offset:1024 sc1\n" \
  "global_load_dwordx4 v[216:219], v238, s[56:57] offset:2048 sc1\n" \
  "global_load_dwordx4 v[220:223], v238, s[56:57] offset:3072 sc1\n" \
  "s_add_u32 s56, s56, 32768\n" \
  "s_addc_u32 s57, s57, 0\n" \
  "global_load_dwordx4 v[224:227], v238, s[56:57] sc1\n" \
  "global_load_dwordx4 v[228:231], v238, s[56:57] offset:1024 sc1\n" \
  "global_load_dwordx4 v[232:235], v238, s[56:57] offset:2048 sc1\n" \
  "global_load_dwordx4 v[236:239], v238, s[56:57] offset:3072 sc1\n" \
  "s_add_u32 s56, s56, 32768\n" \
  "s_addc_u32 s57, s57, 0\n" \
  "s_bitcmp1_b32 s30, 2\n" \
  "s_cbranch_scc1 LB_t_h\n" \
  "s_waitcnt lgkmcnt(12)\n" \
  "v_mfma_f32_16x16x32_bf16 v[160:163], v[128:131], v[0:3], 0\n" \
  "v_mfma_f32_16x16x32_bf16 v[160:163], v[132:135], v[4:7], v[160:163]\n" \
  "s_waitcnt lgkmcnt(8)\n" \
  "v_mfma_f32_16x16x32_bf16 v[160:163], v[136:139], v[8:11], v[160:163]\n" \
  "v_mfma_f32_16x16x32_bf16 v[160:163], v[140:143], v[12:15], v[160:163]\n" \
  "s_waitcnt lgkmcnt(0)\n" \
  "v_mfma_f32_16x16x32_bf16 v[164:167], v[156:159], v[12:15], 0\n" \
  "v_mfma_f32_16x16x32_bf16 v[164:167], v[152:155], v[8:11], v[164:167]\n" \
  "v_mfma_f32_16x16x32_bf16 v[164:167], v[148:151], v[4:7], v[164:167]\n" \
  "v_mfma_f32_16x16x32_bf16 v[164:167], v[144:147], v[0:3], v[164:167]\n" \
  "v_mfma_f32_16x16x32_bf16 v[172:175], v[144:147], v[16:19], 0\n" \
  "v_mfma_f32_16x16x32_bf16 v[172:175], v[148:151], v[20:23], v[172:175]\n" \
  "v_mfma_f32_16x16x32_bf16 v[172:175], v[152:155], v[24:27], v[172:175]\n" \
  "v_cvt_pk_bf16_f32 v176, v160, v161\n" \
  "v_cvt_pk_bf16_f32 v177, v162, v163\n" \
  "v_mfma_f32_16x16x32_bf16 v[172:175], v[156:159], v[28:31], v[172:175]\n" \
  "v_mfma_f32_16x16x32_bf16 v[168:171], v[140:143], v[28:31], 0\n" \
  "v_mfma_f32_16x16x32_bf16 v[168:171], v[136:139], v[24:27], v[168:171]\n" \
  "v_cvt_pk_bf16_f32 v178, v164, v165\n" \
  "v_cvt_pk_bf16_f32 v179, v166, v167\n" \
  "v_mfma_f32_16x16x32_bf16 v[168:171], v[132:135], v[20:23], v[168:171]\n" \
  "v_mfma_f32_16x16x32_bf16 v[168:171], v[128:131], v[16:19], v[168:171]\n" \
  "s_waitcnt vmcnt(63)\n" \
  "global_store_dwordx4 v186, v[176:179], s[46:47] offset:0 sc1\n" \
  "v_mfma_f32_16x16x32_bf16 v[160:163], v[128:131], v[32:35], 0\n" \
  "v_cvt_pk_bf16_f32 v182, v172, v173\n" \
  "v_cvt_pk_bf16_f32 v183, v174, v175\n" \
  "v_mfma_f32_16x16x32_bf16 v[160:163], v[132:135], v[36:39], v[160:163]\n" \
  "v_mfma_f32_16x16x32_bf16 v[160:163], v[136:139], v[40:43], v[160:163]\n" \
  "v_mfma_f32_16x16x32_bf16 v[160:163], v[140:143], v[44:47], v[160:163]\n" \
  "v_cvt_pk_bf16_f32 v180, v168, v169\n" \
  "v_cvt_pk_bf16_f32 v181, v170, v171\n" \
  "v_mfma_f32_16x16x32_bf16 v[164:167], v[156:159], v[44:47], 0\n" \
  "v_mfma_f32_16x16x32_bf16 v[164:167], v[152:155], v[40:43], v[164:167]\n" \
  "v_mfma_f32_16x16x32_bf16 v[164:167], v[148:151], v[36:39], v[164:167]\n" \
  "v_mfma_f32_16x16x32_bf16 v[164:167], v[144:147], v[32:35], v[164:167]\n" \
  "s_waitcnt vmcnt(63)\n" \
  "global_store_dwordx4 v186, v[180:183], s[46:47] offset:256 sc1\n" \
  "v_mfma_f32_16x16x32_bf16 v[172:175], v[144:147], v[48:51], 0\n" \
  "v_cvt_pk_bf16_f32 v176, v160, v161\n" \
  "v_cvt_pk_bf16_f32 v177, v162, v163\n" \
  "v_mfma_f32_16x16x32_bf16 v[172:175], v[148:151], v[52:55], v[172:175]\n" \
  "v_mfma_f32_16x16x32_bf16 v[172:175], v[152:155], v[56:59], v[172:175]\n" \
  "v_mfma_f32_16x16x32_bf16 v[172:175], v[156:159], v[60:63], v[172:175]\n" \
  "v_cvt_pk_bf16_f32 v178, v164, v165\n" \
  "v_cvt_pk_bf16_f32 v179, v166, v167\n" \
  "v_mfma_f32_16x16x32_bf16 v[168:171], v[140:143], v[60:63], 0\n" \
  "v_mfma_f32_16x16x32_bf16 v[168:171], v[136:139], v[56:59], v[168:171]\n" \
  "v_mfma_f32_16x16x32_bf16 v[168:171], v[132:135], v[52:55], v[168:171]\n" \
  "v_mfma_f32_16x16x32_bf16 v[168:171], v[128:131], v[48:51], v[168:171]\n" \
  "s_waitcnt vmcnt(63)\n" \
  "global_store_dwordx4 v186, v[176:179], s[46:47] offset:512 sc1\n" \
  "v_cvt_pk_bf16_f32 v182, v172, v173\n" \
  "v_cvt_pk_bf16_f32 v183, v174, v175\n" \
  "s_nop 3\n" \
  "v_cvt_pk_bf16_f32 v180, v168, v169\n" \
  "v_cvt_pk_bf16_f32 v181, v170, v171\n" \
  "s_waitcnt vmcnt(63)\n" \
  "global_store_dwordx4 v186, v[180:183], s[46:47] offset:768 sc1\n" \
  "s_branch LB_t_e\n" \
  "LT_w:\n" \
  "L9_:\n" \
  "s_sleep 1\n" \
  "ds_read_b32 v192, v190 offset:0\n" \
  "s_waitcnt lgkmcnt(0)\n" \
  "v_readfirstlane_b32 s35, v192\n" \
  "s_cmp_lt_u32 s35, s99\n" \
  "s_cbranch_scc1 L9_\n" \
  "s_branch LT_c\n" \
  "LT_e:\n" \
  "s_bitcmp1_b32 10, s98\n" \
  "s_cbranch_scc0 LT_r\n" \
  "global_load_dwordx4 v[240:243], v238, s[48:49] offset:0 sc1\n" \
  "s_waitcnt vmcnt(0)\n" \
  "global_store_dwordx4 v238, v[240:243], s[48:49] offset:1024 sc1\n" \
  "s_branch LT_r\n" \
  "LT_a:\n" \
  "s_cmp_lg_u32 s35, 4\n" \
  "s_cbranch_scc1 L10_\n" \
  "global_load_dword v193, v193, s[48:49] offset:1024 sc1\n" \
  "s_branch LT_s\n" \
  "L10_:\n" \
  "s_cmp_lg_u32 s35, 5\n" \
  "s_cbranch_scc1 L11_\n" \
  "s_memtime s[58:59]\n" \
  "s_memrealtime s[40:41]\n" \
  "s_mov_b32 s5, 0x10000\n" \
  "LB_ec:\n" \
  "v_readfirstlane_b32 s35, v193\n" \
  "s_cmp_eq_u32 s35, s39\n" \
  "s_cbranch_scc1 LB_ok\n" \
  "s_lshl_b32 s35, s26, 2\n" \
  "v_mov_b32 v193, s35\n" \
  "s_sleep 8\n" \
  "global_load_dword v193, v193, s[48:49] offset:1024 sc1\n" \
  "s_waitcnt vmcnt(0)\n" \
  "s_sub_u32 s5, s5, 1\n" \
  "s_cbranch_scc0 LB_ec\n" \
  "LB_ok:\n" \
  "s_lshl_b32 s35, s26, 2\n" \
  "v_mov_b32 v244, s35\n" \
  "s_waitcnt lgkmcnt(0)\n" \
  "s_sub_u32 s88, s58, s88\n" \
  "s_sub_u32 s88, 104000, s88\n" \
  "s_sub_u32 s90, s58, s90\n" \
  "s_sub_u32 s42, s40, s42\n" \
  "s_lshl_b32 s40, s40, 4\n" \
  "v_cvt_f32_u32 v245, s90\n" \
  "v_cvt_f32_u32 v246, s42\n" \
  "v_rcp_f32 v245, v245\n" \
  "v_cvt_f32_i32 v247, s88\n" \
  "v_mul_f32 v246, 0x4111eb85, v246\n" \
  "v_mul_f32 v245, v245, v246\n" \
  "v_mul_f32 v245, v245, v247\n" \
  "v_cvt_i32_f32 v245, v245\n" \
  "v_add_u32 v245, s40, v245\n" \
  "v_or_b32 v245, 1, v245\n" \
  "s_mov_b64 exec, 1\n" \
  "global_store_dword v244, v245, s[48:49] offset:2048 sc1\n" \
  "s_mov_b64 exec, -1\n" \
  "s_branch LT_s\n" \
  "L11_:\n" \
  "s_cmp_lg_u32 s35, 7\n" \
  "s_cbranch_scc1 L12_\n" \
  "s_mov_b32 exec_hi, 0\n" \
  "global_atomic_add v196, v191, s[48:49] offset:3072 sc1\n" \
  "s_mov_b64 exec, -1\n" \
  "s_branch LT_s\n" \
  "L12_:\n" \
  "s_cmp_lg_u32 s35, 23\n" \
  "s_cbranch_scc1 L13_\n" \
  "s_lshl_b32 s35, s30, 5\n" \
  "v_lshrrev_b32 v206, 3, v255\n" \
  "v_and_b32 v208, 7, v255\n" \
  "v_lshl_add_u32 v210, v206, 2, s35\n" \
  "v_lshl_add_u32 v212, v208, 2, s35\n" \
  "global_load_dword v202, v210, s[48:49] offset:2048 sc1\n" \
  "global_load_dword v204, v212, s[48:49] offset:2048 sc1\n" \
  "s_branch LT_s\n" \
  "L13_:\n" \
  "s_cmp_lg_u32 s35, 24\n" \
  "s_cbranch_scc1 L14_\n" \
  "s_mov_b32 s5, 0x10000\n" \
  "LB_z:\n" \
  "v_min_u32 v214, v202, v204\n" \
  "v_cmp_eq_u32 s[58:59], 0, v214\n" \
  "s_sub_u32 s5, s5, 1\n" \
  "s_cbranch_scc1 LB_zx\n" \
  "s_cmp_eq_u64 s[58:59], 0\n" \
  "s_cbranch_scc1 LB_zx\n" \
  "s_cmp_lg_u32 s33, 1021\n" \
  "s_cbranch_scc1 LB_n1\n" \
  "global_load_dwordx4 v[240:243], v238, s[48:49] offset:0 sc1\n" \
  "s_waitcnt vmcnt(0)\n" \
  "global_store_dwordx4 v238, v[240:243], s[48:49] offset:1024 sc1\n" \
  "LB_n1:\n" \
  "s_sleep 4\n" \
  "global_load_dword v202, v210, s[48:49] offset:2048 sc1\n" \
  "global_load_dword v204, v212, s[48:49] offset:2048 sc1\n" \
  "s_waitcnt vmcnt(0)\n" \
  "s_branch LB_z\n" \
  "LB_zx:\n" \
  "v_and_or_b32 v202, v202, -8, v206\n" \
  "v_and_or_b32 v204, v204, -8, v208\n" \
  "v_cmp_lt_u32 s[58:59], v204, v202\n" \
  "v_sub_u32 v202, v204, v202\n" \
  "s_lshl_b32 s35, s29, 3\n" \
  "s_lshr_b64 s[40:41], s[58:59], s35\n" \
  "s_and_b32 s40, s40, 0xff\n" \
  "s_bcnt1_i32_b32 s40, s40\n" \
  "s_sub_u32 s40, 7, s40\n" \
  "s_mov_b32 s36, s29\n" \
  "s_bfe_u32 s41, s58, 0x80000\n" \
  "s_bcnt1_i32_b32 s41, s41\n" \
  "s_cmp_eq_u32 s41, s40\n" \
  "s_cselect_b32 s36, 0, s36\n" \
  "s_bfe_u32 s41, s58, 0x80008\n" \
  "s_bcnt1_i32_b32 s41, s41\n" \
  "s_cmp_eq_u32 s41, s40\n" \
  "s_cselect_b32 s36, 1, s36\n" \
  "s_bfe_u32 s41, s58, 0x80010\n" \
  "s_bcnt1_i32_b32 s41, s41\n" \
  "s_cmp_eq_u32 s41, s40\n" \
  "s_cselect_b32 s36, 2, s36\n" \
  "s_bfe_u32 s41, s58, 0x80018\n" \
  "s_bcnt1_i32_b32 s41, s41\n" \
  "s_cmp_eq_u32 s41, s40\n" \
  "s_cselect_b32 s36, 3, s36\n" \
  "s_bfe_u32 s41, s59, 0x80000\n" \
  "s_bcnt1_i32_b32 s41, s41\n" \
  "s_cmp_eq_u32 s41, s40\n" \
  "s_cselect_b32 s36, 4, s36\n" \
  "s_bfe_u32 s41, s59, 0x80008\n" \
  "s_bcnt1_i32_b32 s41, s41\n" \
  "s_cmp_eq_u32 s41, s40\n" \
  "s_cselect_b32 s36, 5, s36\n" \
  "s_bfe_u32 s41, s59, 0x80010\n" \
  "s_bcnt1_i32_b32 s41, s41\n" \
  "s_cmp_eq_u32 s41, s40\n" \
  "s_cselect_b32 s36, 6, s36\n" \
  "s_bfe_u32 s41, s59, 0x80018\n" \
  "s_bcnt1_i32_b32 s41, s41\n" \
  "s_cmp_eq_u32 s41, s40\n" \
  "s_cselect_b32 s36, 7, s36\n" \
  "s_add_u32 s35, s35, s36\n" \
  "v_readlane_b32 s41, v202, s35\n" \
  "s_sub_u32 s35, 0, s41\n" \
  "s_cmp_lt_u32 s40, 4\n" \
  "s_cselect_b32 s41, s35, s41\n" \
  "s_min_u32 s41, s41, 0xffff\n" \
  "s_mul_i32 s41, s41, 41\n" \
  "s_add_u32 s41, s41, 28672\n" \
  "s_lshr_b32 s41, s41, 16\n" \
  "s_min_u32 s41, s41, 4\n" \
  "s_sub_u32 s35, 0, s41\n" \
  "s_cmp_lt_u32 s40, 4\n" \
  "s_cselect_b32 s35, s35, s41\n" \
  "s_lshl4_add_u32 s35, s36, s35\n" \
  "s_add_u32 s35, s35, 132\n" \
  "v_mov_b32 v204, s35\n" \
  "ds_write_b32 v190, v204 offset:4\n" \
  "s_branch LT_s\n" \
  "L14_:\n" \
  "s_cmp_lg_u32 s35, 25\n" \
  "s_cbranch_scc1 L15_\n" \
  "s_mov_b64 exec, 0xff\n" \
  "global_load_dword v200, v198, s[48:49] offset:3072 sc1\n" \
  "s_mov_b64 exec, -1\n" \
  "s_branch LT_s\n" \
  "L15_:\n" \
  "s_cmp_lg_u32 s35, 26\n" \
  "s_cbranch_scc1 L16_\n" \
  "s_mov_b64 s[58:59], vcc\n" \
  "s_mov_b32 s5, 0x10000\n" \
  "LB_p:\n" \
  "s_mov_b64 exec, 0xff\n" \
  "v_cmp_le_u32 vcc, 32, v200\n" \
  "s_mov_b64 exec, -1\n" \
  "s_sub_u32 s5, s5, 1\n" \
  "s_cselect_b32 s35, 0xff, vcc_lo\n" \
  "s_cmp_eq_u32 s35, 0xff\n" \
  "s_cbranch_scc1 LB_p_x\n" \
  "s_cmp_lg_u32 s33, 1021\n" \
  "s_cbranch_scc1 LB_n2\n" \
  "global_load_dwordx4 v[240:243], v238, s[48:49] offset:0 sc1\n" \
  "s_waitcnt vmcnt(0)\n" \
  "global_store_dwordx4 v238, v[240:243], s[48:49] offset:1024 sc1\n" \
  "LB_n2:\n" \
  "s_sleep 2\n" \
  "s_mov_b64 exec, 0xff\n" \
  "global_load_dword v200, v198, s[48:49] offset:3072 sc1\n" \
  "s_mov_b64 exec, -1\n" \
  "s_waitcnt vmcnt(0)\n" \
  "s_branch LB_p\n" \
  "LB_p_x:\n" \
  "s_mov_b64 vcc, s[58:59]\n" \
  "s_branch LT_s\n" \
  "L16_:\n" \
  "s_branch LT_s\n" \
  "LT_m:\n" \
  "ds_write_b16_d16_hi v195, v196 offset:0\n" \
  "ds_write_b16_d16_hi v195, v198 offset:16\n" \
  "ds_write_b16_d16_hi v195, v200 offset:32\n" \
  "ds_write_b16_d16_hi v235, v202 offset:64\n" \
  "ds_write_b16_d16_hi v235, v204 offset:80\n" \
  "ds_write_b16_d16_hi v235, v206 offset:96\n" \
  "ds_write_b16_d16_hi v235, v208 offset:112\n" \
  "ds_write_b16_d16_hi v235, v210 offset:128\n" \
  "ds_write_b16_d16_hi v235, v212 offset:144\n" \
  "ds_write_b16_d16_hi v235, v214 offset:160\n" \
  "ds_write_b16_d16_hi v235, v216 offset:176\n" \
  "ds_write_b16_d16_hi v195, v218 offset:48\n" \
  "ds_write_b16_d16_hi v195, v220 offset:192\n" \
  "ds_write_b16_d16_hi v195, v222 offset:208\n" \
  "ds_write_b16_d16_hi v195, v224 offset:224\n" \
  "ds_write_b16_d16_hi v195, v226 offset:240\n" \
  "ds_read_b128 v[244:247], v254 offset:0\n" \
  "ds_read_b128 v[248:251], v254 offset:1088\n" \
  "s_lshr_b32 s35, s7, 5\n" \
  "s_mul_i32 s35, s35, 819200\n" \
  "s_and_b32 s36, s7, 31\n" \
  "s_lshl_b32 s36, s36, 10\n" \
  "s_add_u32 s35, s35, s36\n" \
  "s_lshl_b32 s36, s4, 9\n" \
  "s_add_u32 s35, s35, s36\n" \
  "s_add_u32 s35, s35, 102400000\n" \
  "s_add_u32 s58, s22, s35\n" \
  "s_addc_u32 s59, s23, 0\n" \
  "s_waitcnt lgkmcnt(0)\n" \
  "s_cmp_ge_u32 s4, 768\n" \
  "s_cbranch_scc1 L17_\n" \
  "global_store_dwordx4 v238, v[244:247], s[58:59] sc1\n" \
  "s_cmp_ge_u32 s4, 768\n" \
  "s_cbranch_scc1 L17_\n" \
  "s_add_u32 s58, s58, 32768\n" \
  "s_addc_u32 s59, s59, 0\n" \
  "global_store_dwordx4 v238, v[248:251], s[58:59] sc1\n" \
  "L17_:\n" \
  "v_mov_b32 v234, 0\n" \
  "v_mov_b32 v236, v237\n" \
  "s_mov_b32 s38, 1\n" \
  "s_lshl_b32 s35, s29, 8\n" \
  "v_lshl_add_u32 v196, v255, 11, s35\n" \
  "s_lshl_b32 s35, s30, 11\n" \
  "v_lshl_add_u32 v198, v255, 8, s35\n" \
  "s_branch LT\n" \
  "LB_t_h:\n" \
  "s_waitcnt lgkmcnt(12)\n" \
  "v_mfma_f32_16x16x32_bf16 v[160:163], v[128:131], v[64:67], 0\n" \
  "v_mfma_f32_16x16x32_bf16 v[160:163], v[132:135], v[68:71], v[160:163]\n" \
  "s_waitcnt lgkmcnt(8)\n" \
  "v_mfma_f32_16x16x32_bf16 v[160:163], v[136:139], v[72:75], v[160:163]\n" \
  "v_mfma_f32_16x16x32_bf16 v[160:163], v[140:143], v[76:79], v[160:163]\n" \
  "s_waitcnt lgkmcnt(0)\n" \
  "v_mfma_f32_16x16x32_bf16 v[164:167], v[156:159], v[76:79], 0\n" \
  "v_mfma_f32_16x16x32_bf16 v[164:167], v[152:155], v[72:75], v[164:167]\n" \
  "v_mfma_f32_16x16x32_bf16 v[164:167], v[148:151], v[68:71], v[164:167]\n" \
  "v_mfma_f32_16x16x32_bf16 v[164:167], v[144:147], v[64:67], v[164:167]\n" \
  "v_mfma_f32_16x16x32_bf16 v[172:175], v[144:147], v[80:83], 0\n" \
  "v_mfma_f32_16x16x32_bf16 v[172:175], v[148:151], v[84:87], v[172:175]\n" \
  "v_mfma_f32_16x16x32_bf16 v[172:175], v[152:155], v[88:91], v[172:175]\n" \
  "v_cvt_pk_bf16_f32 v176, v160, v161\n" \
  "v_cvt_pk_bf16_f32 v177, v162, v163\n" \
  "v_mfma_f32_16x16x32_bf16 v[172:175], v[156:159], v[92:95], v[172:175]\n" \
  "v_mfma_f32_16x16x32_bf16 v[168:171], v[140:143], v[92:95], 0\n" \
  "v_mfma_f32_16x16x32_bf16 v[168:171], v[136:139], v[88:91], v[168:171]\n" \
  "v_cvt_pk_bf16_f32 v178, v164, v165\n" \
  "v_cvt_pk_bf16_f32 v179, v166, v167\n" \
  "v_mfma_f32_16x16x32_bf16 v[168:171], v[132:135], v[84:87], v[168:171]\n" \
  "v_mfma_f32_16x16x32_bf16 v[168:171], v[128:131], v[80:83], v[168:171]\n" \
  "s_waitcnt vmcnt(63)\n" \
  "global_store_dwordx4 v186, v[176:179], s[46:47] offset:1024 sc1\n" \
  "v_mfma_f32_16x16x32_bf16 v[160:163], v[128:131], v[96:99], 0\n" \
  "v_cvt_pk_bf16_f32 v182, v172, v173\n" \
  "v_cvt_pk_bf16_f32 v183, v174, v175\n" \
  "v_mfma_f32_16x16x32_bf16 v[160:163], v[132:135], v[100:103], v[160:163]\n" \
  "v_mfma_f32_16x16x32_bf16 v[160:163], v[136:139], v[104:107], v[160:163]\n" \
  "v_mfma_f32_16x16x32_bf16 v[160:163], v[140:143], v[108:111], v[160:163]\n" \
  "v_cvt_pk_bf16_f32 v180, v168, v169\n" \
  "v_cvt_pk_bf16_f32 v181, v170, v171\n" \
  "v_mfma_f32_16x16x32_bf16 v[164:167], v[156:159], v[108:111], 0\n" \
  "v_mfma_f32_16x16x32_bf16 v[164:167], v[152:155], v[104:107], v[164:167]\n" \
  "v_mfma_f32_16x16x32_bf16 v[164:167], v[148:151], v[100:103], v[164:167]\n" \
  "v_mfma_f32_16x16x32_bf16 v[164:167], v[144:147], v[96:99], v[164:167]\n" \
  "s_waitcnt vmcnt(63)\n" \
  "global_store_dwordx4 v186, v[180:183], s[46:47] offset:1280 sc1\n" \
  "v_mfma_f32_16x16x32_bf16 v[168:171], v[128:131], v[112:115], 0\n" \
  "v_cvt_pk_bf16_f32 v176, v160, v161\n" \
  "v_cvt_pk_bf16_f32 v177, v162, v163\n" \
  "v_mfma_f32_16x16x32_bf16 v[168:171], v[132:135], v[116:119], v[168:171]\n" \
  "v_mfma_f32_16x16x32_bf16 v[168:171], v[136:139], v[120:123], v[168:171]\n" \
  "v_mfma_f32_16x16x32_bf16 v[168:171], v[140:143], v[124:127], v[168:171]\n" \
  "v_cvt_pk_bf16_f32 v178, v164, v165\n" \
  "v_cvt_pk_bf16_f32 v179, v166, v167\n" \
  "v_mfma_f32_16x16x32_bf16 v[172:175], v[156:159], v[124:127], 0\n" \
  "v_mfma_f32_16x16x32_bf16 v[172:175], v[152:155], v[120:123], v[172:175]\n" \
  "v_mfma_f32_16x16x32_bf16 v[172:175], v[148:151], v[116:119], v[172:175]\n" \
  "v_mfma_f32_16x16x32_bf16 v[172:175], v[144:147], v[112:115], v[172:175]\n" \
  "s_waitcnt vmcnt(63)\n" \
  "global_store_dwordx4 v187, v[176:179], s[46:47] offset:0 sc1\n" \
  "v_cvt_pk_bf16_f32 v180, v168, v169\n" \
  "v_cvt_pk_bf16_f32 v181, v170, v171\n" \
  "s_nop 3\n" \
  "v_cvt_pk_bf16_f32 v182, v172, v173\n" \
  "v_cvt_pk_bf16_f32 v183, v174, v175\n" \
  "s_mov_b64 exec, s[84:85]\n" \
  "s_cbranch_execnz L19_s\n" \
  "s_waitcnt vmcnt(3)\n" \
  "s_branch L19_\n" \
  "L19_s:\n" \
  "s_waitcnt vmcnt(63)\n" \
  "global_store_dwordx4 v188, v[180:183], s[46:47] offset:0 sc1\n" \
  "L19_:\n" \
  "s_mov_b64 exec, -1\n" \
  "LB_t_e:\n" \
  "s_setprio 2\n" \
  "v_mov_b64 v[240:241], -1\n" \
  "v_mov_b64 v[242:243], -1\n" \
  "s_lshl2_add_u32 s35, s33, s34\n" \
  "s_lshl2_add_u32 s35, s35, s28\n" \
  "s_lshl_b32 s35, s35, 12\n" \
  "s_add_u32 s35, s35, 105676800\n" \
  "s_add_u32 s40, s22, s35\n" \
  "s_addc_u32 s41, s23, 0\n" \
  "v_mbcnt_lo_u32_b32 v189, -1, 0\n" \
  "v_mbcnt_hi_u32_b32 v189, -1, v189\n" \
  "v_lshlrev_b32 v188, 4, v189\n" \
  "s_lshl_b32 s35, s28, 12\n" \
  "v_add_u32 v128, s35, v188\n" \
  "v_mov_b32 v199, 1\n" \
  "v_lshrrev_b32 v180, 1, v189\n" \
  "v_xor_b32 v180, v180, v189\n" \
  "v_and_b32 v180, 8, v180\n" \
  "v_bfe_u32 v181, v189, 2, 1\n" \
  "v_lshlrev_b32_e64 v181, v181, 5\n" \
  "v_lshlrev_b32 v180, v180, v181\n" \
  "v_bfe_u32 v181, v189, 2, 2\n" \
  "v_lshlrev_b32_e64 v181, v181, 1\n" \
  "v_cmp_gt_u32 vcc, 32, v189\n" \
  "v_mov_b32 v191, 0x3f80\n" \
  "v_add_u32 v190, 32000, v188\n" \
  "v_cndmask_b32 v192, v181, v180, vcc\n" \
  "v_bfe_i32 v180, v192, 0, 1\n" \
  "v_and_b32 v180, v180, v191\n" \
  "v_bfe_i32 v181, v192, 1, 1\n" \
  "v_and_b32 v181, v181, v191\n" \
  "v_bfe_i32 v182, v192, 2, 1\n" \
  "v_and_b32 v182, v182, v191\n" \
  "v_bfe_i32 v183, v192, 3, 1\n" \
  "v_and_b32 v183, v183, v191\n" \
  "v_and_b32 v192, 3, v189\n" \
  "v_mul_u32_u24 v192, 0x5555, v192\n" \
  "v_add_u32 v189, 16000, v188\n" \
  "v_add_u32 v191, 48000, v188\n" \
  "s_waitcnt vmcnt(4)\n" \
  "v_lshlrev_b32 v184, 16, v204\n" \
  "v_and_b32 v185, 0xffff0000, v204\n" \
  "v_lshlrev_b32 v186, 16, v205\n" \
  "v_and_b32 v187, 0xffff0000, v205\n" \
  "global_store_dwordx4 v202, v[240:243], s[40:41] offset:0 sc1\n" \
  "global_store_dwordx4 v202, v[240:243], s[40:41] offset:1024 sc1\n" \
  "global_store_dwordx4 v202, v[240:243], s[40:41] offset:2048 sc1\n" \
  "global_store_dwordx4 v202, v[240:243], s[40:41] offset:3072 sc1\n" \
  "s_load_dwordx2 s[24:25], s[0:1], 0x40\n" \
  "s_setprio 0\n" \
  "s_barrier\n" \
  "s_bitcmp1_b32 s30, 2\n" \
  "s_cbranch_scc1 LB_w_h\n" \
  "ds_write_b128 v128, v[64:67] offset:0\n" \
  "ds_write_b128 v128, v[68:71] offset:1024\n" \
  "ds_write_b128 v128, v[72:75] offset:2048\n" \
  "ds_write_b128 v128, v[76:79] offset:3072\n" \
  "ds_write_b128 v128, v[80:83] offset:16384\n" \
  "ds_write_b128 v128, v[84:87] offset:17408\n" \
  "ds_write_b128 v128, v[88:91] offset:18432\n" \
  "ds_write_b128 v128, v[92:95] offset:19456\n" \
  "ds_write_b128 v128, v[96:99] offset:32768\n" \
  "ds_write_b128 v128, v[100:103] offset:33792\n" \
  "ds_write_b128 v128, v[104:107] offset:34816\n" \
  "ds_write_b128 v128, v[108:111] offset:35840\n" \
  "ds_write_b128 v128, v[112:115] offset:49152\n" \
  "ds_write_b128 v128, v[116:119] offset:50176\n" \
  "ds_write_b128 v128, v[120:123] offset:51200\n" \
  "ds_write_b128 v128, v[124:127] offset:52224\n" \
  "s_add_u32 s35, s59, 64000\n" \
  "s_add_u32 s54, s54, s35\n" \
  "s_addc_u32 s55, s55, 0\n" \
  "s_add_u32 s58, s59, 0x10000\n" \
  "s_mov_b32 m0, s58\n" \
  "s_add_u32 s35, s58, 0x8000\n" \
  "global_load_lds_dwordx4 v188, s[54:55] sc1\n" \
  "global_load_lds_dwordx4 v188, s[54:55] offset:1024 sc1\n" \
  "s_add_u32 m0, s58, 0x4000\n" \
  "s_cmp_lt_u32 s35, 0x28000\n" \
  "global_load_lds_dwordx4 v189, s[54:55] sc1\n" \
  "global_load_lds_dwordx4 v189, s[54:55] offset:1024 sc1\n" \
  "s_cselect_b32 s58, s35, s59\n" \
  "s_add_u32 s54, s54, 32000\n" \
  "s_addc_u32 s55, s55, 0\n" \
  "s_branch LB_w_e\n" \
  "LB_w_h:\n" \
  "ds_write_b128 v128, v[0:3] offset:0\n" \
  "ds_write_b128 v128, v[4:7] offset:1024\n" \
  "ds_write_b128 v128, v[8:11] offset:2048\n" \
  "ds_write_b128 v128, v[12:15] offset:3072\n" \
  "ds_write_b128 v128, v[16:19] offset:16384\n" \
  "ds_write_b128 v128, v[20:23] offset:17408\n" \
  "ds_write_b128 v128, v[24:27] offset:18432\n" \
  "ds_write_b128 v128, v[28:31] offset:19456\n" \
  "ds_write_b128 v128, v[32:35] offset:32768\n" \
  "ds_write_b128 v128, v[36:39] offset:33792\n" \
  "ds_write_b128 v128, v[40:43] offset:34816\n" \
  "ds_write_b128 v128, v[44:47] offset:35840\n" \
  "ds_write_b128 v128, v[48:51] offset:49152\n" \
  "ds_write_b128 v128, v[52:55] offset:50176\n" \
  "ds_write_b128 v128, v[56:59] offset:51200\n" \
  "ds_write_b128 v128, v[60:63] offset:52224\n" \
  "s_add_u32 s35, s59, 64000\n" \
  "s_add_u32 s54, s54, s35\n" \
  "s_addc_u32 s55, s55, 0\n" \
  "s_add_u32 s58, s59, 0x10000\n" \
  "s_mov_b32 m0, s58\n" \
  "s_add_u32 s35, s58, 0x8000\n" \
  "global_load_lds_dwordx4 v188, s[54:55] sc1\n" \
  "global_load_lds_dwordx4 v188, s[54:55] offset:1024 sc1\n" \
  "s_add_u32 m0, s58, 0x4000\n" \
  "s_cmp_lt_u32 s35, 0x28000\n" \
  "global_load_lds_dwordx4 v189, s[54:55] sc1\n" \
  "global_load_lds_dwordx4 v189, s[54:55] offset:1024 sc1\n" \
  "s_cselect_b32 s58, s35, s59\n" \
  "s_add_u32 s54, s54, 32000\n" \
  "s_addc_u32 s55, s55, 0\n" \
  "LB_w_e:\n" \
  "s_waitcnt lgkmcnt(0)\n" \
  "s_mov_b32 s51, 7\n" \
  "s_setprio 0\n" \
  "s_barrier\n" \
  "LG_start:\n" \
  "s_sub_u32 s35, 14960, s59\n" \
  "v_min_u32 v156, s35, v188\n" \
  "v_add_u32 v157, 16000, v156\n" \
  "v_add_u32 v158, 32000, v156\n" \
  "v_add_u32 v159, 48000, v156\n" \
  "v_lshlrev_b32_sdwa v64, 4, v208 dst_sel:DWORD dst_unused:UNUSED_PAD src0_sel:DWORD src1_sel:WORD_0\n" \
  "ds_read_b128 v[68:71], v64\n" \
  "v_mov_b64 v[0:1], v[184:185]\n" \
  "v_lshlrev_b32_sdwa v65, 4, v208 dst_sel:DWORD dst_unused:UNUSED_PAD src0_sel:DWORD src1_sel:WORD_1\n" \
  "ds_read_b128 v[72:75], v65\n" \
  "v_mov_b64 v[2:3], v[186:187]\n" \
  "v_lshlrev_b32_sdwa v66, 4, v209 dst_sel:DWORD dst_unused:UNUSED_PAD src0_sel:DWORD src1_sel:WORD_0\n" \
  "ds_read_b128 v[76:79], v66\n" \
  "v_mov_b64 v[4:5], v[184:185]\n" \
  "v_lshlrev_b32_sdwa v67, 4, v209 dst_sel:DWORD dst_unused:UNUSED_PAD src0_sel:DWORD src1_sel:WORD_1\n" \
  "ds_read_b128 v[80:83], v67\n" \
  "v_mov_b64 v[6:7], v[186:187]\n" \
  "s_mov_b32 m0, s58\n" \
  "s_add_u32 s35, s58, 0x8000\n" \
  "global_load_lds_dwordx4 v188, s[54:55] sc1\n" \
  "global_load_lds_dwordx4 v156, s[54:55] offset:1024 sc1\n" \
  "v_lshlrev_b32_sdwa v64, 4, v210 dst_sel:DWORD dst_unused:UNUSED_PAD src0_sel:DWORD src1_sel:WORD_0\n" \
  "ds_read_b128 v[84:87], v64\n" \
  "v_mov_b64 v[8:9], v[184:185]\n" \
  "v_lshlrev_b32_sdwa v65, 4, v210 dst_sel:DWORD dst_unused:UNUSED_PAD src0_sel:DWORD src1_sel:WORD_1\n" \
  "ds_read_b128 v[88:91], v65\n" \
  "v_mov_b64 v[10:11], v[186:187]\n" \
  "v_lshlrev_b32_sdwa v66, 4, v211 dst_sel:DWORD dst_unused:UNUSED_PAD src0_sel:DWORD src1_sel:WORD_0\n" \
  "ds_read_b128 v[92:95], v66\n" \
  "v_mov_b64 v[12:13], v[184:185]\n" \
  "v_lshlrev_b32_sdwa v67, 4, v211 dst_sel:DWORD dst_unused:UNUSED_PAD src0_sel:DWORD src1_sel:WORD_1\n" \
  "ds_read_b128 v[96:99], v67\n" \
  "v_mov_b64 v[14:15], v[186:187]\n" \
  "s_add_u32 m0, s58, 0x4000\n" \
  "s_cmp_lt_u32 s35, 0x28000\n" \
  "global_load_lds_dwordx4 v189, s[54:55] sc1\n" \
  "global_load_lds_dwordx4 v157, s[54:55] offset:1024 sc1\n" \
  "s_cselect_b32 s58, s35, s59\n" \
  "v_lshlrev_b32_sdwa v64, 4, v212 dst_sel:DWORD dst_unused:UNUSED_PAD src0_sel:DWORD src1_sel:WORD_0\n" \
  "ds_read_b128 v[100:103], v64\n" \
  "v_mov_b64 v[16:17], v[184:185]\n" \
  "v_lshlrev_b32_sdwa v65, 4, v212 dst_sel:DWORD dst_unused:UNUSED_PAD src0_sel:DWORD src1_sel:WORD_1\n" \
  "ds_read_b128 v[104:107], v65\n" \
  "v_mov_b64 v[18:19], v[186:187]\n" \
  "v_lshlrev_b32_sdwa v66, 4, v213 dst_sel:DWORD dst_unused:UNUSED_PAD src0_sel:DWORD src1_sel:WORD_0\n" \
  "ds_read_b128 v[108:111], v66\n" \
  "v_mov_b64 v[20:21], v[184:185]\n" \
  "v_lshlrev_b32_sdwa v67, 4, v213 dst_sel:DWORD dst_unused:UNUSED_PAD src0_sel:DWORD src1_sel:WORD_1\n" \
  "ds_read_b128 v[112:115], v67\n" \
  "v_mov_b64 v[22:23], v[186:187]\n" \
  "s_waitcnt lgkmcnt(10)\n" \
  "v_smfmac_f32_16x16x64_bf16 v[0:3], v[180:183], v[68:75], v192\n" \
  "v_lshlrev_b32_sdwa v64, 4, v214 dst_sel:DWORD dst_unused:UNUSED_PAD src0_sel:DWORD src1_sel:WORD_0\n" \
  "ds_read_b128 v[68:71], v64\n" \
  "v_mov_b64 v[24:25], v[184:185]\n" \
  "v_lshlrev_b32_sdwa v65, 4, v214 dst_sel:DWORD dst_unused:UNUSED_PAD src0_sel:DWORD src1_sel:WORD_1\n" \
  "ds_read_b128 v[72:75], v65\n" \
  "v_mov_b64 v[26:27], v[186:187]\n" \
  "s_mov_b32 m0, s58\n" \
  "s_add_u32 s35, s58, 0x8000\n" \
  "global_load_lds_dwordx4 v190, s[54:55] sc1\n" \
  "global_load_lds_dwordx4 v158, s[54:55] offset:1024 sc1\n" \
  "s_waitcnt lgkmcnt(10)\n" \
  "v_smfmac_f32_16x16x64_bf16 v[4:7], v[180:183], v[76:83], v192\n" \
  "v_lshlrev_b32_sdwa v66, 4, v215 dst_sel:DWORD dst_unused:UNUSED_PAD src0_sel:DWORD src1_sel:WORD_0\n" \
  "ds_read_b128 v[76:79], v66\n" \
  "v_mov_b64 v[28:29], v[184:185]\n" \
  "v_lshlrev_b32_sdwa v67, 4, v215 dst_sel:DWORD dst_unused:UNUSED_PAD src0_sel:DWORD src1_sel:WORD_1\n" \
  "ds_read_b128 v[80:83], v67\n" \
  "v_mov_b64 v[30:31], v[186:187]\n" \
  "s_waitcnt lgkmcnt(10)\n" \
  "v_smfmac_f32_16x16x64_bf16 v[8:11], v[180:183], v[84:91], v192\n" \
  "v_lshlrev_b32_sdwa v64, 4, v216 dst_sel:DWORD dst_unused:UNUSED_PAD src0_sel:DWORD src1_sel:WORD_0\n" \
  "ds_read_b128 v[84:87], v64\n" \
  "v_mov_b64 v[32:33], v[184:185]\n" \
  "v_lshlrev_b32_sdwa v65, 4, v216 dst_sel:DWORD dst_unused:UNUSED_PAD src0_sel:DWORD src1_sel:WORD_1\n" \
  "ds_read_b128 v[88:91], v65\n" \
  "v_mov_b64 v[34:35], v[186:187]\n" \
  "s_add_u32 m0, s58, 0x4000\n" \
  "s_cmp_lt_u32 s35, 0x28000\n" \
  "global_load_lds_dwordx4 v191, s[54:55] sc1\n" \
  "global_load_lds_dwordx4 v159, s[54:55] offset:1024 sc1\n" \
  "s_cselect_b32 s58, s35, s59\n" \
  "s_waitcnt lgkmcnt(10)\n" \
  "v_smfmac_f32_16x16x64_bf16 v[12:15], v[180:183], v[92:99], v192\n" \
  "v_lshlrev_b32_sdwa v66, 4, v217 dst_sel:DWORD dst_unused:UNUSED_PAD src0_sel:DWORD src1_sel:WORD_0\n" \
  "ds_read_b128 v[92:95], v66\n" \
  "v_mov_b64 v[36:37], v[184:185]\n" \
  "v_lshlrev_b32_sdwa v67, 4, v217 dst_sel:DWORD dst_unused:UNUSED_PAD src0_sel:DWORD src1_sel:WORD_1\n" \
  "ds_read_b128 v[96:99], v67\n" \
  "v_mov_b64 v[38:39], v[186:187]\n" \
  "s_waitcnt lgkmcnt(10)\n" \
  "v_smfmac_f32_16x16x64_bf16 v[16:19], v[180:183], v[100:107], v192\n" \
  "v_lshlrev_b32_sdwa v64, 4, v218 dst_sel:DWORD dst_unused:UNUSED_PAD src0_sel:DWORD src1_sel:WORD_0\n" \
  "ds_read_b128 v[100:103], v64\n" \
  "v_mov_b64 v[40:41], v[184:185]\n" \
  "v_lshlrev_b32_sdwa v65, 4, v218 dst_sel:DWORD dst_unused:UNUSED_PAD src0_sel:DWORD src1_sel:WORD_1\n" \
  "ds_read_b128 v[104:107], v65\n" \
  "v_mov_b64 v[42:43], v[186:187]\n" \
  "global_load_dwordx4 v[240:243], v188, s[56:57] sc1\n" \
  "global_load_dwordx4 v[244:247], v188, s[56:57] offset:1024 sc1\n" \
  "s_waitcnt lgkmcnt(10)\n" \
  "v_smfmac_f32_16x16x64_bf16 v[20:23], v[180:183], v[108:115], v192\n" \
  "v_lshlrev_b32_sdwa v66, 4, v219 dst_sel:DWORD dst_unused:UNUSED_PAD src0_sel:DWORD src1_sel:WORD_0\n" \
  "ds_read_b128 v[108:111], v66\n" \
  "v_mov_b64 v[44:45], v[184:185]\n" \
  "v_lshlrev_b32_sdwa v67, 4, v219 dst_sel:DWORD dst_unused:UNUSED_PAD src0_sel:DWORD src1_sel:WORD_1\n" \
  "ds_read_b128 v[112:115], v67\n" \
  "v_mov_b64 v[46:47], v[186:187]\n" \
  "s_waitcnt lgkmcnt(10)\n" \
  "v_smfmac_f32_16x16x64_bf16 v[24:27], v[180:183], v[68:75], v192\n" \
  "v_lshlrev_b32_sdwa v64, 4, v220 dst_sel:DWORD dst_unused:UNUSED_PAD src0_sel:DWORD src1_sel:WORD_0\n" \
  "ds_read_b128 v[68:71], v64\n" \
  "v_mov_b64 v[48:49], v[184:185]\n" \
  "v_lshlrev_b32_sdwa v65, 4, v220 dst_sel:DWORD dst_unused:UNUSED_PAD src0_sel:DWORD src1_sel:WORD_1\n" \
  "ds_read_b128 v[72:75], v65\n" \
  "v_mov_b64 v[50:51], v[186:187]\n" \
  "global_load_dwordx4 v[248:251], v188, s[56:57] offset:2048 sc1\n" \
  "global_load_dwordx4 v[252:255], v188, s[56:57] offset:3072 sc1\n" \
  "s_waitcnt lgkmcnt(10)\n" \
  "v_smfmac_f32_16x16x64_bf16 v[28:31], v[180:183], v[76:83], v192\n" \
  "v_lshlrev_b32_sdwa v66, 4, v221 dst_sel:DWORD dst_unused:UNUSED_PAD src0_sel:DWORD src1_sel:WORD_0\n" \
  "ds_read_b128 v[76:79], v66\n" \
  "v_mov_b64 v[52:53], v[184:185]\n" \
  "v_lshlrev_b32_sdwa v67, 4, v221 dst_sel:DWORD dst_unused:UNUSED_PAD src0_sel:DWORD src1_sel:WORD_1\n" \
  "ds_read_b128 v[80:83], v67\n" \
  "v_mov_b64 v[54:55], v[186:187]\n" \
  "s_waitcnt lgkmcnt(10)\n" \
  "v_smfmac_f32_16x16x64_bf16 v[32:35], v[180:183], v[84:91], v192\n" \
  "v_lshlrev_b32_sdwa v64, 4, v222 dst_sel:DWORD dst_unused:UNUSED_PAD src0_sel:DWORD src1_sel:WORD_0\n" \
  "ds_read_b128 v[84:87], v64\n" \
  "v_mov_b64 v[56:57], v[184:185]\n" \
  "v_lshlrev_b32_sdwa v65, 4, v222 dst_sel:DWORD dst_unused:UNUSED_PAD src0_sel:DWORD src1_sel:WORD_1\n" \
  "ds_read_b128 v[88:91], v65\n" \
  "v_mov_b64 v[58:59], v[186:187]\n" \
  "s_waitcnt lgkmcnt(10)\n" \
  "v_smfmac_f32_16x16x64_bf16 v[36:39], v[180:183], v[92:99], v192\n" \
  "v_lshlrev_b32_sdwa v66, 4, v223 dst_sel:DWORD dst_unused:UNUSED_PAD src0_sel:DWORD src1_sel:WORD_0\n" \
  "ds_read_b128 v[92:95], v66\n" \
  "v_mov_b64 v[60:61], v[184:185]\n" \
  "v_lshlrev_b32_sdwa v67, 4, v223 dst_sel:DWORD dst_unused:UNUSED_PAD src0_sel:DWORD src1_sel:WORD_1\n" \
  "ds_read_b128 v[96:99], v67\n" \
  "v_mov_b64 v[62:63], v[186:187]\n" \
  "s_add_u32 s56, s56, 32768\n" \
  "s_addc_u32 s57, s57, 0\n" \
  "s_add_u32 s54, s54, 64000\n" \
  "s_addc_u32 s55, s55, 0\n" \
  "s_cmp_eq_u32 s27, 4\n" \
  "s_cselect_b32 s37, 0x90, 0\n" \
  ".p2align 6\n" \
  "LG_loop:\n" \
  "s_bitcmp1_b32 s37, s51\n" \
  "s_waitcnt lgkmcnt(10)\n" \
  "v_smfmac_f32_16x16x64_bf16 v[40:43], v[180:183], v[100:107], v192\n" \
  "s_waitcnt lgkmcnt(8)\n" \
  "v_smfmac_f32_16x16x64_bf16 v[44:47], v[180:183], v[108:115], v192\n" \
  "s_waitcnt vmcnt(8)\n" \
  "s_cbranch_scc1 LG_h\n" \
  "LG_ret:\n" \
  "s_barrier\n" \
  "LG_aft:\n" \
  "v_lshlrev_b32_sdwa v64, 4, v224 dst_sel:DWORD dst_unused:UNUSED_PAD src0_sel:DWORD src1_sel:WORD_0\n" \
  "ds_read_b128 v[100:103], v64\n" \
  "v_lshlrev_b32_sdwa v65, 4, v224 dst_sel:DWORD dst_unused:UNUSED_PAD src0_sel:DWORD src1_sel:WORD_1\n" \
  "ds_read_b128 v[104:107], v65\n" \
  "v_lshlrev_b32_sdwa v66, 4, v225 dst_sel:DWORD dst_unused:UNUSED_PAD src0_sel:DWORD src1_sel:WORD_0\n" \
  "ds_read_b128 v[108:111], v66\n" \
  "v_lshlrev_b32_sdwa v67, 4, v225 dst_sel:DWORD dst_unused:UNUSED_PAD src0_sel:DWORD src1_sel:WORD_1\n" \
  "ds_read_b128 v[112:115], v67\n" \
  "s_waitcnt lgkmcnt(10)\n" \
  "v_smfmac_f32_16x16x64_bf16 v[48:51], v[180:183], v[68:75], v192\n" \
  "s_waitcnt lgkmcnt(8)\n" \
  "v_smfmac_f32_16x16x64_bf16 v[52:55], v[180:183], v[76:83], v192\n" \
  "s_waitcnt lgkmcnt(6)\n" \
  "v_smfmac_f32_16x16x64_bf16 v[56:59], v[180:183], v[84:91], v192\n" \
  "s_waitcnt lgkmcnt(4)\n" \
  "v_smfmac_f32_16x16x64_bf16 v[60:63], v[180:183], v[92:99], v192\n" \
  "s_barrier\n" \
  "s_mov_b32 m0, s58\n" \
  "s_add_u32 s35, s58, 0x8000\n" \
  "global_load_lds_dwordx4 v188, s[54:55] sc1\n" \
  "global_load_lds_dwordx4 v156, s[54:55] offset:1024 sc1\n" \
  "v_lshlrev_b32_sdwa v64, 4, v226 dst_sel:DWORD dst_unused:UNUSED_PAD src0_sel:DWORD src1_sel:WORD_0\n" \
  "ds_read_b128 v[68:71], v64\n" \
  "v_lshlrev_b32_sdwa v65, 4, v226 dst_sel:DWORD dst_unused:UNUSED_PAD src0_sel:DWORD src1_sel:WORD_1\n" \
  "ds_read_b128 v[72:75], v65\n" \
  "v_lshlrev_b32_sdwa v66, 4, v227 dst_sel:DWORD dst_unused:UNUSED_PAD src0_sel:DWORD src1_sel:WORD_0\n" \
  "ds_read_b128 v[76:79], v66\n" \
  "v_lshlrev_b32_sdwa v67, 4, v227 dst_sel:DWORD dst_unused:UNUSED_PAD src0_sel:DWORD src1_sel:WORD_1\n" \
  "ds_read_b128 v[80:83], v67\n" \
  "s_add_u32 m0, s58, 0x4000\n" \
  "s_cmp_lt_u32 s35, 0x28000\n" \
  "global_load_lds_dwordx4 v189, s[54:55] sc1\n" \
  "global_load_lds_dwordx4 v157, s[54:55] offset:1024 sc1\n" \
  "s_cselect_b32 s58, s35, s59\n" \
  "v_lshlrev_b32_sdwa v64, 4, v228 dst_sel:DWORD dst_unused:UNUSED_PAD src0_sel:DWORD src1_sel:WORD_0\n" \
  "ds_read_b128 v[84:87], v64\n" \
  "v_lshlrev_b32_sdwa v65, 4, v228 dst_sel:DWORD dst_unused:UNUSED_PAD src0_sel:DWORD src1_sel:WORD_1\n" \
  "ds_read_b128 v[88:91], v65\n" \
  "v_lshlrev_b32_sdwa v66, 4, v229 dst_sel:DWORD dst_unused:UNUSED_PAD src0_sel:DWORD src1_sel:WORD_0\n" \
  "ds_read_b128 v[92:95], v66\n" \
  "v_lshlrev_b32_sdwa v67, 4, v229 dst_sel:DWORD dst_unused:UNUSED_PAD src0_sel:DWORD src1_sel:WORD_1\n" \
  "ds_read_b128 v[96:99], v67\n" \
  "s_waitcnt lgkmcnt(10)\n" \
  "v_smfmac_f32_16x16x64_bf16 v[0:3], v[180:183], v[100:107], v192\n" \
  "v_lshlrev_b32_sdwa v64, 4, v230 dst_sel:DWORD dst_unused:UNUSED_PAD src0_sel:DWORD src1_sel:WORD_0\n" \
  "ds_read_b128 v[100:103], v64\n" \
  "v_lshlrev_b32_sdwa v65, 4, v230 dst_sel:DWORD dst_unused:UNUSED_PAD src0_sel:DWORD src1_sel:WORD_1\n" \
  "ds_read_b128 v[104:107], v65\n" \
  "s_mov_b32 m0, s58\n" \
  "s_add_u32 s35, s58, 0x8000\n" \
  "global_load_lds_dwordx4 v190, s[54:55] sc1\n" \
  "global_load_lds_dwordx4 v158, s[54:55] offset:1024 sc1\n" \
  "s_waitcnt lgkmcnt(10)\n" \
  "v_smfmac_f32_16x16x64_bf16 v[4:7], v[180:183], v[108:115], v192\n" \
  "v_lshlrev_b32_sdwa v66, 4, v231 dst_sel:DWORD dst_unused:UNUSED_PAD src0_sel:DWORD src1_sel:WORD_0\n" \
  "ds_read_b128 v[108:111], v66\n" \
  "v_lshlrev_b32_sdwa v67, 4, v231 dst_sel:DWORD dst_unused:UNUSED_PAD src0_sel:DWORD src1_sel:WORD_1\n" \
  "ds_read_b128 v[112:115], v67\n" \
  "s_waitcnt lgkmcnt(10)\n" \
  "v_smfmac_f32_16x16x64_bf16 v[8:11], v[180:183], v[68:75], v192\n" \
  "v_lshlrev_b32_sdwa v64, 4, v232 dst_sel:DWORD dst_unused:UNUSED_PAD src0_sel:DWORD src1_sel:WORD_0\n" \
  "ds_read_b128 v[68:71], v64\n" \
  "v_lshlrev_b32_sdwa v65, 4, v232 dst_sel:DWORD dst_unused:UNUSED_PAD src0_sel:DWORD src1_sel:WORD_1\n" \
  "ds_read_b128 v[72:75], v65\n" \
  "s_add_u32 m0, s58, 0x4000\n" \
  "s_cmp_lt_u32 s35, 0x28000\n" \
  "global_load_lds_dwordx4 v191, s[54:55] sc1\n" \
  "global_load_lds_dwordx4 v159, s[54:55] offset:1024 sc1\n" \
  "s_cselect_b32 s58, s35, s59\n" \
  "s_waitcnt lgkmcnt(10)\n" \
  "v_smfmac_f32_16x16x64_bf16 v[12:15], v[180:183], v[76:83], v192\n" \
  "v_lshlrev_b32_sdwa v66, 4, v233 dst_sel:DWORD dst_unused:UNUSED_PAD src0_sel:DWORD src1_sel:WORD_0\n" \
  "ds_read_b128 v[76:79], v66\n" \
  "v_lshlrev_b32_sdwa v67, 4, v233 dst_sel:DWORD dst_unused:UNUSED_PAD src0_sel:DWORD src1_sel:WORD_1\n" \
  "ds_read_b128 v[80:83], v67\n" \
  "s_waitcnt lgkmcnt(10)\n" \
  "v_smfmac_f32_16x16x64_bf16 v[16:19], v[180:183], v[84:91], v192\n" \
  "v_lshlrev_b32_sdwa v64, 4, v234 dst_sel:DWORD dst_unused:UNUSED_PAD src0_sel:DWORD src1_sel:WORD_0\n" \
  "ds_read_b128 v[84:87], v64\n" \
  "v_lshlrev_b32_sdwa v65, 4, v234 dst_sel:DWORD dst_unused:UNUSED_PAD src0_sel:DWORD src1_sel:WORD_1\n" \
  "ds_read_b128 v[88:91], v65\n" \
  "global_load_dwordx4 v[208:211], v188, s[56:57] sc1\n" \
  "global_load_dwordx4 v[212:215], v188, s[56:57] offset:1024 sc1\n" \
  "s_waitcnt lgkmcnt(10)\n" \
  "v_smfmac_f32_16x16x64_bf16 v[20:23], v[180:183], v[92:99], v192\n" \
  "v_lshlrev_b32_sdwa v66, 4, v235 dst_sel:DWORD dst_unused:UNUSED_PAD src0_sel:DWORD src1_sel:WORD_0\n" \
  "ds_read_b128 v[92:95], v66\n" \
  "v_lshlrev_b32_sdwa v67, 4, v235 dst_sel:DWORD dst_unused:UNUSED_PAD src0_sel:DWORD src1_sel:WORD_1\n" \
  "ds_read_b128 v[96:99], v67\n" \
  "s_waitcnt lgkmcnt(10)\n" \
  "v_smfmac_f32_16x16x64_bf16 v[24:27], v[180:183], v[100:107], v192\n" \
  "v_lshlrev_b32_sdwa v64, 4, v236 dst_sel:DWORD dst_unused:UNUSED_PAD src0_sel:DWORD src1_sel:WORD_0\n" \
  "ds_read_b128 v[100:103], v64\n" \
  "v_lshlrev_b32_sdwa v65, 4, v236 dst_sel:DWORD dst_unused:UNUSED_PAD src0_sel:DWORD src1_sel:WORD_1\n" \
  "ds_read_b128 v[104:107], v65\n" \
  "global_load_dwordx4 v[216:219], v188, s[56:57] offset:2048 sc1\n" \
  "global_load_dwordx4 v[220:223], v188, s[56:57] offset:3072 sc1\n" \
  "s_waitcnt lgkmcnt(10)\n" \
  "v_smfmac_f32_16x16x64_bf16 v[28:31], v[180:183], v[108:115], v192\n" \
  "v_lshlrev_b32_sdwa v66, 4, v237 dst_sel:DWORD dst_unused:UNUSED_PAD src0_sel:DWORD src1_sel:WORD_0\n" \
  "ds_read_b128 v[108:111], v66\n" \
  "v_lshlrev_b32_sdwa v67, 4, v237 dst_sel:DWORD dst_unused:UNUSED_PAD src0_sel:DWORD src1_sel:WORD_1\n" \
  "ds_read_b128 v[112:115], v67\n" \
  "s_waitcnt lgkmcnt(10)\n" \
  "v_smfmac_f32_16x16x64_bf16 v[32:35], v[180:183], v[68:75], v192\n" \
  "v_lshlrev_b32_sdwa v64, 4, v238 dst_sel:DWORD dst_unused:UNUSED_PAD src0_sel:DWORD src1_sel:WORD_0\n" \
  "ds_read_b128 v[68:71], v64\n" \
  "v_lshlrev_b32_sdwa v65, 4, v238 dst_sel:DWORD dst_unused:UNUSED_PAD src0_sel:DWORD src1_sel:WORD_1\n" \
  "ds_read_b128 v[72:75], v65\n" \
  "s_waitcnt lgkmcnt(10)\n" \
  "v_smfmac_f32_16x16x64_bf16 v[36:39], v[180:183], v[76:83], v192\n" \
  "v_lshlrev_b32_sdwa v66, 4, v239 dst_sel:DWORD dst_unused:UNUSED_PAD src0_sel:DWORD src1_sel:WORD_0\n" \
  "ds_read_b128 v[76:79], v66\n" \
  "v_lshlrev_b32_sdwa v67, 4, v239 dst_sel:DWORD dst_unused:UNUSED_PAD src0_sel:DWORD src1_sel:WORD_1\n" \
  "ds_read_b128 v[80:83], v67\n" \
  "s_add_u32 s56, s56, 32768\n" \
  "s_addc_u32 s57, s57, 0\n" \
  "s_add_u32 s54, s54, 64000\n" \
  "s_addc_u32 s55, s55, 0\n" \
  "s_waitcnt lgkmcnt(10)\n" \
  "v_smfmac_f32_16x16x64_bf16 v[40:43], v[180:183], v[84:91], v192\n" \
  "s_waitcnt lgkmcnt(8)\n" \
  "v_smfmac_f32_16x16x64_bf16 v[44:47], v[180:183], v[92:99], v192\n" \
  "s_waitcnt vmcnt(8)\n" \
  "s_barrier\n" \
  "v_lshlrev_b32_sdwa v64, 4, v240 dst_sel:DWORD dst_unused:UNUSED_PAD src0_sel:DWORD src1_sel:WORD_0\n" \
  "ds_read_b128 v[84:87], v64\n" \
  "v_lshlrev_b32_sdwa v65, 4, v240 dst_sel:DWORD dst_unused:UNUSED_PAD src0_sel:DWORD src1_sel:WORD_1\n" \
  "ds_read_b128 v[88:91], v65\n" \
  "v_lshlrev_b32_sdwa v66, 4, v241 dst_sel:DWORD dst_unused:UNUSED_PAD src0_sel:DWORD src1_sel:WORD_0\n" \
  "ds_read_b128 v[92:95], v66\n" \
  "v_lshlrev_b32_sdwa v67, 4, v241 dst_sel:DWORD dst_unused:UNUSED_PAD src0_sel:DWORD src1_sel:WORD_1\n" \
  "ds_read_b128 v[96:99], v67\n" \
  "s_waitcnt lgkmcnt(10)\n" \
  "v_smfmac_f32_16x16x64_bf16 v[48:51], v[180:183], v[100:107], v192\n" \
  "s_waitcnt lgkmcnt(8)\n" \
  "v_smfmac_f32_16x16x64_bf16 v[52:55], v[180:183], v[108:115], v192\n" \
  "s_waitcnt lgkmcnt(6)\n" \
  "v_smfmac_f32_16x16x64_bf16 v[56:59], v[180:183], v[68:75], v192\n" \
  "s_waitcnt lgkmcnt(4)\n" \
  "v_smfmac_f32_16x16x64_bf16 v[60:63], v[180:183], v[76:83], v192\n" \
  "s_barrier\n" \
  "s_mov_b32 m0, s58\n" \
  "s_add_u32 s35, s58, 0x8000\n" \
  "global_load_lds_dwordx4 v188, s[54:55] sc1\n" \
  "global_load_lds_dwordx4 v156, s[54:55] offset:1024 sc1\n" \
  "v_lshlrev_b32_sdwa v64, 4, v242 dst_sel:DWORD dst_unused:UNUSED_PAD src0_sel:DWORD src1_sel:WORD_0\n" \
  "ds_read_b128 v[100:103], v64\n" \
  "v_lshlrev_b32_sdwa v65, 4, v242 dst_sel:DWORD dst_unused:UNUSED_PAD src0_sel:DWORD src1_sel:WORD_1\n" \
  "ds_read_b128 v[104:107], v65\n" \
  "v_lshlrev_b32_sdwa v66, 4, v243 dst_sel:DWORD dst_unused:UNUSED_PAD src0_sel:DWORD src1_sel:WORD_0\n" \
  "ds_read_b128 v[108:111], v66\n" \
  "v_lshlrev_b32_sdwa v67, 4, v243 dst_sel:DWORD dst_unused:UNUSED_PAD src0_sel:DWORD src1_sel:WORD_1\n" \
  "ds_read_b128 v[112:115], v67\n" \
  "s_add_u32 m0, s58, 0x4000\n" \
  "s_cmp_lt_u32 s35, 0x28000\n" \
  "global_load_lds_dwordx4 v189, s[54:55] sc1\n" \
  "global_load_lds_dwordx4 v157, s[54:55] offset:1024 sc1\n" \
  "s_cselect_b32 s58, s35, s59\n" \
  "v_lshlrev_b32_sdwa v64, 4, v244 dst_sel:DWORD dst_unused:UNUSED_PAD src0_sel:DWORD src1_sel:WORD_0\n" \
  "ds_read_b128 v[68:71], v64\n" \
  "v_lshlrev_b32_sdwa v65, 4, v244 dst_sel:DWORD dst_unused:UNUSED_PAD src0_sel:DWORD src1_sel:WORD_1\n" \
  "ds_read_b128 v[72:75], v65\n" \
  "v_lshlrev_b32_sdwa v66, 4, v245 dst_sel:DWORD dst_unused:UNUSED_PAD src0_sel:DWORD src1_sel:WORD_0\n" \
  "ds_read_b128 v[76:79], v66\n" \
  "v_lshlrev_b32_sdwa v67, 4, v245 dst_sel:DWORD dst_unused:UNUSED_PAD src0_sel:DWORD src1_sel:WORD_1\n" \
  "ds_read_b128 v[80:83], v67\n" \
  "s_waitcnt lgkmcnt(10)\n" \
  "v_smfmac_f32_16x16x64_bf16 v[0:3], v[180:183], v[84:91], v192\n" \
  "v_lshlrev_b32_sdwa v64, 4, v246 dst_sel:DWORD dst_unused:UNUSED_PAD src0_sel:DWORD src1_sel:WORD_0\n" \
  "ds_read_b128 v[84:87], v64\n" \
  "v_lshlrev_b32_sdwa v65, 4, v246 dst_sel:DWORD dst_unused:UNUSED_PAD src0_sel:DWORD src1_sel:WORD_1\n" \
  "ds_read_b128 v[88:91], v65\n" \
  "s_mov_b32 m0, s58\n" \
  "s_add_u32 s35, s58, 0x8000\n" \
  "global_load_lds_dwordx4 v190, s[54:55] sc1\n" \
  "global_load_lds_dwordx4 v158, s[54:55] offset:1024 sc1\n" \
  "s_waitcnt lgkmcnt(10)\n" \
  "v_smfmac_f32_16x16x64_bf16 v[4:7], v[180:183], v[92:99], v192\n" \
  "v_lshlrev_b32_sdwa v66, 4, v247 dst_sel:DWORD dst_unused:UNUSED_PAD src0_sel:DWORD src1_sel:WORD_0\n" \
  "ds_read_b128 v[92:95], v66\n" \
  "v_lshlrev_b32_sdwa v67, 4, v247 dst_sel:DWORD dst_unused:UNUSED_PAD src0_sel:DWORD src1_sel:WORD_1\n" \
  "ds_read_b128 v[96:99], v67\n" \
  "s_waitcnt lgkmcnt(10)\n" \
  "v_smfmac_f32_16x16x64_bf16 v[8:11], v[180:183], v[100:107], v192\n" \
  "v_lshlrev_b32_sdwa v64, 4, v248 dst_sel:DWORD dst_unused:UNUSED_PAD src0_sel:DWORD src1_sel:WORD_0\n" \
  "ds_read_b128 v[100:103], v64\n" \
  "v_lshlrev_b32_sdwa v65, 4, v248 dst_sel:DWORD dst_unused:UNUSED_PAD src0_sel:DWORD src1_sel:WORD_1\n" \
  "ds_read_b128 v[104:107], v65\n" \
  "s_add_u32 m0, s58, 0x4000\n" \
  "s_cmp_lt_u32 s35, 0x28000\n" \
  "global_load_lds_dwordx4 v191, s[54:55] sc1\n" \
  "global_load_lds_dwordx4 v159, s[54:55] offset:1024 sc1\n" \
  "s_cselect_b32 s58, s35, s59\n" \
  "s_waitcnt lgkmcnt(10)\n" \
  "v_smfmac_f32_16x16x64_bf16 v[12:15], v[180:183], v[108:115], v192\n" \
  "v_lshlrev_b32_sdwa v66, 4, v249 dst_sel:DWORD dst_unused:UNUSED_PAD src0_sel:DWORD src1_sel:WORD_0\n" \
  "ds_read_b128 v[108:111], v66\n" \
  "v_lshlrev_b32_sdwa v67, 4, v249 dst_sel:DWORD dst_unused:UNUSED_PAD src0_sel:DWORD src1_sel:WORD_1\n" \
  "ds_read_b128 v[112:115], v67\n" \
  "s_waitcnt lgkmcnt(10)\n" \
  "v_smfmac_f32_16x16x64_bf16 v[16:19], v[180:183], v[68:75], v192\n" \
  "v_lshlrev_b32_sdwa v64, 4, v250 dst_sel:DWORD dst_unused:UNUSED_PAD src0_sel:DWORD src1_sel:WORD_0\n" \
  "ds_read_b128 v[68:71], v64\n" \
  "v_lshlrev_b32_sdwa v65, 4, v250 dst_sel:DWORD dst_unused:UNUSED_PAD src0_sel:DWORD src1_sel:WORD_1\n" \
  "ds_read_b128 v[72:75], v65\n" \
  "global_load_dwordx4 v[224:227], v188, s[56:57] sc1\n" \
  "global_load_dwordx4 v[228:231], v188, s[56:57] offset:1024 sc1\n" \
  "s_waitcnt lgkmcnt(10)\n" \
  "v_smfmac_f32_16x16x64_bf16 v[20:23], v[180:183], v[76:83], v192\n" \
  "v_lshlrev_b32_sdwa v66, 4, v251 dst_sel:DWORD dst_unused:UNUSED_PAD src0_sel:DWORD src1_sel:WORD_0\n" \
  "ds_read_b128 v[76:79], v66\n" \
  "v_lshlrev_b32_sdwa v67, 4, v251 dst_sel:DWORD dst_unused:UNUSED_PAD src0_sel:DWORD src1_sel:WORD_1\n" \
  "ds_read_b128 v[80:83], v67\n" \
  "s_waitcnt lgkmcnt(10)\n" \
  "v_smfmac_f32_16x16x64_bf16 v[24:27], v[180:183], v[84:91], v192\n" \
  "v_lshlrev_b32_sdwa v64, 4, v252 dst_sel:DWORD dst_unused:UNUSED_PAD src0_sel:DWORD src1_sel:WORD_0\n" \
  "ds_read_b128 v[84:87], v64\n" \
  "v_lshlrev_b32_sdwa v65, 4, v252 dst_sel:DWORD dst_unused:UNUSED_PAD src0_sel:DWORD src1_sel:WORD_1\n" \
  "ds_read_b128 v[88:91], v65\n" \
  "global_load_dwordx4 v[232:235], v188, s[56:57] offset:2048 sc1\n" \
  "global_load_dwordx4 v[236:239], v188, s[56:57] offset:3072 sc1\n" \
  "s_waitcnt lgkmcnt(10)\n" \
  "v_smfmac_f32_16x16x64_bf16 v[28:31], v[180:183], v[92:99], v192\n" \
  "v_lshlrev_b32_sdwa v66, 4, v253 dst_sel:DWORD dst_unused:UNUSED_PAD src0_sel:DWORD src1_sel:WORD_0\n" \
  "ds_read_b128 v[92:95], v66\n" \
  "v_lshlrev_b32_sdwa v67, 4, v253 dst_sel:DWORD dst_unused:UNUSED_PAD src0_sel:DWORD src1_sel:WORD_1\n" \
  "ds_read_b128 v[96:99], v67\n" \
  "s_waitcnt lgkmcnt(10)\n" \
  "v_smfmac_f32_16x16x64_bf16 v[32:35], v[180:183], v[100:107], v192\n" \
  "v_lshlrev_b32_sdwa v64, 4, v254 dst_sel:DWORD dst_unused:UNUSED_PAD src0_sel:DWORD src1_sel:WORD_0\n" \
  "ds_read_b128 v[100:103], v64\n" \
  "v_lshlrev_b32_sdwa v65, 4, v254 dst_sel:DWORD dst_unused:UNUSED_PAD src0_sel:DWORD src1_sel:WORD_1\n" \
  "ds_read_b128 v[104:107], v65\n" \
  "s_waitcnt lgkmcnt(10)\n" \
  "v_smfmac_f32_16x16x64_bf16 v[36:39], v[180:183], v[108:115], v192\n" \
  "v_lshlrev_b32_sdwa v66, 4, v255 dst_sel:DWORD dst_unused:UNUSED_PAD src0_sel:DWORD src1_sel:WORD_0\n" \
  "ds_read_b128 v[108:111], v66\n" \
  "v_lshlrev_b32_sdwa v67, 4, v255 dst_sel:DWORD dst_unused:UNUSED_PAD src0_sel:DWORD src1_sel:WORD_1\n" \
  "ds_read_b128 v[112:115], v67\n" \
  "s_add_u32 s56, s56, 32768\n" \
  "s_addc_u32 s57, s57, 0\n" \
  "s_add_u32 s54, s54, 64000\n" \
  "s_addc_u32 s55, s55, 0\n" \
  "s_lshl3_add_u32 s36, s27, s51\n" \
  "s_cmp_eq_u32 s36, 37\n" \
  "s_waitcnt lgkmcnt(10)\n" \
  "v_smfmac_f32_16x16x64_bf16 v[40:43], v[180:183], v[68:75], v192\n" \
  "s_waitcnt lgkmcnt(8)\n" \
  "v_smfmac_f32_16x16x64_bf16 v[44:47], v[180:183], v[76:83], v192\n" \
  "s_waitcnt vmcnt(8)\n" \
  "s_cbranch_scc1 LG_pol\n" \
  "LG_rt2:\n" \
  "s_barrier\n" \
  "v_lshlrev_b32_sdwa v64, 4, v208 dst_sel:DWORD dst_unused:UNUSED_PAD src0_sel:DWORD src1_sel:WORD_0\n" \
  "ds_read_b128 v[68:71], v64\n" \
  "v_lshlrev_b32_sdwa v65, 4, v208 dst_sel:DWORD dst_unused:UNUSED_PAD src0_sel:DWORD src1_sel:WORD_1\n" \
  "ds_read_b128 v[72:75], v65\n" \
  "v_lshlrev_b32_sdwa v66, 4, v209 dst_sel:DWORD dst_unused:UNUSED_PAD src0_sel:DWORD src1_sel:WORD_0\n" \
  "ds_read_b128 v[76:79], v66\n" \
  "v_lshlrev_b32_sdwa v67, 4, v209 dst_sel:DWORD dst_unused:UNUSED_PAD src0_sel:DWORD src1_sel:WORD_1\n" \
  "ds_read_b128 v[80:83], v67\n" \
  "s_waitcnt lgkmcnt(10)\n" \
  "v_smfmac_f32_16x16x64_bf16 v[48:51], v[180:183], v[84:91], v192\n" \
  "s_waitcnt lgkmcnt(8)\n" \
  "v_smfmac_f32_16x16x64_bf16 v[52:55], v[180:183], v[92:99], v192\n" \
  "s_waitcnt lgkmcnt(6)\n" \
  "v_smfmac_f32_16x16x64_bf16 v[56:59], v[180:183], v[100:107], v192\n" \
  "s_waitcnt lgkmcnt(4)\n" \
  "v_smfmac_f32_16x16x64_bf16 v[60:63], v[180:183], v[108:115], v192\n" \
  "s_barrier\n" \
  "s_mov_b32 m0, s58\n" \
  "s_add_u32 s35, s58, 0x8000\n" \
  "global_load_lds_dwordx4 v188, s[54:55] sc1\n" \
  "global_load_lds_dwordx4 v156, s[54:55] offset:1024 sc1\n" \
  "v_lshlrev_b32_sdwa v64, 4, v210 dst_sel:DWORD dst_unused:UNUSED_PAD src0_sel:DWORD src1_sel:WORD_0\n" \
  "ds_read_b128 v[84:87], v64\n" \
  "v_lshlrev_b32_sdwa v65, 4, v210 dst_sel:DWORD dst_unused:UNUSED_PAD src0_sel:DWORD src1_sel:WORD_1\n" \
  "ds_read_b128 v[88:91], v65\n" \
  "v_lshlrev_b32_sdwa v66, 4, v211 dst_sel:DWORD dst_unused:UNUSED_PAD src0_sel:DWORD src1_sel:WORD_0\n" \
  "ds_read_b128 v[92:95], v66\n" \
  "v_lshlrev_b32_sdwa v67, 4, v211 dst_sel:DWORD dst_unused:UNUSED_PAD src0_sel:DWORD src1_sel:WORD_1\n" \
  "ds_read_b128 v[96:99], v67\n" \
  "s_add_u32 m0, s58, 0x4000\n" \
  "s_cmp_lt_u32 s35, 0x28000\n" \
  "global_load_lds_dwordx4 v189, s[54:55] sc1\n" \
  "global_load_lds_dwordx4 v157, s[54:55] offset:1024 sc1\n" \
  "s_cselect_b32 s58, s35, s59\n" \
  "v_lshlrev_b32_sdwa v64, 4, v212 dst_sel:DWORD dst_unused:UNUSED_PAD src0_sel:DWORD src1_sel:WORD_0\n" \
  "ds_read_b128 v[100:103], v64\n" \
  "v_lshlrev_b32_sdwa v65, 4, v212 dst_sel:DWORD dst_unused:UNUSED_PAD src0_sel:DWORD src1_sel:WORD_1\n" \
  "ds_read_b128 v[104:107], v65\n" \
  "v_lshlrev_b32_sdwa v66, 4, v213 dst_sel:DWORD dst_unused:UNUSED_PAD src0_sel:DWORD src1_sel:WORD_0\n" \
  "ds_read_b128 v[108:111], v66\n" \
  "v_lshlrev_b32_sdwa v67, 4, v213 dst_sel:DWORD dst_unused:UNUSED_PAD src0_sel:DWORD src1_sel:WORD_1\n" \
  "ds_read_b128 v[112:115], v67\n" \
  "s_waitcnt lgkmcnt(10)\n" \
  "v_smfmac_f32_16x16x64_bf16 v[0:3], v[180:183], v[68:75], v192\n" \
  "v_lshlrev_b32_sdwa v64, 4, v214 dst_sel:DWORD dst_unused:UNUSED_PAD src0_sel:DWORD src1_sel:WORD_0\n" \
  "ds_read_b128 v[68:71], v64\n" \
  "v_lshlrev_b32_sdwa v65, 4, v214 dst_sel:DWORD dst_unused:UNUSED_PAD src0_sel:DWORD src1_sel:WORD_1\n" \
  "ds_read_b128 v[72:75], v65\n" \
  "s_mov_b32 m0, s58\n" \
  "s_add_u32 s35, s58, 0x8000\n" \
  "global_load_lds_dwordx4 v190, s[54:55] sc1\n" \
  "global_load_lds_dwordx4 v158, s[54:55] offset:1024 sc1\n" \
  "s_waitcnt lgkmcnt(10)\n" \
  "v_smfmac_f32_16x16x64_bf16 v[4:7], v[180:183], v[76:83], v192\n" \
  "v_lshlrev_b32_sdwa v66, 4, v215 dst_sel:DWORD dst_unused:UNUSED_PAD src0_sel:DWORD src1_sel:WORD_0\n" \
  "ds_read_b128 v[76:79], v66\n" \
  "v_lshlrev_b32_sdwa v67, 4, v215 dst_sel:DWORD dst_unused:UNUSED_PAD src0_sel:DWORD src1_sel:WORD_1\n" \
  "ds_read_b128 v[80:83], v67\n" \
  "s_waitcnt lgkmcnt(10)\n" \
  "v_smfmac_f32_16x16x64_bf16 v[8:11], v[180:183], v[84:91], v192\n" \
  "v_lshlrev_b32_sdwa v64, 4, v216 dst_sel:DWORD dst_unused:UNUSED_PAD src0_sel:DWORD src1_sel:WORD_0\n" \
  "ds_read_b128 v[84:87], v64\n" \
  "v_lshlrev_b32_sdwa v65, 4, v216 dst_sel:DWORD dst_unused:UNUSED_PAD src0_sel:DWORD src1_sel:WORD_1\n" \
  "ds_read_b128 v[88:91], v65\n" \
  "s_add_u32 m0, s58, 0x4000\n" \
  "s_cmp_lt_u32 s35, 0x28000\n" \
  "global_load_lds_dwordx4 v191, s[54:55] sc1\n" \
  "global_load_lds_dwordx4 v159, s[54:55] offset:1024 sc1\n" \
  "s_cselect_b32 s58, s35, s59\n" \
  "s_waitcnt lgkmcnt(10)\n" \
  "v_smfmac_f32_16x16x64_bf16 v[12:15], v[180:183], v[92:99], v192\n" \
  "v_lshlrev_b32_sdwa v66, 4, v217 dst_sel:DWORD dst_unused:UNUSED_PAD src0_sel:DWORD src1_sel:WORD_0\n" \
  "ds_read_b128 v[92:95], v66\n" \
  "v_lshlrev_b32_sdwa v67, 4, v217 dst_sel:DWORD dst_unused:UNUSED_PAD src0_sel:DWORD src1_sel:WORD_1\n" \
  "ds_read_b128 v[96:99], v67\n" \
  "s_waitcnt lgkmcnt(10)\n" \
  "v_smfmac_f32_16x16x64_bf16 v[16:19], v[180:183], v[100:107], v192\n" \
  "v_lshlrev_b32_sdwa v64, 4, v218 dst_sel:DWORD dst_unused:UNUSED_PAD src0_sel:DWORD src1_sel:WORD_0\n" \
  "ds_read_b128 v[100:103], v64\n" \
  "v_lshlrev_b32_sdwa v65, 4, v218 dst_sel:DWORD dst_unused:UNUSED_PAD src0_sel:DWORD src1_sel:WORD_1\n" \
  "ds_read_b128 v[104:107], v65\n" \
  "global_load_dwordx4 v[240:243], v188, s[56:57] sc1\n" \
  "global_load_dwordx4 v[244:247], v188, s[56:57] offset:1024 sc1\n" \
  "s_waitcnt lgkmcnt(10)\n" \
  "v_smfmac_f32_16x16x64_bf16 v[20:23], v[180:183], v[108:115], v192\n" \
  "v_lshlrev_b32_sdwa v66, 4, v219 dst_sel:DWORD dst_unused:UNUSED_PAD src0_sel:DWORD src1_sel:WORD_0\n" \
  "ds_read_b128 v[108:111], v66\n" \
  "v_lshlrev_b32_sdwa v67, 4, v219 dst_sel:DWORD dst_unused:UNUSED_PAD src0_sel:DWORD src1_sel:WORD_1\n" \
  "ds_read_b128 v[112:115], v67\n" \
  "s_waitcnt lgkmcnt(10)\n" \
  "v_smfmac_f32_16x16x64_bf16 v[24:27], v[180:183], v[68:75], v192\n" \
  "v_lshlrev_b32_sdwa v64, 4, v220 dst_sel:DWORD dst_unused:UNUSED_PAD src0_sel:DWORD src1_sel:WORD_0\n" \
  "ds_read_b128 v[68:71], v64\n" \
  "v_lshlrev_b32_sdwa v65, 4, v220 dst_sel:DWORD dst_unused:UNUSED_PAD src0_sel:DWORD src1_sel:WORD_1\n" \
  "ds_read_b128 v[72:75], v65\n" \
  "global_load_dwordx4 v[248:251], v188, s[56:57] offset:2048 sc1\n" \
  "global_load_dwordx4 v[252:255], v188, s[56:57] offset:3072 sc1\n" \
  "s_waitcnt lgkmcnt(10)\n" \
  "v_smfmac_f32_16x16x64_bf16 v[28:31], v[180:183], v[76:83], v192\n" \
  "v_lshlrev_b32_sdwa v66, 4, v221 dst_sel:DWORD dst_unused:UNUSED_PAD src0_sel:DWORD src1_sel:WORD_0\n" \
  "ds_read_b128 v[76:79], v66\n" \
  "v_lshlrev_b32_sdwa v67, 4, v221 dst_sel:DWORD dst_unused:UNUSED_PAD src0_sel:DWORD src1_sel:WORD_1\n" \
  "ds_read_b128 v[80:83], v67\n" \
  "s_waitcnt lgkmcnt(10)\n" \
  "v_smfmac_f32_16x16x64_bf16 v[32:35], v[180:183], v[84:91], v192\n" \
  "v_lshlrev_b32_sdwa v64, 4, v222 dst_sel:DWORD dst_unused:UNUSED_PAD src0_sel:DWORD src1_sel:WORD_0\n" \
  "ds_read_b128 v[84:87], v64\n" \
  "v_lshlrev_b32_sdwa v65, 4, v222 dst_sel:DWORD dst_unused:UNUSED_PAD src0_sel:DWORD src1_sel:WORD_1\n" \
  "ds_read_b128 v[88:91], v65\n" \
  "s_waitcnt lgkmcnt(10)\n" \
  "v_smfmac_f32_16x16x64_bf16 v[36:39], v[180:183], v[92:99], v192\n" \
  "v_lshlrev_b32_sdwa v66, 4, v223 dst_sel:DWORD dst_unused:UNUSED_PAD src0_sel:DWORD src1_sel:WORD_0\n" \
  "ds_read_b128 v[92:95], v66\n" \
  "v_lshlrev_b32_sdwa v67, 4, v223 dst_sel:DWORD dst_unused:UNUSED_PAD src0_sel:DWORD src1_sel:WORD_1\n" \
  "ds_read_b128 v[96:99], v67\n" \
  "s_add_u32 s56, s56, 32768\n" \
  "s_addc_u32 s57, s57, 0\n" \
  "s_add_u32 s54, s54, 64000\n" \
  "s_addc_u32 s55, s55, 0\n" \
  "s_sub_u32 s51, s51, 1\n" \
  "s_cmp_lg_u32 s51, 0\n" \
  "s_cbranch_scc1 LG_loop\n" \
  "s_waitcnt lgkmcnt(10)\n" \
  "v_smfmac_f32_16x16x64_bf16 v[40:43], v[180:183], v[100:107], v192\n" \
  "s_waitcnt lgkmcnt(8)\n" \
  "v_smfmac_f32_16x16x64_bf16 v[44:47], v[180:183], v[108:115], v192\n" \
  "s_waitcnt vmcnt(8)\n" \
  "s_barrier\n" \
  "v_lshlrev_b32_sdwa v64, 4, v224 dst_sel:DWORD dst_unused:UNUSED_PAD src0_sel:DWORD src1_sel:WORD_0\n" \
  "ds_read_b128 v[100:103], v64\n" \
  "v_lshlrev_b32_sdwa v65, 4, v224 dst_sel:DWORD dst_unused:UNUSED_PAD src0_sel:DWORD src1_sel:WORD_1\n" \
  "ds_read_b128 v[104:107], v65\n" \
  "v_lshlrev_b32_sdwa v66, 4, v225 dst_sel:DWORD dst_unused:UNUSED_PAD src0_sel:DWORD src1_sel:WORD_0\n" \
  "ds_read_b128 v[108:111], v66\n" \
  "v_lshlrev_b32_sdwa v67, 4, v225 dst_sel:DWORD dst_unused:UNUSED_PAD src0_sel:DWORD src1_sel:WORD_1\n" \
  "ds_read_b128 v[112:115], v67\n" \
  "s_waitcnt lgkmcnt(10)\n" \
  "v_smfmac_f32_16x16x64_bf16 v[48:51], v[180:183], v[68:75], v192\n" \
  "s_waitcnt lgkmcnt(8)\n" \
  "v_smfmac_f32_16x16x64_bf16 v[52:55], v[180:183], v[76:83], v192\n" \
  "s_waitcnt lgkmcnt(6)\n" \
  "v_smfmac_f32_16x16x64_bf16 v[56:59], v[180:183], v[84:91], v192\n" \
  "s_waitcnt lgkmcnt(4)\n" \
  "v_smfmac_f32_16x16x64_bf16 v[60:63], v[180:183], v[92:99], v192\n" \
  "s_barrier\n" \
  "s_mov_b32 m0, s58\n" \
  "s_add_u32 s35, s58, 0x8000\n" \
  "global_load_lds_dwordx4 v188, s[54:55] sc1\n" \
  "global_load_lds_dwordx4 v156, s[54:55] offset:1024 sc1\n" \
  "v_lshlrev_b32_sdwa v64, 4, v226 dst_sel:DWORD dst_unused:UNUSED_PAD src0_sel:DWORD src1_sel:WORD_0\n" \
  "ds_read_b128 v[68:71], v64\n" \
  "v_lshlrev_b32_sdwa v65, 4, v226 dst_sel:DWORD dst_unused:UNUSED_PAD src0_sel:DWORD src1_sel:WORD_1\n" \
  "ds_read_b128 v[72:75], v65\n" \
  "v_lshlrev_b32_sdwa v66, 4, v227 dst_sel:DWORD dst_unused:UNUSED_PAD src0_sel:DWORD src1_sel:WORD_0\n" \
  "ds_read_b128 v[76:79], v66\n" \
  "v_lshlrev_b32_sdwa v67, 4, v227 dst_sel:DWORD dst_unused:UNUSED_PAD src0_sel:DWORD src1_sel:WORD_1\n" \
  "ds_read_b128 v[80:83], v67\n" \
  "s_add_u32 m0, s58, 0x4000\n" \
  "s_cmp_lt_u32 s35, 0x28000\n" \
  "global_load_lds_dwordx4 v189, s[54:55] sc1\n" \
  "global_load_lds_dwordx4 v157, s[54:55] offset:1024 sc1\n" \
  "s_cselect_b32 s58, s35, s59\n" \
  "v_lshlrev_b32_sdwa v64, 4, v228 dst_sel:DWORD dst_unused:UNUSED_PAD src0_sel:DWORD src1_sel:WORD_0\n" \
  "ds_read_b128 v[84:87], v64\n" \
  "v_lshlrev_b32_sdwa v65, 4, v228 dst_sel:DWORD dst_unused:UNUSED_PAD src0_sel:DWORD src1_sel:WORD_1\n" \
  "ds_read_b128 v[88:91], v65\n" \
  "v_lshlrev_b32_sdwa v66, 4, v229 dst_sel:DWORD dst_unused:UNUSED_PAD src0_sel:DWORD src1_sel:WORD_0\n" \
  "ds_read_b128 v[92:95], v66\n" \
  "v_lshlrev_b32 v120, 16, v206\n" \
  "v_and_b32 v121, 0xffff0000, v206\n" \
  "v_lshlrev_b32 v122, 16, v207\n" \
  "v_and_b32 v123, 0xffff0000, v207\n" \
  "v_rcp_f32 v120, v120\n" \
  "v_rcp_f32 v121, v121\n" \
  "v_rcp_f32 v122, v122\n" \
  "v_rcp_f32 v123, v123\n" \
  "s_mov_b32 s52, 0x7f000000\n" \
  "v_med3_f32 v120, v120, s52, -s52\n" \
  "v_med3_f32 v121, v121, s52, -s52\n" \
  "v_med3_f32 v122, v122, s52, -s52\n" \
  "v_med3_f32 v123, v123, s52, -s52\n" \
  "v_mov_b32 v124, 0xc0135761\n" \
  "v_mov_b32 v125, 0xc0135761\n" \
  "s_mov_b32 s52, 0xbdd2d3e8\n" \
  "s_mov_b32 s53, s52\n" \
  "s_lshl2_add_u32 s35, s33, s34\n" \
  "s_lshl_b32 s35, s35, 14\n" \
  "s_add_u32 s35, s35, s59\n" \
  "s_add_u32 s35, s35, 105676800\n" \
  "s_add_u32 s40, s22, s35\n" \
  "s_addc_u32 s41, s23, 0\n" \
  "v_lshlrev_b32_sdwa v67, 4, v229 dst_sel:DWORD dst_unused:UNUSED_PAD src0_sel:DWORD src1_sel:WORD_1\n" \
  "ds_read_b128 v[96:99], v67\n" \
  "s_waitcnt lgkmcnt(10)\n" \
  "v_smfmac_f32_16x16x64_bf16 v[0:3], v[180:183], v[100:107], v192\n" \
  "v_lshlrev_b32_sdwa v64, 4, v230 dst_sel:DWORD dst_unused:UNUSED_PAD src0_sel:DWORD src1_sel:WORD_0\n" \
  "ds_read_b128 v[100:103], v64\n" \
  "v_lshlrev_b32_sdwa v65, 4, v230 dst_sel:DWORD dst_unused:UNUSED_PAD src0_sel:DWORD src1_sel:WORD_1\n" \
  "ds_read_b128 v[104:107], v65\n" \
  "s_mov_b32 m0, s58\n" \
  "s_add_u32 s35, s58, 0x8000\n" \
  "global_load_lds_dwordx4 v190, s[54:55] sc1\n" \
  "global_load_lds_dwordx4 v158, s[54:55] offset:1024 sc1\n" \
  "s_waitcnt lgkmcnt(10)\n" \
  "v_smfmac_f32_16x16x64_bf16 v[4:7], v[180:183], v[108:115], v192\n" \
  "v_lshlrev_b32_sdwa v66, 4, v231 dst_sel:DWORD dst_unused:UNUSED_PAD src0_sel:DWORD src1_sel:WORD_0\n" \
  "ds_read_b128 v[108:111], v66\n" \
  "v_lshlrev_b32_sdwa v67, 4, v231 dst_sel:DWORD dst_unused:UNUSED_PAD src0_sel:DWORD src1_sel:WORD_1\n" \
  "ds_read_b128 v[112:115], v67\n" \
  "s_waitcnt lgkmcnt(10)\n" \
  "v_smfmac_f32_16x16x64_bf16 v[8:11], v[180:183], v[68:75], v192\n" \
  "v_lshlrev_b32_sdwa v64, 4, v232 dst_sel:DWORD dst_unused:UNUSED_PAD src0_sel:DWORD src1_sel:WORD_0\n" \
  "ds_read_b128 v[68:71], v64\n" \
  "v_lshlrev_b32_sdwa v65, 4, v232 dst_sel:DWORD dst_unused:UNUSED_PAD src0_sel:DWORD src1_sel:WORD_1\n" \
  "ds_read_b128 v[72:75], v65\n" \
  "s_add_u32 m0, s58, 0x4000\n" \
  "s_cmp_lt_u32 s35, 0x28000\n" \
  "global_load_lds_dwordx4 v191, s[54:55] sc1\n" \
  "global_load_lds_dwordx4 v159, s[54:55] offset:1024 sc1\n" \
  "s_cselect_b32 s58, s35, s59\n" \
  "s_waitcnt lgkmcnt(10)\n" \
  "v_smfmac_f32_16x16x64_bf16 v[12:15], v[180:183], v[76:83], v192\n" \
  "v_lshlrev_b32_sdwa v66, 4, v233 dst_sel:DWORD dst_unused:UNUSED_PAD src0_sel:DWORD src1_sel:WORD_0\n" \
  "ds_read_b128 v[76:79], v66\n" \
  "v_lshlrev_b32_sdwa v67, 4, v233 dst_sel:DWORD dst_unused:UNUSED_PAD src0_sel:DWORD src1_sel:WORD_1\n" \
  "ds_read_b128 v[80:83], v67\n" \
  "s_waitcnt lgkmcnt(10)\n" \
  "v_smfmac_f32_16x16x64_bf16 v[16:19], v[180:183], v[84:91], v192\n" \
  "v_lshlrev_b32_sdwa v64, 4, v234 dst_sel:DWORD dst_unused:UNUSED_PAD src0_sel:DWORD src1_sel:WORD_0\n" \
  "ds_read_b128 v[84:87], v64\n" \
  "v_lshlrev_b32_sdwa v65, 4, v234 dst_sel:DWORD dst_unused:UNUSED_PAD src0_sel:DWORD src1_sel:WORD_1\n" \
  "ds_read_b128 v[88:91], v65\n" \
  "global_load_dwordx4 v[208:211], v188, s[56:57] sc1\n" \
  "global_load_dwordx4 v[212:215], v188, s[56:57] offset:1024 sc1\n" \
  "s_waitcnt lgkmcnt(10)\n" \
  "v_smfmac_f32_16x16x64_bf16 v[20:23], v[180:183], v[92:99], v192\n" \
  "v_lshlrev_b32_sdwa v66, 4, v235 dst_sel:DWORD dst_unused:UNUSED_PAD src0_sel:DWORD src1_sel:WORD_0\n" \
  "ds_read_b128 v[92:95], v66\n" \
  "v_lshlrev_b32_sdwa v67, 4, v235 dst_sel:DWORD dst_unused:UNUSED_PAD src0_sel:DWORD src1_sel:WORD_1\n" \
  "ds_read_b128 v[96:99], v67\n" \
  "s_waitcnt lgkmcnt(10)\n" \
  "v_smfmac_f32_16x16x64_bf16 v[24:27], v[180:183], v[100:107], v192\n" \
  "v_lshlrev_b32_sdwa v64, 4, v236 dst_sel:DWORD dst_unused:UNUSED_PAD src0_sel:DWORD src1_sel:WORD_0\n" \
  "ds_read_b128 v[100:103], v64\n" \
  "v_lshlrev_b32_sdwa v65, 4, v236 dst_sel:DWORD dst_unused:UNUSED_PAD src0_sel:DWORD src1_sel:WORD_1\n" \
  "ds_read_b128 v[104:107], v65\n" \
  "global_load_dwordx4 v[216:219], v188, s[56:57] offset:2048 sc1\n" \
  "global_load_dwordx4 v[220:223], v188, s[56:57] offset:3072 sc1\n" \
  "s_waitcnt lgkmcnt(10)\n" \
  "v_smfmac_f32_16x16x64_bf16 v[28:31], v[180:183], v[108:115], v192\n" \
  "v_lshlrev_b32_sdwa v66, 4, v237 dst_sel:DWORD dst_unused:UNUSED_PAD src0_sel:DWORD src1_sel:WORD_0\n" \
  "ds_read_b128 v[108:111], v66\n" \
  "v_lshlrev_b32_sdwa v67, 4, v237 dst_sel:DWORD dst_unused:UNUSED_PAD src0_sel:DWORD src1_sel:WORD_1\n" \
  "ds_read_b128 v[112:115], v67\n" \
  "s_waitcnt lgkmcnt(10)\n" \
  "v_smfmac_f32_16x16x64_bf16 v[32:35], v[180:183], v[68:75], v192\n" \
  "v_lshlrev_b32_sdwa v64, 4, v238 dst_sel:DWORD dst_unused:UNUSED_PAD src0_sel:DWORD src1_sel:WORD_0\n" \
  "ds_read_b128 v[68:71], v64\n" \
  "v_lshlrev_b32_sdwa v65, 4, v238 dst_sel:DWORD dst_unused:UNUSED_PAD src0_sel:DWORD src1_sel:WORD_1\n" \
  "ds_read_b128 v[72:75], v65\n" \
  "s_waitcnt lgkmcnt(10)\n" \
  "v_smfmac_f32_16x16x64_bf16 v[36:39], v[180:183], v[76:83], v192\n" \
  "v_lshlrev_b32_sdwa v66, 4, v239 dst_sel:DWORD dst_unused:UNUSED_PAD src0_sel:DWORD src1_sel:WORD_0\n" \
  "ds_read_b128 v[76:79], v66\n" \
  "v_lshlrev_b32_sdwa v67, 4, v239 dst_sel:DWORD dst_unused:UNUSED_PAD src0_sel:DWORD src1_sel:WORD_1\n" \
  "ds_read_b128 v[80:83], v67\n" \
  "s_add_u32 s56, s56, 32768\n" \
  "s_addc_u32 s57, s57, 0\n" \
  "s_add_u32 s54, s54, 64000\n" \
  "s_addc_u32 s55, s55, 0\n" \
  "s_waitcnt lgkmcnt(10)\n" \
  "v_smfmac_f32_16x16x64_bf16 v[40:43], v[180:183], v[84:91], v192\n" \
  "s_waitcnt lgkmcnt(8)\n" \
  "v_smfmac_f32_16x16x64_bf16 v[44:47], v[180:183], v[92:99], v192\n" \
  "s_waitcnt vmcnt(8)\n" \
  "s_barrier\n" \
  "v_lshlrev_b32_sdwa v64, 4, v240 dst_sel:DWORD dst_unused:UNUSED_PAD src0_sel:DWORD src1_sel:WORD_0\n" \
  "ds_read_b128 v[84:87], v64\n" \
  "v_lshlrev_b32_sdwa v65, 4, v240 dst_sel:DWORD dst_unused:UNUSED_PAD src0_sel:DWORD src1_sel:WORD_1\n" \
  "ds_read_b128 v[88:91], v65\n" \
  "v_lshlrev_b32_sdwa v66, 4, v241 dst_sel:DWORD dst_unused:UNUSED_PAD src0_sel:DWORD src1_sel:WORD_0\n" \
  "ds_read_b128 v[92:95], v66\n" \
  "v_lshlrev_b32_sdwa v67, 4, v241 dst_sel:DWORD dst_unused:UNUSED_PAD src0_sel:DWORD src1_sel:WORD_1\n" \
  "ds_read_b128 v[96:99], v67\n" \
  "s_waitcnt lgkmcnt(10)\n" \
  "v_smfmac_f32_16x16x64_bf16 v[48:51], v[180:183], v[100:107], v192\n" \
  "s_waitcnt lgkmcnt(8)\n" \
  "v_smfmac_f32_16x16x64_bf16 v[52:55], v[180:183], v[108:115], v192\n" \
  "s_waitcnt lgkmcnt(6)\n" \
  "v_smfmac_f32_16x16x64_bf16 v[56:59], v[180:183], v[68:75], v192\n" \
  "s_waitcnt lgkmcnt(4)\n" \
  "v_smfmac_f32_16x16x64_bf16 v[60:63], v[180:183], v[76:83], v192\n" \
  "s_barrier\n" \
  "s_mov_b32 m0, s58\n" \
  "s_add_u32 s35, s58, 0x8000\n" \
  "global_load_lds_dwordx4 v188, s[54:55] sc1\n" \
  "global_load_lds_dwordx4 v156, s[54:55] offset:1024 sc1\n" \
  "s_add_u32 m0, s58, 0x4000\n" \
  "s_cmp_lt_u32 s35, 0x28000\n" \
  "global_load_lds_dwordx4 v189, s[54:55] sc1\n" \
  "global_load_lds_dwordx4 v157, s[54:55] offset:1024 sc1\n" \
  "s_cselect_b32 s58, s35, s59\n" \
  "v_lshlrev_b32_sdwa v64, 4, v242 dst_sel:DWORD dst_unused:UNUSED_PAD src0_sel:DWORD src1_sel:WORD_0\n" \
  "ds_read_b128 v[100:103], v64\n" \
  "v_lshlrev_b32_sdwa v65, 4, v242 dst_sel:DWORD dst_unused:UNUSED_PAD src0_sel:DWORD src1_sel:WORD_1\n" \
  "ds_read_b128 v[104:107], v65\n" \
  "v_lshlrev_b32_sdwa v66, 4, v243 dst_sel:DWORD dst_unused:UNUSED_PAD src0_sel:DWORD src1_sel:WORD_0\n" \
  "ds_read_b128 v[108:111], v66\n" \
  "v_lshlrev_b32_sdwa v67, 4, v243 dst_sel:DWORD dst_unused:UNUSED_PAD src0_sel:DWORD src1_sel:WORD_1\n" \
  "ds_read_b128 v[112:115], v67\n" \
  "v_lshlrev_b32_sdwa v64, 4, v244 dst_sel:DWORD dst_unused:UNUSED_PAD src0_sel:DWORD src1_sel:WORD_0\n" \
  "ds_read_b128 v[68:71], v64\n" \
  "v_lshlrev_b32_sdwa v65, 4, v244 dst_sel:DWORD dst_unused:UNUSED_PAD src0_sel:DWORD src1_sel:WORD_1\n" \
  "ds_read_b128 v[72:75], v65\n" \
  "v_lshlrev_b32_sdwa v66, 4, v245 dst_sel:DWORD dst_unused:UNUSED_PAD src0_sel:DWORD src1_sel:WORD_0\n" \
  "ds_read_b128 v[76:79], v66\n" \
  "v_lshlrev_b32_sdwa v67, 4, v245 dst_sel:DWORD dst_unused:UNUSED_PAD src0_sel:DWORD src1_sel:WORD_1\n" \
  "ds_read_b128 v[80:83], v67\n" \
  "s_waitcnt lgkmcnt(10)\n" \
  "v_smfmac_f32_16x16x64_bf16 v[0:3], v[180:183], v[84:91], v192\n" \
  "v_lshlrev_b32_sdwa v64, 4, v246 dst_sel:DWORD dst_unused:UNUSED_PAD src0_sel:DWORD src1_sel:WORD_0\n" \
  "ds_read_b128 v[84:87], v64\n" \
  "v_lshlrev_b32_sdwa v65, 4, v246 dst_sel:DWORD dst_unused:UNUSED_PAD src0_sel:DWORD src1_sel:WORD_1\n" \
  "ds_read_b128 v[88:91], v65\n" \
  "s_waitcnt lgkmcnt(10)\n" \
  "v_smfmac_f32_16x16x64_bf16 v[4:7], v[180:183], v[92:99], v192\n" \
  "v_lshlrev_b32_sdwa v66, 4, v247 dst_sel:DWORD dst_unused:UNUSED_PAD src0_sel:DWORD src1_sel:WORD_0\n" \
  "ds_read_b128 v[92:95], v66\n" \
  "v_lshlrev_b32_sdwa v67, 4, v247 dst_sel:DWORD dst_unused:UNUSED_PAD src0_sel:DWORD src1_sel:WORD_1\n" \
  "ds_read_b128 v[96:99], v67\n" \
  "s_waitcnt lgkmcnt(10)\n" \
  "v_smfmac_f32_16x16x64_bf16 v[8:11], v[180:183], v[100:107], v192\n" \
  "v_lshlrev_b32_sdwa v64, 4, v248 dst_sel:DWORD dst_unused:UNUSED_PAD src0_sel:DWORD src1_sel:WORD_0\n" \
  "ds_read_b128 v[100:103], v64\n" \
  "v_lshlrev_b32_sdwa v65, 4, v248 dst_sel:DWORD dst_unused:UNUSED_PAD src0_sel:DWORD src1_sel:WORD_1\n" \
  "ds_read_b128 v[104:107], v65\n" \
  "s_waitcnt lgkmcnt(10)\n" \
  "v_smfmac_f32_16x16x64_bf16 v[12:15], v[180:183], v[108:115], v192\n" \
  "v_lshlrev_b32_sdwa v66, 4, v249 dst_sel:DWORD dst_unused:UNUSED_PAD src0_sel:DWORD src1_sel:WORD_0\n" \
  "ds_read_b128 v[108:111], v66\n" \
  "v_lshlrev_b32_sdwa v67, 4, v249 dst_sel:DWORD dst_unused:UNUSED_PAD src0_sel:DWORD src1_sel:WORD_1\n" \
  "ds_read_b128 v[112:115], v67\n" \
  "s_waitcnt vmcnt(0)\n" \
  "s_barrier\n" \
  "s_cmp_lt_u32 s27, 4\n" \
  "s_cbranch_scc1 LG_tp\n" \
  "s_setprio 1\n" \
  "LG_tp:\n" \
  "s_waitcnt lgkmcnt(10)\n" \
  "v_smfmac_f32_16x16x64_bf16 v[16:19], v[180:183], v[68:75], v192\n" \
  "s_waitcnt lgkmcnt(8)\n" \
  "v_smfmac_f32_16x16x64_bf16 v[20:23], v[180:183], v[76:83], v192\n" \
  "s_waitcnt lgkmcnt(6)\n" \
  "v_smfmac_f32_16x16x64_bf16 v[24:27], v[180:183], v[84:91], v192\n" \
  "s_waitcnt lgkmcnt(4)\n" \
  "v_smfmac_f32_16x16x64_bf16 v[28:31], v[180:183], v[92:99], v192\n" \
  "s_waitcnt lgkmcnt(2)\n" \
  "v_smfmac_f32_16x16x64_bf16 v[32:35], v[180:183], v[100:107], v192\n" \
  "v_lshlrev_b32_sdwa v64, 4, v208 dst_sel:DWORD dst_unused:UNUSED_PAD src0_sel:DWORD src1_sel:WORD_0\n" \
  "ds_read_b128 v[68:71], v64\n" \
  "v_lshlrev_b32_sdwa v65, 4, v208 dst_sel:DWORD dst_unused:UNUSED_PAD src0_sel:DWORD src1_sel:WORD_1\n" \
  "ds_read_b128 v[72:75], v65\n" \
  "s_waitcnt lgkmcnt(2)\n" \
  "v_smfmac_f32_16x16x64_bf16 v[36:39], v[180:183], v[108:115], v192\n" \
  "v_lshlrev_b32_sdwa v66, 4, v209 dst_sel:DWORD dst_unused:UNUSED_PAD src0_sel:DWORD src1_sel:WORD_0\n" \
  "ds_read_b128 v[76:79], v66\n" \
  "v_lshlrev_b32_sdwa v67, 4, v209 dst_sel:DWORD dst_unused:UNUSED_PAD src0_sel:DWORD src1_sel:WORD_1\n" \
  "ds_read_b128 v[80:83], v67\n" \
  "s_waitcnt lgkmcnt(2)\n" \
  "v_smfmac_f32_16x16x64_bf16 v[0:3], v[180:183], v[68:75], v192\n" \
  "v_lshlrev_b32_sdwa v64, 4, v210 dst_sel:DWORD dst_unused:UNUSED_PAD src0_sel:DWORD src1_sel:WORD_0\n" \
  "ds_read_b128 v[84:87], v64\n" \
  "v_lshlrev_b32_sdwa v65, 4, v210 dst_sel:DWORD dst_unused:UNUSED_PAD src0_sel:DWORD src1_sel:WORD_1\n" \
  "ds_read_b128 v[88:91], v65\n" \
  "s_waitcnt lgkmcnt(2)\n" \
  "v_smfmac_f32_16x16x64_bf16 v[4:7], v[180:183], v[76:83], v192\n" \
  "v_lshlrev_b32_sdwa v66, 4, v211 dst_sel:DWORD dst_unused:UNUSED_PAD src0_sel:DWORD src1_sel:WORD_0\n" \
  "ds_read_b128 v[92:95], v66\n" \
  "v_lshlrev_b32_sdwa v67, 4, v211 dst_sel:DWORD dst_unused:UNUSED_PAD src0_sel:DWORD src1_sel:WORD_1\n" \
  "ds_read_b128 v[96:99], v67\n" \
  "v_pk_mul_f32 v[128:129], v[0:1], v[0:1]\n" \
  "v_pk_mul_f32 v[130:131], v[2:3], v[2:3]\n" \
  "v_pk_fma_f32 v[128:129], v[128:129], s[52:53], v[124:125]\n" \
  "v_pk_fma_f32 v[130:131], v[130:131], s[52:53], v[124:125]\n" \
  "v_pk_mul_f32 v[128:129], v[128:129], v[0:1]\n" \
  "v_pk_mul_f32 v[130:131], v[130:131], v[2:3]\n" \
  "v_exp_f32 v128, v128\n" \
  "v_exp_f32 v129, v129\n" \
  "v_exp_f32 v130, v130\n" \
  "v_exp_f32 v131, v131\n" \
  "s_waitcnt lgkmcnt(2)\n" \
  "v_smfmac_f32_16x16x64_bf16 v[8:11], v[180:183], v[84:91], v192\n" \
  "v_lshlrev_b32_sdwa v64, 4, v212 dst_sel:DWORD dst_unused:UNUSED_PAD src0_sel:DWORD src1_sel:WORD_0\n" \
  "ds_read_b128 v[100:103], v64\n" \
  "v_lshlrev_b32_sdwa v65, 4, v212 dst_sel:DWORD dst_unused:UNUSED_PAD src0_sel:DWORD src1_sel:WORD_1\n" \
  "ds_read_b128 v[104:107], v65\n" \
  "v_pk_fma_f32 v[128:129], v[128:129], v[120:121], v[120:121]\n" \
  "v_pk_fma_f32 v[130:131], v[130:131], v[122:123], v[122:123]\n" \
  "v_rcp_f32 v128, v128\n" \
  "v_rcp_f32 v129, v129\n" \
  "v_rcp_f32 v130, v130\n" \
  "v_rcp_f32 v131, v131\n" \
  "v_pk_mul_f32 v[0:1], v[0:1], v[128:129]\n" \
  "v_pk_fma_f32 v[0:1], v[2:3], v[130:131], v[0:1]\n" \
  "v_add_f32 v0, v0, v1\n" \
  "v_pk_mul_f32 v[132:133], v[4:5], v[4:5]\n" \
  "s_waitcnt lgkmcnt(2)\n" \
  "v_smfmac_f32_16x16x64_bf16 v[12:15], v[180:183], v[92:99], v192\n" \
  "v_lshlrev_b32_sdwa v66, 4, v213 dst_sel:DWORD dst_unused:UNUSED_PAD src0_sel:DWORD src1_sel:WORD_0\n" \
  "ds_read_b128 v[108:111], v66\n" \
  "v_lshlrev_b32_sdwa v67, 4, v213 dst_sel:DWORD dst_unused:UNUSED_PAD src0_sel:DWORD src1_sel:WORD_1\n" \
  "ds_read_b128 v[112:115], v67\n" \
  "v_pk_mul_f32 v[134:135], v[6:7], v[6:7]\n" \
  "v_pk_fma_f32 v[132:133], v[132:133], s[52:53], v[124:125]\n" \
  "v_pk_fma_f32 v[134:135], v[134:135], s[52:53], v[124:125]\n" \
  "v_pk_mul_f32 v[132:133], v[132:133], v[4:5]\n" \
  "v_pk_mul_f32 v[134:135], v[134:135], v[6:7]\n" \
  "v_exp_f32 v132, v132\n" \
  "v_exp_f32 v133, v133\n" \
  "v_exp_f32 v134, v134\n" \
  "v_exp_f32 v135, v135\n" \
  "v_pk_fma_f32 v[132:133], v[132:133], v[120:121], v[120:121]\n" \
  "s_waitcnt lgkmcnt(2)\n" \
  "v_smfmac_f32_16x16x64_bf16 v[16:19], v[180:183], v[100:107], v192\n" \
  "v_lshlrev_b32_sdwa v64, 4, v214 dst_sel:DWORD dst_unused:UNUSED_PAD src0_sel:DWORD src1_sel:WORD_0\n" \
  "ds_read_b128 v[68:71], v64\n" \
  "v_lshlrev_b32_sdwa v65, 4, v214 dst_sel:DWORD dst_unused:UNUSED_PAD src0_sel:DWORD src1_sel:WORD_1\n" \
  "ds_read_b128 v[72:75], v65\n" \
  "v_pk_fma_f32 v[134:135], v[134:135], v[122:123], v[122:123]\n" \
  "v_rcp_f32 v132, v132\n" \
  "v_rcp_f32 v133, v133\n" \
  "v_rcp_f32 v134, v134\n" \
  "v_rcp_f32 v135, v135\n" \
  "v_pk_mul_f32 v[4:5], v[4:5], v[132:133]\n" \
  "v_pk_fma_f32 v[4:5], v[6:7], v[134:135], v[4:5]\n" \
  "v_add_f32 v4, v4, v5\n" \
  "v_pk_mul_f32 v[136:137], v[8:9], v[8:9]\n" \
  "v_pk_mul_f32 v[138:139], v[10:11], v[10:11]\n" \
  "s_waitcnt lgkmcnt(2)\n" \
  "v_smfmac_f32_16x16x64_bf16 v[20:23], v[180:183], v[108:115], v192\n" \
  "v_lshlrev_b32_sdwa v66, 4, v215 dst_sel:DWORD dst_unused:UNUSED_PAD src0_sel:DWORD src1_sel:WORD_0\n" \
  "ds_read_b128 v[76:79], v66\n" \
  "v_lshlrev_b32_sdwa v67, 4, v215 dst_sel:DWORD dst_unused:UNUSED_PAD src0_sel:DWORD src1_sel:WORD_1\n" \
  "ds_read_b128 v[80:83], v67\n" \
  "v_pk_fma_f32 v[136:137], v[136:137], s[52:53], v[124:125]\n" \
  "v_pk_fma_f32 v[138:139], v[138:139], s[52:53], v[124:125]\n" \
  "v_pk_mul_f32 v[136:137], v[136:137], v[8:9]\n" \
  "v_pk_mul_f32 v[138:139], v[138:139], v[10:11]\n" \
  "v_exp_f32 v136, v136\n" \
  "v_exp_f32 v137, v137\n" \
  "v_exp_f32 v138, v138\n" \
  "v_exp_f32 v139, v139\n" \
  "v_pk_fma_f32 v[136:137], v[136:137], v[120:121], v[120:121]\n" \
  "v_pk_fma_f32 v[138:139], v[138:139], v[122:123], v[122:123]\n" \
  "s_waitcnt lgkmcnt(2)\n" \
  "v_smfmac_f32_16x16x64_bf16 v[24:27], v[180:183], v[68:75], v192\n" \
  "v_lshlrev_b32_sdwa v64, 4, v216 dst_sel:DWORD dst_unused:UNUSED_PAD src0_sel:DWORD src1_sel:WORD_0\n" \
  "ds_read_b128 v[84:87], v64\n" \
  "v_lshlrev_b32_sdwa v65, 4, v216 dst_sel:DWORD dst_unused:UNUSED_PAD src0_sel:DWORD src1_sel:WORD_1\n" \
  "ds_read_b128 v[88:91], v65\n" \
  "v_rcp_f32 v136, v136\n" \
  "v_rcp_f32 v137, v137\n" \
  "v_rcp_f32 v138, v138\n" \
  "v_rcp_f32 v139, v139\n" \
  "v_pk_mul_f32 v[8:9], v[8:9], v[136:137]\n" \
  "v_pk_fma_f32 v[8:9], v[10:11], v[138:139], v[8:9]\n" \
  "v_add_f32 v8, v8, v9\n" \
  "v_pk_mul_f32 v[140:141], v[12:13], v[12:13]\n" \
  "v_pk_mul_f32 v[142:143], v[14:15], v[14:15]\n" \
  "v_pk_fma_f32 v[140:141], v[140:141], s[52:53], v[124:125]\n" \
  "s_waitcnt lgkmcnt(2)\n" \
  "v_smfmac_f32_16x16x64_bf16 v[28:31], v[180:183], v[76:83], v192\n" \
  "v_lshlrev_b32_sdwa v66, 4, v217 dst_sel:DWORD dst_unused:UNUSED_PAD src0_sel:DWORD src1_sel:WORD_0\n" \
  "ds_read_b128 v[92:95], v66\n" \
  "v_lshlrev_b32_sdwa v67, 4, v217 dst_sel:DWORD dst_unused:UNUSED_PAD src0_sel:DWORD src1_sel:WORD_1\n" \
  "ds_read_b128 v[96:99], v67\n" \
  "v_pk_fma_f32 v[142:143], v[142:143], s[52:53], v[124:125]\n" \
  "v_pk_mul_f32 v[140:141], v[140:141], v[12:13]\n" \
  "v_pk_mul_f32 v[142:143], v[142:143], v[14:15]\n" \
  "v_exp_f32 v140, v140\n" \
  "v_exp_f32 v141, v141\n" \
  "v_exp_f32 v142, v142\n" \
  "v_exp_f32 v143, v143\n" \
  "v_pk_fma_f32 v[140:141], v[140:141], v[120:121], v[120:121]\n" \
  "v_pk_fma_f32 v[142:143], v[142:143], v[122:123], v[122:123]\n" \
  "v_rcp_f32 v140, v140\n" \
  "s_waitcnt lgkmcnt(2)\n" \
  "v_smfmac_f32_16x16x64_bf16 v[32:35], v[180:183], v[84:91], v192\n" \
  "v_lshlrev_b32_sdwa v64, 4, v250 dst_sel:DWORD dst_unused:UNUSED_PAD src0_sel:DWORD src1_sel:WORD_0\n" \
  "ds_read_b128 v[100:103], v64\n" \
  "v_lshlrev_b32_sdwa v65, 4, v250 dst_sel:DWORD dst_unused:UNUSED_PAD src0_sel:DWORD src1_sel:WORD_1\n" \
  "ds_read_b128 v[104:107], v65\n" \
  "v_rcp_f32 v141, v141\n" \
  "v_rcp_f32 v142, v142\n" \
  "v_rcp_f32 v143, v143\n" \
  "v_pk_mul_f32 v[12:13], v[12:13], v[140:141]\n" \
  "v_pk_fma_f32 v[12:13], v[14:15], v[142:143], v[12:13]\n" \
  "v_add_f32 v12, v12, v13\n" \
  "v_pk_mul_f32 v[128:129], v[16:17], v[16:17]\n" \
  "v_pk_mul_f32 v[130:131], v[18:19], v[18:19]\n" \
  "v_pk_fma_f32 v[128:129], v[128:129], s[52:53], v[124:125]\n" \
  "v_pk_fma_f32 v[130:131], v[130:131], s[52:53], v[124:125]\n" \
  "s_waitcnt lgkmcnt(2)\n" \
  "v_smfmac_f32_16x16x64_bf16 v[36:39], v[180:183], v[92:99], v192\n" \
  "v_lshlrev_b32_sdwa v66, 4, v218 dst_sel:DWORD dst_unused:UNUSED_PAD src0_sel:DWORD src1_sel:WORD_0\n" \
  "ds_read_b128 v[108:111], v66\n" \
  "v_lshlrev_b32_sdwa v67, 4, v218 dst_sel:DWORD dst_unused:UNUSED_PAD src0_sel:DWORD src1_sel:WORD_1\n" \
  "ds_read_b128 v[112:115], v67\n" \
  "v_pk_mul_f32 v[128:129], v[128:129], v[16:17]\n" \
  "v_pk_mul_f32 v[130:131], v[130:131], v[18:19]\n" \
  "v_exp_f32 v128, v128\n" \
  "v_exp_f32 v129, v129\n" \
  "v_exp_f32 v130, v130\n" \
  "v_exp_f32 v131, v131\n" \
  "v_pk_fma_f32 v[128:129], v[128:129], v[120:121], v[120:121]\n" \
  "v_pk_fma_f32 v[130:131], v[130:131], v[122:123], v[122:123]\n" \
  "v_rcp_f32 v128, v128\n" \
  "v_rcp_f32 v129, v129\n" \
  "s_waitcnt lgkmcnt(2)\n" \
  "v_smfmac_f32_16x16x64_bf16 v[40:43], v[180:183], v[100:107], v192\n" \
  "v_lshlrev_b32_sdwa v64, 4, v251 dst_sel:DWORD dst_unused:UNUSED_PAD src0_sel:DWORD src1_sel:WORD_0\n" \
  "ds_read_b128 v[68:71], v64\n" \
  "v_lshlrev_b32_sdwa v65, 4, v251 dst_sel:DWORD dst_unused:UNUSED_PAD src0_sel:DWORD src1_sel:WORD_1\n" \
  "ds_read_b128 v[72:75], v65\n" \
  "v_rcp_f32 v130, v130\n" \
  "v_rcp_f32 v131, v131\n" \
  "v_pk_mul_f32 v[16:17], v[16:17], v[128:129]\n" \
  "v_pk_fma_f32 v[16:17], v[18:19], v[130:131], v[16:17]\n" \
  "v_add_f32 v16, v16, v17\n" \
  "v_pk_mul_f32 v[132:133], v[20:21], v[20:21]\n" \
  "v_pk_mul_f32 v[134:135], v[22:23], v[22:23]\n" \
  "v_pk_fma_f32 v[132:133], v[132:133], s[52:53], v[124:125]\n" \
  "v_pk_fma_f32 v[134:135], v[134:135], s[52:53], v[124:125]\n" \
  "v_pk_mul_f32 v[132:133], v[132:133], v[20:21]\n" \
  "s_waitcnt lgkmcnt(2)\n" \
  "v_smfmac_f32_16x16x64_bf16 v[40:43], v[180:183], v[108:115], v192\n" \
  "v_lshlrev_b32_sdwa v66, 4, v219 dst_sel:DWORD dst_unused:UNUSED_PAD src0_sel:DWORD src1_sel:WORD_0\n" \
  "ds_read_b128 v[76:79], v66\n" \
  "v_lshlrev_b32_sdwa v67, 4, v219 dst_sel:DWORD dst_unused:UNUSED_PAD src0_sel:DWORD src1_sel:WORD_1\n" \
  "ds_read_b128 v[80:83], v67\n" \
  "v_pk_mul_f32 v[134:135], v[134:135], v[22:23]\n" \
  "v_exp_f32 v132, v132\n" \
  "v_exp_f32 v133, v133\n" \
  "v_exp_f32 v134, v134\n" \
  "v_exp_f32 v135, v135\n" \
  "v_pk_fma_f32 v[132:133], v[132:133], v[120:121], v[120:121]\n" \
  "v_pk_fma_f32 v[134:135], v[134:135], v[122:123], v[122:123]\n" \
  "v_rcp_f32 v132, v132\n" \
  "v_rcp_f32 v133, v133\n" \
  "v_rcp_f32 v134, v134\n" \
  "s_waitcnt lgkmcnt(2)\n" \
  "v_smfmac_f32_16x16x64_bf16 v[44:47], v[180:183], v[68:75], v192\n" \
  "v_lshlrev_b32_sdwa v64, 4, v252 dst_sel:DWORD dst_unused:UNUSED_PAD src0_sel:DWORD src1_sel:WORD_0\n" \
  "ds_read_b128 v[84:87], v64\n" \
  "v_lshlrev_b32_sdwa v65, 4, v252 dst_sel:DWORD dst_unused:UNUSED_PAD src0_sel:DWORD src1_sel:WORD_1\n" \
  "ds_read_b128 v[88:91], v65\n" \
  "v_rcp_f32 v135, v135\n" \
  "v_pk_mul_f32 v[20:21], v[20:21], v[132:133]\n" \
  "v_pk_fma_f32 v[20:21], v[22:23], v[134:135], v[20:21]\n" \
  "v_add_f32 v20, v20, v21\n" \
  "v_pk_mul_f32 v[136:137], v[24:25], v[24:25]\n" \
  "v_pk_mul_f32 v[138:139], v[26:27], v[26:27]\n" \
  "v_pk_fma_f32 v[136:137], v[136:137], s[52:53], v[124:125]\n" \
  "v_pk_fma_f32 v[138:139], v[138:139], s[52:53], v[124:125]\n" \
  "v_pk_mul_f32 v[136:137], v[136:137], v[24:25]\n" \
  "v_pk_mul_f32 v[138:139], v[138:139], v[26:27]\n" \
  "s_waitcnt lgkmcnt(2)\n" \
  "v_smfmac_f32_16x16x64_bf16 v[44:47], v[180:183], v[76:83], v192\n" \
  "v_lshlrev_b32_sdwa v66, 4, v220 dst_sel:DWORD dst_unused:UNUSED_PAD src0_sel:DWORD src1_sel:WORD_0\n" \
  "ds_read_b128 v[92:95], v66\n" \
  "v_lshlrev_b32_sdwa v67, 4, v220 dst_sel:DWORD dst_unused:UNUSED_PAD src0_sel:DWORD src1_sel:WORD_1\n" \
  "ds_read_b128 v[96:99], v67\n" \
  "v_exp_f32 v136, v136\n" \
  "v_exp_f32 v137, v137\n" \
  "v_exp_f32 v138, v138\n" \
  "v_exp_f32 v139, v139\n" \
  "v_pk_fma_f32 v[136:137], v[136:137], v[120:121], v[120:121]\n" \
  "v_pk_fma_f32 v[138:139], v[138:139], v[122:123], v[122:123]\n" \
  "v_rcp_f32 v136, v136\n" \
  "v_rcp_f32 v137, v137\n" \
  "v_rcp_f32 v138, v138\n" \
  "v_rcp_f32 v139, v139\n" \
  "s_waitcnt lgkmcnt(2)\n" \
  "v_smfmac_f32_16x16x64_bf16 v[48:51], v[180:183], v[84:91], v192\n" \
  "v_lshlrev_b32_sdwa v64, 4, v253 dst_sel:DWORD dst_unused:UNUSED_PAD src0_sel:DWORD src1_sel:WORD_0\n" \
  "ds_read_b128 v[100:103], v64\n" \
  "v_lshlrev_b32_sdwa v65, 4, v253 dst_sel:DWORD dst_unused:UNUSED_PAD src0_sel:DWORD src1_sel:WORD_1\n" \
  "ds_read_b128 v[104:107], v65\n" \
  "v_pk_mul_f32 v[24:25], v[24:25], v[136:137]\n" \
  "v_pk_fma_f32 v[24:25], v[26:27], v[138:139], v[24:25]\n" \
  "v_add_f32 v24, v24, v25\n" \
  "v_pk_mul_f32 v[140:141], v[28:29], v[28:29]\n" \
  "v_pk_mul_f32 v[142:143], v[30:31], v[30:31]\n" \
  "v_pk_fma_f32 v[140:141], v[140:141], s[52:53], v[124:125]\n" \
  "v_pk_fma_f32 v[142:143], v[142:143], s[52:53], v[124:125]\n" \
  "v_pk_mul_f32 v[140:141], v[140:141], v[28:29]\n" \
  "v_pk_mul_f32 v[142:143], v[142:143], v[30:31]\n" \
  "v_exp_f32 v140, v140\n" \
  "s_waitcnt lgkmcnt(2)\n" \
  "v_smfmac_f32_16x16x64_bf16 v[48:51], v[180:183], v[92:99], v192\n" \
  "v_lshlrev_b32_sdwa v66, 4, v221 dst_sel:DWORD dst_unused:UNUSED_PAD src0_sel:DWORD src1_sel:WORD_0\n" \
  "ds_read_b128 v[108:111], v66\n" \
  "v_lshlrev_b32_sdwa v67, 4, v221 dst_sel:DWORD dst_unused:UNUSED_PAD src0_sel:DWORD src1_sel:WORD_1\n" \
  "ds_read_b128 v[112:115], v67\n" \
  "v_exp_f32 v141, v141\n" \
  "v_exp_f32 v142, v142\n" \
  "v_exp_f32 v143, v143\n" \
  "v_pk_fma_f32 v[140:141], v[140:141], v[120:121], v[120:121]\n" \
  "v_pk_fma_f32 v[142:143], v[142:143], v[122:123], v[122:123]\n" \
  "v_rcp_f32 v140, v140\n" \
  "v_rcp_f32 v141, v141\n" \
  "v_rcp_f32 v142, v142\n" \
  "v_rcp_f32 v143, v143\n" \
  "v_pk_mul_f32 v[28:29], v[28:29], v[140:141]\n" \
  "s_waitcnt lgkmcnt(2)\n" \
  "v_smfmac_f32_16x16x64_bf16 v[52:55], v[180:183], v[100:107], v192\n" \
  "v_lshlrev_b32_sdwa v64, 4, v254 dst_sel:DWORD dst_unused:UNUSED_PAD src0_sel:DWORD src1_sel:WORD_0\n" \
  "ds_read_b128 v[68:71], v64\n" \
  "v_lshlrev_b32_sdwa v65, 4, v254 dst_sel:DWORD dst_unused:UNUSED_PAD src0_sel:DWORD src1_sel:WORD_1\n" \
  "ds_read_b128 v[72:75], v65\n" \
  "v_pk_fma_f32 v[28:29], v[30:31], v[142:143], v[28:29]\n" \
  "v_add_f32 v28, v28, v29\n" \
  "v_permlane16_swap_b32 v0, v16\n" \
  "v_permlane16_swap_b32 v4, v20\n" \
  "v_permlane16_swap_b32 v8, v24\n" \
  "v_permlane16_swap_b32 v12, v28\n" \
  "v_add_f32 v144, v0, v16\n" \
  "v_add_f32 v145, v4, v20\n" \
  "v_add_f32 v146, v8, v24\n" \
  "v_add_f32 v147, v12, v28\n" \
  "s_waitcnt lgkmcnt(2)\n" \
  "v_smfmac_f32_16x16x64_bf16 v[52:55], v[180:183], v[108:115], v192\n" \
  "v_lshlrev_b32_sdwa v66, 4, v222 dst_sel:DWORD dst_unused:UNUSED_PAD src0_sel:DWORD src1_sel:WORD_0\n" \
  "ds_read_b128 v[76:79], v66\n" \
  "v_lshlrev_b32_sdwa v67, 4, v222 dst_sel:DWORD dst_unused:UNUSED_PAD src0_sel:DWORD src1_sel:WORD_1\n" \
  "ds_read_b128 v[80:83], v67\n" \
  "v_min_u32 v144, -2, v144\n" \
  "v_min_u32 v145, -2, v145\n" \
  "v_min_u32 v146, -2, v146\n" \
  "v_min_u32 v147, -2, v147\n" \
  "global_store_dwordx4 v188, v[144:147], s[40:41] offset:0 sc1\n" \
  "v_pk_mul_f32 v[128:129], v[32:33], v[32:33]\n" \
  "v_pk_mul_f32 v[130:131], v[34:35], v[34:35]\n" \
  "v_pk_fma_f32 v[128:129], v[128:129], s[52:53], v[124:125]\n" \
  "v_pk_fma_f32 v[130:131], v[130:131], s[52:53], v[124:125]\n" \
  "v_pk_mul_f32 v[128:129], v[128:129], v[32:33]\n" \
  "s_waitcnt lgkmcnt(2)\n" \
  "v_smfmac_f32_16x16x64_bf16 v[56:59], v[180:183], v[68:75], v192\n" \
  "v_lshlrev_b32_sdwa v64, 4, v255 dst_sel:DWORD dst_unused:UNUSED_PAD src0_sel:DWORD src1_sel:WORD_0\n" \
  "ds_read_b128 v[84:87], v64\n" \
  "v_lshlrev_b32_sdwa v65, 4, v255 dst_sel:DWORD dst_unused:UNUSED_PAD src0_sel:DWORD src1_sel:WORD_1\n" \
  "ds_read_b128 v[88:91], v65\n" \
  "v_pk_mul_f32 v[130:131], v[130:131], v[34:35]\n" \
  "v_exp_f32 v128, v128\n" \
  "v_exp_f32 v129, v129\n" \
  "v_exp_f32 v130, v130\n" \
  "v_exp_f32 v131, v131\n" \
  "v_pk_fma_f32 v[128:129], v[128:129], v[120:121], v[120:121]\n" \
  "v_pk_fma_f32 v[130:131], v[130:131], v[122:123], v[122:123]\n" \
  "v_rcp_f32 v128, v128\n" \
  "v_rcp_f32 v129, v129\n" \
  "v_rcp_f32 v130, v130\n" \
  "s_waitcnt lgkmcnt(2)\n" \
  "v_smfmac_f32_16x16x64_bf16 v[56:59], v[180:183], v[76:83], v192\n" \
  "v_lshlrev_b32_sdwa v66, 4, v223 dst_sel:DWORD dst_unused:UNUSED_PAD src0_sel:DWORD src1_sel:WORD_0\n" \
  "ds_read_b128 v[92:95], v66\n" \
  "v_lshlrev_b32_sdwa v67, 4, v223 dst_sel:DWORD dst_unused:UNUSED_PAD src0_sel:DWORD src1_sel:WORD_1\n" \
  "ds_read_b128 v[96:99], v67\n" \
  "v_rcp_f32 v131, v131\n" \
  "v_pk_mul_f32 v[32:33], v[32:33], v[128:129]\n" \
  "v_pk_fma_f32 v[32:33], v[34:35], v[130:131], v[32:33]\n" \
  "v_add_f32 v32, v32, v33\n" \
  "v_pk_mul_f32 v[132:133], v[36:37], v[36:37]\n" \
  "v_pk_mul_f32 v[134:135], v[38:39], v[38:39]\n" \
  "v_pk_fma_f32 v[132:133], v[132:133], s[52:53], v[124:125]\n" \
  "v_pk_fma_f32 v[134:135], v[134:135], s[52:53], v[124:125]\n" \
  "v_pk_mul_f32 v[132:133], v[132:133], v[36:37]\n" \
  "v_pk_mul_f32 v[134:135], v[134:135], v[38:39]\n" \
  "s_setprio 0\n" \
  "s_waitcnt lgkmcnt(2)\n" \
  "v_smfmac_f32_16x16x64_bf16 v[60:63], v[180:183], v[84:91], v192\n" \
  "v_exp_f32 v132, v132\n" \
  "v_exp_f32 v133, v133\n" \
  "v_exp_f32 v134, v134\n" \
  "v_exp_f32 v135, v135\n" \
  "v_pk_fma_f32 v[132:133], v[132:133], v[120:121], v[120:121]\n" \
  "v_pk_fma_f32 v[134:135], v[134:135], v[122:123], v[122:123]\n" \
  "v_rcp_f32 v132, v132\n" \
  "v_rcp_f32 v133, v133\n" \
  "v_rcp_f32 v134, v134\n" \
  "v_rcp_f32 v135, v135\n" \
  "v_pk_mul_f32 v[36:37], v[36:37], v[132:133]\n" \
  "v_pk_fma_f32 v[36:37], v[38:39], v[134:135], v[36:37]\n" \
  "v_add_f32 v36, v36, v37\n" \
  "v_pk_mul_f32 v[136:137], v[40:41], v[40:41]\n" \
  "v_pk_mul_f32 v[138:139], v[42:43], v[42:43]\n" \
  "v_pk_fma_f32 v[136:137], v[136:137], s[52:53], v[124:125]\n" \
  "v_pk_fma_f32 v[138:139], v[138:139], s[52:53], v[124:125]\n" \
  "v_pk_mul_f32 v[136:137], v[136:137], v[40:41]\n" \
  "v_pk_mul_f32 v[138:139], v[138:139], v[42:43]\n" \
  "v_exp_f32 v136, v136\n" \
  "v_exp_f32 v137, v137\n" \
  "v_exp_f32 v138, v138\n" \
  "v_exp_f32 v139, v139\n" \
  "v_pk_fma_f32 v[136:137], v[136:137], v[120:121], v[120:121]\n" \
  "v_pk_fma_f32 v[138:139], v[138:139], v[122:123], v[122:123]\n" \
  "v_rcp_f32 v136, v136\n" \
  "v_rcp_f32 v137, v137\n" \
  "v_rcp_f32 v138, v138\n" \
  "v_rcp_f32 v139, v139\n" \
  "v_pk_mul_f32 v[40:41], v[40:41], v[136:137]\n" \
  "v_pk_fma_f32 v[40:41], v[42:43], v[138:139], v[40:41]\n" \
  "v_add_f32 v40, v40, v41\n" \
  "s_waitcnt lgkmcnt(0)\n" \
  "v_smfmac_f32_16x16x64_bf16 v[60:63], v[180:183], v[92:99], v192\n" \
  "v_pk_mul_f32 v[140:141], v[44:45], v[44:45]\n" \
  "v_pk_mul_f32 v[142:143], v[46:47], v[46:47]\n" \
  "v_pk_fma_f32 v[140:141], v[140:141], s[52:53], v[124:125]\n" \
  "v_pk_fma_f32 v[142:143], v[142:143], s[52:53], v[124:125]\n" \
  "v_pk_mul_f32 v[140:141], v[140:141], v[44:45]\n" \
  "v_pk_mul_f32 v[142:143], v[142:143], v[46:47]\n" \
  "v_exp_f32 v140, v140\n" \
  "v_exp_f32 v141, v141\n" \
  "v_exp_f32 v142, v142\n" \
  "v_exp_f32 v143, v143\n" \
  "v_pk_fma_f32 v[140:141], v[140:141], v[120:121], v[120:121]\n" \
  "v_pk_fma_f32 v[142:143], v[142:143], v[122:123], v[122:123]\n" \
  "v_rcp_f32 v140, v140\n" \
  "v_rcp_f32 v141, v141\n" \
  "v_rcp_f32 v142, v142\n" \
  "v_rcp_f32 v143, v143\n" \
  "v_pk_mul_f32 v[44:45], v[44:45], v[140:141]\n" \
  "v_pk_fma_f32 v[44:45], v[46:47], v[142:143], v[44:45]\n" \
  "v_add_f32 v44, v44, v45\n" \
  "v_pk_mul_f32 v[128:129], v[48:49], v[48:49]\n" \
  "v_pk_mul_f32 v[130:131], v[50:51], v[50:51]\n" \
  "v_pk_fma_f32 v[128:129], v[128:129], s[52:53], v[124:125]\n" \
  "v_pk_fma_f32 v[130:131], v[130:131], s[52:53], v[124:125]\n" \
  "v_pk_mul_f32 v[128:129], v[128:129], v[48:49]\n" \
  "v_pk_mul_f32 v[130:131], v[130:131], v[50:51]\n" \
  "v_exp_f32 v128, v128\n" \
  "v_exp_f32 v129, v129\n" \
  "v_exp_f32 v130, v130\n" \
  "v_exp_f32 v131, v131\n" \
  "v_pk_fma_f32 v[128:129], v[128:129], v[120:121], v[120:121]\n" \
  "v_pk_fma_f32 v[130:131], v[130:131], v[122:123], v[122:123]\n" \
  "v_rcp_f32 v128, v128\n" \
  "v_rcp_f32 v129, v129\n" \
  "v_rcp_f32 v130, v130\n" \
  "v_rcp_f32 v131, v131\n" \
  "v_pk_mul_f32 v[48:49], v[48:49], v[128:129]\n" \
  "v_pk_fma_f32 v[48:49], v[50:51], v[130:131], v[48:49]\n" \
  "v_add_f32 v48, v48, v49\n" \
  "v_pk_mul_f32 v[132:133], v[52:53], v[52:53]\n" \
  "v_pk_mul_f32 v[134:135], v[54:55], v[54:55]\n" \
  "v_pk_fma_f32 v[132:133], v[132:133], s[52:53], v[124:125]\n" \
  "v_pk_fma_f32 v[134:135], v[134:135], s[52:53], v[124:125]\n" \
  "v_pk_mul_f32 v[132:133], v[132:133], v[52:53]\n" \
  "v_pk_mul_f32 v[134:135], v[134:135], v[54:55]\n" \
  "v_exp_f32 v132, v132\n" \
  "v_exp_f32 v133, v133\n" \
  "v_exp_f32 v134, v134\n" \
  "v_exp_f32 v135, v135\n" \
  "v_pk_fma_f32 v[132:133], v[132:133], v[120:121], v[120:121]\n" \
  "v_pk_fma_f32 v[134:135], v[134:135], v[122:123], v[122:123]\n" \
  "v_rcp_f32 v132, v132\n" \
  "v_rcp_f32 v133, v133\n" \
  "v_rcp_f32 v134, v134\n" \
  "v_rcp_f32 v135, v135\n" \
  "v_pk_mul_f32 v[52:53], v[52:53], v[132:133]\n" \
  "v_pk_fma_f32 v[52:53], v[54:55], v[134:135], v[52:53]\n" \
  "v_add_f32 v52, v52, v53\n" \
  "v_pk_mul_f32 v[136:137], v[56:57], v[56:57]\n" \
  "v_pk_mul_f32 v[138:139], v[58:59], v[58:59]\n" \
  "v_pk_fma_f32 v[136:137], v[136:137], s[52:53], v[124:125]\n" \
  "v_pk_fma_f32 v[138:139], v[138:139], s[52:53], v[124:125]\n" \
  "v_pk_mul_f32 v[136:137], v[136:137], v[56:57]\n" \
  "v_pk_mul_f32 v[138:139], v[138:139], v[58:59]\n" \
  "v_exp_f32 v136, v136\n" \
  "v_exp_f32 v137, v137\n" \
  "v_exp_f32 v138, v138\n" \
  "v_exp_f32 v139, v139\n" \
  "v_pk_fma_f32 v[136:137], v[136:137], v[120:121], v[120:121]\n" \
  "v_pk_fma_f32 v[138:139], v[138:139], v[122:123], v[122:123]\n" \
  "v_rcp_f32 v136, v136\n" \
  "v_rcp_f32 v137, v137\n" \
  "v_rcp_f32 v138, v138\n" \
  "v_rcp_f32 v139, v139\n" \
  "v_pk_mul_f32 v[56:57], v[56:57], v[136:137]\n" \
  "v_pk_fma_f32 v[56:57], v[58:59], v[138:139], v[56:57]\n" \
  "v_add_f32 v56, v56, v57\n" \
  "v_pk_mul_f32 v[140:141], v[60:61], v[60:61]\n" \
  "v_pk_mul_f32 v[142:143], v[62:63], v[62:63]\n" \
  "v_pk_fma_f32 v[140:141], v[140:141], s[52:53], v[124:125]\n" \
  "v_pk_fma_f32 v[142:143], v[142:143], s[52:53], v[124:125]\n" \
  "v_pk_mul_f32 v[140:141], v[140:141], v[60:61]\n" \
  "v_pk_mul_f32 v[142:143], v[142:143], v[62:63]\n" \
  "v_exp_f32 v140, v140\n" \
  "v_exp_f32 v141, v141\n" \
  "v_exp_f32 v142, v142\n" \
  "v_exp_f32 v143, v143\n" \
  "v_pk_fma_f32 v[140:141], v[140:141], v[120:121], v[120:121]\n" \
  "v_pk_fma_f32 v[142:143], v[142:143], v[122:123], v[122:123]\n" \
  "v_rcp_f32 v140, v140\n" \
  "v_rcp_f32 v141, v141\n" \
  "v_rcp_f32 v142, v142\n" \
  "v_rcp_f32 v143, v143\n" \
  "v_pk_mul_f32 v[60:61], v[60:61], v[140:141]\n" \
  "v_pk_fma_f32 v[60:61], v[62:63], v[142:143], v[60:61]\n" \
  "v_add_f32 v60, v60, v61\n" \
  "v_permlane16_swap_b32 v32, v48\n" \
  "v_permlane16_swap_b32 v36, v52\n" \
  "v_permlane16_swap_b32 v40, v56\n" \
  "v_permlane16_swap_b32 v44, v60\n" \
  "v_add_f32 v148, v32, v48\n" \
  "v_add_f32 v149, v36, v52\n" \
  "v_add_f32 v150, v40, v56\n" \
  "v_add_f32 v151, v44, v60\n" \
  "v_min_u32 v148, -2, v148\n" \
  "v_min_u32 v149, -2, v149\n" \
  "v_min_u32 v150, -2, v150\n" \
  "v_min_u32 v151, -2, v151\n" \
  "global_store_dwordx4 v188, v[148:151], s[40:41] offset:1024 sc1\n" \
  "s_cmp_ge_u32 s27, 4\n" \
  "s_cbranch_scc1 LF_b\n" \
  "s_load_dwordx2 s[24:25], s[0:1], 0x40\n" \
  "v_lshrrev_b32 v0, 4, v188\n" \
  "v_lshrrev_b32 v1, 4, v0\n" \
  "v_and_b32 v2, 15, v0\n" \
  "v_lshlrev_b32 v2, 4, v2\n" \
  "v_lshl_add_u32 v1, v1, 18, v2\n" \
  "s_lshl_b32 s35, s27, 20\n" \
  "s_lshl_b32 s36, s26, 8\n" \
  "s_add_u32 s35, s35, s36\n" \
  "s_add_u32 s35, s35, 105676800\n" \
  "s_add_u32 s40, s22, s35\n" \
  "s_addc_u32 s41, s23, 0\n" \
  "s_add_u32 s42, s40, 65536\n" \
  "s_addc_u32 s43, s41, 0\n" \
  "s_add_u32 s54, s42, 65536\n" \
  "s_addc_u32 s55, s43, 0\n" \
  "s_add_u32 s56, s54, 65536\n" \
  "s_addc_u32 s57, s55, 0\n" \
  "s_add_u32 s44, s22, 109871104\n" \
  "s_addc_u32 s45, s23, 0\n" \
  "s_add_u32 s46, s44, 65536\n" \
  "s_addc_u32 s47, s45, 0\n" \
  "s_lshr_b32 s35, s26, 2\n" \
  "s_lshl_b32 s35, s35, 8\n" \
  "s_and_b32 s36, s26, 1\n" \
  "s_lshl_b32 s36, s36, 7\n" \
  "s_add_u32 s35, s35, s36\n" \
  "s_and_b32 s36, s26, 2\n" \
  "s_lshl3_add_u32 s35, s36, s35\n" \
  "v_and_b32 v2, 3, v0\n" \
  "v_lshrrev_b32 v3, 2, v0\n" \
  "v_lshl_add_u32 v2, v2, 5, v3\n" \
  "v_add_u32 v2, s35, v2\n" \
  "v_lshlrev_b32 v3, 2, v2\n" \
  "s_cmp_eq_u32 s27, 0\n" \
  "s_cselect_b32 s35, -1, 0\n" \
  "v_and_b32 v3, s35, v3\n" \
  "s_lshl_b32 s6, s6, 16\n" \
  "v_and_b32 v6, 15, v0\n" \
  "v_lshrrev_b32 v7, 4, v0\n" \
  "v_lshl_add_u32 v6, v6, 2, v7\n" \
  "s_lshl_b32 s35, s27, 8\n" \
  "v_lshl_add_u32 v6, v6, 2, s35\n" \
  "v_lshlrev_b32 v7, 2, v0\n" \
  "v_lshlrev_b32 v2, 1, v2\n" \
  "s_mov_b32 s5, 0x10000\n" \
  "s_barrier\n" \
  "s_sleep 12\n" \
  "LF_poll:\n" \
  "global_load_dwordx4 v[8:11], v1, s[40:41] sc1\n" \
  "global_load_dwordx4 v[12:15], v1, s[42:43] sc1\n" \
  "global_load_dwordx4 v[16:19], v1, s[54:55] sc1\n" \
  "global_load_dwordx4 v[20:23], v1, s[56:57] sc1\n" \
  "global_load_dword v24, v3, s[44:45] sc1\n" \
  "global_load_dword v25, v3, s[46:47] sc1\n" \
  "s_waitcnt vmcnt(0)\n" \
  "v_max3_u32 v4, v8, v9, v10\n" \
  "v_max3_u32 v5, v11, v12, v13\n" \
  "v_max3_u32 v4, v4, v14, v15\n" \
  "v_max3_u32 v5, v5, v16, v17\n" \
  "v_max3_u32 v4, v4, v18, v19\n" \
  "v_max3_u32 v5, v5, v20, v21\n" \
  "v_max3_u32 v4, v4, v22, v23\n" \
  "v_max3_u32 v5, v5, v24, v25\n" \
  "v_max_u32 v5, v5, v4\n" \
  "v_cmp_eq_u32 vcc, -1, v5\n" \
  "s_sub_u32 s5, s5, 1\n" \
  "s_cbranch_scc1 LF_go\n" \
  "s_nop 2\n" \
  "s_cbranch_vccz LF_go\n" \
  "s_mov_b64 exec, vcc\n" \
  "s_sleep 1\n" \
  "s_branch LF_poll\n" \
  "LF_go:\n" \
  "s_mov_b64 exec, -1\n" \
  "v_pk_add_f32 v[8:9], v[8:9], v[12:13]\n" \
  "v_pk_add_f32 v[10:11], v[10:11], v[14:15]\n" \
  "v_pk_add_f32 v[16:17], v[16:17], v[20:21]\n" \
  "v_pk_add_f32 v[18:19], v[18:19], v[22:23]\n" \
  "v_pk_add_f32 v[8:9], v[8:9], v[16:17]\n" \
  "v_pk_add_f32 v[10:11], v[10:11], v[18:19]\n" \
  "v_add_f32 v24, v24, v25\n" \
  "v_permlane16_swap_b32 v8, v9\n" \
  "v_permlane16_swap_b32 v10, v11\n" \
  "v_add_f32 v8, v8, v9\n" \
  "v_add_f32 v10, v10, v11\n" \
  "v_cvt_pk_bf16_f32 v24, v24, v24\n" \
  "s_cmp_lg_u32 s27, 0\n" \
  "v_permlane32_swap_b32 v8, v10\n" \
  "v_add_f32 v8, v8, v10\n" \
  "ds_write_b32 v6, v8\n" \
  "s_waitcnt lgkmcnt(0)\n" \
  "s_barrier\n" \
  "s_cbranch_scc1 LF_end\n" \
  "ds_read2st64_b32 v[8:9], v7 offset0:0 offset1:1\n" \
  "ds_read2st64_b32 v[10:11], v7 offset0:2 offset1:3\n" \
  "v_lshlrev_b32 v24, 16, v24\n" \
  "s_waitcnt lgkmcnt(0)\n" \
  "v_pk_add_f32 v[8:9], v[8:9], v[10:11]\n" \
  "v_add_f32 v8, v8, v9\n" \
  "v_cvt_pk_bf16_f32 v8, v8, v8\n" \
  "v_lshlrev_b32 v8, 16, v8\n" \
  "v_add_f32 v8, s6, v8\n" \
  "v_cvt_pk_bf16_f32 v8, v8, v8\n" \
  "v_lshlrev_b32 v8, 16, v8\n" \
  "v_add_f32 v8, v8, v24\n" \
  "v_cvt_pk_bf16_f32 v8, v8, v8\n" \
  "global_store_short v2, v8, s[24:25]\n" \
  "LF_end:\n" \
  "s_endpgm\n" \
  "LF_b:\n" \
  "s_barrier\n" \
  "s_endpgm\n" \
  "LG_h:\n" \
  "s_cmp_eq_u32 s51, 4\n" \
  "s_cbranch_scc1 LG_chk\n" \
  "s_cmp_lg_u32 s51, 7\n" \
  "s_cbranch_scc1 LG_ret\n" \
  "s_barrier\n" \
  "s_mov_b32 exec_hi, 0\n" \
  "global_atomic_add v196, v199, s[48:49] offset:3076 sc1\n" \
  "s_mov_b64 exec, -1\n" \
  "s_branch LG_aft\n" \
  "LG_pol:\n" \
  "s_mov_b64 exec, 0xff\n" \
  "global_load_dword v200, v198, s[48:49] offset:3076 sc1\n" \
  "s_mov_b64 exec, -1\n" \
  "s_branch LG_rt2\n" \
  "LG_chk:\n" \
  "s_mov_b32 s5, 0x10000\n" \
  "LG_p:\n" \
  "s_mov_b64 exec, 0xff\n" \
  "v_cmp_le_u32 vcc, 32, v200\n" \
  "s_mov_b64 exec, -1\n" \
  "s_sub_u32 s5, s5, 1\n" \
  "s_cselect_b32 s35, 0xff, vcc_lo\n" \
  "s_cmp_eq_u32 s35, 0xff\n" \
  "s_cbranch_scc1 LG_p_x\n" \
  "s_sleep 2\n" \
  "s_mov_b64 exec, 0xff\n" \
  "global_load_dword v200, v198, s[48:49] offset:3076 sc1\n" \
  "s_mov_b64 exec, -1\n" \
  "s_waitcnt vmcnt(0)\n" \
  "s_branch LG_p\n" \
  "LG_p_x:\n" \
  "s_branch LG_ret\n" \
  "LZ_end:\n"

#define R10(p, b) p #b "0", p #b "1", p #b "2", p #b "3", p #b "4", p #b "5", p #b "6", p #b "7", p #b "8", p #b "9"
__global__ __launch_bounds__(512, 2) void k1(const int* __restrict__ idx, const unsigned short* __restrict__ table,
                                             const unsigned short* __restrict__ w_p, const unsigned short* __restrict__ w1,
                                             const unsigned short* __restrict__ b1, const unsigned short* __restrict__ w2,
                                             const unsigned short* __restrict__ b2, char* __restrict__ ws, __hip_bfloat16* __restrict__ out) {
  __shared__ __attribute__((aligned(1024))) char smem[163840];
  unsigned tid = threadIdx.x;
  asm volatile(K_ASM
               : "+{v0}"(tid)
               : "{s[0:1]}"(__builtin_amdgcn_kernarg_segment_ptr()), "{s26}"(blockIdx.x), "s"((unsigned)(uintptr_t)smem)
               : "memory", "scc", "vcc", "s3", "s4", "s5", "s6", "s7", "s8", "s9", R10("s", 1), "s20", "s21", "s22", "s23", "s24", "s25", "s27", "s28", "s29", "s30", "s31", "s33", "s34", "s35", "s36", "s37", "s38", "s39", R10("s", 4), R10("s", 5), R10("s", 6), R10("s", 7), R10("s", 8), R10("s", 9),
                 "v1", "v2", "v3", "v4", "v5", "v6", "v7", "v8", "v9", R10("v", 1), R10("v", 2), R10("v", 3), R10("v", 4), R10("v", 5), R10("v", 6), R10("v", 7), R10("v", 8), R10("v", 9),
                 R10("v", 10), R10("v", 11), R10("v", 12), R10("v", 13), R10("v", 14), R10("v", 15), R10("v", 16), R10("v", 17), R10("v", 18), R10("v", 19),
                 R10("v", 20), R10("v", 21), R10("v", 22), R10("v", 23), R10("v", 24), "v250", "v251", "v252", "v253", "v254", "v255");
}

extern "C" void kernel_launch(void* const* d_in, const int* in_sizes, int n_in, void* d_out, int out_size, void* d_ws,
                              size_t ws_size, hipStream_t stream) {
  k1<<<dim3(256), dim3(512), 0, stream>>>((const int*)d_in[0], (const unsigned short*)d_in[1], (const unsigned short*)d_in[2], (const unsigned short*)d_in[3],
                                          (const unsigned short*)d_in[4], (const unsigned short*)d_in[5], (const unsigned short*)d_in[6], (char*)d_ws, (__hip_bfloat16*)d_out);
}
```
